```python
import torch

def _softmax(x, axis=-1):
    return torch.softmax(x, dim=axis)

def _gelu(x):
    return torch.nn.functional.gelu(x, approximate='tanh')

def _silu(x):
    return torch.nn.functional.silu(x)

def _sigmoid(x):
    return torch.sigmoid(x)

def _layernorm(x, axis=-1, eps=1e-05):
    m = x.mean(dim=axis, keepdim=True)
    v = x.var(dim=axis, keepdim=True, unbiased=False)
    return (x - m) / torch.sqrt(v + eps)

def _scatter_add(tgt, idx, src, axis=0):
    return tgt.clone().index_add_(axis, idx, src)

def _segment_sum(src, seg, n):
    out = torch.zeros((n,) + src.shape[1:], dtype=src.dtype)
    return out.index_add_(0, seg, src)

def _pad(x, pw):
    flat = tuple((v for pair in reversed(pw) for v in pair))
    return torch.nn.functional.pad(x, flat)
kernel_name = 'concat_heads__ssm_conv_s4d__memory_aug_recurrent_mem_tr__prompt'

def setup_inputs():

    _w = lambda a: (lambda t: t.to(torch.bfloat16) if t.is_floating_point() else t)(torch.as_tensor(a))

    def _A_setup():
        Bsz, L, D, N = (1, 2048, 448, 256)
        return {'u': _w((lambda *s: torch.randn(*s, dtype=torch.bfloat16) * 0.05)(Bsz, L, D)), 'log_A_real': _w((lambda *s: torch.randn(*s, dtype=torch.bfloat16) * 6.4)(D, N) * 0.5), 'A_imag': _w((lambda *s: torch.randn(*s, dtype=torch.bfloat16) * 0.05)(D, N) * 3.14), 'C_real': _w((lambda *s: torch.randn(*s, dtype=torch.bfloat16) * 0.2)(D, N) / N ** 0.5), 'C_imag': _w((lambda *s: torch.randn(*s, dtype=torch.bfloat16) * 0.2)(D, N) / N ** 0.5), 'log_dt': _w((lambda *s: torch.randn(*s, dtype=torch.bfloat16) * 0.05)(D) * 0.5 - 4.0), 'D_skip': _w((lambda *s: torch.randn(*s, dtype=torch.bfloat16) * 0.05)(D))}
    _A_in = _A_setup()

    def _B_setup():
        B, S, Dm = (1, 2048, 448)
        H, Dh = (16, 256)
        M, seg = (8, 32)
        seg = min(seg, S)
        S = S // seg * seg
        return {'x': _w((lambda *s: torch.randn(*s, dtype=torch.bfloat16) * 0.05)(B, S, Dm)), 'mem0': _w((lambda *s: torch.randn(*s, dtype=torch.bfloat16) * 0.4)(B, M, Dm)), 'Wq': _w((lambda *s: torch.randn(*s, dtype=torch.bfloat16) * 6.4)(Dm, H, Dh) * Dm ** (-0.5)), 'Wk': _w((lambda *s: torch.randn(*s, dtype=torch.bfloat16) * 6.4)(Dm, H, Dh) * Dm ** (-0.5)), 'Wv': _w((lambda *s: torch.randn(*s, dtype=torch.bfloat16) * 0.4)(Dm, H, Dh) * Dm ** (-0.5)), 'Wo': _w((lambda *s: torch.randn(*s, dtype=torch.bfloat16) * 0.4)(H, Dh, Dm) * (H * Dh) ** (-0.5)), 'seg_len': seg}
    _B_in = _B_setup()
    return {'A_u': _A_in['u'], 'A_log_A_real': _A_in['log_A_real'], 'A_A_imag': _A_in['A_imag'], 'A_C_real': _A_in['C_real'], 'A_C_imag': _A_in['C_imag'], 'A_log_dt': _A_in['log_dt'], 'A_D_skip': _A_in['D_skip'], 'B_x': _B_in['x'], 'B_mem0': _B_in['mem0'], 'B_Wq': _B_in['Wq'], 'B_Wk': _B_in['Wk'], 'B_Wv': _B_in['Wv'], 'B_Wo': _B_in['Wo'], 'B_seg_len': _B_in['seg_len']}

def reference(A_u, A_log_A_real, A_A_imag, A_C_real, A_C_imag, A_log_dt, A_D_skip, B_x, B_mem0, B_Wq, B_Wk, B_Wv, B_Wo, B_seg_len):

    def _A_ref(u, log_A_real, A_imag, C_real, C_imag, log_dt, D_skip):
        L = u.shape[1]
        dt = torch.exp(log_dt)
        A = -torch.exp(log_A_real) + 1j * A_imag
        C = (C_real + 1j * C_imag) * dt[:, None]
        pos = (lambda x, dt: x.to(dt))(torch.arange(L), log_dt.dtype)
        P = torch.exp((A * dt[:, None])[:, :, None] * pos[None, None, :])
        K = 2.0 * torch.einsum('dn,dnl->dl', C, P).real
        L2 = 2 * L
        u_t = (lambda x, *d: x.permute(*d))(u, 0, 2, 1)
        Kf = (lambda x, *a, **k: torch.fft.rfft(x.float(), *a, **k))(K, L2)
        Uf = (lambda x, *a, **k: torch.fft.rfft(x.float(), *a, **k))(u_t, L2)
        y = (lambda x, *a, **k: torch.fft.irfft(x, *a, **k).to(torch.bfloat16))(Uf * Kf[None, :, :], L2)[:, :, :L]
        y = (lambda x, *d: x.permute(*d))(y, 0, 2, 1)
        return y + u * D_skip[None, None, :]

    def _B_ref(x, mem0, Wq, Wk, Wv, Wo, seg_len):
        B, S, Dm = x.shape
        H, Dh = (Wq.shape[1], Wq.shape[2])
        M = mem0.shape[1]
        scale = Dh ** (-0.5)
        n_seg = S // seg_len
        L = M + seg_len
        idx = torch.arange(L)
        mask = (idx[:, None] < M) | (idx[:, None] >= idx[None, :])
        mem = mem0
        outs = []
        for s in range(n_seg):
            xs = x[:, s * seg_len:(s + 1) * seg_len, :].clone()
            h = torch.concatenate([mem, xs], axis=1)
            q = torch.einsum('btd,dhe->bhte', h, Wq)
            k = torch.einsum('btd,dhe->bhte', h, Wk)
            v = torch.einsum('btd,dhe->bhte', h, Wv)
            scores = torch.einsum('bhqe,bhke->bhqk', q, k) * scale
            scores = torch.where(mask[None, None], scores, -1000000000.0)
            p = _softmax(scores, axis=-1)
            o = torch.einsum('bhqk,bhke->bhqe', p, v)
            o = torch.einsum('bhte,hed->btd', o, Wo)
            mem = o[:, :M, :].clone()
            outs.append(o[:, M:, :])
        return torch.concatenate(outs, axis=1)
    a = _A_ref(A_u, A_log_A_real, A_A_imag, A_C_real, A_C_imag, A_log_dt, A_D_skip)
    b = _B_ref(B_x, B_mem0, B_Wq, B_Wk, B_Wv, B_Wo, B_seg_len)
    return torch.concatenate([a, b], axis=-1)
```

```cpp
#include <hip/hip_runtime.h>
#include <stdint.h>
typedef unsigned short u16;
typedef unsigned long long u64;
typedef __attribute__((ext_vector_type(8))) __bf16 bf8;
typedef __attribute__((ext_vector_type(16))) __bf16 bf16v;
typedef __attribute__((ext_vector_type(2))) __bf16 bf2;
typedef __attribute__((ext_vector_type(2))) float f2;
typedef __attribute__((ext_vector_type(4))) float f4;
typedef __attribute__((ext_vector_type(4))) unsigned u4;
typedef __attribute__((ext_vector_type(2))) unsigned u2;
typedef __attribute__((ext_vector_type(8))) unsigned u8v;
typedef __attribute__((ext_vector_type(4))) short s4;
#define LAS __attribute__((address_space(3)))
#define DI __device__ __forceinline__
#define SB() __builtin_amdgcn_sched_barrier(0)

DI unsigned pk2(float a, float b) { f2 v = {a, b}; return __builtin_bit_cast(unsigned, __builtin_convertvector(v, bf2)); }
DI float bfr(float a) { return __uint_as_float(pk2(a, a) & 0xffff0000u); }
DI unsigned san(unsigned x) { return x < 0xFFFFFFFEu ? x : 0xFFFFFFFEu; }
DI unsigned umax3(unsigned a, unsigned b, unsigned c) { return max(max(a, b), c); }
DI unsigned mx4(u4 v) { return max(umax3(v[0], v[1], v[2]), v[3]); }
DI void bar() { asm volatile("s_waitcnt lgkmcnt(0)\n\ts_barrier" ::: "memory"); SB(); }
DI void dly(int n) {
#pragma nounroll
    for (int i = 0; i < n; ++i) asm volatile("s_nop 0");
}
DI f4 smf(u4 a, u8v b, f4 c, int idx) { return __builtin_amdgcn_smfmac_f32_16x16x64_bf16(__builtin_bit_cast(bf8, a), __builtin_bit_cast(bf16v, b), c, idx, 0, 0); }
DI f4 mf(u4 a, u4 b, f4 c) { return __builtin_amdgcn_mfma_f32_16x16x32_bf16(__builtin_bit_cast(bf8, a), __builtin_bit_cast(bf8, b), c, 0, 0, 0); }
DI float swapadd32(float a, float b) { auto r = __builtin_amdgcn_permlane32_swap(__float_as_uint(a), __float_as_uint(b), false, false); return __uint_as_float(r[0]) + __uint_as_float(r[1]); }
DI float x16max(float a) { auto r = __builtin_amdgcn_permlane16_swap(__float_as_uint(a), __float_as_uint(a), false, false); return fmaxf(__uint_as_float(r[0]), __uint_as_float(r[1])); }
DI float x16add(float a) { auto r = __builtin_amdgcn_permlane16_swap(__float_as_uint(a), __float_as_uint(a), false, false); return __uint_as_float(r[0]) + __uint_as_float(r[1]); }
DI float x32max(float a) { auto r = __builtin_amdgcn_permlane32_swap(__float_as_uint(a), __float_as_uint(a), false, false); return fmaxf(__uint_as_float(r[0]), __uint_as_float(r[1])); }
DI float x32add(float a) { auto r = __builtin_amdgcn_permlane32_swap(__float_as_uint(a), __float_as_uint(a), false, false); return __uint_as_float(r[0]) + __uint_as_float(r[1]); }
DI f4 sel4(bool c, f4 a, f4 b) { f4 r; r[0] = c ? a[0] : b[0]; r[1] = c ? a[1] : b[1]; r[2] = c ? a[2] : b[2]; r[3] = c ? a[3] : b[3]; return r; }
DI u2 trread(const char LAS* p) { return __builtin_bit_cast(u2, __builtin_amdgcn_ds_read_tr16_b64_v4i16((LAS s4*)p)); }
DI u8v cat8(u4 a, u4 b) { return (u8v){a[0], a[1], a[2], a[3], b[0], b[1], b[2], b[3]}; }
DI u4 mksrd(const void* p, unsigned bytes) { u64 x = (u64)p; return (u4){(unsigned)x, (unsigned)(x >> 32) & 0xffffu, bytes, 0x00020000u}; }
DI u4 ld128(u4 srd, int voff, int soff) { u4 v; asm volatile("buffer_load_dwordx4 %0, %1, %2, %3 offen sc1" : "=v"(v) : "v"(voff), "s"(srd), "s"(soff) : "memory"); return v; }
DI u2 ld64(u4 srd, int voff, int soff) { u2 v; asm volatile("buffer_load_dwordx2 %0, %1, %2, %3 offen sc1" : "=v"(v) : "v"(voff), "s"(srd), "s"(soff) : "memory"); return v; }
DI unsigned ld32(u4 srd, int voff, int soff) { unsigned v; asm volatile("buffer_load_dword %0, %1, %2, %3 offen sc1" : "=v"(v) : "v"(voff), "s"(srd), "s"(soff) : "memory"); return v; }
DI void st128(u4 v, u4 srd, int voff, int soff) { asm volatile("buffer_store_dwordx4 %0, %1, %2, %3 offen sc1\n\ts_nop 1" :: "v"(v), "v"(voff), "s"(srd), "s"(soff) : "memory"); }
DI void st64(u2 v, u4 srd, int voff, int soff) { asm volatile("buffer_store_dwordx2 %0, %1, %2, %3 offen sc1" :: "v"(v), "v"(voff), "s"(srd), "s"(soff) : "memory"); }
DI void st32(unsigned v, u4 srd, int voff, int soff) { asm volatile("buffer_store_dword %0, %1, %2, %3 offen sc1" :: "v"(v), "v"(voff), "s"(srd), "s"(soff) : "memory"); }
#define PIN(x) asm volatile("" : "+v"(x))
#define WAITV(n, ...) asm volatile("s_waitcnt vmcnt(" #n ")" : __VA_ARGS__ :: "memory")

#define NSEG 64
#define SLOT(k, hi) ((k) * 524288 + ((((k) ^ (hi)) & 1) << 14))
#define O_MEM 0
#define O_SMM 32768
#define O_MEMX 16384
#define O_MZ 49152
#define O_RX 65536
#define HB_SZ (3 << 20)
#define O_PROBE 7936
#define O_PAR (2 << 20)
#define O_FGU (O_PAR + 1024)
#define O_FWV (O_FGU + 4096)
#define O_CF (O_FWV + 1024)
#define O_CNT (O_CF + 32768)
#define O_EPOCH (O_CNT + 32768)
#define SPIN_MAX (1u << 18)
#define LOG2E 1.4426950408889634f
#define FF 0xFFFFFFFFu
#define OOB 0x7f000000

struct Args {
    const u16 *Au, *AlogA, *Aim, *ACr, *ACi, *Alogdt, *ADsk, *X, *mem0, *Wq, *Wk, *Wv, *Wo;
    u16* out;
    char* HB;
    u16 *WvoF, *WqkF, *GF, *UF, *EX, *UM, *TF, *PA; float *MZX;
    unsigned* err;
};
typedef const Args __attribute__((address_space(4))) CArgs;
DI CArgs& get_args() { CArgs* p = (CArgs*)__builtin_amdgcn_kernarg_segment_ptr(); asm volatile("" : "+s"(p)); return *p; }
#define L_TS (112 * 1024)
#define TSDECL
#define TS(k) do { } while (0)
#define TSR(k) do { } while (0)
#define TSV(k, v) do { } while (0)
#define TSF(k) do { } while (0)
#define TSW() do { } while (0)
#define TSEND(s) do { } while (0)
#define TSDUMP() do { } while (0)

extern __shared__ __attribute__((aligned(16))) char smem_[];

DI int get_par(u4 r, unsigned tag) {
    unsigned v = 0;
#pragma nounroll
    for (unsigned sp = 0; sp < SPIN_MAX; ++sp) {
        v = ld32(r, O_PAR, 0); WAITV(0, "+v"(v));
        v = __builtin_amdgcn_readfirstlane(v) ^ tag;
        if (v <= 3u) break;
        asm volatile("s_sleep 4" ::: "memory");
    }
    return v & 3;
}

DI unsigned now32() { u64 t; asm volatile("s_memtime %0\n\ts_waitcnt lgkmcnt(0)" : "=s"(t) :: "memory"); return (unsigned)t; }
DI void wait_until(unsigned t0, unsigned d) {
#pragma nounroll
    while (now32() - t0 < d) { }
}
DI float vmax3(float a, float b, float c) { float r; asm("v_max3_f32 %0, %1, %2, %3" : "=v"(r) : "v"(a), "v"(b), "v"(c)); return r; }
DI float vmax(float a, float b) { float r; asm("v_max_f32 %0, %1, %2" : "=v"(r) : "v"(a), "v"(b)); return r; }
DI float vadd(unsigned a, unsigned b) { float r; asm volatile("v_add_f32 %0, %1, %2" : "=v"(r) : "v"(a), "v"(b)); return r; }
DI void mf_acc(f4& y, u4 a, u4 b) { asm volatile("v_mfma_f32_16x16x32_bf16 %0, %1, %2, %0" : "+v"(y) : "v"(a), "v"(b)); }
DI void mf_acc0(f4& y, u4 a, u4 b) { asm volatile("v_mfma_f32_16x16x32_bf16 %0, %1, %2, 0" : "=&v"(y) : "v"(a), "v"(b)); }
DI void smf_v(f4& y, u4 a, u8v w, int idx) { asm volatile("v_smfmac_f32_16x16x64_bf16 %0, %1, %2, %3" : "+v"(y) : "v"(a), "v"(w), "v"(idx)); }
DI void smf32(f4& y, u2 a, u4 w, int idx) { asm volatile("v_smfmac_f32_16x16x32_bf16 %0, %1, %2, %3" : "+v"(y) : "v"(a), "v"(w), "v"(idx)); }
DI float vmax_ror8(float a) { float r; asm volatile("s_nop 1\n\tv_max_f32_dpp %0, %1, %1 row_ror:8 row_mask:0xf bank_mask:0xf" : "=v"(r) : "v"(a)); return r; }
DI float vadd_ror8(float a) { float r; asm volatile("s_nop 1\n\tv_add_f32_dpp %0, %1, %1 row_ror:8 row_mask:0xf bank_mask:0xf" : "=v"(r) : "v"(a)); return r; }
DI void smf_a(f4& y, u4 a, u8v w, int idx) { asm volatile("v_smfmac_f32_16x16x64_bf16 %0, %1, %2, %3" : "+v"(y) : "v"(a), "a"(w), "v"(idx)); }
#define NOP2() asm volatile("s_nop 1")
#define MFW1(x) asm volatile("s_nop 7" : "+v"(x))
#define MFW2(x, y) asm volatile("s_nop 7" : "+v"(x), "+v"(y))
#define ORD2(x, y) asm volatile("" : "+v"(x), "+v"(y))
DI u2 pair_pack(f4 a, f4 b) {
    float x0 = swapadd32(a[0], b[0]), x1 = swapadd32(a[1], b[1]), x2 = swapadd32(a[2], b[2]), x3 = swapadd32(a[3], b[3]);
    return (u2){pk2(x0, x1), pk2(x2, x3)};
}

#define PSTAMP(k) do { } while (0)
#define PDECL
#define PCTL(s, v) do { } while (0)
#define PPROBE(b) do { } while (0)
#define PCAUSE do { } while (0)
#define PLATE(s, a_, b_) do { } while (0)
#define PSD
#define PENTRY
#define PSTAMP0() do { } while (0)
#define PS(k) do { } while (0)
#define PSDUMP(o) do { } while (0)
#define PSTEP(s) do { } while (0)
#define PCNT1(s, v) do { } while (0)
#define PCNT2(s, v) do { } while (0)
#define PDUMP() do { } while (0)
#define P_XSLP 32
#define P_ASLP 8
typedef __attribute__((ext_vector_type(16))) float f16v;
DI f16v mf32(u4 a, u4 b, f16v c) { return __builtin_amdgcn_mfma_f32_32x32x16_bf16(__builtin_bit_cast(bf8, a), __builtin_bit_cast(bf8, b), c, 0, 0, 0); }
DI u4 pack8(const f16v& x, int s, float sc) { return (u4){pk2(x[8 * s] * sc, x[8 * s + 1] * sc), pk2(x[8 * s + 2] * sc, x[8 * s + 3] * sc), pk2(x[8 * s + 4] * sc, x[8 * s + 5] * sc), pk2(x[8 * s + 6] * sc, x[8 * s + 7] * sc)}; }
DI u4 pack8(const f16v& x, int s) { return (u4){pk2(x[8 * s], x[8 * s + 1]), pk2(x[8 * s + 2], x[8 * s + 3]), pk2(x[8 * s + 4], x[8 * s + 5]), pk2(x[8 * s + 6], x[8 * s + 7])}; }
DI u4 ldn128(u4 srd, int voff, int soff) { u4 v; asm volatile("s_nop 4\n\tbuffer_load_dwordx4 %0, %1, %2, %3 offen" : "=v"(v) : "v"(voff), "s"(srd), "s"(soff) : "memory"); return v; }
DI void dma_pair(u4 srd, int v0, int v1, int soff, unsigned m) {
    asm volatile("s_mov_b32 m0, %4\n\ts_nop 0\n\tbuffer_load_dwordx4 %0, %2, %3 offen lds\n\tbuffer_load_dwordx4 %1, %2, %3 offen offset:1024 lds" :: "v"(v0), "v"(v1), "s"(srd), "s"(soff), "s"(m) : "memory");
}
DI void dma_one(u4 srd, int v0, int soff, unsigned m) {
    asm volatile("s_mov_b32 m0, %3\n\ts_nop 0\n\tbuffer_load_dwordx4 %0, %1, %2 offen lds" :: "v"(v0), "s"(srd), "s"(soff), "s"(m) : "memory");
}
DI void mA_vv(f16v& c, u4 x, u4 y) { asm volatile("v_mfma_f32_32x32x16_bf16 %0, %1, %2, %0" : "+a"(c) : "v"(x), "v"(y)); }
DI void mA_vv0(f16v& c, u4 x, u4 y) { asm volatile("v_mfma_f32_32x32x16_bf16 %0, %1, %2, 0" : "=&a"(c) : "v"(x), "v"(y)); }
DI void mV_va(f16v& c, u4 x, u4 y) { asm volatile("v_mfma_f32_32x32x16_bf16 %0, %1, %2, %0" : "+v"(c) : "v"(x), "a"(y)); }
DI void mV_va0(f16v& c, u4 x, u4 y) { asm volatile("v_mfma_f32_32x32x16_bf16 %0, %1, %2, 0" : "=&v"(c) : "v"(x), "a"(y)); }
DI void mV_av(f16v& c, u4 x, u4 y) { asm volatile("v_mfma_f32_32x32x16_bf16 %0, %1, %2, %0" : "+v"(c) : "a"(x), "v"(y)); }
DI void mV_av0(f16v& c, u4 x, u4 y) { asm volatile("v_mfma_f32_32x32x16_bf16 %0, %1, %2, 0" : "=&v"(c) : "a"(x), "v"(y)); }
DI void mA_va(f16v& c, u4 x, u4 y) { asm volatile("v_mfma_f32_32x32x16_bf16 %0, %1, %2, %0" : "+a"(c) : "v"(x), "a"(y)); }
DI void mA_va0(f16v& c, u4 x, u4 y) { asm volatile("v_mfma_f32_32x32x16_bf16 %0, %1, %2, 0" : "=&a"(c) : "v"(x), "a"(y)); }
DI void mV_vv(f16v& c, u4 x, u4 y) { asm volatile("v_mfma_f32_32x32x16_bf16 %0, %1, %2, %0" : "+v"(c) : "v"(x), "v"(y)); }
DI void mV_vv0(f16v& c, u4 x, u4 y) { asm volatile("v_mfma_f32_32x32x16_bf16 %0, %1, %2, 0" : "=&v"(c) : "v"(x), "v"(y)); }
template <int N, int PD, class RD, class MM, class EX> DI void pipe(RD rd, MM mm, EX ex) {
    u4 f[PD];
#pragma unroll
    for (int j = 0; j < PD; ++j) f[j] = rd(j);
    SB();
#pragma unroll
    for (int j = 0; j < N; ++j) { mm(j, f[j % PD]); if (j + PD < N) f[j % PD] = rd(j + PD); ex(j); SB(); }
}
DI u4 sld128(u4 srd, int voff, int soff) { u4 v; asm volatile("s_nop 4\n\tbuffer_load_dwordx4 %0, %1, %2, %3 offen sc1" : "=v"(v) : "v"(voff), "s"(srd), "s"(soff) : "memory"); return v; }
DI u2 sld64(u4 srd, int voff, int soff) { u2 v; asm volatile("s_nop 4\n\tbuffer_load_dwordx2 %0, %1, %2, %3 offen sc1" : "=v"(v) : "v"(voff), "s"(srd), "s"(soff) : "memory"); return v; }
DI void sst128(u4 v, u4 srd, int voff, int soff) { asm volatile("s_nop 4\n\tbuffer_store_dwordx4 %0, %1, %2, %3 offen sc1\n\ts_nop 1" :: "v"(v), "v"(voff), "s"(srd), "s"(soff) : "memory"); }
DI void sst64(u2 v, u4 srd, int voff, int soff) { asm volatile("s_nop 4\n\tbuffer_store_dwordx2 %0, %1, %2, %3 offen sc1" :: "v"(v), "v"(voff), "s"(srd), "s"(soff) : "memory"); }
DI void sst32(unsigned v, u4 srd, int voff, int soff) { asm volatile("s_nop 4\n\tbuffer_store_dword %0, %1, %2, %3 offen sc1" :: "v"(v), "v"(voff), "s"(srd), "s"(soff) : "memory"); }
DI unsigned sld32(u4 srd, int voff, int soff) { unsigned v; asm volatile("s_nop 4\n\tbuffer_load_dword %0, %1, %2, %3 offen sc1" : "=v"(v) : "v"(voff), "s"(srd), "s"(soff) : "memory"); return v; }
template <int N> DI void waitvm() { asm volatile("s_waitcnt vmcnt(%0)" :: "i"(N) : "memory"); }
DI int swzr(int row) { return ((row & 1) << 2) | (row & 8) | ((row >> 1) & 3); }
#define SLICE 32768
#define CSCALE (LOG2E * 0.0625f)

struct SideCtx {
    char LAS* sm; unsigned smb; int lane, wave;
    int vr0, vr1, vc0, vc1;
};
DI int fresh_lane() { int l; asm volatile("v_mbcnt_lo_u32_b32 %0, -1, 0\n\tv_mbcnt_hi_u32_b32 %0, -1, %0" : "=v"(l)); __builtin_assume(l >= 0 && l < 64); return l; }
DI SideCtx make_ctx(int wave) {
    SideCtx c; c.sm = (char LAS*)smem_; c.smb = (unsigned)(size_t)c.sm; c.lane = fresh_lane(); c.wave = wave;
    asm volatile("" : "+s"(c.wave), "+s"(c.smb));
    const int l = c.lane;
    { int row = 4 * c.wave + (l >> 5); c.vr0 = row * 8192 + (((l & 31) ^ swzr(row)) * 16); row += 2; c.vr1 = row * 8192 + (((l & 31) ^ swzr(row)) * 16) - 1024; }
    { int row = l >> 3; c.vc0 = row * 896 + (((l & 7) ^ (((row >> 1) & 1) << 2)) * 16); c.vc1 = c.vc0 + 8 * 896 - 1024; }
    return c;
}
DI void dma_rows_q(const SideCtx& c, u4 srd, int h, int r0, int slot, int g) { dma_pair(srd, c.vr0, c.vr1, (r0 + 16 * g) * 8192 + h * 512, c.smb + slot * SLICE + (8 * g + 2 * c.wave) * 1024); }
DI void dma_cols_q(const SideCtx& c, u4 srd, int h, int c0, int slot, int g) { const int k = 8 * c.wave + 2 * g; dma_pair(srd, c.vc0, c.vc1, (h * 256 + 8 * k) * 896 + c0 * 2, c.smb + slot * SLICE + k * 1024); }
DI void dma_rows(const SideCtx& c, u4 srd, int h, int r0, int slot) {
#pragma unroll
    for (int g = 0; g < 4; ++g) dma_rows_q(c, srd, h, r0, slot, g);
}
DI void dma_cols(const SideCtx& c, u4 srd, int h, int c0, int slot) {
#pragma unroll
    for (int g = 0; g < 4; ++g) dma_cols_q(c, srd, h, c0, slot, g);
}
DI u4 tr_rows(const SideCtx& c, int slot, int ks, int mt) {
    const int l = c.lane, hi = l >> 5, blk = (l >> 4) & 1, r = (l & 15) >> 2, p = l & 3, pp = ((p & 1) << 1) | (p >> 1);
    u2 o[2];
#pragma unroll
    for (int t = 0; t < 2; ++t) {
        const int rowl = 4 * hi + 2 * t + (r & 1) + 8 * (r >> 1);
        const int ch = (4 * (mt & 3) + 2 * blk + (pp >> 1)) ^ swzr(rowl);
        o[t] = trread(c.sm + (rowl * 512 + ch * 16 + 8 * (pp & 1)) + (slot * SLICE + ks * 8192 + (mt >> 2) * 256));
    }
    return (u4){o[0][0], o[0][1], o[1][0], o[1][1]};
}
DI u4 rd_rows(const SideCtx& c, int slot, int ks, int tau) {
    const int l = c.lane, hi = l >> 5, ro = l & 31;
    const int rowc = 32 * (ro >> 4) + 4 * ((ro >> 2) & 1) + 8 * ((ro >> 1) & 1) + 2 * ((ro >> 3) & 1) + (ro & 1);
    const int ch = (2 * (ks & 7) + hi) ^ swzr(rowc);
    return *(const u4 LAS*)(c.sm + (rowc * 512 + ch * 16) + (slot * SLICE + tau * 8192 + (ks >> 3) * 256));
}
DI u4 rd_rows_nat(const SideCtx& c, int base, int ks) {
    const int l = c.lane, hi = l >> 5, ro = l & 31;
    return *(const u4 LAS*)(c.sm + base + (ro * 512 + ((2 * (ks & 7) + hi) ^ swzr(ro)) * 16) + (ks >> 3) * 256);
}
DI u4 tr_cols(const SideCtx& c, int slot, int ks, int nt) {
    const int l = c.lane, hi = l >> 5, blk = (l >> 4) & 1, r = (l & 15) >> 2, p = l & 3;
    u2 o[2];
#pragma unroll
    for (int t = 0; t < 2; ++t) {
        const int ch = (4 * nt + 2 * blk + (p >> 1)) ^ (((r >> 1) & 1) << 2);
        o[t] = trread(c.sm + ((8 * hi + r) * 128 + ch * 16 + 8 * (p & 1)) + (slot * SLICE + ks * 2048 + t * 512));
    }
    return (u4){o[0][0], o[0][1], o[1][0], o[1][1]};
}
DI void set_flag(const SideCtx& c, u4 rhb, int off, unsigned tag) {
    waitvm<0>(); bar();
    if (c.wave == 0) sst32(tag, rhb, c.lane == 0 ? off : OOB, 0);
}

#define MFW32(x, y) asm volatile("s_nop 11" : "+v"(x), "+v"(y))
DI void gu_first(int wave, int type, int h, int tile) {
    CArgs& a = get_args();
    const SideCtx c = make_ctx(wave);
    h = __builtin_amdgcn_readfirstlane(h); tile = __builtin_amdgcn_readfirstlane(tile); asm volatile("" : "+s"(h), "+s"(tile));
    const int s = tile * 4 + c.wave;
    const u4 rw1 = mksrd(type == 0 ? a.Wk : a.Wv, 448 * 8192), rx = mksrd(a.X, 2048 * 896);
    const unsigned xb = c.smb + SLICE + c.wave * 28672;
#pragma unroll
    for (int k = 0; k < 14; ++k) {
        const int n = 64 * k + c.lane, row = (n * 1171) >> 16, pos = n - 56 * row;
        const int vo = row * 896 + ((pos ^ ((row >> 1) & 7)) * 16);
        dma_one(rx, vo, s * 32 * 896, xb + k * 1024); dma_one(rx, vo, (s * 32 + 16) * 896, xb + 14336 + k * 1024);
    }
    dma_rows(c, rw1, h, 0, 0);
}
DI void w_first(int wave, int kind, int h, int sub, int u0) {
    CArgs& a = get_args();
    const SideCtx c = make_ctx(wave);
    h = __builtin_amdgcn_readfirstlane(h); sub = __builtin_amdgcn_readfirstlane(sub); asm volatile("" : "+s"(h), "+s"(sub));
    const u4 rw1 = mksrd(kind == 1 ? a.Wv : a.Wk, 448 * 8192), rw2 = mksrd(kind == 1 ? a.Wo : a.Wq, 448 * 8192);
    if (kind == 1) { dma_cols(c, rw2, h, sub * 128, 2); dma_cols(c, rw2, h, sub == 3 ? 384 : sub * 128 + 64, 3); }
    else {
        const int r0 = sub * 224 + (kind == 0 ? 128 : 0);
#pragma unroll
        for (int g = 0; g < 6; ++g) dma_rows_q(c, rw2, h, r0 + 64 * (g >> 2), 2 + (g >> 2), g & 3);
        if (kind == 2) { dma_rows_q(c, rw2, h, r0 + 64, 3, 2); dma_rows_q(c, rw2, h, r0 + 64, 3, 3); }
    }
    dma_rows(c, rw1, h, 64 * u0, 0); dma_rows(c, rw1, h, 64 * u0 + 64, 1);
}
template <int TYPE>
DI void gu_task(unsigned tag, int vb, int wave, int h, int tile) {
    CArgs& a = get_args();
    const SideCtx c = make_ctx(wave);
    const int lane = c.lane, hi = lane >> 5, l31 = lane & 31;
    asm volatile("" : "+s"(h));
    const int s = tile * 4 + c.wave;
    const u4 rw1 = mksrd(TYPE == 0 ? a.Wk : a.Wv, 448 * 8192), rw2 = mksrd(TYPE == 0 ? a.Wq : a.Wo, 448 * 8192);
    const u4 rout = mksrd(TYPE == 0 ? a.GF : a.UF, 64 * 16 * 28 * 1024);
    u4 xf[28];
    auto issue = [&](int i, int g) { if (i < 7) dma_rows_q(c, rw1, h, 64 * i, i & 3, g); else if (TYPE == 0) dma_rows_q(c, rw2, h, 64 * (i - 7), i & 3, g); else dma_cols_q(c, rw2, h, 64 * (i - 7), i & 3, g); };
    {
        waitvm<8>();
#pragma unroll
        for (int k = 0; k < 28; ++k) {
            const char LAS* b = c.sm + SLICE + c.wave * 28672 + l31 * 896 + hi * 8 + (k >> 2) * 128;
            const u2 c0 = *(const u2 LAS*)(b + ((2 * (k & 3)) ^ ((l31 >> 1) & 7)) * 16), c1 = *(const u2 LAS*)(b + ((2 * (k & 3) + 1) ^ ((l31 >> 1) & 7)) * 16);
            xf[k] = (u4){c0[0], c1[0], c0[1], c1[1]};
        }
        bar();
#pragma unroll
        for (int g = 0; g < 8; ++g) issue(1 + (g >> 2), g & 3);
#pragma unroll
        for (int k = 0; k < 28; ++k) asm volatile("" : "+a"(xf[k]));
    }
    f16v acc[8];
    PSD PS(0);
#pragma unroll
    for (int i = 0; i < 7; ++i) {
        PS(1 + 2 * i); waitvm<16>(); bar(); PS(2 + 2 * i);
        pipe<32, 4>([&](int j) { return tr_rows(c, i & 3, j >> 3, j & 7); },
                    [&](int j, u4 f) { if (i == 0 && j < 8) mV_va0(acc[j & 7], f, xf[4 * i + (j >> 3)]); else mV_va(acc[j & 7], f, xf[4 * i + (j >> 3)]); },
                    [&](int j) { if ((j & 7) == 2) issue(i + 3, j >> 3); });
    }
    asm volatile("s_nop 11" : "+v"(acc[0]), "+v"(acc[1]), "+v"(acc[2]), "+v"(acc[3]), "+v"(acc[4]), "+v"(acc[5]), "+v"(acc[6]), "+v"(acc[7]));
    u4 kf[16];
#pragma unroll
    for (int m = 0; m < 8; ++m) {
        if (TYPE == 0) { kf[2 * m] = pack8(acc[m], 0, CSCALE); kf[2 * m + 1] = pack8(acc[m], 1, CSCALE); } else { kf[2 * m] = pack8(acc[m], 0); kf[2 * m + 1] = pack8(acc[m], 1); }
        asm volatile("" : "+v"(kf[2 * m]), "+v"(kf[2 * m + 1]));
    }
    f16v sx;
    const int o_st = TYPE == 0 ? (l31 >> 4) * 14336 + (hi * 16 + (l31 & 15)) * 16 : (l31 >> 4) * 1024 + (hi * 16 + (l31 & 15)) * 16;
    const int so_st = (s * 16 + h) * 28 * 1024;
    f16v d[2][2];
    auto epi = [&](int v, int e) {
        u4 p = pack8(d[v & 1][e & 1], e >> 1);
        if (TYPE == 0) { st128(p, rout, o_st, so_st + (v * 2 + (e >> 1)) * 1024 + (e & 1) * 512);
            if (v == 0 && e == 0) mA_va0(sx, p, xf[4 * v + e]); else mA_va(sx, p, xf[4 * v + e]); }
        else st128(p, rout, o_st, so_st + (4 * v + 2 * (e & 1)) * 1024 + (e >> 1) * 512);
    };
#pragma unroll
    for (int u = 0; u < 7; ++u) {
        const int i = 7 + u;
        PS(1 + 2 * i);
        if (u <= 1) waitvm<16>(); else if (u == 2) waitvm<20>(); else if (u <= 4) waitvm<24>(); else if (u == 5) waitvm<16>(); else waitvm<8>();
        bar(); PS(2 + 2 * i);
        pipe<32, 4>([&](int j) { return TYPE == 0 ? rd_rows(c, i & 3, j >> 1, j & 1) : tr_cols(c, i & 3, j >> 1, j & 1); },
                    [&](int j, u4 f) { if (TYPE == 0) { if (j < 2) mV_vv0(d[u & 1][j & 1], f, kf[j >> 1]); else mV_vv(d[u & 1][j & 1], f, kf[j >> 1]); }
                                       else { if (j < 2) mV_vv0(d[u & 1][j & 1], kf[j >> 1], f); else mV_vv(d[u & 1][j & 1], kf[j >> 1], f); } },
                    [&](int j) { if (i + 3 < 14 && (j & 7) == 2) issue(i + 3, j >> 3);
                                 if (u > 0 && (j & 7) == 6) { if (j == 6) asm volatile("s_nop 3" : "+v"(d[(u - 1) & 1][0]), "+v"(d[(u - 1) & 1][1])); epi(u - 1, j >> 3); } });
    }
    MFW32(d[0][0], d[0][1]);
#pragma unroll
    for (int e = 0; e < 4; ++e) epi(6, e);
    set_flag(c, mksrd(a.HB, HB_SZ), O_FGU + tile * 256 + TYPE * 64 + h * 4, tag);
    if (TYPE == 0) {
        asm volatile("s_nop 11" : "+a"(sx));
        float m = -3.0e38f;
#pragma unroll
        for (int j = 0; j < 16; ++j) { const int key = (j & 3) + 8 * (j >> 2) + 4 * hi; if (key > l31) sx[j] = -INFINITY; m = fmaxf(m, sx[j]); }
        m = x32max(m);
        float z = 0.f;
#pragma unroll
        for (int j = 0; j < 16; ++j) { sx[j] = __builtin_amdgcn_exp2f(sx[j] - m); z += sx[j]; }
        z = x32add(z);
        const u4 rex = mksrd(a.EX, 64 * 16 * 2048), rmz = mksrd(a.MZX, 64 * 16 * 256);
        sst128(pack8(sx, 0), rex, lane * 16, (s * 16 + h) * 2048); sst128(pack8(sx, 1), rex, lane * 16, (s * 16 + h) * 2048 + 1024);
        sst64((u2){__float_as_uint(m), __float_as_uint(z)}, rmz, lane < 32 ? lane * 8 : OOB, (s * 16 + h) * 256);
    }
    PS(29);
    if (TYPE == 0) set_flag(c, mksrd(a.HB, HB_SZ), O_FGU + tile * 256 + 128 + h * 4, tag);
    PS(30); PSDUMP(64 + 64 * (tile >> 2));
}

template <int TYPE>
DI void w_task(unsigned tag, int vb, int wave, int h, int sub) {
    CArgs& a = get_args();
    const SideCtx c = make_ctx(wave);
    const int lane = c.lane, hi = lane >> 5, l31 = lane & 31;
    h = __builtin_amdgcn_readfirstlane(h); sub = __builtin_amdgcn_readfirstlane(sub); asm volatile("" : "+s"(h), "+s"(sub));
    const u4 rw1 = mksrd(TYPE ? a.Wv : a.Wk, 448 * 8192);
    u4 kf[16];
    waitvm<16>(); bar();
#pragma unroll
    for (int ks = 0; ks < 16; ++ks) kf[ks] = TYPE ? tr_cols(c, 2 + (c.wave >> 1), ks, c.wave & 1) : rd_rows_nat(c, 2 * SLICE + (c.wave < 3 ? c.wave : 0) * 16384, ks);
    bar();
    dma_rows(c, rw1, h, 128, 2);
    const u4 rout = TYPE ? mksrd(a.WvoF, 28 * 16 * 14336) : mksrd(a.WqkF, 32 * 6 * 14336);
    const int c16 = sub * 8 + c.wave * 2 + (l31 >> 4);
    const int o_st = (TYPE ? (c16 > 27 ? OOB : (c16 * 16 + h) * 14336) : (c.wave == 3 ? OOB : ((h * 2 + sub) * 6 + c.wave * 2 + (l31 >> 4)) * 14336)) + (hi * 16 + (l31 & 15)) * 16;
    f16v d[2][2];
    auto epi = [&](int v, int e) { st128(pack8(d[v & 1][e & 1], e >> 1), rout, o_st, (v * 2 + (e >> 1)) * 1024 + (e & 1) * 512); };
#pragma unroll
    for (int u = 0; u < 7; ++u) {
        if (u <= 1) waitvm<16>(); else if (u == 2) waitvm<20>(); else if (u <= 4) waitvm<24>(); else if (u == 5) waitvm<16>(); else waitvm<8>();
        bar();
        pipe<32, 4>([&](int j) { return rd_rows(c, u & 3, j >> 1, j & 1); },
                    [&](int j, u4 f) { if (j < 2) mV_vv0(d[u & 1][j & 1], f, kf[j >> 1]); else mV_vv(d[u & 1][j & 1], f, kf[j >> 1]); },
                    [&](int j) { if (u + 3 < 7 && (j & 7) == 2) dma_rows_q(c, rw1, h, 64 * (u + 3), (u + 3) & 3, j >> 3);
                                 if (u > 0 && (j & 7) == 6) { if (j == 6) asm volatile("s_nop 3" : "+v"(d[(u - 1) & 1][0]), "+v"(d[(u - 1) & 1][1])); epi(u - 1, j >> 3); } });
    }
    MFW32(d[0][0], d[0][1]);
#pragma unroll
    for (int e = 0; e < 4; ++e) epi(6, e);
    set_flag(c, mksrd(a.HB, HB_SZ), TYPE ? O_FWV + sub * 64 + h * 4 : O_FWV + 256 + (h * 2 + sub) * 4, tag);
}

DI void wv_half(unsigned tag, int wave, int h, int sub, int u0) {
    CArgs& a = get_args();
    const SideCtx c = make_ctx(wave);
    const int lane = c.lane, hi = lane >> 5, l31 = lane & 31;
    h = __builtin_amdgcn_readfirstlane(h); sub = __builtin_amdgcn_readfirstlane(sub); u0 = __builtin_amdgcn_readfirstlane(u0); asm volatile("" : "+s"(h), "+s"(sub), "+s"(u0));
    const u4 rw1 = mksrd(a.Wv, 448 * 8192);
    u4 kf[16];
    waitvm<16>(); bar();
#pragma unroll
    for (int ks = 0; ks < 16; ++ks) kf[ks] = tr_cols(c, 2 + (c.wave >> 1), ks, c.wave & 1);
    bar();
    dma_rows(c, rw1, h, 64 * u0 + 128, 2); dma_rows(c, rw1, h, u0 ? 384 : 192, 3);
    const u4 rout = mksrd(a.WvoF, 28 * 16 * 14336);
    const int c16 = sub * 8 + c.wave * 2 + (l31 >> 4);
    const int o_st = (c16 > 27 ? OOB : (c16 * 16 + h) * 14336) + (hi * 16 + (l31 & 15)) * 16;
    f16v d[2][2];
    auto epi = [&](int v, int e) { st128(pack8(d[v & 1][e & 1], e >> 1), rout, o_st, ((u0 + v) * 2 + (e >> 1)) * 1024 + (e & 1) * 512); };
#pragma unroll
    for (int u = 0; u < 4; ++u) {
        if (u == 3 && u0) break;
        if (u == 0) waitvm<24>(); else if (u == 1) waitvm<16>(); else if (u == 2) waitvm<12>(); else waitvm<8>();
        bar();
        pipe<32, 4>([&](int j) { return rd_rows(c, u, j >> 1, j & 1); },
                    [&](int j, u4 f) { if (j < 2) mV_vv0(d[u & 1][j & 1], f, kf[j >> 1]); else mV_vv(d[u & 1][j & 1], f, kf[j >> 1]); },
                    [&](int j) { if (u > 0 && u < 3 && (j & 7) == 6) { if (j == 6) asm volatile("s_nop 3" : "+v"(d[(u - 1) & 1][0]), "+v"(d[(u - 1) & 1][1])); epi(u - 1, j >> 3); } });
        if (u >= 2) {
            MFW32(d[u & 1][0], d[u & 1][1]);
#pragma unroll
            for (int e = 0; e < 4; ++e) epi(u, e);
        }
    }
    set_flag(c, mksrd(a.HB, HB_SZ), O_FWV + (u0 ? 768 : 0) + sub * 64 + h * 4, tag);
}

DI void wait_flags(CArgs& a, u4 rhb, int voff_or_oob, unsigned tag, int code) {
    unsigned sp = 0;
#pragma nounroll
    for (;;) {
        unsigned v = sld32(rhb, voff_or_oob, 0); WAITV(0, "+v"(v));
        if (__all((v == tag) | (voff_or_oob >= OOB))) break;
        asm volatile("s_sleep 4" ::: "memory");
        if (++sp > SPIN_MAX) { a.err[0] = code; __builtin_amdgcn_endpgm(); }
    }
}

DI void slp(int n) {
#pragma nounroll
    for (int i = 0; i < n; i += 8) asm volatile("s_sleep 8");
}
DI void x_task(unsigned tag, int vb, int wave, int s, int cb) {
    CArgs& a = get_args();
    const SideCtx c = make_ctx(wave);
    const int lane = c.lane, hi = lane >> 5, l31 = lane & 31, w = c.wave;
    const u4 rhb = mksrd(a.HB, HB_SZ);
    const u4 ru = mksrd(a.UF, 64 * 16 * 28 * 1024), rex = mksrd(a.EX, 64 * 16 * 2048), rmz = mksrd(a.MZX, 64 * 16 * 256);
    const u4 rum = mksrd(a.UM, 65 * 28 * 4096), rtf = mksrd(a.TF, 65 * 32 * 3584), rx = mksrd(a.X, 2048 * 896);
    bar();
#pragma unroll
    for (int k = 0; k < 7; ++k) {
        const int n = 64 * (7 * (w & 1) + k) + lane, row = (n * 1171) >> 16, pos = n - 56 * row;
        dma_one(rx, row * 896 + ((pos ^ ((row >> 1) & 7)) * 16), (s * 32 + 16 * (w >> 1)) * 896, c.smb + 40960 + (7 * w + k) * 1024);
    }
    wait_flags(a, rhb, lane < 32 ? O_FGU + (s >> 2) * 256 + 64 + lane * 4 : OOB, tag, 10);
    u4 uf[4][2][2], ex[4][2]; u2 mz[4];
    const int o_u = (l31 >> 4) * 1024 + hi * 256 + (l31 & 15) * 16;
#pragma unroll
    for (int k = 0; k < 4; ++k) {
        const int sh = s * 16 + 4 * w + k;
#pragma unroll
        for (int sg = 0; sg < 2; ++sg) {
            ex[k][sg] = sld128(rex, lane * 16, sh * 2048 + sg * 1024);
#pragma unroll
            for (int nt = 0; nt < 2; ++nt) uf[k][sg][nt] = sld128(ru, o_u, (sh * 28 + 4 * cb + 2 * nt) * 1024 + sg * 512);
        }
        mz[k] = sld64(rmz, l31 * 8, sh * 256);
        asm volatile("s_sleep %0" :: "i"(P_XSLP));
    }
    waitvm<0>();
    f16v rr[4][2];
#pragma unroll
    for (int k = 0; k < 4; ++k) {
        asm volatile("" : "+v"(mz[k]), "+v"(ex[k][0]), "+v"(ex[k][1]), "+v"(uf[k][0][0]), "+v"(uf[k][0][1]), "+v"(uf[k][1][0]), "+v"(uf[k][1][1]));
        mV_vv0(rr[k][0], uf[k][0][0], ex[k][0]); mV_vv0(rr[k][1], uf[k][0][1], ex[k][0]);
        mV_vv(rr[k][0], uf[k][1][0], ex[k][1]); mV_vv(rr[k][1], uf[k][1][1], ex[k][1]);
        MFW32(rr[k][0], rr[k][1]);
    }
    bar();
    wait_flags(a, rhb, lane < 32 ? O_CF + s * 512 + lane * 16 : OOB, tag, 11);
    u4 tf[28]; u4 um[2][2];
    const int o_tf = (l31 >> 3) * 7168 + hi * 128 + (l31 & 7) * 16, o_um = (l31 >> 4) * 4096 + hi * 256 + (l31 & 15) * 16;
#pragma unroll
    for (int k = 0; k < 28; ++k) {
        asm volatile("buffer_load_dwordx4 %0, %1, %2, %3 offen sc1" : "=a"(tf[k]) : "v"(o_tf), "s"(rtf), "s"(((s + 1) * 32 + 8 * w) * 3584 + k * 256) : "memory");
        if (k % 7 == 6) slp(s < 58 ? P_XSLP : 0);
    }
#pragma unroll
    for (int k = 0; k < 4; ++k) um[k >> 1][k & 1] = ld128(rum, o_um, (s * 28 + 28 + 4 * cb + 2 * (k & 1)) * 4096 + w * 1024 + (k >> 1) * 512);
    f16v sm_;
    pipe<28, 4>([&](int k) { return *(const u4 LAS*)(c.sm + 40960 + l31 * 896 + (((2 * (k & 3) + hi) ^ ((l31 >> 1) & 7)) * 16) + (k >> 2) * 128); },
                [&](int k, u4 f) { if (k == 0) WAITV(25, "+a"(tf[0]), "+a"(tf[1]), "+a"(tf[2]), "+a"(tf[3]), "+a"(tf[4]), "+a"(tf[5]), "+a"(tf[6]));
                                   if (k == 7) WAITV(18, "+a"(tf[7]), "+a"(tf[8]), "+a"(tf[9]), "+a"(tf[10]), "+a"(tf[11]), "+a"(tf[12]), "+a"(tf[13]));
                                   if (k == 14) WAITV(11, "+a"(tf[14]), "+a"(tf[15]), "+a"(tf[16]), "+a"(tf[17]), "+a"(tf[18]), "+a"(tf[19]), "+a"(tf[20]));
                                   if (k == 21) WAITV(4, "+a"(tf[21]), "+a"(tf[22]), "+a"(tf[23]), "+a"(tf[24]), "+a"(tf[25]), "+a"(tf[26]), "+a"(tf[27]));
                                   if (k == 0) mV_av0(sm_, tf[k], f); else mV_av(sm_, tf[k], f); },
                [&](int) {});
    waitvm<0>();
    asm volatile("s_nop 11" : "+v"(sm_));
#pragma unroll
    for (int k = 0; k < 4; ++k) PIN(um[k >> 1][k & 1]);
    u2 pm[4]; float scx[4];
#pragma unroll
    for (int k = 0; k < 4; ++k) {
        const f4 sc = {sm_[4 * k] * CSCALE, sm_[4 * k + 1] * CSCALE, sm_[4 * k + 2] * CSCALE, sm_[4 * k + 3] * CSCALE};
        const float mx = __uint_as_float(mz[k][0]);
        float m = fmaxf(x32max(fmaxf(fmaxf(sc[0], sc[1]), fmaxf(sc[2], sc[3]))), mx);
        float e[4];
#pragma unroll
        for (int j = 0; j < 4; ++j) e[j] = __builtin_amdgcn_exp2f(sc[j] - m);
        const float cx = __builtin_amdgcn_exp2f(mx - m);
        const float z = x32add((e[0] + e[1]) + (e[2] + e[3])) + cx * __uint_as_float(mz[k][1]);
        const float inv = __builtin_amdgcn_rcpf(z);
        scx[k] = cx * inv;
        pm[k] = (u2){pk2(e[0] * inv, e[1] * inv), pk2(e[2] * inv, e[3] * inv)};
    }
    f16v d0, d1;
    {
        u4 p0 = {pm[0][0], pm[0][1], pm[2][0], pm[2][1]}, p1 = {pm[1][0], pm[1][1], pm[3][0], pm[3][1]};
        asm volatile("s_nop 1\n\tv_mfma_f32_32x32x16_bf16 %0, %2, %6, 0\n\tv_mfma_f32_32x32x16_bf16 %1, %3, %6, 0\n\tv_mfma_f32_32x32x16_bf16 %0, %4, %7, %0\n\tv_mfma_f32_32x32x16_bf16 %1, %5, %7, %1"
                     : "=&v"(d0), "=&v"(d1) : "v"(um[0][0]), "v"(um[0][1]), "v"(um[1][0]), "v"(um[1][1]), "v"(p0), "v"(p1));
    }
    f16v a0 = (rr[0][0] * scx[0] + rr[1][0] * scx[1]) + (rr[2][0] * scx[2] + rr[3][0] * scx[3]);
    f16v a1 = (rr[0][1] * scx[0] + rr[1][1] * scx[1]) + (rr[2][1] * scx[2] + rr[3][1] * scx[3]);
    asm volatile("s_nop 11" : "+v"(d0), "+v"(d1), "+v"(a0), "+v"(a1));
    a0 += d0; a1 += d1;
#pragma unroll
    for (int g = 0; g < 4; ++g) {
        *(f4 LAS*)(c.sm + w * 8704 + l31 * 272 + (8 * g + 4 * hi) * 4) = (f4){a0[4 * g], a0[4 * g + 1], a0[4 * g + 2], a0[4 * g + 3]};
        *(f4 LAS*)(c.sm + w * 8704 + l31 * 272 + 128 + (8 * g + 4 * hi) * 4) = (f4){a1[4 * g], a1[4 * g + 1], a1[4 * g + 2], a1[4 * g + 3]};
    }
    bar();
    {
        const int t = w * 8 + (lane >> 3), c8 = lane & 7;
        f4 s0 = {0.f, 0.f, 0.f, 0.f}, s1 = s0;
#pragma unroll
        for (int p = 0; p < 4; ++p) { s0 += *(const f4 LAS*)(c.sm + p * 8704 + t * 272 + c8 * 32); s1 += *(const f4 LAS*)(c.sm + p * 8704 + t * 272 + c8 * 32 + 16); }
        sst128((u4){pk2(s0[0], s0[1]), pk2(s0[2], s0[3]), pk2(s1[0], s1[1]), pk2(s1[2], s1[3])}, mksrd(a.out, 2048 * 1792), t * 1792 + c8 * 16, s * 32 * 1792 + 896 + cb * 128);
    }
}

#define PA_UT 0
#define PA_TU 33792
#define PA_KH (PA_TU + 35456)
#define PA_KL (PA_KH + 7680)
#define PA_KT (PA_KL + 7680)
#define PA_RED (PA_KT + 12288)
DI float bf2f(u16 x) { return __uint_as_float((unsigned)x << 16); }
DI void parta_task(unsigned tag, int vb, int wave, int task) {
    CArgs& a = get_args();
    const SideCtx c = make_ctx(wave);
    char LAS* sm = c.sm;
    const int lane = c.lane, w = c.wave, tid = w * 64 + lane, hi = lane >> 5, l31 = lane & 31, l15 = lane & 15, kk = lane >> 4;
    const int d0 = task * 8;
    const u4 rpa = mksrd(a.PA, 56 * 32768);
    bar();
#pragma unroll
    for (int i = 0; i < 8; ++i) {
        const int l = tid + 256 * i;
        const u4 v = *(const u4*)(a.Au + (size_t)l * 448 + d0);
#pragma unroll
        for (int ch = 0; ch < 8; ++ch) *(u16 LAS*)(sm + PA_UT + ch * 4224 + (2048 - l) * 2) = (u16)(v[ch >> 1] >> (16 * (ch & 1)));
    }
    for (int i = tid; i < 8 * 64; i += 256) { const int ch = i >> 6, m = i & 63; *(u16 LAS*)(sm + PA_UT + ch * 4224 + (m == 0 ? 0 : (2048 + m) * 2)) = 0; }
    for (int i = tid; i < 2 * 31 * 5; i += 256) { const int x = i / 155, r = i % 155; *(u4 LAS*)(sm + (x ? PA_KL : PA_KH) + r * 16) = (u4){0u, 0u, 0u, 0u}; }
    int toff[8];
#pragma unroll
    for (int j = 0; j < 8; ++j) {
        const int p = (int)bfr((float)(tid * 8 + j));
        int col, ar = (p & 255) >> 4;
        if (p < 256) col = p & 15; else if (p < 512) col = 16 + ((p & 15) >> 1); else if (p < 1024) col = 24 + ((p - 512) >> 8) * 4 + ((p & 15) >> 2);
        else if (p < 2048) col = 32 + ((p - 1024) >> 8) * 2 + ((p & 15) >> 3); else col = 40;
        toff[j] = (col * 16 + ar) * 4;
    }
    float qpos[3];
    qpos[0] = (float)l15;
    qpos[1] = l15 < 8 ? (float)(256 + 2 * l15) : (float)(512 + ((l15 - 8) >> 2) * 256 + (l15 & 3) * 4);
    qpos[2] = l15 < 8 ? (float)(1024 + (l15 >> 1) * 256 + (l15 & 1) * 8) : 2048.f;
    const float ppos = (float)(16 * l15);
#pragma nounroll
    for (int grp = 0; grp < 2; ++grp) {
        {
            const int d = d0 + 4 * grp + w;
            const float dt = bfr(expf(bf2f(a.Alogdt[d])));
            char LAS* pt = sm + PA_RED + w * 9216;
#pragma unroll
            for (int i = 0; i < 4; ++i) {
                const int n = lane + 64 * i;
                const float ar = -bfr(expf(bf2f(a.AlogA[d * 256 + n])));
                *(f4 LAS*)(pt + n * 16) = (f4){ar * dt * LOG2E, bf2f(a.Aim[d * 256 + n]) * dt * 0.15915494309189535f, 2.f * bf2f(a.ACr[d * 256 + n]) * dt, 2.f * bf2f(a.ACi[d * 256 + n]) * dt};
            }
            f4 acc[3] = {{0.f, 0.f, 0.f, 0.f}, {0.f, 0.f, 0.f, 0.f}, {0.f, 0.f, 0.f, 0.f}};
#pragma unroll 2
            for (int st = 0; st < 64; ++st) {
                const f4 z = *(const f4 LAS*)(pt + (4 * st + kk) * 16);
                float er, ei;
                { const float ex = __builtin_amdgcn_exp2f(z[0] * ppos); const float rv = __builtin_amdgcn_fractf(z[1] * ppos);
                  er = ex * __builtin_amdgcn_cosf(rv); ei = ex * __builtin_amdgcn_sinf(rv); }
                const float are = z[2] * er - z[3] * ei, aim = -(z[2] * ei + z[3] * er);
#pragma unroll
                for (int t = 0; t < 3; ++t) {
                    const float ex = __builtin_amdgcn_exp2f(z[0] * qpos[t]); const float rv = __builtin_amdgcn_fractf(z[1] * qpos[t]);
                    const float br = ex * __builtin_amdgcn_cosf(rv), bi = ex * __builtin_amdgcn_sinf(rv);
                    acc[t] = __builtin_amdgcn_mfma_f32_16x16x4f32(are, br, acc[t], 0, 0, 0);
                    acc[t] = __builtin_amdgcn_mfma_f32_16x16x4f32(aim, bi, acc[t], 0, 0, 0);
                }
            }
#pragma unroll
            for (int t = 0; t < 3; ++t) *(f4 LAS*)(sm + PA_KT + w * 3072 + ((t * 16 + l15) * 16 + 4 * kk) * 4) = acc[t];
        }
        bar();
#pragma nounroll
        for (int cc = 0; cc < 4; ++cc) {
            const int ch = 4 * grp + cc;
            {
                float kv[8];
#pragma unroll
                for (int j = 0; j < 8; ++j) kv[j] = *(const float LAS*)(sm + PA_KT + cc * 3072 + toff[j]);
                u4 h4, l4;
#pragma unroll
                for (int j = 0; j < 4; ++j) { h4[j] = pk2(kv[2 * j], kv[2 * j + 1]);
                    l4[j] = pk2(kv[2 * j] - __uint_as_float(h4[j] << 16), kv[2 * j + 1] - __uint_as_float(h4[j] & 0xffff0000u)); }
                const int o = (31 + (tid >> 2)) * 80 + (tid & 3) * 16;
                *(u4 LAS*)(sm + PA_KH + o) = h4; *(u4 LAS*)(sm + PA_KL + o) = l4;
            }
            for (int i = tid; i < 262; i += 256) {
                const u4 x0 = *(const u4 LAS*)(sm + PA_UT + ch * 4224 + i * 16), x1 = *(const u4 LAS*)(sm + PA_UT + ch * 4224 + i * 16 + 16);
                const unsigned r[8] = {x0[0], x0[1], x0[2], x0[3], x1[0], x1[1], x1[2], x1[3]};
#pragma unroll
                for (int cp = 0; cp < 8; ++cp) {
                    u4 o;
#pragma unroll
                    for (int j = 0; j < 4; ++j) o[j] = (cp & 1) ? __builtin_amdgcn_alignbit(r[(cp >> 1) + j + 1], r[(cp >> 1) + j], 16) : r[(cp >> 1) + j];
                    *(u4 LAS*)(sm + PA_TU + (cp * 276 + (cp ? 1 : 0) + i) * 16) = o;
                }
            }
            bar();
            {
                f16v y0, y1;
                const int m00 = 2048 - l31 + 8 * hi, cp = m00 & 7;
                const char LAS* ab = sm + PA_TU + (cp * 276 + (cp ? 1 : 0) + (m00 >> 3)) * 16 - 63 * 64;
                const char LAS* bb = sm + PA_KH + l31 * 80 + hi * 16;
                const char LAS* a1w = ab - w * 64; const char LAS* b1w = bb - w * 80;
                const char LAS* a2w = ab + w * 64; const char LAS* b2w = bb + w * 80;
                u4 fr[2][4];
                auto rdg = [&](int g, int sl) __attribute__((always_inline)) {
                    const int i = (g & 15) >> 1, ks = g & 1;
                    if (g < 16) { fr[sl][0] = *(const u4 LAS*)(b1w + (31 - 4 * i) * 80 + ks * 32); fr[sl][2] = *(const u4 LAS*)(a1w + (63 - 4 * i) * 64 + ks * 32);
                                  fr[sl][3] = *(const u4 LAS*)(a1w + (31 - 4 * i) * 64 + ks * 32); fr[sl][1] = *(const u4 LAS*)(b1w + 7680 + (31 - 4 * i) * 80 + ks * 32); }
                    else { fr[sl][0] = *(const u4 LAS*)(b2w + (32 + 4 * i) * 80 + ks * 32); fr[sl][3] = *(const u4 LAS*)(a2w + (32 + 4 * i) * 64 + ks * 32);
                           fr[sl][1] = *(const u4 LAS*)(b2w + 7680 + (32 + 4 * i) * 80 + ks * 32); }
                };
                rdg(0, 0); SB();
#pragma unroll
                for (int g = 0; g < 32; ++g) {
                    const int sl = g & 1;
                    if (g + 1 < 32) rdg(g + 1, sl ^ 1);
                    SB();
                    if (g == 0) { mV_vv0(y0, fr[sl][2], fr[sl][0]); mV_vv0(y1, fr[sl][3], fr[sl][0]); }
                    else { if (g < 16) mV_vv(y0, fr[sl][2], fr[sl][0]); mV_vv(y1, fr[sl][3], fr[sl][0]); }
                    if (g < 16) mV_vv(y0, fr[sl][2], fr[sl][1]);
                    mV_vv(y1, fr[sl][3], fr[sl][1]);
                    SB();
                }
                MFW32(y0, y1);
#pragma unroll
                for (int g = 0; g < 4; ++g) {
                    *(f4 LAS*)(sm + PA_RED + w * 9216 + l31 * 144 + (8 * g + 4 * hi) * 4) = (f4){y0[4 * g], y0[4 * g + 1], y0[4 * g + 2], y0[4 * g + 3]};
                    *(f4 LAS*)(sm + PA_RED + w * 9216 + (32 + l31) * 144 + (8 * g + 4 * hi) * 4) = (f4){y1[4 * g], y1[4 * g + 1], y1[4 * g + 2], y1[4 * g + 3]};
                }
            }
            bar();
            {
                f4 s0 = {0.f, 0.f, 0.f, 0.f}, s1 = s0;
#pragma unroll
                for (int p = 0; p < 4; ++p) { const char LAS* q_ = sm + PA_RED + p * 9216 + (tid >> 2) * 144 + (tid & 3) * 32; s0 += *(const f4 LAS*)q_; s1 += *(const f4 LAS*)(q_ + 16); }
                const float dsk = bf2f(a.ADsk[d0 + ch]);
                float yv[8];
#pragma unroll
                for (int j = 0; j < 8; ++j) yv[j] = bfr(j < 4 ? s0[j & 3] : s1[j & 3]) + bfr(bf2f(*(const u16 LAS*)(sm + PA_UT + ch * 4224 + (2048 - (tid * 8 + j)) * 2)) * dsk);
                sst128((u4){pk2(yv[0], yv[1]), pk2(yv[2], yv[3]), pk2(yv[4], yv[5]), pk2(yv[6], yv[7])}, rpa, tid * 16, (d0 + ch) * 4096);
            }
            bar();
        }
    }
    set_flag(c, mksrd(a.HB, HB_SZ), O_FWV + 512 + task * 4, tag);
}
DI void pa_out(unsigned tag, int wave, int t) {
    CArgs& a = get_args();
    const SideCtx c = make_ctx(wave);
    const int lane = c.lane, tid = c.wave * 64 + lane;
    t = __builtin_amdgcn_readfirstlane(t); asm volatile("" : "+s"(t));
    const u4 rpa = mksrd(a.PA, 56 * 32768), ro = mksrd(a.out, 2048 * 1792);
    wait_flags(a, mksrd(a.HB, HB_SZ), lane < 8 ? O_FWV + 512 + (t & ~7) * 4 + lane * 4 : OOB, tag, 17);
    u4 v[8];
#pragma unroll
    for (int i = 0; i < 8; ++i) v[i] = sld128(rpa, (tid >> 5) * 4096 + (tid & 31) * 16, ((t >> 3) * 64 + i * 8) * 4096 + (t & 7) * 512);
    WAITV(0, "+v"(v[0]), "+v"(v[1]), "+v"(v[2]), "+v"(v[3]), "+v"(v[4]), "+v"(v[5]), "+v"(v[6]), "+v"(v[7]));
    bar();
#pragma unroll
    for (int i = 0; i < 8; ++i) *(u4 LAS*)(c.sm + (i * 8 + (tid >> 5)) * 528 + (tid & 31) * 16) = v[i];
    bar();
#pragma unroll
    for (int k = 0; k < 8; ++k) {
        const char LAS* p = c.sm + (tid & 7) * 8 * 528 + ((tid >> 3) + 32 * k) * 2;
        u4 o;
#pragma unroll
        for (int j = 0; j < 4; ++j) o[j] = (unsigned)*(const u16 LAS*)(p + (2 * j) * 528) | ((unsigned)*(const u16 LAS*)(p + (2 * j + 1) * 528) << 16);
        sst128(o, ro, (tid >> 3) * 1792 + (tid & 7) * 16, ((t & 7) * 256 + 32 * k) * 1792 + (t >> 3) * 128);
        asm volatile("s_sleep %0" :: "i"(P_ASLP));
    }
}
#define P_D1 2
#define P_D2 0
#define P_D2B 0
#define P_D2C 68
#define P_DX 12
#define P_DR 4
#define P_D1B 88
#define P_MODE 271360
#define P_UP1 8
#define P_UP2 20
#define PW W
#define RLAT 48u
DI unsigned rt_now() { u64 t; asm volatile("s_memrealtime %0\n\ts_waitcnt lgkmcnt(0)" : "=s"(t) :: "memory"); return (unsigned)t; }
DI void sched(unsigned& t, int tol) {
    unsigned n = rt_now();
#pragma nounroll
    while ((int)(n - t) > tol) t += RLAT;
#pragma nounroll
    while ((int)(n - t) < 0) n = rt_now();
}
template <int N> struct IntC { static constexpr int value = N; };
template <bool B> struct BoolC { static constexpr bool value = B; };
#define NPRE 0
#define CODEPF 3
#define P_HOLD 300
#define P_HOLD2 24
#define P_PENLEN 3
#define P_TOUCH 0
#define XE1 2
#define XE2 0
#define P_E1 4
#define P_E2 4
#define P_E3 4
#define P_WIN 5
#define P_DECAY 16
#define P_QUIET 0
#define P_QUIET2 0
#define P_STAG 0
#define P_W3 40
#define P_K0 3
#define P_QUIETA 0
#define P_XOR 3
#define P_FCLK 229u
#define P_FS 99
#define P_XOFF 8
#define P_EA 4
#define P_EX 0
#define P_EB 4
#define XSINGLE 1
#define P_CQ 3540u
#define P_CB 3660u
#define KS0 6
#define KX 6
#define KX3 7
#define KS3 4
#define L_WVO 0
#define L_MEM 57344
#define L_TL (L_MEM + 8192)
#define L_RED (L_TL + 4096)
#define L_UM (L_RED + 4096)

template <int W>
DI void core_loop(unsigned tag, int vb, int idx) {
    CArgs& a = get_args();
    char LAS* sm = (char LAS*)smem_;
    const int part = idx & 1;
    const bool isP = idx < 28; const int c16 = isP ? idx : idx - 28;
    const u4 r = mksrd(a.HB, HB_SZ);
    const u4 rum = mksrd(a.UM, isP ? 65 * 28 * 4096 : 0);
    const u4 rtf = mksrd(a.TF, 65 * 32 * 3584);
    constexpr int NT = W < 3 ? 4 : 2;
    constexpr bool VL = W < 3;
    constexpr int NTOT = NT + (VL ? 3 : 4);
    constexpr int WS = W;
    constexpr int T0 = 4 * W;
    constexpr int NOWN = W < 3 ? 2 : 1;
    u8v wt[NTOT][7];
    {
        const SideCtx c = make_ctx(W);
        const int lane = c.lane;
        int h = idx >> 1; asm volatile("" : "+s"(h));
        const int hi = lane >> 5, l31 = lane & 31;
        const u4 rw1 = mksrd(a.Wk, 448 * 8192);
        {
            const int q_ = lane >> 4, l_ = lane & 15;
            const int op = (q_ < 2) ? (idx <= 28 ? (q_ ? O_MEMX : O_MEM) + idx * 256 + l_ * 16 : OOB) : (q_ == 2) ? O_SMM + idx * 256 + l_ * 16 : OOB;

#pragma unroll
            for (int i = 0; i < 4; ++i) st128((u4){FF, FF, FF, FF}, r, op, W * 524288 + i * 8192);
        }
        waitvm<20>(); bar();
        PSD PS(0);
        if constexpr (W < 2) {
            u4 kf[2][16];
#pragma unroll
            for (int b = 0; b < 2; ++b)
#pragma unroll
                for (int ks = 0; ks < 16; ++ks) kf[b][ks] = rd_rows_nat(c, 2 * SLICE + (2 * W + b) * 16384, ks);
            bar();
            char LAS* stw = sm + 4 * SLICE + W * 16384 + (l31 >> 4) * 2048 + (hi * 16 + (l31 & 15)) * 16;
            const char LAS* str_ = sm + 4 * SLICE + W * 16384 + lane * 16;
            f16v d[2][2];
            auto epi = [&](int p, int e) { *(u4 LAS*)(stw + ((p >> 1) & 1) * 8192 + (e & 1) * 4096 + (e >> 1) * 1024 + (p & 1) * 512) = pack8(d[p & 1][e & 1], e >> 1); };
            auto take = [&](int v, int t) { wt[t][(v + 14 - 2 * W) % 7] = cat8(*(const u4 LAS*)(str_ + (v & 1) * 8192 + t * 2048), *(const u4 LAS*)(str_ + (v & 1) * 8192 + t * 2048 + 1024)); };
            auto run = [&](auto np_, int p0) __attribute__((always_inline)) {
                pipe<16 * decltype(np_)::value, 4>([&](int i) { const int p = p0 + (i >> 4); return rd_rows(c, (p >> 1) & 3, i & 15, p & 1); },
                            [&](int i, u4 f) { const int p = p0 + (i >> 4), j = i & 15;
                                if (j == 0) { mV_vv0(d[p & 1][0], f, kf[0][j]); mV_vv0(d[p & 1][1], f, kf[1][j]); } else { mV_vv(d[p & 1][0], f, kf[0][j]); mV_vv(d[p & 1][1], f, kf[1][j]); } },
                            [&](int i) { const int p = p0 + (i >> 4), j = i & 15;
                                if (p > 0 && j >= 5 && j < 13 && (j & 1) == 1) { if (j == 5) asm volatile("s_nop 3" : "+v"(d[(p - 1) & 1][0]), "+v"(d[(p - 1) & 1][1])); epi(p - 1, (j - 5) >> 1); }
                                if ((p & 1) && p > 2 && (j & 3) == 2) take((p >> 1) - 1, j >> 2); });
            };
#pragma unroll
            for (int u = 0; u < 7; ++u) {
                if (u == 0) waitvm<12>(); else if (u == 1) waitvm<0>();
                if (u <= 4) { PS(1 + 2 * u); bar(); PS(2 + 2 * u); }
                run(IntC<2>{}, 2 * u);
            }
            MFW32(d[1][0], d[1][1]);
#pragma unroll
            for (int e = 0; e < 4; ++e) epi(13, e);
#pragma unroll
            for (int e = 0; e < 4; ++e) take(6, e);
            PS(11);
        } else {
            const SideCtx c2 = make_ctx(W - 2);
            bar();
            dma_rows(c, rw1, h, 128, 2); dma_rows(c2, rw1, h, 128, 2);
#pragma unroll
            for (int u = 0; u <= 4; ++u) {
                if (u == 0) waitvm<28>(); else if (u <= 3) waitvm<32>(); else waitvm<0>();
                bar();
                if (u < 4) { dma_rows(c, rw1, h, 64 * (u + 3), (u + 3) & 3); dma_rows(c2, rw1, h, 64 * (u + 3), (u + 3) & 3); }
            }
        }
        PSDUMP(256);
    }
    PSTAMP(1);
    const int lane = fresh_lane();
    const int q = lane >> 4, l15 = lane & 15, i8 = lane & 7, hb = (lane >> 3) & 1;
    if (W == 3) st32(tag, r, lane == 0 ? O_FWV + 384 + idx * 4 : OOB, 0);
    wait_flags(a, r, lane < 8 ? O_FWV + (lane >> 2) * 768 + (c16 >> 3) * 64 + (4 * W + (lane & 3)) * 4 : OOB, tag, 12);
    PSTAMP(2);
    constexpr int WVB = W == 0 ? 4 * SLICE : W == 1 ? 4 * SLICE + 16384 : 3 * SLICE;
    {
        const __amdgpu_buffer_rsrc_t rv = __builtin_amdgcn_make_buffer_rsrc(a.WvoF, 0, 28 * 16 * 14336, 0x00020000);
#pragma unroll
        for (int t = 0; t < 4; ++t)
#pragma unroll
            for (int k = 0; k < 7; ++k) {
                const int so_ = (c16 * 16 + 4 * W + t) * 14336 + ((2 * W + k) % 7) * 2048;
                const u4 x0 = __builtin_amdgcn_raw_buffer_load_b128(rv, lane * 16, so_, 16), x1 = __builtin_amdgcn_raw_buffer_load_b128(rv, lane * 16 + 1024, so_, 16);
                if (t < 3 || !VL) wt[(NT + t) % NTOT][k] = cat8(x0, x1);
                else { *(u4 LAS*)(sm + WVB + k * 2048 + lane * 16) = x0; *(u4 LAS*)(sm + WVB + k * 2048 + 1024 + lane * 16) = x1; }
            }
        if (W >= 2) {
            wait_flags(a, r, lane == 0 ? O_FWV + 256 + idx * 4 : OOB, tag, 18);
            const __amdgpu_buffer_rsrc_t rq = __builtin_amdgcn_make_buffer_rsrc(a.WqkF, 0, 32 * 6 * 14336, 0x00020000);
#pragma unroll
            for (int t = 0; t < NT; ++t)
#pragma unroll
                for (int k = 0; k < 7; ++k) {
                    const int so_ = (idx * 6 + T0 - 8 + t) * 14336 + ((2 * W + k) % 7) * 2048;
                    wt[t][k] = cat8(__builtin_amdgcn_raw_buffer_load_b128(rq, lane * 16, so_, 16), __builtin_amdgcn_raw_buffer_load_b128(rq, lane * 16 + 1024, so_, 16));
                }
        }
    }
    const int sidx = (lane & 8) ? 0xEEEE : 0x4444;
#define SMF(acc, av, t, k) do { if ((t) * 7 + (k) < 32) smf_a(acc, av, wt[t][k], sidx); else smf_v(acc, av, wt[t][k], sidx); } while (0)
    __builtin_amdgcn_s_waitcnt(0x0F70);
    bar();
    PSTAMP(3);
    wait_flags(a, r, O_FWV + 256 + lane * 4, tag, 15);
    const int par = get_par(r, tag);
    const int pb = (par & 1) * 8192, ph = par >> 1;
    const int hc = 4 * W + ((q & 1) << 1) + (q >> 1);
    u4 mk;
#pragma unroll
    for (int j = 0; j < 4; ++j) mk[j] = (i8 == 2 * j) ? 0x0000ffffu : (i8 == 2 * j + 1) ? 0xffff0000u : 0u;
    const int o_smm = pb + O_SMM + hc * 512 + l15 * 16;
    const int o_mz = pb + O_MZ + hc * 128 + i8 * 8;
    const int o_rx = pb + O_RX + (c16 >> 1) * 32768 + (c16 & 1) * 4096 + hc * 256 + l15 * 16;
    const int o_mem = pb + O_MEM + W * 2048 + lane * 16;
    const int o_um = c16 * 4096 + lane * 16;
    const int o_tf = idx * 3584 + (4 * hb + q) * 128 + i8 * 16, o_tf3 = hb ? OOB : o_tf;
    const char LAS* tre = sm + L_TL + (8 * q + (l15 >> 2) + 32 * ((lane >> 1) & 1)) * 16 + (lane & 1) * 8;
    const int pi = (lane >> 1) & 7, pqq = ((lane >> 4) & 1) * 2 + (lane & 1);
    const bool pubs = idx <= 28;
    const int o_pubm = idx * 256 + (lane & 31) * 8;
    const int o_pub = pubs ? pb + (lane < 32 ? O_MEM : O_MEMX) + o_pubm : OOB;
    const int o_pubs = (lane < 32 && l15 < 8) ? pb + O_SMM + idx * 256 + q * 128 + i8 * 16 : OOB;
    const int o_poi = (q == 0) ? (pubs ? pb + O_MEM + idx * 256 + l15 * 16 : OOB) : (q == 1) ? (pubs ? pb + O_MEMX + idx * 256 + l15 * 16 : OOB) : (q == 2) ? pb + O_SMM + idx * 256 + l15 * 16 : OOB;
    const char LAS* trb = sm + L_TL + (8 * q + (l15 >> 2)) * 16 + (lane & 1) * 8;
    const char LAS* wvl = sm + WVB + lane * 16;
    char LAS* meml = sm + L_MEM + lane * 16;
    char LAS* tlw = sm + L_TL + ((T0 + (q >> 1)) * 16 + l15) * 16 + (q & 1) * 8;
    char LAS* redw = sm + L_RED + W * 1024 + l15 * 64 + q * 16;
    if (W == 3) {
        u2 o = *(const u2*)(a.mem0 + pi * 448 + c16 * 16 + pqq * 4);
        o[0] = san(o[0]); o[1] = san(o[1]);
        if (!isP) o = (u2){(unsigned)(P_D1B + 12) | ((unsigned)P_XOFF << 8) | ((unsigned)(P_D2C + 12) << 16), 0u};
        st64(o, r, o_pub, SLOT(0, ph));
    }
    const f4 z4 = {0.f, 0.f, 0.f, 0.f};
    const unsigned pv_ = lane == 48 ? tag : FF; const u4 poiv = {pv_, pv_, pv_, pv_};
    unsigned tsch = (rt_now() + RLAT + 24) / RLAT * RLAT;
    const int o_mem1 = (W < 3 || lane < 16) ? o_mem + 1024 : OOB;
    int slip = 0, since = 100, lvl = 0, quick = 0, clv = 0; unsigned c00 = 0, t00 = 0;
    u4 xu[4], xt[4];
    auto exp_read = [&]() __attribute__((always_inline)) {
#pragma unroll
        for (int i = 0; i < 4; ++i) { xu[i] = *(const u4 LAS*)(sm + L_UM + i * 1024 + lane * 16); const u2 lo = trread(tre + i * 1024), hi = trread(tre + i * 1024 + 64); xt[i] = (u4){lo[0], lo[1], hi[0], hi[1]}; }
    };
    auto exp_store = [&](int s, int i) __attribute__((always_inline)) {
        if (i < 4) st128(xu[i & 3], rum, o_um, s * 28 * 4096 + i * 1024); else st128(xt[i & 3], rtf, i == 7 ? o_tf3 : o_tf, s * 32 * 3584 + (i & 3) * 1024);
    };
    TSDECL
    PDECL
    PSTAMP(4);
    __builtin_amdgcn_s_waitcnt(0x0F70);
    auto step = [&](const int s, auto last_) __attribute__((always_inline)) {
        constexpr bool LAST = decltype(last_)::value;
        const int sb = SLOT(s & 3, ph);
        TS(0); TSR(11);
        if (W == 0) PSTEP(s);
        u4 af[7];
        f4 d0 = z4, d1 = z4, d2 = z4, d3 = z4;
        f4 e0 = z4, e1 = z4, e2 = z4, e3 = z4;
        ORD2(d0, d1); ORD2(d2, d3); ORD2(e0, e1); ORD2(e2, e3);
        const unsigned tin = tsch;
        sched(tsch, P_UP2); dly(P_D1);
        TS(1);
        u4 c1;
        af[0] = ld128(r, o_mem, sb);
        c1 = ld128(r, o_mem1, sb);
        if (W == 3) { exp_read(); SB(); }
        WAITV(0, "+v"(af[0]), "+v"(c1));
        *(u4 LAS*)(meml + W * 2048) = af[0];
        *(u4 LAS*)(meml + W * 2048 + 1024) = c1;
        SB();
        if (W < 3) { SMF(d0, af[0], 0, 0); SMF(d1, af[0], 1, 0); SMF(d2, af[0], 2, 0); SMF(d3, af[0], 3, 0); }
        if (__builtin_expect(!__all(max(mx4(af[0]), mx4(c1)) != FF), 0)) {
            unsigned sp = 0;
#pragma nounroll
            do {
                tsch += RLAT; sched(tsch, P_UP2); dly(P_D1);
                af[0] = ld128(r, o_mem, sb); c1 = ld128(r, o_mem1, sb);
                WAITV(0, "+v"(af[0]), "+v"(c1));
                if (++sp > SPIN_MAX) { a.err[0] = 1; __builtin_amdgcn_endpgm(); }
            } while (!__all(max(mx4(af[0]), mx4(c1)) != FF));
            *(u4 LAS*)(meml + W * 2048) = af[0];
            *(u4 LAS*)(meml + W * 2048 + 1024) = c1;
            if (W < 3) {
                asm volatile("v_mov_b32 %0, 0\n\tv_mov_b32 %1, 0\n\tv_mov_b32 %2, 0\n\tv_mov_b32 %3, 0" : "+v"(d0[0]), "+v"(d0[1]), "+v"(d0[2]), "+v"(d0[3]));
                asm volatile("v_mov_b32 %0, 0\n\tv_mov_b32 %1, 0\n\tv_mov_b32 %2, 0\n\tv_mov_b32 %3, 0" : "+v"(d1[0]), "+v"(d1[1]), "+v"(d1[2]), "+v"(d1[3]));
                asm volatile("v_mov_b32 %0, 0\n\tv_mov_b32 %1, 0\n\tv_mov_b32 %2, 0\n\tv_mov_b32 %3, 0" : "+v"(d2[0]), "+v"(d2[1]), "+v"(d2[2]), "+v"(d2[3]));
                asm volatile("v_mov_b32 %0, 0\n\tv_mov_b32 %1, 0\n\tv_mov_b32 %2, 0\n\tv_mov_b32 %3, 0" : "+v"(d3[0]), "+v"(d3[1]), "+v"(d3[2]), "+v"(d3[3]));
                asm volatile("s_nop 1" : "+v"(d0), "+v"(d1), "+v"(d2), "+v"(d3), "+v"(af[0]));
                SMF(d0, af[0], 0, 0); SMF(d1, af[0], 1, 0); SMF(d2, af[0], 2, 0); SMF(d3, af[0], 3, 0);
                asm volatile("s_nop 7" : "+v"(d0), "+v"(d1), "+v"(d2), "+v"(d3));
            }
            TSV(0, sp); PCNT1(s, sp);
        } else { TSV(0, 0); }
        if (NOWN == 2) af[1] = c1;
        TS(2);
        bar();
        TS(3); TSF(0); TSF(11); TSF(1); TSF(2);
        unsigned ctlv;
        u4 mb[W == 3 ? 7 : 1];
        if constexpr (W < 3) {
            af[2] = *(const u4 LAS*)(meml + ((2 * W + 2) % 7) * 1024); af[3] = *(const u4 LAS*)(meml + ((2 * W + 3) % 7) * 1024); SB();
            SMF(d0, af[1], 0, 1); af[4] = *(const u4 LAS*)(meml + ((2 * W + 4) % 7) * 1024); SB();
            SMF(d1, af[1], 1, 1); af[5] = *(const u4 LAS*)(meml + ((2 * W + 5) % 7) * 1024); SB();
            SMF(d2, af[1], 2, 1); af[6] = *(const u4 LAS*)(meml + ((2 * W + 6) % 7) * 1024); SB();
            SMF(d3, af[1], 3, 1); ctlv = *(const unsigned LAS*)(sm + L_MEM + 7168); SB();
        } else {
#pragma unroll
            for (int k = NOWN; k < 7; ++k) af[k] = *(const u4 LAS*)(meml + ((2 * W + k) % 7) * 1024);
            ctlv = *(const unsigned LAS*)(sm + L_MEM + 7168);
#pragma unroll
            for (int v = 0; v < 7; ++v) mb[v] = *(const u4 LAS*)(sm + L_MEM + (part * 28 + 4 * v + q) * 128 + i8 * 16);
            SB();
        }
        if constexpr (W < 3) {
#pragma unroll
        for (int k = 2; k < 3; ++k) { SMF(d0, af[k], 0, k); SMF(d1, af[k], 1, k); SMF(d2, af[k], 2, k); SMF(d3, af[k], 3, k); }
#pragma unroll
        for (int k = 3; k < 7; ++k) { SMF(d0, af[k], 0, k); SMF(d1, af[k], 1, k); }
        {
            SMF(d2, af[3], 2, 3); SMF(d3, af[3], 3, 3);
            SMF(d2, af[4], 2, 4);
            asm volatile("s_nop 5" : "+v"(d0), "+v"(d1));
            auto r0 = __builtin_amdgcn_permlane32_swap(__float_as_uint(d0[0]), __float_as_uint(d1[0]), false, false);
            auto r1 = __builtin_amdgcn_permlane32_swap(__float_as_uint(d0[1]), __float_as_uint(d1[1]), false, false);
            unsigned a0 = r0[0], b0 = r0[1], a1 = r1[0], b1 = r1[1]; asm volatile("" : "+v"(a0), "+v"(b0), "+v"(a1), "+v"(b1));
            SMF(d3, af[4], 3, 4);
            auto r2 = __builtin_amdgcn_permlane32_swap(__float_as_uint(d0[2]), __float_as_uint(d1[2]), false, false);
            auto r3 = __builtin_amdgcn_permlane32_swap(__float_as_uint(d0[3]), __float_as_uint(d1[3]), false, false);
            unsigned a2 = r2[0], b2 = r2[1], a3 = r3[0], b3 = r3[1]; asm volatile("" : "+v"(a2), "+v"(b2), "+v"(a3), "+v"(b3));
            SMF(d2, af[5], 2, 5);
            float x0 = vadd(a0, b0), x1 = vadd(a1, b1);
            SMF(d3, af[5], 3, 5);
            float x2 = vadd(a2, b2), x3 = vadd(a3, b3);
            SMF(d2, af[6], 2, 6);
            u2 t01 = {pk2(x0, x1), pk2(x2, x3)}; PIN(t01);
            SMF(d3, af[6], 3, 6);
            *(u2 LAS*)tlw = t01;
            asm volatile("s_nop 6" : "+v"(d2), "+v"(d3) :: "memory");
        }
        { u2 t23 = pair_pack(d2, d3); *(u2 LAS*)(tlw + 512) = t23; }
        SB();
        } else {
            { const int pslot = SLOT((s + 2) & 3, ph);
              st128(poiv, r, lane == 48 ? (s >= 2 ? O_CF + (s - 2) * 512 + idx * 16 - pslot : OOB) : o_poi, pslot); }
            exp_store(s, 0); exp_store(s, 1);
#pragma unroll
            for (int k = 0; k < 7; ++k) { SMF(d0, af[k], 0, k); SMF(d1, af[k], 1, k); if (k < 5) exp_store(s, k + 2); }
            asm volatile("s_nop 7" : "+v"(d0), "+v"(d1));
            *(u2 LAS*)tlw = pair_pack(d0, d1);
            SB();
            exp_store(s, 7);
            SB();
        }
        u4 xa[2], xb[2];
        if (W == 3) {
#pragma unroll
            for (int k = 0; k < NPRE; ++k) SMF(e0, af[k], NT, k);
        }
        SB();
        TS(4);
        bar();
        TS(5); TSF(3); TSF(4);
        if constexpr (LAST) {
            const u2 lo = trread(tre + W * 1024), hi = trread(tre + W * 1024 + 64);
            st128((u4){lo[0], lo[1], hi[0], hi[1]}, rtf, W == 3 ? o_tf3 : o_tf, NSEG * 32 * 3584 + W * 1024);
        }
        if (W == 3 && !LAST) {
            u2 lo[7], hi[7];
#pragma unroll
            for (int v = 0; v < 7; ++v) { lo[v] = trread(trb + v * 512); hi[v] = trread(trb + v * 512 + 64); }
            SB();
            f4 y;
            mf_acc0(y, (u4){lo[0][0], lo[0][1], hi[0][0], hi[0][1]}, mb[0]);
#pragma unroll
            for (int v = 1; v < 7; ++v) mf_acc(y, (u4){lo[v][0], lo[v][1], hi[v][0], hi[v][1]}, mb[v]);
            MFW1(y);
            st128(__builtin_bit_cast(u4, y), r, o_pubs, sb);
            SB();
            if (tsch != tin) slip = 1;
        }
        const unsigned ctl = __builtin_amdgcn_readfirstlane(ctlv);
        tsch += ctl & 255u;
        const unsigned tin2 = tsch;
        TS(6);
        u4 p0, p1, rx; u2 mz;
        auto pollx = [&]() { mz = ld64(r, o_mz, sb); rx = ld128(r, o_rx, sb); };
        auto polls = [&]() { p0 = ld128(r, o_smm, sb); p1 = ld128(r, o_smm, sb + 256); };
#pragma unroll
        for (int k = 0; k < 7; ++k) {
            if (W < 3 || k >= NPRE) SMF(e0, af[k], NT, k);
            SMF(e1, af[k], NT + 1, k);
            if (VL && k == 1) { xa[0] = *(const u4 LAS*)(wvl); xb[0] = *(const u4 LAS*)(wvl + 1024); xa[1] = *(const u4 LAS*)(wvl + 2048); xb[1] = *(const u4 LAS*)(wvl + 3072); }
            SMF(e2, af[k], NT + 2, k);
            if (!VL) SMF(e3, af[k], (NT + 3) % NTOT, k);
            if (VL && k >= 3) { smf_v(e3, af[k - 3], cat8(xa[(k - 3) & 1], xb[(k - 3) & 1]), sidx);
                xa[(k - 3) & 1] = *(const u4 LAS*)(wvl + (k - 1) * 2048); xb[(k - 3) & 1] = *(const u4 LAS*)(wvl + (k - 1) * 2048 + 1024); }
            if (k == (W == 3 ? KS3 : KS0) && !LAST) { sched(tsch, P_UP2); dly(P_D2); TS(8); polls(); }
            SB();
        }
        if (VL) {
            smf_v(e3, af[4], cat8(xa[0], xb[0]), sidx);
            xa[0] = *(const u4 LAS*)(wvl + 6 * 2048); xb[0] = *(const u4 LAS*)(wvl + 6 * 2048 + 1024);
            smf_v(e3, af[5], cat8(xa[1], xb[1]), sidx);
            smf_v(e3, af[6], cat8(xa[0], xb[0]), sidx);
        }
        if (!LAST) { unsigned tx = tsch + ((ctl >> 8) & 255u); sched(tx, 1000); dly(P_D2B); pollx(); }
        MFW2(e0, e1); u2 um0 = pair_pack(e0, e1); PIN(um0);
        MFW2(e2, e3); u2 um1 = pair_pack(e2, e3); PIN(um1);
        if constexpr (LAST) { st128((u4){um0[0], um0[1], um1[0], um1[1]}, rum, o_um, NSEG * 28 * 4096 + W * 1024); return; }
        *(u4 LAS*)(sm + L_UM + W * 1024 + lane * 16) = (u4){um0[0], um0[1], um1[0], um1[1]};
        u2 ua0, ua1;
        { auto s0 = __builtin_amdgcn_permlane16_swap(um0[0], um1[0], false, false); auto s1 = __builtin_amdgcn_permlane16_swap(um0[1], um1[1], false, false);
          ua0 = (u2){s0[0], s1[0]}; ua1 = (u2){s0[1], s1[1]}; PIN(ua0); PIN(ua1); }
        SB();
        TS(7);
        TSW(); TSF(5); TSF(6); TSF(7); TSF(8);
        f4 y0, y1; f4 ee; float mm, zm;
        auto part1 = [&]() {
            const f4 sc = __builtin_bit_cast(f4, p0) + __builtin_bit_cast(f4, p1);
            mm = vmax_ror8(fmaxf(fmaxf(sc[0], sc[1]), fmaxf(sc[2], sc[3]))) * CSCALE;
#pragma unroll
            for (int j = 0; j < 4; ++j) ee[j] = __builtin_amdgcn_exp2f(__builtin_fmaf(sc[j], CSCALE, -mm));
            zm = vadd_ror8((ee[0] + ee[1]) + (ee[2] + ee[3]));
        };
        u4 bfrag;
        auto part2 = [&]() {
            const float mxl = __uint_as_float(mz[0]);
            const float m = vmax(mm, mxl);
            const float am = __builtin_amdgcn_exp2f(mm - m), cx = __builtin_amdgcn_exp2f(mxl - m);
            const float inv = __builtin_amdgcn_rcpf(am * zm + cx * __uint_as_float(mz[1]));
            const float fm = am * inv; float inv2 = inv;
            const u2 pf = {pk2(ee[0] * fm, ee[1] * fm), pk2(ee[2] * fm, ee[3] * fm)};
            asm volatile("s_nop 1\n\tv_mfma_f32_16x16x16_bf16 %0, %2, %4, 0\n\tv_mfma_f32_16x16x16_bf16 %1, %3, %4, 0" : "=&v"(y0), "=&v"(y1) : "v"(ua0), "v"(ua1), "v"(pf));
            PIN(inv2);
            const float scx = cx * inv2; const unsigned sp2 = pk2(scx, scx);
            bfrag = (u4){sp2 & mk[0], sp2 & mk[1], sp2 & mk[2], sp2 & mk[3]};
        };
        auto part3 = [&]() {
            asm volatile("s_nop 1\n\tv_mfma_f32_16x16x32_bf16 %0, %1, %2, %0" : "+v"(y0) : "v"(rx), "v"(bfrag));
        };
        auto part4 = [&]() __attribute__((always_inline)) {
            f4 yz;
            asm volatile("s_nop 4\n\tv_add_f32_dpp %0, %8, %4 row_ror:8 row_mask:0xf bank_mask:0xf\n\tv_add_f32_dpp %1, %9, %5 row_ror:8 row_mask:0xf bank_mask:0xf\n\t"
                         "v_add_f32_dpp %2, %10, %6 row_ror:8 row_mask:0xf bank_mask:0xf\n\tv_add_f32_dpp %3, %11, %7 row_ror:8 row_mask:0xf bank_mask:0xf"
                         : "=&v"(yz[0]), "=&v"(yz[1]), "=&v"(yz[2]), "=&v"(yz[3]) : "v"(y0[0]), "v"(y0[1]), "v"(y0[2]), "v"(y0[3]), "v"(y1[0]), "v"(y1[1]), "v"(y1[2]), "v"(y1[3]));
            *(f4 LAS*)redw = yz;
        };
        unsigned nctl = 0;
        if (W == 3 && !isP) {
            const int early = s < 6 ? P_E1 : s < 27 ? P_E2 : s < P_W3 ? P_E3 : 0;
            if (slip) {
                if (s > 3 && since > P_PENLEN + 1) { if (since <= P_PENLEN + P_WIN) { if (++quick >= 2) { lvl = min(lvl + 1, 4); quick = 0; } } else quick = 0; }
                since = 0; slip = 0;
            } else ++since;
            const bool pen = since < P_PENLEN, busy = early != 0;
            const int lv = lvl + clv;
            const int A = P_D1B + (pen ? P_UP1 : busy ? P_EA : 0) + 4 * (lv >> 1), XO = P_XOFF + (busy ? P_EX : 0), B = P_D2C + (pen ? P_UP1 + (busy ? P_EX : 0) : busy ? P_EB : 0) + 4 * ((lv + 1) >> 1);
            nctl = (unsigned)A | ((unsigned)XO << 8) | ((unsigned)B << 16);
        }
        WAITV(2, "+v"(p0), "+v"(p1));
        part1();
        unsigned chk = max(mx4(p0), mx4(p1));
        asm volatile("" : "+v"(chk), "+v"(ee), "+v"(mm), "+v"(zm)); SB();
        WAITV(1, "+v"(mz));
        TS(9);
        part2();
        asm volatile("s_waitcnt vmcnt(0)" : "+v"(rx), "+v"(bfrag) :: "memory");
        part3();
        asm volatile("" : "+v"(chk));
        chk = umax3(umax3(chk, mz[0], mz[1]), umax3(rx[0], rx[1], rx[2]), rx[3]);
        asm volatile("" : "+v"(chk));
        part4();
        if (__builtin_expect(!__all(chk != FF), 0)) {
            unsigned sp = 0;
            TSV(2, __builtin_amdgcn_readfirstlane(__all(max(mx4(p0), mx4(p1)) != FF) ? 0 : 1));
            PCAUSE;
#pragma nounroll
            do {
                tsch += RLAT; sched(tsch, P_UP2); polls(); { unsigned tx = tsch + ((ctl >> 8) & 255u); sched(tx, 1000); } dly(P_D2B); pollx();
                WAITV(0, "+v"(p0), "+v"(p1), "+v"(mz), "+v"(rx));
                if (++sp > SPIN_MAX) { a.err[0] = 2; __builtin_amdgcn_endpgm(); }
                chk = umax3(umax3(max(mx4(p0), mx4(p1)), mz[0], mz[1]), umax3(rx[0], rx[1], rx[2]), rx[3]);
            } while (!__all(chk != FF));
            part1(); part2();
            MFW2(y0, y1);
            part3();
            MFW2(y0, y1);
            part4();
            TSV(1, sp); PCNT2(s, sp + pcause_);
        } else { TSV(1, 0); TSV(2, 0); }
        bar();
        if (W != 3) TS(10);
        if (W == 3) {
            f4 pr[4];
#pragma unroll
            for (int w = 0; w < 4; ++w) pr[w] = *(const f4 LAS*)(sm + L_RED + w * 1024 + pi * 64 + pqq * 16);
            const f4 acc = (pr[0] + pr[1]) + (pr[2] + pr[3]);
            u2 o = {pk2(acc[0], acc[1]), pk2(acc[2], acc[3])};
            if (!isP) o[0] = nctl;
            st64(o, r, o_pub, SLOT((s + 1) & 3, ph));
            SB();
            if (tsch != tin2) slip = 1;
            if (!isP && (s & 3) == 1) {
                const bool busy = s + 2 < P_W3;
                const unsigned cn = now32(), tn = rt_now(), dc = cn - c00, need = (tn - t00) * (busy ? P_CB : P_CQ);
                const unsigned p0_ = P_D1B + P_D2C + (busy ? P_EA + P_EB : 0);
                if (s > 1) clv = p0_ * dc >= need ? 0 : (p0_ + 4) * dc >= need ? 1 : (p0_ + 8) * dc >= need ? 2 : (p0_ + 12) * dc >= need ? 3 : 4;
                c00 = cn; t00 = tn;
            }
            TS(10);
        }
        PLATE(s, (tin2 - tin - (ctl & 255u)) / RLAT, (tsch - tin2) / RLAT);
        tsch += ctl >> 16;
        PCTL(s, ctl);
        TSW(); TSF(9); TSF(10);
        TSEND(s);
    };
#pragma nounroll
    for (int s = 0; s < NSEG - 1; ++s) step(s, BoolC<false>{});
    step(NSEG - 1, BoolC<true>{});
    if (W == 0) asm volatile("fk_code_hi:");
    waitvm<0>(); bar();
    if (W == 3) st128((u4){tag, tag, tag, tag}, r, lane < 2 ? O_CF + (NSEG - 2 + lane) * 512 + idx * 16 : (lane == 2 && idx == 0) ? O_EPOCH : OOB, 0);
    TSDUMP();
    PDUMP(); PSTAMP(5);
}

#define LX_S 0

DI u4 ldp128(u4 srd, int voff, int soff) { u4 v; asm volatile("buffer_load_dwordx4 %0, %1, %2, %3 offen sc1" : "=v"(v) : "v"(voff), "s"(srd), "s"(soff) : "memory"); return v; }

template <int W>
DI void x_loop(unsigned tag, int vb, int xb) {
    CArgs& a = get_args();
    char LAS* sm = (char LAS*)smem_;
    const int lane = fresh_lane();
    const int h = xb >> 1, half = xb & 1;
    const int q = lane >> 4, l15 = lane & 15, i8 = lane & 7, hb = (lane >> 3) & 1;
    const u4 r = mksrd(a.HB, HB_SZ);
    const u4 rg = mksrd(a.GF, 64 * 16 * 28 * 1024), ru = mksrd(a.UF, 64 * 16 * 28 * 1024);
    const int sidx = (lane & 8) ? 0xEEEE : 0x4444;
    constexpr int NOWN = W < 3 ? 2 : 1;
    constexpr int NU = W < 3 ? 4 : 2;
    struct RS { u4 g[2][NOWN][2], uf[NU]; };
    RS ra, rb;
    const int o_l = lane * 16;
    constexpr int NLD = 4 * NOWN + NU;
    auto ld1 = [&](RS& d, int s, int i) __attribute__((always_inline)) {
        if (i < 4 * NOWN) { const int t = i / (2 * NOWN), c = (i >> 1) % NOWN, x = i & 1; d.g[t][c][x] = ldp128(rg, o_l, ((s * 16 + h) * 28 + (t * 7 + 2 * W + c) * 2 + x) * 1024); }
        else if (i < NLD) { const int t = i - 4 * NOWN; d.uf[t < NU ? t : 0] = ldp128(ru, o_l, ((s * 16 + h) * 28 + half * 14 + 4 * W + t) * 1024); }
    };
    auto ldall = [&](RS& d, int s, int i0) __attribute__((always_inline)) {
#pragma unroll
        for (int i = 0; i < NLD; ++i) if (i >= i0) ld1(d, s, i);
    };
    auto landed = [&](RS& d) __attribute__((always_inline)) {
        if (NOWN == 2) asm volatile("" : "+v"(d.g[0][0][0]), "+v"(d.g[0][0][1]), "+v"(d.g[1][0][0]), "+v"(d.g[1][0][1]), "+v"(d.g[0][NOWN - 1][0]), "+v"(d.g[0][NOWN - 1][1]), "+v"(d.g[1][NOWN - 1][0]), "+v"(d.g[1][NOWN - 1][1]), "+v"(d.uf[0]), "+v"(d.uf[1]), "+v"(d.uf[NU - 2]), "+v"(d.uf[NU - 1]));
        else asm volatile("" : "+v"(d.g[0][0][0]), "+v"(d.g[0][0][1]), "+v"(d.g[1][0][0]), "+v"(d.g[1][0][1]), "+v"(d.uf[0]), "+v"(d.uf[1]));
    };
    if (xb == 2 && W == 3) {
        u4 bs = {1u << 20, 1u << 20, 1u << 20, 1u << 20};
#pragma nounroll
        for (int rep = 0; rep < 3; ++rep) {
            st32(rep, r, lane < 4 ? O_PROBE + lane * 8192 : OOB, 0); asm volatile("s_waitcnt vmcnt(0)" ::: "memory");
#pragma unroll
            for (int k = 0; k < 4; ++k) {
                const unsigned c0 = now32();
                unsigned v = ld32(r, lane == 0 ? O_PROBE + k * 8192 : OOB, 0); WAITV(0, "+v"(v));
                bs[k] = min(bs[k], (now32() - c0) * 4 + k);
            }
        }
        PPROBE(bs);
        st32((min(min(bs[0], bs[1]), min(bs[2], bs[3])) & 3u) ^ tag, r, lane == 0 ? O_PAR : OOB, 0);
    }
    if (xb == 2) bar();
    if (P_TOUCH) {
        unsigned t0 = ld32(rg, lane * 458752 + W * 114688, 0), t1 = ld32(ru, lane * 458752 + W * 114688, 0); WAITV(0, "+v"(t0), "+v"(t1));
    }
    const int o_fl = lane < 2 ? O_FGU + lane * 64 + h * 4 : OOB;
    wait_flags(a, r, o_fl, tag, 13);
    ldall(ra, 0, 0);
    wait_flags(a, r, O_FWV + 256 + lane * 4, tag, 16); landed(ra);
    const int par = get_par(r, tag);
    const int pb = (par & 1) * 8192, ph = par >> 1;
    const int o_rx = (W < 3 || (q & 1) == 0) ? pb + O_RX + (half * 7 + 2 * W + (q & 1)) * 32768 + (q >> 1) * 4096 + h * 256 + l15 * 16 : OOB;
    const int o_rx2 = (lane < 8 && half == 0) ? pb + O_MZ + h * 128 + lane * 8 : OOB;
    const int pt = 4 * W + q;
    const int o_poi = pt < 14 ? pb + O_RX + (half * 7 + (pt >> 1)) * 32768 + (pt & 1) * 4096 + h * 256 + l15 * 16 : (pt == 14 && half == 0 && l15 < 4) ? pb + O_MZ + h * 128 + l15 * 16 : OOB;
    const int o_mx = pb + O_MEMX + W * 2048 + lane * 16;
    const int o_mx1 = (W < 3 || lane < 16) ? o_mx + 1024 : OOB;
    unsigned tsch = rt_now() / RLAT * RLAT + P_DX;
    const char LAS* srd_ = sm + LX_S + i8 * 144 + (q * 8 + hb * 4) * 4;
    const int e_ = 4 * (l15 >> 3) + (l15 & 3);
    const int pos = (2 * (q >> 1) + ((l15 >> 2) & 1)) * 8 + ((e_ >> 1) & 1) * 4 + 2 * (e_ >> 2) + (e_ & 1);
    char LAS* swr = sm + LX_S + W * 1152 + (4 * (q & 1)) * 144 + pos * 4;
    const f4 z4 = {0.f, 0.f, 0.f, 0.f};
    TSDECL
    PDECL
    PSTAMP(4);
    __builtin_amdgcn_s_waitcnt(0x0F70);
    auto step = [&](const int s, RS& cur, RS& nxt) __attribute__((always_inline)) {
        const int sb = SLOT(s & 3, ph);
        auto& g = cur.g; auto& uf = cur.uf;
        TS(0); TSR(11);
        if (W == 0) PSTEP(s);
        f4 d0 = z4, d1 = z4, y0 = z4, y1 = z4, y2 = z4, y3 = z4; ORD2(d0, d1); ORD2(y0, y1); ORD2(y2, y3);
        u4 a0, a1;
        sched(tsch, P_UP2);
        TS(1);
        a0 = ld128(r, o_mx, sb); a1 = ld128(r, o_mx1, sb);
        WAITV(0, "+v"(a0), "+v"(a1));
        smf_v(d0, a0, cat8(g[0][0][0], g[0][0][1]), sidx); smf_v(d1, a0, cat8(g[1][0][0], g[1][0][1]), sidx);
        if (NOWN == 2) { smf_v(d0, a1, cat8(g[0][NOWN - 1][0], g[0][NOWN - 1][1]), sidx); smf_v(d1, a1, cat8(g[1][NOWN - 1][0], g[1][NOWN - 1][1]), sidx); }
        auto wrs = [&]() __attribute__((always_inline)) {
            float x0 = swapadd32(d0[0], d1[0]), x1 = swapadd32(d0[1], d1[1]), x2 = swapadd32(d0[2], d1[2]), x3 = swapadd32(d0[3], d1[3]);
            *(float LAS*)swr = x0; *(float LAS*)(swr + 144) = x1; *(float LAS*)(swr + 288) = x2; *(float LAS*)(swr + 432) = x3;
            if (W == 3) *(unsigned LAS*)(sm + 8192 + lane * 4) = a1[0];
        };
        unsigned chk = 0; asm volatile("" : "+v"(chk));
        chk = umax3(umax3(umax3(umax3(chk, a0[0], a0[1]), a0[2], a0[3]), a1[0], a1[1]), a1[2], a1[3]);
        asm volatile("s_nop 3" : "+v"(d0), "+v"(d1), "+v"(chk));
        wrs();
        if (__builtin_expect(!__all(chk != FF), 0)) {
            unsigned sp = 0;
#pragma nounroll
            do {
                tsch += RLAT; sched(tsch, P_UP2);
                a0 = ld128(r, o_mx, sb); a1 = ld128(r, o_mx1, sb);
                WAITV(0, "+v"(a0), "+v"(a1));
                if (++sp > SPIN_MAX) { a.err[0] = 3; __builtin_amdgcn_endpgm(); }
            } while (!__all(max(mx4(a0), mx4(a1)) != FF));
            d0 = z4; d1 = z4; asm volatile("s_nop 1" : "+v"(d0), "+v"(d1));
            smf_v(d0, a0, cat8(g[0][0][0], g[0][0][1]), sidx); smf_v(d1, a0, cat8(g[1][0][0], g[1][0][1]), sidx);
            if (NOWN == 2) { smf_v(d0, a1, cat8(g[0][NOWN - 1][0], g[0][NOWN - 1][1]), sidx); smf_v(d1, a1, cat8(g[1][NOWN - 1][0], g[1][NOWN - 1][1]), sidx); }
            MFW2(d0, d1);
            wrs();
            TSV(0, sp); PCNT1(s, sp);
        } else { TSV(0, 0); }
        TS(2);
        bar();
        TS(3);
        {
            f4 s0[4];
#pragma unroll
            for (int p = 0; p < 4; ++p) s0[p] = *(const f4 LAS*)(srd_ + p * 1152);
            SB();
#pragma unroll
            for (int i = 0; i < XE1; ++i) ld1(nxt, s + 1, i);
            if (XE1 < 2) { unsigned dm_; asm volatile("v_mov_b32 %0, 0\n\tv_mov_b32 %0, 0\n\tv_mov_b32 %0, 0\n\tv_mov_b32 %0, 0\n\tv_mov_b32 %0, 0\n\tv_mov_b32 %0, 0" : "=v"(dm_)); }
            SB();
            f4 t0 = (s0[0] + s0[1]) + (s0[2] + s0[3]);
            float m, mt;
            asm volatile("v_max3_f32 %0, %2, %3, %4\n\tv_max_f32 %0, %0, %5\n\ts_nop 1\n\t"
                         "v_max_f32_dpp %1, %0, %0 row_ror:8 row_mask:0xf bank_mask:0xf\n\tv_max_f32_dpp %0, %0, %0 row_ror:8 row_mask:0xf bank_mask:0xf\n\ts_nop 1\n\t"
                         "v_permlane16_swap_b32 %0, %1\n\tv_max_f32 %0, %0, %1\n\tv_mov_b32 %1, %0\n\ts_nop 1\n\t"
                         "v_permlane32_swap_b32 %0, %1\n\tv_max_f32 %0, %0, %1"
                         : "=&v"(m), "=&v"(mt) : "v"(t0[0]), "v"(t0[1]), "v"(t0[2]), "v"(t0[3]));
            float e[4];
#pragma unroll
            for (int j = 0; j < 4; ++j) e[j] = __builtin_amdgcn_exp2f(t0[j] - m);
            u2 ef = {pk2(e[0], e[1]), pk2(e[2], e[3])};
            asm volatile("s_nop 1" : "+v"(ef));
            smf32(y0, ef, uf[0], sidx); smf32(y1, ef, uf[1], sidx);
            u2 oa, ob;
            if (W < 3) {
                smf32(y2, ef, uf[NU - 2], sidx); smf32(y3, ef, uf[NU - 1], sidx);
                asm volatile("s_nop 5" : "+v"(y0), "+v"(y1));
                oa = pair_pack(y0, y1); PIN(oa);
                ORD2(y2, y3);
                ob = pair_pack(y2, y3);
            } else {
                float z = vadd_ror8((e[0] + e[1]) + (e[2] + e[3]));
                z = x32add(x16add(z));
                st64((u2){__float_as_uint(m), __float_as_uint(z)}, r, o_rx2, sb);
                asm volatile("s_nop 3" : "+v"(y0), "+v"(y1));
                oa = pair_pack(y0, y1); ob = oa;
            }
            const auto s0_ = __builtin_amdgcn_permlane16_swap(oa[0], ob[0], false, false); const auto s1_ = __builtin_amdgcn_permlane16_swap(oa[1], ob[1], false, false);
            st128((u4){s0_[0], s1_[0], s0_[1], s1_[1]}, r, o_rx, sb);
            SB();
        }
        TS(4);
        { const unsigned ctl = __builtin_amdgcn_readfirstlane(*(const unsigned LAS*)(sm + 8192)); tsch += (ctl & 255u) + (ctl >> 16); }
        ldall(nxt, s + 1, XE1 + XE2);
        st128((u4){FF, FF, FF, FF}, r, o_poi, SLOT((s + 2) & 3, ph));
        TS(5);
        bar();
        waitvm<0>(); landed(nxt);
        TS(6);
        if (W == 3 && ((s + 2) & 3) == 0 && s + 2 < NSEG) wait_flags(a, r, o_fl + ((s + 2) >> 2) * 256, tag, 14);
        TSW(); TSF(0); TSF(11); TSF(1); TSF(2); TSF(3); TSF(4); TSF(5); TSF(6);
        TSEND(s);
    };
#pragma nounroll
    for (int s = 0; s < NSEG - 1; ++s) step(s, ra, ra);
    TSDUMP();
    PDUMP(); PSTAMP(5);
}

#undef PW
#define PW wave
DI void fused_body() {
    CArgs& a = get_args();
    const int wave = __builtin_amdgcn_readfirstlane(threadIdx.x >> 6);
    PENTRY
    const unsigned tentry = rt_now();
    const u4 rhb = mksrd(a.HB, HB_SZ);
    unsigned ep = 0;
    if (wave == 0) ep = ld32(rhb, O_EPOCH, 0);
    else {
        u64 pc; unsigned len;
        asm volatile("s_getpc_b64 %0\nfk_code_lo:\n\ts_mov_b32 %1, fk_code_hi-fk_code_lo" : "=s"(pc), "=s"(len));
        const u4 rc = mksrd((const void*)pc, len & ~15u);
        const int vo = (((blockIdx.x >> 3) * 3 + wave - 1) * 2) * 1024 + (threadIdx.x & 63) * 16;
#pragma unroll
        for (int i = 0; i < 2; ++i) dma_one(rc, vo + i * 1024, 0, (unsigned)(size_t)(char LAS*)smem_ + 147456 + (wave * 3 + i) * 1024);
    }
    auto first = [&](int vb_) __attribute__((always_inline)) {
        const int res = vb_ & 7, idx = vb_ >> 3, nx = res == 1 ? 12 : 10;
        if (res >= 4) { if (idx < 8) gu_first(wave, (idx >> 2) & 1, 4 * (res - 4) + (idx & 3), 0); else waitvm<0>(); }
        else {
            const int nid = (res == 1 ? 0 : res == 2 ? 20 : 42) + idx - nx;
            const int kind = res == 0 ? 2 : idx < nx ? 0 : 1;
            w_first(wave, kind, kind == 2 ? idx >> 1 : kind == 0 ? (idx >> 1) * 3 + (res - 1) : nid >> 2, kind == 1 ? nid & 3 : idx & 1, 0);
        }
    };
    const int xcc = (__builtin_amdgcn_s_getreg((31 << 11) | 20) & 7) ^ P_XOR;
    int vb = xcc + (blockIdx.x & ~7);
    unsigned tag;
#pragma nounroll
    for (int pass = 0;; ++pass) {
        first(vb);
        if (pass) break;
        if (wave == 0) {
            WAITV(28, "+v"(ep));
            if (threadIdx.x == 0) {
                const unsigned tg = ep + 0x9E3779B4u;
                unsigned slot = (unsigned)xcc * 32u + (blockIdx.x >> 3);
                for (int k = 0; k < 256; ++k, slot = (slot + 1u) & 255u)
                    if (__hip_atomic_exchange((unsigned*)(a.HB + O_CNT) + slot * 32u, tg, __ATOMIC_RELAXED, __HIP_MEMORY_SCOPE_AGENT) != tg) break;
                *(u2 LAS*)(smem_ + 147456) = (u2){(slot >> 5) + 8u * (slot & 31u), tg};
            }
        }
        bar();
        const int got = __builtin_amdgcn_readfirstlane(*(const unsigned LAS*)(smem_ + 147456));
        tag = __builtin_amdgcn_readfirstlane(*(const unsigned LAS*)(smem_ + 147460));
        if (got == vb) break;
        vb = got; waitvm<0>(); bar();
    }
    const int res = vb & 7, idx = vb >> 3;
    PSTAMP0();
    if (res == 0) {
        if (wave == 0) core_loop<0>(tag, vb, idx); else if (wave == 1) core_loop<1>(tag, vb, idx); else if (wave == 2) core_loop<2>(tag, vb, idx); else core_loop<3>(tag, vb, idx);
        return;
    }
    if (res <= 3) {
        const int nx = res == 1 ? 12 : 10;
        if (idx < nx) {
            const int xb = ((idx >> 1) * 3 + (res - 1)) * 2 + (idx & 1);
            {
                const int lane = fresh_lane(), pt = 4 * wave + (lane >> 4), l15 = lane & 15, h = xb >> 1, half = xb & 1;
                const int op = pt < 14 ? O_RX + (half * 7 + (pt >> 1)) * 32768 + (pt & 1) * 4096 + h * 256 + l15 * 16 : (pt == 14 && half == 0 && l15 < 4) ? O_MZ + h * 128 + l15 * 16 : OOB;
                const u4 r = mksrd(a.HB, HB_SZ);
#pragma unroll
                for (int i = 0; i < 16; ++i) st128((u4){FF, FF, FF, FF}, r, op, (i >> 2) * 524288 + (i & 3) * 8192);
            }
            w_task<0>(tag, vb, wave, xb >> 1, xb & 1);
            if (wave == 0) x_loop<0>(tag, vb, xb); else if (wave == 1) x_loop<1>(tag, vb, xb); else if (wave == 2) x_loop<2>(tag, vb, xb); else x_loop<3>(tag, vb, xb);
            return;
        }
        const int nid = (res == 1 ? 0 : res == 2 ? 20 : 42) + idx - nx;
        wv_half(tag, wave, nid >> 2, nid & 3, 0);
        PSTAMP(1);
#pragma nounroll
        while (rt_now() - tentry < P_QUIETA + (nid >> 3) * P_STAG) asm volatile("s_sleep 16");
        if (nid >= 8) parta_task(tag, vb, wave, nid - 8);
        PSTAMP(2);
        return;
    }
    const int xq = res - 4;
    const int k0 = idx < 28 ? P_K0 : 4;
    if (idx >= 16) { slp(P_HOLD2); w_first(wave, 1, 4 * xq + ((idx - 16) >> 2), idx & 3, 4); wv_half(tag, wave, 4 * xq + ((idx - 16) >> 2), idx & 3, 4);
#pragma nounroll
        while (rt_now() - tentry < (idx < 24 ? P_QUIET2 : P_QUIET)) asm volatile("s_sleep 16");
    }
    else if (idx >= 8) {
        const unsigned t0 = rt_now();
#pragma nounroll
        while (rt_now() - t0 < P_HOLD) asm volatile("s_sleep 16");
    }
    int k = 0, j = idx;
#pragma nounroll
    for (int it = 0; j < 112; ++it) {
        if (k < 4 && it != k0) {
            if (k || idx >= 8) { bar(); gu_first(wave, (idx >> 2) & 1, 4 * xq + (idx & 3), 4 * k + (idx >> 3)); }
            if (idx & 4) gu_task<1>(tag, vb, wave, 4 * xq + (idx & 3), 4 * k + (idx >> 3)); else gu_task<0>(tag, vb, wave, 4 * xq + (idx & 3), 4 * k + (idx >> 3));
            PSTAMP(1 + k);
            ++k;
        } else {
            x_task(tag, vb, wave, 4 * (j / 7) + xq, j % 7); PSTAMP(5 + (j >> 5));
            if ((j >> 5) == 1 && idx >= 16 && idx < 30) pa_out(tag, wave, (idx - 16) * 4 + xq);
            j += 32;
        }
    }
}

__global__ __launch_bounds__(256, 1) void fused_kernel(Args a_) {
    unsigned t0 = rt_now();
    asm volatile("" : "+s"(t0));
    fused_body();
#pragma nounroll
    while (rt_now() - t0 < 20000u) asm volatile("s_sleep 2");
}

extern "C" void kernel_launch(void* const* d_in, const int* in_sizes, int n_in, void* d_out, int out_size, void* d_ws, size_t ws_size, hipStream_t stream) {
    Args a;
    a.Au = (const u16*)d_in[0]; a.AlogA = (const u16*)d_in[1]; a.Aim = (const u16*)d_in[2]; a.ACr = (const u16*)d_in[3]; a.ACi = (const u16*)d_in[4];
    a.Alogdt = (const u16*)d_in[5]; a.ADsk = (const u16*)d_in[6]; a.X = (const u16*)d_in[7]; a.mem0 = (const u16*)d_in[8];
    a.Wq = (const u16*)d_in[9]; a.Wk = (const u16*)d_in[10]; a.Wv = (const u16*)d_in[11]; a.Wo = (const u16*)d_in[12];
    a.out = (u16*)d_out;
    char* base = (char*)(((size_t)d_ws + (2u << 20) - 1) & ~(size_t)((2u << 20) - 1));
    a.HB = base;
    size_t off = 3u << 20;
    auto alloc = [&](size_t n) { char* p = base + off; off += (n + 4095) & ~(size_t)4095; return p; };
    a.WvoF = (u16*)alloc(28 * 16 * 14336); a.WqkF = (u16*)alloc(32 * 6 * 14336); a.GF = (u16*)alloc((size_t)64 * 16 * 28 * 1024); a.UF = (u16*)alloc((size_t)64 * 16 * 28 * 1024);
    a.EX = (u16*)alloc(64 * 16 * 2048); a.UM = (u16*)alloc(65 * 28 * 4096); a.MZX = (float*)alloc(64 * 16 * 256); a.TF = (u16*)alloc(65 * 32 * 3584); a.PA = (u16*)alloc(56 * 32768);
    (void)alloc(256 * 2048 * 4); a.err = (unsigned*)alloc(4096);
    (void)hipFuncSetAttribute((const void*)fused_kernel, hipFuncAttributeMaxDynamicSharedMemorySize, 160 * 1024);
    fused_kernel<<<256, 256, 160 * 1024, stream>>>(a);
}
```

```cpp
#define P_A 216
#define P_B 272
#include <hip/hip_runtime.h>
#include <stdint.h>
typedef unsigned short u16;
typedef unsigned long long u64;
typedef __attribute__((ext_vector_type(2))) __bf16 bf2;
typedef __attribute__((ext_vector_type(2))) float f2;
typedef __attribute__((ext_vector_type(4))) float f4;
typedef __attribute__((ext_vector_type(16))) float f16v;
typedef __attribute__((ext_vector_type(2))) unsigned u2;
typedef __attribute__((ext_vector_type(4))) unsigned u4;
typedef __attribute__((ext_vector_type(16))) unsigned u16v;
typedef __attribute__((ext_vector_type(4))) short s4;
#define LAS __attribute__((address_space(3)))
#define DI __device__ __forceinline__
#define SB() __builtin_amdgcn_sched_barrier(0)
#define PIN(x) asm volatile("" : "+v"(x))
#define WAITV(n, ...) asm volatile("s_waitcnt vmcnt(" #n ")" : __VA_ARGS__ :: "memory")
#define LOG2E 1.4426950408889634f
#define CSCALE (LOG2E * 0.0625f)
#define FF 0xFFFFFFFFu
#define OOB 0x7f000000
#define SPIN_MAX (1u << 18)

#define HB_SZ (2 << 20)
#define CTL_OFF 0
#define O_CTL ((1 << 20) + CTL_OFF)
#define O_ANN O_CTL
#define O_PAR (O_CTL + 4096)
#define O_RDY (O_CTL + 8192)
#define O_FW (O_CTL + 16384)
#define O_FG (O_CTL + 24576)
#define O_FE (O_CTL + 32768)
#define O_FH (O_CTL + 49152)
#define O_FP (O_CTL + 57344)
#define O_FA (O_CTL + 65536)
#define O_CNT (O_CTL + 73728)
#define O_EPOCH (O_CNT + 32768)
#define O_PROBE (65536 + 7936)

struct Args {
    const u16 *Au, *AlogA, *Aim, *ACr, *ACi, *Alogdt, *ADsk, *X, *mem0, *Wq, *Wk, *Wv, *Wo;
    u16* out;
    char* HB;
    u16 *WvoF, *WqkF, *GF, *UF, *EX, *UM, *PA; float *MZX, *SXM;
    unsigned* TS;
};
typedef const Args __attribute__((address_space(4))) CArgs;
DI CArgs& get_args() { CArgs* p = (CArgs*)__builtin_amdgcn_kernarg_segment_ptr(); asm volatile("" : "+s"(p)); return *p; }
extern __shared__ __attribute__((aligned(16))) char smem_[];

DI unsigned pk2(float a, float b) { f2 v = {a, b}; return __builtin_bit_cast(unsigned, __builtin_convertvector(v, bf2)); }
DI float bfr(float a) { return __uint_as_float(pk2(a, a) & 0xffff0000u); }
DI float bf2f(u16 x) { return __uint_as_float((unsigned)x << 16); }
DI unsigned san(unsigned x) { return x < 0xFFFFFFFEu ? x : 0xFFFFFFFEu; }
DI void bar() { asm volatile("s_waitcnt lgkmcnt(0)\n\ts_barrier" ::: "memory"); SB(); }
DI float x32max(float a) { auto r = __builtin_amdgcn_permlane32_swap(__float_as_uint(a), __float_as_uint(a), false, false); return fmaxf(__uint_as_float(r[0]), __uint_as_float(r[1])); }
DI float x32add(float a) { auto r = __builtin_amdgcn_permlane32_swap(__float_as_uint(a), __float_as_uint(a), false, false); return __uint_as_float(r[0]) + __uint_as_float(r[1]); }
DI u2 trread(const char LAS* p) { return __builtin_bit_cast(u2, __builtin_amdgcn_ds_read_tr16_b64_v4i16((LAS s4*)p)); }
DI u4 mksrd(const void* p, unsigned bytes) { u64 x = (u64)p; return (u4){(unsigned)x, (unsigned)(x >> 32) & 0xffffu, bytes, 0x00020000u}; }
DI unsigned now32() { u64 t; asm volatile("s_memtime %0\n\ts_waitcnt lgkmcnt(0)" : "=s"(t) :: "memory"); return (unsigned)t; }
DI unsigned rt_now() { u64 t; asm volatile("s_memrealtime %0\n\ts_waitcnt lgkmcnt(0)" : "=s"(t) :: "memory"); return (unsigned)t; }
DI u4 ld128(u4 srd, int voff, int soff) { u4 v; asm volatile("buffer_load_dwordx4 %0, %1, %2, %3 offen sc1" : "=v"(v) : "v"(voff), "s"(srd), "s"(soff) : "memory"); return v; }
DI void st128(u4 v, u4 srd, int voff, int soff) { asm volatile("buffer_store_dwordx4 %0, %1, %2, %3 offen sc1\n\ts_nop 1" :: "v"(v), "v"(voff), "s"(srd), "s"(soff) : "memory"); }
DI void st32(unsigned v, u4 srd, int voff, int soff) { asm volatile("buffer_store_dword %0, %1, %2, %3 offen sc1" :: "v"(v), "v"(voff), "s"(srd), "s"(soff) : "memory"); }
DI u4 sld128(u4 srd, int voff, int soff) { u4 v; asm volatile("s_nop 4\n\tbuffer_load_dwordx4 %0, %1, %2, %3 offen sc1" : "=v"(v) : "v"(voff), "s"(srd), "s"(soff) : "memory"); return v; }
DI u2 sld64(u4 srd, int voff, int soff) { u2 v; asm volatile("s_nop 4\n\tbuffer_load_dwordx2 %0, %1, %2, %3 offen sc1" : "=v"(v) : "v"(voff), "s"(srd), "s"(soff) : "memory"); return v; }
DI unsigned sld32(u4 srd, int voff, int soff) { unsigned v; asm volatile("s_nop 4\n\tbuffer_load_dword %0, %1, %2, %3 offen sc1" : "=v"(v) : "v"(voff), "s"(srd), "s"(soff) : "memory"); return v; }
DI void sst128(u4 v, u4 srd, int voff, int soff) { asm volatile("s_nop 4\n\tbuffer_store_dwordx4 %0, %1, %2, %3 offen sc1\n\ts_nop 1" :: "v"(v), "v"(voff), "s"(srd), "s"(soff) : "memory"); }
DI void sst64(u2 v, u4 srd, int voff, int soff) { asm volatile("s_nop 4\n\tbuffer_store_dwordx2 %0, %1, %2, %3 offen sc1" :: "v"(v), "v"(voff), "s"(srd), "s"(soff) : "memory"); }
DI void sst32(unsigned v, u4 srd, int voff, int soff) { asm volatile("s_nop 4\n\tbuffer_store_dword %0, %1, %2, %3 offen sc1" :: "v"(v), "v"(voff), "s"(srd), "s"(soff) : "memory"); }
template <int N> DI void waitvm() { asm volatile("s_waitcnt vmcnt(%0)" :: "i"(N) : "memory"); }
DI int fresh_lane() { int l; asm volatile("v_mbcnt_lo_u32_b32 %0, -1, 0\n\tv_mbcnt_hi_u32_b32 %0, -1, %0" : "=v"(l)); __builtin_assume(l >= 0 && l < 64); return l; }
DI void wait_flags(u4 rhb, int voff_or_oob, unsigned want) {
    unsigned sp = 0;
#pragma nounroll
    for (;;) {
        unsigned v = sld32(rhb, voff_or_oob, 0); WAITV(0, "+v"(v));
        if (__all((v - want <= 72u) | (voff_or_oob >= OOB))) break;
        asm volatile("s_sleep 4" ::: "memory");
        if (++sp > SPIN_MAX) __builtin_amdgcn_endpgm();
    }
}
#define PSTAMP(k) do { } while (0)
#define RFL(x) (unsigned)__builtin_amdgcn_readfirstlane(x)
#define STAMPS 0
struct Ctx { u4 r, rw, rg, rx, rts; unsigned t0, tf0, per, R, tol, tag, sw, sg, sx, dm, des, role; unsigned idx, vl16, vm, vg, vg3, vtl, vtr, vsel, vpub, vraw, vfl, vfo, ve, vs, vrw, vrr, vr1, vr2, vts, vdbg, vtb; };
DI void chain_asm(const Ctx& c) {
    const u16v s0 = {RFL(c.r[0]), RFL(c.r[1]), RFL(c.r[2]), RFL(c.r[3]), RFL(c.rw[0]), RFL(c.rw[1]), RFL(c.rw[2]), RFL(c.rw[3]), RFL(c.rg[0]), RFL(c.rg[1]), RFL(c.rg[2]), RFL(c.rg[3]), RFL(c.rx[0]), RFL(c.rx[1]), RFL(c.rx[2]), RFL(c.rx[3])}, s1 = {RFL(c.rts[0]), RFL(c.rts[1]), RFL(c.rts[2]), RFL(c.rts[3]), RFL(c.t0), RFL(c.tf0), RFL(c.per), RFL(c.R), RFL(c.tol), RFL(c.tag), RFL(c.sw), RFL(c.sg), RFL(c.sx), RFL(c.dm), RFL(c.des), RFL(c.role)}, v0 = {c.idx, c.vl16, c.vm, c.vg, c.vg3, c.vtl, c.vtr, c.vsel, c.vpub, c.vraw, c.vfl, c.vfo, c.ve, c.vs, c.vrw, c.vrr}, v1 = {c.vr1, c.vr2, c.vts, c.vdbg, c.vtb, 0u, 0u, 0u, 0u, 0u, 0u, 0u, 0u, 0u, 0u, 0u};
    asm volatile(
"s_nop 4\ns_cmp_eq_u32 s39, 0\ns_cbranch_scc1 fk_jh0\ns_cmp_eq_u32 s39, 1\n"
"s_cbranch_scc1 fk_jh1\ns_cmp_eq_u32 s39, 2\ns_cbranch_scc1 fk_jh2\ns_cmp_eq_u32 s39, 3\n"
"s_cbranch_scc1 fk_jh3\ns_cmp_eq_u32 s39, 4\ns_cbranch_scc1 fk_jx0\ns_cmp_eq_u32 s39, 5\n"
"s_cbranch_scc1 fk_jx1\ns_cmp_eq_u32 s39, 6\ns_cbranch_scc1 fk_jx2\ns_cmp_eq_u32 s39, 7\n"
"s_cbranch_scc1 fk_jx3\ns_cmp_eq_u32 s39, 8\ns_cbranch_scc1 fk_jp0\ns_cmp_eq_u32 s39, 9\n"
"s_cbranch_scc1 fk_jp1\ns_cmp_eq_u32 s39, 10\ns_cbranch_scc1 fk_jp2\ns_cmp_eq_u32 s39, 11\n"
"s_cbranch_scc1 fk_jp3\ns_endpgm\nfk_jh0:\ns_branch fk_h0\n"
"fk_jh1:\ns_branch fk_h1\nfk_jh2:\ns_branch fk_h2\n"
"fk_jh3:\ns_branch fk_h3\nfk_jx0:\ns_branch fk_x0\n"
"fk_jx1:\ns_branch fk_x1\nfk_jx2:\ns_branch fk_x2\n"
"fk_jx3:\ns_branch fk_x3\nfk_jp0:\ns_branch fk_p0\n"
"fk_jp1:\ns_branch fk_p1\nfk_jp2:\ns_branch fk_p2\n"
"fk_jp3:\ns_branch fk_p3\nfk_h0:\ns_mov_b32 s40, 0\n"
"s_mov_b32 s41, 0\ns_mov_b32 s42, 262144\ns_mov_b32 s43, 393216\ns_mov_b32 s44, s28\n"
"s_mov_b32 s45, 0\ns_mov_b32 s46, 0\ns_mov_b32 s92, 0\ns_mov_b32 s49, 0\n"
"s_mov_b32 s53, 0\ns_mov_b32 s50, s35\ns_mov_b32 s51, s36\nv_mov_b32 v131, v13\n"
"v_mov_b32 v132, v14\nv_mov_b32 v134, 24576\nv_mov_b32 v115, 0\nv_mov_b32 v120, -1\n"
"v_mov_b32 v121, -1\nv_mov_b32 v122, -1\nv_mov_b32 v123, -1\ns_add_u32 s47, s34, 0\n"
"buffer_load_dwordx4 a[0:3], v9, s[12:15], s47 offen sc1\nbuffer_load_dwordx4 a[4:7], v9, s[12:15], s47 offen offset:1024 sc1\ns_add_u32 s47, s34, 2048\nbuffer_load_dwordx4 a[8:11], v9, s[12:15], s47 offen sc1\n"
"buffer_load_dwordx4 a[12:15], v9, s[12:15], s47 offen offset:1024 sc1\ns_add_u32 s47, s34, 4096\nbuffer_load_dwordx4 a[16:19], v9, s[12:15], s47 offen sc1\nbuffer_load_dwordx4 a[20:23], v9, s[12:15], s47 offen offset:1024 sc1\n"
"s_add_u32 s47, s34, 6144\nbuffer_load_dwordx4 a[24:27], v9, s[12:15], s47 offen sc1\nbuffer_load_dwordx4 a[28:31], v9, s[12:15], s47 offen offset:1024 sc1\ns_add_u32 s47, s34, 14336\n"
"buffer_load_dwordx4 a[32:35], v9, s[12:15], s47 offen sc1\nbuffer_load_dwordx4 a[36:39], v9, s[12:15], s47 offen offset:1024 sc1\ns_add_u32 s47, s34, 16384\nbuffer_load_dwordx4 a[40:43], v9, s[12:15], s47 offen sc1\n"
"buffer_load_dwordx4 a[44:47], v9, s[12:15], s47 offen offset:1024 sc1\ns_add_u32 s47, s34, 18432\nbuffer_load_dwordx4 a[48:51], v9, s[12:15], s47 offen sc1\nbuffer_load_dwordx4 a[52:55], v9, s[12:15], s47 offen offset:1024 sc1\n"
"s_add_u32 s47, s34, 20480\nbuffer_load_dwordx4 a[56:59], v9, s[12:15], s47 offen sc1\nbuffer_load_dwordx4 a[60:63], v9, s[12:15], s47 offen offset:1024 sc1\ns_add_u32 s47, s34, 28672\n"
"buffer_load_dwordx4 a[64:67], v9, s[12:15], s47 offen sc1\nbuffer_load_dwordx4 a[68:71], v9, s[12:15], s47 offen offset:1024 sc1\ns_add_u32 s47, s34, 30720\nbuffer_load_dwordx4 a[72:75], v9, s[12:15], s47 offen sc1\n"
"buffer_load_dwordx4 a[76:79], v9, s[12:15], s47 offen offset:1024 sc1\ns_add_u32 s47, s34, 32768\nbuffer_load_dwordx4 a[80:83], v9, s[12:15], s47 offen sc1\nbuffer_load_dwordx4 a[84:87], v9, s[12:15], s47 offen offset:1024 sc1\n"
"s_add_u32 s47, s34, 34816\nbuffer_load_dwordx4 a[88:91], v9, s[12:15], s47 offen sc1\nbuffer_load_dwordx4 a[92:95], v9, s[12:15], s47 offen offset:1024 sc1\ns_add_u32 s47, s34, 43008\n"
"buffer_load_dwordx4 a[96:99], v9, s[12:15], s47 offen sc1\nbuffer_load_dwordx4 a[100:103], v9, s[12:15], s47 offen offset:1024 sc1\ns_add_u32 s47, s34, 45056\nbuffer_load_dwordx4 a[104:107], v9, s[12:15], s47 offen sc1\n"
"buffer_load_dwordx4 a[108:111], v9, s[12:15], s47 offen offset:1024 sc1\ns_add_u32 s47, s34, 47104\nbuffer_load_dwordx4 a[112:115], v9, s[12:15], s47 offen sc1\nbuffer_load_dwordx4 a[116:119], v9, s[12:15], s47 offen offset:1024 sc1\n"
"s_add_u32 s47, s34, 49152\nbuffer_load_dwordx4 a[120:123], v9, s[12:15], s47 offen sc1\nbuffer_load_dwordx4 a[124:127], v9, s[12:15], s47 offen offset:1024 sc1\ns_add_u32 s47, s50, 7168\n"
"buffer_load_dwordx4 v[72:75], v11, s[16:19], s50 offen\nbuffer_load_dwordx4 v[76:79], v12, s[16:19], s50 offen\nbuffer_load_dwordx4 v[80:83], v11, s[16:19], s47 offen\nbuffer_load_dwordx4 v[84:87], v12, s[16:19], s47 offen\n"
"s_add_u32 s50, s50, 28672\ns_waitcnt vmcnt(0)\ns_barrier\n.p2align 6\n"
"top_h0:\nv_mov_b32 v56, 0\nv_mov_b32 v57, 0\nv_mov_b32 v58, 0\n"
"v_mov_b32 v59, 0\nv_mov_b32 v60, 0\nv_mov_b32 v61, 0\nv_mov_b32 v62, 0\n"
"v_mov_b32 v63, 0\nv_mov_b32 v64, 0\nv_mov_b32 v65, 0\nv_mov_b32 v66, 0\n"
"v_mov_b32 v67, 0\nv_mov_b32 v68, 0\nv_mov_b32 v69, 0\nv_mov_b32 v70, 0\n"
"v_mov_b32 v71, 0\nv_mov_b32 v128, 0\ns_mul_i32 s48, s45, 6\ns_and_b32 s48, s48, -4\n"
"s_getpc_b64 s[60:61]\nspc_h0:\ns_sub_u32 s48, sgo_h0-spc_h0, s48\ns_add_u32 s60, s60, s48\n"
"s_addc_u32 s61, s61, 0\nsf_h0:\ns_memrealtime s[54:55]\ns_nop 2\n"
"s_memrealtime s[56:57]\ns_nop 2\ns_memrealtime s[58:59]\ns_waitcnt lgkmcnt(4)\n"
"s_sub_u32 s47, s54, s44\ns_add_u32 s48, s47, 12\ns_cmp_ge_u32 s48, 12\ns_cbranch_scc1 so_h0\n"
"s_memrealtime s[54:55]\ns_waitcnt lgkmcnt(4)\ns_cmp_ge_u32 s56, s44\ns_cbranch_scc1 sx_h0\n"
"s_memrealtime s[56:57]\ns_waitcnt lgkmcnt(4)\ns_cmp_ge_u32 s58, s44\ns_cbranch_scc1 sx_h0\n"
"s_memrealtime s[58:59]\ns_waitcnt lgkmcnt(4)\ns_cmp_ge_u32 s54, s44\ns_cbranch_scc1 sx_h0\n"
"s_memrealtime s[54:55]\ns_waitcnt lgkmcnt(4)\ns_cmp_ge_u32 s56, s44\ns_cbranch_scc1 sx_h0\n"
"s_memrealtime s[56:57]\ns_waitcnt lgkmcnt(4)\ns_cmp_ge_u32 s58, s44\ns_cbranch_scc1 sx_h0\n"
"s_memrealtime s[58:59]\ns_waitcnt lgkmcnt(4)\ns_cmp_ge_u32 s54, s44\ns_cbranch_scc1 sx_h0\n"
"s_memrealtime s[54:55]\ns_waitcnt lgkmcnt(4)\ns_cmp_ge_u32 s56, s44\ns_cbranch_scc1 sx_h0\n"
"s_memrealtime s[56:57]\ns_waitcnt lgkmcnt(4)\ns_cmp_ge_u32 s58, s44\ns_cbranch_scc1 sx_h0\n"
"s_memrealtime s[58:59]\ns_waitcnt lgkmcnt(4)\ns_cmp_ge_u32 s54, s44\ns_cbranch_scc1 sx_h0\n"
"s_memrealtime s[54:55]\ns_waitcnt lgkmcnt(4)\ns_cmp_ge_u32 s56, s44\ns_cbranch_scc1 sx_h0\n"
"s_memrealtime s[56:57]\ns_waitcnt lgkmcnt(4)\ns_cmp_ge_u32 s58, s44\ns_cbranch_scc1 sx_h0\n"
"s_memrealtime s[58:59]\ns_waitcnt lgkmcnt(4)\ns_cmp_ge_u32 s54, s44\ns_cbranch_scc1 sx_h0\n"
"s_memrealtime s[54:55]\ns_waitcnt lgkmcnt(4)\ns_cmp_ge_u32 s56, s44\ns_cbranch_scc1 sx_h0\n"
"s_memrealtime s[56:57]\ns_waitcnt lgkmcnt(4)\ns_cmp_ge_u32 s58, s44\ns_cbranch_scc1 sx_h0\n"
"s_memrealtime s[58:59]\ns_waitcnt lgkmcnt(4)\ns_cmp_ge_u32 s54, s44\ns_cbranch_scc1 sx_h0\n"
"s_memrealtime s[54:55]\ns_waitcnt lgkmcnt(4)\ns_cmp_ge_u32 s56, s44\ns_cbranch_scc1 sx_h0\n"
"s_memrealtime s[56:57]\ns_branch sx_h0\nso_h0:\ns_waitcnt lgkmcnt(0)\n"
"s_cmp_lt_i32 s47, 0\ns_cbranch_scc1 sf_h0\ns_branch sgo_h0\nsx_h0:\n"
"s_setpc_b64 s[60:61]\ns_nop 0\ns_nop 0\ns_nop 0\n"
"s_nop 0\ns_nop 0\ns_nop 0\ns_nop 0\n"
"s_nop 0\ns_nop 0\ns_nop 0\ns_nop 0\n"
"s_nop 0\ns_nop 0\ns_nop 0\ns_nop 0\n"
"s_nop 0\ns_nop 0\ns_nop 0\ns_nop 0\n"
"s_nop 0\ns_nop 0\ns_nop 0\ns_nop 0\n"
"s_nop 0\nsgo_h0:\nbuffer_load_dwordx4 v[40:43], v10, s[8:11], s43 offen offset:0 sc1\nbuffer_load_dwordx4 v[44:47], v10, s[8:11], s43 offen offset:1024 sc1\n"
"s_nop 15\ns_nop 3\nbuffer_load_dwordx4 v[48:51], v10, s[8:11], s43 offen offset:2048 sc1\ns_nop 13\n"
"buffer_load_dwordx4 v[52:55], v10, s[8:11], s43 offen offset:3072 sc1\nlanded_h0:\ns_waitcnt vmcnt(3)\nv_smfmac_f32_16x16x64_bf16 v[56:59], v[40:43], a[0:7], v8\n"
"v_max3_u32 v128, v128, v40, v41\nv_smfmac_f32_16x16x64_bf16 v[60:63], v[40:43], a[32:39], v8\nv_max3_u32 v128, v128, v42, v43\nv_smfmac_f32_16x16x64_bf16 v[64:67], v[40:43], a[64:71], v8\n"
"v_smfmac_f32_16x16x64_bf16 v[68:71], v[40:43], a[96:103], v8\ns_waitcnt vmcnt(2)\nv_smfmac_f32_16x16x64_bf16 v[56:59], v[44:47], a[8:15], v8\nv_max3_u32 v128, v128, v44, v45\n"
"v_smfmac_f32_16x16x64_bf16 v[60:63], v[44:47], a[40:47], v8\nv_max3_u32 v128, v128, v46, v47\nv_smfmac_f32_16x16x64_bf16 v[64:67], v[44:47], a[72:79], v8\nv_smfmac_f32_16x16x64_bf16 v[68:71], v[44:47], a[104:111], v8\n"
"s_waitcnt vmcnt(1)\nv_smfmac_f32_16x16x64_bf16 v[56:59], v[48:51], a[16:23], v8\nv_max3_u32 v128, v128, v48, v49\nv_smfmac_f32_16x16x64_bf16 v[60:63], v[48:51], a[48:55], v8\n"
"v_max3_u32 v128, v128, v50, v51\nv_smfmac_f32_16x16x64_bf16 v[64:67], v[48:51], a[80:87], v8\nv_smfmac_f32_16x16x64_bf16 v[68:71], v[48:51], a[112:119], v8\ns_waitcnt vmcnt(0)\n"
"v_smfmac_f32_16x16x64_bf16 v[56:59], v[52:55], a[24:31], v8\nv_max3_u32 v128, v128, v52, v53\nv_smfmac_f32_16x16x64_bf16 v[60:63], v[52:55], a[56:63], v8\nv_max3_u32 v128, v128, v54, v55\n"
"v_smfmac_f32_16x16x64_bf16 v[64:67], v[52:55], a[88:95], v8\nv_cmp_eq_u32 vcc, -1, v128\nv_smfmac_f32_16x16x64_bf16 v[68:71], v[52:55], a[120:127], v8\nv_add_f32 v129, v56, v57\n"
"v_add_f32 v130, v58, v59\nv_cvt_pk_bf16_f32 v112, v129, v130\nv_add_f32 v129, v60, v61\nv_add_f32 v130, v62, v63\n"
"v_cvt_pk_bf16_f32 v113, v129, v130\nv_add_f32 v129, v64, v65\nv_add_f32 v130, v66, v67\nv_cvt_pk_bf16_f32 v114, v129, v130\n"
"v_add_f32 v129, v68, v69\nv_add_f32 v130, v70, v71\nv_cvt_pk_bf16_f32 v115, v129, v130\nds_write2_b64 v131, v[112:113], v[114:115] offset0:0 offset1:16\n"
"s_cbranch_vccnz retry_h0\nv_mov_b32 v129, 0\ns_nop 1\nv_mov_b32 v129, 0\n"
"s_nop 1\ns_waitcnt lgkmcnt(0)\ns_barrier\ns_nop 9\n"
"ds_read_b64_tr_b16 v[88:89], v132 offset:0\nds_read_b64_tr_b16 v[90:91], v132 offset:32\nds_read_b64_tr_b16 v[92:93], v132 offset:256\nds_read_b64_tr_b16 v[94:95], v132 offset:288\n"
"ds_read_b64_tr_b16 v[96:97], v132 offset:512\nds_read_b64_tr_b16 v[98:99], v132 offset:544\nds_read_b64_tr_b16 v[100:101], v132 offset:768\nds_read_b64_tr_b16 v[102:103], v132 offset:800\n"
"s_waitcnt lgkmcnt(6)\nv_mfma_f32_16x16x32_bf16 v[104:107], v[88:91], v[72:75], 0\nv_mfma_f32_16x16x32_bf16 v[108:111], v[88:91], v[80:83], 0\ns_waitcnt lgkmcnt(4)\n"
"v_mfma_f32_16x16x32_bf16 v[104:107], v[92:95], v[76:79], v[104:107]\nv_mfma_f32_16x16x32_bf16 v[108:111], v[92:95], v[84:87], v[108:111]\ns_waitcnt lgkmcnt(2)\nv_mfma_f32_16x16x32_bf16 v[104:107], v[96:99], v[72:75], v[104:107]\n"
"v_mfma_f32_16x16x32_bf16 v[108:111], v[96:99], v[80:83], v[108:111]\ns_waitcnt lgkmcnt(0)\nv_mfma_f32_16x16x32_bf16 v[104:107], v[100:103], v[76:79], v[104:107]\nv_mfma_f32_16x16x32_bf16 v[108:111], v[100:103], v[84:87], v[108:111]\n"
"s_nop 7\nv_add_f32_dpp v124, v106, v104 row_ror:8 row_mask:0xf bank_mask:0x3\nv_add_f32_dpp v124, v108, v110 row_ror:8 row_mask:0xf bank_mask:0xc\nv_add_f32_dpp v125, v107, v105 row_ror:8 row_mask:0xf bank_mask:0x3\n"
"v_add_f32_dpp v125, v109, v111 row_ror:8 row_mask:0xf bank_mask:0xc\ns_add_u32 s47, s50, 7168\nbuffer_load_dwordx4 v[72:75], v11, s[16:19], s50 offen\nbuffer_load_dwordx4 v[76:79], v12, s[16:19], s50 offen\n"
"buffer_load_dwordx4 v[80:83], v11, s[16:19], s47 offen\nbuffer_load_dwordx4 v[84:87], v12, s[16:19], s47 offen\ns_add_u32 s50, s50, 28672\nbuffer_store_dwordx2 v[124:125], v16, s[20:23], s51 offen sc1\n"
"s_add_u32 s51, s51, 1024\ns_add_u32 s47, s40, s33\nv_mov_b32 v135, s47\nbuffer_store_dword v135, v18, s[8:11], 0 offen sc1\n"
"ds_read_b32 v133, v134\nv_xor_b32 v131, 8192, v131\nv_xor_b32 v132, 8192, v132\ns_waitcnt lgkmcnt(0)\n"
"v_readfirstlane_b32 s47, v133\ns_sub_u32 s47, s47, s40\ns_cmp_eq_u32 s47, 1\ns_cbranch_scc1 adopt_h0\n"
"back_h0:\ns_add_u32 s40, s40, 1\ns_mov_b32 s43, s41\ns_add_u32 s41, s41, 131072\n"
"s_and_b32 s41, s41, 524287\ns_xor_b32 s42, s41, 262144\ns_and_b32 s47, s46, 15\ns_mul_i32 s48, s47, 4\n"
"s_add_u32 s45, s45, s48\ns_add_u32 s45, s45, s30\ns_lshr_b32 s48, s45, 4\ns_lshl_b32 s48, s48, 2\n"
"s_add_u32 s44, s44, s48\ns_and_b32 s45, s45, 15\ns_add_u32 s92, s92, 1\ns_lshr_b32 s48, s46, 4\n"
"s_cmp_gt_u32 s47, s48\ns_cselect_b32 s47, 1, 0\ns_and_b32 s48, s92, 1\ns_cselect_b32 s47, 0, s47\n"
"s_sub_u32 s46, s46, s47\ns_waitcnt lgkmcnt(0)\ns_cmp_lt_u32 s40, 64\ns_cbranch_scc1 top_h0\n"
"s_add_u32 s47, s33, 65\nv_mov_b32 v135, s47\ns_waitcnt vmcnt(0)\nbuffer_store_dword v135, v18, s[8:11], 0 offen sc1\n"
"s_branch end_h0\nretry2_h0:\ns_sleep 3\nretry_h0:\n"
"s_add_u32 s53, s53, 1\ns_add_u32 s49, s49, 1\ns_cmp_gt_u32 s49, 65536\ns_cbranch_scc1 end_h0\n"
"buffer_load_dwordx4 v[40:43], v10, s[8:11], s43 offen offset:0 sc1\nbuffer_load_dwordx4 v[44:47], v10, s[8:11], s43 offen offset:1024 sc1\nbuffer_load_dwordx4 v[48:51], v10, s[8:11], s43 offen offset:2048 sc1\nbuffer_load_dwordx4 v[52:55], v10, s[8:11], s43 offen offset:3072 sc1\n"
"v_mov_b32 v128, 0\ns_add_u32 s47, s40, 1\nv_mov_b32 v133, s47\nds_write_b32 v134, v133\n"
"s_waitcnt vmcnt(0)\nv_max3_u32 v128, v128, v40, v41\nv_max3_u32 v128, v128, v42, v43\nv_max3_u32 v128, v128, v44, v45\n"
"v_max3_u32 v128, v128, v46, v47\nv_max3_u32 v128, v128, v48, v49\nv_max3_u32 v128, v128, v50, v51\nv_max3_u32 v128, v128, v52, v53\n"
"v_max3_u32 v128, v128, v54, v55\nv_cmp_eq_u32 vcc, -1, v128\nv_mov_b32 v56, 0\nv_mov_b32 v57, 0\n"
"v_mov_b32 v58, 0\nv_mov_b32 v59, 0\nv_mov_b32 v60, 0\nv_mov_b32 v61, 0\n"
"v_mov_b32 v62, 0\nv_mov_b32 v63, 0\nv_mov_b32 v64, 0\nv_mov_b32 v65, 0\n"
"v_mov_b32 v66, 0\nv_mov_b32 v67, 0\nv_mov_b32 v68, 0\nv_mov_b32 v69, 0\n"
"v_mov_b32 v70, 0\nv_mov_b32 v71, 0\ns_cbranch_vccnz retry2_h0\ns_branch landed_h0\n"
"adopt_h0:\nbuffer_load_dwordx4 v[116:119], v28, s[8:11], 0 offen sc1\ns_lshl_b32 s52, s40, 26\ns_or_b32 s47, s52, 0x3ffffff\n"
"s_waitcnt vmcnt(0)\nv_cmp_ge_u32 vcc, s47, v116\ns_nop 1\nv_cndmask_b32 v116, 0, v116, vcc\n"
"v_cmp_ge_u32 vcc, s47, v117\ns_nop 1\nv_cndmask_b32 v117, 0, v117, vcc\nv_cmp_ge_u32 vcc, s47, v118\n"
"s_nop 1\nv_cndmask_b32 v118, 0, v118, vcc\nv_cmp_ge_u32 vcc, s47, v119\ns_nop 1\n"
"v_cndmask_b32 v119, 0, v119, vcc\nv_max3_u32 v133, v116, v117, v118\nv_max_u32 v133, v133, v119\ns_nop 1\n"
"v_max_u32_dpp v133, v133, v133 row_ror:8 row_mask:0xf bank_mask:0xf\ns_nop 1\nv_max_u32_dpp v133, v133, v133 row_ror:4 row_mask:0xf bank_mask:0xf\ns_nop 1\n"
"v_max_u32_dpp v133, v133, v133 row_ror:2 row_mask:0xf bank_mask:0xf\ns_nop 1\nv_max_u32_dpp v133, v133, v133 row_ror:1 row_mask:0xf bank_mask:0xf\ns_nop 0\n"
"v_readlane_b32 s47, v133, 0\nv_readlane_b32 s48, v133, 16\ns_max_u32 s48, s48, s47\ns_cmp_le_u32 s48, s52\n"
"s_cbranch_scc1 back_h0\ns_bfe_u32 s52, s48, 1179656\ns_add_u32 s44, s52, s28\ns_mov_b32 s45, 0\n"
"s_and_b32 s46, s48, 255\ns_mov_b32 s92, 0\ns_branch back_h0\nend_h0:\n"
"s_branch fk_done\nfk_h1:\ns_mov_b32 s40, 0\ns_mov_b32 s41, 0\n"
"s_mov_b32 s42, 262144\ns_mov_b32 s43, 393216\ns_mov_b32 s44, s28\ns_mov_b32 s45, 0\n"
"s_mov_b32 s46, 0\ns_mov_b32 s92, 0\ns_mov_b32 s49, 0\ns_mov_b32 s53, 0\n"
"s_mov_b32 s50, s35\ns_mov_b32 s51, s36\nv_mov_b32 v131, v13\nv_mov_b32 v132, v14\n"
"v_mov_b32 v134, 24576\nv_mov_b32 v115, 0\nv_mov_b32 v120, -1\nv_mov_b32 v121, -1\n"
"v_mov_b32 v122, -1\nv_mov_b32 v123, -1\ns_add_u32 s47, s34, 59392\nbuffer_load_dwordx4 a[8:11], v9, s[12:15], s47 offen sc1\n"
"buffer_load_dwordx4 a[12:15], v9, s[12:15], s47 offen offset:1024 sc1\ns_add_u32 s47, s34, 57344\nbuffer_load_dwordx4 a[0:3], v9, s[12:15], s47 offen sc1\nbuffer_load_dwordx4 a[4:7], v9, s[12:15], s47 offen offset:1024 sc1\n"
"s_add_u32 s47, s34, 63488\nbuffer_load_dwordx4 a[24:27], v9, s[12:15], s47 offen sc1\nbuffer_load_dwordx4 a[28:31], v9, s[12:15], s47 offen offset:1024 sc1\ns_add_u32 s47, s34, 61440\n"
"buffer_load_dwordx4 a[16:19], v9, s[12:15], s47 offen sc1\nbuffer_load_dwordx4 a[20:23], v9, s[12:15], s47 offen offset:1024 sc1\ns_add_u32 s47, s34, 73728\nbuffer_load_dwordx4 a[40:43], v9, s[12:15], s47 offen sc1\n"
"buffer_load_dwordx4 a[44:47], v9, s[12:15], s47 offen offset:1024 sc1\ns_add_u32 s47, s34, 71680\nbuffer_load_dwordx4 a[32:35], v9, s[12:15], s47 offen sc1\nbuffer_load_dwordx4 a[36:39], v9, s[12:15], s47 offen offset:1024 sc1\n"
"s_add_u32 s47, s34, 77824\nbuffer_load_dwordx4 a[56:59], v9, s[12:15], s47 offen sc1\nbuffer_load_dwordx4 a[60:63], v9, s[12:15], s47 offen offset:1024 sc1\ns_add_u32 s47, s34, 75776\n"
"buffer_load_dwordx4 a[48:51], v9, s[12:15], s47 offen sc1\nbuffer_load_dwordx4 a[52:55], v9, s[12:15], s47 offen offset:1024 sc1\ns_add_u32 s47, s34, 88064\nbuffer_load_dwordx4 a[72:75], v9, s[12:15], s47 offen sc1\n"
"buffer_load_dwordx4 a[76:79], v9, s[12:15], s47 offen offset:1024 sc1\ns_add_u32 s47, s34, 86016\nbuffer_load_dwordx4 a[64:67], v9, s[12:15], s47 offen sc1\nbuffer_load_dwordx4 a[68:71], v9, s[12:15], s47 offen offset:1024 sc1\n"
"s_add_u32 s47, s34, 92160\nbuffer_load_dwordx4 a[88:91], v9, s[12:15], s47 offen sc1\nbuffer_load_dwordx4 a[92:95], v9, s[12:15], s47 offen offset:1024 sc1\ns_add_u32 s47, s34, 90112\n"
"buffer_load_dwordx4 a[80:83], v9, s[12:15], s47 offen sc1\nbuffer_load_dwordx4 a[84:87], v9, s[12:15], s47 offen offset:1024 sc1\ns_add_u32 s47, s50, 7168\nbuffer_load_dwordx4 v[72:75], v11, s[16:19], s50 offen\n"
"buffer_load_dwordx4 v[76:79], v12, s[16:19], s50 offen\nbuffer_load_dwordx4 v[80:83], v11, s[16:19], s47 offen\nbuffer_load_dwordx4 v[84:87], v12, s[16:19], s47 offen\ns_add_u32 s50, s50, 28672\n"
"s_waitcnt vmcnt(0)\ns_barrier\n.p2align 6\ntop_h1:\n"
"v_mov_b32 v56, 0\nv_mov_b32 v57, 0\nv_mov_b32 v58, 0\nv_mov_b32 v59, 0\n"
"v_mov_b32 v60, 0\nv_mov_b32 v61, 0\nv_mov_b32 v62, 0\nv_mov_b32 v63, 0\n"
"v_mov_b32 v64, 0\nv_mov_b32 v65, 0\nv_mov_b32 v66, 0\nv_mov_b32 v67, 0\n"
"v_mov_b32 v128, 0\ns_mul_i32 s48, s45, 6\ns_and_b32 s48, s48, -4\ns_getpc_b64 s[60:61]\n"
"spc_h1:\ns_sub_u32 s48, sgo_h1-spc_h1, s48\ns_add_u32 s60, s60, s48\ns_addc_u32 s61, s61, 0\n"
"sf_h1:\ns_memrealtime s[54:55]\ns_nop 2\ns_memrealtime s[56:57]\n"
"s_nop 2\ns_memrealtime s[58:59]\ns_waitcnt lgkmcnt(4)\ns_sub_u32 s47, s54, s44\n"
"s_add_u32 s48, s47, 12\ns_cmp_ge_u32 s48, 12\ns_cbranch_scc1 so_h1\ns_memrealtime s[54:55]\n"
"s_waitcnt lgkmcnt(4)\ns_cmp_ge_u32 s56, s44\ns_cbranch_scc1 sx_h1\ns_memrealtime s[56:57]\n"
"s_waitcnt lgkmcnt(4)\ns_cmp_ge_u32 s58, s44\ns_cbranch_scc1 sx_h1\ns_memrealtime s[58:59]\n"
"s_waitcnt lgkmcnt(4)\ns_cmp_ge_u32 s54, s44\ns_cbranch_scc1 sx_h1\ns_memrealtime s[54:55]\n"
"s_waitcnt lgkmcnt(4)\ns_cmp_ge_u32 s56, s44\ns_cbranch_scc1 sx_h1\ns_memrealtime s[56:57]\n"
"s_waitcnt lgkmcnt(4)\ns_cmp_ge_u32 s58, s44\ns_cbranch_scc1 sx_h1\ns_memrealtime s[58:59]\n"
"s_waitcnt lgkmcnt(4)\ns_cmp_ge_u32 s54, s44\ns_cbranch_scc1 sx_h1\ns_memrealtime s[54:55]\n"
"s_waitcnt lgkmcnt(4)\ns_cmp_ge_u32 s56, s44\ns_cbranch_scc1 sx_h1\ns_memrealtime s[56:57]\n"
"s_waitcnt lgkmcnt(4)\ns_cmp_ge_u32 s58, s44\ns_cbranch_scc1 sx_h1\ns_memrealtime s[58:59]\n"
"s_waitcnt lgkmcnt(4)\ns_cmp_ge_u32 s54, s44\ns_cbranch_scc1 sx_h1\ns_memrealtime s[54:55]\n"
"s_waitcnt lgkmcnt(4)\ns_cmp_ge_u32 s56, s44\ns_cbranch_scc1 sx_h1\ns_memrealtime s[56:57]\n"
"s_waitcnt lgkmcnt(4)\ns_cmp_ge_u32 s58, s44\ns_cbranch_scc1 sx_h1\ns_memrealtime s[58:59]\n"
"s_waitcnt lgkmcnt(4)\ns_cmp_ge_u32 s54, s44\ns_cbranch_scc1 sx_h1\ns_memrealtime s[54:55]\n"
"s_waitcnt lgkmcnt(4)\ns_cmp_ge_u32 s56, s44\ns_cbranch_scc1 sx_h1\ns_memrealtime s[56:57]\n"
"s_waitcnt lgkmcnt(4)\ns_cmp_ge_u32 s58, s44\ns_cbranch_scc1 sx_h1\ns_memrealtime s[58:59]\n"
"s_waitcnt lgkmcnt(4)\ns_cmp_ge_u32 s54, s44\ns_cbranch_scc1 sx_h1\ns_memrealtime s[54:55]\n"
"s_waitcnt lgkmcnt(4)\ns_cmp_ge_u32 s56, s44\ns_cbranch_scc1 sx_h1\ns_memrealtime s[56:57]\n"
"s_branch sx_h1\nso_h1:\ns_waitcnt lgkmcnt(0)\ns_cmp_lt_i32 s47, 0\n"
"s_cbranch_scc1 sf_h1\ns_branch sgo_h1\nsx_h1:\ns_setpc_b64 s[60:61]\n"
"s_nop 0\ns_nop 0\ns_nop 0\ns_nop 0\n"
"s_nop 0\ns_nop 0\ns_nop 0\ns_nop 0\n"
"s_nop 0\ns_nop 0\ns_nop 0\ns_nop 0\n"
"s_nop 0\ns_nop 0\ns_nop 0\ns_nop 0\n"
"s_nop 0\ns_nop 0\ns_nop 0\ns_nop 0\n"
"s_nop 0\ns_nop 0\ns_nop 0\ns_nop 0\n"
"sgo_h1:\ns_nop 4\nbuffer_load_dwordx4 v[40:43], v10, s[8:11], s43 offen offset:1024 sc1\ns_nop 4\n"
"buffer_load_dwordx4 v[44:47], v10, s[8:11], s43 offen offset:0 sc1\ns_nop 13\nbuffer_load_dwordx4 v[48:51], v10, s[8:11], s43 offen offset:3072 sc1\ns_nop 13\n"
"buffer_load_dwordx4 v[52:55], v10, s[8:11], s43 offen offset:2048 sc1\nlanded_h1:\ns_waitcnt vmcnt(3)\nv_smfmac_f32_16x16x64_bf16 v[56:59], v[40:43], a[8:15], v8\n"
"v_max3_u32 v128, v128, v40, v41\nv_smfmac_f32_16x16x64_bf16 v[60:63], v[40:43], a[40:47], v8\nv_max3_u32 v128, v128, v42, v43\nv_smfmac_f32_16x16x64_bf16 v[64:67], v[40:43], a[72:79], v8\n"
"s_waitcnt vmcnt(2)\nv_smfmac_f32_16x16x64_bf16 v[56:59], v[44:47], a[0:7], v8\nv_max3_u32 v128, v128, v44, v45\nv_smfmac_f32_16x16x64_bf16 v[60:63], v[44:47], a[32:39], v8\n"
"v_max3_u32 v128, v128, v46, v47\nv_smfmac_f32_16x16x64_bf16 v[64:67], v[44:47], a[64:71], v8\ns_waitcnt vmcnt(1)\nv_smfmac_f32_16x16x64_bf16 v[56:59], v[48:51], a[24:31], v8\n"
"v_max3_u32 v128, v128, v48, v49\nv_smfmac_f32_16x16x64_bf16 v[60:63], v[48:51], a[56:63], v8\nv_max3_u32 v128, v128, v50, v51\nv_smfmac_f32_16x16x64_bf16 v[64:67], v[48:51], a[88:95], v8\n"
"s_waitcnt vmcnt(0)\nv_smfmac_f32_16x16x64_bf16 v[56:59], v[52:55], a[16:23], v8\nv_max3_u32 v128, v128, v52, v53\nv_smfmac_f32_16x16x64_bf16 v[60:63], v[52:55], a[48:55], v8\n"
"v_max3_u32 v128, v128, v54, v55\nv_smfmac_f32_16x16x64_bf16 v[64:67], v[52:55], a[80:87], v8\nv_add_f32 v129, v56, v57\nv_add_f32 v130, v58, v59\n"
"v_cvt_pk_bf16_f32 v112, v129, v130\nv_add_f32 v129, v60, v61\nv_add_f32 v130, v62, v63\nv_cvt_pk_bf16_f32 v113, v129, v130\n"
"v_cmp_eq_u32 vcc, -1, v128\nv_add_f32 v129, v64, v65\nv_add_f32 v130, v66, v67\nv_cvt_pk_bf16_f32 v114, v129, v130\n"
"ds_write2_b64 v131, v[112:113], v[114:115] offset0:32 offset1:48\ns_cbranch_vccnz retry_h1\nv_mov_b32 v129, 0\ns_nop 1\n"
"v_mov_b32 v129, 0\ns_nop 1\ns_waitcnt lgkmcnt(0)\ns_barrier\n"
"s_nop 9\nds_read_b64_tr_b16 v[88:89], v132 offset:0\nds_read_b64_tr_b16 v[90:91], v132 offset:32\nds_read_b64_tr_b16 v[92:93], v132 offset:256\n"
"ds_read_b64_tr_b16 v[94:95], v132 offset:288\nds_read_b64_tr_b16 v[96:97], v132 offset:512\nds_read_b64_tr_b16 v[98:99], v132 offset:544\nds_read_b64_tr_b16 v[100:101], v132 offset:768\n"
"ds_read_b64_tr_b16 v[102:103], v132 offset:800\ns_waitcnt lgkmcnt(6)\nv_mfma_f32_16x16x32_bf16 v[104:107], v[88:91], v[72:75], 0\nv_mfma_f32_16x16x32_bf16 v[108:111], v[88:91], v[80:83], 0\n"
"s_waitcnt lgkmcnt(4)\nv_mfma_f32_16x16x32_bf16 v[104:107], v[92:95], v[76:79], v[104:107]\nv_mfma_f32_16x16x32_bf16 v[108:111], v[92:95], v[84:87], v[108:111]\ns_waitcnt lgkmcnt(2)\n"
"v_mfma_f32_16x16x32_bf16 v[104:107], v[96:99], v[72:75], v[104:107]\nv_mfma_f32_16x16x32_bf16 v[108:111], v[96:99], v[80:83], v[108:111]\ns_waitcnt lgkmcnt(0)\nv_mfma_f32_16x16x32_bf16 v[104:107], v[100:103], v[76:79], v[104:107]\n"
"v_mfma_f32_16x16x32_bf16 v[108:111], v[100:103], v[84:87], v[108:111]\ns_nop 7\nv_add_f32_dpp v124, v106, v104 row_ror:8 row_mask:0xf bank_mask:0x3\nv_add_f32_dpp v124, v108, v110 row_ror:8 row_mask:0xf bank_mask:0xc\n"
"v_add_f32_dpp v125, v107, v105 row_ror:8 row_mask:0xf bank_mask:0x3\nv_add_f32_dpp v125, v109, v111 row_ror:8 row_mask:0xf bank_mask:0xc\ns_add_u32 s47, s50, 7168\nbuffer_load_dwordx4 v[72:75], v11, s[16:19], s50 offen\n"
"buffer_load_dwordx4 v[76:79], v12, s[16:19], s50 offen\nbuffer_load_dwordx4 v[80:83], v11, s[16:19], s47 offen\nbuffer_load_dwordx4 v[84:87], v12, s[16:19], s47 offen\ns_add_u32 s50, s50, 28672\n"
"buffer_store_dwordx2 v[124:125], v16, s[20:23], s51 offen sc1\ns_add_u32 s51, s51, 1024\nds_read_b32 v133, v134\nv_xor_b32 v131, 8192, v131\n"
"v_xor_b32 v132, 8192, v132\ns_waitcnt lgkmcnt(0)\nv_readfirstlane_b32 s47, v133\ns_sub_u32 s47, s47, s40\n"
"s_cmp_eq_u32 s47, 1\ns_cbranch_scc1 adopt_h1\nback_h1:\ns_add_u32 s40, s40, 1\n"
"s_mov_b32 s43, s41\ns_add_u32 s41, s41, 131072\ns_and_b32 s41, s41, 524287\ns_xor_b32 s42, s41, 262144\n"
"s_and_b32 s47, s46, 15\ns_mul_i32 s48, s47, 4\ns_add_u32 s45, s45, s48\ns_add_u32 s45, s45, s30\n"
"s_lshr_b32 s48, s45, 4\ns_lshl_b32 s48, s48, 2\ns_add_u32 s44, s44, s48\ns_and_b32 s45, s45, 15\n"
"s_add_u32 s92, s92, 1\ns_lshr_b32 s48, s46, 4\ns_cmp_gt_u32 s47, s48\ns_cselect_b32 s47, 1, 0\n"
"s_and_b32 s48, s92, 1\ns_cselect_b32 s47, 0, s47\ns_sub_u32 s46, s46, s47\ns_waitcnt lgkmcnt(0)\n"
"s_cmp_lt_u32 s40, 64\ns_cbranch_scc1 top_h1\ns_add_u32 s47, s33, 65\nv_mov_b32 v135, s47\n"
"s_waitcnt vmcnt(0)\nbuffer_store_dword v135, v18, s[8:11], 0 offen sc1\ns_branch end_h1\nretry2_h1:\n"
"s_sleep 3\nretry_h1:\ns_add_u32 s53, s53, 1\ns_add_u32 s49, s49, 1\n"
"s_cmp_gt_u32 s49, 65536\ns_cbranch_scc1 end_h1\nbuffer_load_dwordx4 v[40:43], v10, s[8:11], s43 offen offset:1024 sc1\nbuffer_load_dwordx4 v[44:47], v10, s[8:11], s43 offen offset:0 sc1\n"
"buffer_load_dwordx4 v[48:51], v10, s[8:11], s43 offen offset:3072 sc1\nbuffer_load_dwordx4 v[52:55], v10, s[8:11], s43 offen offset:2048 sc1\nv_mov_b32 v128, 0\ns_add_u32 s47, s40, 1\n"
"v_mov_b32 v133, s47\nds_write_b32 v134, v133\ns_waitcnt vmcnt(0)\nv_max3_u32 v128, v128, v40, v41\n"
"v_max3_u32 v128, v128, v42, v43\nv_max3_u32 v128, v128, v44, v45\nv_max3_u32 v128, v128, v46, v47\nv_max3_u32 v128, v128, v48, v49\n"
"v_max3_u32 v128, v128, v50, v51\nv_max3_u32 v128, v128, v52, v53\nv_max3_u32 v128, v128, v54, v55\nv_cmp_eq_u32 vcc, -1, v128\n"
"v_mov_b32 v56, 0\nv_mov_b32 v57, 0\nv_mov_b32 v58, 0\nv_mov_b32 v59, 0\n"
"v_mov_b32 v60, 0\nv_mov_b32 v61, 0\nv_mov_b32 v62, 0\nv_mov_b32 v63, 0\n"
"v_mov_b32 v64, 0\nv_mov_b32 v65, 0\nv_mov_b32 v66, 0\nv_mov_b32 v67, 0\n"
"s_cbranch_vccnz retry2_h1\ns_branch landed_h1\nadopt_h1:\nbuffer_load_dwordx4 v[116:119], v28, s[8:11], 0 offen sc1\n"
"s_lshl_b32 s52, s40, 26\ns_or_b32 s47, s52, 0x3ffffff\ns_waitcnt vmcnt(0)\nv_cmp_ge_u32 vcc, s47, v116\n"
"s_nop 1\nv_cndmask_b32 v116, 0, v116, vcc\nv_cmp_ge_u32 vcc, s47, v117\ns_nop 1\n"
"v_cndmask_b32 v117, 0, v117, vcc\nv_cmp_ge_u32 vcc, s47, v118\ns_nop 1\nv_cndmask_b32 v118, 0, v118, vcc\n"
"v_cmp_ge_u32 vcc, s47, v119\ns_nop 1\nv_cndmask_b32 v119, 0, v119, vcc\nv_max3_u32 v133, v116, v117, v118\n"
"v_max_u32 v133, v133, v119\ns_nop 1\nv_max_u32_dpp v133, v133, v133 row_ror:8 row_mask:0xf bank_mask:0xf\ns_nop 1\n"
"v_max_u32_dpp v133, v133, v133 row_ror:4 row_mask:0xf bank_mask:0xf\ns_nop 1\nv_max_u32_dpp v133, v133, v133 row_ror:2 row_mask:0xf bank_mask:0xf\ns_nop 1\n"
"v_max_u32_dpp v133, v133, v133 row_ror:1 row_mask:0xf bank_mask:0xf\ns_nop 0\nv_readlane_b32 s47, v133, 0\nv_readlane_b32 s48, v133, 16\n"
"s_max_u32 s48, s48, s47\ns_cmp_le_u32 s48, s52\ns_cbranch_scc1 back_h1\ns_bfe_u32 s52, s48, 1179656\n"
"s_add_u32 s44, s52, s28\ns_mov_b32 s45, 0\ns_and_b32 s46, s48, 255\ns_mov_b32 s92, 0\n"
"s_branch back_h1\nend_h1:\ns_branch fk_done\nfk_h2:\n"
"s_mov_b32 s40, 0\ns_mov_b32 s41, 0\ns_mov_b32 s42, 262144\ns_mov_b32 s43, 393216\n"
"s_mov_b32 s44, s28\ns_mov_b32 s45, 0\ns_mov_b32 s46, 0\ns_mov_b32 s92, 0\n"
"s_mov_b32 s49, 0\ns_mov_b32 s53, 0\ns_mov_b32 s50, s35\ns_mov_b32 s51, s36\n"
"v_mov_b32 v127, v13\nv_mov_b32 v128, v14\nv_mov_b32 v130, 24576\nv_mov_b32 v111, 0\n"
"v_mov_b32 v116, -1\nv_mov_b32 v117, -1\nv_mov_b32 v118, -1\nv_mov_b32 v119, -1\n"
"s_add_u32 s47, s34, 8192\nbuffer_load_dwordx4 a[0:3], v9, s[12:15], s47 offen sc1\nbuffer_load_dwordx4 a[4:7], v9, s[12:15], s47 offen offset:1024 sc1\ns_add_u32 s47, s34, 10240\n"
"buffer_load_dwordx4 a[8:11], v9, s[12:15], s47 offen sc1\nbuffer_load_dwordx4 a[12:15], v9, s[12:15], s47 offen offset:1024 sc1\ns_add_u32 s47, s34, 12288\nbuffer_load_dwordx4 a[16:19], v9, s[12:15], s47 offen sc1\n"
"buffer_load_dwordx4 a[20:23], v9, s[12:15], s47 offen offset:1024 sc1\ns_add_u32 s47, s34, 22528\nbuffer_load_dwordx4 a[32:35], v9, s[12:15], s47 offen sc1\nbuffer_load_dwordx4 a[36:39], v9, s[12:15], s47 offen offset:1024 sc1\n"
"s_add_u32 s47, s34, 24576\nbuffer_load_dwordx4 a[40:43], v9, s[12:15], s47 offen sc1\nbuffer_load_dwordx4 a[44:47], v9, s[12:15], s47 offen offset:1024 sc1\ns_add_u32 s47, s34, 26624\n"
"buffer_load_dwordx4 a[48:51], v9, s[12:15], s47 offen sc1\nbuffer_load_dwordx4 a[52:55], v9, s[12:15], s47 offen offset:1024 sc1\ns_add_u32 s47, s34, 36864\nbuffer_load_dwordx4 a[64:67], v9, s[12:15], s47 offen sc1\n"
"buffer_load_dwordx4 a[68:71], v9, s[12:15], s47 offen offset:1024 sc1\ns_add_u32 s47, s34, 38912\nbuffer_load_dwordx4 a[72:75], v9, s[12:15], s47 offen sc1\nbuffer_load_dwordx4 a[76:79], v9, s[12:15], s47 offen offset:1024 sc1\n"
"s_add_u32 s47, s34, 40960\nbuffer_load_dwordx4 a[80:83], v9, s[12:15], s47 offen sc1\nbuffer_load_dwordx4 a[84:87], v9, s[12:15], s47 offen offset:1024 sc1\ns_add_u32 s47, s34, 51200\n"
"buffer_load_dwordx4 a[96:99], v9, s[12:15], s47 offen sc1\nbuffer_load_dwordx4 a[100:103], v9, s[12:15], s47 offen offset:1024 sc1\ns_add_u32 s47, s34, 53248\nbuffer_load_dwordx4 a[104:107], v9, s[12:15], s47 offen sc1\n"
"buffer_load_dwordx4 a[108:111], v9, s[12:15], s47 offen offset:1024 sc1\ns_add_u32 s47, s34, 55296\nbuffer_load_dwordx4 a[112:115], v9, s[12:15], s47 offen sc1\nbuffer_load_dwordx4 a[116:119], v9, s[12:15], s47 offen offset:1024 sc1\n"
"s_waitcnt vmcnt(0)\ns_barrier\n.p2align 6\ntop_h2:\n"
"v_mov_b32 v52, 0\nv_mov_b32 v53, 0\nv_mov_b32 v54, 0\nv_mov_b32 v55, 0\n"
"v_mov_b32 v56, 0\nv_mov_b32 v57, 0\nv_mov_b32 v58, 0\nv_mov_b32 v59, 0\n"
"v_mov_b32 v60, 0\nv_mov_b32 v61, 0\nv_mov_b32 v62, 0\nv_mov_b32 v63, 0\n"
"v_mov_b32 v64, 0\nv_mov_b32 v65, 0\nv_mov_b32 v66, 0\nv_mov_b32 v67, 0\n"
"v_mov_b32 v124, 0\ns_mul_i32 s48, s45, 6\ns_and_b32 s48, s48, -4\ns_getpc_b64 s[60:61]\n"
"spc_h2:\ns_sub_u32 s48, sgo_h2-spc_h2, s48\ns_add_u32 s60, s60, s48\ns_addc_u32 s61, s61, 0\n"
"sf_h2:\ns_memrealtime s[54:55]\ns_nop 2\ns_memrealtime s[56:57]\n"
"s_nop 2\ns_memrealtime s[58:59]\ns_waitcnt lgkmcnt(4)\ns_sub_u32 s47, s54, s44\n"
"s_add_u32 s48, s47, 12\ns_cmp_ge_u32 s48, 12\ns_cbranch_scc1 so_h2\ns_memrealtime s[54:55]\n"
"s_waitcnt lgkmcnt(4)\ns_cmp_ge_u32 s56, s44\ns_cbranch_scc1 sx_h2\ns_memrealtime s[56:57]\n"
"s_waitcnt lgkmcnt(4)\ns_cmp_ge_u32 s58, s44\ns_cbranch_scc1 sx_h2\ns_memrealtime s[58:59]\n"
"s_waitcnt lgkmcnt(4)\ns_cmp_ge_u32 s54, s44\ns_cbranch_scc1 sx_h2\ns_memrealtime s[54:55]\n"
"s_waitcnt lgkmcnt(4)\ns_cmp_ge_u32 s56, s44\ns_cbranch_scc1 sx_h2\ns_memrealtime s[56:57]\n"
"s_waitcnt lgkmcnt(4)\ns_cmp_ge_u32 s58, s44\ns_cbranch_scc1 sx_h2\ns_memrealtime s[58:59]\n"
"s_waitcnt lgkmcnt(4)\ns_cmp_ge_u32 s54, s44\ns_cbranch_scc1 sx_h2\ns_memrealtime s[54:55]\n"
"s_waitcnt lgkmcnt(4)\ns_cmp_ge_u32 s56, s44\ns_cbranch_scc1 sx_h2\ns_memrealtime s[56:57]\n"
"s_waitcnt lgkmcnt(4)\ns_cmp_ge_u32 s58, s44\ns_cbranch_scc1 sx_h2\ns_memrealtime s[58:59]\n"
"s_waitcnt lgkmcnt(4)\ns_cmp_ge_u32 s54, s44\ns_cbranch_scc1 sx_h2\ns_memrealtime s[54:55]\n"
"s_waitcnt lgkmcnt(4)\ns_cmp_ge_u32 s56, s44\ns_cbranch_scc1 sx_h2\ns_memrealtime s[56:57]\n"
"s_waitcnt lgkmcnt(4)\ns_cmp_ge_u32 s58, s44\ns_cbranch_scc1 sx_h2\ns_memrealtime s[58:59]\n"
"s_waitcnt lgkmcnt(4)\ns_cmp_ge_u32 s54, s44\ns_cbranch_scc1 sx_h2\ns_memrealtime s[54:55]\n"
"s_waitcnt lgkmcnt(4)\ns_cmp_ge_u32 s56, s44\ns_cbranch_scc1 sx_h2\ns_memrealtime s[56:57]\n"
"s_waitcnt lgkmcnt(4)\ns_cmp_ge_u32 s58, s44\ns_cbranch_scc1 sx_h2\ns_memrealtime s[58:59]\n"
"s_waitcnt lgkmcnt(4)\ns_cmp_ge_u32 s54, s44\ns_cbranch_scc1 sx_h2\ns_memrealtime s[54:55]\n"
"s_waitcnt lgkmcnt(4)\ns_cmp_ge_u32 s56, s44\ns_cbranch_scc1 sx_h2\ns_memrealtime s[56:57]\n"
"s_branch sx_h2\nso_h2:\ns_waitcnt lgkmcnt(0)\ns_cmp_lt_i32 s47, 0\n"
"s_cbranch_scc1 sf_h2\ns_branch sgo_h2\nsx_h2:\ns_setpc_b64 s[60:61]\n"
"s_nop 0\ns_nop 0\ns_nop 0\ns_nop 0\n"
"s_nop 0\ns_nop 0\ns_nop 0\ns_nop 0\n"
"s_nop 0\ns_nop 0\ns_nop 0\ns_nop 0\n"
"s_nop 0\ns_nop 0\ns_nop 0\ns_nop 0\n"
"s_nop 0\ns_nop 0\ns_nop 0\ns_nop 0\n"
"s_nop 0\ns_nop 0\ns_nop 0\ns_nop 0\n"
"sgo_h2:\ns_nop 8\nbuffer_load_dwordx4 v[40:43], v10, s[8:11], s43 offen offset:0 sc1\ns_nop 13\n"
"buffer_load_dwordx4 v[44:47], v10, s[8:11], s43 offen offset:1024 sc1\ns_nop 13\nbuffer_load_dwordx4 v[48:51], v10, s[8:11], s43 offen offset:2048 sc1\nlanded_h2:\n"
"s_waitcnt vmcnt(2)\nv_smfmac_f32_16x16x64_bf16 v[52:55], v[40:43], a[0:7], v8\nv_max3_u32 v124, v124, v40, v41\nv_smfmac_f32_16x16x64_bf16 v[56:59], v[40:43], a[32:39], v8\n"
"v_max3_u32 v124, v124, v42, v43\nv_smfmac_f32_16x16x64_bf16 v[60:63], v[40:43], a[64:71], v8\nv_smfmac_f32_16x16x64_bf16 v[64:67], v[40:43], a[96:103], v8\ns_waitcnt vmcnt(1)\n"
"v_smfmac_f32_16x16x64_bf16 v[52:55], v[44:47], a[8:15], v8\nv_max3_u32 v124, v124, v44, v45\nv_smfmac_f32_16x16x64_bf16 v[56:59], v[44:47], a[40:47], v8\nv_max3_u32 v124, v124, v46, v47\n"
"v_smfmac_f32_16x16x64_bf16 v[60:63], v[44:47], a[72:79], v8\nv_smfmac_f32_16x16x64_bf16 v[64:67], v[44:47], a[104:111], v8\ns_waitcnt vmcnt(0)\nv_smfmac_f32_16x16x64_bf16 v[52:55], v[48:51], a[16:23], v8\n"
"v_max3_u32 v124, v124, v48, v49\nv_smfmac_f32_16x16x64_bf16 v[56:59], v[48:51], a[48:55], v8\nv_max3_u32 v124, v124, v50, v51\nv_smfmac_f32_16x16x64_bf16 v[60:63], v[48:51], a[80:87], v8\n"
"v_cmp_eq_u32 vcc, -1, v124\nv_smfmac_f32_16x16x64_bf16 v[64:67], v[48:51], a[112:119], v8\nv_add_f32 v125, v52, v53\nv_add_f32 v126, v54, v55\n"
"v_cvt_pk_bf16_f32 v108, v125, v126\nv_add_f32 v125, v56, v57\nv_add_f32 v126, v58, v59\nv_cvt_pk_bf16_f32 v109, v125, v126\n"
"v_add_f32 v125, v60, v61\nv_add_f32 v126, v62, v63\nv_cvt_pk_bf16_f32 v110, v125, v126\nv_add_f32 v125, v64, v65\n"
"v_add_f32 v126, v66, v67\nv_cvt_pk_bf16_f32 v111, v125, v126\nds_write2_b64 v127, v[108:109], v[110:111] offset0:0 offset1:16\ns_cbranch_vccnz retry_h2\n"
"v_mov_b32 v125, 0\ns_nop 1\nv_mov_b32 v125, 0\ns_nop 1\n"
"s_waitcnt lgkmcnt(0)\ns_barrier\nv_mov_b32 v125, 0\ns_nop 1\n"
"v_mov_b32 v125, 0\ns_nop 1\nv_mov_b32 v125, 0\ns_nop 1\n"
"v_mov_b32 v125, 0\ns_nop 1\nv_mov_b32 v125, 0\ns_nop 1\n"
"v_mov_b32 v125, 0\ns_nop 1\nv_mov_b32 v125, 0\ns_nop 1\n"
"v_mov_b32 v125, 0\ns_nop 1\nv_mov_b32 v125, 0\ns_nop 1\n"
"v_mov_b32 v125, 0\ns_nop 1\nv_mov_b32 v125, 0\ns_nop 1\n"
"v_mov_b32 v125, 0\ns_nop 1\nv_mov_b32 v125, 0\ns_nop 1\n"
"v_mov_b32 v125, 0\ns_nop 1\nv_mov_b32 v125, 0\ns_nop 1\n"
"v_mov_b32 v125, 0\ns_nop 1\nv_mov_b32 v125, 0\ns_nop 1\n"
"v_mov_b32 v125, 0\ns_nop 1\nv_mov_b32 v125, 0\ns_nop 1\n"
"v_mov_b32 v125, 0\ns_nop 1\nv_mov_b32 v125, 0\ns_nop 1\n"
"v_mov_b32 v125, 0\nbuffer_store_dwordx4 v[116:119], v17, s[8:11], s42 offen sc1\nds_read_b32 v129, v130\nv_xor_b32 v127, 8192, v127\n"
"v_xor_b32 v128, 8192, v128\ns_waitcnt lgkmcnt(0)\nv_readfirstlane_b32 s47, v129\ns_sub_u32 s47, s47, s40\n"
"s_cmp_eq_u32 s47, 1\ns_cbranch_scc1 adopt_h2\nback_h2:\ns_add_u32 s40, s40, 1\n"
"s_mov_b32 s43, s41\ns_add_u32 s41, s41, 131072\ns_and_b32 s41, s41, 524287\ns_xor_b32 s42, s41, 262144\n"
"s_and_b32 s47, s46, 15\ns_mul_i32 s48, s47, 4\ns_add_u32 s45, s45, s48\ns_add_u32 s45, s45, s30\n"
"s_lshr_b32 s48, s45, 4\ns_lshl_b32 s48, s48, 2\ns_add_u32 s44, s44, s48\ns_and_b32 s45, s45, 15\n"
"s_add_u32 s92, s92, 1\ns_lshr_b32 s48, s46, 4\ns_cmp_gt_u32 s47, s48\ns_cselect_b32 s47, 1, 0\n"
"s_and_b32 s48, s92, 1\ns_cselect_b32 s47, 0, s47\ns_sub_u32 s46, s46, s47\ns_waitcnt lgkmcnt(0)\n"
"s_cmp_lt_u32 s40, 64\ns_cbranch_scc1 top_h2\ns_branch end_h2\nretry2_h2:\n"
"s_sleep 3\nretry_h2:\ns_add_u32 s53, s53, 1\ns_add_u32 s49, s49, 1\n"
"s_cmp_gt_u32 s49, 65536\ns_cbranch_scc1 end_h2\nbuffer_load_dwordx4 v[40:43], v10, s[8:11], s43 offen offset:0 sc1\nbuffer_load_dwordx4 v[44:47], v10, s[8:11], s43 offen offset:1024 sc1\n"
"buffer_load_dwordx4 v[48:51], v10, s[8:11], s43 offen offset:2048 sc1\nv_mov_b32 v124, 0\ns_add_u32 s47, s40, 1\nv_mov_b32 v129, s47\n"
"ds_write_b32 v130, v129\ns_waitcnt vmcnt(0)\nv_max3_u32 v124, v124, v40, v41\nv_max3_u32 v124, v124, v42, v43\n"
"v_max3_u32 v124, v124, v44, v45\nv_max3_u32 v124, v124, v46, v47\nv_max3_u32 v124, v124, v48, v49\nv_max3_u32 v124, v124, v50, v51\n"
"v_cmp_eq_u32 vcc, -1, v124\nv_mov_b32 v52, 0\nv_mov_b32 v53, 0\nv_mov_b32 v54, 0\n"
"v_mov_b32 v55, 0\nv_mov_b32 v56, 0\nv_mov_b32 v57, 0\nv_mov_b32 v58, 0\n"
"v_mov_b32 v59, 0\nv_mov_b32 v60, 0\nv_mov_b32 v61, 0\nv_mov_b32 v62, 0\n"
"v_mov_b32 v63, 0\nv_mov_b32 v64, 0\nv_mov_b32 v65, 0\nv_mov_b32 v66, 0\n"
"v_mov_b32 v67, 0\ns_cbranch_vccnz retry2_h2\ns_branch landed_h2\nadopt_h2:\n"
"buffer_load_dwordx4 v[112:115], v28, s[8:11], 0 offen sc1\ns_lshl_b32 s52, s40, 26\ns_or_b32 s47, s52, 0x3ffffff\ns_waitcnt vmcnt(0)\n"
"v_cmp_ge_u32 vcc, s47, v112\ns_nop 1\nv_cndmask_b32 v112, 0, v112, vcc\nv_cmp_ge_u32 vcc, s47, v113\n"
"s_nop 1\nv_cndmask_b32 v113, 0, v113, vcc\nv_cmp_ge_u32 vcc, s47, v114\ns_nop 1\n"
"v_cndmask_b32 v114, 0, v114, vcc\nv_cmp_ge_u32 vcc, s47, v115\ns_nop 1\nv_cndmask_b32 v115, 0, v115, vcc\n"
"v_max3_u32 v129, v112, v113, v114\nv_max_u32 v129, v129, v115\ns_nop 1\nv_max_u32_dpp v129, v129, v129 row_ror:8 row_mask:0xf bank_mask:0xf\n"
"s_nop 1\nv_max_u32_dpp v129, v129, v129 row_ror:4 row_mask:0xf bank_mask:0xf\ns_nop 1\nv_max_u32_dpp v129, v129, v129 row_ror:2 row_mask:0xf bank_mask:0xf\n"
"s_nop 1\nv_max_u32_dpp v129, v129, v129 row_ror:1 row_mask:0xf bank_mask:0xf\ns_nop 0\nv_readlane_b32 s47, v129, 0\n"
"v_readlane_b32 s48, v129, 16\ns_max_u32 s48, s48, s47\ns_cmp_le_u32 s48, s52\ns_cbranch_scc1 back_h2\n"
"s_bfe_u32 s52, s48, 1179656\ns_add_u32 s44, s52, s28\ns_mov_b32 s45, 0\ns_and_b32 s46, s48, 255\n"
"s_mov_b32 s92, 0\ns_branch back_h2\nend_h2:\ns_branch fk_done\n"
"fk_h3:\ns_mov_b32 s40, 0\ns_mov_b32 s41, 0\ns_mov_b32 s42, 262144\n"
"s_mov_b32 s43, 393216\ns_mov_b32 s44, s28\ns_mov_b32 s45, 0\ns_mov_b32 s46, 0\n"
"s_mov_b32 s92, 0\ns_mov_b32 s49, 0\ns_mov_b32 s53, 0\ns_mov_b32 s50, s35\n"
"s_mov_b32 s51, s36\nv_mov_b32 v127, v13\nv_mov_b32 v128, v14\nv_mov_b32 v130, 24576\n"
"v_mov_b32 v111, 0\nv_mov_b32 v116, -1\nv_mov_b32 v117, -1\nv_mov_b32 v118, -1\n"
"v_mov_b32 v119, -1\ns_add_u32 s47, s34, 67584\nbuffer_load_dwordx4 a[8:11], v9, s[12:15], s47 offen sc1\nbuffer_load_dwordx4 a[12:15], v9, s[12:15], s47 offen offset:1024 sc1\n"
"s_add_u32 s47, s34, 65536\nbuffer_load_dwordx4 a[0:3], v9, s[12:15], s47 offen sc1\nbuffer_load_dwordx4 a[4:7], v9, s[12:15], s47 offen offset:1024 sc1\ns_add_u32 s47, s34, 69632\n"
"buffer_load_dwordx4 a[16:19], v9, s[12:15], s47 offen sc1\nbuffer_load_dwordx4 a[20:23], v9, s[12:15], s47 offen offset:1024 sc1\ns_add_u32 s47, s34, 81920\nbuffer_load_dwordx4 a[40:43], v9, s[12:15], s47 offen sc1\n"
"buffer_load_dwordx4 a[44:47], v9, s[12:15], s47 offen offset:1024 sc1\ns_add_u32 s47, s34, 79872\nbuffer_load_dwordx4 a[32:35], v9, s[12:15], s47 offen sc1\nbuffer_load_dwordx4 a[36:39], v9, s[12:15], s47 offen offset:1024 sc1\n"
"s_add_u32 s47, s34, 83968\nbuffer_load_dwordx4 a[48:51], v9, s[12:15], s47 offen sc1\nbuffer_load_dwordx4 a[52:55], v9, s[12:15], s47 offen offset:1024 sc1\ns_add_u32 s47, s34, 96256\n"
"buffer_load_dwordx4 a[72:75], v9, s[12:15], s47 offen sc1\nbuffer_load_dwordx4 a[76:79], v9, s[12:15], s47 offen offset:1024 sc1\ns_add_u32 s47, s34, 94208\nbuffer_load_dwordx4 a[64:67], v9, s[12:15], s47 offen sc1\n"
"buffer_load_dwordx4 a[68:71], v9, s[12:15], s47 offen offset:1024 sc1\ns_add_u32 s47, s34, 98304\nbuffer_load_dwordx4 a[80:83], v9, s[12:15], s47 offen sc1\nbuffer_load_dwordx4 a[84:87], v9, s[12:15], s47 offen offset:1024 sc1\n"
"s_waitcnt vmcnt(0)\ns_barrier\n.p2align 6\ntop_h3:\n"
"v_mov_b32 v52, 0\nv_mov_b32 v53, 0\nv_mov_b32 v54, 0\nv_mov_b32 v55, 0\n"
"v_mov_b32 v56, 0\nv_mov_b32 v57, 0\nv_mov_b32 v58, 0\nv_mov_b32 v59, 0\n"
"v_mov_b32 v60, 0\nv_mov_b32 v61, 0\nv_mov_b32 v62, 0\nv_mov_b32 v63, 0\n"
"v_mov_b32 v124, 0\ns_mul_i32 s48, s45, 6\ns_and_b32 s48, s48, -4\ns_getpc_b64 s[60:61]\n"
"spc_h3:\ns_sub_u32 s48, sgo_h3-spc_h3, s48\ns_add_u32 s60, s60, s48\ns_addc_u32 s61, s61, 0\n"
"sf_h3:\ns_memrealtime s[54:55]\ns_nop 2\ns_memrealtime s[56:57]\n"
"s_nop 2\ns_memrealtime s[58:59]\ns_waitcnt lgkmcnt(4)\ns_sub_u32 s47, s54, s44\n"
"s_add_u32 s48, s47, 12\ns_cmp_ge_u32 s48, 12\ns_cbranch_scc1 so_h3\ns_memrealtime s[54:55]\n"
"s_waitcnt lgkmcnt(4)\ns_cmp_ge_u32 s56, s44\ns_cbranch_scc1 sx_h3\ns_memrealtime s[56:57]\n"
"s_waitcnt lgkmcnt(4)\ns_cmp_ge_u32 s58, s44\ns_cbranch_scc1 sx_h3\ns_memrealtime s[58:59]\n"
"s_waitcnt lgkmcnt(4)\ns_cmp_ge_u32 s54, s44\ns_cbranch_scc1 sx_h3\ns_memrealtime s[54:55]\n"
"s_waitcnt lgkmcnt(4)\ns_cmp_ge_u32 s56, s44\ns_cbranch_scc1 sx_h3\ns_memrealtime s[56:57]\n"
"s_waitcnt lgkmcnt(4)\ns_cmp_ge_u32 s58, s44\ns_cbranch_scc1 sx_h3\ns_memrealtime s[58:59]\n"
"s_waitcnt lgkmcnt(4)\ns_cmp_ge_u32 s54, s44\ns_cbranch_scc1 sx_h3\ns_memrealtime s[54:55]\n"
"s_waitcnt lgkmcnt(4)\ns_cmp_ge_u32 s56, s44\ns_cbranch_scc1 sx_h3\ns_memrealtime s[56:57]\n"
"s_waitcnt lgkmcnt(4)\ns_cmp_ge_u32 s58, s44\ns_cbranch_scc1 sx_h3\ns_memrealtime s[58:59]\n"
"s_waitcnt lgkmcnt(4)\ns_cmp_ge_u32 s54, s44\ns_cbranch_scc1 sx_h3\ns_memrealtime s[54:55]\n"
"s_waitcnt lgkmcnt(4)\ns_cmp_ge_u32 s56, s44\ns_cbranch_scc1 sx_h3\ns_memrealtime s[56:57]\n"
"s_waitcnt lgkmcnt(4)\ns_cmp_ge_u32 s58, s44\ns_cbranch_scc1 sx_h3\ns_memrealtime s[58:59]\n"
"s_waitcnt lgkmcnt(4)\ns_cmp_ge_u32 s54, s44\ns_cbranch_scc1 sx_h3\ns_memrealtime s[54:55]\n"
"s_waitcnt lgkmcnt(4)\ns_cmp_ge_u32 s56, s44\ns_cbranch_scc1 sx_h3\ns_memrealtime s[56:57]\n"
"s_waitcnt lgkmcnt(4)\ns_cmp_ge_u32 s58, s44\ns_cbranch_scc1 sx_h3\ns_memrealtime s[58:59]\n"
"s_waitcnt lgkmcnt(4)\ns_cmp_ge_u32 s54, s44\ns_cbranch_scc1 sx_h3\ns_memrealtime s[54:55]\n"
"s_waitcnt lgkmcnt(4)\ns_cmp_ge_u32 s56, s44\ns_cbranch_scc1 sx_h3\ns_memrealtime s[56:57]\n"
"s_branch sx_h3\nso_h3:\ns_waitcnt lgkmcnt(0)\ns_cmp_lt_i32 s47, 0\n"
"s_cbranch_scc1 sf_h3\ns_branch sgo_h3\nsx_h3:\ns_setpc_b64 s[60:61]\n"
"s_nop 0\ns_nop 0\ns_nop 0\ns_nop 0\n"
"s_nop 0\ns_nop 0\ns_nop 0\ns_nop 0\n"
"s_nop 0\ns_nop 0\ns_nop 0\ns_nop 0\n"
"s_nop 0\ns_nop 0\ns_nop 0\ns_nop 0\n"
"s_nop 0\ns_nop 0\ns_nop 0\ns_nop 0\n"
"s_nop 0\ns_nop 0\ns_nop 0\ns_nop 0\n"
"sgo_h3:\ns_nop 15\ns_nop 2\nbuffer_load_dwordx4 v[40:43], v10, s[8:11], s43 offen offset:1024 sc1\n"
"s_nop 13\nbuffer_load_dwordx4 v[44:47], v10, s[8:11], s43 offen offset:0 sc1\ns_nop 13\nbuffer_load_dwordx4 v[48:51], v10, s[8:11], s43 offen offset:2048 sc1\n"
"s_nop 3\nbuffer_load_dwordx4 v[68:71], v11, s[8:11], s43 offen sc1\nbuffer_load_dwordx4 v[72:75], v12, s[8:11], s43 offen sc1\nv_mov_b32 v125, 0\n"
"s_nop 1\nv_mov_b32 v125, 0\ns_nop 1\nv_mov_b32 v125, 0\n"
"s_nop 1\nv_mov_b32 v125, 0\ns_nop 1\nv_mov_b32 v125, 0\n"
"s_nop 1\nv_mov_b32 v125, 0\ns_nop 1\nv_mov_b32 v125, 0\n"
"s_nop 1\nv_mov_b32 v125, 0\ns_nop 1\nv_mov_b32 v125, 0\n"
"s_nop 1\nv_mov_b32 v125, 0\ns_nop 1\nv_mov_b32 v125, 0\n"
"s_nop 1\nv_mov_b32 v125, 0\ns_nop 1\nv_mov_b32 v125, 0\n"
"s_nop 1\nv_mov_b32 v125, 0\ns_nop 1\nv_mov_b32 v125, 0\n"
"s_nop 1\nv_mov_b32 v125, 0\ns_nop 1\nv_mov_b32 v125, 0\n"
"s_nop 0\nlanded_h3:\ns_waitcnt vmcnt(4)\nv_smfmac_f32_16x16x64_bf16 v[52:55], v[40:43], a[8:15], v8\n"
"v_max3_u32 v124, v124, v40, v41\nv_smfmac_f32_16x16x64_bf16 v[56:59], v[40:43], a[40:47], v8\nv_max3_u32 v124, v124, v42, v43\nv_smfmac_f32_16x16x64_bf16 v[60:63], v[40:43], a[72:79], v8\n"
"s_waitcnt vmcnt(3)\nv_smfmac_f32_16x16x64_bf16 v[52:55], v[44:47], a[0:7], v8\nv_max3_u32 v124, v124, v44, v45\nv_smfmac_f32_16x16x64_bf16 v[56:59], v[44:47], a[32:39], v8\n"
"v_max3_u32 v124, v124, v46, v47\nv_smfmac_f32_16x16x64_bf16 v[60:63], v[44:47], a[64:71], v8\ns_waitcnt vmcnt(2)\nv_smfmac_f32_16x16x64_bf16 v[52:55], v[48:51], a[16:23], v8\n"
"v_max3_u32 v124, v124, v48, v49\nv_smfmac_f32_16x16x64_bf16 v[56:59], v[48:51], a[48:55], v8\nv_max3_u32 v124, v124, v50, v51\nv_smfmac_f32_16x16x64_bf16 v[60:63], v[48:51], a[80:87], v8\n"
"v_add_f32 v125, v52, v53\nv_add_f32 v126, v54, v55\nv_cvt_pk_bf16_f32 v108, v125, v126\nv_add_f32 v125, v56, v57\n"
"v_add_f32 v126, v58, v59\nv_cvt_pk_bf16_f32 v109, v125, v126\nv_cmp_eq_u32 vcc, -1, v124\nv_add_f32 v125, v60, v61\n"
"v_add_f32 v126, v62, v63\nv_cvt_pk_bf16_f32 v110, v125, v126\nds_write2_b64 v127, v[108:109], v[110:111] offset0:32 offset1:48\ns_cbranch_vccnz retry_h3\n"
"v_mov_b32 v125, 0\ns_nop 1\nv_mov_b32 v125, 0\ns_nop 1\n"
"s_waitcnt lgkmcnt(0)\ns_barrier\nds_read_b64_tr_b16 v[84:85], v128 offset:0\nds_read_b64_tr_b16 v[86:87], v128 offset:64\n"
"ds_read_b64_tr_b16 v[88:89], v128 offset:256\nds_read_b64_tr_b16 v[90:91], v128 offset:320\nds_read_b64_tr_b16 v[92:93], v128 offset:512\nds_read_b64_tr_b16 v[94:95], v128 offset:576\n"
"ds_read_b64_tr_b16 v[96:97], v128 offset:768\nds_read_b64_tr_b16 v[98:99], v128 offset:832\nmb_h3:\ns_waitcnt vmcnt(0)\n"
"v_max3_u32 v124, v124, v68, v69\nv_max3_u32 v124, v124, v70, v71\nv_max3_u32 v124, v124, v72, v73\nv_max3_u32 v124, v124, v74, v75\n"
"v_cmp_eq_u32 vcc, -1, v124\ns_waitcnt lgkmcnt(6)\nv_mfma_f32_16x16x32_bf16 v[100:103], v[84:87], v[68:71], 0\ns_waitcnt lgkmcnt(4)\n"
"v_mfma_f32_16x16x32_bf16 v[100:103], v[88:91], v[72:75], v[100:103]\ns_waitcnt lgkmcnt(2)\nv_mfma_f32_16x16x32_bf16 v[100:103], v[92:95], v[68:71], v[100:103]\ns_waitcnt lgkmcnt(0)\n"
"v_mfma_f32_16x16x32_bf16 v[100:103], v[96:99], v[72:75], v[100:103]\ns_cbranch_vccnz mbr_h3\ns_nop 6\nv_add_f32_dpp v120, v102, v100 row_ror:8 row_mask:0xf bank_mask:0x3\n"
"v_add_f32_dpp v120, v100, v102 row_ror:8 row_mask:0xf bank_mask:0xc\nv_add_f32_dpp v121, v103, v101 row_ror:8 row_mask:0xf bank_mask:0x3\nv_add_f32_dpp v121, v101, v103 row_ror:8 row_mask:0xf bank_mask:0xc\nv_cvt_pk_f16_f32 v125, v120, v121\n"
"buffer_store_dword v125, v16, s[8:11], s41 offen sc1\ns_nop 15\ns_nop 15\ns_nop 15\n"
"buffer_store_dwordx2 v[120:121], v17, s[8:11], s41 offen sc1\nds_read_b32 v129, v130\nv_xor_b32 v127, 8192, v127\nv_xor_b32 v128, 8192, v128\n"
"s_waitcnt lgkmcnt(0)\nv_readfirstlane_b32 s47, v129\ns_sub_u32 s47, s47, s40\ns_cmp_eq_u32 s47, 1\n"
"s_cbranch_scc1 adopt_h3\nback_h3:\ns_add_u32 s40, s40, 1\ns_mov_b32 s43, s41\n"
"s_add_u32 s41, s41, 131072\ns_and_b32 s41, s41, 524287\ns_xor_b32 s42, s41, 262144\ns_and_b32 s47, s46, 15\n"
"s_mul_i32 s48, s47, 4\ns_add_u32 s45, s45, s48\ns_add_u32 s45, s45, s30\ns_lshr_b32 s48, s45, 4\n"
"s_lshl_b32 s48, s48, 2\ns_add_u32 s44, s44, s48\ns_and_b32 s45, s45, 15\ns_add_u32 s92, s92, 1\n"
"s_lshr_b32 s48, s46, 4\ns_cmp_gt_u32 s47, s48\ns_cselect_b32 s47, 1, 0\ns_and_b32 s48, s92, 1\n"
"s_cselect_b32 s47, 0, s47\ns_sub_u32 s46, s46, s47\ns_waitcnt lgkmcnt(0)\ns_cmp_lt_u32 s40, 64\n"
"s_cbranch_scc1 top_h3\ns_branch end_h3\nretry2_h3:\ns_sleep 3\n"
"retry_h3:\ns_add_u32 s53, s53, 1\ns_add_u32 s49, s49, 1\ns_cmp_gt_u32 s49, 65536\n"
"s_cbranch_scc1 end_h3\nbuffer_load_dwordx4 v[40:43], v10, s[8:11], s43 offen offset:1024 sc1\nbuffer_load_dwordx4 v[44:47], v10, s[8:11], s43 offen offset:0 sc1\nbuffer_load_dwordx4 v[48:51], v10, s[8:11], s43 offen offset:2048 sc1\n"
"buffer_load_dwordx4 v[68:71], v11, s[8:11], s43 offen sc1\nbuffer_load_dwordx4 v[72:75], v12, s[8:11], s43 offen sc1\nv_mov_b32 v124, 0\ns_add_u32 s47, s40, 1\n"
"v_mov_b32 v129, s47\nds_write_b32 v130, v129\ns_waitcnt vmcnt(0)\nv_max3_u32 v124, v124, v40, v41\n"
"v_max3_u32 v124, v124, v42, v43\nv_max3_u32 v124, v124, v44, v45\nv_max3_u32 v124, v124, v46, v47\nv_max3_u32 v124, v124, v48, v49\n"
"v_max3_u32 v124, v124, v50, v51\nv_cmp_eq_u32 vcc, -1, v124\nv_mov_b32 v52, 0\nv_mov_b32 v53, 0\n"
"v_mov_b32 v54, 0\nv_mov_b32 v55, 0\nv_mov_b32 v56, 0\nv_mov_b32 v57, 0\n"
"v_mov_b32 v58, 0\nv_mov_b32 v59, 0\nv_mov_b32 v60, 0\nv_mov_b32 v61, 0\n"
"v_mov_b32 v62, 0\nv_mov_b32 v63, 0\ns_cbranch_vccnz retry2_h3\ns_branch landed_h3\n"
"adopt_h3:\nbuffer_load_dwordx4 v[112:115], v28, s[8:11], 0 offen sc1\ns_lshl_b32 s52, s40, 26\ns_or_b32 s47, s52, 0x3ffffff\n"
"s_waitcnt vmcnt(0)\nv_cmp_ge_u32 vcc, s47, v112\ns_nop 1\nv_cndmask_b32 v112, 0, v112, vcc\n"
"v_cmp_ge_u32 vcc, s47, v113\ns_nop 1\nv_cndmask_b32 v113, 0, v113, vcc\nv_cmp_ge_u32 vcc, s47, v114\n"
"s_nop 1\nv_cndmask_b32 v114, 0, v114, vcc\nv_cmp_ge_u32 vcc, s47, v115\ns_nop 1\n"
"v_cndmask_b32 v115, 0, v115, vcc\nv_max3_u32 v129, v112, v113, v114\nv_max_u32 v129, v129, v115\ns_nop 1\n"
"v_max_u32_dpp v129, v129, v129 row_ror:8 row_mask:0xf bank_mask:0xf\ns_nop 1\nv_max_u32_dpp v129, v129, v129 row_ror:4 row_mask:0xf bank_mask:0xf\ns_nop 1\n"
"v_max_u32_dpp v129, v129, v129 row_ror:2 row_mask:0xf bank_mask:0xf\ns_nop 1\nv_max_u32_dpp v129, v129, v129 row_ror:1 row_mask:0xf bank_mask:0xf\ns_nop 0\n"
"v_readlane_b32 s47, v129, 0\nv_readlane_b32 s48, v129, 16\ns_max_u32 s48, s48, s47\ns_cmp_le_u32 s48, s52\n"
"s_cbranch_scc1 back_h3\ns_bfe_u32 s52, s48, 1179656\ns_add_u32 s44, s52, s28\ns_mov_b32 s45, 0\n"
"s_and_b32 s46, s48, 255\ns_mov_b32 s92, 0\ns_branch back_h3\nmbr_h3:\n"
"s_add_u32 s53, s53, 1\ns_add_u32 s49, s49, 1\ns_cmp_gt_u32 s49, 65536\ns_cbranch_scc1 end_h3\n"
"buffer_load_dwordx4 v[68:71], v11, s[8:11], s43 offen sc1\nbuffer_load_dwordx4 v[72:75], v12, s[8:11], s43 offen sc1\nv_mov_b32 v124, 0\ns_branch mb_h3\n"
"end_h3:\ns_branch fk_done\nfk_x0:\ns_mov_b32 s40, 0\n"
"s_mov_b32 s41, 0\ns_mov_b32 s42, 262144\ns_mov_b32 s43, 393216\ns_mov_b32 s44, s28\n"
"s_mov_b32 s45, 0\ns_mov_b32 s46, 0\ns_mov_b32 s92, 0\ns_mov_b32 s49, 0\n"
"s_mov_b32 s53, 0\ns_mov_b32 s50, s35\ns_mov_b32 s51, s36\nv_mov_b32 v81, v13\n"
"v_mov_b32 v82, v14\nv_mov_b32 v84, 24576\ns_add_u32 s51, s50, 14336\nv_mov_b32 v64, -1\n"
"v_mov_b32 v65, -1\nv_mov_b32 v66, -1\nv_mov_b32 v67, -1\nbuffer_load_dwordx4 a[0:3], v9, s[16:19], s50 offen offset:0 sc1\n"
"buffer_load_dwordx4 a[4:7], v9, s[16:19], s50 offen offset:1024 sc1\nbuffer_load_dwordx4 a[8:11], v9, s[16:19], s50 offen offset:2048 sc1\nbuffer_load_dwordx4 a[12:15], v9, s[16:19], s50 offen offset:3072 sc1\nbuffer_load_dwordx4 a[16:19], v9, s[16:19], s51 offen offset:0 sc1\n"
"buffer_load_dwordx4 a[20:23], v9, s[16:19], s51 offen offset:1024 sc1\nbuffer_load_dwordx4 a[24:27], v9, s[16:19], s51 offen offset:2048 sc1\nbuffer_load_dwordx4 a[28:31], v9, s[16:19], s51 offen offset:3072 sc1\ns_add_u32 s50, s50, 28672\n"
"s_add_u32 s51, s51, 28672\ns_waitcnt vmcnt(0)\ns_barrier\n.p2align 6\n"
"top_x0:\nv_mov_b32 v48, 0\nv_mov_b32 v49, 0\nv_mov_b32 v50, 0\n"
"v_mov_b32 v51, 0\nv_mov_b32 v52, 0\nv_mov_b32 v53, 0\nv_mov_b32 v54, 0\n"
"v_mov_b32 v55, 0\nv_mov_b32 v78, 0\ns_mul_i32 s48, s45, 6\ns_and_b32 s48, s48, -4\n"
"s_getpc_b64 s[60:61]\nspc_x0:\ns_sub_u32 s48, sgo_x0-spc_x0, s48\ns_add_u32 s60, s60, s48\n"
"s_addc_u32 s61, s61, 0\nsf_x0:\ns_memrealtime s[54:55]\ns_nop 2\n"
"s_memrealtime s[56:57]\ns_nop 2\ns_memrealtime s[58:59]\ns_waitcnt lgkmcnt(4)\n"
"s_sub_u32 s47, s54, s44\ns_add_u32 s48, s47, 12\ns_cmp_ge_u32 s48, 12\ns_cbranch_scc1 so_x0\n"
"s_memrealtime s[54:55]\ns_waitcnt lgkmcnt(4)\ns_cmp_ge_u32 s56, s44\ns_cbranch_scc1 sx_x0\n"
"s_memrealtime s[56:57]\ns_waitcnt lgkmcnt(4)\ns_cmp_ge_u32 s58, s44\ns_cbranch_scc1 sx_x0\n"
"s_memrealtime s[58:59]\ns_waitcnt lgkmcnt(4)\ns_cmp_ge_u32 s54, s44\ns_cbranch_scc1 sx_x0\n"
"s_memrealtime s[54:55]\ns_waitcnt lgkmcnt(4)\ns_cmp_ge_u32 s56, s44\ns_cbranch_scc1 sx_x0\n"
"s_memrealtime s[56:57]\ns_waitcnt lgkmcnt(4)\ns_cmp_ge_u32 s58, s44\ns_cbranch_scc1 sx_x0\n"
"s_memrealtime s[58:59]\ns_waitcnt lgkmcnt(4)\ns_cmp_ge_u32 s54, s44\ns_cbranch_scc1 sx_x0\n"
"s_memrealtime s[54:55]\ns_waitcnt lgkmcnt(4)\ns_cmp_ge_u32 s56, s44\ns_cbranch_scc1 sx_x0\n"
"s_memrealtime s[56:57]\ns_waitcnt lgkmcnt(4)\ns_cmp_ge_u32 s58, s44\ns_cbranch_scc1 sx_x0\n"
"s_memrealtime s[58:59]\ns_waitcnt lgkmcnt(4)\ns_cmp_ge_u32 s54, s44\ns_cbranch_scc1 sx_x0\n"
"s_memrealtime s[54:55]\ns_waitcnt lgkmcnt(4)\ns_cmp_ge_u32 s56, s44\ns_cbranch_scc1 sx_x0\n"
"s_memrealtime s[56:57]\ns_waitcnt lgkmcnt(4)\ns_cmp_ge_u32 s58, s44\ns_cbranch_scc1 sx_x0\n"
"s_memrealtime s[58:59]\ns_waitcnt lgkmcnt(4)\ns_cmp_ge_u32 s54, s44\ns_cbranch_scc1 sx_x0\n"
"s_memrealtime s[54:55]\ns_waitcnt lgkmcnt(4)\ns_cmp_ge_u32 s56, s44\ns_cbranch_scc1 sx_x0\n"
"s_memrealtime s[56:57]\ns_waitcnt lgkmcnt(4)\ns_cmp_ge_u32 s58, s44\ns_cbranch_scc1 sx_x0\n"
"s_memrealtime s[58:59]\ns_waitcnt lgkmcnt(4)\ns_cmp_ge_u32 s54, s44\ns_cbranch_scc1 sx_x0\n"
"s_memrealtime s[54:55]\ns_waitcnt lgkmcnt(4)\ns_cmp_ge_u32 s56, s44\ns_cbranch_scc1 sx_x0\n"
"s_memrealtime s[56:57]\ns_branch sx_x0\nso_x0:\ns_waitcnt lgkmcnt(0)\n"
"s_cmp_lt_i32 s47, 0\ns_cbranch_scc1 sf_x0\ns_branch sgo_x0\nsx_x0:\n"
"s_setpc_b64 s[60:61]\ns_nop 0\ns_nop 0\ns_nop 0\n"
"s_nop 0\ns_nop 0\ns_nop 0\ns_nop 0\n"
"s_nop 0\ns_nop 0\ns_nop 0\ns_nop 0\n"
"s_nop 0\ns_nop 0\ns_nop 0\ns_nop 0\n"
"s_nop 0\ns_nop 0\ns_nop 0\ns_nop 0\n"
"s_nop 0\ns_nop 0\ns_nop 0\ns_nop 0\n"
"s_nop 0\nsgo_x0:\nbuffer_load_dwordx4 v[40:43], v10, s[8:11], s43 offen offset:0 sc1\nbuffer_load_dwordx4 v[44:47], v10, s[8:11], s43 offen offset:1024 sc1\n"
"landed_x0:\ns_waitcnt vmcnt(1)\nv_smfmac_f32_16x16x64_bf16 v[48:51], v[40:43], a[0:7], v8\nv_max3_u32 v78, v78, v40, v41\n"
"v_smfmac_f32_16x16x64_bf16 v[52:55], v[40:43], a[16:23], v8\nv_max3_u32 v78, v78, v42, v43\ns_waitcnt vmcnt(0)\nv_smfmac_f32_16x16x64_bf16 v[48:51], v[44:47], a[8:15], v8\n"
"v_max3_u32 v78, v78, v44, v45\nv_smfmac_f32_16x16x64_bf16 v[52:55], v[44:47], a[24:31], v8\nv_max3_u32 v78, v78, v46, v47\nv_cmp_eq_u32 vcc, -1, v78\n"
"s_nop 0\nv_add_f32 v56, v48, v49\nv_add_f32 v57, v50, v51\ns_nop 1\n"
"v_add_f32 v58, v52, v53\nv_add_f32 v59, v54, v55\nds_write2_b32 v81, v56, v57 offset0:0 offset1:40\nds_write2_b32 v81, v58, v59 offset0:16 offset1:56\n"
"s_cbranch_vccnz retry_x0\nv_mov_b32 v79, 0\ns_nop 1\nv_mov_b32 v79, 0\n"
"s_nop 1\nv_mov_b32 v79, 0\ns_waitcnt lgkmcnt(0)\ns_barrier\n"
"ds_read_b64 v[68:69], v82 offset:0\nds_read_b64 v[70:71], v82 offset:1280\nds_read_b64 v[72:73], v82 offset:2560\nds_read_b64 v[74:75], v82 offset:3840\n"
"v_mov_b32 v79, 0\ns_nop 1\nv_mov_b32 v79, 0\ns_nop 1\n"
"v_mov_b32 v79, 0\ns_nop 1\ns_waitcnt lgkmcnt(2)\nv_pk_add_f32 v[68:69], v[68:69], v[70:71]\n"
"s_waitcnt lgkmcnt(0)\nv_pk_add_f32 v[72:73], v[72:73], v[74:75]\nv_pk_add_f32 v[68:69], v[68:69], v[72:73]\nv_exp_f32 v79, v68\n"
"v_exp_f32 v80, v69\ns_nop 0\nv_cvt_pk_bf16_f32 v79, v79, v80\nbuffer_store_dword v79, v16, s[8:11], s41 offen sc1\n"
"s_barrier\nbuffer_load_dword v85, v18, s[8:11], 0 offen sc1\nbuffer_load_dwordx4 a[0:3], v9, s[16:19], s50 offen offset:0 sc1\ns_nop 15\n"
"s_nop 7\nbuffer_load_dwordx4 a[4:7], v9, s[16:19], s50 offen offset:1024 sc1\ns_nop 15\ns_nop 7\n"
"buffer_load_dwordx4 a[8:11], v9, s[16:19], s50 offen offset:2048 sc1\ns_nop 15\ns_nop 7\nbuffer_load_dwordx4 a[12:15], v9, s[16:19], s50 offen offset:3072 sc1\n"
"s_nop 15\ns_nop 7\nbuffer_load_dwordx4 a[16:19], v9, s[16:19], s51 offen offset:0 sc1\ns_nop 15\n"
"s_nop 7\nbuffer_load_dwordx4 a[20:23], v9, s[16:19], s51 offen offset:1024 sc1\ns_nop 15\ns_nop 7\n"
"buffer_load_dwordx4 a[24:27], v9, s[16:19], s51 offen offset:2048 sc1\ns_nop 15\ns_nop 7\nbuffer_load_dwordx4 a[28:31], v9, s[16:19], s51 offen offset:3072 sc1\n"
"s_nop 15\ns_nop 7\ns_add_u32 s50, s50, 28672\ns_add_u32 s51, s51, 28672\n"
"buffer_store_dwordx2 v[68:69], v17, s[8:11], s41 offen sc1\nbuffer_store_dwordx4 v[64:67], v15, s[8:11], s42 offen sc1\nds_read_b32 v83, v84\nv_xor_b32 v81, 8192, v81\n"
"v_xor_b32 v82, 8192, v82\ns_waitcnt lgkmcnt(0)\nv_readfirstlane_b32 s47, v83\ns_sub_u32 s47, s47, s40\n"
"s_cmp_eq_u32 s47, 1\ns_cbranch_scc1 adopt_x0\nback_x0:\ns_waitcnt vmcnt(10)\n"
"fl_x0:\ns_add_u32 s47, s40, 4\ns_min_u32 s47, s47, 64\ns_add_u32 s47, s47, s33\n"
"v_subrev_u32 v85, s47, v85\nv_cmp_lt_u32 vcc, 64, v85\ns_cbranch_vccz flok_x0\ns_add_u32 s53, s53, 0\n"
"s_add_u32 s49, s49, 1\ns_cmp_gt_u32 s49, 65536\ns_cbranch_scc1 end_x0\ns_sleep 2\n"
"buffer_load_dword v85, v18, s[8:11], 0 offen sc1\ns_waitcnt vmcnt(0)\ns_branch fl_x0\nflok_x0:\n"
"s_add_u32 s40, s40, 1\ns_mov_b32 s43, s41\ns_add_u32 s41, s41, 131072\ns_and_b32 s41, s41, 524287\n"
"s_xor_b32 s42, s41, 262144\ns_and_b32 s47, s46, 15\ns_mul_i32 s48, s47, 4\ns_add_u32 s45, s45, s48\n"
"s_add_u32 s45, s45, s30\ns_lshr_b32 s48, s45, 4\ns_lshl_b32 s48, s48, 2\ns_add_u32 s44, s44, s48\n"
"s_and_b32 s45, s45, 15\ns_add_u32 s92, s92, 1\ns_lshr_b32 s48, s46, 4\ns_cmp_gt_u32 s47, s48\n"
"s_cselect_b32 s47, 1, 0\ns_and_b32 s48, s92, 1\ns_cselect_b32 s47, 0, s47\ns_sub_u32 s46, s46, s47\n"
"s_waitcnt lgkmcnt(0)\ns_cmp_lt_u32 s40, 63\ns_cbranch_scc1 top_x0\ns_branch end_x0\n"
"retry2_x0:\ns_sleep 3\nretry_x0:\ns_add_u32 s53, s53, 1\n"
"s_add_u32 s49, s49, 1\ns_cmp_gt_u32 s49, 65536\ns_cbranch_scc1 end_x0\nbuffer_load_dwordx4 v[40:43], v10, s[8:11], s43 offen offset:0 sc1\n"
"buffer_load_dwordx4 v[44:47], v10, s[8:11], s43 offen offset:1024 sc1\nv_mov_b32 v78, 0\ns_add_u32 s47, s40, 1\nv_mov_b32 v83, s47\n"
"ds_write_b32 v84, v83\ns_waitcnt vmcnt(0)\nv_max3_u32 v78, v78, v40, v41\nv_max3_u32 v78, v78, v42, v43\n"
"v_max3_u32 v78, v78, v44, v45\nv_max3_u32 v78, v78, v46, v47\nv_cmp_eq_u32 vcc, -1, v78\nv_mov_b32 v48, 0\n"
"v_mov_b32 v49, 0\nv_mov_b32 v50, 0\nv_mov_b32 v51, 0\nv_mov_b32 v52, 0\n"
"v_mov_b32 v53, 0\nv_mov_b32 v54, 0\nv_mov_b32 v55, 0\ns_cbranch_vccnz retry2_x0\n"
"s_branch landed_x0\nadopt_x0:\nbuffer_load_dwordx4 v[60:63], v28, s[8:11], 0 offen sc1\ns_lshl_b32 s52, s40, 26\n"
"s_or_b32 s47, s52, 0x3ffffff\ns_waitcnt vmcnt(0)\nv_cmp_ge_u32 vcc, s47, v60\ns_nop 1\n"
"v_cndmask_b32 v60, 0, v60, vcc\nv_cmp_ge_u32 vcc, s47, v61\ns_nop 1\nv_cndmask_b32 v61, 0, v61, vcc\n"
"v_cmp_ge_u32 vcc, s47, v62\ns_nop 1\nv_cndmask_b32 v62, 0, v62, vcc\nv_cmp_ge_u32 vcc, s47, v63\n"
"s_nop 1\nv_cndmask_b32 v63, 0, v63, vcc\nv_max3_u32 v83, v60, v61, v62\nv_max_u32 v83, v83, v63\n"
"s_nop 1\nv_max_u32_dpp v83, v83, v83 row_ror:8 row_mask:0xf bank_mask:0xf\ns_nop 1\nv_max_u32_dpp v83, v83, v83 row_ror:4 row_mask:0xf bank_mask:0xf\n"
"s_nop 1\nv_max_u32_dpp v83, v83, v83 row_ror:2 row_mask:0xf bank_mask:0xf\ns_nop 1\nv_max_u32_dpp v83, v83, v83 row_ror:1 row_mask:0xf bank_mask:0xf\n"
"s_nop 0\nv_readlane_b32 s47, v83, 0\nv_readlane_b32 s48, v83, 16\ns_max_u32 s48, s48, s47\n"
"s_cmp_le_u32 s48, s52\ns_cbranch_scc1 back_x0\ns_bfe_u32 s52, s48, 1179656\ns_add_u32 s44, s52, s28\n"
"s_mov_b32 s45, 0\ns_and_b32 s46, s48, 255\ns_mov_b32 s92, 0\ns_branch back_x0\n"
"end_x0:\ns_branch fk_done\nfk_x1:\ns_mov_b32 s40, 0\n"
"s_mov_b32 s41, 0\ns_mov_b32 s42, 262144\ns_mov_b32 s43, 393216\ns_mov_b32 s44, s28\n"
"s_mov_b32 s45, 0\ns_mov_b32 s46, 0\ns_mov_b32 s92, 0\ns_mov_b32 s49, 0\n"
"s_mov_b32 s53, 0\ns_mov_b32 s50, s35\ns_mov_b32 s51, s36\nv_mov_b32 v81, v13\n"
"v_mov_b32 v82, v14\nv_mov_b32 v84, 24576\ns_add_u32 s51, s50, 14336\nv_mov_b32 v64, -1\n"
"v_mov_b32 v65, -1\nv_mov_b32 v66, -1\nv_mov_b32 v67, -1\nbuffer_load_dwordx4 a[8:11], v9, s[16:19], s50 offen offset:2048 sc1\n"
"buffer_load_dwordx4 a[12:15], v9, s[16:19], s50 offen offset:3072 sc1\nbuffer_load_dwordx4 a[0:3], v9, s[16:19], s50 offen offset:0 sc1\nbuffer_load_dwordx4 a[4:7], v9, s[16:19], s50 offen offset:1024 sc1\nbuffer_load_dwordx4 a[24:27], v9, s[16:19], s51 offen offset:2048 sc1\n"
"buffer_load_dwordx4 a[28:31], v9, s[16:19], s51 offen offset:3072 sc1\nbuffer_load_dwordx4 a[16:19], v9, s[16:19], s51 offen offset:0 sc1\nbuffer_load_dwordx4 a[20:23], v9, s[16:19], s51 offen offset:1024 sc1\ns_add_u32 s50, s50, 28672\n"
"s_add_u32 s51, s51, 28672\ns_waitcnt vmcnt(0)\ns_barrier\n.p2align 6\n"
"top_x1:\nv_mov_b32 v48, 0\nv_mov_b32 v49, 0\nv_mov_b32 v50, 0\n"
"v_mov_b32 v51, 0\nv_mov_b32 v52, 0\nv_mov_b32 v53, 0\nv_mov_b32 v54, 0\n"
"v_mov_b32 v55, 0\nv_mov_b32 v78, 0\ns_mul_i32 s48, s45, 6\ns_and_b32 s48, s48, -4\n"
"s_getpc_b64 s[60:61]\nspc_x1:\ns_sub_u32 s48, sgo_x1-spc_x1, s48\ns_add_u32 s60, s60, s48\n"
"s_addc_u32 s61, s61, 0\nsf_x1:\ns_memrealtime s[54:55]\ns_nop 2\n"
"s_memrealtime s[56:57]\ns_nop 2\ns_memrealtime s[58:59]\ns_waitcnt lgkmcnt(4)\n"
"s_sub_u32 s47, s54, s44\ns_add_u32 s48, s47, 12\ns_cmp_ge_u32 s48, 12\ns_cbranch_scc1 so_x1\n"
"s_memrealtime s[54:55]\ns_waitcnt lgkmcnt(4)\ns_cmp_ge_u32 s56, s44\ns_cbranch_scc1 sx_x1\n"
"s_memrealtime s[56:57]\ns_waitcnt lgkmcnt(4)\ns_cmp_ge_u32 s58, s44\ns_cbranch_scc1 sx_x1\n"
"s_memrealtime s[58:59]\ns_waitcnt lgkmcnt(4)\ns_cmp_ge_u32 s54, s44\ns_cbranch_scc1 sx_x1\n"
"s_memrealtime s[54:55]\ns_waitcnt lgkmcnt(4)\ns_cmp_ge_u32 s56, s44\ns_cbranch_scc1 sx_x1\n"
"s_memrealtime s[56:57]\ns_waitcnt lgkmcnt(4)\ns_cmp_ge_u32 s58, s44\ns_cbranch_scc1 sx_x1\n"
"s_memrealtime s[58:59]\ns_waitcnt lgkmcnt(4)\ns_cmp_ge_u32 s54, s44\ns_cbranch_scc1 sx_x1\n"
"s_memrealtime s[54:55]\ns_waitcnt lgkmcnt(4)\ns_cmp_ge_u32 s56, s44\ns_cbranch_scc1 sx_x1\n"
"s_memrealtime s[56:57]\ns_waitcnt lgkmcnt(4)\ns_cmp_ge_u32 s58, s44\ns_cbranch_scc1 sx_x1\n"
"s_memrealtime s[58:59]\ns_waitcnt lgkmcnt(4)\ns_cmp_ge_u32 s54, s44\ns_cbranch_scc1 sx_x1\n"
"s_memrealtime s[54:55]\ns_waitcnt lgkmcnt(4)\ns_cmp_ge_u32 s56, s44\ns_cbranch_scc1 sx_x1\n"
"s_memrealtime s[56:57]\ns_waitcnt lgkmcnt(4)\ns_cmp_ge_u32 s58, s44\ns_cbranch_scc1 sx_x1\n"
"s_memrealtime s[58:59]\ns_waitcnt lgkmcnt(4)\ns_cmp_ge_u32 s54, s44\ns_cbranch_scc1 sx_x1\n"
"s_memrealtime s[54:55]\ns_waitcnt lgkmcnt(4)\ns_cmp_ge_u32 s56, s44\ns_cbranch_scc1 sx_x1\n"
"s_memrealtime s[56:57]\ns_waitcnt lgkmcnt(4)\ns_cmp_ge_u32 s58, s44\ns_cbranch_scc1 sx_x1\n"
"s_memrealtime s[58:59]\ns_waitcnt lgkmcnt(4)\ns_cmp_ge_u32 s54, s44\ns_cbranch_scc1 sx_x1\n"
"s_memrealtime s[54:55]\ns_waitcnt lgkmcnt(4)\ns_cmp_ge_u32 s56, s44\ns_cbranch_scc1 sx_x1\n"
"s_memrealtime s[56:57]\ns_branch sx_x1\nso_x1:\ns_waitcnt lgkmcnt(0)\n"
"s_cmp_lt_i32 s47, 0\ns_cbranch_scc1 sf_x1\ns_branch sgo_x1\nsx_x1:\n"
"s_setpc_b64 s[60:61]\ns_nop 0\ns_nop 0\ns_nop 0\n"
"s_nop 0\ns_nop 0\ns_nop 0\ns_nop 0\n"
"s_nop 0\ns_nop 0\ns_nop 0\ns_nop 0\n"
"s_nop 0\ns_nop 0\ns_nop 0\ns_nop 0\n"
"s_nop 0\ns_nop 0\ns_nop 0\ns_nop 0\n"
"s_nop 0\ns_nop 0\ns_nop 0\ns_nop 0\n"
"s_nop 0\nsgo_x1:\ns_nop 5\nbuffer_load_dwordx4 v[40:43], v10, s[8:11], s43 offen offset:1024 sc1\n"
"buffer_load_dwordx4 v[44:47], v10, s[8:11], s43 offen offset:0 sc1\nlanded_x1:\ns_waitcnt vmcnt(1)\nv_smfmac_f32_16x16x64_bf16 v[48:51], v[40:43], a[8:15], v8\n"
"v_max3_u32 v78, v78, v40, v41\nv_smfmac_f32_16x16x64_bf16 v[52:55], v[40:43], a[24:31], v8\nv_max3_u32 v78, v78, v42, v43\ns_waitcnt vmcnt(0)\n"
"v_smfmac_f32_16x16x64_bf16 v[48:51], v[44:47], a[0:7], v8\nv_max3_u32 v78, v78, v44, v45\nv_smfmac_f32_16x16x64_bf16 v[52:55], v[44:47], a[16:23], v8\nv_max3_u32 v78, v78, v46, v47\n"
"v_cmp_eq_u32 vcc, -1, v78\ns_nop 0\nv_add_f32 v56, v48, v49\nv_add_f32 v57, v50, v51\n"
"s_nop 1\nv_add_f32 v58, v52, v53\nv_add_f32 v59, v54, v55\nds_write2_b32 v81, v56, v57 offset0:0 offset1:40\n"
"ds_write2_b32 v81, v58, v59 offset0:16 offset1:56\ns_cbranch_vccnz retry_x1\nv_mov_b32 v79, 0\ns_nop 1\n"
"v_mov_b32 v79, 0\ns_nop 1\nv_mov_b32 v79, 0\ns_waitcnt lgkmcnt(0)\n"
"s_barrier\nds_read_b64 v[68:69], v82 offset:0\nds_read_b64 v[70:71], v82 offset:1280\nds_read_b64 v[72:73], v82 offset:2560\n"
"ds_read_b64 v[74:75], v82 offset:3840\nv_mov_b32 v79, 0\ns_nop 1\nv_mov_b32 v79, 0\n"
"s_nop 1\nv_mov_b32 v79, 0\ns_nop 1\ns_waitcnt lgkmcnt(2)\n"
"v_pk_add_f32 v[68:69], v[68:69], v[70:71]\ns_waitcnt lgkmcnt(0)\nv_pk_add_f32 v[72:73], v[72:73], v[74:75]\nv_pk_add_f32 v[68:69], v[68:69], v[72:73]\n"
"v_exp_f32 v79, v68\nv_exp_f32 v80, v69\ns_nop 0\nv_cvt_pk_bf16_f32 v79, v79, v80\n"
"buffer_store_dword v79, v16, s[8:11], s41 offen sc1\ns_barrier\nbuffer_load_dword v85, v18, s[8:11], 0 offen sc1\nbuffer_load_dwordx4 a[8:11], v9, s[16:19], s50 offen offset:2048 sc1\n"
"s_nop 15\ns_nop 7\nbuffer_load_dwordx4 a[12:15], v9, s[16:19], s50 offen offset:3072 sc1\ns_nop 15\n"
"s_nop 7\nbuffer_load_dwordx4 a[0:3], v9, s[16:19], s50 offen offset:0 sc1\ns_nop 15\ns_nop 7\n"
"buffer_load_dwordx4 a[4:7], v9, s[16:19], s50 offen offset:1024 sc1\ns_nop 15\ns_nop 7\nbuffer_load_dwordx4 a[24:27], v9, s[16:19], s51 offen offset:2048 sc1\n"
"s_nop 15\ns_nop 7\nbuffer_load_dwordx4 a[28:31], v9, s[16:19], s51 offen offset:3072 sc1\ns_nop 15\n"
"s_nop 7\nbuffer_load_dwordx4 a[16:19], v9, s[16:19], s51 offen offset:0 sc1\ns_nop 15\ns_nop 7\n"
"buffer_load_dwordx4 a[20:23], v9, s[16:19], s51 offen offset:1024 sc1\ns_nop 15\ns_nop 7\ns_add_u32 s50, s50, 28672\n"
"s_add_u32 s51, s51, 28672\nbuffer_store_dwordx2 v[68:69], v17, s[8:11], s41 offen sc1\nbuffer_store_dwordx4 v[64:67], v15, s[8:11], s42 offen sc1\nds_read_b32 v83, v84\n"
"v_xor_b32 v81, 8192, v81\nv_xor_b32 v82, 8192, v82\ns_waitcnt lgkmcnt(0)\nv_readfirstlane_b32 s47, v83\n"
"s_sub_u32 s47, s47, s40\ns_cmp_eq_u32 s47, 1\ns_cbranch_scc1 adopt_x1\nback_x1:\n"
"s_waitcnt vmcnt(10)\nfl_x1:\ns_add_u32 s47, s40, 4\ns_min_u32 s47, s47, 64\n"
"s_add_u32 s47, s47, s33\nv_subrev_u32 v85, s47, v85\nv_cmp_lt_u32 vcc, 64, v85\ns_cbranch_vccz flok_x1\n"
"s_add_u32 s53, s53, 0\ns_add_u32 s49, s49, 1\ns_cmp_gt_u32 s49, 65536\ns_cbranch_scc1 end_x1\n"
"s_sleep 2\nbuffer_load_dword v85, v18, s[8:11], 0 offen sc1\ns_waitcnt vmcnt(0)\ns_branch fl_x1\n"
"flok_x1:\ns_add_u32 s40, s40, 1\ns_mov_b32 s43, s41\ns_add_u32 s41, s41, 131072\n"
"s_and_b32 s41, s41, 524287\ns_xor_b32 s42, s41, 262144\ns_and_b32 s47, s46, 15\ns_mul_i32 s48, s47, 4\n"
"s_add_u32 s45, s45, s48\ns_add_u32 s45, s45, s30\ns_lshr_b32 s48, s45, 4\ns_lshl_b32 s48, s48, 2\n"
"s_add_u32 s44, s44, s48\ns_and_b32 s45, s45, 15\ns_add_u32 s92, s92, 1\ns_lshr_b32 s48, s46, 4\n"
"s_cmp_gt_u32 s47, s48\ns_cselect_b32 s47, 1, 0\ns_and_b32 s48, s92, 1\ns_cselect_b32 s47, 0, s47\n"
"s_sub_u32 s46, s46, s47\ns_waitcnt lgkmcnt(0)\ns_cmp_lt_u32 s40, 63\ns_cbranch_scc1 top_x1\n"
"s_branch end_x1\nretry2_x1:\ns_sleep 3\nretry_x1:\n"
"s_add_u32 s53, s53, 1\ns_add_u32 s49, s49, 1\ns_cmp_gt_u32 s49, 65536\ns_cbranch_scc1 end_x1\n"
"buffer_load_dwordx4 v[40:43], v10, s[8:11], s43 offen offset:1024 sc1\nbuffer_load_dwordx4 v[44:47], v10, s[8:11], s43 offen offset:0 sc1\nv_mov_b32 v78, 0\ns_add_u32 s47, s40, 1\n"
"v_mov_b32 v83, s47\nds_write_b32 v84, v83\ns_waitcnt vmcnt(0)\nv_max3_u32 v78, v78, v40, v41\n"
"v_max3_u32 v78, v78, v42, v43\nv_max3_u32 v78, v78, v44, v45\nv_max3_u32 v78, v78, v46, v47\nv_cmp_eq_u32 vcc, -1, v78\n"
"v_mov_b32 v48, 0\nv_mov_b32 v49, 0\nv_mov_b32 v50, 0\nv_mov_b32 v51, 0\n"
"v_mov_b32 v52, 0\nv_mov_b32 v53, 0\nv_mov_b32 v54, 0\nv_mov_b32 v55, 0\n"
"s_cbranch_vccnz retry2_x1\ns_branch landed_x1\nadopt_x1:\nbuffer_load_dwordx4 v[60:63], v28, s[8:11], 0 offen sc1\n"
"s_lshl_b32 s52, s40, 26\ns_or_b32 s47, s52, 0x3ffffff\ns_waitcnt vmcnt(0)\nv_cmp_ge_u32 vcc, s47, v60\n"
"s_nop 1\nv_cndmask_b32 v60, 0, v60, vcc\nv_cmp_ge_u32 vcc, s47, v61\ns_nop 1\n"
"v_cndmask_b32 v61, 0, v61, vcc\nv_cmp_ge_u32 vcc, s47, v62\ns_nop 1\nv_cndmask_b32 v62, 0, v62, vcc\n"
"v_cmp_ge_u32 vcc, s47, v63\ns_nop 1\nv_cndmask_b32 v63, 0, v63, vcc\nv_max3_u32 v83, v60, v61, v62\n"
"v_max_u32 v83, v83, v63\ns_nop 1\nv_max_u32_dpp v83, v83, v83 row_ror:8 row_mask:0xf bank_mask:0xf\ns_nop 1\n"
"v_max_u32_dpp v83, v83, v83 row_ror:4 row_mask:0xf bank_mask:0xf\ns_nop 1\nv_max_u32_dpp v83, v83, v83 row_ror:2 row_mask:0xf bank_mask:0xf\ns_nop 1\n"
"v_max_u32_dpp v83, v83, v83 row_ror:1 row_mask:0xf bank_mask:0xf\ns_nop 0\nv_readlane_b32 s47, v83, 0\nv_readlane_b32 s48, v83, 16\n"
"s_max_u32 s48, s48, s47\ns_cmp_le_u32 s48, s52\ns_cbranch_scc1 back_x1\ns_bfe_u32 s52, s48, 1179656\n"
"s_add_u32 s44, s52, s28\ns_mov_b32 s45, 0\ns_and_b32 s46, s48, 255\ns_mov_b32 s92, 0\n"
"s_branch back_x1\nend_x1:\ns_branch fk_done\nfk_x2:\n"
"s_mov_b32 s40, 0\ns_mov_b32 s41, 0\ns_mov_b32 s42, 262144\ns_mov_b32 s43, 393216\n"
"s_mov_b32 s44, s28\ns_mov_b32 s45, 0\ns_mov_b32 s46, 0\ns_mov_b32 s92, 0\n"
"s_mov_b32 s49, 0\ns_mov_b32 s53, 0\ns_mov_b32 s50, s35\ns_mov_b32 s51, s36\n"
"v_mov_b32 v81, v13\nv_mov_b32 v82, v14\nv_mov_b32 v84, 24576\ns_add_u32 s51, s50, 14336\n"
"v_mov_b32 v64, -1\nv_mov_b32 v65, -1\nv_mov_b32 v66, -1\nv_mov_b32 v67, -1\n"
"buffer_load_dwordx4 a[0:3], v9, s[16:19], s50 offen offset:0 sc1\nbuffer_load_dwordx4 a[4:7], v9, s[16:19], s50 offen offset:1024 sc1\nbuffer_load_dwordx4 a[8:11], v9, s[16:19], s50 offen offset:2048 sc1\nbuffer_load_dwordx4 a[12:15], v9, s[16:19], s50 offen offset:3072 sc1\n"
"buffer_load_dwordx4 a[16:19], v9, s[16:19], s51 offen offset:0 sc1\nbuffer_load_dwordx4 a[20:23], v9, s[16:19], s51 offen offset:1024 sc1\nbuffer_load_dwordx4 a[24:27], v9, s[16:19], s51 offen offset:2048 sc1\nbuffer_load_dwordx4 a[28:31], v9, s[16:19], s51 offen offset:3072 sc1\n"
"s_add_u32 s50, s50, 28672\ns_add_u32 s51, s51, 28672\ns_waitcnt vmcnt(0)\ns_barrier\n"
".p2align 6\ntop_x2:\nv_mov_b32 v48, 0\nv_mov_b32 v49, 0\n"
"v_mov_b32 v50, 0\nv_mov_b32 v51, 0\nv_mov_b32 v52, 0\nv_mov_b32 v53, 0\n"
"v_mov_b32 v54, 0\nv_mov_b32 v55, 0\nv_mov_b32 v78, 0\ns_mul_i32 s48, s45, 6\n"
"s_and_b32 s48, s48, -4\ns_getpc_b64 s[60:61]\nspc_x2:\ns_sub_u32 s48, sgo_x2-spc_x2, s48\n"
"s_add_u32 s60, s60, s48\ns_addc_u32 s61, s61, 0\nsf_x2:\ns_memrealtime s[54:55]\n"
"s_nop 2\ns_memrealtime s[56:57]\ns_nop 2\ns_memrealtime s[58:59]\n"
"s_waitcnt lgkmcnt(4)\ns_sub_u32 s47, s54, s44\ns_add_u32 s48, s47, 12\ns_cmp_ge_u32 s48, 12\n"
"s_cbranch_scc1 so_x2\ns_memrealtime s[54:55]\ns_waitcnt lgkmcnt(4)\ns_cmp_ge_u32 s56, s44\n"
"s_cbranch_scc1 sx_x2\ns_memrealtime s[56:57]\ns_waitcnt lgkmcnt(4)\ns_cmp_ge_u32 s58, s44\n"
"s_cbranch_scc1 sx_x2\ns_memrealtime s[58:59]\ns_waitcnt lgkmcnt(4)\ns_cmp_ge_u32 s54, s44\n"
"s_cbranch_scc1 sx_x2\ns_memrealtime s[54:55]\ns_waitcnt lgkmcnt(4)\ns_cmp_ge_u32 s56, s44\n"
"s_cbranch_scc1 sx_x2\ns_memrealtime s[56:57]\ns_waitcnt lgkmcnt(4)\ns_cmp_ge_u32 s58, s44\n"
"s_cbranch_scc1 sx_x2\ns_memrealtime s[58:59]\ns_waitcnt lgkmcnt(4)\ns_cmp_ge_u32 s54, s44\n"
"s_cbranch_scc1 sx_x2\ns_memrealtime s[54:55]\ns_waitcnt lgkmcnt(4)\ns_cmp_ge_u32 s56, s44\n"
"s_cbranch_scc1 sx_x2\ns_memrealtime s[56:57]\ns_waitcnt lgkmcnt(4)\ns_cmp_ge_u32 s58, s44\n"
"s_cbranch_scc1 sx_x2\ns_memrealtime s[58:59]\ns_waitcnt lgkmcnt(4)\ns_cmp_ge_u32 s54, s44\n"
"s_cbranch_scc1 sx_x2\ns_memrealtime s[54:55]\ns_waitcnt lgkmcnt(4)\ns_cmp_ge_u32 s56, s44\n"
"s_cbranch_scc1 sx_x2\ns_memrealtime s[56:57]\ns_waitcnt lgkmcnt(4)\ns_cmp_ge_u32 s58, s44\n"
"s_cbranch_scc1 sx_x2\ns_memrealtime s[58:59]\ns_waitcnt lgkmcnt(4)\ns_cmp_ge_u32 s54, s44\n"
"s_cbranch_scc1 sx_x2\ns_memrealtime s[54:55]\ns_waitcnt lgkmcnt(4)\ns_cmp_ge_u32 s56, s44\n"
"s_cbranch_scc1 sx_x2\ns_memrealtime s[56:57]\ns_waitcnt lgkmcnt(4)\ns_cmp_ge_u32 s58, s44\n"
"s_cbranch_scc1 sx_x2\ns_memrealtime s[58:59]\ns_waitcnt lgkmcnt(4)\ns_cmp_ge_u32 s54, s44\n"
"s_cbranch_scc1 sx_x2\ns_memrealtime s[54:55]\ns_waitcnt lgkmcnt(4)\ns_cmp_ge_u32 s56, s44\n"
"s_cbranch_scc1 sx_x2\ns_memrealtime s[56:57]\ns_branch sx_x2\nso_x2:\n"
"s_waitcnt lgkmcnt(0)\ns_cmp_lt_i32 s47, 0\ns_cbranch_scc1 sf_x2\ns_branch sgo_x2\n"
"sx_x2:\ns_setpc_b64 s[60:61]\ns_nop 0\ns_nop 0\n"
"s_nop 0\ns_nop 0\ns_nop 0\ns_nop 0\n"
"s_nop 0\ns_nop 0\ns_nop 0\ns_nop 0\n"
"s_nop 0\ns_nop 0\ns_nop 0\ns_nop 0\n"
"s_nop 0\ns_nop 0\ns_nop 0\ns_nop 0\n"
"s_nop 0\ns_nop 0\ns_nop 0\ns_nop 0\n"
"s_nop 0\ns_nop 0\nsgo_x2:\ns_nop 11\n"
"buffer_load_dwordx4 v[40:43], v10, s[8:11], s43 offen offset:0 sc1\nbuffer_load_dwordx4 v[44:47], v10, s[8:11], s43 offen offset:1024 sc1\nv_mov_b32 v79, 0\ns_nop 1\n"
"v_mov_b32 v79, 0\ns_nop 1\nv_mov_b32 v79, 0\ns_nop 1\n"
"v_mov_b32 v79, 0\ns_nop 1\nv_mov_b32 v79, 0\ns_nop 1\n"
"v_mov_b32 v79, 0\ns_nop 1\nv_mov_b32 v79, 0\ns_nop 1\n"
"v_mov_b32 v79, 0\ns_nop 1\nv_mov_b32 v79, 0\ns_nop 1\n"
"v_mov_b32 v79, 0\ns_nop 1\nv_mov_b32 v79, 0\ns_nop 1\n"
"v_mov_b32 v79, 0\ns_nop 1\nv_mov_b32 v79, 0\ns_nop 1\n"
"v_mov_b32 v79, 0\ns_nop 1\nv_mov_b32 v79, 0\ns_nop 1\n"
"v_mov_b32 v79, 0\ns_nop 1\nv_mov_b32 v79, 0\ns_nop 1\n"
"v_mov_b32 v79, 0\ns_nop 1\nv_mov_b32 v79, 0\ns_nop 1\n"
"v_mov_b32 v79, 0\ns_nop 1\nv_mov_b32 v79, 0\ns_nop 1\n"
"v_mov_b32 v79, 0\ns_nop 1\nv_mov_b32 v79, 0\ns_nop 1\n"
"v_mov_b32 v79, 0\ns_nop 1\nv_mov_b32 v79, 0\ns_nop 1\n"
"v_mov_b32 v79, 0\ns_nop 1\nv_mov_b32 v79, 0\ns_nop 1\n"
"v_mov_b32 v79, 0\ns_nop 1\nv_mov_b32 v79, 0\nlanded_x2:\n"
"s_waitcnt vmcnt(1)\nv_smfmac_f32_16x16x64_bf16 v[48:51], v[40:43], a[0:7], v8\nv_max3_u32 v78, v78, v40, v41\nv_smfmac_f32_16x16x64_bf16 v[52:55], v[40:43], a[16:23], v8\n"
"v_max3_u32 v78, v78, v42, v43\ns_waitcnt vmcnt(0)\nv_smfmac_f32_16x16x64_bf16 v[48:51], v[44:47], a[8:15], v8\nv_max3_u32 v78, v78, v44, v45\n"
"v_smfmac_f32_16x16x64_bf16 v[52:55], v[44:47], a[24:31], v8\nv_max3_u32 v78, v78, v46, v47\nv_cmp_eq_u32 vcc, -1, v78\ns_nop 0\n"
"v_add_f32 v56, v48, v49\nv_add_f32 v57, v50, v51\ns_nop 1\nv_add_f32 v58, v52, v53\n"
"v_add_f32 v59, v54, v55\nds_write2_b32 v81, v56, v57 offset0:0 offset1:40\nds_write2_b32 v81, v58, v59 offset0:16 offset1:56\ns_cbranch_vccnz retry_x2\n"
"v_mov_b32 v79, 0\ns_nop 1\nv_mov_b32 v79, 0\ns_nop 1\n"
"v_mov_b32 v79, 0\ns_waitcnt lgkmcnt(0)\ns_barrier\nds_read_b64 v[68:69], v82 offset:0\n"
"ds_read_b64 v[70:71], v82 offset:1280\nds_read_b64 v[72:73], v82 offset:2560\nds_read_b64 v[74:75], v82 offset:3840\nv_mov_b32 v79, 0\n"
"s_nop 1\nv_mov_b32 v79, 0\ns_nop 1\nv_mov_b32 v79, 0\n"
"s_nop 1\ns_waitcnt lgkmcnt(2)\nv_pk_add_f32 v[68:69], v[68:69], v[70:71]\ns_waitcnt lgkmcnt(0)\n"
"v_pk_add_f32 v[72:73], v[72:73], v[74:75]\nv_pk_add_f32 v[68:69], v[68:69], v[72:73]\nv_exp_f32 v79, v68\nv_exp_f32 v80, v69\n"
"s_nop 0\nv_cvt_pk_bf16_f32 v79, v79, v80\nbuffer_store_dword v79, v16, s[8:11], s41 offen sc1\ns_barrier\n"
"buffer_load_dword v85, v18, s[8:11], 0 offen sc1\nbuffer_load_dwordx4 a[0:3], v9, s[16:19], s50 offen offset:0 sc1\ns_nop 15\ns_nop 7\n"
"buffer_load_dwordx4 a[4:7], v9, s[16:19], s50 offen offset:1024 sc1\ns_nop 15\ns_nop 7\nbuffer_load_dwordx4 a[8:11], v9, s[16:19], s50 offen offset:2048 sc1\n"
"s_nop 15\ns_nop 7\nbuffer_load_dwordx4 a[12:15], v9, s[16:19], s50 offen offset:3072 sc1\ns_nop 15\n"
"s_nop 7\nbuffer_load_dwordx4 a[16:19], v9, s[16:19], s51 offen offset:0 sc1\ns_nop 15\ns_nop 7\n"
"buffer_load_dwordx4 a[20:23], v9, s[16:19], s51 offen offset:1024 sc1\ns_nop 15\ns_nop 7\nbuffer_load_dwordx4 a[24:27], v9, s[16:19], s51 offen offset:2048 sc1\n"
"s_nop 15\ns_nop 7\nbuffer_load_dwordx4 a[28:31], v9, s[16:19], s51 offen offset:3072 sc1\ns_nop 15\n"
"s_nop 7\ns_add_u32 s50, s50, 28672\ns_add_u32 s51, s51, 28672\nbuffer_store_dwordx2 v[68:69], v17, s[8:11], s41 offen sc1\n"
"buffer_store_dwordx4 v[64:67], v15, s[8:11], s42 offen sc1\nds_read_b32 v83, v84\nv_xor_b32 v81, 8192, v81\nv_xor_b32 v82, 8192, v82\n"
"s_waitcnt lgkmcnt(0)\nv_readfirstlane_b32 s47, v83\ns_sub_u32 s47, s47, s40\ns_cmp_eq_u32 s47, 1\n"
"s_cbranch_scc1 adopt_x2\nback_x2:\ns_waitcnt vmcnt(10)\nfl_x2:\n"
"s_add_u32 s47, s40, 4\ns_min_u32 s47, s47, 64\ns_add_u32 s47, s47, s33\nv_subrev_u32 v85, s47, v85\n"
"v_cmp_lt_u32 vcc, 64, v85\ns_cbranch_vccz flok_x2\ns_add_u32 s53, s53, 0\ns_add_u32 s49, s49, 1\n"
"s_cmp_gt_u32 s49, 65536\ns_cbranch_scc1 end_x2\ns_sleep 2\nbuffer_load_dword v85, v18, s[8:11], 0 offen sc1\n"
"s_waitcnt vmcnt(0)\ns_branch fl_x2\nflok_x2:\ns_add_u32 s40, s40, 1\n"
"s_mov_b32 s43, s41\ns_add_u32 s41, s41, 131072\ns_and_b32 s41, s41, 524287\ns_xor_b32 s42, s41, 262144\n"
"s_and_b32 s47, s46, 15\ns_mul_i32 s48, s47, 4\ns_add_u32 s45, s45, s48\ns_add_u32 s45, s45, s30\n"
"s_lshr_b32 s48, s45, 4\ns_lshl_b32 s48, s48, 2\ns_add_u32 s44, s44, s48\ns_and_b32 s45, s45, 15\n"
"s_add_u32 s92, s92, 1\ns_lshr_b32 s48, s46, 4\ns_cmp_gt_u32 s47, s48\ns_cselect_b32 s47, 1, 0\n"
"s_and_b32 s48, s92, 1\ns_cselect_b32 s47, 0, s47\ns_sub_u32 s46, s46, s47\ns_waitcnt lgkmcnt(0)\n"
"s_cmp_lt_u32 s40, 63\ns_cbranch_scc1 top_x2\ns_branch end_x2\nretry2_x2:\n"
"s_sleep 3\nretry_x2:\ns_add_u32 s53, s53, 1\ns_add_u32 s49, s49, 1\n"
"s_cmp_gt_u32 s49, 65536\ns_cbranch_scc1 end_x2\nbuffer_load_dwordx4 v[40:43], v10, s[8:11], s43 offen offset:0 sc1\nbuffer_load_dwordx4 v[44:47], v10, s[8:11], s43 offen offset:1024 sc1\n"
"v_mov_b32 v78, 0\ns_add_u32 s47, s40, 1\nv_mov_b32 v83, s47\nds_write_b32 v84, v83\n"
"s_waitcnt vmcnt(0)\nv_max3_u32 v78, v78, v40, v41\nv_max3_u32 v78, v78, v42, v43\nv_max3_u32 v78, v78, v44, v45\n"
"v_max3_u32 v78, v78, v46, v47\nv_cmp_eq_u32 vcc, -1, v78\nv_mov_b32 v48, 0\nv_mov_b32 v49, 0\n"
"v_mov_b32 v50, 0\nv_mov_b32 v51, 0\nv_mov_b32 v52, 0\nv_mov_b32 v53, 0\n"
"v_mov_b32 v54, 0\nv_mov_b32 v55, 0\ns_cbranch_vccnz retry2_x2\ns_branch landed_x2\n"
"adopt_x2:\nbuffer_load_dwordx4 v[60:63], v28, s[8:11], 0 offen sc1\ns_lshl_b32 s52, s40, 26\ns_or_b32 s47, s52, 0x3ffffff\n"
"s_waitcnt vmcnt(0)\nv_cmp_ge_u32 vcc, s47, v60\ns_nop 1\nv_cndmask_b32 v60, 0, v60, vcc\n"
"v_cmp_ge_u32 vcc, s47, v61\ns_nop 1\nv_cndmask_b32 v61, 0, v61, vcc\nv_cmp_ge_u32 vcc, s47, v62\n"
"s_nop 1\nv_cndmask_b32 v62, 0, v62, vcc\nv_cmp_ge_u32 vcc, s47, v63\ns_nop 1\n"
"v_cndmask_b32 v63, 0, v63, vcc\nv_max3_u32 v83, v60, v61, v62\nv_max_u32 v83, v83, v63\ns_nop 1\n"
"v_max_u32_dpp v83, v83, v83 row_ror:8 row_mask:0xf bank_mask:0xf\ns_nop 1\nv_max_u32_dpp v83, v83, v83 row_ror:4 row_mask:0xf bank_mask:0xf\ns_nop 1\n"
"v_max_u32_dpp v83, v83, v83 row_ror:2 row_mask:0xf bank_mask:0xf\ns_nop 1\nv_max_u32_dpp v83, v83, v83 row_ror:1 row_mask:0xf bank_mask:0xf\ns_nop 0\n"
"v_readlane_b32 s47, v83, 0\nv_readlane_b32 s48, v83, 16\ns_max_u32 s48, s48, s47\ns_cmp_le_u32 s48, s52\n"
"s_cbranch_scc1 back_x2\ns_bfe_u32 s52, s48, 1179656\ns_add_u32 s44, s52, s28\ns_mov_b32 s45, 0\n"
"s_and_b32 s46, s48, 255\ns_mov_b32 s92, 0\ns_branch back_x2\nend_x2:\n"
"s_branch fk_done\nfk_x3:\ns_mov_b32 s40, 0\ns_mov_b32 s41, 0\n"
"s_mov_b32 s42, 262144\ns_mov_b32 s43, 393216\ns_mov_b32 s44, s28\ns_mov_b32 s45, 0\n"
"s_mov_b32 s46, 0\ns_mov_b32 s92, 0\ns_mov_b32 s49, 0\ns_mov_b32 s53, 0\n"
"s_mov_b32 s50, s35\ns_mov_b32 s51, s36\nv_mov_b32 v77, v13\nv_mov_b32 v78, v14\n"
"v_mov_b32 v80, 24576\ns_add_u32 s51, s50, 14336\nv_mov_b32 v60, -1\nv_mov_b32 v61, -1\n"
"v_mov_b32 v62, -1\nv_mov_b32 v63, -1\nbuffer_load_dwordx4 a[0:3], v9, s[16:19], s50 offen offset:0 sc1\nbuffer_load_dwordx4 a[4:7], v9, s[16:19], s50 offen offset:1024 sc1\n"
"buffer_load_dwordx4 a[16:19], v9, s[16:19], s51 offen offset:0 sc1\nbuffer_load_dwordx4 a[20:23], v9, s[16:19], s51 offen offset:1024 sc1\ns_add_u32 s50, s50, 28672\ns_add_u32 s51, s51, 28672\n"
"s_waitcnt vmcnt(0)\ns_barrier\n.p2align 6\ntop_x3:\n"
"v_mov_b32 v44, 0\nv_mov_b32 v45, 0\nv_mov_b32 v46, 0\nv_mov_b32 v47, 0\n"
"v_mov_b32 v48, 0\nv_mov_b32 v49, 0\nv_mov_b32 v50, 0\nv_mov_b32 v51, 0\n"
"v_mov_b32 v74, 0\ns_mul_i32 s48, s45, 6\ns_and_b32 s48, s48, -4\ns_getpc_b64 s[60:61]\n"
"spc_x3:\ns_sub_u32 s48, sgo_x3-spc_x3, s48\ns_add_u32 s60, s60, s48\ns_addc_u32 s61, s61, 0\n"
"sf_x3:\ns_memrealtime s[54:55]\ns_nop 2\ns_memrealtime s[56:57]\n"
"s_nop 2\ns_memrealtime s[58:59]\ns_waitcnt lgkmcnt(4)\ns_sub_u32 s47, s54, s44\n"
"s_add_u32 s48, s47, 12\ns_cmp_ge_u32 s48, 12\ns_cbranch_scc1 so_x3\ns_memrealtime s[54:55]\n"
"s_waitcnt lgkmcnt(4)\ns_cmp_ge_u32 s56, s44\ns_cbranch_scc1 sx_x3\ns_memrealtime s[56:57]\n"
"s_waitcnt lgkmcnt(4)\ns_cmp_ge_u32 s58, s44\ns_cbranch_scc1 sx_x3\ns_memrealtime s[58:59]\n"
"s_waitcnt lgkmcnt(4)\ns_cmp_ge_u32 s54, s44\ns_cbranch_scc1 sx_x3\ns_memrealtime s[54:55]\n"
"s_waitcnt lgkmcnt(4)\ns_cmp_ge_u32 s56, s44\ns_cbranch_scc1 sx_x3\ns_memrealtime s[56:57]\n"
"s_waitcnt lgkmcnt(4)\ns_cmp_ge_u32 s58, s44\ns_cbranch_scc1 sx_x3\ns_memrealtime s[58:59]\n"
"s_waitcnt lgkmcnt(4)\ns_cmp_ge_u32 s54, s44\ns_cbranch_scc1 sx_x3\ns_memrealtime s[54:55]\n"
"s_waitcnt lgkmcnt(4)\ns_cmp_ge_u32 s56, s44\ns_cbranch_scc1 sx_x3\ns_memrealtime s[56:57]\n"
"s_waitcnt lgkmcnt(4)\ns_cmp_ge_u32 s58, s44\ns_cbranch_scc1 sx_x3\ns_memrealtime s[58:59]\n"
"s_waitcnt lgkmcnt(4)\ns_cmp_ge_u32 s54, s44\ns_cbranch_scc1 sx_x3\ns_memrealtime s[54:55]\n"
"s_waitcnt lgkmcnt(4)\ns_cmp_ge_u32 s56, s44\ns_cbranch_scc1 sx_x3\ns_memrealtime s[56:57]\n"
"s_waitcnt lgkmcnt(4)\ns_cmp_ge_u32 s58, s44\ns_cbranch_scc1 sx_x3\ns_memrealtime s[58:59]\n"
"s_waitcnt lgkmcnt(4)\ns_cmp_ge_u32 s54, s44\ns_cbranch_scc1 sx_x3\ns_memrealtime s[54:55]\n"
"s_waitcnt lgkmcnt(4)\ns_cmp_ge_u32 s56, s44\ns_cbranch_scc1 sx_x3\ns_memrealtime s[56:57]\n"
"s_waitcnt lgkmcnt(4)\ns_cmp_ge_u32 s58, s44\ns_cbranch_scc1 sx_x3\ns_memrealtime s[58:59]\n"
"s_waitcnt lgkmcnt(4)\ns_cmp_ge_u32 s54, s44\ns_cbranch_scc1 sx_x3\ns_memrealtime s[54:55]\n"
"s_waitcnt lgkmcnt(4)\ns_cmp_ge_u32 s56, s44\ns_cbranch_scc1 sx_x3\ns_memrealtime s[56:57]\n"
"s_branch sx_x3\nso_x3:\ns_waitcnt lgkmcnt(0)\ns_cmp_lt_i32 s47, 0\n"
"s_cbranch_scc1 sf_x3\ns_branch sgo_x3\nsx_x3:\ns_setpc_b64 s[60:61]\n"
"s_nop 0\ns_nop 0\ns_nop 0\ns_nop 0\n"
"s_nop 0\ns_nop 0\ns_nop 0\ns_nop 0\n"
"s_nop 0\ns_nop 0\ns_nop 0\ns_nop 0\n"
"s_nop 0\ns_nop 0\ns_nop 0\ns_nop 0\n"
"s_nop 0\ns_nop 0\ns_nop 0\ns_nop 0\n"
"s_nop 0\ns_nop 0\ns_nop 0\ns_nop 0\n"
"sgo_x3:\ns_nop 15\ns_nop 1\nbuffer_load_dwordx4 v[40:43], v10, s[8:11], s43 offen offset:0 sc1\n"
"v_mov_b32 v75, 0\ns_nop 1\nv_mov_b32 v75, 0\ns_nop 1\n"
"v_mov_b32 v75, 0\ns_nop 1\nv_mov_b32 v75, 0\ns_nop 1\n"
"v_mov_b32 v75, 0\ns_nop 1\nv_mov_b32 v75, 0\ns_nop 1\n"
"v_mov_b32 v75, 0\ns_nop 1\nv_mov_b32 v75, 0\ns_nop 1\n"
"v_mov_b32 v75, 0\ns_nop 1\nv_mov_b32 v75, 0\ns_nop 1\n"
"v_mov_b32 v75, 0\ns_nop 1\nv_mov_b32 v75, 0\ns_nop 1\n"
"v_mov_b32 v75, 0\ns_nop 1\nv_mov_b32 v75, 0\ns_nop 1\n"
"v_mov_b32 v75, 0\ns_nop 1\nv_mov_b32 v75, 0\ns_nop 1\n"
"v_mov_b32 v75, 0\ns_nop 1\nv_mov_b32 v75, 0\ns_nop 1\n"
"v_mov_b32 v75, 0\ns_nop 1\nv_mov_b32 v75, 0\ns_nop 1\n"
"v_mov_b32 v75, 0\ns_nop 1\nv_mov_b32 v75, 0\ns_nop 1\n"
"v_mov_b32 v75, 0\ns_nop 1\nv_mov_b32 v75, 0\ns_nop 1\n"
"v_mov_b32 v75, 0\ns_nop 1\nv_mov_b32 v75, 0\ns_nop 1\n"
"v_mov_b32 v75, 0\ns_nop 1\nv_mov_b32 v75, 0\ns_nop 1\n"
"v_mov_b32 v75, 0\ns_nop 1\nv_mov_b32 v75, 0\ns_nop 1\n"
"v_mov_b32 v75, 0\ns_nop 1\nv_mov_b32 v75, 0\ns_nop 0\n"
"landed_x3:\ns_waitcnt vmcnt(0)\nv_smfmac_f32_16x16x64_bf16 v[44:47], v[40:43], a[0:7], v8\nv_max3_u32 v74, v74, v40, v41\n"
"v_smfmac_f32_16x16x64_bf16 v[48:51], v[40:43], a[16:23], v8\nv_max3_u32 v74, v74, v42, v43\nv_cmp_eq_u32 vcc, -1, v74\ns_nop 0\n"
"v_add_f32 v52, v44, v45\nv_add_f32 v53, v46, v47\ns_nop 1\nv_add_f32 v54, v48, v49\n"
"v_add_f32 v55, v50, v51\nds_write2_b32 v77, v52, v53 offset0:0 offset1:40\nds_write2_b32 v77, v54, v55 offset0:16 offset1:56\ns_cbranch_vccnz retry_x3\n"
"v_mov_b32 v75, 0\ns_nop 1\nv_mov_b32 v75, 0\ns_nop 1\n"
"v_mov_b32 v75, 0\ns_waitcnt lgkmcnt(0)\ns_barrier\nds_read_b64 v[64:65], v78 offset:0\n"
"ds_read_b64 v[66:67], v78 offset:1280\nds_read_b64 v[68:69], v78 offset:2560\nds_read_b64 v[70:71], v78 offset:3840\nv_mov_b32 v75, 0\n"
"s_nop 1\nv_mov_b32 v75, 0\ns_nop 1\nv_mov_b32 v75, 0\n"
"s_nop 1\ns_waitcnt lgkmcnt(2)\nv_pk_add_f32 v[64:65], v[64:65], v[66:67]\ns_waitcnt lgkmcnt(0)\n"
"v_pk_add_f32 v[68:69], v[68:69], v[70:71]\nv_pk_add_f32 v[64:65], v[64:65], v[68:69]\nv_exp_f32 v75, v64\nv_exp_f32 v76, v65\n"
"s_nop 0\nv_cvt_pk_bf16_f32 v75, v75, v76\nbuffer_store_dword v75, v16, s[8:11], s41 offen sc1\ns_barrier\n"
"buffer_load_dword v81, v18, s[8:11], 0 offen sc1\nbuffer_load_dwordx4 a[0:3], v9, s[16:19], s50 offen offset:0 sc1\ns_nop 15\ns_nop 7\n"
"buffer_load_dwordx4 a[4:7], v9, s[16:19], s50 offen offset:1024 sc1\ns_nop 15\ns_nop 7\nbuffer_load_dwordx4 a[16:19], v9, s[16:19], s51 offen offset:0 sc1\n"
"s_nop 15\ns_nop 7\nbuffer_load_dwordx4 a[20:23], v9, s[16:19], s51 offen offset:1024 sc1\ns_nop 15\n"
"s_nop 7\ns_add_u32 s50, s50, 28672\ns_add_u32 s51, s51, 28672\nbuffer_store_dwordx2 v[64:65], v17, s[8:11], s41 offen sc1\n"
"buffer_store_dwordx4 v[60:63], v15, s[8:11], s42 offen sc1\nds_read_b32 v79, v80\nv_xor_b32 v77, 8192, v77\nv_xor_b32 v78, 8192, v78\n"
"s_waitcnt lgkmcnt(0)\nv_readfirstlane_b32 s47, v79\ns_sub_u32 s47, s47, s40\ns_cmp_eq_u32 s47, 1\n"
"s_cbranch_scc1 adopt_x3\nback_x3:\ns_waitcnt vmcnt(6)\nfl_x3:\n"
"s_add_u32 s47, s40, 4\ns_min_u32 s47, s47, 64\ns_add_u32 s47, s47, s33\nv_subrev_u32 v81, s47, v81\n"
"v_cmp_lt_u32 vcc, 64, v81\ns_cbranch_vccz flok_x3\ns_add_u32 s53, s53, 0\ns_add_u32 s49, s49, 1\n"
"s_cmp_gt_u32 s49, 65536\ns_cbranch_scc1 end_x3\ns_sleep 2\nbuffer_load_dword v81, v18, s[8:11], 0 offen sc1\n"
"s_waitcnt vmcnt(0)\ns_branch fl_x3\nflok_x3:\ns_add_u32 s40, s40, 1\n"
"s_mov_b32 s43, s41\ns_add_u32 s41, s41, 131072\ns_and_b32 s41, s41, 524287\ns_xor_b32 s42, s41, 262144\n"
"s_and_b32 s47, s46, 15\ns_mul_i32 s48, s47, 4\ns_add_u32 s45, s45, s48\ns_add_u32 s45, s45, s30\n"
"s_lshr_b32 s48, s45, 4\ns_lshl_b32 s48, s48, 2\ns_add_u32 s44, s44, s48\ns_and_b32 s45, s45, 15\n"
"s_add_u32 s92, s92, 1\ns_lshr_b32 s48, s46, 4\ns_cmp_gt_u32 s47, s48\ns_cselect_b32 s47, 1, 0\n"
"s_and_b32 s48, s92, 1\ns_cselect_b32 s47, 0, s47\ns_sub_u32 s46, s46, s47\ns_waitcnt lgkmcnt(0)\n"
"s_cmp_lt_u32 s40, 63\ns_cbranch_scc1 top_x3\ns_branch end_x3\nretry2_x3:\n"
"s_sleep 3\nretry_x3:\ns_add_u32 s53, s53, 1\ns_add_u32 s49, s49, 1\n"
"s_cmp_gt_u32 s49, 65536\ns_cbranch_scc1 end_x3\nbuffer_load_dwordx4 v[40:43], v10, s[8:11], s43 offen offset:0 sc1\nv_mov_b32 v74, 0\n"
"s_add_u32 s47, s40, 1\nv_mov_b32 v79, s47\nds_write_b32 v80, v79\ns_waitcnt vmcnt(0)\n"
"v_max3_u32 v74, v74, v40, v41\nv_max3_u32 v74, v74, v42, v43\nv_cmp_eq_u32 vcc, -1, v74\nv_mov_b32 v44, 0\n"
"v_mov_b32 v45, 0\nv_mov_b32 v46, 0\nv_mov_b32 v47, 0\nv_mov_b32 v48, 0\n"
"v_mov_b32 v49, 0\nv_mov_b32 v50, 0\nv_mov_b32 v51, 0\ns_cbranch_vccnz retry2_x3\n"
"s_branch landed_x3\nadopt_x3:\nbuffer_load_dwordx4 v[56:59], v28, s[8:11], 0 offen sc1\ns_lshl_b32 s52, s40, 26\n"
"s_or_b32 s47, s52, 0x3ffffff\ns_waitcnt vmcnt(0)\nv_cmp_ge_u32 vcc, s47, v56\ns_nop 1\n"
"v_cndmask_b32 v56, 0, v56, vcc\nv_cmp_ge_u32 vcc, s47, v57\ns_nop 1\nv_cndmask_b32 v57, 0, v57, vcc\n"
"v_cmp_ge_u32 vcc, s47, v58\ns_nop 1\nv_cndmask_b32 v58, 0, v58, vcc\nv_cmp_ge_u32 vcc, s47, v59\n"
"s_nop 1\nv_cndmask_b32 v59, 0, v59, vcc\nv_max3_u32 v79, v56, v57, v58\nv_max_u32 v79, v79, v59\n"
"s_nop 1\nv_max_u32_dpp v79, v79, v79 row_ror:8 row_mask:0xf bank_mask:0xf\ns_nop 1\nv_max_u32_dpp v79, v79, v79 row_ror:4 row_mask:0xf bank_mask:0xf\n"
"s_nop 1\nv_max_u32_dpp v79, v79, v79 row_ror:2 row_mask:0xf bank_mask:0xf\ns_nop 1\nv_max_u32_dpp v79, v79, v79 row_ror:1 row_mask:0xf bank_mask:0xf\n"
"s_nop 0\nv_readlane_b32 s47, v79, 0\nv_readlane_b32 s48, v79, 16\ns_max_u32 s48, s48, s47\n"
"s_cmp_le_u32 s48, s52\ns_cbranch_scc1 back_x3\ns_bfe_u32 s52, s48, 1179656\ns_add_u32 s44, s52, s28\n"
"s_mov_b32 s45, 0\ns_and_b32 s46, s48, 255\ns_mov_b32 s92, 0\ns_branch back_x3\n"
"end_x3:\ns_branch fk_done\nfk_p0:\ns_mov_b32 s40, 0\n"
"s_mov_b32 s41, 0\ns_mov_b32 s42, 262144\ns_mov_b32 s43, 393216\ns_mov_b32 s44, s28\n"
"s_mov_b32 s45, 0\ns_mov_b32 s46, 0\ns_mov_b32 s92, 0\ns_mov_b32 s49, 0\n"
"s_mov_b32 s53, 0\ns_mov_b32 s50, s35\ns_mov_b32 s51, s36\nv_mov_b32 v222, 24576\n"
"s_mov_b32 s62, 0x00ff00ff\ns_mov_b32 s63, 0x00ff00ff\nv_cndmask_b32 v223, 0, -1, s[62:63]\nv_not_b32 v224, v223\n"
"v_bfe_u32 v219, v9, 4, 1\nv_lshlrev_b32 v220, 4, v219\nv_mov_b32 v218, 0x3c00\nv_lshlrev_b32 v218, v220, v218\n"
"v_bfe_u32 v220, v9, 5, 3\nv_cmp_eq_u32 vcc, 0, v220\ns_nop 1\nv_cndmask_b32 v168, 0, v218, vcc\n"
"v_cmp_eq_u32 vcc, 2, v220\ns_nop 1\nv_cndmask_b32 v169, 0, v218, vcc\nv_cmp_eq_u32 vcc, 4, v220\n"
"s_nop 1\nv_cndmask_b32 v170, 0, v218, vcc\nv_cmp_eq_u32 vcc, 6, v220\ns_nop 1\n"
"v_cndmask_b32 v171, 0, v218, vcc\nv_cmp_eq_u32 vcc, 1, v220\ns_nop 1\nv_cndmask_b32 v172, 0, v218, vcc\n"
"v_cmp_eq_u32 vcc, 3, v220\ns_nop 1\nv_cndmask_b32 v173, 0, v218, vcc\nv_cmp_eq_u32 vcc, 5, v220\n"
"s_nop 1\nv_cndmask_b32 v174, 0, v218, vcc\nv_cmp_eq_u32 vcc, 7, v220\ns_nop 1\n"
"v_cndmask_b32 v175, 0, v218, vcc\nv_mov_b32 v218, 0x3f803f80\nv_cmp_eq_u32 vcc, 0, v219\ns_nop 1\n"
"v_cndmask_b32 v176, 0, v218, vcc\nv_cndmask_b32 v177, 0, v218, vcc\nv_cndmask_b32 v178, 0, v218, vcc\nv_cndmask_b32 v179, 0, v218, vcc\n"
"v_cndmask_b32 v214, 0, v218, vcc\nv_cmp_eq_u32 vcc, 1, v219\ns_nop 1\nv_cndmask_b32 v180, 0, v218, vcc\n"
"v_cndmask_b32 v181, 0, v218, vcc\nv_cndmask_b32 v182, 0, v218, vcc\nv_cndmask_b32 v183, 0, v218, vcc\nv_cndmask_b32 v215, 0, v218, vcc\n"
"s_add_u32 s47, s34, 0\nbuffer_load_dwordx4 a[0:3], v9, s[12:15], s47 offen sc1\nbuffer_load_dwordx4 a[4:7], v9, s[12:15], s47 offen offset:1024 sc1\ns_add_u32 s47, s34, 2048\n"
"buffer_load_dwordx4 a[8:11], v9, s[12:15], s47 offen sc1\nbuffer_load_dwordx4 a[12:15], v9, s[12:15], s47 offen offset:1024 sc1\ns_add_u32 s47, s34, 4096\nbuffer_load_dwordx4 a[16:19], v9, s[12:15], s47 offen sc1\n"
"buffer_load_dwordx4 a[20:23], v9, s[12:15], s47 offen offset:1024 sc1\ns_add_u32 s47, s34, 6144\nbuffer_load_dwordx4 a[24:27], v9, s[12:15], s47 offen sc1\nbuffer_load_dwordx4 a[28:31], v9, s[12:15], s47 offen offset:1024 sc1\n"
"s_add_u32 s47, s34, 8192\nbuffer_load_dwordx4 a[32:35], v9, s[12:15], s47 offen sc1\nbuffer_load_dwordx4 a[36:39], v9, s[12:15], s47 offen offset:1024 sc1\ns_add_u32 s47, s34, 10240\n"
"buffer_load_dwordx4 a[40:43], v9, s[12:15], s47 offen sc1\nbuffer_load_dwordx4 a[44:47], v9, s[12:15], s47 offen offset:1024 sc1\ns_add_u32 s47, s34, 12288\nbuffer_load_dwordx4 a[48:51], v9, s[12:15], s47 offen sc1\n"
"buffer_load_dwordx4 a[52:55], v9, s[12:15], s47 offen offset:1024 sc1\ns_waitcnt vmcnt(14)\ns_add_u32 s47, s34, 14336\nbuffer_load_dwordx4 a[56:59], v9, s[12:15], s47 offen sc1\n"
"buffer_load_dwordx4 a[60:63], v9, s[12:15], s47 offen offset:1024 sc1\ns_add_u32 s47, s34, 16384\nbuffer_load_dwordx4 a[64:67], v9, s[12:15], s47 offen sc1\nbuffer_load_dwordx4 a[68:71], v9, s[12:15], s47 offen offset:1024 sc1\n"
"s_add_u32 s47, s34, 18432\nbuffer_load_dwordx4 a[72:75], v9, s[12:15], s47 offen sc1\nbuffer_load_dwordx4 a[76:79], v9, s[12:15], s47 offen offset:1024 sc1\ns_add_u32 s47, s34, 20480\n"
"buffer_load_dwordx4 a[80:83], v9, s[12:15], s47 offen sc1\nbuffer_load_dwordx4 a[84:87], v9, s[12:15], s47 offen offset:1024 sc1\ns_add_u32 s47, s34, 22528\nbuffer_load_dwordx4 a[88:91], v9, s[12:15], s47 offen sc1\n"
"buffer_load_dwordx4 a[92:95], v9, s[12:15], s47 offen offset:1024 sc1\ns_add_u32 s47, s34, 24576\nbuffer_load_dwordx4 a[96:99], v9, s[12:15], s47 offen sc1\nbuffer_load_dwordx4 a[100:103], v9, s[12:15], s47 offen offset:1024 sc1\n"
"s_add_u32 s47, s34, 26624\nbuffer_load_dwordx4 a[104:107], v9, s[12:15], s47 offen sc1\nbuffer_load_dwordx4 a[108:111], v9, s[12:15], s47 offen offset:1024 sc1\ns_waitcnt vmcnt(14)\n"
"s_add_u32 s47, s34, 28672\nbuffer_load_dwordx4 a[112:115], v9, s[12:15], s47 offen sc1\nbuffer_load_dwordx4 a[116:119], v9, s[12:15], s47 offen offset:1024 sc1\ns_add_u32 s47, s34, 30720\n"
"buffer_load_dwordx4 a[120:123], v9, s[12:15], s47 offen sc1\nbuffer_load_dwordx4 a[124:127], v9, s[12:15], s47 offen offset:1024 sc1\ns_add_u32 s47, s34, 32768\nbuffer_load_dwordx4 a[128:131], v9, s[12:15], s47 offen sc1\n"
"buffer_load_dwordx4 a[132:135], v9, s[12:15], s47 offen offset:1024 sc1\ns_add_u32 s47, s34, 34816\nbuffer_load_dwordx4 a[136:139], v9, s[12:15], s47 offen sc1\nbuffer_load_dwordx4 a[140:143], v9, s[12:15], s47 offen offset:1024 sc1\n"
"s_add_u32 s47, s34, 36864\nbuffer_load_dwordx4 a[144:147], v9, s[12:15], s47 offen sc1\nbuffer_load_dwordx4 a[148:151], v9, s[12:15], s47 offen offset:1024 sc1\ns_add_u32 s47, s34, 38912\n"
"buffer_load_dwordx4 a[152:155], v9, s[12:15], s47 offen sc1\nbuffer_load_dwordx4 a[156:159], v9, s[12:15], s47 offen offset:1024 sc1\ns_add_u32 s47, s34, 40960\nbuffer_load_dwordx4 a[160:163], v9, s[12:15], s47 offen sc1\n"
"buffer_load_dwordx4 a[164:167], v9, s[12:15], s47 offen offset:1024 sc1\ns_waitcnt vmcnt(14)\ns_add_u32 s47, s34, 43008\nbuffer_load_dwordx4 a[168:171], v9, s[12:15], s47 offen sc1\n"
"buffer_load_dwordx4 a[172:175], v9, s[12:15], s47 offen offset:1024 sc1\ns_add_u32 s47, s34, 45056\nbuffer_load_dwordx4 a[176:179], v9, s[12:15], s47 offen sc1\nbuffer_load_dwordx4 a[180:183], v9, s[12:15], s47 offen offset:1024 sc1\n"
"s_add_u32 s47, s34, 47104\nbuffer_load_dwordx4 a[184:187], v9, s[12:15], s47 offen sc1\nbuffer_load_dwordx4 a[188:191], v9, s[12:15], s47 offen offset:1024 sc1\ns_add_u32 s47, s34, 49152\n"
"buffer_load_dwordx4 a[192:195], v9, s[12:15], s47 offen sc1\nbuffer_load_dwordx4 a[196:199], v9, s[12:15], s47 offen offset:1024 sc1\ns_add_u32 s47, s34, 51200\nbuffer_load_dwordx4 a[200:203], v9, s[12:15], s47 offen sc1\n"
"buffer_load_dwordx4 a[204:207], v9, s[12:15], s47 offen offset:1024 sc1\ns_add_u32 s47, s34, 53248\nbuffer_load_dwordx4 a[208:211], v9, s[12:15], s47 offen sc1\nbuffer_load_dwordx4 a[212:215], v9, s[12:15], s47 offen offset:1024 sc1\n"
"s_add_u32 s47, s34, 55296\nbuffer_load_dwordx4 a[216:219], v9, s[12:15], s47 offen sc1\nbuffer_load_dwordx4 a[220:223], v9, s[12:15], s47 offen offset:1024 sc1\ns_waitcnt vmcnt(14)\n"
"buffer_load_dword v218, v11, s[8:11], 0 offen sc1\ns_waitcnt vmcnt(0)\nfl_p0:\ns_add_u32 s47, s40, 2\n"
"s_min_u32 s47, s47, 64\ns_add_u32 s47, s47, s33\nv_subrev_u32 v218, s47, v218\nv_cmp_lt_u32 vcc, 64, v218\n"
"s_cbranch_vccz flok_p0\ns_add_u32 s53, s53, 0\ns_add_u32 s49, s49, 1\ns_cmp_gt_u32 s49, 65536\n"
"s_cbranch_scc1 end_p0\ns_sleep 2\nbuffer_load_dword v218, v11, s[8:11], 0 offen sc1\ns_waitcnt vmcnt(0)\n"
"s_branch fl_p0\nflok_p0:\nbuffer_load_dwordx4 v[100:103], v9, s[16:19], s50 offen offset:0 sc1\nbuffer_load_dwordx4 v[104:107], v9, s[16:19], s50 offen offset:1024 sc1\n"
"buffer_load_dwordx4 v[108:111], v9, s[16:19], s50 offen offset:2048 sc1\nbuffer_load_dwordx4 v[112:115], v9, s[16:19], s50 offen offset:3072 sc1\ns_add_u32 s50, s50, 16384\ns_waitcnt vmcnt(0)\n"
"s_barrier\n.p2align 6\ntop_p0:\nv_mov_b32 v68, 0\n"
"v_mov_b32 v69, 0\nv_mov_b32 v70, 0\nv_mov_b32 v71, 0\nv_mov_b32 v72, 0\n"
"v_mov_b32 v73, 0\nv_mov_b32 v74, 0\nv_mov_b32 v75, 0\nv_mov_b32 v76, 0\n"
"v_mov_b32 v77, 0\nv_mov_b32 v78, 0\nv_mov_b32 v79, 0\nv_mov_b32 v80, 0\n"
"v_mov_b32 v81, 0\nv_mov_b32 v82, 0\nv_mov_b32 v83, 0\nv_mov_b32 v218, 0\n"
"s_add_u32 s52, s44, s37\nm_p0:\ns_memrealtime s[54:55]\ns_waitcnt lgkmcnt(0)\n"
"s_sub_u32 s47, s54, s52\ns_cmp_lt_i32 s47, 0\ns_cbranch_scc1 m_p0\ntop2_p0:\n"
"buffer_load_dwordx4 v[40:43], v10, s[8:11], s43 offen offset:0 sc1\nbuffer_load_dwordx4 v[44:47], v10, s[8:11], s43 offen offset:1024 sc1\ns_waitcnt vmcnt(1)\nds_write_b128 v9, v[40:43] offset:0\n"
"v_max3_u32 v218, v218, v40, v41\nv_max3_u32 v218, v218, v42, v43\ns_waitcnt vmcnt(0)\nds_write_b128 v9, v[44:47] offset:1024\n"
"v_max3_u32 v218, v218, v44, v45\nv_max3_u32 v218, v218, v46, v47\nv_cmp_eq_u32 vcc, -1, v218\nv_smfmac_f32_16x16x64_bf16 v[68:71], v[40:43], a[0:7], v8\n"
"v_smfmac_f32_16x16x64_bf16 v[72:75], v[40:43], a[56:63], v8\ns_cbranch_vccnz retry1_p0\ns_waitcnt lgkmcnt(0)\ns_barrier\n"
"ds_read_b32 v221, v222\nds_read_b128 v[48:51], v9 offset:2048\nds_read_b128 v[52:55], v9 offset:3072\nds_read_b128 v[56:59], v9 offset:4096\n"
"ds_read_b128 v[60:63], v9 offset:5120\nds_read_b128 v[64:67], v9 offset:6144\nv_smfmac_f32_16x16x64_bf16 v[76:79], v[40:43], a[112:119], v8\nv_smfmac_f32_16x16x64_bf16 v[80:83], v[40:43], a[168:175], v8\n"
"v_smfmac_f32_16x16x64_bf16 v[68:71], v[44:47], a[8:15], v8\nv_smfmac_f32_16x16x64_bf16 v[72:75], v[44:47], a[64:71], v8\nv_smfmac_f32_16x16x64_bf16 v[76:79], v[44:47], a[120:127], v8\nv_smfmac_f32_16x16x64_bf16 v[80:83], v[44:47], a[176:183], v8\n"
"s_waitcnt lgkmcnt(0)\nv_readfirstlane_b32 s47, v221\ns_sub_u32 s47, s47, s40\ns_cmp_eq_u32 s47, 1\n"
"s_cbranch_scc1 adopt_p0\nback_p0:\ns_cmp_eq_u32 s40, 63\ns_cbranch_scc1 last_p0\n"
"s_and_b32 s47, s46, 15\ns_mul_i32 s47, s47, 2\ns_add_u32 s47, s47, s29\ns_add_u32 s47, s47, s45\n"
"s_and_b32 s91, s47, 15\ns_lshr_b32 s47, s47, 4\ns_lshl_b32 s47, s47, 2\ns_add_u32 s90, s44, s47\n"
"s_mul_i32 s48, s91, 6\ns_and_b32 s48, s48, -4\ns_getpc_b64 s[60:61]\nspc_p0:\n"
"s_sub_u32 s48, sgo_p0-spc_p0, s48\ns_add_u32 s60, s60, s48\ns_addc_u32 s61, s61, 0\nsf_p0:\n"
"s_memrealtime s[54:55]\ns_nop 2\ns_memrealtime s[56:57]\ns_nop 2\n"
"s_memrealtime s[58:59]\ns_waitcnt lgkmcnt(4)\ns_sub_u32 s47, s54, s90\ns_add_u32 s48, s47, 12\n"
"s_cmp_ge_u32 s48, 12\ns_cbranch_scc1 so_p0\ns_memrealtime s[54:55]\ns_waitcnt lgkmcnt(4)\n"
"s_cmp_ge_u32 s56, s90\ns_cbranch_scc1 sx_p0\ns_memrealtime s[56:57]\ns_waitcnt lgkmcnt(4)\n"
"s_cmp_ge_u32 s58, s90\ns_cbranch_scc1 sx_p0\ns_memrealtime s[58:59]\ns_waitcnt lgkmcnt(4)\n"
"s_cmp_ge_u32 s54, s90\ns_cbranch_scc1 sx_p0\ns_memrealtime s[54:55]\ns_waitcnt lgkmcnt(4)\n"
"s_cmp_ge_u32 s56, s90\ns_cbranch_scc1 sx_p0\ns_memrealtime s[56:57]\ns_waitcnt lgkmcnt(4)\n"
"s_cmp_ge_u32 s58, s90\ns_cbranch_scc1 sx_p0\ns_memrealtime s[58:59]\ns_waitcnt lgkmcnt(4)\n"
"s_cmp_ge_u32 s54, s90\ns_cbranch_scc1 sx_p0\ns_memrealtime s[54:55]\ns_waitcnt lgkmcnt(4)\n"
"s_cmp_ge_u32 s56, s90\ns_cbranch_scc1 sx_p0\ns_memrealtime s[56:57]\ns_waitcnt lgkmcnt(4)\n"
"s_cmp_ge_u32 s58, s90\ns_cbranch_scc1 sx_p0\ns_memrealtime s[58:59]\ns_waitcnt lgkmcnt(4)\n"
"s_cmp_ge_u32 s54, s90\ns_cbranch_scc1 sx_p0\ns_memrealtime s[54:55]\ns_waitcnt lgkmcnt(4)\n"
"s_cmp_ge_u32 s56, s90\ns_cbranch_scc1 sx_p0\ns_memrealtime s[56:57]\ns_waitcnt lgkmcnt(4)\n"
"s_cmp_ge_u32 s58, s90\ns_cbranch_scc1 sx_p0\ns_memrealtime s[58:59]\ns_waitcnt lgkmcnt(4)\n"
"s_cmp_ge_u32 s54, s90\ns_cbranch_scc1 sx_p0\ns_memrealtime s[54:55]\ns_waitcnt lgkmcnt(4)\n"
"s_cmp_ge_u32 s56, s90\ns_cbranch_scc1 sx_p0\ns_memrealtime s[56:57]\ns_waitcnt lgkmcnt(4)\n"
"s_cmp_ge_u32 s58, s90\ns_cbranch_scc1 sx_p0\ns_memrealtime s[58:59]\ns_waitcnt lgkmcnt(4)\n"
"s_cmp_ge_u32 s54, s90\ns_cbranch_scc1 sx_p0\ns_memrealtime s[54:55]\ns_waitcnt lgkmcnt(4)\n"
"s_cmp_ge_u32 s56, s90\ns_cbranch_scc1 sx_p0\ns_memrealtime s[56:57]\ns_branch sx_p0\n"
"so_p0:\ns_waitcnt lgkmcnt(0)\ns_cmp_lt_i32 s47, 0\ns_cbranch_scc1 sf_p0\n"
"s_cmp_gt_i32 s47, s32\ns_cbranch_scc1 late_p0\ns_branch sgo_p0\nsx_p0:\n"
"s_setpc_b64 s[60:61]\ns_nop 0\ns_nop 0\ns_nop 0\n"
"s_nop 0\ns_nop 0\ns_nop 0\ns_nop 0\n"
"s_nop 0\ns_nop 0\ns_nop 0\ns_nop 0\n"
"s_nop 0\ns_nop 0\ns_nop 0\ns_nop 0\n"
"s_nop 0\ns_nop 0\ns_nop 0\ns_nop 0\n"
"s_nop 0\ns_nop 0\ns_nop 0\ns_nop 0\n"
"s_nop 0\nsgo_p0:\nbuffer_load_dwordx4 v[116:119], v20, s[8:11], s41 offen offset:0 sc1\nbuffer_load_dwordx4 v[120:123], v20, s[8:11], s41 offen offset:1024 sc1\n"
"v_smfmac_f32_16x16x64_bf16 v[68:71], v[48:51], a[16:23], v8\nv_mov_b32 v84, v100\nv_smfmac_f32_16x16x64_bf16 v[72:75], v[48:51], a[72:79], v8\nv_mov_b32 v85, v101\n"
"v_smfmac_f32_16x16x64_bf16 v[76:79], v[48:51], a[128:135], v8\nv_mov_b32 v86, v102\nv_smfmac_f32_16x16x64_bf16 v[80:83], v[48:51], a[184:191], v8\nv_mov_b32 v87, v103\n"
"v_smfmac_f32_16x16x64_bf16 v[68:71], v[52:55], a[24:31], v8\nv_mov_b32 v88, v104\nv_smfmac_f32_16x16x64_bf16 v[72:75], v[52:55], a[80:87], v8\nv_mov_b32 v89, v105\n"
"v_smfmac_f32_16x16x64_bf16 v[76:79], v[52:55], a[136:143], v8\nv_mov_b32 v90, v106\nv_smfmac_f32_16x16x64_bf16 v[80:83], v[52:55], a[192:199], v8\nv_mov_b32 v91, v107\n"
"v_smfmac_f32_16x16x64_bf16 v[68:71], v[56:59], a[32:39], v8\nv_mov_b32 v92, v108\nv_smfmac_f32_16x16x64_bf16 v[72:75], v[56:59], a[88:95], v8\nv_mov_b32 v93, v109\n"
"buffer_load_dwordx4 v[124:127], v21, s[8:11], s41 offen offset:0 sc1\nbuffer_load_dwordx4 v[128:131], v21, s[8:11], s41 offen offset:1024 sc1\nv_smfmac_f32_16x16x64_bf16 v[76:79], v[56:59], a[144:151], v8\nv_mov_b32 v94, v110\n"
"v_smfmac_f32_16x16x64_bf16 v[80:83], v[56:59], a[200:207], v8\nv_mov_b32 v95, v111\nv_smfmac_f32_16x16x64_bf16 v[68:71], v[60:63], a[40:47], v8\nv_mov_b32 v96, v112\n"
"v_smfmac_f32_16x16x64_bf16 v[72:75], v[60:63], a[96:103], v8\nv_mov_b32 v97, v113\nv_smfmac_f32_16x16x64_bf16 v[76:79], v[60:63], a[152:159], v8\nv_mov_b32 v98, v114\n"
"v_smfmac_f32_16x16x64_bf16 v[80:83], v[60:63], a[208:215], v8\nv_mov_b32 v99, v115\nv_smfmac_f32_16x16x64_bf16 v[68:71], v[64:67], a[48:55], v8\nv_smfmac_f32_16x16x64_bf16 v[72:75], v[64:67], a[104:111], v8\n"
"v_smfmac_f32_16x16x64_bf16 v[76:79], v[64:67], a[160:167], v8\nv_smfmac_f32_16x16x64_bf16 v[80:83], v[64:67], a[216:223], v8\nv_add_f32 v219, v68, v69\nv_add_f32 v220, v70, v71\n"
"v_cvt_pk_bf16_f32 v156, v219, v220\nv_add_f32 v219, v72, v73\nv_add_f32 v220, v74, v75\nv_cvt_pk_bf16_f32 v157, v219, v220\n"
"v_add_f32 v219, v76, v77\nv_add_f32 v220, v78, v79\nv_cvt_pk_bf16_f32 v158, v219, v220\nv_add_f32 v219, v80, v81\n"
"v_add_f32 v220, v82, v83\nv_cvt_pk_bf16_f32 v159, v219, v220\nbuffer_load_dwordx4 v[100:103], v9, s[16:19], s50 offen offset:0 sc1\nbuffer_load_dwordx4 v[104:107], v9, s[16:19], s50 offen offset:1024 sc1\n"
"buffer_load_dwordx4 v[108:111], v9, s[16:19], s50 offen offset:2048 sc1\nbuffer_load_dwordx4 v[112:115], v9, s[16:19], s50 offen offset:3072 sc1\ns_add_u32 s50, s50, 16384\nbuffer_store_dwordx4 v[156:159], v9, s[20:23], s51 offen sc1\n"
"s_waitcnt vmcnt(8)\nv_mfma_f32_16x16x32_bf16 v[132:135], v[84:87], v[116:119], 0\nv_mfma_f32_16x16x32_bf16 v[136:139], v[88:91], v[116:119], 0\nv_mfma_f32_16x16x32_bf16 v[148:151], v[176:179], v[116:119], 0\n"
"s_waitcnt vmcnt(7)\nv_mfma_f32_16x16x32_bf16 v[140:143], v[92:95], v[120:123], 0\nv_mfma_f32_16x16x32_bf16 v[144:147], v[96:99], v[120:123], 0\nv_mfma_f32_16x16x32_bf16 v[148:151], v[180:183], v[120:123], v[148:151]\n"
"v_cndmask_b32 v132, v136, v132, s[62:63]\nv_cndmask_b32 v133, v137, v133, s[62:63]\nv_cndmask_b32 v134, v138, v134, s[62:63]\nv_cndmask_b32 v135, v139, v135, s[62:63]\n"
"s_waitcnt vmcnt(6)\nv_mfma_f32_16x16x32_f16 v[152:155], v[168:171], v[124:127], 0\ns_waitcnt vmcnt(5)\nv_mfma_f32_16x16x32_f16 v[152:155], v[172:175], v[128:131], v[152:155]\n"
"v_cndmask_b32 v140, v144, v140, s[62:63]\nv_cndmask_b32 v141, v145, v141, s[62:63]\nv_cndmask_b32 v142, v146, v142, s[62:63]\nv_cndmask_b32 v143, v147, v143, s[62:63]\n"
"s_nop 2\nv_exp_f32 v152, v152\nv_exp_f32 v153, v153\nv_exp_f32 v154, v154\n"
"v_exp_f32 v155, v155\nv_cvt_pk_bf16_f32 v200, v152, v153\nv_cvt_pk_bf16_f32 v201, v154, v155\nv_and_b32 v160, v200, v223\n"
"v_and_b32 v161, v200, v224\nv_mfma_f32_16x16x16_bf16 v[148:151], v[214:215], v[200:201], v[148:151]\nv_and_b32 v162, v201, v223\nv_and_b32 v163, v201, v224\n"
"v_mfma_f32_16x16x16_bf16 v[132:135], v[156:157], v[160:161], v[132:135]\ns_nop 0\nv_mfma_f32_16x16x16_bf16 v[140:143], v[158:159], v[162:163], v[140:143]\nv_rcp_f32 v202, v148\n"
"v_rcp_f32 v203, v149\nv_mul_f32 v132, v132, v202\nv_mul_f32 v133, v133, v202\nv_mul_f32 v134, v134, v202\n"
"v_mul_f32 v135, v135, v202\nv_fmac_f32 v132, v140, v203\nv_fmac_f32 v133, v141, v203\nv_fmac_f32 v134, v142, v203\n"
"v_fmac_f32 v135, v143, v203\nv_add_f32_dpp v204, v132, v132 row_ror:8 row_mask:0xf bank_mask:0x3\nv_add_f32_dpp v205, v133, v133 row_ror:8 row_mask:0xf bank_mask:0x3\nv_add_f32_dpp v204, v134, v134 row_ror:8 row_mask:0xf bank_mask:0xc\n"
"v_add_f32_dpp v205, v135, v135 row_ror:8 row_mask:0xf bank_mask:0xc\nv_subrev_u32 v219, 0x2f800000, v148\nv_subrev_u32 v220, 0x2f800000, v149\nv_max_u32 v219, v219, v220\n"
"v_cmp_le_u32 vcc, 0x20000000, v219\nds_write_b64 v22, v[204:205]\ns_cbranch_vccnz slow_p0\njoin_p0:\n"
"v_mov_b32 v219, 0\ns_nop 1\nv_mov_b32 v219, 0\ns_waitcnt lgkmcnt(0)\n"
"s_barrier\nds_read_b64 v[206:207], v23 offset:0\nds_read_b64 v[208:209], v23 offset:1280\nds_read_b64 v[210:211], v23 offset:2560\n"
"ds_read_b64 v[212:213], v23 offset:3840\nds_read_b32 v221, v222\nv_mov_b32 v219, 0\ns_nop 1\n"
"v_mov_b32 v219, 0\ns_nop 1\nv_mov_b32 v219, 0\ns_waitcnt lgkmcnt(3)\n"
"v_pk_add_f32 v[206:207], v[206:207], v[208:209]\ns_waitcnt lgkmcnt(1)\nv_pk_add_f32 v[210:211], v[210:211], v[212:213]\nv_pk_add_f32 v[206:207], v[206:207], v[210:211]\n"
"v_cvt_pk_bf16_f32 v220, v206, v207\nbuffer_store_dword v220, v16, s[8:11], s41 offen sc1\ns_add_u32 s51, s51, 4096\ns_add_u32 s40, s40, 1\n"
"s_mov_b32 s43, s41\ns_add_u32 s41, s41, 131072\ns_and_b32 s41, s41, 524287\ns_xor_b32 s42, s41, 262144\n"
"s_and_b32 s47, s46, 15\ns_mul_i32 s48, s47, 4\ns_add_u32 s45, s45, s48\ns_add_u32 s45, s45, s30\n"
"s_lshr_b32 s48, s45, 4\ns_lshl_b32 s48, s48, 2\ns_add_u32 s44, s44, s48\ns_and_b32 s45, s45, 15\n"
"s_add_u32 s92, s92, 1\ns_lshr_b32 s48, s46, 4\ns_cmp_gt_u32 s47, s48\ns_cselect_b32 s47, 1, 0\n"
"s_and_b32 s48, s92, 1\ns_cselect_b32 s47, 0, s47\ns_sub_u32 s46, s46, s47\ns_waitcnt lgkmcnt(0)\n"
"v_readfirstlane_b32 s47, v221\ns_sub_u32 s47, s47, s40\ns_cmp_eq_u32 s47, 1\ns_cbranch_scc1 rec_p0\n"
"s_branch top_p0\nlast_p0:\nv_smfmac_f32_16x16x64_bf16 v[68:71], v[48:51], a[16:23], v8\nv_smfmac_f32_16x16x64_bf16 v[72:75], v[48:51], a[72:79], v8\n"
"v_smfmac_f32_16x16x64_bf16 v[76:79], v[48:51], a[128:135], v8\nv_smfmac_f32_16x16x64_bf16 v[80:83], v[48:51], a[184:191], v8\nv_smfmac_f32_16x16x64_bf16 v[68:71], v[52:55], a[24:31], v8\nv_smfmac_f32_16x16x64_bf16 v[72:75], v[52:55], a[80:87], v8\n"
"v_smfmac_f32_16x16x64_bf16 v[76:79], v[52:55], a[136:143], v8\nv_smfmac_f32_16x16x64_bf16 v[80:83], v[52:55], a[192:199], v8\nv_smfmac_f32_16x16x64_bf16 v[68:71], v[56:59], a[32:39], v8\nv_smfmac_f32_16x16x64_bf16 v[72:75], v[56:59], a[88:95], v8\n"
"v_smfmac_f32_16x16x64_bf16 v[76:79], v[56:59], a[144:151], v8\nv_smfmac_f32_16x16x64_bf16 v[80:83], v[56:59], a[200:207], v8\nv_smfmac_f32_16x16x64_bf16 v[68:71], v[60:63], a[40:47], v8\nv_smfmac_f32_16x16x64_bf16 v[72:75], v[60:63], a[96:103], v8\n"
"v_smfmac_f32_16x16x64_bf16 v[76:79], v[60:63], a[152:159], v8\nv_smfmac_f32_16x16x64_bf16 v[80:83], v[60:63], a[208:215], v8\nv_smfmac_f32_16x16x64_bf16 v[68:71], v[64:67], a[48:55], v8\nv_smfmac_f32_16x16x64_bf16 v[72:75], v[64:67], a[104:111], v8\n"
"v_smfmac_f32_16x16x64_bf16 v[76:79], v[64:67], a[160:167], v8\nv_smfmac_f32_16x16x64_bf16 v[80:83], v[64:67], a[216:223], v8\nv_add_f32 v219, v68, v69\nv_add_f32 v220, v70, v71\n"
"v_cvt_pk_bf16_f32 v156, v219, v220\nv_add_f32 v219, v72, v73\nv_add_f32 v220, v74, v75\nv_cvt_pk_bf16_f32 v157, v219, v220\n"
"v_add_f32 v219, v76, v77\nv_add_f32 v220, v78, v79\nv_cvt_pk_bf16_f32 v158, v219, v220\nv_add_f32 v219, v80, v81\n"
"v_add_f32 v220, v82, v83\nv_cvt_pk_bf16_f32 v159, v219, v220\nbuffer_store_dwordx4 v[156:159], v9, s[20:23], s51 offen sc1\ns_add_u32 s47, s33, 65\n"
"v_mov_b32 v219, s47\ns_waitcnt vmcnt(0)\nbuffer_store_dword v219, v18, s[8:11], 0 offen sc1\nv_mov_b32 v220, s33\n"
"buffer_store_dword v220, v15, s[8:11], 0 offen sc1\ns_branch end_p0\nretry1_p0:\ns_add_u32 s53, s53, 256\n"
"s_add_u32 s49, s49, 1\ns_cmp_gt_u32 s49, 65536\ns_cbranch_scc1 end_p0\ns_add_u32 s47, s40, 1\n"
"v_mov_b32 v221, s47\nds_write_b32 v222, v221\nrec_p0:\ns_sleep 4\n"
"v_mov_b32 v68, 0\nv_mov_b32 v69, 0\nv_mov_b32 v70, 0\nv_mov_b32 v71, 0\n"
"v_mov_b32 v72, 0\nv_mov_b32 v73, 0\nv_mov_b32 v74, 0\nv_mov_b32 v75, 0\n"
"v_mov_b32 v76, 0\nv_mov_b32 v77, 0\nv_mov_b32 v78, 0\nv_mov_b32 v79, 0\n"
"v_mov_b32 v80, 0\nv_mov_b32 v81, 0\nv_mov_b32 v82, 0\nv_mov_b32 v83, 0\n"
"v_mov_b32 v218, 0\ns_branch top2_p0\nadopt_p0:\nbuffer_load_dwordx4 v[164:167], v28, s[8:11], 0 offen sc1\n"
"s_lshl_b32 s52, s40, 26\ns_or_b32 s47, s52, 0x3ffffff\ns_waitcnt vmcnt(0)\nv_cmp_ge_u32 vcc, s47, v164\n"
"s_nop 1\nv_cndmask_b32 v164, 0, v164, vcc\nv_cmp_ge_u32 vcc, s47, v165\ns_nop 1\n"
"v_cndmask_b32 v165, 0, v165, vcc\nv_cmp_ge_u32 vcc, s47, v166\ns_nop 1\nv_cndmask_b32 v166, 0, v166, vcc\n"
"v_cmp_ge_u32 vcc, s47, v167\ns_nop 1\nv_cndmask_b32 v167, 0, v167, vcc\nv_max3_u32 v221, v164, v165, v166\n"
"v_max_u32 v221, v221, v167\ns_nop 1\nv_max_u32_dpp v221, v221, v221 row_ror:8 row_mask:0xf bank_mask:0xf\ns_nop 1\n"
"v_max_u32_dpp v221, v221, v221 row_ror:4 row_mask:0xf bank_mask:0xf\ns_nop 1\nv_max_u32_dpp v221, v221, v221 row_ror:2 row_mask:0xf bank_mask:0xf\ns_nop 1\n"
"v_max_u32_dpp v221, v221, v221 row_ror:1 row_mask:0xf bank_mask:0xf\ns_nop 0\nv_readlane_b32 s47, v221, 0\nv_readlane_b32 s48, v221, 16\n"
"s_max_u32 s48, s48, s47\ns_cmp_le_u32 s48, s52\ns_cbranch_scc1 back_p0\ns_bfe_u32 s52, s48, 1179656\n"
"s_add_u32 s44, s52, s28\ns_mov_b32 s45, 0\ns_and_b32 s46, s48, 255\ns_mov_b32 s92, 0\n"
"s_branch back_p0\nlate_p0:\ns_add_u32 s53, s53, 16777216\nv_mov_b32 v84, v100\n"
"v_mov_b32 v85, v101\nv_mov_b32 v86, v102\nv_mov_b32 v87, v103\nv_mov_b32 v88, v104\n"
"v_mov_b32 v89, v105\nv_mov_b32 v90, v106\nv_mov_b32 v91, v107\nv_mov_b32 v92, v108\n"
"v_mov_b32 v93, v109\nv_mov_b32 v94, v110\nv_mov_b32 v95, v111\nv_mov_b32 v96, v112\n"
"v_mov_b32 v97, v113\nv_mov_b32 v98, v114\nv_mov_b32 v99, v115\nv_smfmac_f32_16x16x64_bf16 v[68:71], v[48:51], a[16:23], v8\n"
"v_smfmac_f32_16x16x64_bf16 v[72:75], v[48:51], a[72:79], v8\nv_smfmac_f32_16x16x64_bf16 v[76:79], v[48:51], a[128:135], v8\nv_smfmac_f32_16x16x64_bf16 v[80:83], v[48:51], a[184:191], v8\nv_smfmac_f32_16x16x64_bf16 v[68:71], v[52:55], a[24:31], v8\n"
"v_smfmac_f32_16x16x64_bf16 v[72:75], v[52:55], a[80:87], v8\nv_smfmac_f32_16x16x64_bf16 v[76:79], v[52:55], a[136:143], v8\nv_smfmac_f32_16x16x64_bf16 v[80:83], v[52:55], a[192:199], v8\nv_smfmac_f32_16x16x64_bf16 v[68:71], v[56:59], a[32:39], v8\n"
"v_smfmac_f32_16x16x64_bf16 v[72:75], v[56:59], a[88:95], v8\nv_smfmac_f32_16x16x64_bf16 v[76:79], v[56:59], a[144:151], v8\nv_smfmac_f32_16x16x64_bf16 v[80:83], v[56:59], a[200:207], v8\nv_smfmac_f32_16x16x64_bf16 v[68:71], v[60:63], a[40:47], v8\n"
"v_smfmac_f32_16x16x64_bf16 v[72:75], v[60:63], a[96:103], v8\nv_smfmac_f32_16x16x64_bf16 v[76:79], v[60:63], a[152:159], v8\nv_smfmac_f32_16x16x64_bf16 v[80:83], v[60:63], a[208:215], v8\nv_smfmac_f32_16x16x64_bf16 v[68:71], v[64:67], a[48:55], v8\n"
"v_smfmac_f32_16x16x64_bf16 v[72:75], v[64:67], a[104:111], v8\nv_smfmac_f32_16x16x64_bf16 v[76:79], v[64:67], a[160:167], v8\nv_smfmac_f32_16x16x64_bf16 v[80:83], v[64:67], a[216:223], v8\nv_add_f32 v219, v68, v69\n"
"v_add_f32 v220, v70, v71\nv_cvt_pk_bf16_f32 v156, v219, v220\nv_add_f32 v219, v72, v73\nv_add_f32 v220, v74, v75\n"
"v_cvt_pk_bf16_f32 v157, v219, v220\nv_add_f32 v219, v76, v77\nv_add_f32 v220, v78, v79\nv_cvt_pk_bf16_f32 v158, v219, v220\n"
"v_add_f32 v219, v80, v81\nv_add_f32 v220, v82, v83\nv_cvt_pk_bf16_f32 v159, v219, v220\nbuffer_load_dwordx4 v[100:103], v9, s[16:19], s50 offen offset:0 sc1\n"
"buffer_load_dwordx4 v[104:107], v9, s[16:19], s50 offen offset:1024 sc1\nbuffer_load_dwordx4 v[108:111], v9, s[16:19], s50 offen offset:2048 sc1\nbuffer_load_dwordx4 v[112:115], v9, s[16:19], s50 offen offset:3072 sc1\ns_add_u32 s50, s50, 16384\n"
"buffer_store_dwordx4 v[156:159], v9, s[20:23], s51 offen sc1\ns_branch spin_p0\nslow_p0:\nv_max3_u32 v218, v124, v125, v126\n"
"v_max_u32 v218, v218, v127\nv_max3_u32 v218, v218, v128, v129\nv_max3_u32 v218, v218, v130, v131\nv_max3_u32 v218, v218, v116, v117\n"
"v_max3_u32 v218, v218, v118, v119\nv_max3_u32 v218, v218, v120, v121\nv_max3_u32 v218, v218, v122, v123\nv_cmp_eq_u32 vcc, -1, v218\n"
"s_cbranch_vccz exact_p0\ns_branch spin_p0\nspin2_p0:\ns_sleep 3\n"
"spin_p0:\ns_add_u32 s53, s53, 1\ns_add_u32 s49, s49, 1\ns_cmp_gt_u32 s49, 65536\n"
"s_cbranch_scc1 end_p0\nbuffer_load_dwordx4 v[116:119], v20, s[8:11], s41 offen offset:0 sc1\nbuffer_load_dwordx4 v[120:123], v20, s[8:11], s41 offen offset:1024 sc1\nbuffer_load_dwordx4 v[124:127], v21, s[8:11], s41 offen offset:0 sc1\n"
"buffer_load_dwordx4 v[128:131], v21, s[8:11], s41 offen offset:1024 sc1\ns_waitcnt vmcnt(0)\nv_max3_u32 v218, v124, v125, v126\nv_max_u32 v218, v218, v127\n"
"v_max3_u32 v218, v218, v128, v129\nv_max3_u32 v218, v218, v130, v131\nv_max3_u32 v218, v218, v116, v117\nv_max3_u32 v218, v218, v118, v119\n"
"v_max3_u32 v218, v218, v120, v121\nv_max3_u32 v218, v218, v122, v123\nv_cmp_eq_u32 vcc, -1, v218\ns_cbranch_vccnz spin2_p0\n"
"s_memrealtime s[54:55]\ns_add_u32 s52, s40, 1\ns_lshl_b32 s52, s52, 26\ns_waitcnt lgkmcnt(0)\n"
"s_sub_u32 s47, s54, s28\ns_add_u32 s47, s47, s38\ns_min_u32 s47, s47, 0x3ffff\ns_lshl_b32 s47, s47, 8\n"
"s_or_b32 s47, s47, s52\ns_and_b32 s48, s46, 15\ns_add_u32 s52, s48, 0\ns_min_u32 s52, s52, 15\n"
"s_or_b32 s47, s47, s52\ns_add_u32 s48, s48, 0\ns_min_u32 s48, s48, s52\ns_lshl_b32 s48, s48, 4\n"
"s_and_b32 s52, s46, 0xf0\ns_cmp_ge_u32 s92, 99\ns_cselect_b32 s48, s48, s52\ns_or_b32 s47, s47, s48\n"
"v_mov_b32 v225, s47\nbuffer_store_dword v225, v19, s[8:11], 0 offen sc1\ns_add_u32 s47, s40, 2\nv_mov_b32 v221, s47\n"
"ds_write_b32 v222, v221\nv_mfma_f32_16x16x32_bf16 v[132:135], v[84:87], v[116:119], 0\nv_mfma_f32_16x16x32_bf16 v[136:139], v[88:91], v[116:119], 0\nv_mfma_f32_16x16x32_bf16 v[148:151], v[176:179], v[116:119], 0\n"
"v_mfma_f32_16x16x32_bf16 v[140:143], v[92:95], v[120:123], 0\nv_mfma_f32_16x16x32_bf16 v[144:147], v[96:99], v[120:123], 0\nv_mfma_f32_16x16x32_bf16 v[148:151], v[180:183], v[120:123], v[148:151]\nv_cndmask_b32 v132, v136, v132, s[62:63]\n"
"v_cndmask_b32 v133, v137, v133, s[62:63]\nv_cndmask_b32 v134, v138, v134, s[62:63]\nv_cndmask_b32 v135, v139, v135, s[62:63]\nv_mfma_f32_16x16x32_f16 v[152:155], v[168:171], v[124:127], 0\n"
"v_mfma_f32_16x16x32_f16 v[152:155], v[172:175], v[128:131], v[152:155]\nv_cndmask_b32 v140, v144, v140, s[62:63]\nv_cndmask_b32 v141, v145, v141, s[62:63]\nv_cndmask_b32 v142, v146, v142, s[62:63]\n"
"v_cndmask_b32 v143, v147, v143, s[62:63]\ns_nop 2\nv_exp_f32 v152, v152\nv_exp_f32 v153, v153\n"
"v_exp_f32 v154, v154\nv_exp_f32 v155, v155\nv_cvt_pk_bf16_f32 v200, v152, v153\nv_cvt_pk_bf16_f32 v201, v154, v155\n"
"v_and_b32 v160, v200, v223\nv_and_b32 v161, v200, v224\nv_mfma_f32_16x16x16_bf16 v[148:151], v[214:215], v[200:201], v[148:151]\nv_and_b32 v162, v201, v223\n"
"v_and_b32 v163, v201, v224\nv_mfma_f32_16x16x16_bf16 v[132:135], v[156:157], v[160:161], v[132:135]\ns_nop 0\nv_mfma_f32_16x16x16_bf16 v[140:143], v[158:159], v[162:163], v[140:143]\n"
"v_rcp_f32 v202, v148\nv_rcp_f32 v203, v149\nv_mul_f32 v132, v132, v202\nv_mul_f32 v133, v133, v202\n"
"v_mul_f32 v134, v134, v202\nv_mul_f32 v135, v135, v202\nv_fmac_f32 v132, v140, v203\nv_fmac_f32 v133, v141, v203\n"
"v_fmac_f32 v134, v142, v203\nv_fmac_f32 v135, v143, v203\nv_add_f32_dpp v204, v132, v132 row_ror:8 row_mask:0xf bank_mask:0x3\nv_add_f32_dpp v205, v133, v133 row_ror:8 row_mask:0xf bank_mask:0x3\n"
"v_add_f32_dpp v204, v134, v134 row_ror:8 row_mask:0xf bank_mask:0xc\nv_add_f32_dpp v205, v135, v135 row_ror:8 row_mask:0xf bank_mask:0xc\nv_subrev_u32 v219, 0x2f800000, v148\nv_subrev_u32 v220, 0x2f800000, v149\n"
"v_max_u32 v219, v219, v220\nv_cmp_le_u32 vcc, 0x20000000, v219\nds_write_b64 v22, v[204:205]\ns_cbranch_vccz join_p0\n"
"exact_p0:\ns_add_u32 s53, s53, 65536\nex0_p0:\ns_add_u32 s47, s41, 0\n"
"buffer_load_dwordx2 v[184:185], v24, s[8:11], s47 offen offset:0 sc1\nbuffer_load_dwordx2 v[186:187], v24, s[8:11], s47 offen offset:512 sc1\nbuffer_load_dwordx2 v[188:189], v24, s[8:11], s47 offen offset:1024 sc1\nbuffer_load_dwordx2 v[190:191], v24, s[8:11], s47 offen offset:1536 sc1\n"
"buffer_load_dwordx4 v[192:195], v25, s[8:11], s47 offen sc1\nbuffer_load_dwordx4 v[196:199], v25, s[8:11], s47 offen offset:16 sc1\nv_mov_b32 v218, 0\ns_waitcnt vmcnt(0)\n"
"v_max3_u32 v218, v218, v184, v185\nv_max3_u32 v218, v218, v186, v187\nv_max3_u32 v218, v218, v188, v189\nv_max3_u32 v218, v218, v190, v191\n"
"v_max3_u32 v218, v218, v192, v193\nv_max3_u32 v218, v218, v194, v195\nv_max3_u32 v218, v218, v196, v197\nv_max3_u32 v218, v218, v198, v199\n"
"v_cmp_eq_u32 vcc, -1, v218\ns_cbranch_vccz exok0_p0\ns_add_u32 s53, s53, 0\ns_add_u32 s49, s49, 1\n"
"s_cmp_gt_u32 s49, 65536\ns_cbranch_scc1 end_p0\ns_branch ex0_p0\nexok0_p0:\n"
"v_add_f32 v184, v184, v186\nv_add_f32 v188, v188, v190\nv_add_f32 v184, v184, v188\nv_add_f32 v185, v185, v187\n"
"v_add_f32 v189, v189, v191\nv_add_f32 v185, v185, v189\nv_max_f32 v225, v184, v185\nv_max3_f32 v225, v225, v192, v193\n"
"v_max3_f32 v225, v225, v194, v195\nv_max3_f32 v225, v225, v196, v197\nv_max3_f32 v225, v225, v198, v199\nv_mov_b32 v219, v225\n"
"s_nop 1\nv_permlane16_swap_b32 v225, v219\nv_max_f32 v225, v225, v219\nv_mov_b32 v219, v225\n"
"s_nop 1\nv_permlane32_swap_b32 v225, v219\nv_max_f32 v225, v225, v219\nv_sub_f32 v184, v184, v225\n"
"v_exp_f32 v152, v184\nv_sub_f32 v185, v185, v225\nv_exp_f32 v153, v185\nv_sub_f32 v192, v192, v225\n"
"v_exp_f32 v192, v192\nv_sub_f32 v193, v193, v225\nv_exp_f32 v193, v193\nv_sub_f32 v194, v194, v225\n"
"v_exp_f32 v194, v194\nv_sub_f32 v195, v195, v225\nv_exp_f32 v195, v195\nv_sub_f32 v196, v196, v225\n"
"v_exp_f32 v196, v196\nv_sub_f32 v197, v197, v225\nv_exp_f32 v197, v197\nv_sub_f32 v198, v198, v225\n"
"v_exp_f32 v198, v198\nv_sub_f32 v199, v199, v225\nv_exp_f32 v199, v199\ns_nop 0\n"
"v_cvt_pk_bf16_f32 v116, v192, v193\nv_cvt_pk_bf16_f32 v117, v194, v195\nv_cvt_pk_bf16_f32 v118, v196, v197\nv_cvt_pk_bf16_f32 v119, v198, v199\n"
"ex1_p0:\ns_add_u32 s47, s41, 4096\nbuffer_load_dwordx2 v[184:185], v24, s[8:11], s47 offen offset:0 sc1\nbuffer_load_dwordx2 v[186:187], v24, s[8:11], s47 offen offset:512 sc1\n"
"buffer_load_dwordx2 v[188:189], v24, s[8:11], s47 offen offset:1024 sc1\nbuffer_load_dwordx2 v[190:191], v24, s[8:11], s47 offen offset:1536 sc1\nbuffer_load_dwordx4 v[192:195], v25, s[8:11], s47 offen sc1\nbuffer_load_dwordx4 v[196:199], v25, s[8:11], s47 offen offset:16 sc1\n"
"v_mov_b32 v218, 0\ns_waitcnt vmcnt(0)\nv_max3_u32 v218, v218, v184, v185\nv_max3_u32 v218, v218, v186, v187\n"
"v_max3_u32 v218, v218, v188, v189\nv_max3_u32 v218, v218, v190, v191\nv_max3_u32 v218, v218, v192, v193\nv_max3_u32 v218, v218, v194, v195\n"
"v_max3_u32 v218, v218, v196, v197\nv_max3_u32 v218, v218, v198, v199\nv_cmp_eq_u32 vcc, -1, v218\ns_cbranch_vccz exok1_p0\n"
"s_add_u32 s53, s53, 0\ns_add_u32 s49, s49, 1\ns_cmp_gt_u32 s49, 65536\ns_cbranch_scc1 end_p0\n"
"s_branch ex1_p0\nexok1_p0:\nv_add_f32 v184, v184, v186\nv_add_f32 v188, v188, v190\n"
"v_add_f32 v184, v184, v188\nv_add_f32 v185, v185, v187\nv_add_f32 v189, v189, v191\nv_add_f32 v185, v185, v189\n"
"v_max_f32 v225, v184, v185\nv_max3_f32 v225, v225, v192, v193\nv_max3_f32 v225, v225, v194, v195\nv_max3_f32 v225, v225, v196, v197\n"
"v_max3_f32 v225, v225, v198, v199\nv_mov_b32 v219, v225\ns_nop 1\nv_permlane16_swap_b32 v225, v219\n"
"v_max_f32 v225, v225, v219\nv_mov_b32 v219, v225\ns_nop 1\nv_permlane32_swap_b32 v225, v219\n"
"v_max_f32 v225, v225, v219\nv_sub_f32 v184, v184, v225\nv_exp_f32 v154, v184\nv_sub_f32 v185, v185, v225\n"
"v_exp_f32 v155, v185\nv_sub_f32 v192, v192, v225\nv_exp_f32 v192, v192\nv_sub_f32 v193, v193, v225\n"
"v_exp_f32 v193, v193\nv_sub_f32 v194, v194, v225\nv_exp_f32 v194, v194\nv_sub_f32 v195, v195, v225\n"
"v_exp_f32 v195, v195\nv_sub_f32 v196, v196, v225\nv_exp_f32 v196, v196\nv_sub_f32 v197, v197, v225\n"
"v_exp_f32 v197, v197\nv_sub_f32 v198, v198, v225\nv_exp_f32 v198, v198\nv_sub_f32 v199, v199, v225\n"
"v_exp_f32 v199, v199\ns_nop 0\nv_cvt_pk_bf16_f32 v120, v192, v193\nv_cvt_pk_bf16_f32 v121, v194, v195\n"
"v_cvt_pk_bf16_f32 v122, v196, v197\nv_cvt_pk_bf16_f32 v123, v198, v199\ns_memrealtime s[54:55]\ns_add_u32 s52, s40, 1\n"
"s_lshl_b32 s52, s52, 26\ns_waitcnt lgkmcnt(0)\ns_sub_u32 s47, s54, s28\ns_add_u32 s47, s47, s38\n"
"s_min_u32 s47, s47, 0x3ffff\ns_lshl_b32 s47, s47, 8\ns_or_b32 s47, s47, s52\ns_and_b32 s48, s46, 15\n"
"s_add_u32 s52, s48, 0\ns_min_u32 s52, s52, 15\ns_or_b32 s47, s47, s52\ns_add_u32 s48, s48, 0\n"
"s_min_u32 s48, s48, s52\ns_lshl_b32 s48, s48, 4\ns_and_b32 s52, s46, 0xf0\ns_cmp_ge_u32 s92, 99\n"
"s_cselect_b32 s48, s48, s52\ns_or_b32 s47, s47, s48\nv_mov_b32 v225, s47\nbuffer_store_dword v225, v19, s[8:11], 0 offen sc1\n"
"s_add_u32 s47, s40, 2\nv_mov_b32 v221, s47\nds_write_b32 v222, v221\nv_mfma_f32_16x16x32_bf16 v[132:135], v[84:87], v[116:119], 0\n"
"v_mfma_f32_16x16x32_bf16 v[136:139], v[88:91], v[116:119], 0\nv_mfma_f32_16x16x32_bf16 v[148:151], v[176:179], v[116:119], 0\nv_mfma_f32_16x16x32_bf16 v[140:143], v[92:95], v[120:123], 0\nv_mfma_f32_16x16x32_bf16 v[144:147], v[96:99], v[120:123], 0\n"
"v_mfma_f32_16x16x32_bf16 v[148:151], v[180:183], v[120:123], v[148:151]\nv_cndmask_b32 v132, v136, v132, s[62:63]\nv_cndmask_b32 v133, v137, v133, s[62:63]\nv_cndmask_b32 v134, v138, v134, s[62:63]\n"
"v_cndmask_b32 v135, v139, v135, s[62:63]\ns_nop 0\nv_cndmask_b32 v140, v144, v140, s[62:63]\nv_cndmask_b32 v141, v145, v141, s[62:63]\n"
"v_cndmask_b32 v142, v146, v142, s[62:63]\nv_cndmask_b32 v143, v147, v143, s[62:63]\nv_cvt_pk_bf16_f32 v200, v152, v153\nv_cvt_pk_bf16_f32 v201, v154, v155\n"
"v_and_b32 v160, v200, v223\nv_and_b32 v161, v200, v224\nv_mfma_f32_16x16x16_bf16 v[148:151], v[214:215], v[200:201], v[148:151]\nv_and_b32 v162, v201, v223\n"
"v_and_b32 v163, v201, v224\nv_mfma_f32_16x16x16_bf16 v[132:135], v[156:157], v[160:161], v[132:135]\ns_nop 0\nv_mfma_f32_16x16x16_bf16 v[140:143], v[158:159], v[162:163], v[140:143]\n"
"v_rcp_f32 v202, v148\nv_rcp_f32 v203, v149\nv_mul_f32 v132, v132, v202\nv_mul_f32 v133, v133, v202\n"
"v_mul_f32 v134, v134, v202\nv_mul_f32 v135, v135, v202\nv_fmac_f32 v132, v140, v203\nv_fmac_f32 v133, v141, v203\n"
"v_fmac_f32 v134, v142, v203\nv_fmac_f32 v135, v143, v203\nv_add_f32_dpp v204, v132, v132 row_ror:8 row_mask:0xf bank_mask:0x3\nv_add_f32_dpp v205, v133, v133 row_ror:8 row_mask:0xf bank_mask:0x3\n"
"v_add_f32_dpp v204, v134, v134 row_ror:8 row_mask:0xf bank_mask:0xc\nv_add_f32_dpp v205, v135, v135 row_ror:8 row_mask:0xf bank_mask:0xc\nds_write_b64 v22, v[204:205]\ns_branch join_p0\n"
"end_p0:\ns_branch fk_done\nfk_p1:\ns_mov_b32 s40, 0\n"
"s_mov_b32 s41, 0\ns_mov_b32 s42, 262144\ns_mov_b32 s43, 393216\ns_mov_b32 s44, s28\n"
"s_mov_b32 s45, 0\ns_mov_b32 s46, 0\ns_mov_b32 s92, 0\ns_mov_b32 s49, 0\n"
"s_mov_b32 s53, 0\ns_mov_b32 s50, s35\ns_mov_b32 s51, s36\nv_mov_b32 v222, 24576\n"
"s_mov_b32 s62, 0x00ff00ff\ns_mov_b32 s63, 0x00ff00ff\nv_cndmask_b32 v223, 0, -1, s[62:63]\nv_not_b32 v224, v223\n"
"v_bfe_u32 v219, v9, 4, 1\nv_lshlrev_b32 v220, 4, v219\nv_mov_b32 v218, 0x3c00\nv_lshlrev_b32 v218, v220, v218\n"
"v_bfe_u32 v220, v9, 5, 3\nv_cmp_eq_u32 vcc, 0, v220\ns_nop 1\nv_cndmask_b32 v168, 0, v218, vcc\n"
"v_cmp_eq_u32 vcc, 2, v220\ns_nop 1\nv_cndmask_b32 v169, 0, v218, vcc\nv_cmp_eq_u32 vcc, 4, v220\n"
"s_nop 1\nv_cndmask_b32 v170, 0, v218, vcc\nv_cmp_eq_u32 vcc, 6, v220\ns_nop 1\n"
"v_cndmask_b32 v171, 0, v218, vcc\nv_cmp_eq_u32 vcc, 1, v220\ns_nop 1\nv_cndmask_b32 v172, 0, v218, vcc\n"
"v_cmp_eq_u32 vcc, 3, v220\ns_nop 1\nv_cndmask_b32 v173, 0, v218, vcc\nv_cmp_eq_u32 vcc, 5, v220\n"
"s_nop 1\nv_cndmask_b32 v174, 0, v218, vcc\nv_cmp_eq_u32 vcc, 7, v220\ns_nop 1\n"
"v_cndmask_b32 v175, 0, v218, vcc\nv_mov_b32 v218, 0x3f803f80\nv_cmp_eq_u32 vcc, 0, v219\ns_nop 1\n"
"v_cndmask_b32 v176, 0, v218, vcc\nv_cndmask_b32 v177, 0, v218, vcc\nv_cndmask_b32 v178, 0, v218, vcc\nv_cndmask_b32 v179, 0, v218, vcc\n"
"v_cndmask_b32 v214, 0, v218, vcc\nv_cmp_eq_u32 vcc, 1, v219\ns_nop 1\nv_cndmask_b32 v180, 0, v218, vcc\n"
"v_cndmask_b32 v181, 0, v218, vcc\nv_cndmask_b32 v182, 0, v218, vcc\nv_cndmask_b32 v183, 0, v218, vcc\nv_cndmask_b32 v215, 0, v218, vcc\n"
"s_add_u32 s47, s34, 28672\nbuffer_load_dwordx4 a[0:3], v9, s[12:15], s47 offen sc1\nbuffer_load_dwordx4 a[4:7], v9, s[12:15], s47 offen offset:1024 sc1\ns_add_u32 s47, s34, 30720\n"
"buffer_load_dwordx4 a[8:11], v9, s[12:15], s47 offen sc1\nbuffer_load_dwordx4 a[12:15], v9, s[12:15], s47 offen offset:1024 sc1\ns_add_u32 s47, s34, 32768\nbuffer_load_dwordx4 a[16:19], v9, s[12:15], s47 offen sc1\n"
"buffer_load_dwordx4 a[20:23], v9, s[12:15], s47 offen offset:1024 sc1\ns_add_u32 s47, s34, 34816\nbuffer_load_dwordx4 a[24:27], v9, s[12:15], s47 offen sc1\nbuffer_load_dwordx4 a[28:31], v9, s[12:15], s47 offen offset:1024 sc1\n"
"s_add_u32 s47, s34, 36864\nbuffer_load_dwordx4 a[32:35], v9, s[12:15], s47 offen sc1\nbuffer_load_dwordx4 a[36:39], v9, s[12:15], s47 offen offset:1024 sc1\ns_add_u32 s47, s34, 38912\n"
"buffer_load_dwordx4 a[40:43], v9, s[12:15], s47 offen sc1\nbuffer_load_dwordx4 a[44:47], v9, s[12:15], s47 offen offset:1024 sc1\ns_add_u32 s47, s34, 40960\nbuffer_load_dwordx4 a[48:51], v9, s[12:15], s47 offen sc1\n"
"buffer_load_dwordx4 a[52:55], v9, s[12:15], s47 offen offset:1024 sc1\ns_waitcnt vmcnt(14)\ns_add_u32 s47, s34, 43008\nbuffer_load_dwordx4 a[56:59], v9, s[12:15], s47 offen sc1\n"
"buffer_load_dwordx4 a[60:63], v9, s[12:15], s47 offen offset:1024 sc1\ns_add_u32 s47, s34, 45056\nbuffer_load_dwordx4 a[64:67], v9, s[12:15], s47 offen sc1\nbuffer_load_dwordx4 a[68:71], v9, s[12:15], s47 offen offset:1024 sc1\n"
"s_add_u32 s47, s34, 47104\nbuffer_load_dwordx4 a[72:75], v9, s[12:15], s47 offen sc1\nbuffer_load_dwordx4 a[76:79], v9, s[12:15], s47 offen offset:1024 sc1\ns_add_u32 s47, s34, 49152\n"
"buffer_load_dwordx4 a[80:83], v9, s[12:15], s47 offen sc1\nbuffer_load_dwordx4 a[84:87], v9, s[12:15], s47 offen offset:1024 sc1\ns_add_u32 s47, s34, 51200\nbuffer_load_dwordx4 a[88:91], v9, s[12:15], s47 offen sc1\n"
"buffer_load_dwordx4 a[92:95], v9, s[12:15], s47 offen offset:1024 sc1\ns_add_u32 s47, s34, 53248\nbuffer_load_dwordx4 a[96:99], v9, s[12:15], s47 offen sc1\nbuffer_load_dwordx4 a[100:103], v9, s[12:15], s47 offen offset:1024 sc1\n"
"s_add_u32 s47, s34, 55296\nbuffer_load_dwordx4 a[104:107], v9, s[12:15], s47 offen sc1\nbuffer_load_dwordx4 a[108:111], v9, s[12:15], s47 offen offset:1024 sc1\ns_waitcnt vmcnt(14)\n"
"s_add_u32 s47, s34, 0\nbuffer_load_dwordx4 a[112:115], v9, s[12:15], s47 offen sc1\nbuffer_load_dwordx4 a[116:119], v9, s[12:15], s47 offen offset:1024 sc1\ns_add_u32 s47, s34, 2048\n"
"buffer_load_dwordx4 a[120:123], v9, s[12:15], s47 offen sc1\nbuffer_load_dwordx4 a[124:127], v9, s[12:15], s47 offen offset:1024 sc1\ns_add_u32 s47, s34, 4096\nbuffer_load_dwordx4 a[128:131], v9, s[12:15], s47 offen sc1\n"
"buffer_load_dwordx4 a[132:135], v9, s[12:15], s47 offen offset:1024 sc1\ns_add_u32 s47, s34, 6144\nbuffer_load_dwordx4 a[136:139], v9, s[12:15], s47 offen sc1\nbuffer_load_dwordx4 a[140:143], v9, s[12:15], s47 offen offset:1024 sc1\n"
"s_add_u32 s47, s34, 8192\nbuffer_load_dwordx4 a[144:147], v9, s[12:15], s47 offen sc1\nbuffer_load_dwordx4 a[148:151], v9, s[12:15], s47 offen offset:1024 sc1\ns_add_u32 s47, s34, 10240\n"
"buffer_load_dwordx4 a[152:155], v9, s[12:15], s47 offen sc1\nbuffer_load_dwordx4 a[156:159], v9, s[12:15], s47 offen offset:1024 sc1\ns_add_u32 s47, s34, 12288\nbuffer_load_dwordx4 a[160:163], v9, s[12:15], s47 offen sc1\n"
"buffer_load_dwordx4 a[164:167], v9, s[12:15], s47 offen offset:1024 sc1\ns_waitcnt vmcnt(14)\ns_add_u32 s47, s34, 14336\nbuffer_load_dwordx4 a[168:171], v9, s[12:15], s47 offen sc1\n"
"buffer_load_dwordx4 a[172:175], v9, s[12:15], s47 offen offset:1024 sc1\ns_add_u32 s47, s34, 16384\nbuffer_load_dwordx4 a[176:179], v9, s[12:15], s47 offen sc1\nbuffer_load_dwordx4 a[180:183], v9, s[12:15], s47 offen offset:1024 sc1\n"
"s_add_u32 s47, s34, 18432\nbuffer_load_dwordx4 a[184:187], v9, s[12:15], s47 offen sc1\nbuffer_load_dwordx4 a[188:191], v9, s[12:15], s47 offen offset:1024 sc1\ns_add_u32 s47, s34, 20480\n"
"buffer_load_dwordx4 a[192:195], v9, s[12:15], s47 offen sc1\nbuffer_load_dwordx4 a[196:199], v9, s[12:15], s47 offen offset:1024 sc1\ns_add_u32 s47, s34, 22528\nbuffer_load_dwordx4 a[200:203], v9, s[12:15], s47 offen sc1\n"
"buffer_load_dwordx4 a[204:207], v9, s[12:15], s47 offen offset:1024 sc1\ns_add_u32 s47, s34, 24576\nbuffer_load_dwordx4 a[208:211], v9, s[12:15], s47 offen sc1\nbuffer_load_dwordx4 a[212:215], v9, s[12:15], s47 offen offset:1024 sc1\n"
"s_add_u32 s47, s34, 26624\nbuffer_load_dwordx4 a[216:219], v9, s[12:15], s47 offen sc1\nbuffer_load_dwordx4 a[220:223], v9, s[12:15], s47 offen offset:1024 sc1\ns_waitcnt vmcnt(14)\n"
"buffer_load_dword v218, v11, s[8:11], 0 offen sc1\ns_waitcnt vmcnt(0)\nfl_p1:\ns_add_u32 s47, s40, 2\n"
"s_min_u32 s47, s47, 64\ns_add_u32 s47, s47, s33\nv_subrev_u32 v218, s47, v218\nv_cmp_lt_u32 vcc, 64, v218\n"
"s_cbranch_vccz flok_p1\ns_add_u32 s53, s53, 0\ns_add_u32 s49, s49, 1\ns_cmp_gt_u32 s49, 65536\n"
"s_cbranch_scc1 end_p1\ns_sleep 2\nbuffer_load_dword v218, v11, s[8:11], 0 offen sc1\ns_waitcnt vmcnt(0)\n"
"s_branch fl_p1\nflok_p1:\nbuffer_load_dwordx4 v[100:103], v9, s[16:19], s50 offen offset:2048 sc1\nbuffer_load_dwordx4 v[104:107], v9, s[16:19], s50 offen offset:3072 sc1\n"
"buffer_load_dwordx4 v[108:111], v9, s[16:19], s50 offen offset:0 sc1\nbuffer_load_dwordx4 v[112:115], v9, s[16:19], s50 offen offset:1024 sc1\ns_add_u32 s50, s50, 16384\ns_waitcnt vmcnt(0)\n"
"s_barrier\n.p2align 6\ntop_p1:\nv_mov_b32 v68, 0\n"
"v_mov_b32 v69, 0\nv_mov_b32 v70, 0\nv_mov_b32 v71, 0\nv_mov_b32 v72, 0\n"
"v_mov_b32 v73, 0\nv_mov_b32 v74, 0\nv_mov_b32 v75, 0\nv_mov_b32 v76, 0\n"
"v_mov_b32 v77, 0\nv_mov_b32 v78, 0\nv_mov_b32 v79, 0\nv_mov_b32 v80, 0\n"
"v_mov_b32 v81, 0\nv_mov_b32 v82, 0\nv_mov_b32 v83, 0\nv_mov_b32 v218, 0\n"
"s_add_u32 s52, s44, s37\nm_p1:\ns_memrealtime s[54:55]\ns_waitcnt lgkmcnt(0)\n"
"s_sub_u32 s47, s54, s52\ns_cmp_lt_i32 s47, 0\ns_cbranch_scc1 m_p1\ntop2_p1:\n"
"buffer_load_dwordx4 v[52:55], v10, s[8:11], s43 offen offset:1024 sc1\nbuffer_load_dwordx4 v[48:51], v10, s[8:11], s43 offen offset:0 sc1\ns_waitcnt vmcnt(1)\nds_write_b128 v9, v[52:55] offset:3072\n"
"v_max3_u32 v218, v218, v52, v53\nv_max3_u32 v218, v218, v54, v55\ns_waitcnt vmcnt(0)\nds_write_b128 v9, v[48:51] offset:2048\n"
"v_max3_u32 v218, v218, v48, v49\nv_max3_u32 v218, v218, v50, v51\nv_cmp_eq_u32 vcc, -1, v218\nv_smfmac_f32_16x16x64_bf16 v[68:71], v[52:55], a[24:31], v8\n"
"v_smfmac_f32_16x16x64_bf16 v[72:75], v[52:55], a[80:87], v8\ns_cbranch_vccnz retry1_p1\ns_waitcnt lgkmcnt(0)\ns_barrier\n"
"ds_read_b32 v221, v222\nds_read_b128 v[56:59], v9 offset:4096\nds_read_b128 v[60:63], v9 offset:5120\nds_read_b128 v[64:67], v9 offset:6144\n"
"ds_read_b128 v[40:43], v9 offset:0\nds_read_b128 v[44:47], v9 offset:1024\nv_smfmac_f32_16x16x64_bf16 v[76:79], v[52:55], a[136:143], v8\nv_smfmac_f32_16x16x64_bf16 v[80:83], v[52:55], a[192:199], v8\n"
"v_smfmac_f32_16x16x64_bf16 v[68:71], v[48:51], a[16:23], v8\nv_smfmac_f32_16x16x64_bf16 v[72:75], v[48:51], a[72:79], v8\nv_smfmac_f32_16x16x64_bf16 v[76:79], v[48:51], a[128:135], v8\nv_smfmac_f32_16x16x64_bf16 v[80:83], v[48:51], a[184:191], v8\n"
"s_waitcnt lgkmcnt(0)\nv_readfirstlane_b32 s47, v221\ns_sub_u32 s47, s47, s40\ns_cmp_eq_u32 s47, 1\n"
"s_cbranch_scc1 adopt_p1\nback_p1:\ns_cmp_eq_u32 s40, 63\ns_cbranch_scc1 last_p1\n"
"s_and_b32 s47, s46, 15\ns_mul_i32 s47, s47, 2\ns_add_u32 s47, s47, s29\ns_add_u32 s47, s47, s45\n"
"s_and_b32 s91, s47, 15\ns_lshr_b32 s47, s47, 4\ns_lshl_b32 s47, s47, 2\ns_add_u32 s90, s44, s47\n"
"s_mul_i32 s48, s91, 6\ns_and_b32 s48, s48, -4\ns_getpc_b64 s[60:61]\nspc_p1:\n"
"s_sub_u32 s48, sgo_p1-spc_p1, s48\ns_add_u32 s60, s60, s48\ns_addc_u32 s61, s61, 0\nsf_p1:\n"
"s_memrealtime s[54:55]\ns_nop 2\ns_memrealtime s[56:57]\ns_nop 2\n"
"s_memrealtime s[58:59]\ns_waitcnt lgkmcnt(4)\ns_sub_u32 s47, s54, s90\ns_add_u32 s48, s47, 12\n"
"s_cmp_ge_u32 s48, 12\ns_cbranch_scc1 so_p1\ns_memrealtime s[54:55]\ns_waitcnt lgkmcnt(4)\n"
"s_cmp_ge_u32 s56, s90\ns_cbranch_scc1 sx_p1\ns_memrealtime s[56:57]\ns_waitcnt lgkmcnt(4)\n"
"s_cmp_ge_u32 s58, s90\ns_cbranch_scc1 sx_p1\ns_memrealtime s[58:59]\ns_waitcnt lgkmcnt(4)\n"
"s_cmp_ge_u32 s54, s90\ns_cbranch_scc1 sx_p1\ns_memrealtime s[54:55]\ns_waitcnt lgkmcnt(4)\n"
"s_cmp_ge_u32 s56, s90\ns_cbranch_scc1 sx_p1\ns_memrealtime s[56:57]\ns_waitcnt lgkmcnt(4)\n"
"s_cmp_ge_u32 s58, s90\ns_cbranch_scc1 sx_p1\ns_memrealtime s[58:59]\ns_waitcnt lgkmcnt(4)\n"
"s_cmp_ge_u32 s54, s90\ns_cbranch_scc1 sx_p1\ns_memrealtime s[54:55]\ns_waitcnt lgkmcnt(4)\n"
"s_cmp_ge_u32 s56, s90\ns_cbranch_scc1 sx_p1\ns_memrealtime s[56:57]\ns_waitcnt lgkmcnt(4)\n"
"s_cmp_ge_u32 s58, s90\ns_cbranch_scc1 sx_p1\ns_memrealtime s[58:59]\ns_waitcnt lgkmcnt(4)\n"
"s_cmp_ge_u32 s54, s90\ns_cbranch_scc1 sx_p1\ns_memrealtime s[54:55]\ns_waitcnt lgkmcnt(4)\n"
"s_cmp_ge_u32 s56, s90\ns_cbranch_scc1 sx_p1\ns_memrealtime s[56:57]\ns_waitcnt lgkmcnt(4)\n"
"s_cmp_ge_u32 s58, s90\ns_cbranch_scc1 sx_p1\ns_memrealtime s[58:59]\ns_waitcnt lgkmcnt(4)\n"
"s_cmp_ge_u32 s54, s90\ns_cbranch_scc1 sx_p1\ns_memrealtime s[54:55]\ns_waitcnt lgkmcnt(4)\n"
"s_cmp_ge_u32 s56, s90\ns_cbranch_scc1 sx_p1\ns_memrealtime s[56:57]\ns_waitcnt lgkmcnt(4)\n"
"s_cmp_ge_u32 s58, s90\ns_cbranch_scc1 sx_p1\ns_memrealtime s[58:59]\ns_waitcnt lgkmcnt(4)\n"
"s_cmp_ge_u32 s54, s90\ns_cbranch_scc1 sx_p1\ns_memrealtime s[54:55]\ns_waitcnt lgkmcnt(4)\n"
"s_cmp_ge_u32 s56, s90\ns_cbranch_scc1 sx_p1\ns_memrealtime s[56:57]\ns_branch sx_p1\n"
"so_p1:\ns_waitcnt lgkmcnt(0)\ns_cmp_lt_i32 s47, 0\ns_cbranch_scc1 sf_p1\n"
"s_cmp_gt_i32 s47, s32\ns_cbranch_scc1 late_p1\ns_branch sgo_p1\nsx_p1:\n"
"s_setpc_b64 s[60:61]\ns_nop 0\ns_nop 0\ns_nop 0\n"
"s_nop 0\ns_nop 0\ns_nop 0\ns_nop 0\n"
"s_nop 0\ns_nop 0\ns_nop 0\ns_nop 0\n"
"s_nop 0\ns_nop 0\ns_nop 0\ns_nop 0\n"
"s_nop 0\ns_nop 0\ns_nop 0\ns_nop 0\n"
"s_nop 0\ns_nop 0\ns_nop 0\ns_nop 0\n"
"s_nop 0\nsgo_p1:\ns_nop 5\nbuffer_load_dwordx4 v[116:119], v20, s[8:11], s41 offen offset:1024 sc1\n"
"buffer_load_dwordx4 v[120:123], v20, s[8:11], s41 offen offset:0 sc1\nv_smfmac_f32_16x16x64_bf16 v[68:71], v[56:59], a[32:39], v8\nv_mov_b32 v84, v100\nv_smfmac_f32_16x16x64_bf16 v[72:75], v[56:59], a[88:95], v8\n"
"v_mov_b32 v85, v101\nv_smfmac_f32_16x16x64_bf16 v[76:79], v[56:59], a[144:151], v8\nv_mov_b32 v86, v102\nv_smfmac_f32_16x16x64_bf16 v[80:83], v[56:59], a[200:207], v8\n"
"v_mov_b32 v87, v103\nv_smfmac_f32_16x16x64_bf16 v[68:71], v[60:63], a[40:47], v8\nv_mov_b32 v88, v104\nv_smfmac_f32_16x16x64_bf16 v[72:75], v[60:63], a[96:103], v8\n"
"v_mov_b32 v89, v105\nv_smfmac_f32_16x16x64_bf16 v[76:79], v[60:63], a[152:159], v8\nv_mov_b32 v90, v106\nv_smfmac_f32_16x16x64_bf16 v[80:83], v[60:63], a[208:215], v8\n"
"v_mov_b32 v91, v107\nv_smfmac_f32_16x16x64_bf16 v[68:71], v[64:67], a[48:55], v8\nv_mov_b32 v92, v108\nv_smfmac_f32_16x16x64_bf16 v[72:75], v[64:67], a[104:111], v8\n"
"v_mov_b32 v93, v109\nbuffer_load_dwordx4 v[124:127], v21, s[8:11], s41 offen offset:1024 sc1\nbuffer_load_dwordx4 v[128:131], v21, s[8:11], s41 offen offset:0 sc1\nv_smfmac_f32_16x16x64_bf16 v[76:79], v[64:67], a[160:167], v8\n"
"v_mov_b32 v94, v110\nv_smfmac_f32_16x16x64_bf16 v[80:83], v[64:67], a[216:223], v8\nv_mov_b32 v95, v111\nv_smfmac_f32_16x16x64_bf16 v[68:71], v[40:43], a[0:7], v8\n"
"v_mov_b32 v96, v112\nv_smfmac_f32_16x16x64_bf16 v[72:75], v[40:43], a[56:63], v8\nv_mov_b32 v97, v113\nv_smfmac_f32_16x16x64_bf16 v[76:79], v[40:43], a[112:119], v8\n"
"v_mov_b32 v98, v114\nv_smfmac_f32_16x16x64_bf16 v[80:83], v[40:43], a[168:175], v8\nv_mov_b32 v99, v115\nv_smfmac_f32_16x16x64_bf16 v[68:71], v[44:47], a[8:15], v8\n"
"v_smfmac_f32_16x16x64_bf16 v[72:75], v[44:47], a[64:71], v8\nv_smfmac_f32_16x16x64_bf16 v[76:79], v[44:47], a[120:127], v8\nv_smfmac_f32_16x16x64_bf16 v[80:83], v[44:47], a[176:183], v8\nv_add_f32 v219, v68, v69\n"
"v_add_f32 v220, v70, v71\nv_cvt_pk_bf16_f32 v156, v219, v220\nv_add_f32 v219, v72, v73\nv_add_f32 v220, v74, v75\n"
"v_cvt_pk_bf16_f32 v157, v219, v220\nv_add_f32 v219, v76, v77\nv_add_f32 v220, v78, v79\nv_cvt_pk_bf16_f32 v158, v219, v220\n"
"v_add_f32 v219, v80, v81\nv_add_f32 v220, v82, v83\nv_cvt_pk_bf16_f32 v159, v219, v220\nbuffer_load_dwordx4 v[100:103], v9, s[16:19], s50 offen offset:2048 sc1\n"
"buffer_load_dwordx4 v[104:107], v9, s[16:19], s50 offen offset:3072 sc1\nbuffer_load_dwordx4 v[108:111], v9, s[16:19], s50 offen offset:0 sc1\nbuffer_load_dwordx4 v[112:115], v9, s[16:19], s50 offen offset:1024 sc1\ns_add_u32 s50, s50, 16384\n"
"buffer_store_dwordx4 v[156:159], v9, s[20:23], s51 offen sc1\ns_waitcnt vmcnt(8)\nv_mfma_f32_16x16x32_bf16 v[132:135], v[84:87], v[116:119], 0\nv_mfma_f32_16x16x32_bf16 v[136:139], v[88:91], v[116:119], 0\n"
"v_mfma_f32_16x16x32_bf16 v[148:151], v[176:179], v[116:119], 0\ns_waitcnt vmcnt(7)\nv_mfma_f32_16x16x32_bf16 v[140:143], v[92:95], v[120:123], 0\nv_mfma_f32_16x16x32_bf16 v[144:147], v[96:99], v[120:123], 0\n"
"v_mfma_f32_16x16x32_bf16 v[148:151], v[180:183], v[120:123], v[148:151]\nv_cndmask_b32 v132, v136, v132, s[62:63]\nv_cndmask_b32 v133, v137, v133, s[62:63]\nv_cndmask_b32 v134, v138, v134, s[62:63]\n"
"v_cndmask_b32 v135, v139, v135, s[62:63]\ns_waitcnt vmcnt(6)\nv_mfma_f32_16x16x32_f16 v[152:155], v[168:171], v[124:127], 0\ns_waitcnt vmcnt(5)\n"
"v_mfma_f32_16x16x32_f16 v[152:155], v[172:175], v[128:131], v[152:155]\nv_cndmask_b32 v140, v144, v140, s[62:63]\nv_cndmask_b32 v141, v145, v141, s[62:63]\nv_cndmask_b32 v142, v146, v142, s[62:63]\n"
"v_cndmask_b32 v143, v147, v143, s[62:63]\ns_nop 2\nv_exp_f32 v152, v152\nv_exp_f32 v153, v153\n"
"v_exp_f32 v154, v154\nv_exp_f32 v155, v155\nv_cvt_pk_bf16_f32 v200, v152, v153\nv_cvt_pk_bf16_f32 v201, v154, v155\n"
"v_and_b32 v160, v200, v223\nv_and_b32 v161, v200, v224\nv_mfma_f32_16x16x16_bf16 v[148:151], v[214:215], v[200:201], v[148:151]\nv_and_b32 v162, v201, v223\n"
"v_and_b32 v163, v201, v224\nv_mfma_f32_16x16x16_bf16 v[132:135], v[156:157], v[160:161], v[132:135]\ns_nop 0\nv_mfma_f32_16x16x16_bf16 v[140:143], v[158:159], v[162:163], v[140:143]\n"
"v_rcp_f32 v202, v148\nv_rcp_f32 v203, v149\nv_mul_f32 v132, v132, v202\nv_mul_f32 v133, v133, v202\n"
"v_mul_f32 v134, v134, v202\nv_mul_f32 v135, v135, v202\nv_fmac_f32 v132, v140, v203\nv_fmac_f32 v133, v141, v203\n"
"v_fmac_f32 v134, v142, v203\nv_fmac_f32 v135, v143, v203\nv_add_f32_dpp v204, v132, v132 row_ror:8 row_mask:0xf bank_mask:0x3\nv_add_f32_dpp v205, v133, v133 row_ror:8 row_mask:0xf bank_mask:0x3\n"
"v_add_f32_dpp v204, v134, v134 row_ror:8 row_mask:0xf bank_mask:0xc\nv_add_f32_dpp v205, v135, v135 row_ror:8 row_mask:0xf bank_mask:0xc\nv_subrev_u32 v219, 0x2f800000, v148\nv_subrev_u32 v220, 0x2f800000, v149\n"
"v_max_u32 v219, v219, v220\nv_cmp_le_u32 vcc, 0x20000000, v219\nds_write_b64 v22, v[204:205]\ns_cbranch_vccnz slow_p1\n"
"join_p1:\nv_mov_b32 v219, 0\ns_nop 1\nv_mov_b32 v219, 0\n"
"s_waitcnt lgkmcnt(0)\ns_barrier\nds_read_b64 v[206:207], v23 offset:0\nds_read_b64 v[208:209], v23 offset:1280\n"
"ds_read_b64 v[210:211], v23 offset:2560\nds_read_b64 v[212:213], v23 offset:3840\nds_read_b32 v221, v222\nv_mov_b32 v219, 0\n"
"s_nop 1\nv_mov_b32 v219, 0\ns_nop 1\nv_mov_b32 v219, 0\n"
"s_waitcnt lgkmcnt(3)\nv_pk_add_f32 v[206:207], v[206:207], v[208:209]\ns_waitcnt lgkmcnt(1)\nv_pk_add_f32 v[210:211], v[210:211], v[212:213]\n"
"v_pk_add_f32 v[206:207], v[206:207], v[210:211]\nv_cvt_pk_bf16_f32 v220, v206, v207\nbuffer_store_dword v220, v16, s[8:11], s41 offen sc1\ns_add_u32 s51, s51, 4096\n"
"s_add_u32 s40, s40, 1\ns_mov_b32 s43, s41\ns_add_u32 s41, s41, 131072\ns_and_b32 s41, s41, 524287\n"
"s_xor_b32 s42, s41, 262144\ns_and_b32 s47, s46, 15\ns_mul_i32 s48, s47, 4\ns_add_u32 s45, s45, s48\n"
"s_add_u32 s45, s45, s30\ns_lshr_b32 s48, s45, 4\ns_lshl_b32 s48, s48, 2\ns_add_u32 s44, s44, s48\n"
"s_and_b32 s45, s45, 15\ns_add_u32 s92, s92, 1\ns_lshr_b32 s48, s46, 4\ns_cmp_gt_u32 s47, s48\n"
"s_cselect_b32 s47, 1, 0\ns_and_b32 s48, s92, 1\ns_cselect_b32 s47, 0, s47\ns_sub_u32 s46, s46, s47\n"
"s_waitcnt lgkmcnt(0)\nv_readfirstlane_b32 s47, v221\ns_sub_u32 s47, s47, s40\ns_cmp_eq_u32 s47, 1\n"
"s_cbranch_scc1 rec_p1\ns_branch top_p1\nlast_p1:\nv_smfmac_f32_16x16x64_bf16 v[68:71], v[56:59], a[32:39], v8\n"
"v_smfmac_f32_16x16x64_bf16 v[72:75], v[56:59], a[88:95], v8\nv_smfmac_f32_16x16x64_bf16 v[76:79], v[56:59], a[144:151], v8\nv_smfmac_f32_16x16x64_bf16 v[80:83], v[56:59], a[200:207], v8\nv_smfmac_f32_16x16x64_bf16 v[68:71], v[60:63], a[40:47], v8\n"
"v_smfmac_f32_16x16x64_bf16 v[72:75], v[60:63], a[96:103], v8\nv_smfmac_f32_16x16x64_bf16 v[76:79], v[60:63], a[152:159], v8\nv_smfmac_f32_16x16x64_bf16 v[80:83], v[60:63], a[208:215], v8\nv_smfmac_f32_16x16x64_bf16 v[68:71], v[64:67], a[48:55], v8\n"
"v_smfmac_f32_16x16x64_bf16 v[72:75], v[64:67], a[104:111], v8\nv_smfmac_f32_16x16x64_bf16 v[76:79], v[64:67], a[160:167], v8\nv_smfmac_f32_16x16x64_bf16 v[80:83], v[64:67], a[216:223], v8\nv_smfmac_f32_16x16x64_bf16 v[68:71], v[40:43], a[0:7], v8\n"
"v_smfmac_f32_16x16x64_bf16 v[72:75], v[40:43], a[56:63], v8\nv_smfmac_f32_16x16x64_bf16 v[76:79], v[40:43], a[112:119], v8\nv_smfmac_f32_16x16x64_bf16 v[80:83], v[40:43], a[168:175], v8\nv_smfmac_f32_16x16x64_bf16 v[68:71], v[44:47], a[8:15], v8\n"
"v_smfmac_f32_16x16x64_bf16 v[72:75], v[44:47], a[64:71], v8\nv_smfmac_f32_16x16x64_bf16 v[76:79], v[44:47], a[120:127], v8\nv_smfmac_f32_16x16x64_bf16 v[80:83], v[44:47], a[176:183], v8\nv_add_f32 v219, v68, v69\n"
"v_add_f32 v220, v70, v71\nv_cvt_pk_bf16_f32 v156, v219, v220\nv_add_f32 v219, v72, v73\nv_add_f32 v220, v74, v75\n"
"v_cvt_pk_bf16_f32 v157, v219, v220\nv_add_f32 v219, v76, v77\nv_add_f32 v220, v78, v79\nv_cvt_pk_bf16_f32 v158, v219, v220\n"
"v_add_f32 v219, v80, v81\nv_add_f32 v220, v82, v83\nv_cvt_pk_bf16_f32 v159, v219, v220\nbuffer_store_dwordx4 v[156:159], v9, s[20:23], s51 offen sc1\n"
"s_add_u32 s47, s33, 65\nv_mov_b32 v219, s47\ns_waitcnt vmcnt(0)\nbuffer_store_dword v219, v18, s[8:11], 0 offen sc1\n"
"s_branch end_p1\nretry1_p1:\ns_add_u32 s53, s53, 256\ns_add_u32 s49, s49, 1\n"
"s_cmp_gt_u32 s49, 65536\ns_cbranch_scc1 end_p1\ns_add_u32 s47, s40, 1\nv_mov_b32 v221, s47\n"
"ds_write_b32 v222, v221\nrec_p1:\ns_sleep 4\nv_mov_b32 v68, 0\n"
"v_mov_b32 v69, 0\nv_mov_b32 v70, 0\nv_mov_b32 v71, 0\nv_mov_b32 v72, 0\n"
"v_mov_b32 v73, 0\nv_mov_b32 v74, 0\nv_mov_b32 v75, 0\nv_mov_b32 v76, 0\n"
"v_mov_b32 v77, 0\nv_mov_b32 v78, 0\nv_mov_b32 v79, 0\nv_mov_b32 v80, 0\n"
"v_mov_b32 v81, 0\nv_mov_b32 v82, 0\nv_mov_b32 v83, 0\nv_mov_b32 v218, 0\n"
"s_branch top2_p1\nadopt_p1:\nbuffer_load_dwordx4 v[164:167], v28, s[8:11], 0 offen sc1\ns_lshl_b32 s52, s40, 26\n"
"s_or_b32 s47, s52, 0x3ffffff\ns_waitcnt vmcnt(0)\nv_cmp_ge_u32 vcc, s47, v164\ns_nop 1\n"
"v_cndmask_b32 v164, 0, v164, vcc\nv_cmp_ge_u32 vcc, s47, v165\ns_nop 1\nv_cndmask_b32 v165, 0, v165, vcc\n"
"v_cmp_ge_u32 vcc, s47, v166\ns_nop 1\nv_cndmask_b32 v166, 0, v166, vcc\nv_cmp_ge_u32 vcc, s47, v167\n"
"s_nop 1\nv_cndmask_b32 v167, 0, v167, vcc\nv_max3_u32 v221, v164, v165, v166\nv_max_u32 v221, v221, v167\n"
"s_nop 1\nv_max_u32_dpp v221, v221, v221 row_ror:8 row_mask:0xf bank_mask:0xf\ns_nop 1\nv_max_u32_dpp v221, v221, v221 row_ror:4 row_mask:0xf bank_mask:0xf\n"
"s_nop 1\nv_max_u32_dpp v221, v221, v221 row_ror:2 row_mask:0xf bank_mask:0xf\ns_nop 1\nv_max_u32_dpp v221, v221, v221 row_ror:1 row_mask:0xf bank_mask:0xf\n"
"s_nop 0\nv_readlane_b32 s47, v221, 0\nv_readlane_b32 s48, v221, 16\ns_max_u32 s48, s48, s47\n"
"s_cmp_le_u32 s48, s52\ns_cbranch_scc1 back_p1\ns_bfe_u32 s52, s48, 1179656\ns_add_u32 s44, s52, s28\n"
"s_mov_b32 s45, 0\ns_and_b32 s46, s48, 255\ns_mov_b32 s92, 0\ns_branch back_p1\n"
"late_p1:\ns_add_u32 s53, s53, 16777216\nv_mov_b32 v84, v100\nv_mov_b32 v85, v101\n"
"v_mov_b32 v86, v102\nv_mov_b32 v87, v103\nv_mov_b32 v88, v104\nv_mov_b32 v89, v105\n"
"v_mov_b32 v90, v106\nv_mov_b32 v91, v107\nv_mov_b32 v92, v108\nv_mov_b32 v93, v109\n"
"v_mov_b32 v94, v110\nv_mov_b32 v95, v111\nv_mov_b32 v96, v112\nv_mov_b32 v97, v113\n"
"v_mov_b32 v98, v114\nv_mov_b32 v99, v115\nv_smfmac_f32_16x16x64_bf16 v[68:71], v[56:59], a[32:39], v8\nv_smfmac_f32_16x16x64_bf16 v[72:75], v[56:59], a[88:95], v8\n"
"v_smfmac_f32_16x16x64_bf16 v[76:79], v[56:59], a[144:151], v8\nv_smfmac_f32_16x16x64_bf16 v[80:83], v[56:59], a[200:207], v8\nv_smfmac_f32_16x16x64_bf16 v[68:71], v[60:63], a[40:47], v8\nv_smfmac_f32_16x16x64_bf16 v[72:75], v[60:63], a[96:103], v8\n"
"v_smfmac_f32_16x16x64_bf16 v[76:79], v[60:63], a[152:159], v8\nv_smfmac_f32_16x16x64_bf16 v[80:83], v[60:63], a[208:215], v8\nv_smfmac_f32_16x16x64_bf16 v[68:71], v[64:67], a[48:55], v8\nv_smfmac_f32_16x16x64_bf16 v[72:75], v[64:67], a[104:111], v8\n"
"v_smfmac_f32_16x16x64_bf16 v[76:79], v[64:67], a[160:167], v8\nv_smfmac_f32_16x16x64_bf16 v[80:83], v[64:67], a[216:223], v8\nv_smfmac_f32_16x16x64_bf16 v[68:71], v[40:43], a[0:7], v8\nv_smfmac_f32_16x16x64_bf16 v[72:75], v[40:43], a[56:63], v8\n"
"v_smfmac_f32_16x16x64_bf16 v[76:79], v[40:43], a[112:119], v8\nv_smfmac_f32_16x16x64_bf16 v[80:83], v[40:43], a[168:175], v8\nv_smfmac_f32_16x16x64_bf16 v[68:71], v[44:47], a[8:15], v8\nv_smfmac_f32_16x16x64_bf16 v[72:75], v[44:47], a[64:71], v8\n"
"v_smfmac_f32_16x16x64_bf16 v[76:79], v[44:47], a[120:127], v8\nv_smfmac_f32_16x16x64_bf16 v[80:83], v[44:47], a[176:183], v8\nv_add_f32 v219, v68, v69\nv_add_f32 v220, v70, v71\n"
"v_cvt_pk_bf16_f32 v156, v219, v220\nv_add_f32 v219, v72, v73\nv_add_f32 v220, v74, v75\nv_cvt_pk_bf16_f32 v157, v219, v220\n"
"v_add_f32 v219, v76, v77\nv_add_f32 v220, v78, v79\nv_cvt_pk_bf16_f32 v158, v219, v220\nv_add_f32 v219, v80, v81\n"
"v_add_f32 v220, v82, v83\nv_cvt_pk_bf16_f32 v159, v219, v220\nbuffer_load_dwordx4 v[100:103], v9, s[16:19], s50 offen offset:2048 sc1\nbuffer_load_dwordx4 v[104:107], v9, s[16:19], s50 offen offset:3072 sc1\n"
"buffer_load_dwordx4 v[108:111], v9, s[16:19], s50 offen offset:0 sc1\nbuffer_load_dwordx4 v[112:115], v9, s[16:19], s50 offen offset:1024 sc1\ns_add_u32 s50, s50, 16384\nbuffer_store_dwordx4 v[156:159], v9, s[20:23], s51 offen sc1\n"
"s_branch spin_p1\nslow_p1:\nv_max3_u32 v218, v124, v125, v126\nv_max_u32 v218, v218, v127\n"
"v_max3_u32 v218, v218, v128, v129\nv_max3_u32 v218, v218, v130, v131\nv_max3_u32 v218, v218, v116, v117\nv_max3_u32 v218, v218, v118, v119\n"
"v_max3_u32 v218, v218, v120, v121\nv_max3_u32 v218, v218, v122, v123\nv_cmp_eq_u32 vcc, -1, v218\ns_cbranch_vccz exact_p1\n"
"s_branch spin_p1\nspin2_p1:\ns_sleep 3\nspin_p1:\n"
"s_add_u32 s53, s53, 1\ns_add_u32 s49, s49, 1\ns_cmp_gt_u32 s49, 65536\ns_cbranch_scc1 end_p1\n"
"buffer_load_dwordx4 v[116:119], v20, s[8:11], s41 offen offset:1024 sc1\nbuffer_load_dwordx4 v[120:123], v20, s[8:11], s41 offen offset:0 sc1\nbuffer_load_dwordx4 v[124:127], v21, s[8:11], s41 offen offset:1024 sc1\nbuffer_load_dwordx4 v[128:131], v21, s[8:11], s41 offen offset:0 sc1\n"
"s_waitcnt vmcnt(0)\nv_max3_u32 v218, v124, v125, v126\nv_max_u32 v218, v218, v127\nv_max3_u32 v218, v218, v128, v129\n"
"v_max3_u32 v218, v218, v130, v131\nv_max3_u32 v218, v218, v116, v117\nv_max3_u32 v218, v218, v118, v119\nv_max3_u32 v218, v218, v120, v121\n"
"v_max3_u32 v218, v218, v122, v123\nv_cmp_eq_u32 vcc, -1, v218\ns_cbranch_vccnz spin2_p1\ns_memrealtime s[54:55]\n"
"s_add_u32 s52, s40, 1\ns_lshl_b32 s52, s52, 26\ns_waitcnt lgkmcnt(0)\ns_sub_u32 s47, s54, s28\n"
"s_add_u32 s47, s47, s38\ns_min_u32 s47, s47, 0x3ffff\ns_lshl_b32 s47, s47, 8\ns_or_b32 s47, s47, s52\n"
"s_and_b32 s48, s46, 15\ns_add_u32 s52, s48, 0\ns_min_u32 s52, s52, 15\ns_or_b32 s47, s47, s52\n"
"s_add_u32 s48, s48, 0\ns_min_u32 s48, s48, s52\ns_lshl_b32 s48, s48, 4\ns_and_b32 s52, s46, 0xf0\n"
"s_cmp_ge_u32 s92, 99\ns_cselect_b32 s48, s48, s52\ns_or_b32 s47, s47, s48\nv_mov_b32 v225, s47\n"
"buffer_store_dword v225, v19, s[8:11], 0 offen sc1\ns_add_u32 s47, s40, 2\nv_mov_b32 v221, s47\nds_write_b32 v222, v221\n"
"v_mfma_f32_16x16x32_bf16 v[132:135], v[84:87], v[116:119], 0\nv_mfma_f32_16x16x32_bf16 v[136:139], v[88:91], v[116:119], 0\nv_mfma_f32_16x16x32_bf16 v[148:151], v[176:179], v[116:119], 0\nv_mfma_f32_16x16x32_bf16 v[140:143], v[92:95], v[120:123], 0\n"
"v_mfma_f32_16x16x32_bf16 v[144:147], v[96:99], v[120:123], 0\nv_mfma_f32_16x16x32_bf16 v[148:151], v[180:183], v[120:123], v[148:151]\nv_cndmask_b32 v132, v136, v132, s[62:63]\nv_cndmask_b32 v133, v137, v133, s[62:63]\n"
"v_cndmask_b32 v134, v138, v134, s[62:63]\nv_cndmask_b32 v135, v139, v135, s[62:63]\nv_mfma_f32_16x16x32_f16 v[152:155], v[168:171], v[124:127], 0\nv_mfma_f32_16x16x32_f16 v[152:155], v[172:175], v[128:131], v[152:155]\n"
"v_cndmask_b32 v140, v144, v140, s[62:63]\nv_cndmask_b32 v141, v145, v141, s[62:63]\nv_cndmask_b32 v142, v146, v142, s[62:63]\nv_cndmask_b32 v143, v147, v143, s[62:63]\n"
"s_nop 2\nv_exp_f32 v152, v152\nv_exp_f32 v153, v153\nv_exp_f32 v154, v154\n"
"v_exp_f32 v155, v155\nv_cvt_pk_bf16_f32 v200, v152, v153\nv_cvt_pk_bf16_f32 v201, v154, v155\nv_and_b32 v160, v200, v223\n"
"v_and_b32 v161, v200, v224\nv_mfma_f32_16x16x16_bf16 v[148:151], v[214:215], v[200:201], v[148:151]\nv_and_b32 v162, v201, v223\nv_and_b32 v163, v201, v224\n"
"v_mfma_f32_16x16x16_bf16 v[132:135], v[156:157], v[160:161], v[132:135]\ns_nop 0\nv_mfma_f32_16x16x16_bf16 v[140:143], v[158:159], v[162:163], v[140:143]\nv_rcp_f32 v202, v148\n"
"v_rcp_f32 v203, v149\nv_mul_f32 v132, v132, v202\nv_mul_f32 v133, v133, v202\nv_mul_f32 v134, v134, v202\n"
"v_mul_f32 v135, v135, v202\nv_fmac_f32 v132, v140, v203\nv_fmac_f32 v133, v141, v203\nv_fmac_f32 v134, v142, v203\n"
"v_fmac_f32 v135, v143, v203\nv_add_f32_dpp v204, v132, v132 row_ror:8 row_mask:0xf bank_mask:0x3\nv_add_f32_dpp v205, v133, v133 row_ror:8 row_mask:0xf bank_mask:0x3\nv_add_f32_dpp v204, v134, v134 row_ror:8 row_mask:0xf bank_mask:0xc\n"
"v_add_f32_dpp v205, v135, v135 row_ror:8 row_mask:0xf bank_mask:0xc\nv_subrev_u32 v219, 0x2f800000, v148\nv_subrev_u32 v220, 0x2f800000, v149\nv_max_u32 v219, v219, v220\n"
"v_cmp_le_u32 vcc, 0x20000000, v219\nds_write_b64 v22, v[204:205]\ns_cbranch_vccz join_p1\nexact_p1:\n"
"s_add_u32 s53, s53, 65536\nex0_p1:\ns_add_u32 s47, s41, 4096\nbuffer_load_dwordx2 v[184:185], v24, s[8:11], s47 offen offset:0 sc1\n"
"buffer_load_dwordx2 v[186:187], v24, s[8:11], s47 offen offset:512 sc1\nbuffer_load_dwordx2 v[188:189], v24, s[8:11], s47 offen offset:1024 sc1\nbuffer_load_dwordx2 v[190:191], v24, s[8:11], s47 offen offset:1536 sc1\nbuffer_load_dwordx4 v[192:195], v25, s[8:11], s47 offen sc1\n"
"buffer_load_dwordx4 v[196:199], v25, s[8:11], s47 offen offset:16 sc1\nv_mov_b32 v218, 0\ns_waitcnt vmcnt(0)\nv_max3_u32 v218, v218, v184, v185\n"
"v_max3_u32 v218, v218, v186, v187\nv_max3_u32 v218, v218, v188, v189\nv_max3_u32 v218, v218, v190, v191\nv_max3_u32 v218, v218, v192, v193\n"
"v_max3_u32 v218, v218, v194, v195\nv_max3_u32 v218, v218, v196, v197\nv_max3_u32 v218, v218, v198, v199\nv_cmp_eq_u32 vcc, -1, v218\n"
"s_cbranch_vccz exok0_p1\ns_add_u32 s53, s53, 0\ns_add_u32 s49, s49, 1\ns_cmp_gt_u32 s49, 65536\n"
"s_cbranch_scc1 end_p1\ns_branch ex0_p1\nexok0_p1:\nv_add_f32 v184, v184, v186\n"
"v_add_f32 v188, v188, v190\nv_add_f32 v184, v184, v188\nv_add_f32 v185, v185, v187\nv_add_f32 v189, v189, v191\n"
"v_add_f32 v185, v185, v189\nv_max_f32 v225, v184, v185\nv_max3_f32 v225, v225, v192, v193\nv_max3_f32 v225, v225, v194, v195\n"
"v_max3_f32 v225, v225, v196, v197\nv_max3_f32 v225, v225, v198, v199\nv_mov_b32 v219, v225\ns_nop 1\n"
"v_permlane16_swap_b32 v225, v219\nv_max_f32 v225, v225, v219\nv_mov_b32 v219, v225\ns_nop 1\n"
"v_permlane32_swap_b32 v225, v219\nv_max_f32 v225, v225, v219\nv_sub_f32 v184, v184, v225\nv_exp_f32 v152, v184\n"
"v_sub_f32 v185, v185, v225\nv_exp_f32 v153, v185\nv_sub_f32 v192, v192, v225\nv_exp_f32 v192, v192\n"
"v_sub_f32 v193, v193, v225\nv_exp_f32 v193, v193\nv_sub_f32 v194, v194, v225\nv_exp_f32 v194, v194\n"
"v_sub_f32 v195, v195, v225\nv_exp_f32 v195, v195\nv_sub_f32 v196, v196, v225\nv_exp_f32 v196, v196\n"
"v_sub_f32 v197, v197, v225\nv_exp_f32 v197, v197\nv_sub_f32 v198, v198, v225\nv_exp_f32 v198, v198\n"
"v_sub_f32 v199, v199, v225\nv_exp_f32 v199, v199\ns_nop 0\nv_cvt_pk_bf16_f32 v116, v192, v193\n"
"v_cvt_pk_bf16_f32 v117, v194, v195\nv_cvt_pk_bf16_f32 v118, v196, v197\nv_cvt_pk_bf16_f32 v119, v198, v199\nex1_p1:\n"
"s_add_u32 s47, s41, 0\nbuffer_load_dwordx2 v[184:185], v24, s[8:11], s47 offen offset:0 sc1\nbuffer_load_dwordx2 v[186:187], v24, s[8:11], s47 offen offset:512 sc1\nbuffer_load_dwordx2 v[188:189], v24, s[8:11], s47 offen offset:1024 sc1\n"
"buffer_load_dwordx2 v[190:191], v24, s[8:11], s47 offen offset:1536 sc1\nbuffer_load_dwordx4 v[192:195], v25, s[8:11], s47 offen sc1\nbuffer_load_dwordx4 v[196:199], v25, s[8:11], s47 offen offset:16 sc1\nv_mov_b32 v218, 0\n"
"s_waitcnt vmcnt(0)\nv_max3_u32 v218, v218, v184, v185\nv_max3_u32 v218, v218, v186, v187\nv_max3_u32 v218, v218, v188, v189\n"
"v_max3_u32 v218, v218, v190, v191\nv_max3_u32 v218, v218, v192, v193\nv_max3_u32 v218, v218, v194, v195\nv_max3_u32 v218, v218, v196, v197\n"
"v_max3_u32 v218, v218, v198, v199\nv_cmp_eq_u32 vcc, -1, v218\ns_cbranch_vccz exok1_p1\ns_add_u32 s53, s53, 0\n"
"s_add_u32 s49, s49, 1\ns_cmp_gt_u32 s49, 65536\ns_cbranch_scc1 end_p1\ns_branch ex1_p1\n"
"exok1_p1:\nv_add_f32 v184, v184, v186\nv_add_f32 v188, v188, v190\nv_add_f32 v184, v184, v188\n"
"v_add_f32 v185, v185, v187\nv_add_f32 v189, v189, v191\nv_add_f32 v185, v185, v189\nv_max_f32 v225, v184, v185\n"
"v_max3_f32 v225, v225, v192, v193\nv_max3_f32 v225, v225, v194, v195\nv_max3_f32 v225, v225, v196, v197\nv_max3_f32 v225, v225, v198, v199\n"
"v_mov_b32 v219, v225\ns_nop 1\nv_permlane16_swap_b32 v225, v219\nv_max_f32 v225, v225, v219\n"
"v_mov_b32 v219, v225\ns_nop 1\nv_permlane32_swap_b32 v225, v219\nv_max_f32 v225, v225, v219\n"
"v_sub_f32 v184, v184, v225\nv_exp_f32 v154, v184\nv_sub_f32 v185, v185, v225\nv_exp_f32 v155, v185\n"
"v_sub_f32 v192, v192, v225\nv_exp_f32 v192, v192\nv_sub_f32 v193, v193, v225\nv_exp_f32 v193, v193\n"
"v_sub_f32 v194, v194, v225\nv_exp_f32 v194, v194\nv_sub_f32 v195, v195, v225\nv_exp_f32 v195, v195\n"
"v_sub_f32 v196, v196, v225\nv_exp_f32 v196, v196\nv_sub_f32 v197, v197, v225\nv_exp_f32 v197, v197\n"
"v_sub_f32 v198, v198, v225\nv_exp_f32 v198, v198\nv_sub_f32 v199, v199, v225\nv_exp_f32 v199, v199\n"
"s_nop 0\nv_cvt_pk_bf16_f32 v120, v192, v193\nv_cvt_pk_bf16_f32 v121, v194, v195\nv_cvt_pk_bf16_f32 v122, v196, v197\n"
"v_cvt_pk_bf16_f32 v123, v198, v199\ns_memrealtime s[54:55]\ns_add_u32 s52, s40, 1\ns_lshl_b32 s52, s52, 26\n"
"s_waitcnt lgkmcnt(0)\ns_sub_u32 s47, s54, s28\ns_add_u32 s47, s47, s38\ns_min_u32 s47, s47, 0x3ffff\n"
"s_lshl_b32 s47, s47, 8\ns_or_b32 s47, s47, s52\ns_and_b32 s48, s46, 15\ns_add_u32 s52, s48, 0\n"
"s_min_u32 s52, s52, 15\ns_or_b32 s47, s47, s52\ns_add_u32 s48, s48, 0\ns_min_u32 s48, s48, s52\n"
"s_lshl_b32 s48, s48, 4\ns_and_b32 s52, s46, 0xf0\ns_cmp_ge_u32 s92, 99\ns_cselect_b32 s48, s48, s52\n"
"s_or_b32 s47, s47, s48\nv_mov_b32 v225, s47\nbuffer_store_dword v225, v19, s[8:11], 0 offen sc1\ns_add_u32 s47, s40, 2\n"
"v_mov_b32 v221, s47\nds_write_b32 v222, v221\nv_mfma_f32_16x16x32_bf16 v[132:135], v[84:87], v[116:119], 0\nv_mfma_f32_16x16x32_bf16 v[136:139], v[88:91], v[116:119], 0\n"
"v_mfma_f32_16x16x32_bf16 v[148:151], v[176:179], v[116:119], 0\nv_mfma_f32_16x16x32_bf16 v[140:143], v[92:95], v[120:123], 0\nv_mfma_f32_16x16x32_bf16 v[144:147], v[96:99], v[120:123], 0\nv_mfma_f32_16x16x32_bf16 v[148:151], v[180:183], v[120:123], v[148:151]\n"
"v_cndmask_b32 v132, v136, v132, s[62:63]\nv_cndmask_b32 v133, v137, v133, s[62:63]\nv_cndmask_b32 v134, v138, v134, s[62:63]\nv_cndmask_b32 v135, v139, v135, s[62:63]\n"
"s_nop 0\nv_cndmask_b32 v140, v144, v140, s[62:63]\nv_cndmask_b32 v141, v145, v141, s[62:63]\nv_cndmask_b32 v142, v146, v142, s[62:63]\n"
"v_cndmask_b32 v143, v147, v143, s[62:63]\nv_cvt_pk_bf16_f32 v200, v152, v153\nv_cvt_pk_bf16_f32 v201, v154, v155\nv_and_b32 v160, v200, v223\n"
"v_and_b32 v161, v200, v224\nv_mfma_f32_16x16x16_bf16 v[148:151], v[214:215], v[200:201], v[148:151]\nv_and_b32 v162, v201, v223\nv_and_b32 v163, v201, v224\n"
"v_mfma_f32_16x16x16_bf16 v[132:135], v[156:157], v[160:161], v[132:135]\ns_nop 0\nv_mfma_f32_16x16x16_bf16 v[140:143], v[158:159], v[162:163], v[140:143]\nv_rcp_f32 v202, v148\n"
"v_rcp_f32 v203, v149\nv_mul_f32 v132, v132, v202\nv_mul_f32 v133, v133, v202\nv_mul_f32 v134, v134, v202\n"
"v_mul_f32 v135, v135, v202\nv_fmac_f32 v132, v140, v203\nv_fmac_f32 v133, v141, v203\nv_fmac_f32 v134, v142, v203\n"
"v_fmac_f32 v135, v143, v203\nv_add_f32_dpp v204, v132, v132 row_ror:8 row_mask:0xf bank_mask:0x3\nv_add_f32_dpp v205, v133, v133 row_ror:8 row_mask:0xf bank_mask:0x3\nv_add_f32_dpp v204, v134, v134 row_ror:8 row_mask:0xf bank_mask:0xc\n"
"v_add_f32_dpp v205, v135, v135 row_ror:8 row_mask:0xf bank_mask:0xc\nds_write_b64 v22, v[204:205]\ns_branch join_p1\nend_p1:\n"
"s_branch fk_done\nfk_p2:\ns_mov_b32 s40, 0\ns_mov_b32 s41, 0\n"
"s_mov_b32 s42, 262144\ns_mov_b32 s43, 393216\ns_mov_b32 s44, s28\ns_mov_b32 s45, 0\n"
"s_mov_b32 s46, 0\ns_mov_b32 s92, 0\ns_mov_b32 s49, 0\ns_mov_b32 s53, 0\n"
"s_mov_b32 s50, s35\ns_mov_b32 s51, s36\nv_mov_b32 v222, 24576\ns_mov_b32 s62, 0x00ff00ff\n"
"s_mov_b32 s63, 0x00ff00ff\nv_cndmask_b32 v223, 0, -1, s[62:63]\nv_not_b32 v224, v223\nv_bfe_u32 v219, v9, 4, 1\n"
"v_lshlrev_b32 v220, 4, v219\nv_mov_b32 v218, 0x3c00\nv_lshlrev_b32 v218, v220, v218\nv_bfe_u32 v220, v9, 5, 3\n"
"v_cmp_eq_u32 vcc, 0, v220\ns_nop 1\nv_cndmask_b32 v168, 0, v218, vcc\nv_cmp_eq_u32 vcc, 2, v220\n"
"s_nop 1\nv_cndmask_b32 v169, 0, v218, vcc\nv_cmp_eq_u32 vcc, 4, v220\ns_nop 1\n"
"v_cndmask_b32 v170, 0, v218, vcc\nv_cmp_eq_u32 vcc, 6, v220\ns_nop 1\nv_cndmask_b32 v171, 0, v218, vcc\n"
"v_cmp_eq_u32 vcc, 1, v220\ns_nop 1\nv_cndmask_b32 v172, 0, v218, vcc\nv_cmp_eq_u32 vcc, 3, v220\n"
"s_nop 1\nv_cndmask_b32 v173, 0, v218, vcc\nv_cmp_eq_u32 vcc, 5, v220\ns_nop 1\n"
"v_cndmask_b32 v174, 0, v218, vcc\nv_cmp_eq_u32 vcc, 7, v220\ns_nop 1\nv_cndmask_b32 v175, 0, v218, vcc\n"
"v_mov_b32 v218, 0x3f803f80\nv_cmp_eq_u32 vcc, 0, v219\ns_nop 1\nv_cndmask_b32 v176, 0, v218, vcc\n"
"v_cndmask_b32 v177, 0, v218, vcc\nv_cndmask_b32 v178, 0, v218, vcc\nv_cndmask_b32 v179, 0, v218, vcc\nv_cndmask_b32 v214, 0, v218, vcc\n"
"v_cmp_eq_u32 vcc, 1, v219\ns_nop 1\nv_cndmask_b32 v180, 0, v218, vcc\nv_cndmask_b32 v181, 0, v218, vcc\n"
"v_cndmask_b32 v182, 0, v218, vcc\nv_cndmask_b32 v183, 0, v218, vcc\nv_cndmask_b32 v215, 0, v218, vcc\ns_add_u32 s47, s34, 0\n"
"buffer_load_dwordx4 a[0:3], v9, s[12:15], s47 offen sc1\nbuffer_load_dwordx4 a[4:7], v9, s[12:15], s47 offen offset:1024 sc1\ns_add_u32 s47, s34, 2048\nbuffer_load_dwordx4 a[8:11], v9, s[12:15], s47 offen sc1\n"
"buffer_load_dwordx4 a[12:15], v9, s[12:15], s47 offen offset:1024 sc1\ns_add_u32 s47, s34, 4096\nbuffer_load_dwordx4 a[16:19], v9, s[12:15], s47 offen sc1\nbuffer_load_dwordx4 a[20:23], v9, s[12:15], s47 offen offset:1024 sc1\n"
"s_add_u32 s47, s34, 6144\nbuffer_load_dwordx4 a[24:27], v9, s[12:15], s47 offen sc1\nbuffer_load_dwordx4 a[28:31], v9, s[12:15], s47 offen offset:1024 sc1\ns_add_u32 s47, s34, 8192\n"
"buffer_load_dwordx4 a[32:35], v9, s[12:15], s47 offen sc1\nbuffer_load_dwordx4 a[36:39], v9, s[12:15], s47 offen offset:1024 sc1\ns_add_u32 s47, s34, 10240\nbuffer_load_dwordx4 a[40:43], v9, s[12:15], s47 offen sc1\n"
"buffer_load_dwordx4 a[44:47], v9, s[12:15], s47 offen offset:1024 sc1\ns_add_u32 s47, s34, 12288\nbuffer_load_dwordx4 a[48:51], v9, s[12:15], s47 offen sc1\nbuffer_load_dwordx4 a[52:55], v9, s[12:15], s47 offen offset:1024 sc1\n"
"s_waitcnt vmcnt(14)\ns_add_u32 s47, s34, 14336\nbuffer_load_dwordx4 a[56:59], v9, s[12:15], s47 offen sc1\nbuffer_load_dwordx4 a[60:63], v9, s[12:15], s47 offen offset:1024 sc1\n"
"s_add_u32 s47, s34, 16384\nbuffer_load_dwordx4 a[64:67], v9, s[12:15], s47 offen sc1\nbuffer_load_dwordx4 a[68:71], v9, s[12:15], s47 offen offset:1024 sc1\ns_add_u32 s47, s34, 18432\n"
"buffer_load_dwordx4 a[72:75], v9, s[12:15], s47 offen sc1\nbuffer_load_dwordx4 a[76:79], v9, s[12:15], s47 offen offset:1024 sc1\ns_add_u32 s47, s34, 20480\nbuffer_load_dwordx4 a[80:83], v9, s[12:15], s47 offen sc1\n"
"buffer_load_dwordx4 a[84:87], v9, s[12:15], s47 offen offset:1024 sc1\ns_add_u32 s47, s34, 22528\nbuffer_load_dwordx4 a[88:91], v9, s[12:15], s47 offen sc1\nbuffer_load_dwordx4 a[92:95], v9, s[12:15], s47 offen offset:1024 sc1\n"
"s_add_u32 s47, s34, 24576\nbuffer_load_dwordx4 a[96:99], v9, s[12:15], s47 offen sc1\nbuffer_load_dwordx4 a[100:103], v9, s[12:15], s47 offen offset:1024 sc1\ns_add_u32 s47, s34, 26624\n"
"buffer_load_dwordx4 a[104:107], v9, s[12:15], s47 offen sc1\nbuffer_load_dwordx4 a[108:111], v9, s[12:15], s47 offen offset:1024 sc1\ns_waitcnt vmcnt(14)\ns_add_u32 s47, s34, 28672\n"
"buffer_load_dwordx4 a[112:115], v9, s[12:15], s47 offen sc1\nbuffer_load_dwordx4 a[116:119], v9, s[12:15], s47 offen offset:1024 sc1\ns_add_u32 s47, s34, 30720\nbuffer_load_dwordx4 a[120:123], v9, s[12:15], s47 offen sc1\n"
"buffer_load_dwordx4 a[124:127], v9, s[12:15], s47 offen offset:1024 sc1\ns_add_u32 s47, s34, 32768\nbuffer_load_dwordx4 a[128:131], v9, s[12:15], s47 offen sc1\nbuffer_load_dwordx4 a[132:135], v9, s[12:15], s47 offen offset:1024 sc1\n"
"s_add_u32 s47, s34, 34816\nbuffer_load_dwordx4 a[136:139], v9, s[12:15], s47 offen sc1\nbuffer_load_dwordx4 a[140:143], v9, s[12:15], s47 offen offset:1024 sc1\ns_add_u32 s47, s34, 36864\n"
"buffer_load_dwordx4 a[144:147], v9, s[12:15], s47 offen sc1\nbuffer_load_dwordx4 a[148:151], v9, s[12:15], s47 offen offset:1024 sc1\ns_add_u32 s47, s34, 38912\nbuffer_load_dwordx4 a[152:155], v9, s[12:15], s47 offen sc1\n"
"buffer_load_dwordx4 a[156:159], v9, s[12:15], s47 offen offset:1024 sc1\ns_add_u32 s47, s34, 40960\nbuffer_load_dwordx4 a[160:163], v9, s[12:15], s47 offen sc1\nbuffer_load_dwordx4 a[164:167], v9, s[12:15], s47 offen offset:1024 sc1\n"
"s_waitcnt vmcnt(14)\ns_add_u32 s47, s34, 43008\nbuffer_load_dwordx4 a[168:171], v9, s[12:15], s47 offen sc1\nbuffer_load_dwordx4 a[172:175], v9, s[12:15], s47 offen offset:1024 sc1\n"
"s_add_u32 s47, s34, 45056\nbuffer_load_dwordx4 a[176:179], v9, s[12:15], s47 offen sc1\nbuffer_load_dwordx4 a[180:183], v9, s[12:15], s47 offen offset:1024 sc1\ns_add_u32 s47, s34, 47104\n"
"buffer_load_dwordx4 a[184:187], v9, s[12:15], s47 offen sc1\nbuffer_load_dwordx4 a[188:191], v9, s[12:15], s47 offen offset:1024 sc1\ns_add_u32 s47, s34, 49152\nbuffer_load_dwordx4 a[192:195], v9, s[12:15], s47 offen sc1\n"
"buffer_load_dwordx4 a[196:199], v9, s[12:15], s47 offen offset:1024 sc1\ns_add_u32 s47, s34, 51200\nbuffer_load_dwordx4 a[200:203], v9, s[12:15], s47 offen sc1\nbuffer_load_dwordx4 a[204:207], v9, s[12:15], s47 offen offset:1024 sc1\n"
"s_add_u32 s47, s34, 53248\nbuffer_load_dwordx4 a[208:211], v9, s[12:15], s47 offen sc1\nbuffer_load_dwordx4 a[212:215], v9, s[12:15], s47 offen offset:1024 sc1\ns_add_u32 s47, s34, 55296\n"
"buffer_load_dwordx4 a[216:219], v9, s[12:15], s47 offen sc1\nbuffer_load_dwordx4 a[220:223], v9, s[12:15], s47 offen offset:1024 sc1\ns_waitcnt vmcnt(14)\nbuffer_load_dword v218, v11, s[8:11], 0 offen sc1\n"
"s_waitcnt vmcnt(0)\nfl_p2:\ns_add_u32 s47, s40, 2\ns_min_u32 s47, s47, 64\n"
"s_add_u32 s47, s47, s33\nv_subrev_u32 v218, s47, v218\nv_cmp_lt_u32 vcc, 64, v218\ns_cbranch_vccz flok_p2\n"
"s_add_u32 s53, s53, 0\ns_add_u32 s49, s49, 1\ns_cmp_gt_u32 s49, 65536\ns_cbranch_scc1 end_p2\n"
"s_sleep 2\nbuffer_load_dword v218, v11, s[8:11], 0 offen sc1\ns_waitcnt vmcnt(0)\ns_branch fl_p2\n"
"flok_p2:\nbuffer_load_dwordx4 v[100:103], v9, s[16:19], s50 offen offset:0 sc1\nbuffer_load_dwordx4 v[104:107], v9, s[16:19], s50 offen offset:1024 sc1\nbuffer_load_dwordx4 v[108:111], v9, s[16:19], s50 offen offset:2048 sc1\n"
"buffer_load_dwordx4 v[112:115], v9, s[16:19], s50 offen offset:3072 sc1\ns_add_u32 s50, s50, 16384\ns_waitcnt vmcnt(0)\ns_barrier\n"
".p2align 6\ntop_p2:\nv_mov_b32 v68, 0\nv_mov_b32 v69, 0\n"
"v_mov_b32 v70, 0\nv_mov_b32 v71, 0\nv_mov_b32 v72, 0\nv_mov_b32 v73, 0\n"
"v_mov_b32 v74, 0\nv_mov_b32 v75, 0\nv_mov_b32 v76, 0\nv_mov_b32 v77, 0\n"
"v_mov_b32 v78, 0\nv_mov_b32 v79, 0\nv_mov_b32 v80, 0\nv_mov_b32 v81, 0\n"
"v_mov_b32 v82, 0\nv_mov_b32 v83, 0\nv_mov_b32 v218, 0\ns_add_u32 s52, s44, s37\n"
"m_p2:\ns_memrealtime s[54:55]\ns_waitcnt lgkmcnt(0)\ns_sub_u32 s47, s54, s52\n"
"s_cmp_lt_i32 s47, 0\ns_cbranch_scc1 m_p2\ntop2_p2:\nbuffer_load_dwordx4 v[56:59], v10, s[8:11], s43 offen offset:0 sc1\n"
"buffer_load_dwordx4 v[60:63], v10, s[8:11], s43 offen offset:1024 sc1\ns_waitcnt vmcnt(1)\nds_write_b128 v9, v[56:59] offset:4096\nv_max3_u32 v218, v218, v56, v57\n"
"v_max3_u32 v218, v218, v58, v59\ns_waitcnt vmcnt(0)\nds_write_b128 v9, v[60:63] offset:5120\nv_max3_u32 v218, v218, v60, v61\n"
"v_max3_u32 v218, v218, v62, v63\nv_cmp_eq_u32 vcc, -1, v218\nv_smfmac_f32_16x16x64_bf16 v[68:71], v[56:59], a[32:39], v8\nv_smfmac_f32_16x16x64_bf16 v[72:75], v[56:59], a[88:95], v8\n"
"s_cbranch_vccnz retry1_p2\ns_waitcnt lgkmcnt(0)\ns_barrier\nds_read_b32 v221, v222\n"
"ds_read_b128 v[64:67], v9 offset:6144\nds_read_b128 v[40:43], v9 offset:0\nds_read_b128 v[44:47], v9 offset:1024\nds_read_b128 v[48:51], v9 offset:2048\n"
"ds_read_b128 v[52:55], v9 offset:3072\nv_smfmac_f32_16x16x64_bf16 v[76:79], v[56:59], a[144:151], v8\nv_smfmac_f32_16x16x64_bf16 v[80:83], v[56:59], a[200:207], v8\nv_smfmac_f32_16x16x64_bf16 v[68:71], v[60:63], a[40:47], v8\n"
"v_smfmac_f32_16x16x64_bf16 v[72:75], v[60:63], a[96:103], v8\nv_smfmac_f32_16x16x64_bf16 v[76:79], v[60:63], a[152:159], v8\nv_smfmac_f32_16x16x64_bf16 v[80:83], v[60:63], a[208:215], v8\ns_waitcnt lgkmcnt(0)\n"
"v_readfirstlane_b32 s47, v221\ns_sub_u32 s47, s47, s40\ns_cmp_eq_u32 s47, 1\ns_cbranch_scc1 adopt_p2\n"
"back_p2:\ns_cmp_eq_u32 s40, 63\ns_cbranch_scc1 last_p2\ns_and_b32 s47, s46, 15\n"
"s_mul_i32 s47, s47, 2\ns_add_u32 s47, s47, s29\ns_add_u32 s47, s47, s45\ns_and_b32 s91, s47, 15\n"
"s_lshr_b32 s47, s47, 4\ns_lshl_b32 s47, s47, 2\ns_add_u32 s90, s44, s47\ns_mul_i32 s48, s91, 6\n"
"s_and_b32 s48, s48, -4\ns_getpc_b64 s[60:61]\nspc_p2:\ns_sub_u32 s48, sgo_p2-spc_p2, s48\n"
"s_add_u32 s60, s60, s48\ns_addc_u32 s61, s61, 0\nsf_p2:\ns_memrealtime s[54:55]\n"
"s_nop 2\ns_memrealtime s[56:57]\ns_nop 2\ns_memrealtime s[58:59]\n"
"s_waitcnt lgkmcnt(4)\ns_sub_u32 s47, s54, s90\ns_add_u32 s48, s47, 12\ns_cmp_ge_u32 s48, 12\n"
"s_cbranch_scc1 so_p2\ns_memrealtime s[54:55]\ns_waitcnt lgkmcnt(4)\ns_cmp_ge_u32 s56, s90\n"
"s_cbranch_scc1 sx_p2\ns_memrealtime s[56:57]\ns_waitcnt lgkmcnt(4)\ns_cmp_ge_u32 s58, s90\n"
"s_cbranch_scc1 sx_p2\ns_memrealtime s[58:59]\ns_waitcnt lgkmcnt(4)\ns_cmp_ge_u32 s54, s90\n"
"s_cbranch_scc1 sx_p2\ns_memrealtime s[54:55]\ns_waitcnt lgkmcnt(4)\ns_cmp_ge_u32 s56, s90\n"
"s_cbranch_scc1 sx_p2\ns_memrealtime s[56:57]\ns_waitcnt lgkmcnt(4)\ns_cmp_ge_u32 s58, s90\n"
"s_cbranch_scc1 sx_p2\ns_memrealtime s[58:59]\ns_waitcnt lgkmcnt(4)\ns_cmp_ge_u32 s54, s90\n"
"s_cbranch_scc1 sx_p2\ns_memrealtime s[54:55]\ns_waitcnt lgkmcnt(4)\ns_cmp_ge_u32 s56, s90\n"
"s_cbranch_scc1 sx_p2\ns_memrealtime s[56:57]\ns_waitcnt lgkmcnt(4)\ns_cmp_ge_u32 s58, s90\n"
"s_cbranch_scc1 sx_p2\ns_memrealtime s[58:59]\ns_waitcnt lgkmcnt(4)\ns_cmp_ge_u32 s54, s90\n"
"s_cbranch_scc1 sx_p2\ns_memrealtime s[54:55]\ns_waitcnt lgkmcnt(4)\ns_cmp_ge_u32 s56, s90\n"
"s_cbranch_scc1 sx_p2\ns_memrealtime s[56:57]\ns_waitcnt lgkmcnt(4)\ns_cmp_ge_u32 s58, s90\n"
"s_cbranch_scc1 sx_p2\ns_memrealtime s[58:59]\ns_waitcnt lgkmcnt(4)\ns_cmp_ge_u32 s54, s90\n"
"s_cbranch_scc1 sx_p2\ns_memrealtime s[54:55]\ns_waitcnt lgkmcnt(4)\ns_cmp_ge_u32 s56, s90\n"
"s_cbranch_scc1 sx_p2\ns_memrealtime s[56:57]\ns_waitcnt lgkmcnt(4)\ns_cmp_ge_u32 s58, s90\n"
"s_cbranch_scc1 sx_p2\ns_memrealtime s[58:59]\ns_waitcnt lgkmcnt(4)\ns_cmp_ge_u32 s54, s90\n"
"s_cbranch_scc1 sx_p2\ns_memrealtime s[54:55]\ns_waitcnt lgkmcnt(4)\ns_cmp_ge_u32 s56, s90\n"
"s_cbranch_scc1 sx_p2\ns_memrealtime s[56:57]\ns_branch sx_p2\nso_p2:\n"
"s_waitcnt lgkmcnt(0)\ns_cmp_lt_i32 s47, 0\ns_cbranch_scc1 sf_p2\ns_cmp_gt_i32 s47, s32\n"
"s_cbranch_scc1 late_p2\ns_branch sgo_p2\nsx_p2:\ns_setpc_b64 s[60:61]\n"
"s_nop 0\ns_nop 0\ns_nop 0\ns_nop 0\n"
"s_nop 0\ns_nop 0\ns_nop 0\ns_nop 0\n"
"s_nop 0\ns_nop 0\ns_nop 0\ns_nop 0\n"
"s_nop 0\ns_nop 0\ns_nop 0\ns_nop 0\n"
"s_nop 0\ns_nop 0\ns_nop 0\ns_nop 0\n"
"s_nop 0\ns_nop 0\ns_nop 0\ns_nop 0\n"
"sgo_p2:\ns_nop 11\nbuffer_load_dwordx4 v[116:119], v20, s[8:11], s41 offen offset:0 sc1\nbuffer_load_dwordx4 v[120:123], v20, s[8:11], s41 offen offset:1024 sc1\n"
"v_smfmac_f32_16x16x64_bf16 v[68:71], v[64:67], a[48:55], v8\nv_mov_b32 v84, v100\nv_smfmac_f32_16x16x64_bf16 v[72:75], v[64:67], a[104:111], v8\nv_mov_b32 v85, v101\n"
"v_smfmac_f32_16x16x64_bf16 v[76:79], v[64:67], a[160:167], v8\nv_mov_b32 v86, v102\nv_smfmac_f32_16x16x64_bf16 v[80:83], v[64:67], a[216:223], v8\nv_mov_b32 v87, v103\n"
"v_smfmac_f32_16x16x64_bf16 v[68:71], v[40:43], a[0:7], v8\nv_mov_b32 v88, v104\nv_smfmac_f32_16x16x64_bf16 v[72:75], v[40:43], a[56:63], v8\nv_mov_b32 v89, v105\n"
"v_smfmac_f32_16x16x64_bf16 v[76:79], v[40:43], a[112:119], v8\nv_mov_b32 v90, v106\nv_smfmac_f32_16x16x64_bf16 v[80:83], v[40:43], a[168:175], v8\nv_mov_b32 v91, v107\n"
"v_smfmac_f32_16x16x64_bf16 v[68:71], v[44:47], a[8:15], v8\nv_mov_b32 v92, v108\nv_smfmac_f32_16x16x64_bf16 v[72:75], v[44:47], a[64:71], v8\nv_mov_b32 v93, v109\n"
"buffer_load_dwordx4 v[124:127], v21, s[8:11], s41 offen offset:0 sc1\nbuffer_load_dwordx4 v[128:131], v21, s[8:11], s41 offen offset:1024 sc1\nv_smfmac_f32_16x16x64_bf16 v[76:79], v[44:47], a[120:127], v8\nv_mov_b32 v94, v110\n"
"v_smfmac_f32_16x16x64_bf16 v[80:83], v[44:47], a[176:183], v8\nv_mov_b32 v95, v111\nv_smfmac_f32_16x16x64_bf16 v[68:71], v[48:51], a[16:23], v8\nv_mov_b32 v96, v112\n"
"v_smfmac_f32_16x16x64_bf16 v[72:75], v[48:51], a[72:79], v8\nv_mov_b32 v97, v113\nv_smfmac_f32_16x16x64_bf16 v[76:79], v[48:51], a[128:135], v8\nv_mov_b32 v98, v114\n"
"v_smfmac_f32_16x16x64_bf16 v[80:83], v[48:51], a[184:191], v8\nv_mov_b32 v99, v115\nv_smfmac_f32_16x16x64_bf16 v[68:71], v[52:55], a[24:31], v8\nv_smfmac_f32_16x16x64_bf16 v[72:75], v[52:55], a[80:87], v8\n"
"v_smfmac_f32_16x16x64_bf16 v[76:79], v[52:55], a[136:143], v8\nv_smfmac_f32_16x16x64_bf16 v[80:83], v[52:55], a[192:199], v8\nv_add_f32 v219, v68, v69\nv_add_f32 v220, v70, v71\n"
"v_cvt_pk_bf16_f32 v156, v219, v220\nv_add_f32 v219, v72, v73\nv_add_f32 v220, v74, v75\nv_cvt_pk_bf16_f32 v157, v219, v220\n"
"v_add_f32 v219, v76, v77\nv_add_f32 v220, v78, v79\nv_cvt_pk_bf16_f32 v158, v219, v220\nv_add_f32 v219, v80, v81\n"
"v_add_f32 v220, v82, v83\nv_cvt_pk_bf16_f32 v159, v219, v220\nbuffer_load_dwordx4 v[100:103], v9, s[16:19], s50 offen offset:0 sc1\nbuffer_load_dwordx4 v[104:107], v9, s[16:19], s50 offen offset:1024 sc1\n"
"buffer_load_dwordx4 v[108:111], v9, s[16:19], s50 offen offset:2048 sc1\nbuffer_load_dwordx4 v[112:115], v9, s[16:19], s50 offen offset:3072 sc1\ns_add_u32 s50, s50, 16384\nbuffer_store_dwordx4 v[156:159], v9, s[20:23], s51 offen sc1\n"
"s_waitcnt vmcnt(8)\nv_mfma_f32_16x16x32_bf16 v[132:135], v[84:87], v[116:119], 0\nv_mfma_f32_16x16x32_bf16 v[136:139], v[88:91], v[116:119], 0\nv_mfma_f32_16x16x32_bf16 v[148:151], v[176:179], v[116:119], 0\n"
"s_waitcnt vmcnt(7)\nv_mfma_f32_16x16x32_bf16 v[140:143], v[92:95], v[120:123], 0\nv_mfma_f32_16x16x32_bf16 v[144:147], v[96:99], v[120:123], 0\nv_mfma_f32_16x16x32_bf16 v[148:151], v[180:183], v[120:123], v[148:151]\n"
"v_cndmask_b32 v132, v136, v132, s[62:63]\nv_cndmask_b32 v133, v137, v133, s[62:63]\nv_cndmask_b32 v134, v138, v134, s[62:63]\nv_cndmask_b32 v135, v139, v135, s[62:63]\n"
"s_waitcnt vmcnt(6)\nv_mfma_f32_16x16x32_f16 v[152:155], v[168:171], v[124:127], 0\ns_waitcnt vmcnt(5)\nv_mfma_f32_16x16x32_f16 v[152:155], v[172:175], v[128:131], v[152:155]\n"
"v_cndmask_b32 v140, v144, v140, s[62:63]\nv_cndmask_b32 v141, v145, v141, s[62:63]\nv_cndmask_b32 v142, v146, v142, s[62:63]\nv_cndmask_b32 v143, v147, v143, s[62:63]\n"
"s_nop 2\nv_exp_f32 v152, v152\nv_exp_f32 v153, v153\nv_exp_f32 v154, v154\n"
"v_exp_f32 v155, v155\nv_cvt_pk_bf16_f32 v200, v152, v153\nv_cvt_pk_bf16_f32 v201, v154, v155\nv_and_b32 v160, v200, v223\n"
"v_and_b32 v161, v200, v224\nv_mfma_f32_16x16x16_bf16 v[148:151], v[214:215], v[200:201], v[148:151]\nv_and_b32 v162, v201, v223\nv_and_b32 v163, v201, v224\n"
"v_mfma_f32_16x16x16_bf16 v[132:135], v[156:157], v[160:161], v[132:135]\ns_nop 0\nv_mfma_f32_16x16x16_bf16 v[140:143], v[158:159], v[162:163], v[140:143]\nv_rcp_f32 v202, v148\n"
"v_rcp_f32 v203, v149\nv_mul_f32 v132, v132, v202\nv_mul_f32 v133, v133, v202\nv_mul_f32 v134, v134, v202\n"
"v_mul_f32 v135, v135, v202\nv_fmac_f32 v132, v140, v203\nv_fmac_f32 v133, v141, v203\nv_fmac_f32 v134, v142, v203\n"
"v_fmac_f32 v135, v143, v203\nv_add_f32_dpp v204, v132, v132 row_ror:8 row_mask:0xf bank_mask:0x3\nv_add_f32_dpp v205, v133, v133 row_ror:8 row_mask:0xf bank_mask:0x3\nv_add_f32_dpp v204, v134, v134 row_ror:8 row_mask:0xf bank_mask:0xc\n"
"v_add_f32_dpp v205, v135, v135 row_ror:8 row_mask:0xf bank_mask:0xc\nv_subrev_u32 v219, 0x2f800000, v148\nv_subrev_u32 v220, 0x2f800000, v149\nv_max_u32 v219, v219, v220\n"
"v_cmp_le_u32 vcc, 0x20000000, v219\nds_write_b64 v22, v[204:205]\ns_cbranch_vccnz slow_p2\njoin_p2:\n"
"v_mov_b32 v219, 0\ns_nop 1\nv_mov_b32 v219, 0\ns_waitcnt lgkmcnt(0)\n"
"s_barrier\nds_read_b64 v[206:207], v23 offset:0\nds_read_b64 v[208:209], v23 offset:1280\nds_read_b64 v[210:211], v23 offset:2560\n"
"ds_read_b64 v[212:213], v23 offset:3840\nds_read_b32 v221, v222\nv_mov_b32 v219, 0\ns_nop 1\n"
"v_mov_b32 v219, 0\ns_nop 1\nv_mov_b32 v219, 0\ns_waitcnt lgkmcnt(3)\n"
"v_pk_add_f32 v[206:207], v[206:207], v[208:209]\ns_waitcnt lgkmcnt(1)\nv_pk_add_f32 v[210:211], v[210:211], v[212:213]\nv_pk_add_f32 v[206:207], v[206:207], v[210:211]\n"
"v_cvt_pk_bf16_f32 v220, v206, v207\nbuffer_store_dword v220, v16, s[8:11], s41 offen sc1\ns_add_u32 s51, s51, 4096\ns_add_u32 s40, s40, 1\n"
"s_mov_b32 s43, s41\ns_add_u32 s41, s41, 131072\ns_and_b32 s41, s41, 524287\ns_xor_b32 s42, s41, 262144\n"
"s_and_b32 s47, s46, 15\ns_mul_i32 s48, s47, 4\ns_add_u32 s45, s45, s48\ns_add_u32 s45, s45, s30\n"
"s_lshr_b32 s48, s45, 4\ns_lshl_b32 s48, s48, 2\ns_add_u32 s44, s44, s48\ns_and_b32 s45, s45, 15\n"
"s_add_u32 s92, s92, 1\ns_lshr_b32 s48, s46, 4\ns_cmp_gt_u32 s47, s48\ns_cselect_b32 s47, 1, 0\n"
"s_and_b32 s48, s92, 1\ns_cselect_b32 s47, 0, s47\ns_sub_u32 s46, s46, s47\ns_waitcnt lgkmcnt(0)\n"
"v_readfirstlane_b32 s47, v221\ns_sub_u32 s47, s47, s40\ns_cmp_eq_u32 s47, 1\ns_cbranch_scc1 rec_p2\n"
"s_branch top_p2\nlast_p2:\nv_smfmac_f32_16x16x64_bf16 v[68:71], v[64:67], a[48:55], v8\nv_smfmac_f32_16x16x64_bf16 v[72:75], v[64:67], a[104:111], v8\n"
"v_smfmac_f32_16x16x64_bf16 v[76:79], v[64:67], a[160:167], v8\nv_smfmac_f32_16x16x64_bf16 v[80:83], v[64:67], a[216:223], v8\nv_smfmac_f32_16x16x64_bf16 v[68:71], v[40:43], a[0:7], v8\nv_smfmac_f32_16x16x64_bf16 v[72:75], v[40:43], a[56:63], v8\n"
"v_smfmac_f32_16x16x64_bf16 v[76:79], v[40:43], a[112:119], v8\nv_smfmac_f32_16x16x64_bf16 v[80:83], v[40:43], a[168:175], v8\nv_smfmac_f32_16x16x64_bf16 v[68:71], v[44:47], a[8:15], v8\nv_smfmac_f32_16x16x64_bf16 v[72:75], v[44:47], a[64:71], v8\n"
"v_smfmac_f32_16x16x64_bf16 v[76:79], v[44:47], a[120:127], v8\nv_smfmac_f32_16x16x64_bf16 v[80:83], v[44:47], a[176:183], v8\nv_smfmac_f32_16x16x64_bf16 v[68:71], v[48:51], a[16:23], v8\nv_smfmac_f32_16x16x64_bf16 v[72:75], v[48:51], a[72:79], v8\n"
"v_smfmac_f32_16x16x64_bf16 v[76:79], v[48:51], a[128:135], v8\nv_smfmac_f32_16x16x64_bf16 v[80:83], v[48:51], a[184:191], v8\nv_smfmac_f32_16x16x64_bf16 v[68:71], v[52:55], a[24:31], v8\nv_smfmac_f32_16x16x64_bf16 v[72:75], v[52:55], a[80:87], v8\n"
"v_smfmac_f32_16x16x64_bf16 v[76:79], v[52:55], a[136:143], v8\nv_smfmac_f32_16x16x64_bf16 v[80:83], v[52:55], a[192:199], v8\nv_add_f32 v219, v68, v69\nv_add_f32 v220, v70, v71\n"
"v_cvt_pk_bf16_f32 v156, v219, v220\nv_add_f32 v219, v72, v73\nv_add_f32 v220, v74, v75\nv_cvt_pk_bf16_f32 v157, v219, v220\n"
"v_add_f32 v219, v76, v77\nv_add_f32 v220, v78, v79\nv_cvt_pk_bf16_f32 v158, v219, v220\nv_add_f32 v219, v80, v81\n"
"v_add_f32 v220, v82, v83\nv_cvt_pk_bf16_f32 v159, v219, v220\nbuffer_store_dwordx4 v[156:159], v9, s[20:23], s51 offen sc1\ns_add_u32 s47, s33, 65\n"
"v_mov_b32 v219, s47\ns_waitcnt vmcnt(0)\nbuffer_store_dword v219, v18, s[8:11], 0 offen sc1\ns_branch end_p2\n"
"retry1_p2:\ns_add_u32 s53, s53, 256\ns_add_u32 s49, s49, 1\ns_cmp_gt_u32 s49, 65536\n"
"s_cbranch_scc1 end_p2\ns_add_u32 s47, s40, 1\nv_mov_b32 v221, s47\nds_write_b32 v222, v221\n"
"rec_p2:\ns_sleep 4\nv_mov_b32 v68, 0\nv_mov_b32 v69, 0\n"
"v_mov_b32 v70, 0\nv_mov_b32 v71, 0\nv_mov_b32 v72, 0\nv_mov_b32 v73, 0\n"
"v_mov_b32 v74, 0\nv_mov_b32 v75, 0\nv_mov_b32 v76, 0\nv_mov_b32 v77, 0\n"
"v_mov_b32 v78, 0\nv_mov_b32 v79, 0\nv_mov_b32 v80, 0\nv_mov_b32 v81, 0\n"
"v_mov_b32 v82, 0\nv_mov_b32 v83, 0\nv_mov_b32 v218, 0\ns_branch top2_p2\n"
"adopt_p2:\nbuffer_load_dwordx4 v[164:167], v28, s[8:11], 0 offen sc1\ns_lshl_b32 s52, s40, 26\ns_or_b32 s47, s52, 0x3ffffff\n"
"s_waitcnt vmcnt(0)\nv_cmp_ge_u32 vcc, s47, v164\ns_nop 1\nv_cndmask_b32 v164, 0, v164, vcc\n"
"v_cmp_ge_u32 vcc, s47, v165\ns_nop 1\nv_cndmask_b32 v165, 0, v165, vcc\nv_cmp_ge_u32 vcc, s47, v166\n"
"s_nop 1\nv_cndmask_b32 v166, 0, v166, vcc\nv_cmp_ge_u32 vcc, s47, v167\ns_nop 1\n"
"v_cndmask_b32 v167, 0, v167, vcc\nv_max3_u32 v221, v164, v165, v166\nv_max_u32 v221, v221, v167\ns_nop 1\n"
"v_max_u32_dpp v221, v221, v221 row_ror:8 row_mask:0xf bank_mask:0xf\ns_nop 1\nv_max_u32_dpp v221, v221, v221 row_ror:4 row_mask:0xf bank_mask:0xf\ns_nop 1\n"
"v_max_u32_dpp v221, v221, v221 row_ror:2 row_mask:0xf bank_mask:0xf\ns_nop 1\nv_max_u32_dpp v221, v221, v221 row_ror:1 row_mask:0xf bank_mask:0xf\ns_nop 0\n"
"v_readlane_b32 s47, v221, 0\nv_readlane_b32 s48, v221, 16\ns_max_u32 s48, s48, s47\ns_cmp_le_u32 s48, s52\n"
"s_cbranch_scc1 back_p2\ns_bfe_u32 s52, s48, 1179656\ns_add_u32 s44, s52, s28\ns_mov_b32 s45, 0\n"
"s_and_b32 s46, s48, 255\ns_mov_b32 s92, 0\ns_branch back_p2\nlate_p2:\n"
"s_add_u32 s53, s53, 16777216\nv_mov_b32 v84, v100\nv_mov_b32 v85, v101\nv_mov_b32 v86, v102\n"
"v_mov_b32 v87, v103\nv_mov_b32 v88, v104\nv_mov_b32 v89, v105\nv_mov_b32 v90, v106\n"
"v_mov_b32 v91, v107\nv_mov_b32 v92, v108\nv_mov_b32 v93, v109\nv_mov_b32 v94, v110\n"
"v_mov_b32 v95, v111\nv_mov_b32 v96, v112\nv_mov_b32 v97, v113\nv_mov_b32 v98, v114\n"
"v_mov_b32 v99, v115\nv_smfmac_f32_16x16x64_bf16 v[68:71], v[64:67], a[48:55], v8\nv_smfmac_f32_16x16x64_bf16 v[72:75], v[64:67], a[104:111], v8\nv_smfmac_f32_16x16x64_bf16 v[76:79], v[64:67], a[160:167], v8\n"
"v_smfmac_f32_16x16x64_bf16 v[80:83], v[64:67], a[216:223], v8\nv_smfmac_f32_16x16x64_bf16 v[68:71], v[40:43], a[0:7], v8\nv_smfmac_f32_16x16x64_bf16 v[72:75], v[40:43], a[56:63], v8\nv_smfmac_f32_16x16x64_bf16 v[76:79], v[40:43], a[112:119], v8\n"
"v_smfmac_f32_16x16x64_bf16 v[80:83], v[40:43], a[168:175], v8\nv_smfmac_f32_16x16x64_bf16 v[68:71], v[44:47], a[8:15], v8\nv_smfmac_f32_16x16x64_bf16 v[72:75], v[44:47], a[64:71], v8\nv_smfmac_f32_16x16x64_bf16 v[76:79], v[44:47], a[120:127], v8\n"
"v_smfmac_f32_16x16x64_bf16 v[80:83], v[44:47], a[176:183], v8\nv_smfmac_f32_16x16x64_bf16 v[68:71], v[48:51], a[16:23], v8\nv_smfmac_f32_16x16x64_bf16 v[72:75], v[48:51], a[72:79], v8\nv_smfmac_f32_16x16x64_bf16 v[76:79], v[48:51], a[128:135], v8\n"
"v_smfmac_f32_16x16x64_bf16 v[80:83], v[48:51], a[184:191], v8\nv_smfmac_f32_16x16x64_bf16 v[68:71], v[52:55], a[24:31], v8\nv_smfmac_f32_16x16x64_bf16 v[72:75], v[52:55], a[80:87], v8\nv_smfmac_f32_16x16x64_bf16 v[76:79], v[52:55], a[136:143], v8\n"
"v_smfmac_f32_16x16x64_bf16 v[80:83], v[52:55], a[192:199], v8\nv_add_f32 v219, v68, v69\nv_add_f32 v220, v70, v71\nv_cvt_pk_bf16_f32 v156, v219, v220\n"
"v_add_f32 v219, v72, v73\nv_add_f32 v220, v74, v75\nv_cvt_pk_bf16_f32 v157, v219, v220\nv_add_f32 v219, v76, v77\n"
"v_add_f32 v220, v78, v79\nv_cvt_pk_bf16_f32 v158, v219, v220\nv_add_f32 v219, v80, v81\nv_add_f32 v220, v82, v83\n"
"v_cvt_pk_bf16_f32 v159, v219, v220\nbuffer_load_dwordx4 v[100:103], v9, s[16:19], s50 offen offset:0 sc1\nbuffer_load_dwordx4 v[104:107], v9, s[16:19], s50 offen offset:1024 sc1\nbuffer_load_dwordx4 v[108:111], v9, s[16:19], s50 offen offset:2048 sc1\n"
"buffer_load_dwordx4 v[112:115], v9, s[16:19], s50 offen offset:3072 sc1\ns_add_u32 s50, s50, 16384\nbuffer_store_dwordx4 v[156:159], v9, s[20:23], s51 offen sc1\ns_branch spin_p2\n"
"slow_p2:\nv_max3_u32 v218, v124, v125, v126\nv_max_u32 v218, v218, v127\nv_max3_u32 v218, v218, v128, v129\n"
"v_max3_u32 v218, v218, v130, v131\nv_max3_u32 v218, v218, v116, v117\nv_max3_u32 v218, v218, v118, v119\nv_max3_u32 v218, v218, v120, v121\n"
"v_max3_u32 v218, v218, v122, v123\nv_cmp_eq_u32 vcc, -1, v218\ns_cbranch_vccz exact_p2\ns_branch spin_p2\n"
"spin2_p2:\ns_sleep 3\nspin_p2:\ns_add_u32 s53, s53, 1\n"
"s_add_u32 s49, s49, 1\ns_cmp_gt_u32 s49, 65536\ns_cbranch_scc1 end_p2\nbuffer_load_dwordx4 v[116:119], v20, s[8:11], s41 offen offset:0 sc1\n"
"buffer_load_dwordx4 v[120:123], v20, s[8:11], s41 offen offset:1024 sc1\nbuffer_load_dwordx4 v[124:127], v21, s[8:11], s41 offen offset:0 sc1\nbuffer_load_dwordx4 v[128:131], v21, s[8:11], s41 offen offset:1024 sc1\ns_waitcnt vmcnt(0)\n"
"v_max3_u32 v218, v124, v125, v126\nv_max_u32 v218, v218, v127\nv_max3_u32 v218, v218, v128, v129\nv_max3_u32 v218, v218, v130, v131\n"
"v_max3_u32 v218, v218, v116, v117\nv_max3_u32 v218, v218, v118, v119\nv_max3_u32 v218, v218, v120, v121\nv_max3_u32 v218, v218, v122, v123\n"
"v_cmp_eq_u32 vcc, -1, v218\ns_cbranch_vccnz spin2_p2\ns_memrealtime s[54:55]\ns_add_u32 s52, s40, 1\n"
"s_lshl_b32 s52, s52, 26\ns_waitcnt lgkmcnt(0)\ns_sub_u32 s47, s54, s28\ns_add_u32 s47, s47, s38\n"
"s_min_u32 s47, s47, 0x3ffff\ns_lshl_b32 s47, s47, 8\ns_or_b32 s47, s47, s52\ns_and_b32 s48, s46, 15\n"
"s_add_u32 s52, s48, 0\ns_min_u32 s52, s52, 15\ns_or_b32 s47, s47, s52\ns_add_u32 s48, s48, 0\n"
"s_min_u32 s48, s48, s52\ns_lshl_b32 s48, s48, 4\ns_and_b32 s52, s46, 0xf0\ns_cmp_ge_u32 s92, 99\n"
"s_cselect_b32 s48, s48, s52\ns_or_b32 s47, s47, s48\nv_mov_b32 v225, s47\nbuffer_store_dword v225, v19, s[8:11], 0 offen sc1\n"
"s_add_u32 s47, s40, 2\nv_mov_b32 v221, s47\nds_write_b32 v222, v221\nv_mfma_f32_16x16x32_bf16 v[132:135], v[84:87], v[116:119], 0\n"
"v_mfma_f32_16x16x32_bf16 v[136:139], v[88:91], v[116:119], 0\nv_mfma_f32_16x16x32_bf16 v[148:151], v[176:179], v[116:119], 0\nv_mfma_f32_16x16x32_bf16 v[140:143], v[92:95], v[120:123], 0\nv_mfma_f32_16x16x32_bf16 v[144:147], v[96:99], v[120:123], 0\n"
"v_mfma_f32_16x16x32_bf16 v[148:151], v[180:183], v[120:123], v[148:151]\nv_cndmask_b32 v132, v136, v132, s[62:63]\nv_cndmask_b32 v133, v137, v133, s[62:63]\nv_cndmask_b32 v134, v138, v134, s[62:63]\n"
"v_cndmask_b32 v135, v139, v135, s[62:63]\nv_mfma_f32_16x16x32_f16 v[152:155], v[168:171], v[124:127], 0\nv_mfma_f32_16x16x32_f16 v[152:155], v[172:175], v[128:131], v[152:155]\nv_cndmask_b32 v140, v144, v140, s[62:63]\n"
"v_cndmask_b32 v141, v145, v141, s[62:63]\nv_cndmask_b32 v142, v146, v142, s[62:63]\nv_cndmask_b32 v143, v147, v143, s[62:63]\ns_nop 2\n"
"v_exp_f32 v152, v152\nv_exp_f32 v153, v153\nv_exp_f32 v154, v154\nv_exp_f32 v155, v155\n"
"v_cvt_pk_bf16_f32 v200, v152, v153\nv_cvt_pk_bf16_f32 v201, v154, v155\nv_and_b32 v160, v200, v223\nv_and_b32 v161, v200, v224\n"
"v_mfma_f32_16x16x16_bf16 v[148:151], v[214:215], v[200:201], v[148:151]\nv_and_b32 v162, v201, v223\nv_and_b32 v163, v201, v224\nv_mfma_f32_16x16x16_bf16 v[132:135], v[156:157], v[160:161], v[132:135]\n"
"s_nop 0\nv_mfma_f32_16x16x16_bf16 v[140:143], v[158:159], v[162:163], v[140:143]\nv_rcp_f32 v202, v148\nv_rcp_f32 v203, v149\n"
"v_mul_f32 v132, v132, v202\nv_mul_f32 v133, v133, v202\nv_mul_f32 v134, v134, v202\nv_mul_f32 v135, v135, v202\n"
"v_fmac_f32 v132, v140, v203\nv_fmac_f32 v133, v141, v203\nv_fmac_f32 v134, v142, v203\nv_fmac_f32 v135, v143, v203\n"
"v_add_f32_dpp v204, v132, v132 row_ror:8 row_mask:0xf bank_mask:0x3\nv_add_f32_dpp v205, v133, v133 row_ror:8 row_mask:0xf bank_mask:0x3\nv_add_f32_dpp v204, v134, v134 row_ror:8 row_mask:0xf bank_mask:0xc\nv_add_f32_dpp v205, v135, v135 row_ror:8 row_mask:0xf bank_mask:0xc\n"
"v_subrev_u32 v219, 0x2f800000, v148\nv_subrev_u32 v220, 0x2f800000, v149\nv_max_u32 v219, v219, v220\nv_cmp_le_u32 vcc, 0x20000000, v219\n"
"ds_write_b64 v22, v[204:205]\ns_cbranch_vccz join_p2\nexact_p2:\ns_add_u32 s53, s53, 65536\n"
"ex0_p2:\ns_add_u32 s47, s41, 0\nbuffer_load_dwordx2 v[184:185], v24, s[8:11], s47 offen offset:0 sc1\nbuffer_load_dwordx2 v[186:187], v24, s[8:11], s47 offen offset:512 sc1\n"
"buffer_load_dwordx2 v[188:189], v24, s[8:11], s47 offen offset:1024 sc1\nbuffer_load_dwordx2 v[190:191], v24, s[8:11], s47 offen offset:1536 sc1\nbuffer_load_dwordx4 v[192:195], v25, s[8:11], s47 offen sc1\nbuffer_load_dwordx4 v[196:199], v25, s[8:11], s47 offen offset:16 sc1\n"
"v_mov_b32 v218, 0\ns_waitcnt vmcnt(0)\nv_max3_u32 v218, v218, v184, v185\nv_max3_u32 v218, v218, v186, v187\n"
"v_max3_u32 v218, v218, v188, v189\nv_max3_u32 v218, v218, v190, v191\nv_max3_u32 v218, v218, v192, v193\nv_max3_u32 v218, v218, v194, v195\n"
"v_max3_u32 v218, v218, v196, v197\nv_max3_u32 v218, v218, v198, v199\nv_cmp_eq_u32 vcc, -1, v218\ns_cbranch_vccz exok0_p2\n"
"s_add_u32 s53, s53, 0\ns_add_u32 s49, s49, 1\ns_cmp_gt_u32 s49, 65536\ns_cbranch_scc1 end_p2\n"
"s_branch ex0_p2\nexok0_p2:\nv_add_f32 v184, v184, v186\nv_add_f32 v188, v188, v190\n"
"v_add_f32 v184, v184, v188\nv_add_f32 v185, v185, v187\nv_add_f32 v189, v189, v191\nv_add_f32 v185, v185, v189\n"
"v_max_f32 v225, v184, v185\nv_max3_f32 v225, v225, v192, v193\nv_max3_f32 v225, v225, v194, v195\nv_max3_f32 v225, v225, v196, v197\n"
"v_max3_f32 v225, v225, v198, v199\nv_mov_b32 v219, v225\ns_nop 1\nv_permlane16_swap_b32 v225, v219\n"
"v_max_f32 v225, v225, v219\nv_mov_b32 v219, v225\ns_nop 1\nv_permlane32_swap_b32 v225, v219\n"
"v_max_f32 v225, v225, v219\nv_sub_f32 v184, v184, v225\nv_exp_f32 v152, v184\nv_sub_f32 v185, v185, v225\n"
"v_exp_f32 v153, v185\nv_sub_f32 v192, v192, v225\nv_exp_f32 v192, v192\nv_sub_f32 v193, v193, v225\n"
"v_exp_f32 v193, v193\nv_sub_f32 v194, v194, v225\nv_exp_f32 v194, v194\nv_sub_f32 v195, v195, v225\n"
"v_exp_f32 v195, v195\nv_sub_f32 v196, v196, v225\nv_exp_f32 v196, v196\nv_sub_f32 v197, v197, v225\n"
"v_exp_f32 v197, v197\nv_sub_f32 v198, v198, v225\nv_exp_f32 v198, v198\nv_sub_f32 v199, v199, v225\n"
"v_exp_f32 v199, v199\ns_nop 0\nv_cvt_pk_bf16_f32 v116, v192, v193\nv_cvt_pk_bf16_f32 v117, v194, v195\n"
"v_cvt_pk_bf16_f32 v118, v196, v197\nv_cvt_pk_bf16_f32 v119, v198, v199\nex1_p2:\ns_add_u32 s47, s41, 4096\n"
"buffer_load_dwordx2 v[184:185], v24, s[8:11], s47 offen offset:0 sc1\nbuffer_load_dwordx2 v[186:187], v24, s[8:11], s47 offen offset:512 sc1\nbuffer_load_dwordx2 v[188:189], v24, s[8:11], s47 offen offset:1024 sc1\nbuffer_load_dwordx2 v[190:191], v24, s[8:11], s47 offen offset:1536 sc1\n"
"buffer_load_dwordx4 v[192:195], v25, s[8:11], s47 offen sc1\nbuffer_load_dwordx4 v[196:199], v25, s[8:11], s47 offen offset:16 sc1\nv_mov_b32 v218, 0\ns_waitcnt vmcnt(0)\n"
"v_max3_u32 v218, v218, v184, v185\nv_max3_u32 v218, v218, v186, v187\nv_max3_u32 v218, v218, v188, v189\nv_max3_u32 v218, v218, v190, v191\n"
"v_max3_u32 v218, v218, v192, v193\nv_max3_u32 v218, v218, v194, v195\nv_max3_u32 v218, v218, v196, v197\nv_max3_u32 v218, v218, v198, v199\n"
"v_cmp_eq_u32 vcc, -1, v218\ns_cbranch_vccz exok1_p2\ns_add_u32 s53, s53, 0\ns_add_u32 s49, s49, 1\n"
"s_cmp_gt_u32 s49, 65536\ns_cbranch_scc1 end_p2\ns_branch ex1_p2\nexok1_p2:\n"
"v_add_f32 v184, v184, v186\nv_add_f32 v188, v188, v190\nv_add_f32 v184, v184, v188\nv_add_f32 v185, v185, v187\n"
"v_add_f32 v189, v189, v191\nv_add_f32 v185, v185, v189\nv_max_f32 v225, v184, v185\nv_max3_f32 v225, v225, v192, v193\n"
"v_max3_f32 v225, v225, v194, v195\nv_max3_f32 v225, v225, v196, v197\nv_max3_f32 v225, v225, v198, v199\nv_mov_b32 v219, v225\n"
"s_nop 1\nv_permlane16_swap_b32 v225, v219\nv_max_f32 v225, v225, v219\nv_mov_b32 v219, v225\n"
"s_nop 1\nv_permlane32_swap_b32 v225, v219\nv_max_f32 v225, v225, v219\nv_sub_f32 v184, v184, v225\n"
"v_exp_f32 v154, v184\nv_sub_f32 v185, v185, v225\nv_exp_f32 v155, v185\nv_sub_f32 v192, v192, v225\n"
"v_exp_f32 v192, v192\nv_sub_f32 v193, v193, v225\nv_exp_f32 v193, v193\nv_sub_f32 v194, v194, v225\n"
"v_exp_f32 v194, v194\nv_sub_f32 v195, v195, v225\nv_exp_f32 v195, v195\nv_sub_f32 v196, v196, v225\n"
"v_exp_f32 v196, v196\nv_sub_f32 v197, v197, v225\nv_exp_f32 v197, v197\nv_sub_f32 v198, v198, v225\n"
"v_exp_f32 v198, v198\nv_sub_f32 v199, v199, v225\nv_exp_f32 v199, v199\ns_nop 0\n"
"v_cvt_pk_bf16_f32 v120, v192, v193\nv_cvt_pk_bf16_f32 v121, v194, v195\nv_cvt_pk_bf16_f32 v122, v196, v197\nv_cvt_pk_bf16_f32 v123, v198, v199\n"
"s_memrealtime s[54:55]\ns_add_u32 s52, s40, 1\ns_lshl_b32 s52, s52, 26\ns_waitcnt lgkmcnt(0)\n"
"s_sub_u32 s47, s54, s28\ns_add_u32 s47, s47, s38\ns_min_u32 s47, s47, 0x3ffff\ns_lshl_b32 s47, s47, 8\n"
"s_or_b32 s47, s47, s52\ns_and_b32 s48, s46, 15\ns_add_u32 s52, s48, 0\ns_min_u32 s52, s52, 15\n"
"s_or_b32 s47, s47, s52\ns_add_u32 s48, s48, 0\ns_min_u32 s48, s48, s52\ns_lshl_b32 s48, s48, 4\n"
"s_and_b32 s52, s46, 0xf0\ns_cmp_ge_u32 s92, 99\ns_cselect_b32 s48, s48, s52\ns_or_b32 s47, s47, s48\n"
"v_mov_b32 v225, s47\nbuffer_store_dword v225, v19, s[8:11], 0 offen sc1\ns_add_u32 s47, s40, 2\nv_mov_b32 v221, s47\n"
"ds_write_b32 v222, v221\nv_mfma_f32_16x16x32_bf16 v[132:135], v[84:87], v[116:119], 0\nv_mfma_f32_16x16x32_bf16 v[136:139], v[88:91], v[116:119], 0\nv_mfma_f32_16x16x32_bf16 v[148:151], v[176:179], v[116:119], 0\n"
"v_mfma_f32_16x16x32_bf16 v[140:143], v[92:95], v[120:123], 0\nv_mfma_f32_16x16x32_bf16 v[144:147], v[96:99], v[120:123], 0\nv_mfma_f32_16x16x32_bf16 v[148:151], v[180:183], v[120:123], v[148:151]\nv_cndmask_b32 v132, v136, v132, s[62:63]\n"
"v_cndmask_b32 v133, v137, v133, s[62:63]\nv_cndmask_b32 v134, v138, v134, s[62:63]\nv_cndmask_b32 v135, v139, v135, s[62:63]\ns_nop 0\n"
"v_cndmask_b32 v140, v144, v140, s[62:63]\nv_cndmask_b32 v141, v145, v141, s[62:63]\nv_cndmask_b32 v142, v146, v142, s[62:63]\nv_cndmask_b32 v143, v147, v143, s[62:63]\n"
"v_cvt_pk_bf16_f32 v200, v152, v153\nv_cvt_pk_bf16_f32 v201, v154, v155\nv_and_b32 v160, v200, v223\nv_and_b32 v161, v200, v224\n"
"v_mfma_f32_16x16x16_bf16 v[148:151], v[214:215], v[200:201], v[148:151]\nv_and_b32 v162, v201, v223\nv_and_b32 v163, v201, v224\nv_mfma_f32_16x16x16_bf16 v[132:135], v[156:157], v[160:161], v[132:135]\n"
"s_nop 0\nv_mfma_f32_16x16x16_bf16 v[140:143], v[158:159], v[162:163], v[140:143]\nv_rcp_f32 v202, v148\nv_rcp_f32 v203, v149\n"
"v_mul_f32 v132, v132, v202\nv_mul_f32 v133, v133, v202\nv_mul_f32 v134, v134, v202\nv_mul_f32 v135, v135, v202\n"
"v_fmac_f32 v132, v140, v203\nv_fmac_f32 v133, v141, v203\nv_fmac_f32 v134, v142, v203\nv_fmac_f32 v135, v143, v203\n"
"v_add_f32_dpp v204, v132, v132 row_ror:8 row_mask:0xf bank_mask:0x3\nv_add_f32_dpp v205, v133, v133 row_ror:8 row_mask:0xf bank_mask:0x3\nv_add_f32_dpp v204, v134, v134 row_ror:8 row_mask:0xf bank_mask:0xc\nv_add_f32_dpp v205, v135, v135 row_ror:8 row_mask:0xf bank_mask:0xc\n"
"ds_write_b64 v22, v[204:205]\ns_branch join_p2\nend_p2:\ns_branch fk_done\n"
"fk_p3:\ns_mov_b32 s40, 0\ns_mov_b32 s41, 0\ns_mov_b32 s42, 262144\n"
"s_mov_b32 s43, 393216\ns_mov_b32 s44, s28\ns_mov_b32 s45, 0\ns_mov_b32 s46, 0\n"
"s_mov_b32 s92, 0\ns_mov_b32 s49, 0\ns_mov_b32 s53, 0\ns_mov_b32 s50, s35\n"
"s_mov_b32 s51, s36\nv_mov_b32 v222, 24576\ns_mov_b32 s62, 0x00ff00ff\ns_mov_b32 s63, 0x00ff00ff\n"
"v_cndmask_b32 v223, 0, -1, s[62:63]\nv_not_b32 v224, v223\nv_bfe_u32 v219, v9, 4, 1\nv_lshlrev_b32 v220, 4, v219\n"
"v_mov_b32 v218, 0x3c00\nv_lshlrev_b32 v218, v220, v218\nv_bfe_u32 v220, v9, 5, 3\nv_cmp_eq_u32 vcc, 0, v220\n"
"s_nop 1\nv_cndmask_b32 v168, 0, v218, vcc\nv_cmp_eq_u32 vcc, 2, v220\ns_nop 1\n"
"v_cndmask_b32 v169, 0, v218, vcc\nv_cmp_eq_u32 vcc, 4, v220\ns_nop 1\nv_cndmask_b32 v170, 0, v218, vcc\n"
"v_cmp_eq_u32 vcc, 6, v220\ns_nop 1\nv_cndmask_b32 v171, 0, v218, vcc\nv_cmp_eq_u32 vcc, 1, v220\n"
"s_nop 1\nv_cndmask_b32 v172, 0, v218, vcc\nv_cmp_eq_u32 vcc, 3, v220\ns_nop 1\n"
"v_cndmask_b32 v173, 0, v218, vcc\nv_cmp_eq_u32 vcc, 5, v220\ns_nop 1\nv_cndmask_b32 v174, 0, v218, vcc\n"
"v_cmp_eq_u32 vcc, 7, v220\ns_nop 1\nv_cndmask_b32 v175, 0, v218, vcc\nv_mov_b32 v218, 0x3f803f80\n"
"v_cmp_eq_u32 vcc, 0, v219\ns_nop 1\nv_cndmask_b32 v176, 0, v218, vcc\nv_cndmask_b32 v177, 0, v218, vcc\n"
"v_cndmask_b32 v178, 0, v218, vcc\nv_cndmask_b32 v179, 0, v218, vcc\nv_cndmask_b32 v214, 0, v218, vcc\nv_cmp_eq_u32 vcc, 1, v219\n"
"s_nop 1\nv_cndmask_b32 v180, 0, v218, vcc\nv_cndmask_b32 v181, 0, v218, vcc\nv_cndmask_b32 v182, 0, v218, vcc\n"
"v_cndmask_b32 v183, 0, v218, vcc\nv_cndmask_b32 v215, 0, v218, vcc\ns_add_u32 s47, s34, 28672\nbuffer_load_dwordx4 a[0:3], v9, s[12:15], s47 offen sc1\n"
"buffer_load_dwordx4 a[4:7], v9, s[12:15], s47 offen offset:1024 sc1\ns_add_u32 s47, s34, 30720\nbuffer_load_dwordx4 a[8:11], v9, s[12:15], s47 offen sc1\nbuffer_load_dwordx4 a[12:15], v9, s[12:15], s47 offen offset:1024 sc1\n"
"s_add_u32 s47, s34, 32768\nbuffer_load_dwordx4 a[16:19], v9, s[12:15], s47 offen sc1\nbuffer_load_dwordx4 a[20:23], v9, s[12:15], s47 offen offset:1024 sc1\ns_add_u32 s47, s34, 34816\n"
"buffer_load_dwordx4 a[24:27], v9, s[12:15], s47 offen sc1\nbuffer_load_dwordx4 a[28:31], v9, s[12:15], s47 offen offset:1024 sc1\ns_add_u32 s47, s34, 36864\nbuffer_load_dwordx4 a[32:35], v9, s[12:15], s47 offen sc1\n"
"buffer_load_dwordx4 a[36:39], v9, s[12:15], s47 offen offset:1024 sc1\ns_add_u32 s47, s34, 38912\nbuffer_load_dwordx4 a[40:43], v9, s[12:15], s47 offen sc1\nbuffer_load_dwordx4 a[44:47], v9, s[12:15], s47 offen offset:1024 sc1\n"
"s_add_u32 s47, s34, 40960\nbuffer_load_dwordx4 a[48:51], v9, s[12:15], s47 offen sc1\nbuffer_load_dwordx4 a[52:55], v9, s[12:15], s47 offen offset:1024 sc1\ns_waitcnt vmcnt(14)\n"
"s_add_u32 s47, s34, 43008\nbuffer_load_dwordx4 a[56:59], v9, s[12:15], s47 offen sc1\nbuffer_load_dwordx4 a[60:63], v9, s[12:15], s47 offen offset:1024 sc1\ns_add_u32 s47, s34, 45056\n"
"buffer_load_dwordx4 a[64:67], v9, s[12:15], s47 offen sc1\nbuffer_load_dwordx4 a[68:71], v9, s[12:15], s47 offen offset:1024 sc1\ns_add_u32 s47, s34, 47104\nbuffer_load_dwordx4 a[72:75], v9, s[12:15], s47 offen sc1\n"
"buffer_load_dwordx4 a[76:79], v9, s[12:15], s47 offen offset:1024 sc1\ns_add_u32 s47, s34, 49152\nbuffer_load_dwordx4 a[80:83], v9, s[12:15], s47 offen sc1\nbuffer_load_dwordx4 a[84:87], v9, s[12:15], s47 offen offset:1024 sc1\n"
"s_add_u32 s47, s34, 51200\nbuffer_load_dwordx4 a[88:91], v9, s[12:15], s47 offen sc1\nbuffer_load_dwordx4 a[92:95], v9, s[12:15], s47 offen offset:1024 sc1\ns_add_u32 s47, s34, 53248\n"
"buffer_load_dwordx4 a[96:99], v9, s[12:15], s47 offen sc1\nbuffer_load_dwordx4 a[100:103], v9, s[12:15], s47 offen offset:1024 sc1\ns_add_u32 s47, s34, 55296\nbuffer_load_dwordx4 a[104:107], v9, s[12:15], s47 offen sc1\n"
"buffer_load_dwordx4 a[108:111], v9, s[12:15], s47 offen offset:1024 sc1\ns_waitcnt vmcnt(14)\ns_add_u32 s47, s34, 0\nbuffer_load_dwordx4 a[112:115], v9, s[12:15], s47 offen sc1\n"
"buffer_load_dwordx4 a[116:119], v9, s[12:15], s47 offen offset:1024 sc1\ns_add_u32 s47, s34, 2048\nbuffer_load_dwordx4 a[120:123], v9, s[12:15], s47 offen sc1\nbuffer_load_dwordx4 a[124:127], v9, s[12:15], s47 offen offset:1024 sc1\n"
"s_add_u32 s47, s34, 4096\nbuffer_load_dwordx4 a[128:131], v9, s[12:15], s47 offen sc1\nbuffer_load_dwordx4 a[132:135], v9, s[12:15], s47 offen offset:1024 sc1\ns_add_u32 s47, s34, 6144\n"
"buffer_load_dwordx4 a[136:139], v9, s[12:15], s47 offen sc1\nbuffer_load_dwordx4 a[140:143], v9, s[12:15], s47 offen offset:1024 sc1\ns_add_u32 s47, s34, 8192\nbuffer_load_dwordx4 a[144:147], v9, s[12:15], s47 offen sc1\n"
"buffer_load_dwordx4 a[148:151], v9, s[12:15], s47 offen offset:1024 sc1\ns_add_u32 s47, s34, 10240\nbuffer_load_dwordx4 a[152:155], v9, s[12:15], s47 offen sc1\nbuffer_load_dwordx4 a[156:159], v9, s[12:15], s47 offen offset:1024 sc1\n"
"s_add_u32 s47, s34, 12288\nbuffer_load_dwordx4 a[160:163], v9, s[12:15], s47 offen sc1\nbuffer_load_dwordx4 a[164:167], v9, s[12:15], s47 offen offset:1024 sc1\ns_waitcnt vmcnt(14)\n"
"s_add_u32 s47, s34, 14336\nbuffer_load_dwordx4 a[168:171], v9, s[12:15], s47 offen sc1\nbuffer_load_dwordx4 a[172:175], v9, s[12:15], s47 offen offset:1024 sc1\ns_add_u32 s47, s34, 16384\n"
"buffer_load_dwordx4 a[176:179], v9, s[12:15], s47 offen sc1\nbuffer_load_dwordx4 a[180:183], v9, s[12:15], s47 offen offset:1024 sc1\ns_add_u32 s47, s34, 18432\nbuffer_load_dwordx4 a[184:187], v9, s[12:15], s47 offen sc1\n"
"buffer_load_dwordx4 a[188:191], v9, s[12:15], s47 offen offset:1024 sc1\ns_add_u32 s47, s34, 20480\nbuffer_load_dwordx4 a[192:195], v9, s[12:15], s47 offen sc1\nbuffer_load_dwordx4 a[196:199], v9, s[12:15], s47 offen offset:1024 sc1\n"
"s_add_u32 s47, s34, 22528\nbuffer_load_dwordx4 a[200:203], v9, s[12:15], s47 offen sc1\nbuffer_load_dwordx4 a[204:207], v9, s[12:15], s47 offen offset:1024 sc1\ns_add_u32 s47, s34, 24576\n"
"buffer_load_dwordx4 a[208:211], v9, s[12:15], s47 offen sc1\nbuffer_load_dwordx4 a[212:215], v9, s[12:15], s47 offen offset:1024 sc1\ns_add_u32 s47, s34, 26624\nbuffer_load_dwordx4 a[216:219], v9, s[12:15], s47 offen sc1\n"
"buffer_load_dwordx4 a[220:223], v9, s[12:15], s47 offen offset:1024 sc1\ns_waitcnt vmcnt(14)\nbuffer_load_dword v218, v11, s[8:11], 0 offen sc1\ns_waitcnt vmcnt(0)\n"
"fl_p3:\ns_add_u32 s47, s40, 2\ns_min_u32 s47, s47, 64\ns_add_u32 s47, s47, s33\n"
"v_subrev_u32 v218, s47, v218\nv_cmp_lt_u32 vcc, 64, v218\ns_cbranch_vccz flok_p3\ns_add_u32 s53, s53, 0\n"
"s_add_u32 s49, s49, 1\ns_cmp_gt_u32 s49, 65536\ns_cbranch_scc1 end_p3\ns_sleep 2\n"
"buffer_load_dword v218, v11, s[8:11], 0 offen sc1\ns_waitcnt vmcnt(0)\ns_branch fl_p3\nflok_p3:\n"
"buffer_load_dwordx4 v[100:103], v9, s[16:19], s50 offen offset:2048 sc1\nbuffer_load_dwordx4 v[104:107], v9, s[16:19], s50 offen offset:3072 sc1\nbuffer_load_dwordx4 v[108:111], v9, s[16:19], s50 offen offset:0 sc1\nbuffer_load_dwordx4 v[112:115], v9, s[16:19], s50 offen offset:1024 sc1\n"
"s_add_u32 s50, s50, 16384\ns_waitcnt vmcnt(0)\ns_barrier\n.p2align 6\n"
"top_p3:\nv_mov_b32 v68, 0\nv_mov_b32 v69, 0\nv_mov_b32 v70, 0\n"
"v_mov_b32 v71, 0\nv_mov_b32 v72, 0\nv_mov_b32 v73, 0\nv_mov_b32 v74, 0\n"
"v_mov_b32 v75, 0\nv_mov_b32 v76, 0\nv_mov_b32 v77, 0\nv_mov_b32 v78, 0\n"
"v_mov_b32 v79, 0\nv_mov_b32 v80, 0\nv_mov_b32 v81, 0\nv_mov_b32 v82, 0\n"
"v_mov_b32 v83, 0\nv_mov_b32 v218, 0\ns_add_u32 s52, s44, s37\nm_p3:\n"
"s_memrealtime s[54:55]\ns_waitcnt lgkmcnt(0)\ns_sub_u32 s47, s54, s52\ns_cmp_lt_i32 s47, 0\n"
"s_cbranch_scc1 m_p3\ntop2_p3:\nbuffer_load_dwordx4 v[64:67], v10, s[8:11], s43 offen offset:0 sc1\ns_waitcnt vmcnt(0)\n"
"ds_write_b128 v9, v[64:67] offset:6144\nv_max3_u32 v218, v218, v64, v65\nv_max3_u32 v218, v218, v66, v67\nv_cmp_eq_u32 vcc, -1, v218\n"
"v_smfmac_f32_16x16x64_bf16 v[68:71], v[64:67], a[48:55], v8\nv_smfmac_f32_16x16x64_bf16 v[72:75], v[64:67], a[104:111], v8\ns_cbranch_vccnz retry1_p3\ns_waitcnt lgkmcnt(0)\n"
"s_barrier\nds_read_b32 v221, v222\nds_read_b128 v[40:43], v9 offset:0\nds_read_b128 v[44:47], v9 offset:1024\n"
"ds_read_b128 v[48:51], v9 offset:2048\nds_read_b128 v[52:55], v9 offset:3072\nds_read_b128 v[56:59], v9 offset:4096\nds_read_b128 v[60:63], v9 offset:5120\n"
"v_smfmac_f32_16x16x64_bf16 v[76:79], v[64:67], a[160:167], v8\nv_smfmac_f32_16x16x64_bf16 v[80:83], v[64:67], a[216:223], v8\ns_waitcnt lgkmcnt(5)\nv_smfmac_f32_16x16x64_bf16 v[68:71], v[40:43], a[0:7], v8\n"
"v_smfmac_f32_16x16x64_bf16 v[72:75], v[40:43], a[56:63], v8\nv_smfmac_f32_16x16x64_bf16 v[76:79], v[40:43], a[112:119], v8\nv_smfmac_f32_16x16x64_bf16 v[80:83], v[40:43], a[168:175], v8\ns_waitcnt lgkmcnt(0)\n"
"v_readfirstlane_b32 s47, v221\ns_sub_u32 s47, s47, s40\ns_cmp_eq_u32 s47, 1\ns_cbranch_scc1 adopt_p3\n"
"back_p3:\ns_cmp_eq_u32 s40, 63\ns_cbranch_scc1 last_p3\ns_and_b32 s47, s46, 15\n"
"s_mul_i32 s47, s47, 2\ns_add_u32 s47, s47, s29\ns_add_u32 s47, s47, s45\ns_and_b32 s91, s47, 15\n"
"s_lshr_b32 s47, s47, 4\ns_lshl_b32 s47, s47, 2\ns_add_u32 s90, s44, s47\ns_mul_i32 s48, s91, 6\n"
"s_and_b32 s48, s48, -4\ns_getpc_b64 s[60:61]\nspc_p3:\ns_sub_u32 s48, sgo_p3-spc_p3, s48\n"
"s_add_u32 s60, s60, s48\ns_addc_u32 s61, s61, 0\nsf_p3:\ns_memrealtime s[54:55]\n"
"s_nop 2\ns_memrealtime s[56:57]\ns_nop 2\ns_memrealtime s[58:59]\n"
"s_waitcnt lgkmcnt(4)\ns_sub_u32 s47, s54, s90\ns_add_u32 s48, s47, 12\ns_cmp_ge_u32 s48, 12\n"
"s_cbranch_scc1 so_p3\ns_memrealtime s[54:55]\ns_waitcnt lgkmcnt(4)\ns_cmp_ge_u32 s56, s90\n"
"s_cbranch_scc1 sx_p3\ns_memrealtime s[56:57]\ns_waitcnt lgkmcnt(4)\ns_cmp_ge_u32 s58, s90\n"
"s_cbranch_scc1 sx_p3\ns_memrealtime s[58:59]\ns_waitcnt lgkmcnt(4)\ns_cmp_ge_u32 s54, s90\n"
"s_cbranch_scc1 sx_p3\ns_memrealtime s[54:55]\ns_waitcnt lgkmcnt(4)\ns_cmp_ge_u32 s56, s90\n"
"s_cbranch_scc1 sx_p3\ns_memrealtime s[56:57]\ns_waitcnt lgkmcnt(4)\ns_cmp_ge_u32 s58, s90\n"
"s_cbranch_scc1 sx_p3\ns_memrealtime s[58:59]\ns_waitcnt lgkmcnt(4)\ns_cmp_ge_u32 s54, s90\n"
"s_cbranch_scc1 sx_p3\ns_memrealtime s[54:55]\ns_waitcnt lgkmcnt(4)\ns_cmp_ge_u32 s56, s90\n"
"s_cbranch_scc1 sx_p3\ns_memrealtime s[56:57]\ns_waitcnt lgkmcnt(4)\ns_cmp_ge_u32 s58, s90\n"
"s_cbranch_scc1 sx_p3\ns_memrealtime s[58:59]\ns_waitcnt lgkmcnt(4)\ns_cmp_ge_u32 s54, s90\n"
"s_cbranch_scc1 sx_p3\ns_memrealtime s[54:55]\ns_waitcnt lgkmcnt(4)\ns_cmp_ge_u32 s56, s90\n"
"s_cbranch_scc1 sx_p3\ns_memrealtime s[56:57]\ns_waitcnt lgkmcnt(4)\ns_cmp_ge_u32 s58, s90\n"
"s_cbranch_scc1 sx_p3\ns_memrealtime s[58:59]\ns_waitcnt lgkmcnt(4)\ns_cmp_ge_u32 s54, s90\n"
"s_cbranch_scc1 sx_p3\ns_memrealtime s[54:55]\ns_waitcnt lgkmcnt(4)\ns_cmp_ge_u32 s56, s90\n"
"s_cbranch_scc1 sx_p3\ns_memrealtime s[56:57]\ns_waitcnt lgkmcnt(4)\ns_cmp_ge_u32 s58, s90\n"
"s_cbranch_scc1 sx_p3\ns_memrealtime s[58:59]\ns_waitcnt lgkmcnt(4)\ns_cmp_ge_u32 s54, s90\n"
"s_cbranch_scc1 sx_p3\ns_memrealtime s[54:55]\ns_waitcnt lgkmcnt(4)\ns_cmp_ge_u32 s56, s90\n"
"s_cbranch_scc1 sx_p3\ns_memrealtime s[56:57]\ns_branch sx_p3\nso_p3:\n"
"s_waitcnt lgkmcnt(0)\ns_cmp_lt_i32 s47, 0\ns_cbranch_scc1 sf_p3\ns_cmp_gt_i32 s47, s32\n"
"s_cbranch_scc1 late_p3\ns_branch sgo_p3\nsx_p3:\ns_setpc_b64 s[60:61]\n"
"s_nop 0\ns_nop 0\ns_nop 0\ns_nop 0\n"
"s_nop 0\ns_nop 0\ns_nop 0\ns_nop 0\n"
"s_nop 0\ns_nop 0\ns_nop 0\ns_nop 0\n"
"s_nop 0\ns_nop 0\ns_nop 0\ns_nop 0\n"
"s_nop 0\ns_nop 0\ns_nop 0\ns_nop 0\n"
"s_nop 0\ns_nop 0\ns_nop 0\ns_nop 0\n"
"sgo_p3:\ns_nop 15\ns_nop 1\nbuffer_load_dwordx4 v[116:119], v20, s[8:11], s41 offen offset:1024 sc1\n"
"buffer_load_dwordx4 v[120:123], v20, s[8:11], s41 offen offset:0 sc1\nv_smfmac_f32_16x16x64_bf16 v[68:71], v[44:47], a[8:15], v8\nv_mov_b32 v84, v100\nv_smfmac_f32_16x16x64_bf16 v[72:75], v[44:47], a[64:71], v8\n"
"v_mov_b32 v85, v101\nv_smfmac_f32_16x16x64_bf16 v[76:79], v[44:47], a[120:127], v8\nv_mov_b32 v86, v102\nv_smfmac_f32_16x16x64_bf16 v[80:83], v[44:47], a[176:183], v8\n"
"v_mov_b32 v87, v103\nv_smfmac_f32_16x16x64_bf16 v[68:71], v[48:51], a[16:23], v8\nv_mov_b32 v88, v104\nv_smfmac_f32_16x16x64_bf16 v[72:75], v[48:51], a[72:79], v8\n"
"v_mov_b32 v89, v105\nv_smfmac_f32_16x16x64_bf16 v[76:79], v[48:51], a[128:135], v8\nv_mov_b32 v90, v106\nv_smfmac_f32_16x16x64_bf16 v[80:83], v[48:51], a[184:191], v8\n"
"v_mov_b32 v91, v107\nv_smfmac_f32_16x16x64_bf16 v[68:71], v[52:55], a[24:31], v8\nv_mov_b32 v92, v108\nv_smfmac_f32_16x16x64_bf16 v[72:75], v[52:55], a[80:87], v8\n"
"v_mov_b32 v93, v109\nbuffer_load_dwordx4 v[124:127], v21, s[8:11], s41 offen offset:1024 sc1\nbuffer_load_dwordx4 v[128:131], v21, s[8:11], s41 offen offset:0 sc1\nv_smfmac_f32_16x16x64_bf16 v[76:79], v[52:55], a[136:143], v8\n"
"v_mov_b32 v94, v110\nv_smfmac_f32_16x16x64_bf16 v[80:83], v[52:55], a[192:199], v8\nv_mov_b32 v95, v111\nv_smfmac_f32_16x16x64_bf16 v[68:71], v[56:59], a[32:39], v8\n"
"v_mov_b32 v96, v112\nv_smfmac_f32_16x16x64_bf16 v[72:75], v[56:59], a[88:95], v8\nv_mov_b32 v97, v113\nv_smfmac_f32_16x16x64_bf16 v[76:79], v[56:59], a[144:151], v8\n"
"v_mov_b32 v98, v114\nv_smfmac_f32_16x16x64_bf16 v[80:83], v[56:59], a[200:207], v8\nv_mov_b32 v99, v115\nv_smfmac_f32_16x16x64_bf16 v[68:71], v[60:63], a[40:47], v8\n"
"v_smfmac_f32_16x16x64_bf16 v[72:75], v[60:63], a[96:103], v8\nv_smfmac_f32_16x16x64_bf16 v[76:79], v[60:63], a[152:159], v8\nv_smfmac_f32_16x16x64_bf16 v[80:83], v[60:63], a[208:215], v8\nv_add_f32 v219, v68, v69\n"
"v_add_f32 v220, v70, v71\nv_cvt_pk_bf16_f32 v156, v219, v220\nv_add_f32 v219, v72, v73\nv_add_f32 v220, v74, v75\n"
"v_cvt_pk_bf16_f32 v157, v219, v220\nv_add_f32 v219, v76, v77\nv_add_f32 v220, v78, v79\nv_cvt_pk_bf16_f32 v158, v219, v220\n"
"v_add_f32 v219, v80, v81\nv_add_f32 v220, v82, v83\nv_cvt_pk_bf16_f32 v159, v219, v220\nbuffer_load_dwordx4 v[100:103], v9, s[16:19], s50 offen offset:2048 sc1\n"
"buffer_load_dwordx4 v[104:107], v9, s[16:19], s50 offen offset:3072 sc1\nbuffer_load_dwordx4 v[108:111], v9, s[16:19], s50 offen offset:0 sc1\nbuffer_load_dwordx4 v[112:115], v9, s[16:19], s50 offen offset:1024 sc1\ns_add_u32 s50, s50, 16384\n"
"buffer_store_dwordx4 v[156:159], v9, s[20:23], s51 offen sc1\ns_waitcnt vmcnt(8)\nv_mfma_f32_16x16x32_bf16 v[132:135], v[84:87], v[116:119], 0\nv_mfma_f32_16x16x32_bf16 v[136:139], v[88:91], v[116:119], 0\n"
"v_mfma_f32_16x16x32_bf16 v[148:151], v[176:179], v[116:119], 0\ns_waitcnt vmcnt(7)\nv_mfma_f32_16x16x32_bf16 v[140:143], v[92:95], v[120:123], 0\nv_mfma_f32_16x16x32_bf16 v[144:147], v[96:99], v[120:123], 0\n"
"v_mfma_f32_16x16x32_bf16 v[148:151], v[180:183], v[120:123], v[148:151]\nv_cndmask_b32 v132, v136, v132, s[62:63]\nv_cndmask_b32 v133, v137, v133, s[62:63]\nv_cndmask_b32 v134, v138, v134, s[62:63]\n"
"v_cndmask_b32 v135, v139, v135, s[62:63]\ns_waitcnt vmcnt(6)\nv_mfma_f32_16x16x32_f16 v[152:155], v[168:171], v[124:127], 0\ns_waitcnt vmcnt(5)\n"
"v_mfma_f32_16x16x32_f16 v[152:155], v[172:175], v[128:131], v[152:155]\nv_cndmask_b32 v140, v144, v140, s[62:63]\nv_cndmask_b32 v141, v145, v141, s[62:63]\nv_cndmask_b32 v142, v146, v142, s[62:63]\n"
"v_cndmask_b32 v143, v147, v143, s[62:63]\ns_nop 2\nv_exp_f32 v152, v152\nv_exp_f32 v153, v153\n"
"v_exp_f32 v154, v154\nv_exp_f32 v155, v155\nv_cvt_pk_bf16_f32 v200, v152, v153\nv_cvt_pk_bf16_f32 v201, v154, v155\n"
"v_and_b32 v160, v200, v223\nv_and_b32 v161, v200, v224\nv_mfma_f32_16x16x16_bf16 v[148:151], v[214:215], v[200:201], v[148:151]\nv_and_b32 v162, v201, v223\n"
"v_and_b32 v163, v201, v224\nv_mfma_f32_16x16x16_bf16 v[132:135], v[156:157], v[160:161], v[132:135]\ns_nop 0\nv_mfma_f32_16x16x16_bf16 v[140:143], v[158:159], v[162:163], v[140:143]\n"
"v_rcp_f32 v202, v148\nv_rcp_f32 v203, v149\nv_mul_f32 v132, v132, v202\nv_mul_f32 v133, v133, v202\n"
"v_mul_f32 v134, v134, v202\nv_mul_f32 v135, v135, v202\nv_fmac_f32 v132, v140, v203\nv_fmac_f32 v133, v141, v203\n"
"v_fmac_f32 v134, v142, v203\nv_fmac_f32 v135, v143, v203\nv_add_f32_dpp v204, v132, v132 row_ror:8 row_mask:0xf bank_mask:0x3\nv_add_f32_dpp v205, v133, v133 row_ror:8 row_mask:0xf bank_mask:0x3\n"
"v_add_f32_dpp v204, v134, v134 row_ror:8 row_mask:0xf bank_mask:0xc\nv_add_f32_dpp v205, v135, v135 row_ror:8 row_mask:0xf bank_mask:0xc\nv_subrev_u32 v219, 0x2f800000, v148\nv_subrev_u32 v220, 0x2f800000, v149\n"
"v_max_u32 v219, v219, v220\nv_cmp_le_u32 vcc, 0x20000000, v219\nds_write_b64 v22, v[204:205]\ns_cbranch_vccnz slow_p3\n"
"join_p3:\nv_mov_b32 v219, 0\ns_nop 1\nv_mov_b32 v219, 0\n"
"s_waitcnt lgkmcnt(0)\ns_barrier\nds_read_b64 v[206:207], v23 offset:0\nds_read_b64 v[208:209], v23 offset:1280\n"
"ds_read_b64 v[210:211], v23 offset:2560\nds_read_b64 v[212:213], v23 offset:3840\nds_read_b32 v221, v222\nv_mov_b32 v219, 0\n"
"s_nop 1\nv_mov_b32 v219, 0\ns_nop 1\nv_mov_b32 v219, 0\n"
"s_waitcnt lgkmcnt(3)\nv_pk_add_f32 v[206:207], v[206:207], v[208:209]\ns_waitcnt lgkmcnt(1)\nv_pk_add_f32 v[210:211], v[210:211], v[212:213]\n"
"v_pk_add_f32 v[206:207], v[206:207], v[210:211]\nv_cvt_pk_bf16_f32 v220, v206, v207\nbuffer_store_dword v220, v16, s[8:11], s41 offen sc1\ns_add_u32 s51, s51, 4096\n"
"s_add_u32 s40, s40, 1\ns_mov_b32 s43, s41\ns_add_u32 s41, s41, 131072\ns_and_b32 s41, s41, 524287\n"
"s_xor_b32 s42, s41, 262144\ns_and_b32 s47, s46, 15\ns_mul_i32 s48, s47, 4\ns_add_u32 s45, s45, s48\n"
"s_add_u32 s45, s45, s30\ns_lshr_b32 s48, s45, 4\ns_lshl_b32 s48, s48, 2\ns_add_u32 s44, s44, s48\n"
"s_and_b32 s45, s45, 15\ns_add_u32 s92, s92, 1\ns_lshr_b32 s48, s46, 4\ns_cmp_gt_u32 s47, s48\n"
"s_cselect_b32 s47, 1, 0\ns_and_b32 s48, s92, 1\ns_cselect_b32 s47, 0, s47\ns_sub_u32 s46, s46, s47\n"
"s_waitcnt lgkmcnt(0)\nv_readfirstlane_b32 s47, v221\ns_sub_u32 s47, s47, s40\ns_cmp_eq_u32 s47, 1\n"
"s_cbranch_scc1 rec_p3\ns_branch top_p3\nlast_p3:\nv_smfmac_f32_16x16x64_bf16 v[68:71], v[44:47], a[8:15], v8\n"
"v_smfmac_f32_16x16x64_bf16 v[72:75], v[44:47], a[64:71], v8\nv_smfmac_f32_16x16x64_bf16 v[76:79], v[44:47], a[120:127], v8\nv_smfmac_f32_16x16x64_bf16 v[80:83], v[44:47], a[176:183], v8\nv_smfmac_f32_16x16x64_bf16 v[68:71], v[48:51], a[16:23], v8\n"
"v_smfmac_f32_16x16x64_bf16 v[72:75], v[48:51], a[72:79], v8\nv_smfmac_f32_16x16x64_bf16 v[76:79], v[48:51], a[128:135], v8\nv_smfmac_f32_16x16x64_bf16 v[80:83], v[48:51], a[184:191], v8\nv_smfmac_f32_16x16x64_bf16 v[68:71], v[52:55], a[24:31], v8\n"
"v_smfmac_f32_16x16x64_bf16 v[72:75], v[52:55], a[80:87], v8\nv_smfmac_f32_16x16x64_bf16 v[76:79], v[52:55], a[136:143], v8\nv_smfmac_f32_16x16x64_bf16 v[80:83], v[52:55], a[192:199], v8\nv_smfmac_f32_16x16x64_bf16 v[68:71], v[56:59], a[32:39], v8\n"
"v_smfmac_f32_16x16x64_bf16 v[72:75], v[56:59], a[88:95], v8\nv_smfmac_f32_16x16x64_bf16 v[76:79], v[56:59], a[144:151], v8\nv_smfmac_f32_16x16x64_bf16 v[80:83], v[56:59], a[200:207], v8\nv_smfmac_f32_16x16x64_bf16 v[68:71], v[60:63], a[40:47], v8\n"
"v_smfmac_f32_16x16x64_bf16 v[72:75], v[60:63], a[96:103], v8\nv_smfmac_f32_16x16x64_bf16 v[76:79], v[60:63], a[152:159], v8\nv_smfmac_f32_16x16x64_bf16 v[80:83], v[60:63], a[208:215], v8\nv_add_f32 v219, v68, v69\n"
"v_add_f32 v220, v70, v71\nv_cvt_pk_bf16_f32 v156, v219, v220\nv_add_f32 v219, v72, v73\nv_add_f32 v220, v74, v75\n"
"v_cvt_pk_bf16_f32 v157, v219, v220\nv_add_f32 v219, v76, v77\nv_add_f32 v220, v78, v79\nv_cvt_pk_bf16_f32 v158, v219, v220\n"
"v_add_f32 v219, v80, v81\nv_add_f32 v220, v82, v83\nv_cvt_pk_bf16_f32 v159, v219, v220\nbuffer_store_dwordx4 v[156:159], v9, s[20:23], s51 offen sc1\n"
"s_add_u32 s47, s33, 65\nv_mov_b32 v219, s47\ns_waitcnt vmcnt(0)\nbuffer_store_dword v219, v18, s[8:11], 0 offen sc1\n"
"s_branch end_p3\nretry1_p3:\ns_add_u32 s53, s53, 256\ns_add_u32 s49, s49, 1\n"
"s_cmp_gt_u32 s49, 65536\ns_cbranch_scc1 end_p3\ns_add_u32 s47, s40, 1\nv_mov_b32 v221, s47\n"
"ds_write_b32 v222, v221\nrec_p3:\ns_sleep 4\nv_mov_b32 v68, 0\n"
"v_mov_b32 v69, 0\nv_mov_b32 v70, 0\nv_mov_b32 v71, 0\nv_mov_b32 v72, 0\n"
"v_mov_b32 v73, 0\nv_mov_b32 v74, 0\nv_mov_b32 v75, 0\nv_mov_b32 v76, 0\n"
"v_mov_b32 v77, 0\nv_mov_b32 v78, 0\nv_mov_b32 v79, 0\nv_mov_b32 v80, 0\n"
"v_mov_b32 v81, 0\nv_mov_b32 v82, 0\nv_mov_b32 v83, 0\nv_mov_b32 v218, 0\n"
"s_branch top2_p3\nadopt_p3:\nbuffer_load_dwordx4 v[164:167], v28, s[8:11], 0 offen sc1\ns_lshl_b32 s52, s40, 26\n"
"s_or_b32 s47, s52, 0x3ffffff\ns_waitcnt vmcnt(0)\nv_cmp_ge_u32 vcc, s47, v164\ns_nop 1\n"
"v_cndmask_b32 v164, 0, v164, vcc\nv_cmp_ge_u32 vcc, s47, v165\ns_nop 1\nv_cndmask_b32 v165, 0, v165, vcc\n"
"v_cmp_ge_u32 vcc, s47, v166\ns_nop 1\nv_cndmask_b32 v166, 0, v166, vcc\nv_cmp_ge_u32 vcc, s47, v167\n"
"s_nop 1\nv_cndmask_b32 v167, 0, v167, vcc\nv_max3_u32 v221, v164, v165, v166\nv_max_u32 v221, v221, v167\n"
"s_nop 1\nv_max_u32_dpp v221, v221, v221 row_ror:8 row_mask:0xf bank_mask:0xf\ns_nop 1\nv_max_u32_dpp v221, v221, v221 row_ror:4 row_mask:0xf bank_mask:0xf\n"
"s_nop 1\nv_max_u32_dpp v221, v221, v221 row_ror:2 row_mask:0xf bank_mask:0xf\ns_nop 1\nv_max_u32_dpp v221, v221, v221 row_ror:1 row_mask:0xf bank_mask:0xf\n"
"s_nop 0\nv_readlane_b32 s47, v221, 0\nv_readlane_b32 s48, v221, 16\ns_max_u32 s48, s48, s47\n"
"s_cmp_le_u32 s48, s52\ns_cbranch_scc1 back_p3\ns_bfe_u32 s52, s48, 1179656\ns_add_u32 s44, s52, s28\n"
"s_mov_b32 s45, 0\ns_and_b32 s46, s48, 255\ns_mov_b32 s92, 0\ns_branch back_p3\n"
"late_p3:\ns_add_u32 s53, s53, 16777216\nv_mov_b32 v84, v100\nv_mov_b32 v85, v101\n"
"v_mov_b32 v86, v102\nv_mov_b32 v87, v103\nv_mov_b32 v88, v104\nv_mov_b32 v89, v105\n"
"v_mov_b32 v90, v106\nv_mov_b32 v91, v107\nv_mov_b32 v92, v108\nv_mov_b32 v93, v109\n"
"v_mov_b32 v94, v110\nv_mov_b32 v95, v111\nv_mov_b32 v96, v112\nv_mov_b32 v97, v113\n"
"v_mov_b32 v98, v114\nv_mov_b32 v99, v115\nv_smfmac_f32_16x16x64_bf16 v[68:71], v[44:47], a[8:15], v8\nv_smfmac_f32_16x16x64_bf16 v[72:75], v[44:47], a[64:71], v8\n"
"v_smfmac_f32_16x16x64_bf16 v[76:79], v[44:47], a[120:127], v8\nv_smfmac_f32_16x16x64_bf16 v[80:83], v[44:47], a[176:183], v8\nv_smfmac_f32_16x16x64_bf16 v[68:71], v[48:51], a[16:23], v8\nv_smfmac_f32_16x16x64_bf16 v[72:75], v[48:51], a[72:79], v8\n"
"v_smfmac_f32_16x16x64_bf16 v[76:79], v[48:51], a[128:135], v8\nv_smfmac_f32_16x16x64_bf16 v[80:83], v[48:51], a[184:191], v8\nv_smfmac_f32_16x16x64_bf16 v[68:71], v[52:55], a[24:31], v8\nv_smfmac_f32_16x16x64_bf16 v[72:75], v[52:55], a[80:87], v8\n"
"v_smfmac_f32_16x16x64_bf16 v[76:79], v[52:55], a[136:143], v8\nv_smfmac_f32_16x16x64_bf16 v[80:83], v[52:55], a[192:199], v8\nv_smfmac_f32_16x16x64_bf16 v[68:71], v[56:59], a[32:39], v8\nv_smfmac_f32_16x16x64_bf16 v[72:75], v[56:59], a[88:95], v8\n"
"v_smfmac_f32_16x16x64_bf16 v[76:79], v[56:59], a[144:151], v8\nv_smfmac_f32_16x16x64_bf16 v[80:83], v[56:59], a[200:207], v8\nv_smfmac_f32_16x16x64_bf16 v[68:71], v[60:63], a[40:47], v8\nv_smfmac_f32_16x16x64_bf16 v[72:75], v[60:63], a[96:103], v8\n"
"v_smfmac_f32_16x16x64_bf16 v[76:79], v[60:63], a[152:159], v8\nv_smfmac_f32_16x16x64_bf16 v[80:83], v[60:63], a[208:215], v8\nv_add_f32 v219, v68, v69\nv_add_f32 v220, v70, v71\n"
"v_cvt_pk_bf16_f32 v156, v219, v220\nv_add_f32 v219, v72, v73\nv_add_f32 v220, v74, v75\nv_cvt_pk_bf16_f32 v157, v219, v220\n"
"v_add_f32 v219, v76, v77\nv_add_f32 v220, v78, v79\nv_cvt_pk_bf16_f32 v158, v219, v220\nv_add_f32 v219, v80, v81\n"
"v_add_f32 v220, v82, v83\nv_cvt_pk_bf16_f32 v159, v219, v220\nbuffer_load_dwordx4 v[100:103], v9, s[16:19], s50 offen offset:2048 sc1\nbuffer_load_dwordx4 v[104:107], v9, s[16:19], s50 offen offset:3072 sc1\n"
"buffer_load_dwordx4 v[108:111], v9, s[16:19], s50 offen offset:0 sc1\nbuffer_load_dwordx4 v[112:115], v9, s[16:19], s50 offen offset:1024 sc1\ns_add_u32 s50, s50, 16384\nbuffer_store_dwordx4 v[156:159], v9, s[20:23], s51 offen sc1\n"
"s_branch spin_p3\nslow_p3:\nv_max3_u32 v218, v124, v125, v126\nv_max_u32 v218, v218, v127\n"
"v_max3_u32 v218, v218, v128, v129\nv_max3_u32 v218, v218, v130, v131\nv_max3_u32 v218, v218, v116, v117\nv_max3_u32 v218, v218, v118, v119\n"
"v_max3_u32 v218, v218, v120, v121\nv_max3_u32 v218, v218, v122, v123\nv_cmp_eq_u32 vcc, -1, v218\ns_cbranch_vccz exact_p3\n"
"s_branch spin_p3\nspin2_p3:\ns_sleep 3\nspin_p3:\n"
"s_add_u32 s53, s53, 1\ns_add_u32 s49, s49, 1\ns_cmp_gt_u32 s49, 65536\ns_cbranch_scc1 end_p3\n"
"buffer_load_dwordx4 v[116:119], v20, s[8:11], s41 offen offset:1024 sc1\nbuffer_load_dwordx4 v[120:123], v20, s[8:11], s41 offen offset:0 sc1\nbuffer_load_dwordx4 v[124:127], v21, s[8:11], s41 offen offset:1024 sc1\nbuffer_load_dwordx4 v[128:131], v21, s[8:11], s41 offen offset:0 sc1\n"
"s_waitcnt vmcnt(0)\nv_max3_u32 v218, v124, v125, v126\nv_max_u32 v218, v218, v127\nv_max3_u32 v218, v218, v128, v129\n"
"v_max3_u32 v218, v218, v130, v131\nv_max3_u32 v218, v218, v116, v117\nv_max3_u32 v218, v218, v118, v119\nv_max3_u32 v218, v218, v120, v121\n"
"v_max3_u32 v218, v218, v122, v123\nv_cmp_eq_u32 vcc, -1, v218\ns_cbranch_vccnz spin2_p3\ns_memrealtime s[54:55]\n"
"s_add_u32 s52, s40, 1\ns_lshl_b32 s52, s52, 26\ns_waitcnt lgkmcnt(0)\ns_sub_u32 s47, s54, s28\n"
"s_add_u32 s47, s47, s38\ns_min_u32 s47, s47, 0x3ffff\ns_lshl_b32 s47, s47, 8\ns_or_b32 s47, s47, s52\n"
"s_and_b32 s48, s46, 15\ns_add_u32 s52, s48, 0\ns_min_u32 s52, s52, 15\ns_or_b32 s47, s47, s52\n"
"s_add_u32 s48, s48, 0\ns_min_u32 s48, s48, s52\ns_lshl_b32 s48, s48, 4\ns_and_b32 s52, s46, 0xf0\n"
"s_cmp_ge_u32 s92, 99\ns_cselect_b32 s48, s48, s52\ns_or_b32 s47, s47, s48\nv_mov_b32 v225, s47\n"
"buffer_store_dword v225, v19, s[8:11], 0 offen sc1\ns_add_u32 s47, s40, 2\nv_mov_b32 v221, s47\nds_write_b32 v222, v221\n"
"v_mfma_f32_16x16x32_bf16 v[132:135], v[84:87], v[116:119], 0\nv_mfma_f32_16x16x32_bf16 v[136:139], v[88:91], v[116:119], 0\nv_mfma_f32_16x16x32_bf16 v[148:151], v[176:179], v[116:119], 0\nv_mfma_f32_16x16x32_bf16 v[140:143], v[92:95], v[120:123], 0\n"
"v_mfma_f32_16x16x32_bf16 v[144:147], v[96:99], v[120:123], 0\nv_mfma_f32_16x16x32_bf16 v[148:151], v[180:183], v[120:123], v[148:151]\nv_cndmask_b32 v132, v136, v132, s[62:63]\nv_cndmask_b32 v133, v137, v133, s[62:63]\n"
"v_cndmask_b32 v134, v138, v134, s[62:63]\nv_cndmask_b32 v135, v139, v135, s[62:63]\nv_mfma_f32_16x16x32_f16 v[152:155], v[168:171], v[124:127], 0\nv_mfma_f32_16x16x32_f16 v[152:155], v[172:175], v[128:131], v[152:155]\n"
"v_cndmask_b32 v140, v144, v140, s[62:63]\nv_cndmask_b32 v141, v145, v141, s[62:63]\nv_cndmask_b32 v142, v146, v142, s[62:63]\nv_cndmask_b32 v143, v147, v143, s[62:63]\n"
"s_nop 2\nv_exp_f32 v152, v152\nv_exp_f32 v153, v153\nv_exp_f32 v154, v154\n"
"v_exp_f32 v155, v155\nv_cvt_pk_bf16_f32 v200, v152, v153\nv_cvt_pk_bf16_f32 v201, v154, v155\nv_and_b32 v160, v200, v223\n"
"v_and_b32 v161, v200, v224\nv_mfma_f32_16x16x16_bf16 v[148:151], v[214:215], v[200:201], v[148:151]\nv_and_b32 v162, v201, v223\nv_and_b32 v163, v201, v224\n"
"v_mfma_f32_16x16x16_bf16 v[132:135], v[156:157], v[160:161], v[132:135]\ns_nop 0\nv_mfma_f32_16x16x16_bf16 v[140:143], v[158:159], v[162:163], v[140:143]\nv_rcp_f32 v202, v148\n"
"v_rcp_f32 v203, v149\nv_mul_f32 v132, v132, v202\nv_mul_f32 v133, v133, v202\nv_mul_f32 v134, v134, v202\n"
"v_mul_f32 v135, v135, v202\nv_fmac_f32 v132, v140, v203\nv_fmac_f32 v133, v141, v203\nv_fmac_f32 v134, v142, v203\n"
"v_fmac_f32 v135, v143, v203\nv_add_f32_dpp v204, v132, v132 row_ror:8 row_mask:0xf bank_mask:0x3\nv_add_f32_dpp v205, v133, v133 row_ror:8 row_mask:0xf bank_mask:0x3\nv_add_f32_dpp v204, v134, v134 row_ror:8 row_mask:0xf bank_mask:0xc\n"
"v_add_f32_dpp v205, v135, v135 row_ror:8 row_mask:0xf bank_mask:0xc\nv_subrev_u32 v219, 0x2f800000, v148\nv_subrev_u32 v220, 0x2f800000, v149\nv_max_u32 v219, v219, v220\n"
"v_cmp_le_u32 vcc, 0x20000000, v219\nds_write_b64 v22, v[204:205]\ns_cbranch_vccz join_p3\nexact_p3:\n"
"s_add_u32 s53, s53, 65536\nex0_p3:\ns_add_u32 s47, s41, 4096\nbuffer_load_dwordx2 v[184:185], v24, s[8:11], s47 offen offset:0 sc1\n"
"buffer_load_dwordx2 v[186:187], v24, s[8:11], s47 offen offset:512 sc1\nbuffer_load_dwordx2 v[188:189], v24, s[8:11], s47 offen offset:1024 sc1\nbuffer_load_dwordx2 v[190:191], v24, s[8:11], s47 offen offset:1536 sc1\nbuffer_load_dwordx4 v[192:195], v25, s[8:11], s47 offen sc1\n"
"buffer_load_dwordx4 v[196:199], v25, s[8:11], s47 offen offset:16 sc1\nv_mov_b32 v218, 0\ns_waitcnt vmcnt(0)\nv_max3_u32 v218, v218, v184, v185\n"
"v_max3_u32 v218, v218, v186, v187\nv_max3_u32 v218, v218, v188, v189\nv_max3_u32 v218, v218, v190, v191\nv_max3_u32 v218, v218, v192, v193\n"
"v_max3_u32 v218, v218, v194, v195\nv_max3_u32 v218, v218, v196, v197\nv_max3_u32 v218, v218, v198, v199\nv_cmp_eq_u32 vcc, -1, v218\n"
"s_cbranch_vccz exok0_p3\ns_add_u32 s53, s53, 0\ns_add_u32 s49, s49, 1\ns_cmp_gt_u32 s49, 65536\n"
"s_cbranch_scc1 end_p3\ns_branch ex0_p3\nexok0_p3:\nv_add_f32 v184, v184, v186\n"
"v_add_f32 v188, v188, v190\nv_add_f32 v184, v184, v188\nv_add_f32 v185, v185, v187\nv_add_f32 v189, v189, v191\n"
"v_add_f32 v185, v185, v189\nv_max_f32 v225, v184, v185\nv_max3_f32 v225, v225, v192, v193\nv_max3_f32 v225, v225, v194, v195\n"
"v_max3_f32 v225, v225, v196, v197\nv_max3_f32 v225, v225, v198, v199\nv_mov_b32 v219, v225\ns_nop 1\n"
"v_permlane16_swap_b32 v225, v219\nv_max_f32 v225, v225, v219\nv_mov_b32 v219, v225\ns_nop 1\n"
"v_permlane32_swap_b32 v225, v219\nv_max_f32 v225, v225, v219\nv_sub_f32 v184, v184, v225\nv_exp_f32 v152, v184\n"
"v_sub_f32 v185, v185, v225\nv_exp_f32 v153, v185\nv_sub_f32 v192, v192, v225\nv_exp_f32 v192, v192\n"
"v_sub_f32 v193, v193, v225\nv_exp_f32 v193, v193\nv_sub_f32 v194, v194, v225\nv_exp_f32 v194, v194\n"
"v_sub_f32 v195, v195, v225\nv_exp_f32 v195, v195\nv_sub_f32 v196, v196, v225\nv_exp_f32 v196, v196\n"
"v_sub_f32 v197, v197, v225\nv_exp_f32 v197, v197\nv_sub_f32 v198, v198, v225\nv_exp_f32 v198, v198\n"
"v_sub_f32 v199, v199, v225\nv_exp_f32 v199, v199\ns_nop 0\nv_cvt_pk_bf16_f32 v116, v192, v193\n"
"v_cvt_pk_bf16_f32 v117, v194, v195\nv_cvt_pk_bf16_f32 v118, v196, v197\nv_cvt_pk_bf16_f32 v119, v198, v199\nex1_p3:\n"
"s_add_u32 s47, s41, 0\nbuffer_load_dwordx2 v[184:185], v24, s[8:11], s47 offen offset:0 sc1\nbuffer_load_dwordx2 v[186:187], v24, s[8:11], s47 offen offset:512 sc1\nbuffer_load_dwordx2 v[188:189], v24, s[8:11], s47 offen offset:1024 sc1\n"
"buffer_load_dwordx2 v[190:191], v24, s[8:11], s47 offen offset:1536 sc1\nbuffer_load_dwordx4 v[192:195], v25, s[8:11], s47 offen sc1\nbuffer_load_dwordx4 v[196:199], v25, s[8:11], s47 offen offset:16 sc1\nv_mov_b32 v218, 0\n"
"s_waitcnt vmcnt(0)\nv_max3_u32 v218, v218, v184, v185\nv_max3_u32 v218, v218, v186, v187\nv_max3_u32 v218, v218, v188, v189\n"
"v_max3_u32 v218, v218, v190, v191\nv_max3_u32 v218, v218, v192, v193\nv_max3_u32 v218, v218, v194, v195\nv_max3_u32 v218, v218, v196, v197\n"
"v_max3_u32 v218, v218, v198, v199\nv_cmp_eq_u32 vcc, -1, v218\ns_cbranch_vccz exok1_p3\ns_add_u32 s53, s53, 0\n"
"s_add_u32 s49, s49, 1\ns_cmp_gt_u32 s49, 65536\ns_cbranch_scc1 end_p3\ns_branch ex1_p3\n"
"exok1_p3:\nv_add_f32 v184, v184, v186\nv_add_f32 v188, v188, v190\nv_add_f32 v184, v184, v188\n"
"v_add_f32 v185, v185, v187\nv_add_f32 v189, v189, v191\nv_add_f32 v185, v185, v189\nv_max_f32 v225, v184, v185\n"
"v_max3_f32 v225, v225, v192, v193\nv_max3_f32 v225, v225, v194, v195\nv_max3_f32 v225, v225, v196, v197\nv_max3_f32 v225, v225, v198, v199\n"
"v_mov_b32 v219, v225\ns_nop 1\nv_permlane16_swap_b32 v225, v219\nv_max_f32 v225, v225, v219\n"
"v_mov_b32 v219, v225\ns_nop 1\nv_permlane32_swap_b32 v225, v219\nv_max_f32 v225, v225, v219\n"
"v_sub_f32 v184, v184, v225\nv_exp_f32 v154, v184\nv_sub_f32 v185, v185, v225\nv_exp_f32 v155, v185\n"
"v_sub_f32 v192, v192, v225\nv_exp_f32 v192, v192\nv_sub_f32 v193, v193, v225\nv_exp_f32 v193, v193\n"
"v_sub_f32 v194, v194, v225\nv_exp_f32 v194, v194\nv_sub_f32 v195, v195, v225\nv_exp_f32 v195, v195\n"
"v_sub_f32 v196, v196, v225\nv_exp_f32 v196, v196\nv_sub_f32 v197, v197, v225\nv_exp_f32 v197, v197\n"
"v_sub_f32 v198, v198, v225\nv_exp_f32 v198, v198\nv_sub_f32 v199, v199, v225\nv_exp_f32 v199, v199\n"
"s_nop 0\nv_cvt_pk_bf16_f32 v120, v192, v193\nv_cvt_pk_bf16_f32 v121, v194, v195\nv_cvt_pk_bf16_f32 v122, v196, v197\n"
"v_cvt_pk_bf16_f32 v123, v198, v199\ns_memrealtime s[54:55]\ns_add_u32 s52, s40, 1\ns_lshl_b32 s52, s52, 26\n"
"s_waitcnt lgkmcnt(0)\ns_sub_u32 s47, s54, s28\ns_add_u32 s47, s47, s38\ns_min_u32 s47, s47, 0x3ffff\n"
"s_lshl_b32 s47, s47, 8\ns_or_b32 s47, s47, s52\ns_and_b32 s48, s46, 15\ns_add_u32 s52, s48, 0\n"
"s_min_u32 s52, s52, 15\ns_or_b32 s47, s47, s52\ns_add_u32 s48, s48, 0\ns_min_u32 s48, s48, s52\n"
"s_lshl_b32 s48, s48, 4\ns_and_b32 s52, s46, 0xf0\ns_cmp_ge_u32 s92, 99\ns_cselect_b32 s48, s48, s52\n"
"s_or_b32 s47, s47, s48\nv_mov_b32 v225, s47\nbuffer_store_dword v225, v19, s[8:11], 0 offen sc1\ns_add_u32 s47, s40, 2\n"
"v_mov_b32 v221, s47\nds_write_b32 v222, v221\nv_mfma_f32_16x16x32_bf16 v[132:135], v[84:87], v[116:119], 0\nv_mfma_f32_16x16x32_bf16 v[136:139], v[88:91], v[116:119], 0\n"
"v_mfma_f32_16x16x32_bf16 v[148:151], v[176:179], v[116:119], 0\nv_mfma_f32_16x16x32_bf16 v[140:143], v[92:95], v[120:123], 0\nv_mfma_f32_16x16x32_bf16 v[144:147], v[96:99], v[120:123], 0\nv_mfma_f32_16x16x32_bf16 v[148:151], v[180:183], v[120:123], v[148:151]\n"
"v_cndmask_b32 v132, v136, v132, s[62:63]\nv_cndmask_b32 v133, v137, v133, s[62:63]\nv_cndmask_b32 v134, v138, v134, s[62:63]\nv_cndmask_b32 v135, v139, v135, s[62:63]\n"
"s_nop 0\nv_cndmask_b32 v140, v144, v140, s[62:63]\nv_cndmask_b32 v141, v145, v141, s[62:63]\nv_cndmask_b32 v142, v146, v142, s[62:63]\n"
"v_cndmask_b32 v143, v147, v143, s[62:63]\nv_cvt_pk_bf16_f32 v200, v152, v153\nv_cvt_pk_bf16_f32 v201, v154, v155\nv_and_b32 v160, v200, v223\n"
"v_and_b32 v161, v200, v224\nv_mfma_f32_16x16x16_bf16 v[148:151], v[214:215], v[200:201], v[148:151]\nv_and_b32 v162, v201, v223\nv_and_b32 v163, v201, v224\n"
"v_mfma_f32_16x16x16_bf16 v[132:135], v[156:157], v[160:161], v[132:135]\ns_nop 0\nv_mfma_f32_16x16x16_bf16 v[140:143], v[158:159], v[162:163], v[140:143]\nv_rcp_f32 v202, v148\n"
"v_rcp_f32 v203, v149\nv_mul_f32 v132, v132, v202\nv_mul_f32 v133, v133, v202\nv_mul_f32 v134, v134, v202\n"
"v_mul_f32 v135, v135, v202\nv_fmac_f32 v132, v140, v203\nv_fmac_f32 v133, v141, v203\nv_fmac_f32 v134, v142, v203\n"
"v_fmac_f32 v135, v143, v203\nv_add_f32_dpp v204, v132, v132 row_ror:8 row_mask:0xf bank_mask:0x3\nv_add_f32_dpp v205, v133, v133 row_ror:8 row_mask:0xf bank_mask:0x3\nv_add_f32_dpp v204, v134, v134 row_ror:8 row_mask:0xf bank_mask:0xc\n"
"v_add_f32_dpp v205, v135, v135 row_ror:8 row_mask:0xf bank_mask:0xc\nds_write_b64 v22, v[204:205]\ns_branch join_p3\nend_p3:\n"
"s_branch fk_done\nfk_done:\ns_endpgm\n"
    :: "{s[8:23]}"(s0), "{s[24:39]}"(s1), "{v[8:23]}"(v0), "{v[24:39]}"(v1) : "v40", "v41", "v42", "v43", "v44", "v45", "v46", "v47", "v48", "v49", "v50", "v51", "v52", "v53", "v54", "v55", "v56", "v57", "v58", "v59", "v60", "v61", "v62", "v63", "v64", "v65", "v66", "v67", "v68", "v69", "v70", "v71", "v72", "v73", "v74", "v75", "v76", "v77", "v78", "v79", "v80", "v81", "v82", "v83", "v84", "v85", "v86", "v87", "v88", "v89", "v90", "v91", "v92", "v93", "v94", "v95", "v96", "v97", "v98", "v99", "v100", "v101", "v102", "v103", "v104", "v105", "v106", "v107", "v108", "v109", "v110", "v111", "v112", "v113", "v114", "v115", "v116", "v117", "v118", "v119", "v120", "v121", "v122", "v123", "v124", "v125", "v126", "v127", "v128", "v129", "v130", "v131", "v132", "v133", "v134", "v135", "v136", "v137", "v138", "v139", "v140", "v141", "v142", "v143", "v144", "v145", "v146", "v147", "v148", "v149", "v150", "v151", "v152", "v153", "v154", "v155", "v156", "v157", "v158", "v159", "v160", "v161", "v162", "v163", "v164", "v165", "v166", "v167", "v168", "v169", "v170", "v171", "v172", "v173", "v174", "v175", "v176", "v177", "v178", "v179", "v180", "v181", "v182", "v183", "v184", "v185", "v186", "v187", "v188", "v189", "v190", "v191", "v192", "v193", "v194", "v195", "v196", "v197", "v198", "v199", "v200", "v201", "v202", "v203", "v204", "v205", "v206", "v207", "v208", "v209", "v210", "v211", "v212", "v213", "v214", "v215", "v216", "v217", "v218", "v219", "v220", "v221", "v222", "v223", "v224", "v225", "v226", "v227", "v228", "v229", "v230", "v231", "v232", "v233", "v234", "v235", "v236", "v237", "v238", "v239", "v240", "v241", "v242", "v243", "v244", "v245", "v246", "v247", "v248", "v249", "v250", "v251", "v252", "v253", "v254", "v255", "a0", "a1", "a2", "a3", "a4", "a5", "a6", "a7", "a8", "a9", "a10", "a11", "a12", "a13", "a14", "a15", "a16", "a17", "a18", "a19", "a20", "a21", "a22", "a23", "a24", "a25", "a26", "a27", "a28", "a29", "a30", "a31", "a32", "a33", "a34", "a35", "a36", "a37", "a38", "a39", "a40", "a41", "a42", "a43", "a44", "a45", "a46", "a47", "a48", "a49", "a50", "a51", "a52", "a53", "a54", "a55", "a56", "a57", "a58", "a59", "a60", "a61", "a62", "a63", "a64", "a65", "a66", "a67", "a68", "a69", "a70", "a71", "a72", "a73", "a74", "a75", "a76", "a77", "a78", "a79", "a80", "a81", "a82", "a83", "a84", "a85", "a86", "a87", "a88", "a89", "a90", "a91", "a92", "a93", "a94", "a95", "a96", "a97", "a98", "a99", "a100", "a101", "a102", "a103", "a104", "a105", "a106", "a107", "a108", "a109", "a110", "a111", "a112", "a113", "a114", "a115", "a116", "a117", "a118", "a119", "a120", "a121", "a122", "a123", "a124", "a125", "a126", "a127", "a128", "a129", "a130", "a131", "a132", "a133", "a134", "a135", "a136", "a137", "a138", "a139", "a140", "a141", "a142", "a143", "a144", "a145", "a146", "a147", "a148", "a149", "a150", "a151", "a152", "a153", "a154", "a155", "a156", "a157", "a158", "a159", "a160", "a161", "a162", "a163", "a164", "a165", "a166", "a167", "a168", "a169", "a170", "a171", "a172", "a173", "a174", "a175", "a176", "a177", "a178", "a179", "a180", "a181", "a182", "a183", "a184", "a185", "a186", "a187", "a188", "a189", "a190", "a191", "a192", "a193", "a194", "a195", "a196", "a197", "a198", "a199", "a200", "a201", "a202", "a203", "a204", "a205", "a206", "a207", "a208", "a209", "a210", "a211", "a212", "a213", "a214", "a215", "a216", "a217", "a218", "a219", "a220", "a221", "a222", "a223", "a224", "a225", "a226", "a227", "a228", "a229", "a230", "a231", "a232", "a233", "a234", "a235", "a236", "a237", "a238", "a239", "a240", "a241", "a242", "a243", "a244", "a245", "a246", "a247", "a248", "a249", "a250", "a251", "a252", "a253", "a254", "a255", "s40", "s41", "s42", "s43", "s44", "s45", "s46", "s47", "s48", "s49", "s50", "s51", "s52", "s53", "s54", "s55", "s56", "s57", "s58", "s59", "s60", "s61", "s62", "s63", "s64", "s65", "s66", "s67", "s68", "s69", "s70", "s71", "s72", "s73", "s74", "s75", "s76", "s77", "s78", "s79", "s80", "s81", "s82", "s83", "s84", "s85", "s86", "s87", "s88", "s89", "s90", "s91", "s92", "s93", "s94", "s95", "vcc", "scc", "memory");
    __builtin_unreachable();
}
struct GCtx { u4 rw1, rx, rexp, rout, rhb; unsigned tag, rot, sw1, mw, mx, sfg, stay, csc, ostr, p0, p1, p2; unsigned vr0, vr1, vx, wa, xa, vexp, vimp, vo0, vo1, vo2, vo3, vfl, vpf, p3, p4, p5; };
DI unsigned gemm_asm0(const GCtx& c, u16v k0, u16v k1, u16v k2, u16v k3) {
    const u16v s0 = {RFL(c.rw1[0]), RFL(c.rw1[1]), RFL(c.rw1[2]), RFL(c.rw1[3]), RFL(c.rx[0]), RFL(c.rx[1]), RFL(c.rx[2]), RFL(c.rx[3]), RFL(c.rexp[0]), RFL(c.rexp[1]), RFL(c.rexp[2]), RFL(c.rexp[3]), RFL(c.rout[0]), RFL(c.rout[1]), RFL(c.rout[2]), RFL(c.rout[3])}, s1 = {RFL(c.rhb[0]), RFL(c.rhb[1]), RFL(c.rhb[2]), RFL(c.rhb[3]), RFL(c.tag), RFL(c.rot), RFL(c.sw1), RFL(c.mw), RFL(c.mx), RFL(c.sfg), RFL(c.stay), RFL(c.csc), RFL(c.ostr), RFL(c.p0), RFL(c.p1), RFL(c.p2)}, v0 = {c.vr0, c.vr1, c.vx, c.wa, c.xa, c.vexp, c.vimp, c.vo0, c.vo1, c.vo2, c.vo3, c.vfl, c.vpf, c.p3, c.p4, c.p5};
    unsigned o = 0;
    asm volatile(
"s_nop 4\ns_add_u32 s40, s29, 0\ns_sub_u32 s53, s40, 7\ns_cmp_ge_u32 s40, 7\n"
"s_cselect_b32 s40, s53, s40\ns_add_u32 s41, s29, 1\ns_sub_u32 s53, s41, 7\ns_cmp_ge_u32 s41, 7\n"
"s_cselect_b32 s41, s53, s41\ns_add_u32 s42, s29, 2\ns_sub_u32 s53, s42, 7\ns_cmp_ge_u32 s42, 7\n"
"s_cselect_b32 s42, s53, s42\ns_add_u32 s43, s29, 3\ns_sub_u32 s53, s43, 7\ns_cmp_ge_u32 s43, 7\n"
"s_cselect_b32 s43, s53, s43\ns_add_u32 s44, s29, 4\ns_sub_u32 s53, s44, 7\ns_cmp_ge_u32 s44, 7\n"
"s_cselect_b32 s44, s53, s44\ns_add_u32 s45, s29, 5\ns_sub_u32 s53, s45, 7\ns_cmp_ge_u32 s45, 7\n"
"s_cselect_b32 s45, s53, s45\ns_add_u32 s46, s29, 6\ns_sub_u32 s53, s46, 7\ns_cmp_ge_u32 s46, 7\n"
"s_cselect_b32 s46, s53, s46\nv_xor_b32 v24, 0, v11\nv_add_u32 v24, 65536, v24\nv_xor_b32 v25, 64, v11\n"
"v_add_u32 v25, 65536, v25\nv_xor_b32 v26, 128, v11\nv_add_u32 v26, 65536, v26\nv_xor_b32 v27, 192, v11\n"
"v_add_u32 v27, 65536, v27\nv_xor_b32 v28, 16, v11\nv_add_u32 v28, 67584, v28\nv_xor_b32 v29, 80, v11\n"
"v_add_u32 v29, 67584, v29\nv_xor_b32 v30, 144, v11\nv_add_u32 v30, 67584, v30\nv_xor_b32 v31, 208, v11\n"
"v_add_u32 v31, 67584, v31\nv_xor_b32 v32, 0, v12\nv_add_u32 v32, 0, v32\nv_xor_b32 v33, 0, v12\n"
"v_add_u32 v33, 2048, v33\nv_xor_b32 v34, 64, v12\nv_add_u32 v34, 0, v34\nv_xor_b32 v35, 64, v12\n"
"v_add_u32 v35, 2048, v35\ns_mov_b32 s55, 0\ns_waitcnt vmcnt(23)\ns_barrier\n"
"ds_read_b128 v[40:43], v24 offset:0\nds_read_b128 v[44:47], v28 offset:0\nds_read_b128 v[48:51], v25 offset:0\nds_read_b128 v[52:55], v29 offset:0\n"
"s_waitcnt lgkmcnt(3)\nv_mfma_f32_16x16x32_bf16 v[64:67], v[40:43], a[128:131], 0\nv_mfma_f32_16x16x32_bf16 v[68:71], v[40:43], a[160:163], 0\nds_read_b128 v[40:43], v26 offset:0\n"
"s_waitcnt lgkmcnt(3)\nv_mfma_f32_16x16x32_bf16 v[72:75], v[44:47], a[128:131], 0\nv_mfma_f32_16x16x32_bf16 v[76:79], v[44:47], a[160:163], 0\nds_read_b128 v[44:47], v30 offset:0\n"
"s_lshl_b32 s53, s43, 19\ns_add_u32 s53, s53, s30\ns_waitcnt lgkmcnt(3)\nv_mfma_f32_16x16x32_bf16 v[64:67], v[48:51], a[132:135], v[64:67]\n"
"s_add_u32 m0, s31, 32768\ns_add_u32 s53, s53, 0\nv_mfma_f32_16x16x32_bf16 v[68:71], v[48:51], a[164:167], v[68:71]\nds_read_b128 v[48:51], v27 offset:0\n"
"buffer_load_dwordx4 v8, s[8:11], s53 offen lds\ns_waitcnt lgkmcnt(3)\nv_mfma_f32_16x16x32_bf16 v[72:75], v[52:55], a[132:135], v[72:75]\nbuffer_load_dwordx4 v9, s[8:11], s53 offen offset:1024 lds\n"
"v_mfma_f32_16x16x32_bf16 v[76:79], v[52:55], a[164:167], v[76:79]\nds_read_b128 v[52:55], v31 offset:0\ns_lshl_b32 s53, s43, 19\ns_add_u32 s53, s53, s30\n"
"s_waitcnt lgkmcnt(3)\nv_mfma_f32_16x16x32_bf16 v[64:67], v[40:43], a[136:139], v[64:67]\ns_add_u32 m0, s31, 40960\ns_add_u32 s53, s53, 131072\n"
"v_mfma_f32_16x16x32_bf16 v[68:71], v[40:43], a[168:171], v[68:71]\nds_read_b128 v[40:43], v24 offset:256\nbuffer_load_dwordx4 v8, s[8:11], s53 offen lds\ns_waitcnt lgkmcnt(3)\n"
"v_mfma_f32_16x16x32_bf16 v[72:75], v[44:47], a[136:139], v[72:75]\nbuffer_load_dwordx4 v9, s[8:11], s53 offen offset:1024 lds\nv_mfma_f32_16x16x32_bf16 v[76:79], v[44:47], a[168:171], v[76:79]\nds_read_b128 v[44:47], v28 offset:256\n"
"s_waitcnt lgkmcnt(3)\nv_mfma_f32_16x16x32_bf16 v[64:67], v[48:51], a[140:143], v[64:67]\nv_mfma_f32_16x16x32_bf16 v[68:71], v[48:51], a[172:175], v[68:71]\nds_read_b128 v[48:51], v25 offset:256\n"
"s_waitcnt lgkmcnt(3)\nv_mfma_f32_16x16x32_bf16 v[72:75], v[52:55], a[140:143], v[72:75]\nv_mfma_f32_16x16x32_bf16 v[76:79], v[52:55], a[172:175], v[76:79]\nds_read_b128 v[52:55], v29 offset:256\n"
"s_waitcnt lgkmcnt(3)\nv_mfma_f32_16x16x32_bf16 v[64:67], v[40:43], a[144:147], v[64:67]\nv_mfma_f32_16x16x32_bf16 v[68:71], v[40:43], a[176:179], v[68:71]\nds_read_b128 v[40:43], v26 offset:256\n"
"s_waitcnt lgkmcnt(3)\nv_mfma_f32_16x16x32_bf16 v[72:75], v[44:47], a[144:147], v[72:75]\nv_mfma_f32_16x16x32_bf16 v[76:79], v[44:47], a[176:179], v[76:79]\nds_read_b128 v[44:47], v30 offset:256\n"
"s_waitcnt lgkmcnt(3)\nv_mfma_f32_16x16x32_bf16 v[64:67], v[48:51], a[148:151], v[64:67]\nv_mfma_f32_16x16x32_bf16 v[68:71], v[48:51], a[180:183], v[68:71]\nds_read_b128 v[48:51], v27 offset:256\n"
"s_waitcnt lgkmcnt(3)\nv_mfma_f32_16x16x32_bf16 v[72:75], v[52:55], a[148:151], v[72:75]\nv_mfma_f32_16x16x32_bf16 v[76:79], v[52:55], a[180:183], v[76:79]\nds_read_b128 v[52:55], v31 offset:256\n"
"s_waitcnt lgkmcnt(3)\nv_mfma_f32_16x16x32_bf16 v[64:67], v[40:43], a[152:155], v[64:67]\nv_mfma_f32_16x16x32_bf16 v[68:71], v[40:43], a[184:187], v[68:71]\nds_read_b128 v[40:43], v24 offset:16384\n"
"s_waitcnt lgkmcnt(3)\nv_mfma_f32_16x16x32_bf16 v[72:75], v[44:47], a[152:155], v[72:75]\nv_mfma_f32_16x16x32_bf16 v[76:79], v[44:47], a[184:187], v[76:79]\nds_read_b128 v[44:47], v28 offset:16384\n"
"s_waitcnt lgkmcnt(3)\nv_mfma_f32_16x16x32_bf16 v[64:67], v[48:51], a[156:159], v[64:67]\nv_mfma_f32_16x16x32_bf16 v[68:71], v[48:51], a[188:191], v[68:71]\nds_read_b128 v[48:51], v25 offset:16384\n"
"s_waitcnt lgkmcnt(3)\nv_mfma_f32_16x16x32_bf16 v[72:75], v[52:55], a[156:159], v[72:75]\nv_mfma_f32_16x16x32_bf16 v[76:79], v[52:55], a[188:191], v[76:79]\nds_read_b128 v[52:55], v29 offset:16384\n"
"s_waitcnt lgkmcnt(3)\nv_mfma_f32_16x16x32_bf16 v[80:83], v[40:43], a[128:131], 0\nv_mfma_f32_16x16x32_bf16 v[84:87], v[40:43], a[160:163], 0\nds_read_b128 v[40:43], v26 offset:16384\n"
"s_waitcnt lgkmcnt(3)\nv_mfma_f32_16x16x32_bf16 v[88:91], v[44:47], a[128:131], 0\nv_mfma_f32_16x16x32_bf16 v[92:95], v[44:47], a[160:163], 0\nds_read_b128 v[44:47], v30 offset:16384\n"
"v_mul_f32 v64, s35, v64\nv_mul_f32 v65, s35, v65\ns_waitcnt lgkmcnt(3)\nv_mfma_f32_16x16x32_bf16 v[80:83], v[48:51], a[132:135], v[80:83]\n"
"v_mul_f32 v66, s35, v66\nv_mul_f32 v67, s35, v67\nv_mfma_f32_16x16x32_bf16 v[84:87], v[48:51], a[164:167], v[84:87]\nds_read_b128 v[48:51], v27 offset:16384\n"
"v_cvt_pk_bf16_f32 v128, v64, v65\nv_cvt_pk_bf16_f32 v129, v66, v67\ns_waitcnt lgkmcnt(3)\nv_mfma_f32_16x16x32_bf16 v[88:91], v[52:55], a[132:135], v[88:91]\n"
"v_mul_f32 v72, s35, v72\nv_mul_f32 v73, s35, v73\nv_mfma_f32_16x16x32_bf16 v[92:95], v[52:55], a[164:167], v[92:95]\nds_read_b128 v[52:55], v31 offset:16384\n"
"v_mul_f32 v74, s35, v74\nv_mul_f32 v75, s35, v75\ns_waitcnt lgkmcnt(3)\nv_mfma_f32_16x16x32_bf16 v[80:83], v[40:43], a[136:139], v[80:83]\n"
"v_cvt_pk_bf16_f32 v130, v72, v73\nv_cvt_pk_bf16_f32 v131, v74, v75\nv_mfma_f32_16x16x32_bf16 v[84:87], v[40:43], a[168:171], v[84:87]\nds_read_b128 v[40:43], v24 offset:16640\n"
"v_mul_f32 v68, s35, v68\nv_mul_f32 v69, s35, v69\ns_waitcnt lgkmcnt(3)\nv_mfma_f32_16x16x32_bf16 v[88:91], v[44:47], a[136:139], v[88:91]\n"
"v_mul_f32 v70, s35, v70\nv_mul_f32 v71, s35, v71\nv_mfma_f32_16x16x32_bf16 v[92:95], v[44:47], a[168:171], v[92:95]\nds_read_b128 v[44:47], v28 offset:16640\n"
"v_cvt_pk_bf16_f32 v132, v68, v69\nv_cvt_pk_bf16_f32 v133, v70, v71\ns_waitcnt lgkmcnt(3)\nv_mfma_f32_16x16x32_bf16 v[80:83], v[48:51], a[140:143], v[80:83]\n"
"v_mul_f32 v76, s35, v76\nv_mul_f32 v77, s35, v77\nv_mfma_f32_16x16x32_bf16 v[84:87], v[48:51], a[172:175], v[84:87]\nds_read_b128 v[48:51], v25 offset:16640\n"
"v_mul_f32 v78, s35, v78\nv_mul_f32 v79, s35, v79\ns_waitcnt lgkmcnt(3)\nv_mfma_f32_16x16x32_bf16 v[88:91], v[52:55], a[140:143], v[88:91]\n"
"v_cvt_pk_bf16_f32 v134, v76, v77\nv_cvt_pk_bf16_f32 v135, v78, v79\nv_mfma_f32_16x16x32_bf16 v[92:95], v[52:55], a[172:175], v[92:95]\nds_read_b128 v[52:55], v29 offset:16640\n"
"s_lshl_b32 s54, s40, 11\ns_add_u32 s54, s54, 0\ns_waitcnt lgkmcnt(3)\nv_mfma_f32_16x16x32_bf16 v[80:83], v[40:43], a[144:147], v[80:83]\n"
"v_mov_b32 v56, v128\nv_mov_b32 v60, v132\nv_mfma_f32_16x16x32_bf16 v[84:87], v[40:43], a[176:179], v[84:87]\nds_read_b128 v[40:43], v26 offset:16640\n"
"v_mov_b32 v57, v129\nv_mov_b32 v61, v133\ns_waitcnt lgkmcnt(3)\nv_mfma_f32_16x16x32_bf16 v[88:91], v[44:47], a[144:147], v[88:91]\n"
"v_mov_b32 v58, v130\nv_mov_b32 v62, v134\nv_mfma_f32_16x16x32_bf16 v[92:95], v[44:47], a[176:179], v[92:95]\nds_read_b128 v[44:47], v30 offset:16640\n"
"v_mov_b32 v59, v131\nv_mov_b32 v63, v135\ns_waitcnt lgkmcnt(3)\nv_mfma_f32_16x16x32_bf16 v[80:83], v[48:51], a[148:151], v[80:83]\n"
"v_mov_b32_dpp v56, v132 row_ror:8 row_mask:0xf bank_mask:0xc\nv_mov_b32_dpp v60, v128 row_ror:8 row_mask:0xf bank_mask:0x3\nv_mfma_f32_16x16x32_bf16 v[84:87], v[48:51], a[180:183], v[84:87]\nds_read_b128 v[48:51], v27 offset:16640\n"
"v_mov_b32_dpp v57, v133 row_ror:8 row_mask:0xf bank_mask:0xc\nv_mov_b32_dpp v61, v129 row_ror:8 row_mask:0xf bank_mask:0x3\ns_waitcnt lgkmcnt(3)\nv_mfma_f32_16x16x32_bf16 v[88:91], v[52:55], a[148:151], v[88:91]\n"
"v_mov_b32_dpp v58, v134 row_ror:8 row_mask:0xf bank_mask:0xc\nv_mov_b32_dpp v62, v130 row_ror:8 row_mask:0xf bank_mask:0x3\nv_mfma_f32_16x16x32_bf16 v[92:95], v[52:55], a[180:183], v[92:95]\nds_read_b128 v[52:55], v31 offset:16640\n"
"v_mov_b32_dpp v59, v135 row_ror:8 row_mask:0xf bank_mask:0xc\nv_mov_b32_dpp v63, v131 row_ror:8 row_mask:0xf bank_mask:0x3\ns_waitcnt lgkmcnt(3)\nv_mfma_f32_16x16x32_bf16 v[80:83], v[40:43], a[152:155], v[80:83]\n"
"v_mfma_f32_16x16x32_bf16 v[84:87], v[40:43], a[184:187], v[84:87]\nbuffer_store_dwordx4 v[56:59], v13, s[16:19], s54 offen sc1\ns_waitcnt lgkmcnt(2)\nv_mfma_f32_16x16x32_bf16 v[88:91], v[44:47], a[152:155], v[88:91]\n"
"s_add_u32 s54, s54, 14336\nv_mfma_f32_16x16x32_bf16 v[92:95], v[44:47], a[184:187], v[92:95]\nbuffer_store_dwordx4 v[60:63], v13, s[16:19], s54 offen sc1\ns_waitcnt lgkmcnt(1)\n"
"v_mfma_f32_16x16x32_bf16 v[80:83], v[48:51], a[156:159], v[80:83]\ns_lshl_b32 s53, s43, 19\ns_add_u32 s53, s53, s30\nv_mfma_f32_16x16x32_bf16 v[84:87], v[48:51], a[188:191], v[84:87]\n"
"s_add_u32 m0, s31, 49152\ns_add_u32 s53, s53, 262144\ns_waitcnt lgkmcnt(0)\nv_mfma_f32_16x16x32_bf16 v[88:91], v[52:55], a[156:159], v[88:91]\n"
"buffer_load_dwordx4 v8, s[8:11], s53 offen lds\nv_mfma_f32_16x16x32_bf16 v[92:95], v[52:55], a[188:191], v[92:95]\nbuffer_load_dwordx4 v9, s[8:11], s53 offen offset:1024 lds\ns_lshl_b32 s53, s43, 19\n"
"s_add_u32 s53, s53, s30\ns_add_u32 m0, s31, 57344\ns_add_u32 s53, s53, 393216\nbuffer_load_dwordx4 v8, s[8:11], s53 offen lds\n"
"buffer_load_dwordx4 v9, s[8:11], s53 offen offset:1024 lds\nv_add_u32 v24, 32768, v24\nv_add_u32 v25, 32768, v25\nv_add_u32 v26, 32768, v26\n"
"v_add_u32 v27, 32768, v27\nv_add_u32 v28, 32768, v28\nv_add_u32 v29, 32768, v29\nv_add_u32 v30, 32768, v30\n"
"v_add_u32 v31, 32768, v31\ns_waitcnt vmcnt(25)\ns_barrier\nds_read_b128 v[40:43], v24 offset:0\n"
"ds_read_b128 v[44:47], v28 offset:0\nds_read_b128 v[48:51], v25 offset:0\nds_read_b128 v[52:55], v29 offset:0\ns_waitcnt lgkmcnt(3)\n"
"v_mfma_f32_16x16x32_bf16 v[64:67], v[40:43], a[128:131], 0\nv_mfma_f32_16x16x32_bf16 v[68:71], v[40:43], a[160:163], 0\nds_read_b128 v[40:43], v26 offset:0\ns_waitcnt lgkmcnt(3)\n"
"v_mfma_f32_16x16x32_bf16 v[72:75], v[44:47], a[128:131], 0\nv_mfma_f32_16x16x32_bf16 v[76:79], v[44:47], a[160:163], 0\nds_read_b128 v[44:47], v30 offset:0\nv_mul_f32 v80, s35, v80\n"
"v_mul_f32 v81, s35, v81\ns_waitcnt lgkmcnt(3)\nv_mfma_f32_16x16x32_bf16 v[64:67], v[48:51], a[132:135], v[64:67]\nv_mul_f32 v82, s35, v82\n"
"v_mul_f32 v83, s35, v83\nv_mfma_f32_16x16x32_bf16 v[68:71], v[48:51], a[164:167], v[68:71]\nds_read_b128 v[48:51], v27 offset:0\nv_cvt_pk_bf16_f32 v136, v80, v81\n"
"v_cvt_pk_bf16_f32 v137, v82, v83\ns_waitcnt lgkmcnt(3)\nv_mfma_f32_16x16x32_bf16 v[72:75], v[52:55], a[132:135], v[72:75]\nv_mul_f32 v88, s35, v88\n"
"v_mul_f32 v89, s35, v89\nv_mfma_f32_16x16x32_bf16 v[76:79], v[52:55], a[164:167], v[76:79]\nds_read_b128 v[52:55], v31 offset:0\nv_mul_f32 v90, s35, v90\n"
"v_mul_f32 v91, s35, v91\ns_waitcnt lgkmcnt(3)\nv_mfma_f32_16x16x32_bf16 v[64:67], v[40:43], a[136:139], v[64:67]\nv_cvt_pk_bf16_f32 v138, v88, v89\n"
"v_cvt_pk_bf16_f32 v139, v90, v91\nv_mfma_f32_16x16x32_bf16 v[68:71], v[40:43], a[168:171], v[68:71]\nds_read_b128 v[40:43], v24 offset:256\nv_mul_f32 v84, s35, v84\n"
"v_mul_f32 v85, s35, v85\ns_waitcnt lgkmcnt(3)\nv_mfma_f32_16x16x32_bf16 v[72:75], v[44:47], a[136:139], v[72:75]\nv_mul_f32 v86, s35, v86\n"
"v_mul_f32 v87, s35, v87\nv_mfma_f32_16x16x32_bf16 v[76:79], v[44:47], a[168:171], v[76:79]\nds_read_b128 v[44:47], v28 offset:256\nv_cvt_pk_bf16_f32 v140, v84, v85\n"
"v_cvt_pk_bf16_f32 v141, v86, v87\ns_waitcnt lgkmcnt(3)\nv_mfma_f32_16x16x32_bf16 v[64:67], v[48:51], a[140:143], v[64:67]\nv_mul_f32 v92, s35, v92\n"
"v_mul_f32 v93, s35, v93\nv_mfma_f32_16x16x32_bf16 v[68:71], v[48:51], a[172:175], v[68:71]\nds_read_b128 v[48:51], v25 offset:256\nv_mul_f32 v94, s35, v94\n"
"v_mul_f32 v95, s35, v95\ns_waitcnt lgkmcnt(3)\nv_mfma_f32_16x16x32_bf16 v[72:75], v[52:55], a[140:143], v[72:75]\nv_cvt_pk_bf16_f32 v142, v92, v93\n"
"v_cvt_pk_bf16_f32 v143, v94, v95\nv_mfma_f32_16x16x32_bf16 v[76:79], v[52:55], a[172:175], v[76:79]\nds_read_b128 v[52:55], v29 offset:256\ns_lshl_b32 s54, s40, 11\n"
"s_add_u32 s54, s54, 1024\ns_waitcnt lgkmcnt(3)\nv_mfma_f32_16x16x32_bf16 v[64:67], v[40:43], a[144:147], v[64:67]\nv_mov_b32 v56, v136\n"
"v_mov_b32 v60, v140\nv_mfma_f32_16x16x32_bf16 v[68:71], v[40:43], a[176:179], v[68:71]\nds_read_b128 v[40:43], v26 offset:256\nv_mov_b32 v57, v137\n"
"v_mov_b32 v61, v141\ns_waitcnt lgkmcnt(3)\nv_mfma_f32_16x16x32_bf16 v[72:75], v[44:47], a[144:147], v[72:75]\nv_mov_b32 v58, v138\n"
"v_mov_b32 v62, v142\nv_mfma_f32_16x16x32_bf16 v[76:79], v[44:47], a[176:179], v[76:79]\nds_read_b128 v[44:47], v30 offset:256\nv_mov_b32 v59, v139\n"
"v_mov_b32 v63, v143\ns_waitcnt lgkmcnt(3)\nv_mfma_f32_16x16x32_bf16 v[64:67], v[48:51], a[148:151], v[64:67]\nv_mov_b32_dpp v56, v140 row_ror:8 row_mask:0xf bank_mask:0xc\n"
"v_mov_b32_dpp v60, v136 row_ror:8 row_mask:0xf bank_mask:0x3\nv_mfma_f32_16x16x32_bf16 v[68:71], v[48:51], a[180:183], v[68:71]\nds_read_b128 v[48:51], v27 offset:256\nv_mov_b32_dpp v57, v141 row_ror:8 row_mask:0xf bank_mask:0xc\n"
"v_mov_b32_dpp v61, v137 row_ror:8 row_mask:0xf bank_mask:0x3\ns_waitcnt lgkmcnt(3)\nv_mfma_f32_16x16x32_bf16 v[72:75], v[52:55], a[148:151], v[72:75]\nv_mov_b32_dpp v58, v142 row_ror:8 row_mask:0xf bank_mask:0xc\n"
"v_mov_b32_dpp v62, v138 row_ror:8 row_mask:0xf bank_mask:0x3\nv_mfma_f32_16x16x32_bf16 v[76:79], v[52:55], a[180:183], v[76:79]\nds_read_b128 v[52:55], v31 offset:256\nv_mov_b32_dpp v59, v143 row_ror:8 row_mask:0xf bank_mask:0xc\n"
"v_mov_b32_dpp v63, v139 row_ror:8 row_mask:0xf bank_mask:0x3\ns_waitcnt lgkmcnt(3)\nv_mfma_f32_16x16x32_bf16 v[64:67], v[40:43], a[152:155], v[64:67]\nv_mfma_f32_16x16x32_bf16 v[68:71], v[40:43], a[184:187], v[68:71]\n"
"ds_read_b128 v[40:43], v24 offset:16384\nbuffer_store_dwordx4 v[56:59], v13, s[16:19], s54 offen sc1\ns_waitcnt lgkmcnt(3)\nv_mfma_f32_16x16x32_bf16 v[72:75], v[44:47], a[152:155], v[72:75]\n"
"s_add_u32 s54, s54, 14336\nv_mfma_f32_16x16x32_bf16 v[76:79], v[44:47], a[184:187], v[76:79]\nds_read_b128 v[44:47], v28 offset:16384\nbuffer_store_dwordx4 v[60:63], v13, s[16:19], s54 offen sc1\n"
"s_waitcnt lgkmcnt(3)\nv_mfma_f32_16x16x32_bf16 v[64:67], v[48:51], a[156:159], v[64:67]\ns_lshl_b32 s53, s44, 19\ns_add_u32 s53, s53, s30\n"
"v_mfma_f32_16x16x32_bf16 v[68:71], v[48:51], a[188:191], v[68:71]\nds_read_b128 v[48:51], v25 offset:16384\ns_add_u32 m0, s31, 65536\ns_add_u32 s53, s53, 0\n"
"s_waitcnt lgkmcnt(3)\nv_mfma_f32_16x16x32_bf16 v[72:75], v[52:55], a[156:159], v[72:75]\nbuffer_load_dwordx4 v8, s[8:11], s53 offen lds\nv_mfma_f32_16x16x32_bf16 v[76:79], v[52:55], a[188:191], v[76:79]\n"
"ds_read_b128 v[52:55], v29 offset:16384\nbuffer_load_dwordx4 v9, s[8:11], s53 offen offset:1024 lds\ns_lshl_b32 s53, s44, 19\ns_add_u32 s53, s53, s30\n"
"s_add_u32 m0, s31, 73728\ns_add_u32 s53, s53, 131072\nbuffer_load_dwordx4 v8, s[8:11], s53 offen lds\nbuffer_load_dwordx4 v9, s[8:11], s53 offen offset:1024 lds\n"
"s_waitcnt lgkmcnt(3)\nv_mfma_f32_16x16x32_bf16 v[80:83], v[40:43], a[128:131], 0\nv_mfma_f32_16x16x32_bf16 v[84:87], v[40:43], a[160:163], 0\nds_read_b128 v[40:43], v26 offset:16384\n"
"s_waitcnt lgkmcnt(3)\nv_mfma_f32_16x16x32_bf16 v[88:91], v[44:47], a[128:131], 0\nv_mfma_f32_16x16x32_bf16 v[92:95], v[44:47], a[160:163], 0\nds_read_b128 v[44:47], v30 offset:16384\n"
"v_mul_f32 v64, s35, v64\nv_mul_f32 v65, s35, v65\ns_waitcnt lgkmcnt(3)\nv_mfma_f32_16x16x32_bf16 v[80:83], v[48:51], a[132:135], v[80:83]\n"
"v_mul_f32 v66, s35, v66\nv_mul_f32 v67, s35, v67\nv_mfma_f32_16x16x32_bf16 v[84:87], v[48:51], a[164:167], v[84:87]\nds_read_b128 v[48:51], v27 offset:16384\n"
"v_cvt_pk_bf16_f32 v144, v64, v65\nv_cvt_pk_bf16_f32 v145, v66, v67\ns_waitcnt lgkmcnt(3)\nv_mfma_f32_16x16x32_bf16 v[88:91], v[52:55], a[132:135], v[88:91]\n"
"v_mul_f32 v72, s35, v72\nv_mul_f32 v73, s35, v73\nv_mfma_f32_16x16x32_bf16 v[92:95], v[52:55], a[164:167], v[92:95]\nds_read_b128 v[52:55], v31 offset:16384\n"
"v_mul_f32 v74, s35, v74\nv_mul_f32 v75, s35, v75\ns_waitcnt lgkmcnt(3)\nv_mfma_f32_16x16x32_bf16 v[80:83], v[40:43], a[136:139], v[80:83]\n"
"v_cvt_pk_bf16_f32 v146, v72, v73\nv_cvt_pk_bf16_f32 v147, v74, v75\nv_mfma_f32_16x16x32_bf16 v[84:87], v[40:43], a[168:171], v[84:87]\nds_read_b128 v[40:43], v24 offset:16640\n"
"v_mul_f32 v68, s35, v68\nv_mul_f32 v69, s35, v69\ns_waitcnt lgkmcnt(3)\nv_mfma_f32_16x16x32_bf16 v[88:91], v[44:47], a[136:139], v[88:91]\n"
"v_mul_f32 v70, s35, v70\nv_mul_f32 v71, s35, v71\nv_mfma_f32_16x16x32_bf16 v[92:95], v[44:47], a[168:171], v[92:95]\nds_read_b128 v[44:47], v28 offset:16640\n"
"v_cvt_pk_bf16_f32 v148, v68, v69\nv_cvt_pk_bf16_f32 v149, v70, v71\ns_waitcnt lgkmcnt(3)\nv_mfma_f32_16x16x32_bf16 v[80:83], v[48:51], a[140:143], v[80:83]\n"
"v_mul_f32 v76, s35, v76\nv_mul_f32 v77, s35, v77\nv_mfma_f32_16x16x32_bf16 v[84:87], v[48:51], a[172:175], v[84:87]\nds_read_b128 v[48:51], v25 offset:16640\n"
"v_mul_f32 v78, s35, v78\nv_mul_f32 v79, s35, v79\ns_waitcnt lgkmcnt(3)\nv_mfma_f32_16x16x32_bf16 v[88:91], v[52:55], a[140:143], v[88:91]\n"
"v_cvt_pk_bf16_f32 v150, v76, v77\nv_cvt_pk_bf16_f32 v151, v78, v79\nv_mfma_f32_16x16x32_bf16 v[92:95], v[52:55], a[172:175], v[92:95]\nds_read_b128 v[52:55], v29 offset:16640\n"
"s_lshl_b32 s54, s41, 11\ns_add_u32 s54, s54, 0\ns_waitcnt lgkmcnt(3)\nv_mfma_f32_16x16x32_bf16 v[80:83], v[40:43], a[144:147], v[80:83]\n"
"v_mov_b32 v56, v144\nv_mov_b32 v60, v148\nv_mfma_f32_16x16x32_bf16 v[84:87], v[40:43], a[176:179], v[84:87]\nds_read_b128 v[40:43], v26 offset:16640\n"
"v_mov_b32 v57, v145\nv_mov_b32 v61, v149\ns_waitcnt lgkmcnt(3)\nv_mfma_f32_16x16x32_bf16 v[88:91], v[44:47], a[144:147], v[88:91]\n"
"v_mov_b32 v58, v146\nv_mov_b32 v62, v150\nv_mfma_f32_16x16x32_bf16 v[92:95], v[44:47], a[176:179], v[92:95]\nds_read_b128 v[44:47], v30 offset:16640\n"
"v_mov_b32 v59, v147\nv_mov_b32 v63, v151\ns_waitcnt lgkmcnt(3)\nv_mfma_f32_16x16x32_bf16 v[80:83], v[48:51], a[148:151], v[80:83]\n"
"v_mov_b32_dpp v56, v148 row_ror:8 row_mask:0xf bank_mask:0xc\nv_mov_b32_dpp v60, v144 row_ror:8 row_mask:0xf bank_mask:0x3\nv_mfma_f32_16x16x32_bf16 v[84:87], v[48:51], a[180:183], v[84:87]\nds_read_b128 v[48:51], v27 offset:16640\n"
"v_mov_b32_dpp v57, v149 row_ror:8 row_mask:0xf bank_mask:0xc\nv_mov_b32_dpp v61, v145 row_ror:8 row_mask:0xf bank_mask:0x3\ns_waitcnt lgkmcnt(3)\nv_mfma_f32_16x16x32_bf16 v[88:91], v[52:55], a[148:151], v[88:91]\n"
"v_mov_b32_dpp v58, v150 row_ror:8 row_mask:0xf bank_mask:0xc\nv_mov_b32_dpp v62, v146 row_ror:8 row_mask:0xf bank_mask:0x3\nv_mfma_f32_16x16x32_bf16 v[92:95], v[52:55], a[180:183], v[92:95]\nds_read_b128 v[52:55], v31 offset:16640\n"
"v_mov_b32_dpp v59, v151 row_ror:8 row_mask:0xf bank_mask:0xc\nv_mov_b32_dpp v63, v147 row_ror:8 row_mask:0xf bank_mask:0x3\ns_waitcnt lgkmcnt(3)\nv_mfma_f32_16x16x32_bf16 v[80:83], v[40:43], a[152:155], v[80:83]\n"
"v_mfma_f32_16x16x32_bf16 v[84:87], v[40:43], a[184:187], v[84:87]\nbuffer_store_dwordx4 v[56:59], v13, s[16:19], s54 offen sc1\ns_waitcnt lgkmcnt(2)\nv_mfma_f32_16x16x32_bf16 v[88:91], v[44:47], a[152:155], v[88:91]\n"
"s_add_u32 s54, s54, 14336\nv_mfma_f32_16x16x32_bf16 v[92:95], v[44:47], a[184:187], v[92:95]\nbuffer_store_dwordx4 v[60:63], v13, s[16:19], s54 offen sc1\ns_waitcnt lgkmcnt(1)\n"
"v_mfma_f32_16x16x32_bf16 v[80:83], v[48:51], a[156:159], v[80:83]\ns_lshl_b32 s53, s44, 19\ns_add_u32 s53, s53, s30\nv_mfma_f32_16x16x32_bf16 v[84:87], v[48:51], a[188:191], v[84:87]\n"
"s_add_u32 m0, s31, 81920\ns_add_u32 s53, s53, 262144\ns_waitcnt lgkmcnt(0)\nv_mfma_f32_16x16x32_bf16 v[88:91], v[52:55], a[156:159], v[88:91]\n"
"buffer_load_dwordx4 v8, s[8:11], s53 offen lds\nv_mfma_f32_16x16x32_bf16 v[92:95], v[52:55], a[188:191], v[92:95]\nbuffer_load_dwordx4 v9, s[8:11], s53 offen offset:1024 lds\ns_lshl_b32 s53, s44, 19\n"
"s_add_u32 s53, s53, s30\ns_add_u32 m0, s31, 90112\ns_add_u32 s53, s53, 393216\nbuffer_load_dwordx4 v8, s[8:11], s53 offen lds\n"
"buffer_load_dwordx4 v9, s[8:11], s53 offen offset:1024 lds\nv_add_u32 v24, 32768, v24\nv_add_u32 v25, 32768, v25\nv_add_u32 v26, 32768, v26\n"
"v_add_u32 v27, 32768, v27\nv_add_u32 v28, 32768, v28\nv_add_u32 v29, 32768, v29\nv_add_u32 v30, 32768, v30\n"
"v_add_u32 v31, 32768, v31\ns_waitcnt vmcnt(22)\ns_barrier\nds_read_b128 v[40:43], v24 offset:0\n"
"ds_read_b128 v[44:47], v28 offset:0\nds_read_b128 v[48:51], v25 offset:0\nds_read_b128 v[52:55], v29 offset:0\ns_waitcnt lgkmcnt(3)\n"
"v_mfma_f32_16x16x32_bf16 v[64:67], v[40:43], a[128:131], 0\nv_mfma_f32_16x16x32_bf16 v[68:71], v[40:43], a[160:163], 0\nds_read_b128 v[40:43], v26 offset:0\ns_waitcnt lgkmcnt(3)\n"
"v_mfma_f32_16x16x32_bf16 v[72:75], v[44:47], a[128:131], 0\nv_mfma_f32_16x16x32_bf16 v[76:79], v[44:47], a[160:163], 0\nds_read_b128 v[44:47], v30 offset:0\nv_mul_f32 v80, s35, v80\n"
"v_mul_f32 v81, s35, v81\ns_waitcnt lgkmcnt(3)\nv_mfma_f32_16x16x32_bf16 v[64:67], v[48:51], a[132:135], v[64:67]\nv_mul_f32 v82, s35, v82\n"
"v_mul_f32 v83, s35, v83\nv_mfma_f32_16x16x32_bf16 v[68:71], v[48:51], a[164:167], v[68:71]\nds_read_b128 v[48:51], v27 offset:0\nv_cvt_pk_bf16_f32 v152, v80, v81\n"
"v_cvt_pk_bf16_f32 v153, v82, v83\ns_waitcnt lgkmcnt(3)\nv_mfma_f32_16x16x32_bf16 v[72:75], v[52:55], a[132:135], v[72:75]\nv_mul_f32 v88, s35, v88\n"
"v_mul_f32 v89, s35, v89\nv_mfma_f32_16x16x32_bf16 v[76:79], v[52:55], a[164:167], v[76:79]\nds_read_b128 v[52:55], v31 offset:0\nv_mul_f32 v90, s35, v90\n"
"v_mul_f32 v91, s35, v91\ns_waitcnt lgkmcnt(3)\nv_mfma_f32_16x16x32_bf16 v[64:67], v[40:43], a[136:139], v[64:67]\nv_cvt_pk_bf16_f32 v154, v88, v89\n"
"v_cvt_pk_bf16_f32 v155, v90, v91\nv_mfma_f32_16x16x32_bf16 v[68:71], v[40:43], a[168:171], v[68:71]\nds_read_b128 v[40:43], v24 offset:256\nv_mul_f32 v84, s35, v84\n"
"v_mul_f32 v85, s35, v85\ns_waitcnt lgkmcnt(3)\nv_mfma_f32_16x16x32_bf16 v[72:75], v[44:47], a[136:139], v[72:75]\nv_mul_f32 v86, s35, v86\n"
"v_mul_f32 v87, s35, v87\nv_mfma_f32_16x16x32_bf16 v[76:79], v[44:47], a[168:171], v[76:79]\nds_read_b128 v[44:47], v28 offset:256\nv_cvt_pk_bf16_f32 v156, v84, v85\n"
"v_cvt_pk_bf16_f32 v157, v86, v87\ns_waitcnt lgkmcnt(3)\nv_mfma_f32_16x16x32_bf16 v[64:67], v[48:51], a[140:143], v[64:67]\nv_mul_f32 v92, s35, v92\n"
"v_mul_f32 v93, s35, v93\nv_mfma_f32_16x16x32_bf16 v[68:71], v[48:51], a[172:175], v[68:71]\nds_read_b128 v[48:51], v25 offset:256\nv_mul_f32 v94, s35, v94\n"
"v_mul_f32 v95, s35, v95\ns_waitcnt lgkmcnt(3)\nv_mfma_f32_16x16x32_bf16 v[72:75], v[52:55], a[140:143], v[72:75]\nv_cvt_pk_bf16_f32 v158, v92, v93\n"
"v_cvt_pk_bf16_f32 v159, v94, v95\nv_mfma_f32_16x16x32_bf16 v[76:79], v[52:55], a[172:175], v[76:79]\nds_read_b128 v[52:55], v29 offset:256\ns_lshl_b32 s54, s41, 11\n"
"s_add_u32 s54, s54, 1024\ns_waitcnt lgkmcnt(3)\nv_mfma_f32_16x16x32_bf16 v[64:67], v[40:43], a[144:147], v[64:67]\nv_mov_b32 v56, v152\n"
"v_mov_b32 v60, v156\nv_mfma_f32_16x16x32_bf16 v[68:71], v[40:43], a[176:179], v[68:71]\nds_read_b128 v[40:43], v26 offset:256\nv_mov_b32 v57, v153\n"
"v_mov_b32 v61, v157\ns_waitcnt lgkmcnt(3)\nv_mfma_f32_16x16x32_bf16 v[72:75], v[44:47], a[144:147], v[72:75]\nv_mov_b32 v58, v154\n"
"v_mov_b32 v62, v158\nv_mfma_f32_16x16x32_bf16 v[76:79], v[44:47], a[176:179], v[76:79]\nds_read_b128 v[44:47], v30 offset:256\nv_mov_b32 v59, v155\n"
"v_mov_b32 v63, v159\ns_waitcnt lgkmcnt(3)\nv_mfma_f32_16x16x32_bf16 v[64:67], v[48:51], a[148:151], v[64:67]\nv_mov_b32_dpp v56, v156 row_ror:8 row_mask:0xf bank_mask:0xc\n"
"v_mov_b32_dpp v60, v152 row_ror:8 row_mask:0xf bank_mask:0x3\nv_mfma_f32_16x16x32_bf16 v[68:71], v[48:51], a[180:183], v[68:71]\nds_read_b128 v[48:51], v27 offset:256\nv_mov_b32_dpp v57, v157 row_ror:8 row_mask:0xf bank_mask:0xc\n"
"v_mov_b32_dpp v61, v153 row_ror:8 row_mask:0xf bank_mask:0x3\ns_waitcnt lgkmcnt(3)\nv_mfma_f32_16x16x32_bf16 v[72:75], v[52:55], a[148:151], v[72:75]\nv_mov_b32_dpp v58, v158 row_ror:8 row_mask:0xf bank_mask:0xc\n"
"v_mov_b32_dpp v62, v154 row_ror:8 row_mask:0xf bank_mask:0x3\nv_mfma_f32_16x16x32_bf16 v[76:79], v[52:55], a[180:183], v[76:79]\nds_read_b128 v[52:55], v31 offset:256\nv_mov_b32_dpp v59, v159 row_ror:8 row_mask:0xf bank_mask:0xc\n"
"v_mov_b32_dpp v63, v155 row_ror:8 row_mask:0xf bank_mask:0x3\ns_waitcnt lgkmcnt(3)\nv_mfma_f32_16x16x32_bf16 v[64:67], v[40:43], a[152:155], v[64:67]\nv_mfma_f32_16x16x32_bf16 v[68:71], v[40:43], a[184:187], v[68:71]\n"
"ds_read_b128 v[40:43], v24 offset:16384\nbuffer_store_dwordx4 v[56:59], v13, s[16:19], s54 offen sc1\ns_waitcnt lgkmcnt(3)\nv_mfma_f32_16x16x32_bf16 v[72:75], v[44:47], a[152:155], v[72:75]\n"
"s_add_u32 s54, s54, 14336\nv_mfma_f32_16x16x32_bf16 v[76:79], v[44:47], a[184:187], v[76:79]\nds_read_b128 v[44:47], v28 offset:16384\nbuffer_store_dwordx4 v[60:63], v13, s[16:19], s54 offen sc1\n"
"s_waitcnt lgkmcnt(3)\nv_mfma_f32_16x16x32_bf16 v[64:67], v[48:51], a[156:159], v[64:67]\ns_lshl_b32 s53, s45, 19\ns_add_u32 s53, s53, s30\n"
"v_mfma_f32_16x16x32_bf16 v[68:71], v[48:51], a[188:191], v[68:71]\nds_read_b128 v[48:51], v25 offset:16384\ns_add_u32 m0, s31, 98304\ns_add_u32 s53, s53, 0\n"
"s_waitcnt lgkmcnt(3)\nv_mfma_f32_16x16x32_bf16 v[72:75], v[52:55], a[156:159], v[72:75]\nbuffer_load_dwordx4 v8, s[8:11], s53 offen lds\nv_mfma_f32_16x16x32_bf16 v[76:79], v[52:55], a[188:191], v[76:79]\n"
"ds_read_b128 v[52:55], v29 offset:16384\nbuffer_load_dwordx4 v9, s[8:11], s53 offen offset:1024 lds\ns_lshl_b32 s53, s45, 19\ns_add_u32 s53, s53, s30\n"
"s_add_u32 m0, s31, 106496\ns_add_u32 s53, s53, 131072\nbuffer_load_dwordx4 v8, s[8:11], s53 offen lds\nbuffer_load_dwordx4 v9, s[8:11], s53 offen offset:1024 lds\n"
"s_waitcnt lgkmcnt(3)\nv_mfma_f32_16x16x32_bf16 v[80:83], v[40:43], a[128:131], 0\nv_mfma_f32_16x16x32_bf16 v[84:87], v[40:43], a[160:163], 0\nds_read_b128 v[40:43], v26 offset:16384\n"
"s_waitcnt lgkmcnt(3)\nv_mfma_f32_16x16x32_bf16 v[88:91], v[44:47], a[128:131], 0\nv_mfma_f32_16x16x32_bf16 v[92:95], v[44:47], a[160:163], 0\nds_read_b128 v[44:47], v30 offset:16384\n"
"v_mul_f32 v64, s35, v64\nv_mul_f32 v65, s35, v65\ns_waitcnt lgkmcnt(3)\nv_mfma_f32_16x16x32_bf16 v[80:83], v[48:51], a[132:135], v[80:83]\n"
"v_mul_f32 v66, s35, v66\nv_mul_f32 v67, s35, v67\nv_mfma_f32_16x16x32_bf16 v[84:87], v[48:51], a[164:167], v[84:87]\nds_read_b128 v[48:51], v27 offset:16384\n"
"v_cvt_pk_bf16_f32 v160, v64, v65\nv_cvt_pk_bf16_f32 v161, v66, v67\ns_waitcnt lgkmcnt(3)\nv_mfma_f32_16x16x32_bf16 v[88:91], v[52:55], a[132:135], v[88:91]\n"
"v_mul_f32 v72, s35, v72\nv_mul_f32 v73, s35, v73\nv_mfma_f32_16x16x32_bf16 v[92:95], v[52:55], a[164:167], v[92:95]\nds_read_b128 v[52:55], v31 offset:16384\n"
"v_mul_f32 v74, s35, v74\nv_mul_f32 v75, s35, v75\ns_waitcnt lgkmcnt(3)\nv_mfma_f32_16x16x32_bf16 v[80:83], v[40:43], a[136:139], v[80:83]\n"
"v_cvt_pk_bf16_f32 v162, v72, v73\nv_cvt_pk_bf16_f32 v163, v74, v75\nv_mfma_f32_16x16x32_bf16 v[84:87], v[40:43], a[168:171], v[84:87]\nds_read_b128 v[40:43], v24 offset:16640\n"
"v_mul_f32 v68, s35, v68\nv_mul_f32 v69, s35, v69\ns_waitcnt lgkmcnt(3)\nv_mfma_f32_16x16x32_bf16 v[88:91], v[44:47], a[136:139], v[88:91]\n"
"v_mul_f32 v70, s35, v70\nv_mul_f32 v71, s35, v71\nv_mfma_f32_16x16x32_bf16 v[92:95], v[44:47], a[168:171], v[92:95]\nds_read_b128 v[44:47], v28 offset:16640\n"
"v_cvt_pk_bf16_f32 v164, v68, v69\nv_cvt_pk_bf16_f32 v165, v70, v71\ns_waitcnt lgkmcnt(3)\nv_mfma_f32_16x16x32_bf16 v[80:83], v[48:51], a[140:143], v[80:83]\n"
"v_mul_f32 v76, s35, v76\nv_mul_f32 v77, s35, v77\nv_mfma_f32_16x16x32_bf16 v[84:87], v[48:51], a[172:175], v[84:87]\nds_read_b128 v[48:51], v25 offset:16640\n"
"v_mul_f32 v78, s35, v78\nv_mul_f32 v79, s35, v79\ns_waitcnt lgkmcnt(3)\nv_mfma_f32_16x16x32_bf16 v[88:91], v[52:55], a[140:143], v[88:91]\n"
"v_cvt_pk_bf16_f32 v166, v76, v77\nv_cvt_pk_bf16_f32 v167, v78, v79\nv_mfma_f32_16x16x32_bf16 v[92:95], v[52:55], a[172:175], v[92:95]\nds_read_b128 v[52:55], v29 offset:16640\n"
"s_lshl_b32 s54, s42, 11\ns_add_u32 s54, s54, 0\ns_waitcnt lgkmcnt(3)\nv_mfma_f32_16x16x32_bf16 v[80:83], v[40:43], a[144:147], v[80:83]\n"
"v_mov_b32 v56, v160\nv_mov_b32 v60, v164\nv_mfma_f32_16x16x32_bf16 v[84:87], v[40:43], a[176:179], v[84:87]\nds_read_b128 v[40:43], v26 offset:16640\n"
"v_mov_b32 v57, v161\nv_mov_b32 v61, v165\ns_waitcnt lgkmcnt(3)\nv_mfma_f32_16x16x32_bf16 v[88:91], v[44:47], a[144:147], v[88:91]\n"
"v_mov_b32 v58, v162\nv_mov_b32 v62, v166\nv_mfma_f32_16x16x32_bf16 v[92:95], v[44:47], a[176:179], v[92:95]\nds_read_b128 v[44:47], v30 offset:16640\n"
"v_mov_b32 v59, v163\nv_mov_b32 v63, v167\ns_waitcnt lgkmcnt(3)\nv_mfma_f32_16x16x32_bf16 v[80:83], v[48:51], a[148:151], v[80:83]\n"
"v_mov_b32_dpp v56, v164 row_ror:8 row_mask:0xf bank_mask:0xc\nv_mov_b32_dpp v60, v160 row_ror:8 row_mask:0xf bank_mask:0x3\nv_mfma_f32_16x16x32_bf16 v[84:87], v[48:51], a[180:183], v[84:87]\nds_read_b128 v[48:51], v27 offset:16640\n"
"v_mov_b32_dpp v57, v165 row_ror:8 row_mask:0xf bank_mask:0xc\nv_mov_b32_dpp v61, v161 row_ror:8 row_mask:0xf bank_mask:0x3\ns_waitcnt lgkmcnt(3)\nv_mfma_f32_16x16x32_bf16 v[88:91], v[52:55], a[148:151], v[88:91]\n"
"v_mov_b32_dpp v58, v166 row_ror:8 row_mask:0xf bank_mask:0xc\nv_mov_b32_dpp v62, v162 row_ror:8 row_mask:0xf bank_mask:0x3\nv_mfma_f32_16x16x32_bf16 v[92:95], v[52:55], a[180:183], v[92:95]\nds_read_b128 v[52:55], v31 offset:16640\n"
"v_mov_b32_dpp v59, v167 row_ror:8 row_mask:0xf bank_mask:0xc\nv_mov_b32_dpp v63, v163 row_ror:8 row_mask:0xf bank_mask:0x3\ns_waitcnt lgkmcnt(3)\nv_mfma_f32_16x16x32_bf16 v[80:83], v[40:43], a[152:155], v[80:83]\n"
"v_mfma_f32_16x16x32_bf16 v[84:87], v[40:43], a[184:187], v[84:87]\nbuffer_store_dwordx4 v[56:59], v13, s[16:19], s54 offen sc1\ns_waitcnt lgkmcnt(2)\nv_mfma_f32_16x16x32_bf16 v[88:91], v[44:47], a[152:155], v[88:91]\n"
"s_add_u32 s54, s54, 14336\nv_mfma_f32_16x16x32_bf16 v[92:95], v[44:47], a[184:187], v[92:95]\nbuffer_store_dwordx4 v[60:63], v13, s[16:19], s54 offen sc1\ns_waitcnt lgkmcnt(1)\n"
"v_mfma_f32_16x16x32_bf16 v[80:83], v[48:51], a[156:159], v[80:83]\ns_lshl_b32 s53, s45, 19\ns_add_u32 s53, s53, s30\nv_mfma_f32_16x16x32_bf16 v[84:87], v[48:51], a[188:191], v[84:87]\n"
"s_add_u32 m0, s31, 114688\ns_add_u32 s53, s53, 262144\ns_waitcnt lgkmcnt(0)\nv_mfma_f32_16x16x32_bf16 v[88:91], v[52:55], a[156:159], v[88:91]\n"
"buffer_load_dwordx4 v8, s[8:11], s53 offen lds\nv_mfma_f32_16x16x32_bf16 v[92:95], v[52:55], a[188:191], v[92:95]\nbuffer_load_dwordx4 v9, s[8:11], s53 offen offset:1024 lds\ns_lshl_b32 s53, s45, 19\n"
"s_add_u32 s53, s53, s30\ns_add_u32 m0, s31, 122880\ns_add_u32 s53, s53, 393216\nbuffer_load_dwordx4 v8, s[8:11], s53 offen lds\n"
"buffer_load_dwordx4 v9, s[8:11], s53 offen offset:1024 lds\nv_add_u32 v24, -98304, v24\nv_add_u32 v25, -98304, v25\nv_add_u32 v26, -98304, v26\n"
"v_add_u32 v27, -98304, v27\nv_add_u32 v28, -98304, v28\nv_add_u32 v29, -98304, v29\nv_add_u32 v30, -98304, v30\n"
"v_add_u32 v31, -98304, v31\ns_waitcnt vmcnt(24)\ns_barrier\nds_read_b128 v[40:43], v24 offset:0\n"
"ds_read_b128 v[44:47], v28 offset:0\nds_read_b128 v[48:51], v25 offset:0\nds_read_b128 v[52:55], v29 offset:0\ns_waitcnt lgkmcnt(3)\n"
"v_mfma_f32_16x16x32_bf16 v[64:67], v[40:43], a[128:131], 0\nv_mfma_f32_16x16x32_bf16 v[68:71], v[40:43], a[160:163], 0\nds_read_b128 v[40:43], v26 offset:0\ns_waitcnt lgkmcnt(3)\n"
"v_mfma_f32_16x16x32_bf16 v[72:75], v[44:47], a[128:131], 0\nv_mfma_f32_16x16x32_bf16 v[76:79], v[44:47], a[160:163], 0\nds_read_b128 v[44:47], v30 offset:0\nv_mul_f32 v80, s35, v80\n"
"v_mul_f32 v81, s35, v81\ns_waitcnt lgkmcnt(3)\nv_mfma_f32_16x16x32_bf16 v[64:67], v[48:51], a[132:135], v[64:67]\nv_mul_f32 v82, s35, v82\n"
"v_mul_f32 v83, s35, v83\nv_mfma_f32_16x16x32_bf16 v[68:71], v[48:51], a[164:167], v[68:71]\nds_read_b128 v[48:51], v27 offset:0\nv_cvt_pk_bf16_f32 v168, v80, v81\n"
"v_cvt_pk_bf16_f32 v169, v82, v83\ns_waitcnt lgkmcnt(3)\nv_mfma_f32_16x16x32_bf16 v[72:75], v[52:55], a[132:135], v[72:75]\nv_mul_f32 v88, s35, v88\n"
"v_mul_f32 v89, s35, v89\nv_mfma_f32_16x16x32_bf16 v[76:79], v[52:55], a[164:167], v[76:79]\nds_read_b128 v[52:55], v31 offset:0\nv_mul_f32 v90, s35, v90\n"
"v_mul_f32 v91, s35, v91\ns_waitcnt lgkmcnt(3)\nv_mfma_f32_16x16x32_bf16 v[64:67], v[40:43], a[136:139], v[64:67]\nv_cvt_pk_bf16_f32 v170, v88, v89\n"
"v_cvt_pk_bf16_f32 v171, v90, v91\nv_mfma_f32_16x16x32_bf16 v[68:71], v[40:43], a[168:171], v[68:71]\nds_read_b128 v[40:43], v24 offset:256\nv_mul_f32 v84, s35, v84\n"
"v_mul_f32 v85, s35, v85\ns_waitcnt lgkmcnt(3)\nv_mfma_f32_16x16x32_bf16 v[72:75], v[44:47], a[136:139], v[72:75]\nv_mul_f32 v86, s35, v86\n"
"v_mul_f32 v87, s35, v87\nv_mfma_f32_16x16x32_bf16 v[76:79], v[44:47], a[168:171], v[76:79]\nds_read_b128 v[44:47], v28 offset:256\nv_cvt_pk_bf16_f32 v172, v84, v85\n"
"v_cvt_pk_bf16_f32 v173, v86, v87\ns_waitcnt lgkmcnt(3)\nv_mfma_f32_16x16x32_bf16 v[64:67], v[48:51], a[140:143], v[64:67]\nv_mul_f32 v92, s35, v92\n"
"v_mul_f32 v93, s35, v93\nv_mfma_f32_16x16x32_bf16 v[68:71], v[48:51], a[172:175], v[68:71]\nds_read_b128 v[48:51], v25 offset:256\nv_mul_f32 v94, s35, v94\n"
"v_mul_f32 v95, s35, v95\ns_waitcnt lgkmcnt(3)\nv_mfma_f32_16x16x32_bf16 v[72:75], v[52:55], a[140:143], v[72:75]\nv_cvt_pk_bf16_f32 v174, v92, v93\n"
"v_cvt_pk_bf16_f32 v175, v94, v95\nv_mfma_f32_16x16x32_bf16 v[76:79], v[52:55], a[172:175], v[76:79]\nds_read_b128 v[52:55], v29 offset:256\ns_lshl_b32 s54, s42, 11\n"
"s_add_u32 s54, s54, 1024\ns_waitcnt lgkmcnt(3)\nv_mfma_f32_16x16x32_bf16 v[64:67], v[40:43], a[144:147], v[64:67]\nv_mov_b32 v56, v168\n"
"v_mov_b32 v60, v172\nv_mfma_f32_16x16x32_bf16 v[68:71], v[40:43], a[176:179], v[68:71]\nds_read_b128 v[40:43], v26 offset:256\nv_mov_b32 v57, v169\n"
"v_mov_b32 v61, v173\ns_waitcnt lgkmcnt(3)\nv_mfma_f32_16x16x32_bf16 v[72:75], v[44:47], a[144:147], v[72:75]\nv_mov_b32 v58, v170\n"
"v_mov_b32 v62, v174\nv_mfma_f32_16x16x32_bf16 v[76:79], v[44:47], a[176:179], v[76:79]\nds_read_b128 v[44:47], v30 offset:256\nv_mov_b32 v59, v171\n"
"v_mov_b32 v63, v175\ns_waitcnt lgkmcnt(3)\nv_mfma_f32_16x16x32_bf16 v[64:67], v[48:51], a[148:151], v[64:67]\nv_mov_b32_dpp v56, v172 row_ror:8 row_mask:0xf bank_mask:0xc\n"
"v_mov_b32_dpp v60, v168 row_ror:8 row_mask:0xf bank_mask:0x3\nv_mfma_f32_16x16x32_bf16 v[68:71], v[48:51], a[180:183], v[68:71]\nds_read_b128 v[48:51], v27 offset:256\nv_mov_b32_dpp v57, v173 row_ror:8 row_mask:0xf bank_mask:0xc\n"
"v_mov_b32_dpp v61, v169 row_ror:8 row_mask:0xf bank_mask:0x3\ns_waitcnt lgkmcnt(3)\nv_mfma_f32_16x16x32_bf16 v[72:75], v[52:55], a[148:151], v[72:75]\nv_mov_b32_dpp v58, v174 row_ror:8 row_mask:0xf bank_mask:0xc\n"
"v_mov_b32_dpp v62, v170 row_ror:8 row_mask:0xf bank_mask:0x3\nv_mfma_f32_16x16x32_bf16 v[76:79], v[52:55], a[180:183], v[76:79]\nds_read_b128 v[52:55], v31 offset:256\nv_mov_b32_dpp v59, v175 row_ror:8 row_mask:0xf bank_mask:0xc\n"
"v_mov_b32_dpp v63, v171 row_ror:8 row_mask:0xf bank_mask:0x3\ns_waitcnt lgkmcnt(3)\nv_mfma_f32_16x16x32_bf16 v[64:67], v[40:43], a[152:155], v[64:67]\nv_mfma_f32_16x16x32_bf16 v[68:71], v[40:43], a[184:187], v[68:71]\n"
"ds_read_b128 v[40:43], v24 offset:16384\nbuffer_store_dwordx4 v[56:59], v13, s[16:19], s54 offen sc1\ns_waitcnt lgkmcnt(3)\nv_mfma_f32_16x16x32_bf16 v[72:75], v[44:47], a[152:155], v[72:75]\n"
"s_add_u32 s54, s54, 14336\nv_mfma_f32_16x16x32_bf16 v[76:79], v[44:47], a[184:187], v[76:79]\nds_read_b128 v[44:47], v28 offset:16384\nbuffer_store_dwordx4 v[60:63], v13, s[16:19], s54 offen sc1\n"
"s_waitcnt lgkmcnt(3)\nv_mfma_f32_16x16x32_bf16 v[64:67], v[48:51], a[156:159], v[64:67]\ns_lshl_b32 s53, s46, 19\ns_add_u32 s53, s53, s30\n"
"v_mfma_f32_16x16x32_bf16 v[68:71], v[48:51], a[188:191], v[68:71]\nds_read_b128 v[48:51], v25 offset:16384\ns_add_u32 m0, s31, 131072\ns_add_u32 s53, s53, 0\n"
"s_waitcnt lgkmcnt(3)\nv_mfma_f32_16x16x32_bf16 v[72:75], v[52:55], a[156:159], v[72:75]\nbuffer_load_dwordx4 v8, s[8:11], s53 offen lds\nv_mfma_f32_16x16x32_bf16 v[76:79], v[52:55], a[188:191], v[76:79]\n"
"ds_read_b128 v[52:55], v29 offset:16384\nbuffer_load_dwordx4 v9, s[8:11], s53 offen offset:1024 lds\ns_lshl_b32 s53, s46, 19\ns_add_u32 s53, s53, s30\n"
"s_add_u32 m0, s31, 139264\ns_add_u32 s53, s53, 131072\nbuffer_load_dwordx4 v8, s[8:11], s53 offen lds\nbuffer_load_dwordx4 v9, s[8:11], s53 offen offset:1024 lds\n"
"s_waitcnt lgkmcnt(3)\nv_mfma_f32_16x16x32_bf16 v[80:83], v[40:43], a[128:131], 0\nv_mfma_f32_16x16x32_bf16 v[84:87], v[40:43], a[160:163], 0\nds_read_b128 v[40:43], v26 offset:16384\n"
"s_waitcnt lgkmcnt(3)\nv_mfma_f32_16x16x32_bf16 v[88:91], v[44:47], a[128:131], 0\nv_mfma_f32_16x16x32_bf16 v[92:95], v[44:47], a[160:163], 0\nds_read_b128 v[44:47], v30 offset:16384\n"
"v_mul_f32 v64, s35, v64\nv_mul_f32 v65, s35, v65\ns_waitcnt lgkmcnt(3)\nv_mfma_f32_16x16x32_bf16 v[80:83], v[48:51], a[132:135], v[80:83]\n"
"v_mul_f32 v66, s35, v66\nv_mul_f32 v67, s35, v67\nv_mfma_f32_16x16x32_bf16 v[84:87], v[48:51], a[164:167], v[84:87]\nds_read_b128 v[48:51], v27 offset:16384\n"
"v_cvt_pk_bf16_f32 v176, v64, v65\nv_cvt_pk_bf16_f32 v177, v66, v67\ns_waitcnt lgkmcnt(3)\nv_mfma_f32_16x16x32_bf16 v[88:91], v[52:55], a[132:135], v[88:91]\n"
"v_mul_f32 v72, s35, v72\nv_mul_f32 v73, s35, v73\nv_mfma_f32_16x16x32_bf16 v[92:95], v[52:55], a[164:167], v[92:95]\nds_read_b128 v[52:55], v31 offset:16384\n"
"v_mul_f32 v74, s35, v74\nv_mul_f32 v75, s35, v75\ns_waitcnt lgkmcnt(3)\nv_mfma_f32_16x16x32_bf16 v[80:83], v[40:43], a[136:139], v[80:83]\n"
"v_cvt_pk_bf16_f32 v178, v72, v73\nv_cvt_pk_bf16_f32 v179, v74, v75\nv_mfma_f32_16x16x32_bf16 v[84:87], v[40:43], a[168:171], v[84:87]\nds_read_b128 v[40:43], v24 offset:16640\n"
"v_mul_f32 v68, s35, v68\nv_mul_f32 v69, s35, v69\ns_waitcnt lgkmcnt(3)\nv_mfma_f32_16x16x32_bf16 v[88:91], v[44:47], a[136:139], v[88:91]\n"
"v_mul_f32 v70, s35, v70\nv_mul_f32 v71, s35, v71\nv_mfma_f32_16x16x32_bf16 v[92:95], v[44:47], a[168:171], v[92:95]\nds_read_b128 v[44:47], v28 offset:16640\n"
"v_cvt_pk_bf16_f32 v180, v68, v69\nv_cvt_pk_bf16_f32 v181, v70, v71\ns_waitcnt lgkmcnt(3)\nv_mfma_f32_16x16x32_bf16 v[80:83], v[48:51], a[140:143], v[80:83]\n"
"v_mul_f32 v76, s35, v76\nv_mul_f32 v77, s35, v77\nv_mfma_f32_16x16x32_bf16 v[84:87], v[48:51], a[172:175], v[84:87]\nds_read_b128 v[48:51], v25 offset:16640\n"
"v_mul_f32 v78, s35, v78\nv_mul_f32 v79, s35, v79\ns_waitcnt lgkmcnt(3)\nv_mfma_f32_16x16x32_bf16 v[88:91], v[52:55], a[140:143], v[88:91]\n"
"v_cvt_pk_bf16_f32 v182, v76, v77\nv_cvt_pk_bf16_f32 v183, v78, v79\nv_mfma_f32_16x16x32_bf16 v[92:95], v[52:55], a[172:175], v[92:95]\nds_read_b128 v[52:55], v29 offset:16640\n"
"s_lshl_b32 s54, s43, 11\ns_add_u32 s54, s54, 0\ns_waitcnt lgkmcnt(3)\nv_mfma_f32_16x16x32_bf16 v[80:83], v[40:43], a[144:147], v[80:83]\n"
"v_mov_b32 v56, v176\nv_mov_b32 v60, v180\nv_mfma_f32_16x16x32_bf16 v[84:87], v[40:43], a[176:179], v[84:87]\nds_read_b128 v[40:43], v26 offset:16640\n"
"v_mov_b32 v57, v177\nv_mov_b32 v61, v181\ns_waitcnt lgkmcnt(3)\nv_mfma_f32_16x16x32_bf16 v[88:91], v[44:47], a[144:147], v[88:91]\n"
"v_mov_b32 v58, v178\nv_mov_b32 v62, v182\nv_mfma_f32_16x16x32_bf16 v[92:95], v[44:47], a[176:179], v[92:95]\nds_read_b128 v[44:47], v30 offset:16640\n"
"v_mov_b32 v59, v179\nv_mov_b32 v63, v183\ns_waitcnt lgkmcnt(3)\nv_mfma_f32_16x16x32_bf16 v[80:83], v[48:51], a[148:151], v[80:83]\n"
"v_mov_b32_dpp v56, v180 row_ror:8 row_mask:0xf bank_mask:0xc\nv_mov_b32_dpp v60, v176 row_ror:8 row_mask:0xf bank_mask:0x3\nv_mfma_f32_16x16x32_bf16 v[84:87], v[48:51], a[180:183], v[84:87]\nds_read_b128 v[48:51], v27 offset:16640\n"
"v_mov_b32_dpp v57, v181 row_ror:8 row_mask:0xf bank_mask:0xc\nv_mov_b32_dpp v61, v177 row_ror:8 row_mask:0xf bank_mask:0x3\ns_waitcnt lgkmcnt(3)\nv_mfma_f32_16x16x32_bf16 v[88:91], v[52:55], a[148:151], v[88:91]\n"
"v_mov_b32_dpp v58, v182 row_ror:8 row_mask:0xf bank_mask:0xc\nv_mov_b32_dpp v62, v178 row_ror:8 row_mask:0xf bank_mask:0x3\nv_mfma_f32_16x16x32_bf16 v[92:95], v[52:55], a[180:183], v[92:95]\nds_read_b128 v[52:55], v31 offset:16640\n"
"v_mov_b32_dpp v59, v183 row_ror:8 row_mask:0xf bank_mask:0xc\nv_mov_b32_dpp v63, v179 row_ror:8 row_mask:0xf bank_mask:0x3\ns_waitcnt lgkmcnt(3)\nv_mfma_f32_16x16x32_bf16 v[80:83], v[40:43], a[152:155], v[80:83]\n"
"v_mfma_f32_16x16x32_bf16 v[84:87], v[40:43], a[184:187], v[84:87]\nbuffer_store_dwordx4 v[56:59], v13, s[16:19], s54 offen sc1\ns_waitcnt lgkmcnt(2)\nv_mfma_f32_16x16x32_bf16 v[88:91], v[44:47], a[152:155], v[88:91]\n"
"s_add_u32 s54, s54, 14336\nv_mfma_f32_16x16x32_bf16 v[92:95], v[44:47], a[184:187], v[92:95]\nbuffer_store_dwordx4 v[60:63], v13, s[16:19], s54 offen sc1\ns_waitcnt lgkmcnt(1)\n"
"v_mfma_f32_16x16x32_bf16 v[80:83], v[48:51], a[156:159], v[80:83]\ns_lshl_b32 s53, s46, 19\ns_add_u32 s53, s53, s30\nv_mfma_f32_16x16x32_bf16 v[84:87], v[48:51], a[188:191], v[84:87]\n"
"s_add_u32 m0, s31, 147456\ns_add_u32 s53, s53, 262144\ns_waitcnt lgkmcnt(0)\nv_mfma_f32_16x16x32_bf16 v[88:91], v[52:55], a[156:159], v[88:91]\n"
"buffer_load_dwordx4 v8, s[8:11], s53 offen lds\nv_mfma_f32_16x16x32_bf16 v[92:95], v[52:55], a[188:191], v[92:95]\nbuffer_load_dwordx4 v9, s[8:11], s53 offen offset:1024 lds\ns_lshl_b32 s53, s46, 19\n"
"s_add_u32 s53, s53, s30\ns_add_u32 m0, s31, 155648\ns_add_u32 s53, s53, 393216\nbuffer_load_dwordx4 v8, s[8:11], s53 offen lds\n"
"buffer_load_dwordx4 v9, s[8:11], s53 offen offset:1024 lds\nv_add_u32 v24, 32768, v24\nv_add_u32 v25, 32768, v25\nv_add_u32 v26, 32768, v26\n"
"v_add_u32 v27, 32768, v27\nv_add_u32 v28, 32768, v28\nv_add_u32 v29, 32768, v29\nv_add_u32 v30, 32768, v30\n"
"v_add_u32 v31, 32768, v31\ns_waitcnt vmcnt(24)\ns_barrier\nds_read_b128 v[40:43], v24 offset:0\n"
"ds_read_b128 v[44:47], v28 offset:0\nds_read_b128 v[48:51], v25 offset:0\nds_read_b128 v[52:55], v29 offset:0\ns_waitcnt lgkmcnt(3)\n"
"v_mfma_f32_16x16x32_bf16 v[64:67], v[40:43], a[128:131], 0\nv_mfma_f32_16x16x32_bf16 v[68:71], v[40:43], a[160:163], 0\nds_read_b128 v[40:43], v26 offset:0\ns_waitcnt lgkmcnt(3)\n"
"v_mfma_f32_16x16x32_bf16 v[72:75], v[44:47], a[128:131], 0\nv_mfma_f32_16x16x32_bf16 v[76:79], v[44:47], a[160:163], 0\nds_read_b128 v[44:47], v30 offset:0\nv_mul_f32 v80, s35, v80\n"
"v_mul_f32 v81, s35, v81\ns_waitcnt lgkmcnt(3)\nv_mfma_f32_16x16x32_bf16 v[64:67], v[48:51], a[132:135], v[64:67]\nv_mul_f32 v82, s35, v82\n"
"v_mul_f32 v83, s35, v83\nv_mfma_f32_16x16x32_bf16 v[68:71], v[48:51], a[164:167], v[68:71]\nds_read_b128 v[48:51], v27 offset:0\nv_cvt_pk_bf16_f32 v184, v80, v81\n"
"v_cvt_pk_bf16_f32 v185, v82, v83\ns_waitcnt lgkmcnt(3)\nv_mfma_f32_16x16x32_bf16 v[72:75], v[52:55], a[132:135], v[72:75]\nv_mul_f32 v88, s35, v88\n"
"v_mul_f32 v89, s35, v89\nv_mfma_f32_16x16x32_bf16 v[76:79], v[52:55], a[164:167], v[76:79]\nds_read_b128 v[52:55], v31 offset:0\nv_mul_f32 v90, s35, v90\n"
"v_mul_f32 v91, s35, v91\ns_waitcnt lgkmcnt(3)\nv_mfma_f32_16x16x32_bf16 v[64:67], v[40:43], a[136:139], v[64:67]\nv_cvt_pk_bf16_f32 v186, v88, v89\n"
"v_cvt_pk_bf16_f32 v187, v90, v91\nv_mfma_f32_16x16x32_bf16 v[68:71], v[40:43], a[168:171], v[68:71]\nds_read_b128 v[40:43], v24 offset:256\nv_mul_f32 v84, s35, v84\n"
"v_mul_f32 v85, s35, v85\ns_waitcnt lgkmcnt(3)\nv_mfma_f32_16x16x32_bf16 v[72:75], v[44:47], a[136:139], v[72:75]\nv_mul_f32 v86, s35, v86\n"
"v_mul_f32 v87, s35, v87\nv_mfma_f32_16x16x32_bf16 v[76:79], v[44:47], a[168:171], v[76:79]\nds_read_b128 v[44:47], v28 offset:256\nv_cvt_pk_bf16_f32 v188, v84, v85\n"
"v_cvt_pk_bf16_f32 v189, v86, v87\ns_waitcnt lgkmcnt(3)\nv_mfma_f32_16x16x32_bf16 v[64:67], v[48:51], a[140:143], v[64:67]\nv_mul_f32 v92, s35, v92\n"
"v_mul_f32 v93, s35, v93\nv_mfma_f32_16x16x32_bf16 v[68:71], v[48:51], a[172:175], v[68:71]\nds_read_b128 v[48:51], v25 offset:256\nv_mul_f32 v94, s35, v94\n"
"v_mul_f32 v95, s35, v95\ns_waitcnt lgkmcnt(3)\nv_mfma_f32_16x16x32_bf16 v[72:75], v[52:55], a[140:143], v[72:75]\nv_cvt_pk_bf16_f32 v190, v92, v93\n"
"v_cvt_pk_bf16_f32 v191, v94, v95\nv_mfma_f32_16x16x32_bf16 v[76:79], v[52:55], a[172:175], v[76:79]\nds_read_b128 v[52:55], v29 offset:256\ns_lshl_b32 s54, s43, 11\n"
"s_add_u32 s54, s54, 1024\ns_waitcnt lgkmcnt(3)\nv_mfma_f32_16x16x32_bf16 v[64:67], v[40:43], a[144:147], v[64:67]\nv_mov_b32 v56, v184\n"
"v_mov_b32 v60, v188\nv_mfma_f32_16x16x32_bf16 v[68:71], v[40:43], a[176:179], v[68:71]\nds_read_b128 v[40:43], v26 offset:256\nv_mov_b32 v57, v185\n"
"v_mov_b32 v61, v189\ns_waitcnt lgkmcnt(3)\nv_mfma_f32_16x16x32_bf16 v[72:75], v[44:47], a[144:147], v[72:75]\nv_mov_b32 v58, v186\n"
"v_mov_b32 v62, v190\nv_mfma_f32_16x16x32_bf16 v[76:79], v[44:47], a[176:179], v[76:79]\nds_read_b128 v[44:47], v30 offset:256\nv_mov_b32 v59, v187\n"
"v_mov_b32 v63, v191\ns_waitcnt lgkmcnt(3)\nv_mfma_f32_16x16x32_bf16 v[64:67], v[48:51], a[148:151], v[64:67]\nv_mov_b32_dpp v56, v188 row_ror:8 row_mask:0xf bank_mask:0xc\n"
"v_mov_b32_dpp v60, v184 row_ror:8 row_mask:0xf bank_mask:0x3\nv_mfma_f32_16x16x32_bf16 v[68:71], v[48:51], a[180:183], v[68:71]\nds_read_b128 v[48:51], v27 offset:256\nv_mov_b32_dpp v57, v189 row_ror:8 row_mask:0xf bank_mask:0xc\n"
"v_mov_b32_dpp v61, v185 row_ror:8 row_mask:0xf bank_mask:0x3\ns_waitcnt lgkmcnt(3)\nv_mfma_f32_16x16x32_bf16 v[72:75], v[52:55], a[148:151], v[72:75]\nv_mov_b32_dpp v58, v190 row_ror:8 row_mask:0xf bank_mask:0xc\n"
"v_mov_b32_dpp v62, v186 row_ror:8 row_mask:0xf bank_mask:0x3\nv_mfma_f32_16x16x32_bf16 v[76:79], v[52:55], a[180:183], v[76:79]\nds_read_b128 v[52:55], v31 offset:256\nv_mov_b32_dpp v59, v191 row_ror:8 row_mask:0xf bank_mask:0xc\n"
"v_mov_b32_dpp v63, v187 row_ror:8 row_mask:0xf bank_mask:0x3\ns_waitcnt lgkmcnt(3)\nv_mfma_f32_16x16x32_bf16 v[64:67], v[40:43], a[152:155], v[64:67]\nv_mfma_f32_16x16x32_bf16 v[68:71], v[40:43], a[184:187], v[68:71]\n"
"ds_read_b128 v[40:43], v24 offset:16384\nbuffer_store_dwordx4 v[56:59], v13, s[16:19], s54 offen sc1\ns_waitcnt lgkmcnt(3)\nv_mfma_f32_16x16x32_bf16 v[72:75], v[44:47], a[152:155], v[72:75]\n"
"s_add_u32 s54, s54, 14336\nv_mfma_f32_16x16x32_bf16 v[76:79], v[44:47], a[184:187], v[76:79]\nds_read_b128 v[44:47], v28 offset:16384\nbuffer_store_dwordx4 v[60:63], v13, s[16:19], s54 offen sc1\n"
"s_waitcnt lgkmcnt(3)\nv_mfma_f32_16x16x32_bf16 v[64:67], v[48:51], a[156:159], v[64:67]\ns_add_u32 m0, s32, 32768\nv_mfma_f32_16x16x32_bf16 v[68:71], v[48:51], a[188:191], v[68:71]\n"
"ds_read_b128 v[48:51], v25 offset:16384\ns_lshl_b32 s53, s40, 7\ns_add_u32 s53, s53, 28672\ns_waitcnt lgkmcnt(3)\n"
"v_mfma_f32_16x16x32_bf16 v[72:75], v[52:55], a[156:159], v[72:75]\nbuffer_load_dwordx4 v10, s[12:15], s53 offen lds\nv_mfma_f32_16x16x32_bf16 v[76:79], v[52:55], a[188:191], v[76:79]\nds_read_b128 v[52:55], v29 offset:16384\n"
"s_add_u32 m0, s32, 36864\ns_lshl_b32 s53, s41, 7\ns_add_u32 s53, s53, 28672\nbuffer_load_dwordx4 v10, s[12:15], s53 offen lds\n"
"s_add_u32 m0, s32, 40960\ns_lshl_b32 s53, s42, 7\ns_add_u32 s53, s53, 28672\nbuffer_load_dwordx4 v10, s[12:15], s53 offen lds\n"
"s_waitcnt lgkmcnt(3)\nv_mfma_f32_16x16x32_bf16 v[80:83], v[40:43], a[128:131], 0\nv_mfma_f32_16x16x32_bf16 v[84:87], v[40:43], a[160:163], 0\nds_read_b128 v[40:43], v26 offset:16384\n"
"s_waitcnt lgkmcnt(3)\nv_mfma_f32_16x16x32_bf16 v[88:91], v[44:47], a[128:131], 0\nv_mfma_f32_16x16x32_bf16 v[92:95], v[44:47], a[160:163], 0\nds_read_b128 v[44:47], v30 offset:16384\n"
"v_mul_f32 v64, s35, v64\nv_mul_f32 v65, s35, v65\ns_waitcnt lgkmcnt(3)\nv_mfma_f32_16x16x32_bf16 v[80:83], v[48:51], a[132:135], v[80:83]\n"
"v_mul_f32 v66, s35, v66\nv_mul_f32 v67, s35, v67\nv_mfma_f32_16x16x32_bf16 v[84:87], v[48:51], a[164:167], v[84:87]\nds_read_b128 v[48:51], v27 offset:16384\n"
"v_cvt_pk_bf16_f32 v192, v64, v65\nv_cvt_pk_bf16_f32 v193, v66, v67\ns_waitcnt lgkmcnt(3)\nv_mfma_f32_16x16x32_bf16 v[88:91], v[52:55], a[132:135], v[88:91]\n"
"v_mul_f32 v72, s35, v72\nv_mul_f32 v73, s35, v73\nv_mfma_f32_16x16x32_bf16 v[92:95], v[52:55], a[164:167], v[92:95]\nds_read_b128 v[52:55], v31 offset:16384\n"
"v_mul_f32 v74, s35, v74\nv_mul_f32 v75, s35, v75\ns_waitcnt lgkmcnt(3)\nv_mfma_f32_16x16x32_bf16 v[80:83], v[40:43], a[136:139], v[80:83]\n"
"v_cvt_pk_bf16_f32 v194, v72, v73\nv_cvt_pk_bf16_f32 v195, v74, v75\nv_mfma_f32_16x16x32_bf16 v[84:87], v[40:43], a[168:171], v[84:87]\nds_read_b128 v[40:43], v24 offset:16640\n"
"v_mul_f32 v68, s35, v68\nv_mul_f32 v69, s35, v69\ns_waitcnt lgkmcnt(3)\nv_mfma_f32_16x16x32_bf16 v[88:91], v[44:47], a[136:139], v[88:91]\n"
"v_mul_f32 v70, s35, v70\nv_mul_f32 v71, s35, v71\nv_mfma_f32_16x16x32_bf16 v[92:95], v[44:47], a[168:171], v[92:95]\nds_read_b128 v[44:47], v28 offset:16640\n"
"v_cvt_pk_bf16_f32 v196, v68, v69\nv_cvt_pk_bf16_f32 v197, v70, v71\ns_waitcnt lgkmcnt(3)\nv_mfma_f32_16x16x32_bf16 v[80:83], v[48:51], a[140:143], v[80:83]\n"
"v_mul_f32 v76, s35, v76\nv_mul_f32 v77, s35, v77\nv_mfma_f32_16x16x32_bf16 v[84:87], v[48:51], a[172:175], v[84:87]\nds_read_b128 v[48:51], v25 offset:16640\n"
"v_mul_f32 v78, s35, v78\nv_mul_f32 v79, s35, v79\ns_waitcnt lgkmcnt(3)\nv_mfma_f32_16x16x32_bf16 v[88:91], v[52:55], a[140:143], v[88:91]\n"
"v_cvt_pk_bf16_f32 v198, v76, v77\nv_cvt_pk_bf16_f32 v199, v78, v79\nv_mfma_f32_16x16x32_bf16 v[92:95], v[52:55], a[172:175], v[92:95]\nds_read_b128 v[52:55], v29 offset:16640\n"
"s_lshl_b32 s54, s44, 11\ns_add_u32 s54, s54, 0\ns_waitcnt lgkmcnt(3)\nv_mfma_f32_16x16x32_bf16 v[80:83], v[40:43], a[144:147], v[80:83]\n"
"v_mov_b32 v56, v192\nv_mov_b32 v60, v196\nv_mfma_f32_16x16x32_bf16 v[84:87], v[40:43], a[176:179], v[84:87]\nds_read_b128 v[40:43], v26 offset:16640\n"
"v_mov_b32 v57, v193\nv_mov_b32 v61, v197\ns_waitcnt lgkmcnt(3)\nv_mfma_f32_16x16x32_bf16 v[88:91], v[44:47], a[144:147], v[88:91]\n"
"v_mov_b32 v58, v194\nv_mov_b32 v62, v198\nv_mfma_f32_16x16x32_bf16 v[92:95], v[44:47], a[176:179], v[92:95]\nds_read_b128 v[44:47], v30 offset:16640\n"
"v_mov_b32 v59, v195\nv_mov_b32 v63, v199\ns_waitcnt lgkmcnt(3)\nv_mfma_f32_16x16x32_bf16 v[80:83], v[48:51], a[148:151], v[80:83]\n"
"v_mov_b32_dpp v56, v196 row_ror:8 row_mask:0xf bank_mask:0xc\nv_mov_b32_dpp v60, v192 row_ror:8 row_mask:0xf bank_mask:0x3\nv_mfma_f32_16x16x32_bf16 v[84:87], v[48:51], a[180:183], v[84:87]\nds_read_b128 v[48:51], v27 offset:16640\n"
"v_mov_b32_dpp v57, v197 row_ror:8 row_mask:0xf bank_mask:0xc\nv_mov_b32_dpp v61, v193 row_ror:8 row_mask:0xf bank_mask:0x3\ns_waitcnt lgkmcnt(3)\nv_mfma_f32_16x16x32_bf16 v[88:91], v[52:55], a[148:151], v[88:91]\n"
"v_mov_b32_dpp v58, v198 row_ror:8 row_mask:0xf bank_mask:0xc\nv_mov_b32_dpp v62, v194 row_ror:8 row_mask:0xf bank_mask:0x3\nv_mfma_f32_16x16x32_bf16 v[92:95], v[52:55], a[180:183], v[92:95]\nds_read_b128 v[52:55], v31 offset:16640\n"
"v_mov_b32_dpp v59, v199 row_ror:8 row_mask:0xf bank_mask:0xc\nv_mov_b32_dpp v63, v195 row_ror:8 row_mask:0xf bank_mask:0x3\ns_waitcnt lgkmcnt(3)\nv_mfma_f32_16x16x32_bf16 v[80:83], v[40:43], a[152:155], v[80:83]\n"
"v_mfma_f32_16x16x32_bf16 v[84:87], v[40:43], a[184:187], v[84:87]\nbuffer_store_dwordx4 v[56:59], v13, s[16:19], s54 offen sc1\ns_waitcnt lgkmcnt(2)\nv_mfma_f32_16x16x32_bf16 v[88:91], v[44:47], a[152:155], v[88:91]\n"
"s_add_u32 s54, s54, 14336\nv_mfma_f32_16x16x32_bf16 v[92:95], v[44:47], a[184:187], v[92:95]\nbuffer_store_dwordx4 v[60:63], v13, s[16:19], s54 offen sc1\ns_waitcnt lgkmcnt(1)\n"
"v_mfma_f32_16x16x32_bf16 v[80:83], v[48:51], a[156:159], v[80:83]\ns_add_u32 m0, s32, 45056\nv_mfma_f32_16x16x32_bf16 v[84:87], v[48:51], a[188:191], v[84:87]\ns_lshl_b32 s53, s43, 7\n"
"s_add_u32 s53, s53, 28672\ns_waitcnt lgkmcnt(0)\nv_mfma_f32_16x16x32_bf16 v[88:91], v[52:55], a[156:159], v[88:91]\nbuffer_load_dwordx4 v10, s[12:15], s53 offen lds\n"
"v_mfma_f32_16x16x32_bf16 v[92:95], v[52:55], a[188:191], v[92:95]\ns_add_u32 m0, s32, 49152\ns_lshl_b32 s53, s44, 7\ns_add_u32 s53, s53, 28672\n"
"buffer_load_dwordx4 v10, s[12:15], s53 offen lds\ns_add_u32 m0, s32, 53248\ns_lshl_b32 s53, s45, 7\ns_add_u32 s53, s53, 28672\n"
"buffer_load_dwordx4 v10, s[12:15], s53 offen lds\ns_add_u32 m0, s32, 57344\ns_lshl_b32 s53, s46, 7\ns_add_u32 s53, s53, 28672\n"
"buffer_load_dwordx4 v10, s[12:15], s53 offen lds\nv_add_u32 v24, 32768, v24\nv_add_u32 v25, 32768, v25\nv_add_u32 v26, 32768, v26\n"
"v_add_u32 v27, 32768, v27\nv_add_u32 v28, 32768, v28\nv_add_u32 v29, 32768, v29\nv_add_u32 v30, 32768, v30\n"
"v_add_u32 v31, 32768, v31\ns_waitcnt vmcnt(23)\ns_barrier\nds_read_b128 v[40:43], v24 offset:0\n"
"ds_read_b128 v[44:47], v28 offset:0\nds_read_b128 v[48:51], v25 offset:0\nds_read_b128 v[52:55], v29 offset:0\ns_waitcnt lgkmcnt(3)\n"
"v_mfma_f32_16x16x32_bf16 v[64:67], v[40:43], a[128:131], 0\nv_mfma_f32_16x16x32_bf16 v[68:71], v[40:43], a[160:163], 0\nds_read_b128 v[40:43], v26 offset:0\ns_waitcnt lgkmcnt(3)\n"
"v_mfma_f32_16x16x32_bf16 v[72:75], v[44:47], a[128:131], 0\nv_mfma_f32_16x16x32_bf16 v[76:79], v[44:47], a[160:163], 0\nds_read_b128 v[44:47], v30 offset:0\nv_mul_f32 v80, s35, v80\n"
"v_mul_f32 v81, s35, v81\ns_waitcnt lgkmcnt(3)\nv_mfma_f32_16x16x32_bf16 v[64:67], v[48:51], a[132:135], v[64:67]\nv_mul_f32 v82, s35, v82\n"
"v_mul_f32 v83, s35, v83\nv_mfma_f32_16x16x32_bf16 v[68:71], v[48:51], a[164:167], v[68:71]\nds_read_b128 v[48:51], v27 offset:0\nv_cvt_pk_bf16_f32 v200, v80, v81\n"
"v_cvt_pk_bf16_f32 v201, v82, v83\ns_waitcnt lgkmcnt(3)\nv_mfma_f32_16x16x32_bf16 v[72:75], v[52:55], a[132:135], v[72:75]\nv_mul_f32 v88, s35, v88\n"
"v_mul_f32 v89, s35, v89\nv_mfma_f32_16x16x32_bf16 v[76:79], v[52:55], a[164:167], v[76:79]\nds_read_b128 v[52:55], v31 offset:0\nv_mul_f32 v90, s35, v90\n"
"v_mul_f32 v91, s35, v91\ns_waitcnt lgkmcnt(3)\nv_mfma_f32_16x16x32_bf16 v[64:67], v[40:43], a[136:139], v[64:67]\nv_cvt_pk_bf16_f32 v202, v88, v89\n"
"v_cvt_pk_bf16_f32 v203, v90, v91\nv_mfma_f32_16x16x32_bf16 v[68:71], v[40:43], a[168:171], v[68:71]\nds_read_b128 v[40:43], v24 offset:256\nv_mul_f32 v84, s35, v84\n"
"v_mul_f32 v85, s35, v85\ns_waitcnt lgkmcnt(3)\nv_mfma_f32_16x16x32_bf16 v[72:75], v[44:47], a[136:139], v[72:75]\nv_mul_f32 v86, s35, v86\n"
"v_mul_f32 v87, s35, v87\nv_mfma_f32_16x16x32_bf16 v[76:79], v[44:47], a[168:171], v[76:79]\nds_read_b128 v[44:47], v28 offset:256\nv_cvt_pk_bf16_f32 v204, v84, v85\n"
"v_cvt_pk_bf16_f32 v205, v86, v87\ns_waitcnt lgkmcnt(3)\nv_mfma_f32_16x16x32_bf16 v[64:67], v[48:51], a[140:143], v[64:67]\nv_mul_f32 v92, s35, v92\n"
"v_mul_f32 v93, s35, v93\nv_mfma_f32_16x16x32_bf16 v[68:71], v[48:51], a[172:175], v[68:71]\nds_read_b128 v[48:51], v25 offset:256\nv_mul_f32 v94, s35, v94\n"
"v_mul_f32 v95, s35, v95\ns_waitcnt lgkmcnt(3)\nv_mfma_f32_16x16x32_bf16 v[72:75], v[52:55], a[140:143], v[72:75]\nv_cvt_pk_bf16_f32 v206, v92, v93\n"
"v_cvt_pk_bf16_f32 v207, v94, v95\nv_mfma_f32_16x16x32_bf16 v[76:79], v[52:55], a[172:175], v[76:79]\nds_read_b128 v[52:55], v29 offset:256\ns_lshl_b32 s54, s44, 11\n"
"s_add_u32 s54, s54, 1024\ns_waitcnt lgkmcnt(3)\nv_mfma_f32_16x16x32_bf16 v[64:67], v[40:43], a[144:147], v[64:67]\nv_mov_b32 v56, v200\n"
"v_mov_b32 v60, v204\nv_mfma_f32_16x16x32_bf16 v[68:71], v[40:43], a[176:179], v[68:71]\nds_read_b128 v[40:43], v26 offset:256\nv_mov_b32 v57, v201\n"
"v_mov_b32 v61, v205\ns_waitcnt lgkmcnt(3)\nv_mfma_f32_16x16x32_bf16 v[72:75], v[44:47], a[144:147], v[72:75]\nv_mov_b32 v58, v202\n"
"v_mov_b32 v62, v206\nv_mfma_f32_16x16x32_bf16 v[76:79], v[44:47], a[176:179], v[76:79]\nds_read_b128 v[44:47], v30 offset:256\nv_mov_b32 v59, v203\n"
"v_mov_b32 v63, v207\ns_waitcnt lgkmcnt(3)\nv_mfma_f32_16x16x32_bf16 v[64:67], v[48:51], a[148:151], v[64:67]\nv_mov_b32_dpp v56, v204 row_ror:8 row_mask:0xf bank_mask:0xc\n"
"v_mov_b32_dpp v60, v200 row_ror:8 row_mask:0xf bank_mask:0x3\nv_mfma_f32_16x16x32_bf16 v[68:71], v[48:51], a[180:183], v[68:71]\nds_read_b128 v[48:51], v27 offset:256\nv_mov_b32_dpp v57, v205 row_ror:8 row_mask:0xf bank_mask:0xc\n"
"v_mov_b32_dpp v61, v201 row_ror:8 row_mask:0xf bank_mask:0x3\ns_waitcnt lgkmcnt(3)\nv_mfma_f32_16x16x32_bf16 v[72:75], v[52:55], a[148:151], v[72:75]\nv_mov_b32_dpp v58, v206 row_ror:8 row_mask:0xf bank_mask:0xc\n"
"v_mov_b32_dpp v62, v202 row_ror:8 row_mask:0xf bank_mask:0x3\nv_mfma_f32_16x16x32_bf16 v[76:79], v[52:55], a[180:183], v[76:79]\nds_read_b128 v[52:55], v31 offset:256\nv_mov_b32_dpp v59, v207 row_ror:8 row_mask:0xf bank_mask:0xc\n"
"v_mov_b32_dpp v63, v203 row_ror:8 row_mask:0xf bank_mask:0x3\ns_waitcnt lgkmcnt(3)\nv_mfma_f32_16x16x32_bf16 v[64:67], v[40:43], a[152:155], v[64:67]\nv_mfma_f32_16x16x32_bf16 v[68:71], v[40:43], a[184:187], v[68:71]\n"
"ds_read_b128 v[40:43], v24 offset:16384\nbuffer_store_dwordx4 v[56:59], v13, s[16:19], s54 offen sc1\ns_waitcnt lgkmcnt(3)\nv_mfma_f32_16x16x32_bf16 v[72:75], v[44:47], a[152:155], v[72:75]\n"
"s_add_u32 s54, s54, 14336\nv_mfma_f32_16x16x32_bf16 v[76:79], v[44:47], a[184:187], v[76:79]\nds_read_b128 v[44:47], v28 offset:16384\nbuffer_store_dwordx4 v[60:63], v13, s[16:19], s54 offen sc1\n"
"s_waitcnt lgkmcnt(3)\nv_mfma_f32_16x16x32_bf16 v[64:67], v[48:51], a[156:159], v[64:67]\ns_add_u32 m0, s32, 65536\nv_mfma_f32_16x16x32_bf16 v[68:71], v[48:51], a[188:191], v[68:71]\n"
"ds_read_b128 v[48:51], v25 offset:16384\ns_lshl_b32 s53, s40, 7\ns_add_u32 s53, s53, 57344\ns_waitcnt lgkmcnt(3)\n"
"v_mfma_f32_16x16x32_bf16 v[72:75], v[52:55], a[156:159], v[72:75]\nbuffer_load_dwordx4 v10, s[12:15], s53 offen lds\nv_mfma_f32_16x16x32_bf16 v[76:79], v[52:55], a[188:191], v[76:79]\nds_read_b128 v[52:55], v29 offset:16384\n"
"s_add_u32 m0, s32, 69632\ns_lshl_b32 s53, s41, 7\ns_add_u32 s53, s53, 57344\nbuffer_load_dwordx4 v10, s[12:15], s53 offen lds\n"
"s_add_u32 m0, s32, 73728\ns_lshl_b32 s53, s42, 7\ns_add_u32 s53, s53, 57344\nbuffer_load_dwordx4 v10, s[12:15], s53 offen lds\n"
"s_waitcnt lgkmcnt(3)\nv_mfma_f32_16x16x32_bf16 v[80:83], v[40:43], a[128:131], 0\nv_mfma_f32_16x16x32_bf16 v[84:87], v[40:43], a[160:163], 0\nds_read_b128 v[40:43], v26 offset:16384\n"
"s_waitcnt lgkmcnt(3)\nv_mfma_f32_16x16x32_bf16 v[88:91], v[44:47], a[128:131], 0\nv_mfma_f32_16x16x32_bf16 v[92:95], v[44:47], a[160:163], 0\nds_read_b128 v[44:47], v30 offset:16384\n"
"v_mul_f32 v64, s35, v64\nv_mul_f32 v65, s35, v65\ns_waitcnt lgkmcnt(3)\nv_mfma_f32_16x16x32_bf16 v[80:83], v[48:51], a[132:135], v[80:83]\n"
"v_mul_f32 v66, s35, v66\nv_mul_f32 v67, s35, v67\nv_mfma_f32_16x16x32_bf16 v[84:87], v[48:51], a[164:167], v[84:87]\nds_read_b128 v[48:51], v27 offset:16384\n"
"v_cvt_pk_bf16_f32 v208, v64, v65\nv_cvt_pk_bf16_f32 v209, v66, v67\ns_waitcnt lgkmcnt(3)\nv_mfma_f32_16x16x32_bf16 v[88:91], v[52:55], a[132:135], v[88:91]\n"
"v_mul_f32 v72, s35, v72\nv_mul_f32 v73, s35, v73\nv_mfma_f32_16x16x32_bf16 v[92:95], v[52:55], a[164:167], v[92:95]\nds_read_b128 v[52:55], v31 offset:16384\n"
"v_mul_f32 v74, s35, v74\nv_mul_f32 v75, s35, v75\ns_waitcnt lgkmcnt(3)\nv_mfma_f32_16x16x32_bf16 v[80:83], v[40:43], a[136:139], v[80:83]\n"
"v_cvt_pk_bf16_f32 v210, v72, v73\nv_cvt_pk_bf16_f32 v211, v74, v75\nv_mfma_f32_16x16x32_bf16 v[84:87], v[40:43], a[168:171], v[84:87]\nds_read_b128 v[40:43], v24 offset:16640\n"
"v_mul_f32 v68, s35, v68\nv_mul_f32 v69, s35, v69\ns_waitcnt lgkmcnt(3)\nv_mfma_f32_16x16x32_bf16 v[88:91], v[44:47], a[136:139], v[88:91]\n"
"v_mul_f32 v70, s35, v70\nv_mul_f32 v71, s35, v71\nv_mfma_f32_16x16x32_bf16 v[92:95], v[44:47], a[168:171], v[92:95]\nds_read_b128 v[44:47], v28 offset:16640\n"
"v_cvt_pk_bf16_f32 v212, v68, v69\nv_cvt_pk_bf16_f32 v213, v70, v71\ns_waitcnt lgkmcnt(3)\nv_mfma_f32_16x16x32_bf16 v[80:83], v[48:51], a[140:143], v[80:83]\n"
"v_mul_f32 v76, s35, v76\nv_mul_f32 v77, s35, v77\nv_mfma_f32_16x16x32_bf16 v[84:87], v[48:51], a[172:175], v[84:87]\nds_read_b128 v[48:51], v25 offset:16640\n"
"v_mul_f32 v78, s35, v78\nv_mul_f32 v79, s35, v79\ns_waitcnt lgkmcnt(3)\nv_mfma_f32_16x16x32_bf16 v[88:91], v[52:55], a[140:143], v[88:91]\n"
"v_cvt_pk_bf16_f32 v214, v76, v77\nv_cvt_pk_bf16_f32 v215, v78, v79\nv_mfma_f32_16x16x32_bf16 v[92:95], v[52:55], a[172:175], v[92:95]\nds_read_b128 v[52:55], v29 offset:16640\n"
"s_lshl_b32 s54, s45, 11\ns_add_u32 s54, s54, 0\ns_waitcnt lgkmcnt(3)\nv_mfma_f32_16x16x32_bf16 v[80:83], v[40:43], a[144:147], v[80:83]\n"
"v_mov_b32 v56, v208\nv_mov_b32 v60, v212\nv_mfma_f32_16x16x32_bf16 v[84:87], v[40:43], a[176:179], v[84:87]\nds_read_b128 v[40:43], v26 offset:16640\n"
"v_mov_b32 v57, v209\nv_mov_b32 v61, v213\ns_waitcnt lgkmcnt(3)\nv_mfma_f32_16x16x32_bf16 v[88:91], v[44:47], a[144:147], v[88:91]\n"
"v_mov_b32 v58, v210\nv_mov_b32 v62, v214\nv_mfma_f32_16x16x32_bf16 v[92:95], v[44:47], a[176:179], v[92:95]\nds_read_b128 v[44:47], v30 offset:16640\n"
"v_mov_b32 v59, v211\nv_mov_b32 v63, v215\ns_waitcnt lgkmcnt(3)\nv_mfma_f32_16x16x32_bf16 v[80:83], v[48:51], a[148:151], v[80:83]\n"
"v_mov_b32_dpp v56, v212 row_ror:8 row_mask:0xf bank_mask:0xc\nv_mov_b32_dpp v60, v208 row_ror:8 row_mask:0xf bank_mask:0x3\nv_mfma_f32_16x16x32_bf16 v[84:87], v[48:51], a[180:183], v[84:87]\nds_read_b128 v[48:51], v27 offset:16640\n"
"v_mov_b32_dpp v57, v213 row_ror:8 row_mask:0xf bank_mask:0xc\nv_mov_b32_dpp v61, v209 row_ror:8 row_mask:0xf bank_mask:0x3\ns_waitcnt lgkmcnt(3)\nv_mfma_f32_16x16x32_bf16 v[88:91], v[52:55], a[148:151], v[88:91]\n"
"v_mov_b32_dpp v58, v214 row_ror:8 row_mask:0xf bank_mask:0xc\nv_mov_b32_dpp v62, v210 row_ror:8 row_mask:0xf bank_mask:0x3\nv_mfma_f32_16x16x32_bf16 v[92:95], v[52:55], a[180:183], v[92:95]\nds_read_b128 v[52:55], v31 offset:16640\n"
"v_mov_b32_dpp v59, v215 row_ror:8 row_mask:0xf bank_mask:0xc\nv_mov_b32_dpp v63, v211 row_ror:8 row_mask:0xf bank_mask:0x3\ns_waitcnt lgkmcnt(3)\nv_mfma_f32_16x16x32_bf16 v[80:83], v[40:43], a[152:155], v[80:83]\n"
"v_mfma_f32_16x16x32_bf16 v[84:87], v[40:43], a[184:187], v[84:87]\nbuffer_store_dwordx4 v[56:59], v13, s[16:19], s54 offen sc1\ns_waitcnt lgkmcnt(2)\nv_mfma_f32_16x16x32_bf16 v[88:91], v[44:47], a[152:155], v[88:91]\n"
"s_add_u32 s54, s54, 14336\nv_mfma_f32_16x16x32_bf16 v[92:95], v[44:47], a[184:187], v[92:95]\nbuffer_store_dwordx4 v[60:63], v13, s[16:19], s54 offen sc1\ns_waitcnt lgkmcnt(1)\n"
"v_mfma_f32_16x16x32_bf16 v[80:83], v[48:51], a[156:159], v[80:83]\ns_add_u32 m0, s32, 77824\nv_mfma_f32_16x16x32_bf16 v[84:87], v[48:51], a[188:191], v[84:87]\ns_lshl_b32 s53, s43, 7\n"
"s_add_u32 s53, s53, 57344\ns_waitcnt lgkmcnt(0)\nv_mfma_f32_16x16x32_bf16 v[88:91], v[52:55], a[156:159], v[88:91]\nbuffer_load_dwordx4 v10, s[12:15], s53 offen lds\n"
"v_mfma_f32_16x16x32_bf16 v[92:95], v[52:55], a[188:191], v[92:95]\ns_add_u32 m0, s32, 81920\ns_lshl_b32 s53, s44, 7\ns_add_u32 s53, s53, 57344\n"
"buffer_load_dwordx4 v10, s[12:15], s53 offen lds\ns_add_u32 m0, s32, 86016\ns_lshl_b32 s53, s45, 7\ns_add_u32 s53, s53, 57344\n"
"buffer_load_dwordx4 v10, s[12:15], s53 offen lds\ns_add_u32 m0, s32, 90112\ns_lshl_b32 s53, s46, 7\ns_add_u32 s53, s53, 57344\n"
"buffer_load_dwordx4 v10, s[12:15], s53 offen lds\nv_add_u32 v24, 32768, v24\nv_add_u32 v25, 32768, v25\nv_add_u32 v26, 32768, v26\n"
"v_add_u32 v27, 32768, v27\nv_add_u32 v28, 32768, v28\nv_add_u32 v29, 32768, v29\nv_add_u32 v30, 32768, v30\n"
"v_add_u32 v31, 32768, v31\ns_waitcnt vmcnt(22)\ns_barrier\nds_read_b128 v[40:43], v24 offset:0\n"
"ds_read_b128 v[44:47], v28 offset:0\nds_read_b128 v[48:51], v25 offset:0\nds_read_b128 v[52:55], v29 offset:0\ns_waitcnt lgkmcnt(3)\n"
"v_mfma_f32_16x16x32_bf16 v[64:67], v[40:43], a[128:131], 0\nv_mfma_f32_16x16x32_bf16 v[68:71], v[40:43], a[160:163], 0\nds_read_b128 v[40:43], v26 offset:0\ns_waitcnt lgkmcnt(3)\n"
"v_mfma_f32_16x16x32_bf16 v[72:75], v[44:47], a[128:131], 0\nv_mfma_f32_16x16x32_bf16 v[76:79], v[44:47], a[160:163], 0\nds_read_b128 v[44:47], v30 offset:0\nv_mul_f32 v80, s35, v80\n"
"v_mul_f32 v81, s35, v81\ns_waitcnt lgkmcnt(3)\nv_mfma_f32_16x16x32_bf16 v[64:67], v[48:51], a[132:135], v[64:67]\nv_mul_f32 v82, s35, v82\n"
"v_mul_f32 v83, s35, v83\nv_mfma_f32_16x16x32_bf16 v[68:71], v[48:51], a[164:167], v[68:71]\nds_read_b128 v[48:51], v27 offset:0\nv_cvt_pk_bf16_f32 v216, v80, v81\n"
"v_cvt_pk_bf16_f32 v217, v82, v83\ns_waitcnt lgkmcnt(3)\nv_mfma_f32_16x16x32_bf16 v[72:75], v[52:55], a[132:135], v[72:75]\nv_mul_f32 v88, s35, v88\n"
"v_mul_f32 v89, s35, v89\nv_mfma_f32_16x16x32_bf16 v[76:79], v[52:55], a[164:167], v[76:79]\nds_read_b128 v[52:55], v31 offset:0\nv_mul_f32 v90, s35, v90\n"
"v_mul_f32 v91, s35, v91\ns_waitcnt lgkmcnt(3)\nv_mfma_f32_16x16x32_bf16 v[64:67], v[40:43], a[136:139], v[64:67]\nv_cvt_pk_bf16_f32 v218, v88, v89\n"
"v_cvt_pk_bf16_f32 v219, v90, v91\nv_mfma_f32_16x16x32_bf16 v[68:71], v[40:43], a[168:171], v[68:71]\nds_read_b128 v[40:43], v24 offset:256\nv_mul_f32 v84, s35, v84\n"
"v_mul_f32 v85, s35, v85\ns_waitcnt lgkmcnt(3)\nv_mfma_f32_16x16x32_bf16 v[72:75], v[44:47], a[136:139], v[72:75]\nv_mul_f32 v86, s35, v86\n"
"v_mul_f32 v87, s35, v87\nv_mfma_f32_16x16x32_bf16 v[76:79], v[44:47], a[168:171], v[76:79]\nds_read_b128 v[44:47], v28 offset:256\nv_cvt_pk_bf16_f32 v220, v84, v85\n"
"v_cvt_pk_bf16_f32 v221, v86, v87\ns_waitcnt lgkmcnt(3)\nv_mfma_f32_16x16x32_bf16 v[64:67], v[48:51], a[140:143], v[64:67]\nv_mul_f32 v92, s35, v92\n"
"v_mul_f32 v93, s35, v93\nv_mfma_f32_16x16x32_bf16 v[68:71], v[48:51], a[172:175], v[68:71]\nds_read_b128 v[48:51], v25 offset:256\nv_mul_f32 v94, s35, v94\n"
"v_mul_f32 v95, s35, v95\ns_waitcnt lgkmcnt(3)\nv_mfma_f32_16x16x32_bf16 v[72:75], v[52:55], a[140:143], v[72:75]\nv_cvt_pk_bf16_f32 v222, v92, v93\n"
"v_cvt_pk_bf16_f32 v223, v94, v95\nv_mfma_f32_16x16x32_bf16 v[76:79], v[52:55], a[172:175], v[76:79]\nds_read_b128 v[52:55], v29 offset:256\ns_lshl_b32 s54, s45, 11\n"
"s_add_u32 s54, s54, 1024\ns_waitcnt lgkmcnt(3)\nv_mfma_f32_16x16x32_bf16 v[64:67], v[40:43], a[144:147], v[64:67]\nv_mov_b32 v56, v216\n"
"v_mov_b32 v60, v220\nv_mfma_f32_16x16x32_bf16 v[68:71], v[40:43], a[176:179], v[68:71]\nds_read_b128 v[40:43], v26 offset:256\nv_mov_b32 v57, v217\n"
"v_mov_b32 v61, v221\ns_waitcnt lgkmcnt(3)\nv_mfma_f32_16x16x32_bf16 v[72:75], v[44:47], a[144:147], v[72:75]\nv_mov_b32 v58, v218\n"
"v_mov_b32 v62, v222\nv_mfma_f32_16x16x32_bf16 v[76:79], v[44:47], a[176:179], v[76:79]\nds_read_b128 v[44:47], v30 offset:256\nv_mov_b32 v59, v219\n"
"v_mov_b32 v63, v223\ns_waitcnt lgkmcnt(3)\nv_mfma_f32_16x16x32_bf16 v[64:67], v[48:51], a[148:151], v[64:67]\nv_mov_b32_dpp v56, v220 row_ror:8 row_mask:0xf bank_mask:0xc\n"
"v_mov_b32_dpp v60, v216 row_ror:8 row_mask:0xf bank_mask:0x3\nv_mfma_f32_16x16x32_bf16 v[68:71], v[48:51], a[180:183], v[68:71]\nds_read_b128 v[48:51], v27 offset:256\nv_mov_b32_dpp v57, v221 row_ror:8 row_mask:0xf bank_mask:0xc\n"
"v_mov_b32_dpp v61, v217 row_ror:8 row_mask:0xf bank_mask:0x3\ns_waitcnt lgkmcnt(3)\nv_mfma_f32_16x16x32_bf16 v[72:75], v[52:55], a[148:151], v[72:75]\nv_mov_b32_dpp v58, v222 row_ror:8 row_mask:0xf bank_mask:0xc\n"
"v_mov_b32_dpp v62, v218 row_ror:8 row_mask:0xf bank_mask:0x3\nv_mfma_f32_16x16x32_bf16 v[76:79], v[52:55], a[180:183], v[76:79]\nds_read_b128 v[52:55], v31 offset:256\nv_mov_b32_dpp v59, v223 row_ror:8 row_mask:0xf bank_mask:0xc\n"
"v_mov_b32_dpp v63, v219 row_ror:8 row_mask:0xf bank_mask:0x3\ns_waitcnt lgkmcnt(3)\nv_mfma_f32_16x16x32_bf16 v[64:67], v[40:43], a[152:155], v[64:67]\nv_mfma_f32_16x16x32_bf16 v[68:71], v[40:43], a[184:187], v[68:71]\n"
"ds_read_b128 v[40:43], v24 offset:16384\nbuffer_store_dwordx4 v[56:59], v13, s[16:19], s54 offen sc1\ns_waitcnt lgkmcnt(3)\nv_mfma_f32_16x16x32_bf16 v[72:75], v[44:47], a[152:155], v[72:75]\n"
"s_add_u32 s54, s54, 14336\nv_mfma_f32_16x16x32_bf16 v[76:79], v[44:47], a[184:187], v[76:79]\nds_read_b128 v[44:47], v28 offset:16384\nbuffer_store_dwordx4 v[60:63], v13, s[16:19], s54 offen sc1\n"
"s_waitcnt lgkmcnt(3)\nv_mfma_f32_16x16x32_bf16 v[64:67], v[48:51], a[156:159], v[64:67]\ns_add_u32 m0, s32, 98304\nv_mfma_f32_16x16x32_bf16 v[68:71], v[48:51], a[188:191], v[68:71]\n"
"ds_read_b128 v[48:51], v25 offset:16384\ns_lshl_b32 s53, s40, 7\ns_add_u32 s53, s53, 86016\ns_waitcnt lgkmcnt(3)\n"
"v_mfma_f32_16x16x32_bf16 v[72:75], v[52:55], a[156:159], v[72:75]\nbuffer_load_dwordx4 v10, s[12:15], s53 offen lds\nv_mfma_f32_16x16x32_bf16 v[76:79], v[52:55], a[188:191], v[76:79]\nds_read_b128 v[52:55], v29 offset:16384\n"
"s_add_u32 m0, s32, 102400\ns_lshl_b32 s53, s41, 7\ns_add_u32 s53, s53, 86016\nbuffer_load_dwordx4 v10, s[12:15], s53 offen lds\n"
"s_add_u32 m0, s32, 106496\ns_lshl_b32 s53, s42, 7\ns_add_u32 s53, s53, 86016\nbuffer_load_dwordx4 v10, s[12:15], s53 offen lds\n"
"s_waitcnt lgkmcnt(3)\nv_mfma_f32_16x16x32_bf16 v[80:83], v[40:43], a[128:131], 0\nv_mfma_f32_16x16x32_bf16 v[84:87], v[40:43], a[160:163], 0\nds_read_b128 v[40:43], v26 offset:16384\n"
"s_waitcnt lgkmcnt(3)\nv_mfma_f32_16x16x32_bf16 v[88:91], v[44:47], a[128:131], 0\nv_mfma_f32_16x16x32_bf16 v[92:95], v[44:47], a[160:163], 0\nds_read_b128 v[44:47], v30 offset:16384\n"
"v_mul_f32 v64, s35, v64\nv_mul_f32 v65, s35, v65\ns_waitcnt lgkmcnt(3)\nv_mfma_f32_16x16x32_bf16 v[80:83], v[48:51], a[132:135], v[80:83]\n"
"v_mul_f32 v66, s35, v66\nv_mul_f32 v67, s35, v67\nv_mfma_f32_16x16x32_bf16 v[84:87], v[48:51], a[164:167], v[84:87]\nds_read_b128 v[48:51], v27 offset:16384\n"
"v_cvt_pk_bf16_f32 v224, v64, v65\nv_cvt_pk_bf16_f32 v225, v66, v67\ns_waitcnt lgkmcnt(3)\nv_mfma_f32_16x16x32_bf16 v[88:91], v[52:55], a[132:135], v[88:91]\n"
"v_mul_f32 v72, s35, v72\nv_mul_f32 v73, s35, v73\nv_mfma_f32_16x16x32_bf16 v[92:95], v[52:55], a[164:167], v[92:95]\nds_read_b128 v[52:55], v31 offset:16384\n"
"v_mul_f32 v74, s35, v74\nv_mul_f32 v75, s35, v75\ns_waitcnt lgkmcnt(3)\nv_mfma_f32_16x16x32_bf16 v[80:83], v[40:43], a[136:139], v[80:83]\n"
"v_cvt_pk_bf16_f32 v226, v72, v73\nv_cvt_pk_bf16_f32 v227, v74, v75\nv_mfma_f32_16x16x32_bf16 v[84:87], v[40:43], a[168:171], v[84:87]\nds_read_b128 v[40:43], v24 offset:16640\n"
"v_mul_f32 v68, s35, v68\nv_mul_f32 v69, s35, v69\ns_waitcnt lgkmcnt(3)\nv_mfma_f32_16x16x32_bf16 v[88:91], v[44:47], a[136:139], v[88:91]\n"
"v_mul_f32 v70, s35, v70\nv_mul_f32 v71, s35, v71\nv_mfma_f32_16x16x32_bf16 v[92:95], v[44:47], a[168:171], v[92:95]\nds_read_b128 v[44:47], v28 offset:16640\n"
"v_cvt_pk_bf16_f32 v228, v68, v69\nv_cvt_pk_bf16_f32 v229, v70, v71\ns_waitcnt lgkmcnt(3)\nv_mfma_f32_16x16x32_bf16 v[80:83], v[48:51], a[140:143], v[80:83]\n"
"v_mul_f32 v76, s35, v76\nv_mul_f32 v77, s35, v77\nv_mfma_f32_16x16x32_bf16 v[84:87], v[48:51], a[172:175], v[84:87]\nds_read_b128 v[48:51], v25 offset:16640\n"
"v_mul_f32 v78, s35, v78\nv_mul_f32 v79, s35, v79\ns_waitcnt lgkmcnt(3)\nv_mfma_f32_16x16x32_bf16 v[88:91], v[52:55], a[140:143], v[88:91]\n"
"v_cvt_pk_bf16_f32 v230, v76, v77\nv_cvt_pk_bf16_f32 v231, v78, v79\nv_mfma_f32_16x16x32_bf16 v[92:95], v[52:55], a[172:175], v[92:95]\nds_read_b128 v[52:55], v29 offset:16640\n"
"s_lshl_b32 s54, s46, 11\ns_add_u32 s54, s54, 0\ns_waitcnt lgkmcnt(3)\nv_mfma_f32_16x16x32_bf16 v[80:83], v[40:43], a[144:147], v[80:83]\n"
"v_mov_b32 v56, v224\nv_mov_b32 v60, v228\nv_mfma_f32_16x16x32_bf16 v[84:87], v[40:43], a[176:179], v[84:87]\nds_read_b128 v[40:43], v26 offset:16640\n"
"v_mov_b32 v57, v225\nv_mov_b32 v61, v229\ns_waitcnt lgkmcnt(3)\nv_mfma_f32_16x16x32_bf16 v[88:91], v[44:47], a[144:147], v[88:91]\n"
"v_mov_b32 v58, v226\nv_mov_b32 v62, v230\nv_mfma_f32_16x16x32_bf16 v[92:95], v[44:47], a[176:179], v[92:95]\nds_read_b128 v[44:47], v30 offset:16640\n"
"v_mov_b32 v59, v227\nv_mov_b32 v63, v231\ns_waitcnt lgkmcnt(3)\nv_mfma_f32_16x16x32_bf16 v[80:83], v[48:51], a[148:151], v[80:83]\n"
"v_mov_b32_dpp v56, v228 row_ror:8 row_mask:0xf bank_mask:0xc\nv_mov_b32_dpp v60, v224 row_ror:8 row_mask:0xf bank_mask:0x3\nv_mfma_f32_16x16x32_bf16 v[84:87], v[48:51], a[180:183], v[84:87]\nds_read_b128 v[48:51], v27 offset:16640\n"
"v_mov_b32_dpp v57, v229 row_ror:8 row_mask:0xf bank_mask:0xc\nv_mov_b32_dpp v61, v225 row_ror:8 row_mask:0xf bank_mask:0x3\ns_waitcnt lgkmcnt(3)\nv_mfma_f32_16x16x32_bf16 v[88:91], v[52:55], a[148:151], v[88:91]\n"
"v_mov_b32_dpp v58, v230 row_ror:8 row_mask:0xf bank_mask:0xc\nv_mov_b32_dpp v62, v226 row_ror:8 row_mask:0xf bank_mask:0x3\nv_mfma_f32_16x16x32_bf16 v[92:95], v[52:55], a[180:183], v[92:95]\nds_read_b128 v[52:55], v31 offset:16640\n"
"v_mov_b32_dpp v59, v231 row_ror:8 row_mask:0xf bank_mask:0xc\nv_mov_b32_dpp v63, v227 row_ror:8 row_mask:0xf bank_mask:0x3\ns_waitcnt lgkmcnt(3)\nv_mfma_f32_16x16x32_bf16 v[80:83], v[40:43], a[152:155], v[80:83]\n"
"v_mfma_f32_16x16x32_bf16 v[84:87], v[40:43], a[184:187], v[84:87]\nbuffer_store_dwordx4 v[56:59], v13, s[16:19], s54 offen sc1\ns_waitcnt lgkmcnt(2)\nv_mfma_f32_16x16x32_bf16 v[88:91], v[44:47], a[152:155], v[88:91]\n"
"s_add_u32 s54, s54, 14336\nv_mfma_f32_16x16x32_bf16 v[92:95], v[44:47], a[184:187], v[92:95]\nbuffer_store_dwordx4 v[60:63], v13, s[16:19], s54 offen sc1\ns_waitcnt lgkmcnt(1)\n"
"v_mfma_f32_16x16x32_bf16 v[80:83], v[48:51], a[156:159], v[80:83]\ns_add_u32 m0, s32, 110592\nv_mfma_f32_16x16x32_bf16 v[84:87], v[48:51], a[188:191], v[84:87]\ns_lshl_b32 s53, s43, 7\n"
"s_add_u32 s53, s53, 86016\ns_waitcnt lgkmcnt(0)\nv_mfma_f32_16x16x32_bf16 v[88:91], v[52:55], a[156:159], v[88:91]\nbuffer_load_dwordx4 v10, s[12:15], s53 offen lds\n"
"v_mfma_f32_16x16x32_bf16 v[92:95], v[52:55], a[188:191], v[92:95]\ns_add_u32 m0, s32, 114688\ns_lshl_b32 s53, s44, 7\ns_add_u32 s53, s53, 86016\n"
"buffer_load_dwordx4 v10, s[12:15], s53 offen lds\ns_add_u32 m0, s32, 118784\ns_lshl_b32 s53, s45, 7\ns_add_u32 s53, s53, 86016\n"
"buffer_load_dwordx4 v10, s[12:15], s53 offen lds\ns_add_u32 m0, s32, 122880\ns_lshl_b32 s53, s46, 7\ns_add_u32 s53, s53, 86016\n"
"buffer_load_dwordx4 v10, s[12:15], s53 offen lds\ns_nop 7\nv_mul_f32 v80, s35, v80\nv_mul_f32 v81, s35, v81\n"
"v_mul_f32 v82, s35, v82\nv_mul_f32 v83, s35, v83\nv_cvt_pk_bf16_f32 v232, v80, v81\nv_cvt_pk_bf16_f32 v233, v82, v83\n"
"v_mul_f32 v88, s35, v88\nv_mul_f32 v89, s35, v89\nv_mul_f32 v90, s35, v90\nv_mul_f32 v91, s35, v91\n"
"v_cvt_pk_bf16_f32 v234, v88, v89\nv_cvt_pk_bf16_f32 v235, v90, v91\nv_mul_f32 v84, s35, v84\nv_mul_f32 v85, s35, v85\n"
"v_mul_f32 v86, s35, v86\nv_mul_f32 v87, s35, v87\nv_cvt_pk_bf16_f32 v236, v84, v85\nv_cvt_pk_bf16_f32 v237, v86, v87\n"
"v_mul_f32 v92, s35, v92\nv_mul_f32 v93, s35, v93\nv_mul_f32 v94, s35, v94\nv_mul_f32 v95, s35, v95\n"
"v_cvt_pk_bf16_f32 v238, v92, v93\nv_cvt_pk_bf16_f32 v239, v94, v95\ns_lshl_b32 s54, s46, 11\ns_add_u32 s54, s54, 1024\n"
"v_mov_b32 v56, v232\nv_mov_b32 v60, v236\nv_mov_b32 v57, v233\nv_mov_b32 v61, v237\n"
"v_mov_b32 v58, v234\nv_mov_b32 v62, v238\nv_mov_b32 v59, v235\nv_mov_b32 v63, v239\n"
"v_mov_b32_dpp v56, v236 row_ror:8 row_mask:0xf bank_mask:0xc\nv_mov_b32_dpp v60, v232 row_ror:8 row_mask:0xf bank_mask:0x3\nv_mov_b32_dpp v57, v237 row_ror:8 row_mask:0xf bank_mask:0xc\nv_mov_b32_dpp v61, v233 row_ror:8 row_mask:0xf bank_mask:0x3\n"
"v_mov_b32_dpp v58, v238 row_ror:8 row_mask:0xf bank_mask:0xc\nv_mov_b32_dpp v62, v234 row_ror:8 row_mask:0xf bank_mask:0x3\nv_mov_b32_dpp v59, v239 row_ror:8 row_mask:0xf bank_mask:0xc\nv_mov_b32_dpp v63, v235 row_ror:8 row_mask:0xf bank_mask:0x3\n"
"s_nop 1\nbuffer_store_dwordx4 v[56:59], v13, s[16:19], s54 offen sc1\ns_add_u32 s54, s54, 14336\nbuffer_store_dwordx4 v[60:63], v13, s[16:19], s54 offen sc1\n"
"s_sub_u32 s48, s28, 1\ns_mov_b32 s49, 114688\ns_mov_b32 s50, 0\ns_mov_b32 s51, 0\n"
"s_add_u32 s52, s32, 131072\nds_read_b128 v[40:43], v32 offset:0\nds_read_b128 v[44:47], v33 offset:0\nds_read_b128 v[48:51], v34 offset:0\n"
"ds_read_b128 v[52:55], v35 offset:0\ns_waitcnt vmcnt(24)\ns_barrier\ns_waitcnt lgkmcnt(3)\n"
"v_mfma_f32_16x16x32_bf16 v[64:67], v[128:131], v[40:43], 0\nv_mov_b32 v36, s48\ns_add_u32 s48, s48, 1\ns_nop 0\n"
"buffer_store_dword v36, v19, s[24:27], s33 offen sc1\nv_mfma_f32_16x16x32_bf16 v[68:71], v[132:135], v[40:43], 0\nds_read_b128 v[40:43], v32 offset:4096\ns_waitcnt lgkmcnt(3)\n"
"v_mfma_f32_16x16x32_bf16 v[72:75], v[128:131], v[44:47], 0\nv_mfma_f32_16x16x32_bf16 v[76:79], v[132:135], v[44:47], 0\nds_read_b128 v[44:47], v33 offset:4096\ns_waitcnt lgkmcnt(3)\n"
"v_mfma_f32_16x16x32_bf16 v[64:67], v[136:139], v[48:51], v[64:67]\ns_add_u32 m0, s52, 0\nv_mfma_f32_16x16x32_bf16 v[68:71], v[140:143], v[48:51], v[68:71]\nds_read_b128 v[48:51], v34 offset:4096\n"
"s_lshl_b32 s53, s40, 7\ns_add_u32 s53, s53, s49\ns_waitcnt lgkmcnt(3)\nv_mfma_f32_16x16x32_bf16 v[72:75], v[136:139], v[52:55], v[72:75]\n"
"buffer_load_dwordx4 v10, s[12:15], s53 offen lds\nv_mfma_f32_16x16x32_bf16 v[76:79], v[140:143], v[52:55], v[76:79]\nds_read_b128 v[52:55], v35 offset:4096\ns_add_u32 m0, s52, 4096\n"
"s_waitcnt lgkmcnt(3)\nv_mfma_f32_16x16x32_bf16 v[64:67], v[144:147], v[40:43], v[64:67]\ns_lshl_b32 s53, s41, 7\ns_add_u32 s53, s53, s49\n"
"v_mfma_f32_16x16x32_bf16 v[68:71], v[148:151], v[40:43], v[68:71]\nds_read_b128 v[40:43], v32 offset:8192\nbuffer_load_dwordx4 v10, s[12:15], s53 offen lds\ns_waitcnt lgkmcnt(3)\n"
"v_mfma_f32_16x16x32_bf16 v[72:75], v[144:147], v[44:47], v[72:75]\ns_add_u32 m0, s52, 8192\nv_mfma_f32_16x16x32_bf16 v[76:79], v[148:151], v[44:47], v[76:79]\nds_read_b128 v[44:47], v33 offset:8192\n"
"s_lshl_b32 s53, s42, 7\ns_add_u32 s53, s53, s49\ns_waitcnt lgkmcnt(3)\nv_mfma_f32_16x16x32_bf16 v[64:67], v[152:155], v[48:51], v[64:67]\n"
"buffer_load_dwordx4 v10, s[12:15], s53 offen lds\nv_mfma_f32_16x16x32_bf16 v[68:71], v[156:159], v[48:51], v[68:71]\nds_read_b128 v[48:51], v34 offset:8192\ns_add_u32 m0, s52, 12288\n"
"s_waitcnt lgkmcnt(3)\nv_mfma_f32_16x16x32_bf16 v[72:75], v[152:155], v[52:55], v[72:75]\ns_lshl_b32 s53, s43, 7\ns_add_u32 s53, s53, s49\n"
"v_mfma_f32_16x16x32_bf16 v[76:79], v[156:159], v[52:55], v[76:79]\nds_read_b128 v[52:55], v35 offset:8192\nbuffer_load_dwordx4 v10, s[12:15], s53 offen lds\ns_waitcnt lgkmcnt(3)\n"
"v_mfma_f32_16x16x32_bf16 v[64:67], v[160:163], v[40:43], v[64:67]\ns_add_u32 m0, s52, 16384\nv_mfma_f32_16x16x32_bf16 v[68:71], v[164:167], v[40:43], v[68:71]\nds_read_b128 v[40:43], v32 offset:12288\n"
"s_lshl_b32 s53, s44, 7\ns_add_u32 s53, s53, s49\ns_waitcnt lgkmcnt(3)\nv_mfma_f32_16x16x32_bf16 v[72:75], v[160:163], v[44:47], v[72:75]\n"
"buffer_load_dwordx4 v10, s[12:15], s53 offen lds\nv_mfma_f32_16x16x32_bf16 v[76:79], v[164:167], v[44:47], v[76:79]\nds_read_b128 v[44:47], v33 offset:12288\ns_add_u32 m0, s52, 20480\n"
"s_waitcnt lgkmcnt(3)\nv_mfma_f32_16x16x32_bf16 v[64:67], v[168:171], v[48:51], v[64:67]\ns_lshl_b32 s53, s45, 7\ns_add_u32 s53, s53, s49\n"
"v_mfma_f32_16x16x32_bf16 v[68:71], v[172:175], v[48:51], v[68:71]\nds_read_b128 v[48:51], v34 offset:12288\nbuffer_load_dwordx4 v10, s[12:15], s53 offen lds\ns_waitcnt lgkmcnt(3)\n"
"v_mfma_f32_16x16x32_bf16 v[72:75], v[168:171], v[52:55], v[72:75]\ns_add_u32 m0, s52, 24576\nv_mfma_f32_16x16x32_bf16 v[76:79], v[172:175], v[52:55], v[76:79]\nds_read_b128 v[52:55], v35 offset:12288\n"
"s_lshl_b32 s53, s46, 7\ns_add_u32 s53, s53, s49\ns_waitcnt lgkmcnt(3)\nv_mfma_f32_16x16x32_bf16 v[64:67], v[176:179], v[40:43], v[64:67]\n"
"buffer_load_dwordx4 v10, s[12:15], s53 offen lds\nv_mfma_f32_16x16x32_bf16 v[68:71], v[180:183], v[40:43], v[68:71]\nds_read_b128 v[40:43], v32 offset:16384\ns_waitcnt lgkmcnt(3)\n"
"v_mfma_f32_16x16x32_bf16 v[72:75], v[176:179], v[44:47], v[72:75]\nv_mfma_f32_16x16x32_bf16 v[76:79], v[180:183], v[44:47], v[76:79]\nds_read_b128 v[44:47], v33 offset:16384\ns_waitcnt lgkmcnt(3)\n"
"v_mfma_f32_16x16x32_bf16 v[64:67], v[184:187], v[48:51], v[64:67]\nv_mfma_f32_16x16x32_bf16 v[68:71], v[188:191], v[48:51], v[68:71]\nds_read_b128 v[48:51], v34 offset:16384\ns_waitcnt lgkmcnt(3)\n"
"v_mfma_f32_16x16x32_bf16 v[72:75], v[184:187], v[52:55], v[72:75]\nv_mfma_f32_16x16x32_bf16 v[76:79], v[188:191], v[52:55], v[76:79]\nds_read_b128 v[52:55], v35 offset:16384\ns_waitcnt lgkmcnt(3)\n"
"v_mfma_f32_16x16x32_bf16 v[64:67], v[192:195], v[40:43], v[64:67]\nv_mfma_f32_16x16x32_bf16 v[68:71], v[196:199], v[40:43], v[68:71]\nds_read_b128 v[40:43], v32 offset:20480\ns_waitcnt lgkmcnt(3)\n"
"v_mfma_f32_16x16x32_bf16 v[72:75], v[192:195], v[44:47], v[72:75]\nv_mfma_f32_16x16x32_bf16 v[76:79], v[196:199], v[44:47], v[76:79]\nds_read_b128 v[44:47], v33 offset:20480\ns_waitcnt lgkmcnt(3)\n"
"v_mfma_f32_16x16x32_bf16 v[64:67], v[200:203], v[48:51], v[64:67]\nv_mfma_f32_16x16x32_bf16 v[68:71], v[204:207], v[48:51], v[68:71]\nds_read_b128 v[48:51], v34 offset:20480\ns_waitcnt lgkmcnt(3)\n"
"v_mfma_f32_16x16x32_bf16 v[72:75], v[200:203], v[52:55], v[72:75]\nv_mfma_f32_16x16x32_bf16 v[76:79], v[204:207], v[52:55], v[76:79]\nds_read_b128 v[52:55], v35 offset:20480\ns_waitcnt lgkmcnt(3)\n"
"v_mfma_f32_16x16x32_bf16 v[64:67], v[208:211], v[40:43], v[64:67]\nv_mfma_f32_16x16x32_bf16 v[68:71], v[212:215], v[40:43], v[68:71]\nds_read_b128 v[40:43], v32 offset:24576\ns_waitcnt lgkmcnt(3)\n"
"v_mfma_f32_16x16x32_bf16 v[72:75], v[208:211], v[44:47], v[72:75]\nv_mfma_f32_16x16x32_bf16 v[76:79], v[212:215], v[44:47], v[76:79]\nds_read_b128 v[44:47], v33 offset:24576\ns_waitcnt lgkmcnt(3)\n"
"v_mfma_f32_16x16x32_bf16 v[64:67], v[216:219], v[48:51], v[64:67]\ns_add_u32 s54, s51, 32768\ns_cmp_eq_u32 s54, 163840\nv_mfma_f32_16x16x32_bf16 v[68:71], v[220:223], v[48:51], v[68:71]\n"
"s_cselect_b32 s54, 0, s54\ns_sub_u32 s54, s54, s51\nds_read_b128 v[48:51], v34 offset:24576\ns_waitcnt lgkmcnt(3)\n"
"v_mfma_f32_16x16x32_bf16 v[72:75], v[216:219], v[52:55], v[72:75]\nv_mfma_f32_16x16x32_bf16 v[76:79], v[220:223], v[52:55], v[76:79]\nds_read_b128 v[52:55], v35 offset:24576\ns_waitcnt lgkmcnt(3)\n"
"v_mfma_f32_16x16x32_bf16 v[64:67], v[224:227], v[40:43], v[64:67]\nv_add_u32 v32, s54, v32\nv_mfma_f32_16x16x32_bf16 v[68:71], v[228:231], v[40:43], v[68:71]\nds_read_b128 v[40:43], v32 offset:0\n"
"s_waitcnt lgkmcnt(3)\nv_mfma_f32_16x16x32_bf16 v[72:75], v[224:227], v[44:47], v[72:75]\nv_add_u32 v33, s54, v33\nv_mfma_f32_16x16x32_bf16 v[76:79], v[228:231], v[44:47], v[76:79]\n"
"ds_read_b128 v[44:47], v33 offset:0\ns_waitcnt lgkmcnt(3)\nv_mfma_f32_16x16x32_bf16 v[64:67], v[232:235], v[48:51], v[64:67]\nv_add_u32 v34, s54, v34\n"
"v_mfma_f32_16x16x32_bf16 v[68:71], v[236:239], v[48:51], v[68:71]\nds_read_b128 v[48:51], v34 offset:0\ns_waitcnt lgkmcnt(3)\nv_mfma_f32_16x16x32_bf16 v[72:75], v[232:235], v[52:55], v[72:75]\n"
"v_add_u32 v35, s54, v35\nv_mfma_f32_16x16x32_bf16 v[76:79], v[236:239], v[52:55], v[76:79]\nds_read_b128 v[52:55], v35 offset:0\ns_add_u32 s52, s51, s32\n"
"s_add_u32 s51, s51, s54\ns_add_u32 s49, s49, 28672\ns_waitcnt vmcnt(8)\ns_barrier\n"
"s_waitcnt lgkmcnt(3)\nv_mfma_f32_16x16x32_bf16 v[96:99], v[128:131], v[40:43], 0\nv_mov_b32 v36, s48\ns_add_u32 s48, s48, 1\n"
"s_nop 0\nbuffer_store_dword v36, v19, s[24:27], 0 offen sc1\nv_mfma_f32_16x16x32_bf16 v[100:103], v[132:135], v[40:43], 0\nds_read_b128 v[40:43], v32 offset:4096\n"
"s_waitcnt lgkmcnt(3)\nv_mfma_f32_16x16x32_bf16 v[104:107], v[128:131], v[44:47], 0\nv_mfma_f32_16x16x32_bf16 v[108:111], v[132:135], v[44:47], 0\nds_read_b128 v[44:47], v33 offset:4096\n"
"v_cvt_pk_bf16_f32 v56, v64, v65\ns_waitcnt lgkmcnt(3)\nv_mfma_f32_16x16x32_bf16 v[96:99], v[136:139], v[48:51], v[96:99]\nv_cvt_pk_bf16_f32 v57, v66, v67\n"
"v_mfma_f32_16x16x32_bf16 v[100:103], v[140:143], v[48:51], v[100:103]\nds_read_b128 v[48:51], v34 offset:4096\nv_cvt_pk_bf16_f32 v58, v68, v69\ns_waitcnt lgkmcnt(3)\n"
"v_mfma_f32_16x16x32_bf16 v[104:107], v[136:139], v[52:55], v[104:107]\nv_cvt_pk_bf16_f32 v59, v70, v71\nv_mfma_f32_16x16x32_bf16 v[108:111], v[140:143], v[52:55], v[108:111]\nds_read_b128 v[52:55], v35 offset:4096\n"
"s_waitcnt lgkmcnt(3)\nv_mfma_f32_16x16x32_bf16 v[96:99], v[144:147], v[40:43], v[96:99]\nv_mfma_f32_16x16x32_bf16 v[100:103], v[148:151], v[40:43], v[100:103]\nds_read_b128 v[40:43], v32 offset:8192\n"
"buffer_store_dwordx4 v[56:59], v15, s[20:23], s50 offen sc1\ns_waitcnt lgkmcnt(3)\nv_mfma_f32_16x16x32_bf16 v[104:107], v[144:147], v[44:47], v[104:107]\nv_cvt_pk_bf16_f32 v60, v72, v73\n"
"v_mfma_f32_16x16x32_bf16 v[108:111], v[148:151], v[44:47], v[108:111]\nds_read_b128 v[44:47], v33 offset:8192\nv_cvt_pk_bf16_f32 v61, v74, v75\ns_waitcnt lgkmcnt(3)\n"
"v_mfma_f32_16x16x32_bf16 v[96:99], v[152:155], v[48:51], v[96:99]\nv_cvt_pk_bf16_f32 v62, v76, v77\nv_mfma_f32_16x16x32_bf16 v[100:103], v[156:159], v[48:51], v[100:103]\nds_read_b128 v[48:51], v34 offset:8192\n"
"v_cvt_pk_bf16_f32 v63, v78, v79\ns_waitcnt lgkmcnt(3)\nv_mfma_f32_16x16x32_bf16 v[104:107], v[152:155], v[52:55], v[104:107]\nv_mfma_f32_16x16x32_bf16 v[108:111], v[156:159], v[52:55], v[108:111]\n"
"ds_read_b128 v[52:55], v35 offset:8192\ns_waitcnt lgkmcnt(3)\nv_mfma_f32_16x16x32_bf16 v[96:99], v[160:163], v[40:43], v[96:99]\nbuffer_store_dwordx4 v[60:63], v16, s[20:23], s50 offen sc1\n"
"v_mfma_f32_16x16x32_bf16 v[100:103], v[164:167], v[40:43], v[100:103]\nds_read_b128 v[40:43], v32 offset:12288\ns_add_u32 s50, s50, s36\ns_waitcnt lgkmcnt(3)\n"
"v_mfma_f32_16x16x32_bf16 v[104:107], v[160:163], v[44:47], v[104:107]\ns_add_u32 m0, s52, 0\nv_mfma_f32_16x16x32_bf16 v[108:111], v[164:167], v[44:47], v[108:111]\nds_read_b128 v[44:47], v33 offset:12288\n"
"s_lshl_b32 s53, s40, 7\ns_add_u32 s53, s53, s49\ns_waitcnt lgkmcnt(3)\nv_mfma_f32_16x16x32_bf16 v[96:99], v[168:171], v[48:51], v[96:99]\n"
"buffer_load_dwordx4 v10, s[12:15], s53 offen lds\nv_mfma_f32_16x16x32_bf16 v[100:103], v[172:175], v[48:51], v[100:103]\nds_read_b128 v[48:51], v34 offset:12288\ns_add_u32 m0, s52, 4096\n"
"s_waitcnt lgkmcnt(3)\nv_mfma_f32_16x16x32_bf16 v[104:107], v[168:171], v[52:55], v[104:107]\ns_lshl_b32 s53, s41, 7\ns_add_u32 s53, s53, s49\n"
"v_mfma_f32_16x16x32_bf16 v[108:111], v[172:175], v[52:55], v[108:111]\nds_read_b128 v[52:55], v35 offset:12288\nbuffer_load_dwordx4 v10, s[12:15], s53 offen lds\ns_waitcnt lgkmcnt(3)\n"
"v_mfma_f32_16x16x32_bf16 v[96:99], v[176:179], v[40:43], v[96:99]\ns_add_u32 m0, s52, 8192\nv_mfma_f32_16x16x32_bf16 v[100:103], v[180:183], v[40:43], v[100:103]\nds_read_b128 v[40:43], v32 offset:16384\n"
"s_lshl_b32 s53, s42, 7\ns_add_u32 s53, s53, s49\ns_waitcnt lgkmcnt(3)\nv_mfma_f32_16x16x32_bf16 v[104:107], v[176:179], v[44:47], v[104:107]\n"
"buffer_load_dwordx4 v10, s[12:15], s53 offen lds\nv_mfma_f32_16x16x32_bf16 v[108:111], v[180:183], v[44:47], v[108:111]\nds_read_b128 v[44:47], v33 offset:16384\ns_add_u32 m0, s52, 12288\n"
"s_waitcnt lgkmcnt(3)\nv_mfma_f32_16x16x32_bf16 v[96:99], v[184:187], v[48:51], v[96:99]\ns_lshl_b32 s53, s43, 7\ns_add_u32 s53, s53, s49\n"
"v_mfma_f32_16x16x32_bf16 v[100:103], v[188:191], v[48:51], v[100:103]\nds_read_b128 v[48:51], v34 offset:16384\nbuffer_load_dwordx4 v10, s[12:15], s53 offen lds\ns_waitcnt lgkmcnt(3)\n"
"v_mfma_f32_16x16x32_bf16 v[104:107], v[184:187], v[52:55], v[104:107]\ns_add_u32 m0, s52, 16384\nv_mfma_f32_16x16x32_bf16 v[108:111], v[188:191], v[52:55], v[108:111]\nds_read_b128 v[52:55], v35 offset:16384\n"
"s_lshl_b32 s53, s44, 7\ns_add_u32 s53, s53, s49\ns_waitcnt lgkmcnt(3)\nv_mfma_f32_16x16x32_bf16 v[96:99], v[192:195], v[40:43], v[96:99]\n"
"buffer_load_dwordx4 v10, s[12:15], s53 offen lds\nv_mfma_f32_16x16x32_bf16 v[100:103], v[196:199], v[40:43], v[100:103]\nds_read_b128 v[40:43], v32 offset:20480\ns_add_u32 m0, s52, 20480\n"
"s_waitcnt lgkmcnt(3)\nv_mfma_f32_16x16x32_bf16 v[104:107], v[192:195], v[44:47], v[104:107]\ns_lshl_b32 s53, s45, 7\ns_add_u32 s53, s53, s49\n"
"v_mfma_f32_16x16x32_bf16 v[108:111], v[196:199], v[44:47], v[108:111]\nds_read_b128 v[44:47], v33 offset:20480\nbuffer_load_dwordx4 v10, s[12:15], s53 offen lds\ns_waitcnt lgkmcnt(3)\n"
"v_mfma_f32_16x16x32_bf16 v[96:99], v[200:203], v[48:51], v[96:99]\ns_add_u32 m0, s52, 24576\nv_mfma_f32_16x16x32_bf16 v[100:103], v[204:207], v[48:51], v[100:103]\nds_read_b128 v[48:51], v34 offset:20480\n"
"s_lshl_b32 s53, s46, 7\ns_add_u32 s53, s53, s49\ns_waitcnt lgkmcnt(3)\nv_mfma_f32_16x16x32_bf16 v[104:107], v[200:203], v[52:55], v[104:107]\n"
"buffer_load_dwordx4 v10, s[12:15], s53 offen lds\nv_mfma_f32_16x16x32_bf16 v[108:111], v[204:207], v[52:55], v[108:111]\nds_read_b128 v[52:55], v35 offset:20480\ns_waitcnt lgkmcnt(3)\n"
"v_mfma_f32_16x16x32_bf16 v[96:99], v[208:211], v[40:43], v[96:99]\nv_mfma_f32_16x16x32_bf16 v[100:103], v[212:215], v[40:43], v[100:103]\nds_read_b128 v[40:43], v32 offset:24576\ns_waitcnt lgkmcnt(3)\n"
"v_mfma_f32_16x16x32_bf16 v[104:107], v[208:211], v[44:47], v[104:107]\nv_mfma_f32_16x16x32_bf16 v[108:111], v[212:215], v[44:47], v[108:111]\nds_read_b128 v[44:47], v33 offset:24576\ns_waitcnt lgkmcnt(3)\n"
"v_mfma_f32_16x16x32_bf16 v[96:99], v[216:219], v[48:51], v[96:99]\ns_add_u32 s54, s51, 32768\ns_cmp_eq_u32 s54, 163840\nv_mfma_f32_16x16x32_bf16 v[100:103], v[220:223], v[48:51], v[100:103]\n"
"s_cselect_b32 s54, 0, s54\ns_sub_u32 s54, s54, s51\nds_read_b128 v[48:51], v34 offset:24576\ns_waitcnt lgkmcnt(3)\n"
"v_mfma_f32_16x16x32_bf16 v[104:107], v[216:219], v[52:55], v[104:107]\nv_mfma_f32_16x16x32_bf16 v[108:111], v[220:223], v[52:55], v[108:111]\nds_read_b128 v[52:55], v35 offset:24576\ns_waitcnt lgkmcnt(3)\n"
"v_mfma_f32_16x16x32_bf16 v[96:99], v[224:227], v[40:43], v[96:99]\nv_add_u32 v32, s54, v32\nv_mfma_f32_16x16x32_bf16 v[100:103], v[228:231], v[40:43], v[100:103]\nds_read_b128 v[40:43], v32 offset:0\n"
"s_waitcnt lgkmcnt(3)\nv_mfma_f32_16x16x32_bf16 v[104:107], v[224:227], v[44:47], v[104:107]\nv_add_u32 v33, s54, v33\nv_mfma_f32_16x16x32_bf16 v[108:111], v[228:231], v[44:47], v[108:111]\n"
"ds_read_b128 v[44:47], v33 offset:0\ns_waitcnt lgkmcnt(3)\nv_mfma_f32_16x16x32_bf16 v[96:99], v[232:235], v[48:51], v[96:99]\nv_add_u32 v34, s54, v34\n"
"v_mfma_f32_16x16x32_bf16 v[100:103], v[236:239], v[48:51], v[100:103]\nds_read_b128 v[48:51], v34 offset:0\ns_waitcnt lgkmcnt(3)\nv_mfma_f32_16x16x32_bf16 v[104:107], v[232:235], v[52:55], v[104:107]\n"
"v_add_u32 v35, s54, v35\nv_mfma_f32_16x16x32_bf16 v[108:111], v[236:239], v[52:55], v[108:111]\nds_read_b128 v[52:55], v35 offset:0\ns_add_u32 s52, s51, s32\n"
"s_add_u32 s51, s51, s54\ns_add_u32 s49, s49, 28672\ns_waitcnt vmcnt(7)\ns_barrier\n"
"s_waitcnt lgkmcnt(3)\nv_mfma_f32_16x16x32_bf16 v[64:67], v[128:131], v[40:43], 0\nv_mov_b32 v36, s48\ns_add_u32 s48, s48, 1\n"
"s_nop 0\nbuffer_store_dword v36, v19, s[24:27], s33 offen sc1\nbuffer_load_dword v37, v20, s[24:27], 0 offen sc1\nv_mfma_f32_16x16x32_bf16 v[68:71], v[132:135], v[40:43], 0\n"
"ds_read_b128 v[40:43], v32 offset:4096\ns_waitcnt lgkmcnt(3)\nv_mfma_f32_16x16x32_bf16 v[72:75], v[128:131], v[44:47], 0\nv_mfma_f32_16x16x32_bf16 v[76:79], v[132:135], v[44:47], 0\n"
"ds_read_b128 v[44:47], v33 offset:4096\nv_cvt_pk_bf16_f32 v56, v96, v97\ns_waitcnt lgkmcnt(3)\nv_mfma_f32_16x16x32_bf16 v[64:67], v[136:139], v[48:51], v[64:67]\n"
"v_cvt_pk_bf16_f32 v57, v98, v99\nv_mfma_f32_16x16x32_bf16 v[68:71], v[140:143], v[48:51], v[68:71]\nds_read_b128 v[48:51], v34 offset:4096\nv_cvt_pk_bf16_f32 v58, v100, v101\n"
"s_waitcnt lgkmcnt(3)\nv_mfma_f32_16x16x32_bf16 v[72:75], v[136:139], v[52:55], v[72:75]\nv_cvt_pk_bf16_f32 v59, v102, v103\nv_mfma_f32_16x16x32_bf16 v[76:79], v[140:143], v[52:55], v[76:79]\n"
"ds_read_b128 v[52:55], v35 offset:4096\ns_waitcnt lgkmcnt(3)\nv_mfma_f32_16x16x32_bf16 v[64:67], v[144:147], v[40:43], v[64:67]\nv_mfma_f32_16x16x32_bf16 v[68:71], v[148:151], v[40:43], v[68:71]\n"
"ds_read_b128 v[40:43], v32 offset:8192\nbuffer_store_dwordx4 v[56:59], v15, s[20:23], s50 offen sc1\ns_waitcnt lgkmcnt(3)\nv_mfma_f32_16x16x32_bf16 v[72:75], v[144:147], v[44:47], v[72:75]\n"
"v_cvt_pk_bf16_f32 v60, v104, v105\nv_mfma_f32_16x16x32_bf16 v[76:79], v[148:151], v[44:47], v[76:79]\nds_read_b128 v[44:47], v33 offset:8192\nv_cvt_pk_bf16_f32 v61, v106, v107\n"
"s_waitcnt lgkmcnt(3)\nv_mfma_f32_16x16x32_bf16 v[64:67], v[152:155], v[48:51], v[64:67]\nv_cvt_pk_bf16_f32 v62, v108, v109\nv_mfma_f32_16x16x32_bf16 v[68:71], v[156:159], v[48:51], v[68:71]\n"
"ds_read_b128 v[48:51], v34 offset:8192\nv_cvt_pk_bf16_f32 v63, v110, v111\ns_waitcnt lgkmcnt(3)\nv_mfma_f32_16x16x32_bf16 v[72:75], v[152:155], v[52:55], v[72:75]\n"
"v_mfma_f32_16x16x32_bf16 v[76:79], v[156:159], v[52:55], v[76:79]\nds_read_b128 v[52:55], v35 offset:8192\ns_waitcnt lgkmcnt(3)\nv_mfma_f32_16x16x32_bf16 v[64:67], v[160:163], v[40:43], v[64:67]\n"
"buffer_store_dwordx4 v[60:63], v16, s[20:23], s50 offen sc1\nv_mfma_f32_16x16x32_bf16 v[68:71], v[164:167], v[40:43], v[68:71]\nds_read_b128 v[40:43], v32 offset:12288\ns_add_u32 s50, s50, s36\n"
"s_waitcnt lgkmcnt(3)\nv_mfma_f32_16x16x32_bf16 v[72:75], v[160:163], v[44:47], v[72:75]\ns_add_u32 m0, s52, 0\nv_mfma_f32_16x16x32_bf16 v[76:79], v[164:167], v[44:47], v[76:79]\n"
"ds_read_b128 v[44:47], v33 offset:12288\ns_lshl_b32 s53, s40, 7\ns_add_u32 s53, s53, s49\ns_waitcnt lgkmcnt(3)\n"
"v_mfma_f32_16x16x32_bf16 v[64:67], v[168:171], v[48:51], v[64:67]\nbuffer_load_dwordx4 v10, s[12:15], s53 offen lds\nv_mfma_f32_16x16x32_bf16 v[68:71], v[172:175], v[48:51], v[68:71]\nds_read_b128 v[48:51], v34 offset:12288\n"
"s_add_u32 m0, s52, 4096\ns_waitcnt lgkmcnt(3)\nv_mfma_f32_16x16x32_bf16 v[72:75], v[168:171], v[52:55], v[72:75]\ns_lshl_b32 s53, s41, 7\n"
"s_add_u32 s53, s53, s49\nv_mfma_f32_16x16x32_bf16 v[76:79], v[172:175], v[52:55], v[76:79]\nds_read_b128 v[52:55], v35 offset:12288\nbuffer_load_dwordx4 v10, s[12:15], s53 offen lds\n"
"s_waitcnt lgkmcnt(3)\nv_mfma_f32_16x16x32_bf16 v[64:67], v[176:179], v[40:43], v[64:67]\ns_add_u32 m0, s52, 8192\nv_mfma_f32_16x16x32_bf16 v[68:71], v[180:183], v[40:43], v[68:71]\n"
"ds_read_b128 v[40:43], v32 offset:16384\ns_lshl_b32 s53, s42, 7\ns_add_u32 s53, s53, s49\ns_waitcnt lgkmcnt(3)\n"
"v_mfma_f32_16x16x32_bf16 v[72:75], v[176:179], v[44:47], v[72:75]\nbuffer_load_dwordx4 v10, s[12:15], s53 offen lds\nv_mfma_f32_16x16x32_bf16 v[76:79], v[180:183], v[44:47], v[76:79]\nds_read_b128 v[44:47], v33 offset:16384\n"
"s_add_u32 m0, s52, 12288\ns_waitcnt lgkmcnt(3)\nv_mfma_f32_16x16x32_bf16 v[64:67], v[184:187], v[48:51], v[64:67]\ns_lshl_b32 s53, s43, 7\n"
"s_add_u32 s53, s53, s49\nv_mfma_f32_16x16x32_bf16 v[68:71], v[188:191], v[48:51], v[68:71]\nds_read_b128 v[48:51], v34 offset:16384\nbuffer_load_dwordx4 v10, s[12:15], s53 offen lds\n"
"s_waitcnt lgkmcnt(3)\nv_mfma_f32_16x16x32_bf16 v[72:75], v[184:187], v[52:55], v[72:75]\ns_add_u32 m0, s52, 16384\nv_mfma_f32_16x16x32_bf16 v[76:79], v[188:191], v[52:55], v[76:79]\n"
"ds_read_b128 v[52:55], v35 offset:16384\ns_lshl_b32 s53, s44, 7\ns_add_u32 s53, s53, s49\ns_waitcnt lgkmcnt(3)\n"
"v_mfma_f32_16x16x32_bf16 v[64:67], v[192:195], v[40:43], v[64:67]\nbuffer_load_dwordx4 v10, s[12:15], s53 offen lds\nv_mfma_f32_16x16x32_bf16 v[68:71], v[196:199], v[40:43], v[68:71]\nds_read_b128 v[40:43], v32 offset:20480\n"
"s_add_u32 m0, s52, 20480\ns_waitcnt lgkmcnt(3)\nv_mfma_f32_16x16x32_bf16 v[72:75], v[192:195], v[44:47], v[72:75]\ns_lshl_b32 s53, s45, 7\n"
"s_add_u32 s53, s53, s49\nv_mfma_f32_16x16x32_bf16 v[76:79], v[196:199], v[44:47], v[76:79]\nds_read_b128 v[44:47], v33 offset:20480\nbuffer_load_dwordx4 v10, s[12:15], s53 offen lds\n"
"s_waitcnt lgkmcnt(3)\nv_mfma_f32_16x16x32_bf16 v[64:67], v[200:203], v[48:51], v[64:67]\ns_add_u32 m0, s52, 24576\nv_mfma_f32_16x16x32_bf16 v[68:71], v[204:207], v[48:51], v[68:71]\n"
"ds_read_b128 v[48:51], v34 offset:20480\ns_lshl_b32 s53, s46, 7\ns_add_u32 s53, s53, s49\ns_waitcnt lgkmcnt(3)\n"
"v_mfma_f32_16x16x32_bf16 v[72:75], v[200:203], v[52:55], v[72:75]\nbuffer_load_dwordx4 v10, s[12:15], s53 offen lds\nv_mfma_f32_16x16x32_bf16 v[76:79], v[204:207], v[52:55], v[76:79]\nds_read_b128 v[52:55], v35 offset:20480\n"
"s_waitcnt lgkmcnt(3)\nv_mfma_f32_16x16x32_bf16 v[64:67], v[208:211], v[40:43], v[64:67]\nv_mfma_f32_16x16x32_bf16 v[68:71], v[212:215], v[40:43], v[68:71]\nds_read_b128 v[40:43], v32 offset:24576\n"
"s_waitcnt lgkmcnt(3)\nv_mfma_f32_16x16x32_bf16 v[72:75], v[208:211], v[44:47], v[72:75]\nv_mfma_f32_16x16x32_bf16 v[76:79], v[212:215], v[44:47], v[76:79]\nds_read_b128 v[44:47], v33 offset:24576\n"
"s_waitcnt lgkmcnt(3)\nv_mfma_f32_16x16x32_bf16 v[64:67], v[216:219], v[48:51], v[64:67]\ns_add_u32 s54, s51, 32768\ns_cmp_eq_u32 s54, 163840\n"
"v_mfma_f32_16x16x32_bf16 v[68:71], v[220:223], v[48:51], v[68:71]\ns_cselect_b32 s54, 0, s54\ns_sub_u32 s54, s54, s51\nds_read_b128 v[48:51], v34 offset:24576\n"
"s_waitcnt lgkmcnt(3)\nv_mfma_f32_16x16x32_bf16 v[72:75], v[216:219], v[52:55], v[72:75]\nv_mfma_f32_16x16x32_bf16 v[76:79], v[220:223], v[52:55], v[76:79]\nds_read_b128 v[52:55], v35 offset:24576\n"
"s_waitcnt lgkmcnt(3)\nv_mfma_f32_16x16x32_bf16 v[64:67], v[224:227], v[40:43], v[64:67]\nv_add_u32 v32, s54, v32\nv_mfma_f32_16x16x32_bf16 v[68:71], v[228:231], v[40:43], v[68:71]\n"
"ds_read_b128 v[40:43], v32 offset:0\ns_waitcnt lgkmcnt(3)\nv_mfma_f32_16x16x32_bf16 v[72:75], v[224:227], v[44:47], v[72:75]\nv_add_u32 v33, s54, v33\n"
"v_mfma_f32_16x16x32_bf16 v[76:79], v[228:231], v[44:47], v[76:79]\nds_read_b128 v[44:47], v33 offset:0\ns_waitcnt lgkmcnt(3)\nv_mfma_f32_16x16x32_bf16 v[64:67], v[232:235], v[48:51], v[64:67]\n"
"v_add_u32 v34, s54, v34\nv_mfma_f32_16x16x32_bf16 v[68:71], v[236:239], v[48:51], v[68:71]\nds_read_b128 v[48:51], v34 offset:0\ns_waitcnt lgkmcnt(3)\n"
"v_mfma_f32_16x16x32_bf16 v[72:75], v[232:235], v[52:55], v[72:75]\nv_add_u32 v35, s54, v35\nv_mfma_f32_16x16x32_bf16 v[76:79], v[236:239], v[52:55], v[76:79]\nds_read_b128 v[52:55], v35 offset:0\n"
"s_add_u32 s52, s51, s32\ns_add_u32 s51, s51, s54\ns_add_u32 s49, s49, 28672\ns_waitcnt vmcnt(7)\n"
"fg0_pf:\nv_cmp_ne_u32 vcc, s28, v37\ns_cbranch_vccz fg0_pfok\ns_add_u32 s55, s55, 1\n"
"s_cmp_gt_u32 s55, 262144\ns_cbranch_scc0 fg0_pf2\ns_endpgm\nfg0_pf2:\n"
"s_sleep 2\nbuffer_load_dword v37, v20, s[24:27], 0 offen sc1\ns_waitcnt vmcnt(0)\ns_branch fg0_pf\n"
"fg0_pfok:\ns_barrier\ns_waitcnt lgkmcnt(3)\nv_mfma_f32_16x16x32_bf16 v[96:99], v[128:131], v[40:43], 0\n"
"v_mov_b32 v36, s48\ns_add_u32 s48, s48, 1\ns_nop 0\nbuffer_store_dword v36, v19, s[24:27], s33 offen sc1\n"
"v_mfma_f32_16x16x32_bf16 v[100:103], v[132:135], v[40:43], 0\nds_read_b128 v[40:43], v32 offset:4096\ns_waitcnt lgkmcnt(3)\nv_mfma_f32_16x16x32_bf16 v[104:107], v[128:131], v[44:47], 0\n"
"v_mfma_f32_16x16x32_bf16 v[108:111], v[132:135], v[44:47], 0\nds_read_b128 v[44:47], v33 offset:4096\nv_cvt_pk_bf16_f32 v56, v64, v65\ns_waitcnt lgkmcnt(3)\n"
"v_mfma_f32_16x16x32_bf16 v[96:99], v[136:139], v[48:51], v[96:99]\nv_cvt_pk_bf16_f32 v57, v66, v67\nv_mfma_f32_16x16x32_bf16 v[100:103], v[140:143], v[48:51], v[100:103]\nds_read_b128 v[48:51], v34 offset:4096\n"
"v_cvt_pk_bf16_f32 v58, v68, v69\ns_waitcnt lgkmcnt(3)\nv_mfma_f32_16x16x32_bf16 v[104:107], v[136:139], v[52:55], v[104:107]\nv_cvt_pk_bf16_f32 v59, v70, v71\n"
"v_mfma_f32_16x16x32_bf16 v[108:111], v[140:143], v[52:55], v[108:111]\nds_read_b128 v[52:55], v35 offset:4096\ns_waitcnt lgkmcnt(3)\nv_mfma_f32_16x16x32_bf16 v[96:99], v[144:147], v[40:43], v[96:99]\n"
"v_mfma_f32_16x16x32_bf16 v[100:103], v[148:151], v[40:43], v[100:103]\nds_read_b128 v[40:43], v32 offset:8192\nbuffer_store_dwordx4 v[56:59], v15, s[20:23], s50 offen sc1\ns_waitcnt lgkmcnt(3)\n"
"v_mfma_f32_16x16x32_bf16 v[104:107], v[144:147], v[44:47], v[104:107]\nv_cvt_pk_bf16_f32 v60, v72, v73\nv_mfma_f32_16x16x32_bf16 v[108:111], v[148:151], v[44:47], v[108:111]\nds_read_b128 v[44:47], v33 offset:8192\n"
"v_cvt_pk_bf16_f32 v61, v74, v75\ns_waitcnt lgkmcnt(3)\nv_mfma_f32_16x16x32_bf16 v[96:99], v[152:155], v[48:51], v[96:99]\nv_cvt_pk_bf16_f32 v62, v76, v77\n"
"v_mfma_f32_16x16x32_bf16 v[100:103], v[156:159], v[48:51], v[100:103]\nds_read_b128 v[48:51], v34 offset:8192\nv_cvt_pk_bf16_f32 v63, v78, v79\ns_waitcnt lgkmcnt(3)\n"
"v_mfma_f32_16x16x32_bf16 v[104:107], v[152:155], v[52:55], v[104:107]\nv_mfma_f32_16x16x32_bf16 v[108:111], v[156:159], v[52:55], v[108:111]\nds_read_b128 v[52:55], v35 offset:8192\ns_waitcnt lgkmcnt(3)\n"
"v_mfma_f32_16x16x32_bf16 v[96:99], v[160:163], v[40:43], v[96:99]\nbuffer_store_dwordx4 v[60:63], v16, s[20:23], s50 offen sc1\nv_mfma_f32_16x16x32_bf16 v[100:103], v[164:167], v[40:43], v[100:103]\nds_read_b128 v[40:43], v32 offset:12288\n"
"s_add_u32 s50, s50, s36\ns_waitcnt lgkmcnt(3)\nv_mfma_f32_16x16x32_bf16 v[104:107], v[160:163], v[44:47], v[104:107]\ns_add_u32 m0, s52, 0\n"
"v_mfma_f32_16x16x32_bf16 v[108:111], v[164:167], v[44:47], v[108:111]\nds_read_b128 v[44:47], v33 offset:12288\ns_lshl_b32 s53, s40, 7\ns_add_u32 s53, s53, s49\n"
"s_waitcnt lgkmcnt(3)\nv_mfma_f32_16x16x32_bf16 v[96:99], v[168:171], v[48:51], v[96:99]\nbuffer_load_dwordx4 v10, s[12:15], s53 offen lds\nv_mfma_f32_16x16x32_bf16 v[100:103], v[172:175], v[48:51], v[100:103]\n"
"ds_read_b128 v[48:51], v34 offset:12288\ns_add_u32 m0, s52, 4096\ns_waitcnt lgkmcnt(3)\nv_mfma_f32_16x16x32_bf16 v[104:107], v[168:171], v[52:55], v[104:107]\n"
"s_lshl_b32 s53, s41, 7\ns_add_u32 s53, s53, s49\nv_mfma_f32_16x16x32_bf16 v[108:111], v[172:175], v[52:55], v[108:111]\nds_read_b128 v[52:55], v35 offset:12288\n"
"buffer_load_dwordx4 v10, s[12:15], s53 offen lds\ns_waitcnt lgkmcnt(3)\nv_mfma_f32_16x16x32_bf16 v[96:99], v[176:179], v[40:43], v[96:99]\ns_add_u32 m0, s52, 8192\n"
"v_mfma_f32_16x16x32_bf16 v[100:103], v[180:183], v[40:43], v[100:103]\nds_read_b128 v[40:43], v32 offset:16384\ns_lshl_b32 s53, s42, 7\ns_add_u32 s53, s53, s49\n"
"s_waitcnt lgkmcnt(3)\nv_mfma_f32_16x16x32_bf16 v[104:107], v[176:179], v[44:47], v[104:107]\nbuffer_load_dwordx4 v10, s[12:15], s53 offen lds\nv_mfma_f32_16x16x32_bf16 v[108:111], v[180:183], v[44:47], v[108:111]\n"
"ds_read_b128 v[44:47], v33 offset:16384\ns_add_u32 m0, s52, 12288\ns_waitcnt lgkmcnt(3)\nv_mfma_f32_16x16x32_bf16 v[96:99], v[184:187], v[48:51], v[96:99]\n"
"s_lshl_b32 s53, s43, 7\ns_add_u32 s53, s53, s49\nv_mfma_f32_16x16x32_bf16 v[100:103], v[188:191], v[48:51], v[100:103]\nds_read_b128 v[48:51], v34 offset:16384\n"
"buffer_load_dwordx4 v10, s[12:15], s53 offen lds\ns_waitcnt lgkmcnt(3)\nv_mfma_f32_16x16x32_bf16 v[104:107], v[184:187], v[52:55], v[104:107]\ns_add_u32 m0, s52, 16384\n"
"v_mfma_f32_16x16x32_bf16 v[108:111], v[188:191], v[52:55], v[108:111]\nds_read_b128 v[52:55], v35 offset:16384\ns_lshl_b32 s53, s44, 7\ns_add_u32 s53, s53, s49\n"
"s_waitcnt lgkmcnt(3)\nv_mfma_f32_16x16x32_bf16 v[96:99], v[192:195], v[40:43], v[96:99]\nbuffer_load_dwordx4 v10, s[12:15], s53 offen lds\nv_mfma_f32_16x16x32_bf16 v[100:103], v[196:199], v[40:43], v[100:103]\n"
"ds_read_b128 v[40:43], v32 offset:20480\ns_add_u32 m0, s52, 20480\ns_waitcnt lgkmcnt(3)\nv_mfma_f32_16x16x32_bf16 v[104:107], v[192:195], v[44:47], v[104:107]\n"
"s_lshl_b32 s53, s45, 7\ns_add_u32 s53, s53, s49\nv_mfma_f32_16x16x32_bf16 v[108:111], v[196:199], v[44:47], v[108:111]\nds_read_b128 v[44:47], v33 offset:20480\n"
"buffer_load_dwordx4 v10, s[12:15], s53 offen lds\ns_waitcnt lgkmcnt(3)\nv_mfma_f32_16x16x32_bf16 v[96:99], v[200:203], v[48:51], v[96:99]\ns_add_u32 m0, s52, 24576\n"
"v_mfma_f32_16x16x32_bf16 v[100:103], v[204:207], v[48:51], v[100:103]\nds_read_b128 v[48:51], v34 offset:20480\ns_lshl_b32 s53, s46, 7\ns_add_u32 s53, s53, s49\n"
"s_waitcnt lgkmcnt(3)\nv_mfma_f32_16x16x32_bf16 v[104:107], v[200:203], v[52:55], v[104:107]\nbuffer_load_dwordx4 v10, s[12:15], s53 offen lds\nv_mfma_f32_16x16x32_bf16 v[108:111], v[204:207], v[52:55], v[108:111]\n"
"ds_read_b128 v[52:55], v35 offset:20480\ns_lshl_b32 s53, s40, 11\ns_add_u32 s53, s53, 0\ns_waitcnt lgkmcnt(3)\n"
"v_mfma_f32_16x16x32_bf16 v[96:99], v[208:211], v[40:43], v[96:99]\nbuffer_load_dwordx4 a[0:3], v14, s[16:19], s53 offen sc1\nv_mfma_f32_16x16x32_bf16 v[100:103], v[212:215], v[40:43], v[100:103]\nds_read_b128 v[40:43], v32 offset:24576\n"
"buffer_load_dwordx4 a[4:7], v14, s[16:19], s53 offen offset:64 sc1\ns_waitcnt lgkmcnt(3)\nv_mfma_f32_16x16x32_bf16 v[104:107], v[208:211], v[44:47], v[104:107]\ns_lshl_b32 s53, s40, 11\n"
"s_add_u32 s53, s53, 1024\nv_mfma_f32_16x16x32_bf16 v[108:111], v[212:215], v[44:47], v[108:111]\nds_read_b128 v[44:47], v33 offset:24576\nbuffer_load_dwordx4 a[8:11], v14, s[16:19], s53 offen sc1\n"
"s_waitcnt lgkmcnt(3)\nv_mfma_f32_16x16x32_bf16 v[96:99], v[216:219], v[48:51], v[96:99]\ns_add_u32 s54, s51, 32768\ns_cmp_eq_u32 s54, 163840\n"
"v_mfma_f32_16x16x32_bf16 v[100:103], v[220:223], v[48:51], v[100:103]\ns_cselect_b32 s54, 0, s54\ns_sub_u32 s54, s54, s51\nds_read_b128 v[48:51], v34 offset:24576\n"
"buffer_load_dwordx4 a[12:15], v14, s[16:19], s53 offen offset:64 sc1\ns_waitcnt lgkmcnt(3)\nv_mfma_f32_16x16x32_bf16 v[104:107], v[216:219], v[52:55], v[104:107]\ns_lshl_b32 s53, s41, 11\n"
"s_add_u32 s53, s53, 0\nv_mfma_f32_16x16x32_bf16 v[108:111], v[220:223], v[52:55], v[108:111]\nds_read_b128 v[52:55], v35 offset:24576\nbuffer_load_dwordx4 a[16:19], v14, s[16:19], s53 offen sc1\n"
"s_waitcnt lgkmcnt(3)\nv_mfma_f32_16x16x32_bf16 v[96:99], v[224:227], v[40:43], v[96:99]\nv_add_u32 v32, s54, v32\nbuffer_load_dwordx4 a[20:23], v14, s[16:19], s53 offen offset:64 sc1\n"
"v_mfma_f32_16x16x32_bf16 v[100:103], v[228:231], v[40:43], v[100:103]\nds_read_b128 v[40:43], v32 offset:0\ns_lshl_b32 s53, s41, 11\ns_add_u32 s53, s53, 1024\n"
"s_waitcnt lgkmcnt(3)\nv_mfma_f32_16x16x32_bf16 v[104:107], v[224:227], v[44:47], v[104:107]\nv_add_u32 v33, s54, v33\nbuffer_load_dwordx4 a[24:27], v14, s[16:19], s53 offen sc1\n"
"v_mfma_f32_16x16x32_bf16 v[108:111], v[228:231], v[44:47], v[108:111]\nds_read_b128 v[44:47], v33 offset:0\nbuffer_load_dwordx4 a[28:31], v14, s[16:19], s53 offen offset:64 sc1\ns_waitcnt lgkmcnt(3)\n"
"v_mfma_f32_16x16x32_bf16 v[96:99], v[232:235], v[48:51], v[96:99]\nv_add_u32 v34, s54, v34\ns_lshl_b32 s53, s42, 11\ns_add_u32 s53, s53, 0\n"
"v_mfma_f32_16x16x32_bf16 v[100:103], v[236:239], v[48:51], v[100:103]\nds_read_b128 v[48:51], v34 offset:0\nbuffer_load_dwordx4 a[32:35], v14, s[16:19], s53 offen sc1\ns_waitcnt lgkmcnt(3)\n"
"v_mfma_f32_16x16x32_bf16 v[104:107], v[232:235], v[52:55], v[104:107]\nv_add_u32 v35, s54, v35\nbuffer_load_dwordx4 a[36:39], v14, s[16:19], s53 offen offset:64 sc1\nv_mfma_f32_16x16x32_bf16 v[108:111], v[236:239], v[52:55], v[108:111]\n"
"ds_read_b128 v[52:55], v35 offset:0\ns_add_u32 s52, s51, s32\ns_add_u32 s51, s51, s54\ns_add_u32 s49, s49, 28672\n"
"s_waitcnt vmcnt(17)\ns_barrier\ns_waitcnt lgkmcnt(3)\nv_mfma_f32_16x16x32_bf16 v[64:67], v[128:131], v[40:43], 0\n"
"v_mov_b32 v36, s48\ns_add_u32 s48, s48, 1\ns_nop 0\nbuffer_store_dword v36, v19, s[24:27], s33 offen sc1\n"
"v_mfma_f32_16x16x32_bf16 v[68:71], v[132:135], v[40:43], 0\nds_read_b128 v[40:43], v32 offset:4096\ns_waitcnt lgkmcnt(3)\nv_mfma_f32_16x16x32_bf16 v[72:75], v[128:131], v[44:47], 0\n"
"v_mfma_f32_16x16x32_bf16 v[76:79], v[132:135], v[44:47], 0\nds_read_b128 v[44:47], v33 offset:4096\nv_cvt_pk_bf16_f32 v56, v96, v97\ns_waitcnt lgkmcnt(3)\n"
"v_mfma_f32_16x16x32_bf16 v[64:67], v[136:139], v[48:51], v[64:67]\nv_cvt_pk_bf16_f32 v57, v98, v99\nv_mfma_f32_16x16x32_bf16 v[68:71], v[140:143], v[48:51], v[68:71]\nds_read_b128 v[48:51], v34 offset:4096\n"
"v_cvt_pk_bf16_f32 v58, v100, v101\ns_waitcnt lgkmcnt(3)\nv_mfma_f32_16x16x32_bf16 v[72:75], v[136:139], v[52:55], v[72:75]\nv_cvt_pk_bf16_f32 v59, v102, v103\n"
"v_mfma_f32_16x16x32_bf16 v[76:79], v[140:143], v[52:55], v[76:79]\nds_read_b128 v[52:55], v35 offset:4096\ns_waitcnt lgkmcnt(3)\nv_mfma_f32_16x16x32_bf16 v[64:67], v[144:147], v[40:43], v[64:67]\n"
"v_mfma_f32_16x16x32_bf16 v[68:71], v[148:151], v[40:43], v[68:71]\nds_read_b128 v[40:43], v32 offset:8192\nbuffer_store_dwordx4 v[56:59], v15, s[20:23], s50 offen sc1\ns_waitcnt lgkmcnt(3)\n"
"v_mfma_f32_16x16x32_bf16 v[72:75], v[144:147], v[44:47], v[72:75]\nv_cvt_pk_bf16_f32 v60, v104, v105\nv_mfma_f32_16x16x32_bf16 v[76:79], v[148:151], v[44:47], v[76:79]\nds_read_b128 v[44:47], v33 offset:8192\n"
"v_cvt_pk_bf16_f32 v61, v106, v107\ns_waitcnt lgkmcnt(3)\nv_mfma_f32_16x16x32_bf16 v[64:67], v[152:155], v[48:51], v[64:67]\nv_cvt_pk_bf16_f32 v62, v108, v109\n"
"v_mfma_f32_16x16x32_bf16 v[68:71], v[156:159], v[48:51], v[68:71]\nds_read_b128 v[48:51], v34 offset:8192\nv_cvt_pk_bf16_f32 v63, v110, v111\ns_waitcnt lgkmcnt(3)\n"
"v_mfma_f32_16x16x32_bf16 v[72:75], v[152:155], v[52:55], v[72:75]\nv_mfma_f32_16x16x32_bf16 v[76:79], v[156:159], v[52:55], v[76:79]\nds_read_b128 v[52:55], v35 offset:8192\ns_waitcnt lgkmcnt(3)\n"
"v_mfma_f32_16x16x32_bf16 v[64:67], v[160:163], v[40:43], v[64:67]\nbuffer_store_dwordx4 v[60:63], v16, s[20:23], s50 offen sc1\nv_mfma_f32_16x16x32_bf16 v[68:71], v[164:167], v[40:43], v[68:71]\nds_read_b128 v[40:43], v32 offset:12288\n"
"s_add_u32 s50, s50, s36\ns_waitcnt lgkmcnt(3)\nv_mfma_f32_16x16x32_bf16 v[72:75], v[160:163], v[44:47], v[72:75]\ns_add_u32 m0, s52, 0\n"
"v_mfma_f32_16x16x32_bf16 v[76:79], v[164:167], v[44:47], v[76:79]\nds_read_b128 v[44:47], v33 offset:12288\ns_lshl_b32 s53, s40, 7\ns_add_u32 s53, s53, s49\n"
"s_waitcnt lgkmcnt(3)\nv_mfma_f32_16x16x32_bf16 v[64:67], v[168:171], v[48:51], v[64:67]\nbuffer_load_dwordx4 v10, s[12:15], s53 offen lds\nv_mfma_f32_16x16x32_bf16 v[68:71], v[172:175], v[48:51], v[68:71]\n"
"ds_read_b128 v[48:51], v34 offset:12288\ns_add_u32 m0, s52, 4096\ns_waitcnt lgkmcnt(3)\nv_mfma_f32_16x16x32_bf16 v[72:75], v[168:171], v[52:55], v[72:75]\n"
"s_lshl_b32 s53, s41, 7\ns_add_u32 s53, s53, s49\nv_mfma_f32_16x16x32_bf16 v[76:79], v[172:175], v[52:55], v[76:79]\nds_read_b128 v[52:55], v35 offset:12288\n"
"buffer_load_dwordx4 v10, s[12:15], s53 offen lds\ns_waitcnt lgkmcnt(3)\nv_mfma_f32_16x16x32_bf16 v[64:67], v[176:179], v[40:43], v[64:67]\ns_add_u32 m0, s52, 8192\n"
"v_mfma_f32_16x16x32_bf16 v[68:71], v[180:183], v[40:43], v[68:71]\nds_read_b128 v[40:43], v32 offset:16384\ns_lshl_b32 s53, s42, 7\ns_add_u32 s53, s53, s49\n"
"s_waitcnt lgkmcnt(3)\nv_mfma_f32_16x16x32_bf16 v[72:75], v[176:179], v[44:47], v[72:75]\nbuffer_load_dwordx4 v10, s[12:15], s53 offen lds\nv_mfma_f32_16x16x32_bf16 v[76:79], v[180:183], v[44:47], v[76:79]\n"
"ds_read_b128 v[44:47], v33 offset:16384\ns_add_u32 m0, s52, 12288\ns_waitcnt lgkmcnt(3)\nv_mfma_f32_16x16x32_bf16 v[64:67], v[184:187], v[48:51], v[64:67]\n"
"s_lshl_b32 s53, s43, 7\ns_add_u32 s53, s53, s49\nv_mfma_f32_16x16x32_bf16 v[68:71], v[188:191], v[48:51], v[68:71]\nds_read_b128 v[48:51], v34 offset:16384\n"
"buffer_load_dwordx4 v10, s[12:15], s53 offen lds\ns_waitcnt lgkmcnt(3)\nv_mfma_f32_16x16x32_bf16 v[72:75], v[184:187], v[52:55], v[72:75]\ns_add_u32 m0, s52, 16384\n"
"v_mfma_f32_16x16x32_bf16 v[76:79], v[188:191], v[52:55], v[76:79]\nds_read_b128 v[52:55], v35 offset:16384\ns_lshl_b32 s53, s44, 7\ns_add_u32 s53, s53, s49\n"
"s_waitcnt lgkmcnt(3)\nv_mfma_f32_16x16x32_bf16 v[64:67], v[192:195], v[40:43], v[64:67]\nbuffer_load_dwordx4 v10, s[12:15], s53 offen lds\nv_mfma_f32_16x16x32_bf16 v[68:71], v[196:199], v[40:43], v[68:71]\n"
"ds_read_b128 v[40:43], v32 offset:20480\ns_add_u32 m0, s52, 20480\ns_waitcnt lgkmcnt(3)\nv_mfma_f32_16x16x32_bf16 v[72:75], v[192:195], v[44:47], v[72:75]\n"
"s_lshl_b32 s53, s45, 7\ns_add_u32 s53, s53, s49\nv_mfma_f32_16x16x32_bf16 v[76:79], v[196:199], v[44:47], v[76:79]\nds_read_b128 v[44:47], v33 offset:20480\n"
"buffer_load_dwordx4 v10, s[12:15], s53 offen lds\ns_waitcnt lgkmcnt(3)\nv_mfma_f32_16x16x32_bf16 v[64:67], v[200:203], v[48:51], v[64:67]\ns_add_u32 m0, s52, 24576\n"
"v_mfma_f32_16x16x32_bf16 v[68:71], v[204:207], v[48:51], v[68:71]\nds_read_b128 v[48:51], v34 offset:20480\ns_lshl_b32 s53, s46, 7\ns_add_u32 s53, s53, s49\n"
"s_waitcnt lgkmcnt(3)\nv_mfma_f32_16x16x32_bf16 v[72:75], v[200:203], v[52:55], v[72:75]\nbuffer_load_dwordx4 v10, s[12:15], s53 offen lds\nv_mfma_f32_16x16x32_bf16 v[76:79], v[204:207], v[52:55], v[76:79]\n"
"ds_read_b128 v[52:55], v35 offset:20480\ns_lshl_b32 s53, s42, 11\ns_add_u32 s53, s53, 1024\ns_waitcnt lgkmcnt(3)\n"
"v_mfma_f32_16x16x32_bf16 v[64:67], v[208:211], v[40:43], v[64:67]\nbuffer_load_dwordx4 a[40:43], v14, s[16:19], s53 offen sc1\nv_mfma_f32_16x16x32_bf16 v[68:71], v[212:215], v[40:43], v[68:71]\nds_read_b128 v[40:43], v32 offset:24576\n"
"buffer_load_dwordx4 a[44:47], v14, s[16:19], s53 offen offset:64 sc1\ns_waitcnt lgkmcnt(3)\nv_mfma_f32_16x16x32_bf16 v[72:75], v[208:211], v[44:47], v[72:75]\ns_lshl_b32 s53, s43, 11\n"
"s_add_u32 s53, s53, 0\nv_mfma_f32_16x16x32_bf16 v[76:79], v[212:215], v[44:47], v[76:79]\nds_read_b128 v[44:47], v33 offset:24576\nbuffer_load_dwordx4 a[48:51], v14, s[16:19], s53 offen sc1\n"
"s_waitcnt lgkmcnt(3)\nv_mfma_f32_16x16x32_bf16 v[64:67], v[216:219], v[48:51], v[64:67]\ns_add_u32 s54, s51, 32768\ns_cmp_eq_u32 s54, 163840\n"
"v_mfma_f32_16x16x32_bf16 v[68:71], v[220:223], v[48:51], v[68:71]\ns_cselect_b32 s54, 0, s54\ns_sub_u32 s54, s54, s51\nds_read_b128 v[48:51], v34 offset:24576\n"
"buffer_load_dwordx4 a[52:55], v14, s[16:19], s53 offen offset:64 sc1\ns_waitcnt lgkmcnt(3)\nv_mfma_f32_16x16x32_bf16 v[72:75], v[216:219], v[52:55], v[72:75]\ns_lshl_b32 s53, s43, 11\n"
"s_add_u32 s53, s53, 1024\nv_mfma_f32_16x16x32_bf16 v[76:79], v[220:223], v[52:55], v[76:79]\nds_read_b128 v[52:55], v35 offset:24576\nbuffer_load_dwordx4 a[56:59], v14, s[16:19], s53 offen sc1\n"
"s_waitcnt lgkmcnt(3)\nv_mfma_f32_16x16x32_bf16 v[64:67], v[224:227], v[40:43], v[64:67]\nv_add_u32 v32, s54, v32\nbuffer_load_dwordx4 a[60:63], v14, s[16:19], s53 offen offset:64 sc1\n"
"v_mfma_f32_16x16x32_bf16 v[68:71], v[228:231], v[40:43], v[68:71]\nds_read_b128 v[40:43], v32 offset:0\ns_lshl_b32 s53, s44, 11\ns_add_u32 s53, s53, 0\n"
"s_waitcnt lgkmcnt(3)\nv_mfma_f32_16x16x32_bf16 v[72:75], v[224:227], v[44:47], v[72:75]\nv_add_u32 v33, s54, v33\nbuffer_load_dwordx4 a[64:67], v14, s[16:19], s53 offen sc1\n"
"v_mfma_f32_16x16x32_bf16 v[76:79], v[228:231], v[44:47], v[76:79]\nds_read_b128 v[44:47], v33 offset:0\nbuffer_load_dwordx4 a[68:71], v14, s[16:19], s53 offen offset:64 sc1\ns_waitcnt lgkmcnt(3)\n"
"v_mfma_f32_16x16x32_bf16 v[64:67], v[232:235], v[48:51], v[64:67]\nv_add_u32 v34, s54, v34\ns_lshl_b32 s53, s44, 11\ns_add_u32 s53, s53, 1024\n"
"v_mfma_f32_16x16x32_bf16 v[68:71], v[236:239], v[48:51], v[68:71]\nds_read_b128 v[48:51], v34 offset:0\nbuffer_load_dwordx4 a[72:75], v14, s[16:19], s53 offen sc1\ns_waitcnt lgkmcnt(3)\n"
"v_mfma_f32_16x16x32_bf16 v[72:75], v[232:235], v[52:55], v[72:75]\nv_add_u32 v35, s54, v35\nbuffer_load_dwordx4 a[76:79], v14, s[16:19], s53 offen offset:64 sc1\nv_mfma_f32_16x16x32_bf16 v[76:79], v[236:239], v[52:55], v[76:79]\n"
"ds_read_b128 v[52:55], v35 offset:0\ns_add_u32 s52, s51, s32\ns_add_u32 s51, s51, s54\ns_add_u32 s49, s49, 28672\n"
"s_waitcnt vmcnt(17)\ns_barrier\ns_waitcnt lgkmcnt(3)\nv_mfma_f32_16x16x32_bf16 v[96:99], v[128:131], v[40:43], 0\n"
"v_mov_b32 v36, s48\ns_add_u32 s48, s48, 1\ns_nop 0\nbuffer_store_dword v36, v19, s[24:27], s33 offen sc1\n"
"v_mfma_f32_16x16x32_bf16 v[100:103], v[132:135], v[40:43], 0\nds_read_b128 v[40:43], v32 offset:4096\ns_waitcnt lgkmcnt(3)\nv_mfma_f32_16x16x32_bf16 v[104:107], v[128:131], v[44:47], 0\n"
"v_mfma_f32_16x16x32_bf16 v[108:111], v[132:135], v[44:47], 0\nds_read_b128 v[44:47], v33 offset:4096\nv_cvt_pk_bf16_f32 v56, v64, v65\ns_waitcnt lgkmcnt(3)\n"
"v_mfma_f32_16x16x32_bf16 v[96:99], v[136:139], v[48:51], v[96:99]\nv_cvt_pk_bf16_f32 v57, v66, v67\nv_mfma_f32_16x16x32_bf16 v[100:103], v[140:143], v[48:51], v[100:103]\nds_read_b128 v[48:51], v34 offset:4096\n"
"v_cvt_pk_bf16_f32 v58, v68, v69\ns_waitcnt lgkmcnt(3)\nv_mfma_f32_16x16x32_bf16 v[104:107], v[136:139], v[52:55], v[104:107]\nv_cvt_pk_bf16_f32 v59, v70, v71\n"
"v_mfma_f32_16x16x32_bf16 v[108:111], v[140:143], v[52:55], v[108:111]\nds_read_b128 v[52:55], v35 offset:4096\ns_waitcnt lgkmcnt(3)\nv_mfma_f32_16x16x32_bf16 v[96:99], v[144:147], v[40:43], v[96:99]\n"
"v_mfma_f32_16x16x32_bf16 v[100:103], v[148:151], v[40:43], v[100:103]\nds_read_b128 v[40:43], v32 offset:8192\nbuffer_store_dwordx4 v[56:59], v15, s[20:23], s50 offen sc1\ns_waitcnt lgkmcnt(3)\n"
"v_mfma_f32_16x16x32_bf16 v[104:107], v[144:147], v[44:47], v[104:107]\nv_cvt_pk_bf16_f32 v60, v72, v73\nv_mfma_f32_16x16x32_bf16 v[108:111], v[148:151], v[44:47], v[108:111]\nds_read_b128 v[44:47], v33 offset:8192\n"
"v_cvt_pk_bf16_f32 v61, v74, v75\ns_waitcnt lgkmcnt(3)\nv_mfma_f32_16x16x32_bf16 v[96:99], v[152:155], v[48:51], v[96:99]\nv_cvt_pk_bf16_f32 v62, v76, v77\n"
"v_mfma_f32_16x16x32_bf16 v[100:103], v[156:159], v[48:51], v[100:103]\nds_read_b128 v[48:51], v34 offset:8192\nv_cvt_pk_bf16_f32 v63, v78, v79\ns_waitcnt lgkmcnt(3)\n"
"v_mfma_f32_16x16x32_bf16 v[104:107], v[152:155], v[52:55], v[104:107]\nv_mfma_f32_16x16x32_bf16 v[108:111], v[156:159], v[52:55], v[108:111]\nds_read_b128 v[52:55], v35 offset:8192\ns_waitcnt lgkmcnt(3)\n"
"v_mfma_f32_16x16x32_bf16 v[96:99], v[160:163], v[40:43], v[96:99]\nbuffer_store_dwordx4 v[60:63], v16, s[20:23], s50 offen sc1\nv_mfma_f32_16x16x32_bf16 v[100:103], v[164:167], v[40:43], v[100:103]\nds_read_b128 v[40:43], v32 offset:12288\n"
"s_add_u32 s50, s50, s36\ns_waitcnt lgkmcnt(3)\nv_mfma_f32_16x16x32_bf16 v[104:107], v[160:163], v[44:47], v[104:107]\ns_add_u32 m0, s52, 0\n"
"v_mfma_f32_16x16x32_bf16 v[108:111], v[164:167], v[44:47], v[108:111]\nds_read_b128 v[44:47], v33 offset:12288\ns_lshl_b32 s53, s40, 7\ns_add_u32 s53, s53, s49\n"
"s_waitcnt lgkmcnt(3)\nv_mfma_f32_16x16x32_bf16 v[96:99], v[168:171], v[48:51], v[96:99]\nbuffer_load_dwordx4 v10, s[12:15], s53 offen lds\nv_mfma_f32_16x16x32_bf16 v[100:103], v[172:175], v[48:51], v[100:103]\n"
"ds_read_b128 v[48:51], v34 offset:12288\ns_add_u32 m0, s52, 4096\ns_waitcnt lgkmcnt(3)\nv_mfma_f32_16x16x32_bf16 v[104:107], v[168:171], v[52:55], v[104:107]\n"
"s_lshl_b32 s53, s41, 7\ns_add_u32 s53, s53, s49\nv_mfma_f32_16x16x32_bf16 v[108:111], v[172:175], v[52:55], v[108:111]\nds_read_b128 v[52:55], v35 offset:12288\n"
"buffer_load_dwordx4 v10, s[12:15], s53 offen lds\ns_waitcnt lgkmcnt(3)\nv_mfma_f32_16x16x32_bf16 v[96:99], v[176:179], v[40:43], v[96:99]\ns_add_u32 m0, s52, 8192\n"
"v_mfma_f32_16x16x32_bf16 v[100:103], v[180:183], v[40:43], v[100:103]\nds_read_b128 v[40:43], v32 offset:16384\ns_lshl_b32 s53, s42, 7\ns_add_u32 s53, s53, s49\n"
"s_waitcnt lgkmcnt(3)\nv_mfma_f32_16x16x32_bf16 v[104:107], v[176:179], v[44:47], v[104:107]\nbuffer_load_dwordx4 v10, s[12:15], s53 offen lds\nv_mfma_f32_16x16x32_bf16 v[108:111], v[180:183], v[44:47], v[108:111]\n"
"ds_read_b128 v[44:47], v33 offset:16384\ns_add_u32 m0, s52, 12288\ns_waitcnt lgkmcnt(3)\nv_mfma_f32_16x16x32_bf16 v[96:99], v[184:187], v[48:51], v[96:99]\n"
"s_lshl_b32 s53, s43, 7\ns_add_u32 s53, s53, s49\nv_mfma_f32_16x16x32_bf16 v[100:103], v[188:191], v[48:51], v[100:103]\nds_read_b128 v[48:51], v34 offset:16384\n"
"buffer_load_dwordx4 v10, s[12:15], s53 offen lds\ns_waitcnt lgkmcnt(3)\nv_mfma_f32_16x16x32_bf16 v[104:107], v[184:187], v[52:55], v[104:107]\ns_add_u32 m0, s52, 16384\n"
"v_mfma_f32_16x16x32_bf16 v[108:111], v[188:191], v[52:55], v[108:111]\nds_read_b128 v[52:55], v35 offset:16384\ns_lshl_b32 s53, s44, 7\ns_add_u32 s53, s53, s49\n"
"s_waitcnt lgkmcnt(3)\nv_mfma_f32_16x16x32_bf16 v[96:99], v[192:195], v[40:43], v[96:99]\nbuffer_load_dwordx4 v10, s[12:15], s53 offen lds\nv_mfma_f32_16x16x32_bf16 v[100:103], v[196:199], v[40:43], v[100:103]\n"
"ds_read_b128 v[40:43], v32 offset:20480\ns_add_u32 m0, s52, 20480\ns_waitcnt lgkmcnt(3)\nv_mfma_f32_16x16x32_bf16 v[104:107], v[192:195], v[44:47], v[104:107]\n"
"s_lshl_b32 s53, s45, 7\ns_add_u32 s53, s53, s49\nv_mfma_f32_16x16x32_bf16 v[108:111], v[196:199], v[44:47], v[108:111]\nds_read_b128 v[44:47], v33 offset:20480\n"
"buffer_load_dwordx4 v10, s[12:15], s53 offen lds\ns_waitcnt lgkmcnt(3)\nv_mfma_f32_16x16x32_bf16 v[96:99], v[200:203], v[48:51], v[96:99]\ns_add_u32 m0, s52, 24576\n"
"v_mfma_f32_16x16x32_bf16 v[100:103], v[204:207], v[48:51], v[100:103]\nds_read_b128 v[48:51], v34 offset:20480\ns_lshl_b32 s53, s46, 7\ns_add_u32 s53, s53, s49\n"
"s_waitcnt lgkmcnt(3)\nv_mfma_f32_16x16x32_bf16 v[104:107], v[200:203], v[52:55], v[104:107]\nbuffer_load_dwordx4 v10, s[12:15], s53 offen lds\nv_mfma_f32_16x16x32_bf16 v[108:111], v[204:207], v[52:55], v[108:111]\n"
"ds_read_b128 v[52:55], v35 offset:20480\ns_lshl_b32 s53, s45, 11\ns_add_u32 s53, s53, 0\ns_waitcnt lgkmcnt(3)\n"
"v_mfma_f32_16x16x32_bf16 v[96:99], v[208:211], v[40:43], v[96:99]\nbuffer_load_dwordx4 a[80:83], v14, s[16:19], s53 offen sc1\nv_mfma_f32_16x16x32_bf16 v[100:103], v[212:215], v[40:43], v[100:103]\nds_read_b128 v[40:43], v32 offset:24576\n"
"buffer_load_dwordx4 a[84:87], v14, s[16:19], s53 offen offset:64 sc1\ns_waitcnt lgkmcnt(3)\nv_mfma_f32_16x16x32_bf16 v[104:107], v[208:211], v[44:47], v[104:107]\ns_lshl_b32 s53, s45, 11\n"
"s_add_u32 s53, s53, 1024\nv_mfma_f32_16x16x32_bf16 v[108:111], v[212:215], v[44:47], v[108:111]\nds_read_b128 v[44:47], v33 offset:24576\nbuffer_load_dwordx4 a[88:91], v14, s[16:19], s53 offen sc1\n"
"s_waitcnt lgkmcnt(3)\nv_mfma_f32_16x16x32_bf16 v[96:99], v[216:219], v[48:51], v[96:99]\ns_add_u32 s54, s51, 32768\ns_cmp_eq_u32 s54, 163840\n"
"v_mfma_f32_16x16x32_bf16 v[100:103], v[220:223], v[48:51], v[100:103]\ns_cselect_b32 s54, 0, s54\ns_sub_u32 s54, s54, s51\nds_read_b128 v[48:51], v34 offset:24576\n"
"buffer_load_dwordx4 a[92:95], v14, s[16:19], s53 offen offset:64 sc1\ns_waitcnt lgkmcnt(3)\nv_mfma_f32_16x16x32_bf16 v[104:107], v[216:219], v[52:55], v[104:107]\ns_lshl_b32 s53, s46, 11\n"
"s_add_u32 s53, s53, 0\nv_mfma_f32_16x16x32_bf16 v[108:111], v[220:223], v[52:55], v[108:111]\nds_read_b128 v[52:55], v35 offset:24576\nbuffer_load_dwordx4 a[96:99], v14, s[16:19], s53 offen sc1\n"
"s_waitcnt lgkmcnt(3)\nv_mfma_f32_16x16x32_bf16 v[96:99], v[224:227], v[40:43], v[96:99]\nv_add_u32 v32, s54, v32\nbuffer_load_dwordx4 a[100:103], v14, s[16:19], s53 offen offset:64 sc1\n"
"v_mfma_f32_16x16x32_bf16 v[100:103], v[228:231], v[40:43], v[100:103]\nds_read_b128 v[40:43], v32 offset:0\ns_lshl_b32 s53, s46, 11\ns_add_u32 s53, s53, 1024\n"
"s_waitcnt lgkmcnt(3)\nv_mfma_f32_16x16x32_bf16 v[104:107], v[224:227], v[44:47], v[104:107]\nv_add_u32 v33, s54, v33\nbuffer_load_dwordx4 a[104:107], v14, s[16:19], s53 offen sc1\n"
"v_mfma_f32_16x16x32_bf16 v[108:111], v[228:231], v[44:47], v[108:111]\nds_read_b128 v[44:47], v33 offset:0\nbuffer_load_dwordx4 a[108:111], v14, s[16:19], s53 offen offset:64 sc1\ns_waitcnt lgkmcnt(3)\n"
"v_mfma_f32_16x16x32_bf16 v[96:99], v[232:235], v[48:51], v[96:99]\nv_add_u32 v34, s54, v34\nv_mfma_f32_16x16x32_bf16 v[100:103], v[236:239], v[48:51], v[100:103]\nds_read_b128 v[48:51], v34 offset:0\n"
"s_waitcnt lgkmcnt(3)\nv_mfma_f32_16x16x32_bf16 v[104:107], v[232:235], v[52:55], v[104:107]\nv_add_u32 v35, s54, v35\nv_mfma_f32_16x16x32_bf16 v[108:111], v[236:239], v[52:55], v[108:111]\n"
"ds_read_b128 v[52:55], v35 offset:0\ns_add_u32 s52, s51, s32\ns_add_u32 s51, s51, s54\ns_add_u32 s49, s49, 28672\n"
"s_cmp_lg_u32 s34, 0\ns_cbranch_scc1 fg0_stay\ns_nop 7\nv_cvt_pk_bf16_f32 v56, v96, v97\n"
"v_cvt_pk_bf16_f32 v57, v98, v99\nv_cvt_pk_bf16_f32 v58, v100, v101\nv_cvt_pk_bf16_f32 v59, v102, v103\ns_nop 0\n"
"s_nop 0\nbuffer_store_dwordx4 v[56:59], v15, s[20:23], s50 offen sc1\nv_cvt_pk_bf16_f32 v60, v104, v105\nv_cvt_pk_bf16_f32 v61, v106, v107\n"
"v_cvt_pk_bf16_f32 v62, v108, v109\nv_cvt_pk_bf16_f32 v63, v110, v111\ns_nop 0\ns_nop 0\n"
"buffer_store_dwordx4 v[60:63], v16, s[20:23], s50 offen sc1\ns_add_u32 s48, s28, 64\nv_mov_b32 v36, s48\ns_waitcnt vmcnt(0) lgkmcnt(0)\n"
"s_barrier\nbuffer_store_dword v36, v19, s[24:27], s33 offen sc1\ns_branch fg0_end\nfg0_stay:\n"
"s_waitcnt vmcnt(0)\ns_barrier\ns_waitcnt lgkmcnt(3)\nv_mfma_f32_16x16x32_bf16 v[64:67], v[128:131], v[40:43], 0\n"
"v_mov_b32 v36, s48\ns_add_u32 s48, s48, 1\ns_nop 0\nbuffer_store_dword v36, v19, s[24:27], s33 offen sc1\n"
"v_mfma_f32_16x16x32_bf16 v[68:71], v[132:135], v[40:43], 0\nv_mfma_f32_16x16x32_bf16 v[72:75], a[0:3], v[40:43], 0\nv_mfma_f32_16x16x32_bf16 v[76:79], a[4:7], v[40:43], 0\nds_read_b128 v[40:43], v32 offset:4096\n"
"v_cvt_pk_bf16_f32 v56, v96, v97\ns_waitcnt lgkmcnt(3)\nv_mfma_f32_16x16x32_bf16 v[80:83], v[128:131], v[44:47], 0\nv_cvt_pk_bf16_f32 v57, v98, v99\n"
"v_mfma_f32_16x16x32_bf16 v[84:87], v[132:135], v[44:47], 0\nv_cvt_pk_bf16_f32 v58, v100, v101\nv_mfma_f32_16x16x32_bf16 v[88:91], a[0:3], v[44:47], 0\nv_cvt_pk_bf16_f32 v59, v102, v103\n"
"v_mfma_f32_16x16x32_bf16 v[92:95], a[4:7], v[44:47], 0\nds_read_b128 v[44:47], v33 offset:4096\ns_waitcnt lgkmcnt(3)\nv_mfma_f32_16x16x32_bf16 v[64:67], v[136:139], v[48:51], v[64:67]\n"
"v_mfma_f32_16x16x32_bf16 v[68:71], v[140:143], v[48:51], v[68:71]\nbuffer_store_dwordx4 v[56:59], v15, s[20:23], s50 offen sc1\nv_mfma_f32_16x16x32_bf16 v[72:75], a[8:11], v[48:51], v[72:75]\nv_cvt_pk_bf16_f32 v60, v104, v105\n"
"v_mfma_f32_16x16x32_bf16 v[76:79], a[12:15], v[48:51], v[76:79]\nds_read_b128 v[48:51], v34 offset:4096\nv_cvt_pk_bf16_f32 v61, v106, v107\ns_waitcnt lgkmcnt(3)\n"
"v_mfma_f32_16x16x32_bf16 v[80:83], v[136:139], v[52:55], v[80:83]\nv_cvt_pk_bf16_f32 v62, v108, v109\nv_mfma_f32_16x16x32_bf16 v[84:87], v[140:143], v[52:55], v[84:87]\nv_cvt_pk_bf16_f32 v63, v110, v111\n"
"v_mfma_f32_16x16x32_bf16 v[88:91], a[8:11], v[52:55], v[88:91]\nv_mfma_f32_16x16x32_bf16 v[92:95], a[12:15], v[52:55], v[92:95]\nds_read_b128 v[52:55], v35 offset:4096\ns_waitcnt lgkmcnt(3)\n"
"v_mfma_f32_16x16x32_bf16 v[64:67], v[144:147], v[40:43], v[64:67]\nbuffer_store_dwordx4 v[60:63], v16, s[20:23], s50 offen sc1\nv_mfma_f32_16x16x32_bf16 v[68:71], v[148:151], v[40:43], v[68:71]\ns_add_u32 s50, s50, s36\n"
"v_mfma_f32_16x16x32_bf16 v[72:75], a[16:19], v[40:43], v[72:75]\ns_add_u32 m0, s52, 0\nv_mfma_f32_16x16x32_bf16 v[76:79], a[20:23], v[40:43], v[76:79]\nds_read_b128 v[40:43], v32 offset:8192\n"
"s_lshl_b32 s53, s40, 7\ns_add_u32 s53, s53, s49\ns_waitcnt lgkmcnt(3)\nv_mfma_f32_16x16x32_bf16 v[80:83], v[144:147], v[44:47], v[80:83]\n"
"buffer_load_dwordx4 v10, s[12:15], s53 offen lds\nv_mfma_f32_16x16x32_bf16 v[84:87], v[148:151], v[44:47], v[84:87]\nv_mfma_f32_16x16x32_bf16 v[88:91], a[16:19], v[44:47], v[88:91]\nv_mfma_f32_16x16x32_bf16 v[92:95], a[20:23], v[44:47], v[92:95]\n"
"ds_read_b128 v[44:47], v33 offset:8192\ns_waitcnt lgkmcnt(3)\nv_mfma_f32_16x16x32_bf16 v[64:67], v[152:155], v[48:51], v[64:67]\nv_mfma_f32_16x16x32_bf16 v[68:71], v[156:159], v[48:51], v[68:71]\n"
"v_mfma_f32_16x16x32_bf16 v[72:75], a[24:27], v[48:51], v[72:75]\nv_mfma_f32_16x16x32_bf16 v[76:79], a[28:31], v[48:51], v[76:79]\nds_read_b128 v[48:51], v34 offset:8192\ns_waitcnt lgkmcnt(3)\n"
"v_mfma_f32_16x16x32_bf16 v[80:83], v[152:155], v[52:55], v[80:83]\nv_mfma_f32_16x16x32_bf16 v[84:87], v[156:159], v[52:55], v[84:87]\ns_add_u32 m0, s52, 4096\nv_mfma_f32_16x16x32_bf16 v[88:91], a[24:27], v[52:55], v[88:91]\n"
"s_lshl_b32 s53, s41, 7\ns_add_u32 s53, s53, s49\nv_mfma_f32_16x16x32_bf16 v[92:95], a[28:31], v[52:55], v[92:95]\nds_read_b128 v[52:55], v35 offset:8192\n"
"buffer_load_dwordx4 v10, s[12:15], s53 offen lds\ns_waitcnt lgkmcnt(3)\nv_mfma_f32_16x16x32_bf16 v[64:67], v[160:163], v[40:43], v[64:67]\nv_mfma_f32_16x16x32_bf16 v[68:71], v[164:167], v[40:43], v[68:71]\n"
"v_mfma_f32_16x16x32_bf16 v[72:75], a[32:35], v[40:43], v[72:75]\nv_mfma_f32_16x16x32_bf16 v[76:79], a[36:39], v[40:43], v[76:79]\nds_read_b128 v[40:43], v32 offset:12288\ns_waitcnt lgkmcnt(3)\n"
"v_mfma_f32_16x16x32_bf16 v[80:83], v[160:163], v[44:47], v[80:83]\nv_mfma_f32_16x16x32_bf16 v[84:87], v[164:167], v[44:47], v[84:87]\nv_mfma_f32_16x16x32_bf16 v[88:91], a[32:35], v[44:47], v[88:91]\nv_mfma_f32_16x16x32_bf16 v[92:95], a[36:39], v[44:47], v[92:95]\n"
"ds_read_b128 v[44:47], v33 offset:12288\ns_waitcnt lgkmcnt(3)\nv_mfma_f32_16x16x32_bf16 v[64:67], v[168:171], v[48:51], v[64:67]\ns_add_u32 m0, s52, 8192\n"
"v_mfma_f32_16x16x32_bf16 v[68:71], v[172:175], v[48:51], v[68:71]\ns_lshl_b32 s53, s42, 7\ns_add_u32 s53, s53, s49\nv_mfma_f32_16x16x32_bf16 v[72:75], a[40:43], v[48:51], v[72:75]\n"
"buffer_load_dwordx4 v10, s[12:15], s53 offen lds\nv_mfma_f32_16x16x32_bf16 v[76:79], a[44:47], v[48:51], v[76:79]\nds_read_b128 v[48:51], v34 offset:12288\ns_waitcnt lgkmcnt(3)\n"
"v_mfma_f32_16x16x32_bf16 v[80:83], v[168:171], v[52:55], v[80:83]\nv_mfma_f32_16x16x32_bf16 v[84:87], v[172:175], v[52:55], v[84:87]\nv_mfma_f32_16x16x32_bf16 v[88:91], a[40:43], v[52:55], v[88:91]\nv_mfma_f32_16x16x32_bf16 v[92:95], a[44:47], v[52:55], v[92:95]\n"
"ds_read_b128 v[52:55], v35 offset:12288\ns_waitcnt lgkmcnt(3)\nv_mfma_f32_16x16x32_bf16 v[64:67], v[176:179], v[40:43], v[64:67]\nv_mfma_f32_16x16x32_bf16 v[68:71], v[180:183], v[40:43], v[68:71]\n"
"v_mfma_f32_16x16x32_bf16 v[72:75], a[48:51], v[40:43], v[72:75]\nv_mfma_f32_16x16x32_bf16 v[76:79], a[52:55], v[40:43], v[76:79]\nds_read_b128 v[40:43], v32 offset:16384\ns_add_u32 m0, s52, 12288\n"
"s_waitcnt lgkmcnt(3)\nv_mfma_f32_16x16x32_bf16 v[80:83], v[176:179], v[44:47], v[80:83]\ns_lshl_b32 s53, s43, 7\ns_add_u32 s53, s53, s49\n"
"v_mfma_f32_16x16x32_bf16 v[84:87], v[180:183], v[44:47], v[84:87]\nbuffer_load_dwordx4 v10, s[12:15], s53 offen lds\nv_mfma_f32_16x16x32_bf16 v[88:91], a[48:51], v[44:47], v[88:91]\nv_mfma_f32_16x16x32_bf16 v[92:95], a[52:55], v[44:47], v[92:95]\n"
"ds_read_b128 v[44:47], v33 offset:16384\ns_waitcnt lgkmcnt(3)\nv_mfma_f32_16x16x32_bf16 v[64:67], v[184:187], v[48:51], v[64:67]\nv_mfma_f32_16x16x32_bf16 v[68:71], v[188:191], v[48:51], v[68:71]\n"
"v_mfma_f32_16x16x32_bf16 v[72:75], a[56:59], v[48:51], v[72:75]\nv_mfma_f32_16x16x32_bf16 v[76:79], a[60:63], v[48:51], v[76:79]\nds_read_b128 v[48:51], v34 offset:16384\ns_waitcnt lgkmcnt(3)\n"
"v_mfma_f32_16x16x32_bf16 v[80:83], v[184:187], v[52:55], v[80:83]\nv_mfma_f32_16x16x32_bf16 v[84:87], v[188:191], v[52:55], v[84:87]\nv_mfma_f32_16x16x32_bf16 v[88:91], a[56:59], v[52:55], v[88:91]\ns_add_u32 m0, s52, 16384\n"
"v_mfma_f32_16x16x32_bf16 v[92:95], a[60:63], v[52:55], v[92:95]\nds_read_b128 v[52:55], v35 offset:16384\ns_lshl_b32 s53, s44, 7\ns_add_u32 s53, s53, s49\n"
"s_waitcnt lgkmcnt(3)\nv_mfma_f32_16x16x32_bf16 v[64:67], v[192:195], v[40:43], v[64:67]\nbuffer_load_dwordx4 v10, s[12:15], s53 offen lds\nv_mfma_f32_16x16x32_bf16 v[68:71], v[196:199], v[40:43], v[68:71]\n"
"v_mfma_f32_16x16x32_bf16 v[72:75], a[64:67], v[40:43], v[72:75]\nv_mfma_f32_16x16x32_bf16 v[76:79], a[68:71], v[40:43], v[76:79]\nds_read_b128 v[40:43], v32 offset:20480\ns_waitcnt lgkmcnt(3)\n"
"v_mfma_f32_16x16x32_bf16 v[80:83], v[192:195], v[44:47], v[80:83]\nv_mfma_f32_16x16x32_bf16 v[84:87], v[196:199], v[44:47], v[84:87]\nv_mfma_f32_16x16x32_bf16 v[88:91], a[64:67], v[44:47], v[88:91]\nv_mfma_f32_16x16x32_bf16 v[92:95], a[68:71], v[44:47], v[92:95]\n"
"ds_read_b128 v[44:47], v33 offset:20480\ns_waitcnt lgkmcnt(3)\nv_mfma_f32_16x16x32_bf16 v[64:67], v[200:203], v[48:51], v[64:67]\nv_mfma_f32_16x16x32_bf16 v[68:71], v[204:207], v[48:51], v[68:71]\n"
"s_add_u32 m0, s52, 20480\nv_mfma_f32_16x16x32_bf16 v[72:75], a[72:75], v[48:51], v[72:75]\ns_lshl_b32 s53, s45, 7\ns_add_u32 s53, s53, s49\n"
"v_mfma_f32_16x16x32_bf16 v[76:79], a[76:79], v[48:51], v[76:79]\nds_read_b128 v[48:51], v34 offset:20480\nbuffer_load_dwordx4 v10, s[12:15], s53 offen lds\ns_waitcnt lgkmcnt(3)\n"
"v_mfma_f32_16x16x32_bf16 v[80:83], v[200:203], v[52:55], v[80:83]\nv_mfma_f32_16x16x32_bf16 v[84:87], v[204:207], v[52:55], v[84:87]\nv_mfma_f32_16x16x32_bf16 v[88:91], a[72:75], v[52:55], v[88:91]\nv_mfma_f32_16x16x32_bf16 v[92:95], a[76:79], v[52:55], v[92:95]\n"
"ds_read_b128 v[52:55], v35 offset:20480\ns_waitcnt lgkmcnt(3)\nv_mfma_f32_16x16x32_bf16 v[64:67], v[208:211], v[40:43], v[64:67]\nv_mfma_f32_16x16x32_bf16 v[68:71], v[212:215], v[40:43], v[68:71]\n"
"v_mfma_f32_16x16x32_bf16 v[72:75], a[80:83], v[40:43], v[72:75]\nv_mfma_f32_16x16x32_bf16 v[76:79], a[84:87], v[40:43], v[76:79]\nds_read_b128 v[40:43], v32 offset:24576\ns_waitcnt lgkmcnt(3)\n"
"v_mfma_f32_16x16x32_bf16 v[80:83], v[208:211], v[44:47], v[80:83]\ns_add_u32 m0, s52, 24576\nv_mfma_f32_16x16x32_bf16 v[84:87], v[212:215], v[44:47], v[84:87]\ns_lshl_b32 s53, s46, 7\n"
"s_add_u32 s53, s53, s49\nv_mfma_f32_16x16x32_bf16 v[88:91], a[80:83], v[44:47], v[88:91]\nbuffer_load_dwordx4 v10, s[12:15], s53 offen lds\nv_mfma_f32_16x16x32_bf16 v[92:95], a[84:87], v[44:47], v[92:95]\n"
"ds_read_b128 v[44:47], v33 offset:24576\ns_waitcnt lgkmcnt(3)\nv_mfma_f32_16x16x32_bf16 v[64:67], v[216:219], v[48:51], v[64:67]\ns_add_u32 s54, s51, 32768\n"
"s_cmp_eq_u32 s54, 163840\nv_mfma_f32_16x16x32_bf16 v[68:71], v[220:223], v[48:51], v[68:71]\nv_mfma_f32_16x16x32_bf16 v[72:75], a[88:91], v[48:51], v[72:75]\nv_mfma_f32_16x16x32_bf16 v[76:79], a[92:95], v[48:51], v[76:79]\n"
"s_cselect_b32 s54, 0, s54\ns_sub_u32 s54, s54, s51\nds_read_b128 v[48:51], v34 offset:24576\ns_waitcnt lgkmcnt(3)\n"
"v_mfma_f32_16x16x32_bf16 v[80:83], v[216:219], v[52:55], v[80:83]\nv_mfma_f32_16x16x32_bf16 v[84:87], v[220:223], v[52:55], v[84:87]\nv_mfma_f32_16x16x32_bf16 v[88:91], a[88:91], v[52:55], v[88:91]\nv_mfma_f32_16x16x32_bf16 v[92:95], a[92:95], v[52:55], v[92:95]\n"
"ds_read_b128 v[52:55], v35 offset:24576\ns_waitcnt lgkmcnt(3)\nv_mfma_f32_16x16x32_bf16 v[64:67], v[224:227], v[40:43], v[64:67]\nv_add_u32 v32, s54, v32\n"
"v_mfma_f32_16x16x32_bf16 v[68:71], v[228:231], v[40:43], v[68:71]\nv_mfma_f32_16x16x32_bf16 v[72:75], a[96:99], v[40:43], v[72:75]\nv_mfma_f32_16x16x32_bf16 v[76:79], a[100:103], v[40:43], v[76:79]\nds_read_b128 v[40:43], v32 offset:0\n"
"s_waitcnt lgkmcnt(3)\nv_mfma_f32_16x16x32_bf16 v[80:83], v[224:227], v[44:47], v[80:83]\nv_add_u32 v33, s54, v33\nv_mfma_f32_16x16x32_bf16 v[84:87], v[228:231], v[44:47], v[84:87]\n"
"v_mfma_f32_16x16x32_bf16 v[88:91], a[96:99], v[44:47], v[88:91]\nv_mfma_f32_16x16x32_bf16 v[92:95], a[100:103], v[44:47], v[92:95]\nds_read_b128 v[44:47], v33 offset:0\ns_waitcnt lgkmcnt(3)\n"
"v_mfma_f32_16x16x32_bf16 v[64:67], v[232:235], v[48:51], v[64:67]\nv_add_u32 v34, s54, v34\nv_mfma_f32_16x16x32_bf16 v[68:71], v[236:239], v[48:51], v[68:71]\nv_mfma_f32_16x16x32_bf16 v[72:75], a[104:107], v[48:51], v[72:75]\n"
"v_mfma_f32_16x16x32_bf16 v[76:79], a[108:111], v[48:51], v[76:79]\nds_read_b128 v[48:51], v34 offset:0\ns_waitcnt lgkmcnt(3)\nv_mfma_f32_16x16x32_bf16 v[80:83], v[232:235], v[52:55], v[80:83]\n"
"v_add_u32 v35, s54, v35\nv_mfma_f32_16x16x32_bf16 v[84:87], v[236:239], v[52:55], v[84:87]\nv_mfma_f32_16x16x32_bf16 v[88:91], a[104:107], v[52:55], v[88:91]\nv_mfma_f32_16x16x32_bf16 v[92:95], a[108:111], v[52:55], v[92:95]\n"
"ds_read_b128 v[52:55], v35 offset:0\ns_add_u32 s52, s51, s32\ns_add_u32 s51, s51, s54\ns_add_u32 s49, s49, 28672\n"
"s_waitcnt vmcnt(7)\ns_barrier\ns_waitcnt lgkmcnt(3)\nv_mfma_f32_16x16x32_bf16 v[96:99], v[128:131], v[40:43], 0\n"
"v_mov_b32 v36, s48\ns_add_u32 s48, s48, 1\ns_nop 0\nbuffer_store_dword v36, v19, s[24:27], s33 offen sc1\n"
"v_mfma_f32_16x16x32_bf16 v[100:103], v[132:135], v[40:43], 0\nv_mfma_f32_16x16x32_bf16 v[104:107], a[0:3], v[40:43], 0\nv_mfma_f32_16x16x32_bf16 v[108:111], a[4:7], v[40:43], 0\nds_read_b128 v[40:43], v32 offset:4096\n"
"v_cvt_pk_bf16_f32 v56, v64, v65\ns_waitcnt lgkmcnt(3)\nv_mfma_f32_16x16x32_bf16 v[112:115], v[128:131], v[44:47], 0\nv_cvt_pk_bf16_f32 v57, v66, v67\n"
"v_mfma_f32_16x16x32_bf16 v[116:119], v[132:135], v[44:47], 0\nv_cvt_pk_bf16_f32 v58, v68, v69\nv_mfma_f32_16x16x32_bf16 v[120:123], a[0:3], v[44:47], 0\nv_cvt_pk_bf16_f32 v59, v70, v71\n"
"v_mfma_f32_16x16x32_bf16 v[124:127], a[4:7], v[44:47], 0\nds_read_b128 v[44:47], v33 offset:4096\ns_waitcnt lgkmcnt(3)\nv_mfma_f32_16x16x32_bf16 v[96:99], v[136:139], v[48:51], v[96:99]\n"
"v_mfma_f32_16x16x32_bf16 v[100:103], v[140:143], v[48:51], v[100:103]\nbuffer_store_dwordx4 v[56:59], v15, s[20:23], s50 offen sc1\nv_mfma_f32_16x16x32_bf16 v[104:107], a[8:11], v[48:51], v[104:107]\nv_cvt_pk_bf16_f32 v60, v80, v81\n"
"v_mfma_f32_16x16x32_bf16 v[108:111], a[12:15], v[48:51], v[108:111]\nds_read_b128 v[48:51], v34 offset:4096\nv_cvt_pk_bf16_f32 v61, v82, v83\ns_waitcnt lgkmcnt(3)\n"
"v_mfma_f32_16x16x32_bf16 v[112:115], v[136:139], v[52:55], v[112:115]\nv_cvt_pk_bf16_f32 v62, v84, v85\nv_mfma_f32_16x16x32_bf16 v[116:119], v[140:143], v[52:55], v[116:119]\nv_cvt_pk_bf16_f32 v63, v86, v87\n"
"v_mfma_f32_16x16x32_bf16 v[120:123], a[8:11], v[52:55], v[120:123]\nv_mfma_f32_16x16x32_bf16 v[124:127], a[12:15], v[52:55], v[124:127]\nds_read_b128 v[52:55], v35 offset:4096\ns_waitcnt lgkmcnt(3)\n"
"v_mfma_f32_16x16x32_bf16 v[96:99], v[144:147], v[40:43], v[96:99]\nbuffer_store_dwordx4 v[60:63], v16, s[20:23], s50 offen sc1\nv_mfma_f32_16x16x32_bf16 v[100:103], v[148:151], v[40:43], v[100:103]\nv_cvt_pk_bf16_f32 v56, v72, v73\n"
"v_mfma_f32_16x16x32_bf16 v[104:107], a[16:19], v[40:43], v[104:107]\nv_cvt_pk_bf16_f32 v57, v74, v75\nv_mfma_f32_16x16x32_bf16 v[108:111], a[20:23], v[40:43], v[108:111]\nds_read_b128 v[40:43], v32 offset:8192\n"
"v_cvt_pk_bf16_f32 v58, v76, v77\ns_waitcnt lgkmcnt(3)\nv_mfma_f32_16x16x32_bf16 v[112:115], v[144:147], v[44:47], v[112:115]\nv_cvt_pk_bf16_f32 v59, v78, v79\n"
"v_mfma_f32_16x16x32_bf16 v[116:119], v[148:151], v[44:47], v[116:119]\nv_mfma_f32_16x16x32_bf16 v[120:123], a[16:19], v[44:47], v[120:123]\nv_mfma_f32_16x16x32_bf16 v[124:127], a[20:23], v[44:47], v[124:127]\nds_read_b128 v[44:47], v33 offset:8192\n"
"buffer_store_dwordx4 v[56:59], v17, s[20:23], s50 offen sc1\ns_waitcnt lgkmcnt(3)\nv_mfma_f32_16x16x32_bf16 v[96:99], v[152:155], v[48:51], v[96:99]\nv_cvt_pk_bf16_f32 v60, v88, v89\n"
"v_mfma_f32_16x16x32_bf16 v[100:103], v[156:159], v[48:51], v[100:103]\nv_cvt_pk_bf16_f32 v61, v90, v91\nv_mfma_f32_16x16x32_bf16 v[104:107], a[24:27], v[48:51], v[104:107]\nv_cvt_pk_bf16_f32 v62, v92, v93\n"
"v_mfma_f32_16x16x32_bf16 v[108:111], a[28:31], v[48:51], v[108:111]\nds_read_b128 v[48:51], v34 offset:8192\nv_cvt_pk_bf16_f32 v63, v94, v95\ns_waitcnt lgkmcnt(3)\n"
"v_mfma_f32_16x16x32_bf16 v[112:115], v[152:155], v[52:55], v[112:115]\nv_mfma_f32_16x16x32_bf16 v[116:119], v[156:159], v[52:55], v[116:119]\nv_mfma_f32_16x16x32_bf16 v[120:123], a[24:27], v[52:55], v[120:123]\nbuffer_store_dwordx4 v[60:63], v18, s[20:23], s50 offen sc1\n"
"v_mfma_f32_16x16x32_bf16 v[124:127], a[28:31], v[52:55], v[124:127]\nds_read_b128 v[52:55], v35 offset:8192\ns_add_u32 s50, s50, s36\ns_waitcnt lgkmcnt(3)\n"
"v_mfma_f32_16x16x32_bf16 v[96:99], v[160:163], v[40:43], v[96:99]\ns_add_u32 m0, s52, 0\nv_mfma_f32_16x16x32_bf16 v[100:103], v[164:167], v[40:43], v[100:103]\ns_lshl_b32 s53, s40, 7\n"
"s_add_u32 s53, s53, s49\nv_mfma_f32_16x16x32_bf16 v[104:107], a[32:35], v[40:43], v[104:107]\nbuffer_load_dwordx4 v10, s[12:15], s53 offen lds\nv_mfma_f32_16x16x32_bf16 v[108:111], a[36:39], v[40:43], v[108:111]\n"
"ds_read_b128 v[40:43], v32 offset:12288\ns_waitcnt lgkmcnt(3)\nv_mfma_f32_16x16x32_bf16 v[112:115], v[160:163], v[44:47], v[112:115]\nv_mfma_f32_16x16x32_bf16 v[116:119], v[164:167], v[44:47], v[116:119]\n"
"v_mfma_f32_16x16x32_bf16 v[120:123], a[32:35], v[44:47], v[120:123]\nv_mfma_f32_16x16x32_bf16 v[124:127], a[36:39], v[44:47], v[124:127]\nds_read_b128 v[44:47], v33 offset:12288\ns_waitcnt lgkmcnt(3)\n"
"v_mfma_f32_16x16x32_bf16 v[96:99], v[168:171], v[48:51], v[96:99]\nv_mfma_f32_16x16x32_bf16 v[100:103], v[172:175], v[48:51], v[100:103]\nv_mfma_f32_16x16x32_bf16 v[104:107], a[40:43], v[48:51], v[104:107]\nv_mfma_f32_16x16x32_bf16 v[108:111], a[44:47], v[48:51], v[108:111]\n"
"ds_read_b128 v[48:51], v34 offset:12288\ns_add_u32 m0, s52, 4096\ns_waitcnt lgkmcnt(3)\nv_mfma_f32_16x16x32_bf16 v[112:115], v[168:171], v[52:55], v[112:115]\n"
"s_lshl_b32 s53, s41, 7\ns_add_u32 s53, s53, s49\nv_mfma_f32_16x16x32_bf16 v[116:119], v[172:175], v[52:55], v[116:119]\nbuffer_load_dwordx4 v10, s[12:15], s53 offen lds\n"
"v_mfma_f32_16x16x32_bf16 v[120:123], a[40:43], v[52:55], v[120:123]\nv_mfma_f32_16x16x32_bf16 v[124:127], a[44:47], v[52:55], v[124:127]\nds_read_b128 v[52:55], v35 offset:12288\ns_waitcnt lgkmcnt(3)\n"
"v_mfma_f32_16x16x32_bf16 v[96:99], v[176:179], v[40:43], v[96:99]\nv_mfma_f32_16x16x32_bf16 v[100:103], v[180:183], v[40:43], v[100:103]\nv_mfma_f32_16x16x32_bf16 v[104:107], a[48:51], v[40:43], v[104:107]\nv_mfma_f32_16x16x32_bf16 v[108:111], a[52:55], v[40:43], v[108:111]\n"
"ds_read_b128 v[40:43], v32 offset:16384\ns_waitcnt lgkmcnt(3)\nv_mfma_f32_16x16x32_bf16 v[112:115], v[176:179], v[44:47], v[112:115]\nv_mfma_f32_16x16x32_bf16 v[116:119], v[180:183], v[44:47], v[116:119]\n"
"v_mfma_f32_16x16x32_bf16 v[120:123], a[48:51], v[44:47], v[120:123]\ns_add_u32 m0, s52, 8192\nv_mfma_f32_16x16x32_bf16 v[124:127], a[52:55], v[44:47], v[124:127]\nds_read_b128 v[44:47], v33 offset:16384\n"
"s_lshl_b32 s53, s42, 7\ns_add_u32 s53, s53, s49\ns_waitcnt lgkmcnt(3)\nv_mfma_f32_16x16x32_bf16 v[96:99], v[184:187], v[48:51], v[96:99]\n"
"buffer_load_dwordx4 v10, s[12:15], s53 offen lds\nv_mfma_f32_16x16x32_bf16 v[100:103], v[188:191], v[48:51], v[100:103]\nv_mfma_f32_16x16x32_bf16 v[104:107], a[56:59], v[48:51], v[104:107]\nv_mfma_f32_16x16x32_bf16 v[108:111], a[60:63], v[48:51], v[108:111]\n"
"ds_read_b128 v[48:51], v34 offset:16384\ns_waitcnt lgkmcnt(3)\nv_mfma_f32_16x16x32_bf16 v[112:115], v[184:187], v[52:55], v[112:115]\nv_mfma_f32_16x16x32_bf16 v[116:119], v[188:191], v[52:55], v[116:119]\n"
"v_mfma_f32_16x16x32_bf16 v[120:123], a[56:59], v[52:55], v[120:123]\nv_mfma_f32_16x16x32_bf16 v[124:127], a[60:63], v[52:55], v[124:127]\nds_read_b128 v[52:55], v35 offset:16384\ns_waitcnt lgkmcnt(3)\n"
"v_mfma_f32_16x16x32_bf16 v[96:99], v[192:195], v[40:43], v[96:99]\nv_mfma_f32_16x16x32_bf16 v[100:103], v[196:199], v[40:43], v[100:103]\ns_add_u32 m0, s52, 12288\nv_mfma_f32_16x16x32_bf16 v[104:107], a[64:67], v[40:43], v[104:107]\n"
"s_lshl_b32 s53, s43, 7\ns_add_u32 s53, s53, s49\nv_mfma_f32_16x16x32_bf16 v[108:111], a[68:71], v[40:43], v[108:111]\nds_read_b128 v[40:43], v32 offset:20480\n"
"buffer_load_dwordx4 v10, s[12:15], s53 offen lds\ns_waitcnt lgkmcnt(3)\nv_mfma_f32_16x16x32_bf16 v[112:115], v[192:195], v[44:47], v[112:115]\nv_mfma_f32_16x16x32_bf16 v[116:119], v[196:199], v[44:47], v[116:119]\n"
"v_mfma_f32_16x16x32_bf16 v[120:123], a[64:67], v[44:47], v[120:123]\nv_mfma_f32_16x16x32_bf16 v[124:127], a[68:71], v[44:47], v[124:127]\nds_read_b128 v[44:47], v33 offset:20480\ns_waitcnt lgkmcnt(3)\n"
"v_mfma_f32_16x16x32_bf16 v[96:99], v[200:203], v[48:51], v[96:99]\nv_mfma_f32_16x16x32_bf16 v[100:103], v[204:207], v[48:51], v[100:103]\nv_mfma_f32_16x16x32_bf16 v[104:107], a[72:75], v[48:51], v[104:107]\nv_mfma_f32_16x16x32_bf16 v[108:111], a[76:79], v[48:51], v[108:111]\n"
"ds_read_b128 v[48:51], v34 offset:20480\ns_waitcnt lgkmcnt(3)\nv_mfma_f32_16x16x32_bf16 v[112:115], v[200:203], v[52:55], v[112:115]\ns_add_u32 m0, s52, 16384\n"
"v_mfma_f32_16x16x32_bf16 v[116:119], v[204:207], v[52:55], v[116:119]\ns_lshl_b32 s53, s44, 7\ns_add_u32 s53, s53, s49\nv_mfma_f32_16x16x32_bf16 v[120:123], a[72:75], v[52:55], v[120:123]\n"
"buffer_load_dwordx4 v10, s[12:15], s53 offen lds\nv_mfma_f32_16x16x32_bf16 v[124:127], a[76:79], v[52:55], v[124:127]\nds_read_b128 v[52:55], v35 offset:20480\ns_waitcnt lgkmcnt(3)\n"
"v_mfma_f32_16x16x32_bf16 v[96:99], v[208:211], v[40:43], v[96:99]\nv_mfma_f32_16x16x32_bf16 v[100:103], v[212:215], v[40:43], v[100:103]\nv_mfma_f32_16x16x32_bf16 v[104:107], a[80:83], v[40:43], v[104:107]\nv_mfma_f32_16x16x32_bf16 v[108:111], a[84:87], v[40:43], v[108:111]\n"
"ds_read_b128 v[40:43], v32 offset:24576\ns_waitcnt lgkmcnt(3)\nv_mfma_f32_16x16x32_bf16 v[112:115], v[208:211], v[44:47], v[112:115]\nv_mfma_f32_16x16x32_bf16 v[116:119], v[212:215], v[44:47], v[116:119]\n"
"v_mfma_f32_16x16x32_bf16 v[120:123], a[80:83], v[44:47], v[120:123]\nv_mfma_f32_16x16x32_bf16 v[124:127], a[84:87], v[44:47], v[124:127]\nds_read_b128 v[44:47], v33 offset:24576\ns_add_u32 m0, s52, 20480\n"
"s_waitcnt lgkmcnt(3)\nv_mfma_f32_16x16x32_bf16 v[96:99], v[216:219], v[48:51], v[96:99]\ns_add_u32 s54, s51, 32768\ns_cmp_eq_u32 s54, 163840\n"
"v_mfma_f32_16x16x32_bf16 v[100:103], v[220:223], v[48:51], v[100:103]\nv_mfma_f32_16x16x32_bf16 v[104:107], a[88:91], v[48:51], v[104:107]\nv_mfma_f32_16x16x32_bf16 v[108:111], a[92:95], v[48:51], v[108:111]\ns_cselect_b32 s54, 0, s54\n"
"s_sub_u32 s54, s54, s51\nds_read_b128 v[48:51], v34 offset:24576\ns_lshl_b32 s53, s45, 7\ns_add_u32 s53, s53, s49\n"
"s_waitcnt lgkmcnt(3)\nv_mfma_f32_16x16x32_bf16 v[112:115], v[216:219], v[52:55], v[112:115]\nbuffer_load_dwordx4 v10, s[12:15], s53 offen lds\nv_mfma_f32_16x16x32_bf16 v[116:119], v[220:223], v[52:55], v[116:119]\n"
"v_mfma_f32_16x16x32_bf16 v[120:123], a[88:91], v[52:55], v[120:123]\nv_mfma_f32_16x16x32_bf16 v[124:127], a[92:95], v[52:55], v[124:127]\nds_read_b128 v[52:55], v35 offset:24576\ns_waitcnt lgkmcnt(3)\n"
"v_mfma_f32_16x16x32_bf16 v[96:99], v[224:227], v[40:43], v[96:99]\nv_add_u32 v32, s54, v32\nv_mfma_f32_16x16x32_bf16 v[100:103], v[228:231], v[40:43], v[100:103]\nv_mfma_f32_16x16x32_bf16 v[104:107], a[96:99], v[40:43], v[104:107]\n"
"v_mfma_f32_16x16x32_bf16 v[108:111], a[100:103], v[40:43], v[108:111]\nds_read_b128 v[40:43], v32 offset:0\ns_waitcnt lgkmcnt(3)\nv_mfma_f32_16x16x32_bf16 v[112:115], v[224:227], v[44:47], v[112:115]\n"
"v_add_u32 v33, s54, v33\nv_mfma_f32_16x16x32_bf16 v[116:119], v[228:231], v[44:47], v[116:119]\ns_add_u32 m0, s52, 24576\nv_mfma_f32_16x16x32_bf16 v[120:123], a[96:99], v[44:47], v[120:123]\n"
"s_lshl_b32 s53, s46, 7\ns_add_u32 s53, s53, s49\nv_mfma_f32_16x16x32_bf16 v[124:127], a[100:103], v[44:47], v[124:127]\nds_read_b128 v[44:47], v33 offset:0\n"
"buffer_load_dwordx4 v10, s[12:15], s53 offen lds\ns_waitcnt lgkmcnt(3)\nv_mfma_f32_16x16x32_bf16 v[96:99], v[232:235], v[48:51], v[96:99]\nv_add_u32 v34, s54, v34\n"
"v_mfma_f32_16x16x32_bf16 v[100:103], v[236:239], v[48:51], v[100:103]\nv_mfma_f32_16x16x32_bf16 v[104:107], a[104:107], v[48:51], v[104:107]\nv_mfma_f32_16x16x32_bf16 v[108:111], a[108:111], v[48:51], v[108:111]\nds_read_b128 v[48:51], v34 offset:0\n"
"s_waitcnt lgkmcnt(3)\nv_mfma_f32_16x16x32_bf16 v[112:115], v[232:235], v[52:55], v[112:115]\nv_add_u32 v35, s54, v35\nv_mfma_f32_16x16x32_bf16 v[116:119], v[236:239], v[52:55], v[116:119]\n"
"v_mfma_f32_16x16x32_bf16 v[120:123], a[104:107], v[52:55], v[120:123]\nv_mfma_f32_16x16x32_bf16 v[124:127], a[108:111], v[52:55], v[124:127]\nds_read_b128 v[52:55], v35 offset:0\ns_add_u32 s52, s51, s32\n"
"s_add_u32 s51, s51, s54\ns_add_u32 s49, s49, 28672\n.p2align 6\nfg0_loop:\n"
"s_waitcnt vmcnt(7)\ns_barrier\ns_waitcnt lgkmcnt(3)\nv_mfma_f32_16x16x32_bf16 v[64:67], v[128:131], v[40:43], 0\n"
"v_mov_b32 v36, s48\ns_add_u32 s48, s48, 1\ns_nop 0\nbuffer_store_dword v36, v19, s[24:27], s33 offen sc1\n"
"v_mfma_f32_16x16x32_bf16 v[68:71], v[132:135], v[40:43], 0\nv_mfma_f32_16x16x32_bf16 v[72:75], a[0:3], v[40:43], 0\nv_mfma_f32_16x16x32_bf16 v[76:79], a[4:7], v[40:43], 0\nds_read_b128 v[40:43], v32 offset:4096\n"
"v_cvt_pk_bf16_f32 v56, v96, v97\ns_waitcnt lgkmcnt(3)\nv_mfma_f32_16x16x32_bf16 v[80:83], v[128:131], v[44:47], 0\nv_cvt_pk_bf16_f32 v57, v98, v99\n"
"v_mfma_f32_16x16x32_bf16 v[84:87], v[132:135], v[44:47], 0\nv_cvt_pk_bf16_f32 v58, v100, v101\nv_mfma_f32_16x16x32_bf16 v[88:91], a[0:3], v[44:47], 0\nv_cvt_pk_bf16_f32 v59, v102, v103\n"
"v_mfma_f32_16x16x32_bf16 v[92:95], a[4:7], v[44:47], 0\nds_read_b128 v[44:47], v33 offset:4096\ns_waitcnt lgkmcnt(3)\nv_mfma_f32_16x16x32_bf16 v[64:67], v[136:139], v[48:51], v[64:67]\n"
"v_mfma_f32_16x16x32_bf16 v[68:71], v[140:143], v[48:51], v[68:71]\nbuffer_store_dwordx4 v[56:59], v15, s[20:23], s50 offen sc1\nv_mfma_f32_16x16x32_bf16 v[72:75], a[8:11], v[48:51], v[72:75]\nv_cvt_pk_bf16_f32 v60, v112, v113\n"
"v_mfma_f32_16x16x32_bf16 v[76:79], a[12:15], v[48:51], v[76:79]\nds_read_b128 v[48:51], v34 offset:4096\nv_cvt_pk_bf16_f32 v61, v114, v115\ns_waitcnt lgkmcnt(3)\n"
"v_mfma_f32_16x16x32_bf16 v[80:83], v[136:139], v[52:55], v[80:83]\nv_cvt_pk_bf16_f32 v62, v116, v117\nv_mfma_f32_16x16x32_bf16 v[84:87], v[140:143], v[52:55], v[84:87]\nv_cvt_pk_bf16_f32 v63, v118, v119\n"
"v_mfma_f32_16x16x32_bf16 v[88:91], a[8:11], v[52:55], v[88:91]\nv_mfma_f32_16x16x32_bf16 v[92:95], a[12:15], v[52:55], v[92:95]\nds_read_b128 v[52:55], v35 offset:4096\ns_waitcnt lgkmcnt(3)\n"
"v_mfma_f32_16x16x32_bf16 v[64:67], v[144:147], v[40:43], v[64:67]\nbuffer_store_dwordx4 v[60:63], v16, s[20:23], s50 offen sc1\nv_mfma_f32_16x16x32_bf16 v[68:71], v[148:151], v[40:43], v[68:71]\nv_cvt_pk_bf16_f32 v56, v104, v105\n"
"v_mfma_f32_16x16x32_bf16 v[72:75], a[16:19], v[40:43], v[72:75]\nv_cvt_pk_bf16_f32 v57, v106, v107\nv_mfma_f32_16x16x32_bf16 v[76:79], a[20:23], v[40:43], v[76:79]\nds_read_b128 v[40:43], v32 offset:8192\n"
"v_cvt_pk_bf16_f32 v58, v108, v109\ns_waitcnt lgkmcnt(3)\nv_mfma_f32_16x16x32_bf16 v[80:83], v[144:147], v[44:47], v[80:83]\nv_cvt_pk_bf16_f32 v59, v110, v111\n"
"v_mfma_f32_16x16x32_bf16 v[84:87], v[148:151], v[44:47], v[84:87]\nv_mfma_f32_16x16x32_bf16 v[88:91], a[16:19], v[44:47], v[88:91]\nv_mfma_f32_16x16x32_bf16 v[92:95], a[20:23], v[44:47], v[92:95]\nds_read_b128 v[44:47], v33 offset:8192\n"
"buffer_store_dwordx4 v[56:59], v17, s[20:23], s50 offen sc1\ns_waitcnt lgkmcnt(3)\nv_mfma_f32_16x16x32_bf16 v[64:67], v[152:155], v[48:51], v[64:67]\nv_cvt_pk_bf16_f32 v60, v120, v121\n"
"v_mfma_f32_16x16x32_bf16 v[68:71], v[156:159], v[48:51], v[68:71]\nv_cvt_pk_bf16_f32 v61, v122, v123\nv_mfma_f32_16x16x32_bf16 v[72:75], a[24:27], v[48:51], v[72:75]\nv_cvt_pk_bf16_f32 v62, v124, v125\n"
"v_mfma_f32_16x16x32_bf16 v[76:79], a[28:31], v[48:51], v[76:79]\nds_read_b128 v[48:51], v34 offset:8192\nv_cvt_pk_bf16_f32 v63, v126, v127\ns_waitcnt lgkmcnt(3)\n"
"v_mfma_f32_16x16x32_bf16 v[80:83], v[152:155], v[52:55], v[80:83]\nv_mfma_f32_16x16x32_bf16 v[84:87], v[156:159], v[52:55], v[84:87]\nv_mfma_f32_16x16x32_bf16 v[88:91], a[24:27], v[52:55], v[88:91]\nbuffer_store_dwordx4 v[60:63], v18, s[20:23], s50 offen sc1\n"
"v_mfma_f32_16x16x32_bf16 v[92:95], a[28:31], v[52:55], v[92:95]\nds_read_b128 v[52:55], v35 offset:8192\ns_add_u32 s50, s50, s36\ns_waitcnt lgkmcnt(3)\n"
"v_mfma_f32_16x16x32_bf16 v[64:67], v[160:163], v[40:43], v[64:67]\ns_add_u32 m0, s52, 0\nv_mfma_f32_16x16x32_bf16 v[68:71], v[164:167], v[40:43], v[68:71]\ns_lshl_b32 s53, s40, 7\n"
"s_add_u32 s53, s53, s49\nv_mfma_f32_16x16x32_bf16 v[72:75], a[32:35], v[40:43], v[72:75]\nbuffer_load_dwordx4 v10, s[12:15], s53 offen lds\nv_mfma_f32_16x16x32_bf16 v[76:79], a[36:39], v[40:43], v[76:79]\n"
"ds_read_b128 v[40:43], v32 offset:12288\ns_waitcnt lgkmcnt(3)\nv_mfma_f32_16x16x32_bf16 v[80:83], v[160:163], v[44:47], v[80:83]\nv_mfma_f32_16x16x32_bf16 v[84:87], v[164:167], v[44:47], v[84:87]\n"
"v_mfma_f32_16x16x32_bf16 v[88:91], a[32:35], v[44:47], v[88:91]\nv_mfma_f32_16x16x32_bf16 v[92:95], a[36:39], v[44:47], v[92:95]\nds_read_b128 v[44:47], v33 offset:12288\ns_waitcnt lgkmcnt(3)\n"
"v_mfma_f32_16x16x32_bf16 v[64:67], v[168:171], v[48:51], v[64:67]\nv_mfma_f32_16x16x32_bf16 v[68:71], v[172:175], v[48:51], v[68:71]\nv_mfma_f32_16x16x32_bf16 v[72:75], a[40:43], v[48:51], v[72:75]\nv_mfma_f32_16x16x32_bf16 v[76:79], a[44:47], v[48:51], v[76:79]\n"
"ds_read_b128 v[48:51], v34 offset:12288\ns_add_u32 m0, s52, 4096\ns_waitcnt lgkmcnt(3)\nv_mfma_f32_16x16x32_bf16 v[80:83], v[168:171], v[52:55], v[80:83]\n"
"s_lshl_b32 s53, s41, 7\ns_add_u32 s53, s53, s49\nv_mfma_f32_16x16x32_bf16 v[84:87], v[172:175], v[52:55], v[84:87]\nbuffer_load_dwordx4 v10, s[12:15], s53 offen lds\n"
"v_mfma_f32_16x16x32_bf16 v[88:91], a[40:43], v[52:55], v[88:91]\nv_mfma_f32_16x16x32_bf16 v[92:95], a[44:47], v[52:55], v[92:95]\nds_read_b128 v[52:55], v35 offset:12288\ns_waitcnt lgkmcnt(3)\n"
"v_mfma_f32_16x16x32_bf16 v[64:67], v[176:179], v[40:43], v[64:67]\nv_mfma_f32_16x16x32_bf16 v[68:71], v[180:183], v[40:43], v[68:71]\nv_mfma_f32_16x16x32_bf16 v[72:75], a[48:51], v[40:43], v[72:75]\nv_mfma_f32_16x16x32_bf16 v[76:79], a[52:55], v[40:43], v[76:79]\n"
"ds_read_b128 v[40:43], v32 offset:16384\ns_waitcnt lgkmcnt(3)\nv_mfma_f32_16x16x32_bf16 v[80:83], v[176:179], v[44:47], v[80:83]\nv_mfma_f32_16x16x32_bf16 v[84:87], v[180:183], v[44:47], v[84:87]\n"
"v_mfma_f32_16x16x32_bf16 v[88:91], a[48:51], v[44:47], v[88:91]\ns_add_u32 m0, s52, 8192\nv_mfma_f32_16x16x32_bf16 v[92:95], a[52:55], v[44:47], v[92:95]\nds_read_b128 v[44:47], v33 offset:16384\n"
"s_lshl_b32 s53, s42, 7\ns_add_u32 s53, s53, s49\ns_waitcnt lgkmcnt(3)\nv_mfma_f32_16x16x32_bf16 v[64:67], v[184:187], v[48:51], v[64:67]\n"
"buffer_load_dwordx4 v10, s[12:15], s53 offen lds\nv_mfma_f32_16x16x32_bf16 v[68:71], v[188:191], v[48:51], v[68:71]\nv_mfma_f32_16x16x32_bf16 v[72:75], a[56:59], v[48:51], v[72:75]\nv_mfma_f32_16x16x32_bf16 v[76:79], a[60:63], v[48:51], v[76:79]\n"
"ds_read_b128 v[48:51], v34 offset:16384\ns_waitcnt lgkmcnt(3)\nv_mfma_f32_16x16x32_bf16 v[80:83], v[184:187], v[52:55], v[80:83]\nv_mfma_f32_16x16x32_bf16 v[84:87], v[188:191], v[52:55], v[84:87]\n"
"v_mfma_f32_16x16x32_bf16 v[88:91], a[56:59], v[52:55], v[88:91]\nv_mfma_f32_16x16x32_bf16 v[92:95], a[60:63], v[52:55], v[92:95]\nds_read_b128 v[52:55], v35 offset:16384\ns_waitcnt lgkmcnt(3)\n"
"v_mfma_f32_16x16x32_bf16 v[64:67], v[192:195], v[40:43], v[64:67]\nv_mfma_f32_16x16x32_bf16 v[68:71], v[196:199], v[40:43], v[68:71]\ns_add_u32 m0, s52, 12288\nv_mfma_f32_16x16x32_bf16 v[72:75], a[64:67], v[40:43], v[72:75]\n"
"s_lshl_b32 s53, s43, 7\ns_add_u32 s53, s53, s49\nv_mfma_f32_16x16x32_bf16 v[76:79], a[68:71], v[40:43], v[76:79]\nds_read_b128 v[40:43], v32 offset:20480\n"
"buffer_load_dwordx4 v10, s[12:15], s53 offen lds\ns_waitcnt lgkmcnt(3)\nv_mfma_f32_16x16x32_bf16 v[80:83], v[192:195], v[44:47], v[80:83]\nv_mfma_f32_16x16x32_bf16 v[84:87], v[196:199], v[44:47], v[84:87]\n"
"v_mfma_f32_16x16x32_bf16 v[88:91], a[64:67], v[44:47], v[88:91]\nv_mfma_f32_16x16x32_bf16 v[92:95], a[68:71], v[44:47], v[92:95]\nds_read_b128 v[44:47], v33 offset:20480\ns_waitcnt lgkmcnt(3)\n"
"v_mfma_f32_16x16x32_bf16 v[64:67], v[200:203], v[48:51], v[64:67]\nv_mfma_f32_16x16x32_bf16 v[68:71], v[204:207], v[48:51], v[68:71]\nv_mfma_f32_16x16x32_bf16 v[72:75], a[72:75], v[48:51], v[72:75]\nv_mfma_f32_16x16x32_bf16 v[76:79], a[76:79], v[48:51], v[76:79]\n"
"ds_read_b128 v[48:51], v34 offset:20480\ns_waitcnt lgkmcnt(3)\nv_mfma_f32_16x16x32_bf16 v[80:83], v[200:203], v[52:55], v[80:83]\ns_add_u32 m0, s52, 16384\n"
"v_mfma_f32_16x16x32_bf16 v[84:87], v[204:207], v[52:55], v[84:87]\ns_lshl_b32 s53, s44, 7\ns_add_u32 s53, s53, s49\nv_mfma_f32_16x16x32_bf16 v[88:91], a[72:75], v[52:55], v[88:91]\n"
"buffer_load_dwordx4 v10, s[12:15], s53 offen lds\nv_mfma_f32_16x16x32_bf16 v[92:95], a[76:79], v[52:55], v[92:95]\nds_read_b128 v[52:55], v35 offset:20480\ns_waitcnt lgkmcnt(3)\n"
"v_mfma_f32_16x16x32_bf16 v[64:67], v[208:211], v[40:43], v[64:67]\nv_mfma_f32_16x16x32_bf16 v[68:71], v[212:215], v[40:43], v[68:71]\nv_mfma_f32_16x16x32_bf16 v[72:75], a[80:83], v[40:43], v[72:75]\nv_mfma_f32_16x16x32_bf16 v[76:79], a[84:87], v[40:43], v[76:79]\n"
"ds_read_b128 v[40:43], v32 offset:24576\ns_waitcnt lgkmcnt(3)\nv_mfma_f32_16x16x32_bf16 v[80:83], v[208:211], v[44:47], v[80:83]\nv_mfma_f32_16x16x32_bf16 v[84:87], v[212:215], v[44:47], v[84:87]\n"
"v_mfma_f32_16x16x32_bf16 v[88:91], a[80:83], v[44:47], v[88:91]\nv_mfma_f32_16x16x32_bf16 v[92:95], a[84:87], v[44:47], v[92:95]\nds_read_b128 v[44:47], v33 offset:24576\ns_add_u32 m0, s52, 20480\n"
"s_waitcnt lgkmcnt(3)\nv_mfma_f32_16x16x32_bf16 v[64:67], v[216:219], v[48:51], v[64:67]\ns_add_u32 s54, s51, 32768\ns_cmp_eq_u32 s54, 163840\n"
"v_mfma_f32_16x16x32_bf16 v[68:71], v[220:223], v[48:51], v[68:71]\nv_mfma_f32_16x16x32_bf16 v[72:75], a[88:91], v[48:51], v[72:75]\nv_mfma_f32_16x16x32_bf16 v[76:79], a[92:95], v[48:51], v[76:79]\ns_cselect_b32 s54, 0, s54\n"
"s_sub_u32 s54, s54, s51\nds_read_b128 v[48:51], v34 offset:24576\ns_lshl_b32 s53, s45, 7\ns_add_u32 s53, s53, s49\n"
"s_waitcnt lgkmcnt(3)\nv_mfma_f32_16x16x32_bf16 v[80:83], v[216:219], v[52:55], v[80:83]\nbuffer_load_dwordx4 v10, s[12:15], s53 offen lds\nv_mfma_f32_16x16x32_bf16 v[84:87], v[220:223], v[52:55], v[84:87]\n"
"v_mfma_f32_16x16x32_bf16 v[88:91], a[88:91], v[52:55], v[88:91]\nv_mfma_f32_16x16x32_bf16 v[92:95], a[92:95], v[52:55], v[92:95]\nds_read_b128 v[52:55], v35 offset:24576\ns_waitcnt lgkmcnt(3)\n"
"v_mfma_f32_16x16x32_bf16 v[64:67], v[224:227], v[40:43], v[64:67]\nv_add_u32 v32, s54, v32\nv_mfma_f32_16x16x32_bf16 v[68:71], v[228:231], v[40:43], v[68:71]\nv_mfma_f32_16x16x32_bf16 v[72:75], a[96:99], v[40:43], v[72:75]\n"
"v_mfma_f32_16x16x32_bf16 v[76:79], a[100:103], v[40:43], v[76:79]\nds_read_b128 v[40:43], v32 offset:0\ns_waitcnt lgkmcnt(3)\nv_mfma_f32_16x16x32_bf16 v[80:83], v[224:227], v[44:47], v[80:83]\n"
"v_add_u32 v33, s54, v33\nv_mfma_f32_16x16x32_bf16 v[84:87], v[228:231], v[44:47], v[84:87]\ns_add_u32 m0, s52, 24576\nv_mfma_f32_16x16x32_bf16 v[88:91], a[96:99], v[44:47], v[88:91]\n"
"s_lshl_b32 s53, s46, 7\ns_add_u32 s53, s53, s49\nv_mfma_f32_16x16x32_bf16 v[92:95], a[100:103], v[44:47], v[92:95]\nds_read_b128 v[44:47], v33 offset:0\n"
"buffer_load_dwordx4 v10, s[12:15], s53 offen lds\ns_waitcnt lgkmcnt(3)\nv_mfma_f32_16x16x32_bf16 v[64:67], v[232:235], v[48:51], v[64:67]\nv_add_u32 v34, s54, v34\n"
"v_mfma_f32_16x16x32_bf16 v[68:71], v[236:239], v[48:51], v[68:71]\nv_mfma_f32_16x16x32_bf16 v[72:75], a[104:107], v[48:51], v[72:75]\nv_mfma_f32_16x16x32_bf16 v[76:79], a[108:111], v[48:51], v[76:79]\nds_read_b128 v[48:51], v34 offset:0\n"
"s_waitcnt lgkmcnt(3)\nv_mfma_f32_16x16x32_bf16 v[80:83], v[232:235], v[52:55], v[80:83]\nv_add_u32 v35, s54, v35\nv_mfma_f32_16x16x32_bf16 v[84:87], v[236:239], v[52:55], v[84:87]\n"
"v_mfma_f32_16x16x32_bf16 v[88:91], a[104:107], v[52:55], v[88:91]\nv_mfma_f32_16x16x32_bf16 v[92:95], a[108:111], v[52:55], v[92:95]\nds_read_b128 v[52:55], v35 offset:0\ns_add_u32 s52, s51, s32\n"
"s_add_u32 s51, s51, s54\ns_add_u32 s49, s49, 28672\ns_waitcnt vmcnt(7)\ns_barrier\n"
"s_waitcnt lgkmcnt(3)\nv_mfma_f32_16x16x32_bf16 v[96:99], v[128:131], v[40:43], 0\nv_mov_b32 v36, s48\ns_add_u32 s48, s48, 1\n"
"s_nop 0\nbuffer_store_dword v36, v19, s[24:27], s33 offen sc1\nv_mfma_f32_16x16x32_bf16 v[100:103], v[132:135], v[40:43], 0\nv_mfma_f32_16x16x32_bf16 v[104:107], a[0:3], v[40:43], 0\n"
"v_mfma_f32_16x16x32_bf16 v[108:111], a[4:7], v[40:43], 0\nds_read_b128 v[40:43], v32 offset:4096\nv_cvt_pk_bf16_f32 v56, v64, v65\ns_waitcnt lgkmcnt(3)\n"
"v_mfma_f32_16x16x32_bf16 v[112:115], v[128:131], v[44:47], 0\nv_cvt_pk_bf16_f32 v57, v66, v67\nv_mfma_f32_16x16x32_bf16 v[116:119], v[132:135], v[44:47], 0\nv_cvt_pk_bf16_f32 v58, v68, v69\n"
"v_mfma_f32_16x16x32_bf16 v[120:123], a[0:3], v[44:47], 0\nv_cvt_pk_bf16_f32 v59, v70, v71\nv_mfma_f32_16x16x32_bf16 v[124:127], a[4:7], v[44:47], 0\nds_read_b128 v[44:47], v33 offset:4096\n"
"s_waitcnt lgkmcnt(3)\nv_mfma_f32_16x16x32_bf16 v[96:99], v[136:139], v[48:51], v[96:99]\nv_mfma_f32_16x16x32_bf16 v[100:103], v[140:143], v[48:51], v[100:103]\nbuffer_store_dwordx4 v[56:59], v15, s[20:23], s50 offen sc1\n"
"v_mfma_f32_16x16x32_bf16 v[104:107], a[8:11], v[48:51], v[104:107]\nv_cvt_pk_bf16_f32 v60, v80, v81\nv_mfma_f32_16x16x32_bf16 v[108:111], a[12:15], v[48:51], v[108:111]\nds_read_b128 v[48:51], v34 offset:4096\n"
"v_cvt_pk_bf16_f32 v61, v82, v83\ns_waitcnt lgkmcnt(3)\nv_mfma_f32_16x16x32_bf16 v[112:115], v[136:139], v[52:55], v[112:115]\nv_cvt_pk_bf16_f32 v62, v84, v85\n"
"v_mfma_f32_16x16x32_bf16 v[116:119], v[140:143], v[52:55], v[116:119]\nv_cvt_pk_bf16_f32 v63, v86, v87\nv_mfma_f32_16x16x32_bf16 v[120:123], a[8:11], v[52:55], v[120:123]\nv_mfma_f32_16x16x32_bf16 v[124:127], a[12:15], v[52:55], v[124:127]\n"
"ds_read_b128 v[52:55], v35 offset:4096\ns_waitcnt lgkmcnt(3)\nv_mfma_f32_16x16x32_bf16 v[96:99], v[144:147], v[40:43], v[96:99]\nbuffer_store_dwordx4 v[60:63], v16, s[20:23], s50 offen sc1\n"
"v_mfma_f32_16x16x32_bf16 v[100:103], v[148:151], v[40:43], v[100:103]\nv_cvt_pk_bf16_f32 v56, v72, v73\nv_mfma_f32_16x16x32_bf16 v[104:107], a[16:19], v[40:43], v[104:107]\nv_cvt_pk_bf16_f32 v57, v74, v75\n"
"v_mfma_f32_16x16x32_bf16 v[108:111], a[20:23], v[40:43], v[108:111]\nds_read_b128 v[40:43], v32 offset:8192\nv_cvt_pk_bf16_f32 v58, v76, v77\ns_waitcnt lgkmcnt(3)\n"
"v_mfma_f32_16x16x32_bf16 v[112:115], v[144:147], v[44:47], v[112:115]\nv_cvt_pk_bf16_f32 v59, v78, v79\nv_mfma_f32_16x16x32_bf16 v[116:119], v[148:151], v[44:47], v[116:119]\nv_mfma_f32_16x16x32_bf16 v[120:123], a[16:19], v[44:47], v[120:123]\n"
"v_mfma_f32_16x16x32_bf16 v[124:127], a[20:23], v[44:47], v[124:127]\nds_read_b128 v[44:47], v33 offset:8192\nbuffer_store_dwordx4 v[56:59], v17, s[20:23], s50 offen sc1\ns_waitcnt lgkmcnt(3)\n"
"v_mfma_f32_16x16x32_bf16 v[96:99], v[152:155], v[48:51], v[96:99]\nv_cvt_pk_bf16_f32 v60, v88, v89\nv_mfma_f32_16x16x32_bf16 v[100:103], v[156:159], v[48:51], v[100:103]\nv_cvt_pk_bf16_f32 v61, v90, v91\n"
"v_mfma_f32_16x16x32_bf16 v[104:107], a[24:27], v[48:51], v[104:107]\nv_cvt_pk_bf16_f32 v62, v92, v93\nv_mfma_f32_16x16x32_bf16 v[108:111], a[28:31], v[48:51], v[108:111]\nds_read_b128 v[48:51], v34 offset:8192\n"
"v_cvt_pk_bf16_f32 v63, v94, v95\ns_waitcnt lgkmcnt(3)\nv_mfma_f32_16x16x32_bf16 v[112:115], v[152:155], v[52:55], v[112:115]\nv_mfma_f32_16x16x32_bf16 v[116:119], v[156:159], v[52:55], v[116:119]\n"
"v_mfma_f32_16x16x32_bf16 v[120:123], a[24:27], v[52:55], v[120:123]\nbuffer_store_dwordx4 v[60:63], v18, s[20:23], s50 offen sc1\nv_mfma_f32_16x16x32_bf16 v[124:127], a[28:31], v[52:55], v[124:127]\nds_read_b128 v[52:55], v35 offset:8192\n"
"s_add_u32 s50, s50, s36\ns_waitcnt lgkmcnt(3)\nv_mfma_f32_16x16x32_bf16 v[96:99], v[160:163], v[40:43], v[96:99]\ns_add_u32 m0, s52, 0\n"
"v_mfma_f32_16x16x32_bf16 v[100:103], v[164:167], v[40:43], v[100:103]\ns_lshl_b32 s53, s40, 7\ns_add_u32 s53, s53, s49\nv_mfma_f32_16x16x32_bf16 v[104:107], a[32:35], v[40:43], v[104:107]\n"
"buffer_load_dwordx4 v10, s[12:15], s53 offen lds\nv_mfma_f32_16x16x32_bf16 v[108:111], a[36:39], v[40:43], v[108:111]\nds_read_b128 v[40:43], v32 offset:12288\ns_waitcnt lgkmcnt(3)\n"
"v_mfma_f32_16x16x32_bf16 v[112:115], v[160:163], v[44:47], v[112:115]\nv_mfma_f32_16x16x32_bf16 v[116:119], v[164:167], v[44:47], v[116:119]\nv_mfma_f32_16x16x32_bf16 v[120:123], a[32:35], v[44:47], v[120:123]\nv_mfma_f32_16x16x32_bf16 v[124:127], a[36:39], v[44:47], v[124:127]\n"
"ds_read_b128 v[44:47], v33 offset:12288\ns_waitcnt lgkmcnt(3)\nv_mfma_f32_16x16x32_bf16 v[96:99], v[168:171], v[48:51], v[96:99]\nv_mfma_f32_16x16x32_bf16 v[100:103], v[172:175], v[48:51], v[100:103]\n"
"v_mfma_f32_16x16x32_bf16 v[104:107], a[40:43], v[48:51], v[104:107]\nv_mfma_f32_16x16x32_bf16 v[108:111], a[44:47], v[48:51], v[108:111]\nds_read_b128 v[48:51], v34 offset:12288\ns_add_u32 m0, s52, 4096\n"
"s_waitcnt lgkmcnt(3)\nv_mfma_f32_16x16x32_bf16 v[112:115], v[168:171], v[52:55], v[112:115]\ns_lshl_b32 s53, s41, 7\ns_add_u32 s53, s53, s49\n"
"v_mfma_f32_16x16x32_bf16 v[116:119], v[172:175], v[52:55], v[116:119]\nbuffer_load_dwordx4 v10, s[12:15], s53 offen lds\nv_mfma_f32_16x16x32_bf16 v[120:123], a[40:43], v[52:55], v[120:123]\nv_mfma_f32_16x16x32_bf16 v[124:127], a[44:47], v[52:55], v[124:127]\n"
"ds_read_b128 v[52:55], v35 offset:12288\ns_waitcnt lgkmcnt(3)\nv_mfma_f32_16x16x32_bf16 v[96:99], v[176:179], v[40:43], v[96:99]\nv_mfma_f32_16x16x32_bf16 v[100:103], v[180:183], v[40:43], v[100:103]\n"
"v_mfma_f32_16x16x32_bf16 v[104:107], a[48:51], v[40:43], v[104:107]\nv_mfma_f32_16x16x32_bf16 v[108:111], a[52:55], v[40:43], v[108:111]\nds_read_b128 v[40:43], v32 offset:16384\ns_waitcnt lgkmcnt(3)\n"
"v_mfma_f32_16x16x32_bf16 v[112:115], v[176:179], v[44:47], v[112:115]\nv_mfma_f32_16x16x32_bf16 v[116:119], v[180:183], v[44:47], v[116:119]\nv_mfma_f32_16x16x32_bf16 v[120:123], a[48:51], v[44:47], v[120:123]\ns_add_u32 m0, s52, 8192\n"
"v_mfma_f32_16x16x32_bf16 v[124:127], a[52:55], v[44:47], v[124:127]\nds_read_b128 v[44:47], v33 offset:16384\ns_lshl_b32 s53, s42, 7\ns_add_u32 s53, s53, s49\n"
"s_waitcnt lgkmcnt(3)\nv_mfma_f32_16x16x32_bf16 v[96:99], v[184:187], v[48:51], v[96:99]\nbuffer_load_dwordx4 v10, s[12:15], s53 offen lds\nv_mfma_f32_16x16x32_bf16 v[100:103], v[188:191], v[48:51], v[100:103]\n"
"v_mfma_f32_16x16x32_bf16 v[104:107], a[56:59], v[48:51], v[104:107]\nv_mfma_f32_16x16x32_bf16 v[108:111], a[60:63], v[48:51], v[108:111]\nds_read_b128 v[48:51], v34 offset:16384\ns_waitcnt lgkmcnt(3)\n"
"v_mfma_f32_16x16x32_bf16 v[112:115], v[184:187], v[52:55], v[112:115]\nv_mfma_f32_16x16x32_bf16 v[116:119], v[188:191], v[52:55], v[116:119]\nv_mfma_f32_16x16x32_bf16 v[120:123], a[56:59], v[52:55], v[120:123]\nv_mfma_f32_16x16x32_bf16 v[124:127], a[60:63], v[52:55], v[124:127]\n"
"ds_read_b128 v[52:55], v35 offset:16384\ns_waitcnt lgkmcnt(3)\nv_mfma_f32_16x16x32_bf16 v[96:99], v[192:195], v[40:43], v[96:99]\nv_mfma_f32_16x16x32_bf16 v[100:103], v[196:199], v[40:43], v[100:103]\n"
"s_add_u32 m0, s52, 12288\nv_mfma_f32_16x16x32_bf16 v[104:107], a[64:67], v[40:43], v[104:107]\ns_lshl_b32 s53, s43, 7\ns_add_u32 s53, s53, s49\n"
"v_mfma_f32_16x16x32_bf16 v[108:111], a[68:71], v[40:43], v[108:111]\nds_read_b128 v[40:43], v32 offset:20480\nbuffer_load_dwordx4 v10, s[12:15], s53 offen lds\ns_waitcnt lgkmcnt(3)\n"
"v_mfma_f32_16x16x32_bf16 v[112:115], v[192:195], v[44:47], v[112:115]\nv_mfma_f32_16x16x32_bf16 v[116:119], v[196:199], v[44:47], v[116:119]\nv_mfma_f32_16x16x32_bf16 v[120:123], a[64:67], v[44:47], v[120:123]\nv_mfma_f32_16x16x32_bf16 v[124:127], a[68:71], v[44:47], v[124:127]\n"
"ds_read_b128 v[44:47], v33 offset:20480\ns_waitcnt lgkmcnt(3)\nv_mfma_f32_16x16x32_bf16 v[96:99], v[200:203], v[48:51], v[96:99]\nv_mfma_f32_16x16x32_bf16 v[100:103], v[204:207], v[48:51], v[100:103]\n"
"v_mfma_f32_16x16x32_bf16 v[104:107], a[72:75], v[48:51], v[104:107]\nv_mfma_f32_16x16x32_bf16 v[108:111], a[76:79], v[48:51], v[108:111]\nds_read_b128 v[48:51], v34 offset:20480\ns_waitcnt lgkmcnt(3)\n"
"v_mfma_f32_16x16x32_bf16 v[112:115], v[200:203], v[52:55], v[112:115]\ns_add_u32 m0, s52, 16384\nv_mfma_f32_16x16x32_bf16 v[116:119], v[204:207], v[52:55], v[116:119]\ns_lshl_b32 s53, s44, 7\n"
"s_add_u32 s53, s53, s49\nv_mfma_f32_16x16x32_bf16 v[120:123], a[72:75], v[52:55], v[120:123]\nbuffer_load_dwordx4 v10, s[12:15], s53 offen lds\nv_mfma_f32_16x16x32_bf16 v[124:127], a[76:79], v[52:55], v[124:127]\n"
"ds_read_b128 v[52:55], v35 offset:20480\ns_waitcnt lgkmcnt(3)\nv_mfma_f32_16x16x32_bf16 v[96:99], v[208:211], v[40:43], v[96:99]\nv_mfma_f32_16x16x32_bf16 v[100:103], v[212:215], v[40:43], v[100:103]\n"
"v_mfma_f32_16x16x32_bf16 v[104:107], a[80:83], v[40:43], v[104:107]\nv_mfma_f32_16x16x32_bf16 v[108:111], a[84:87], v[40:43], v[108:111]\nds_read_b128 v[40:43], v32 offset:24576\ns_waitcnt lgkmcnt(3)\n"
"v_mfma_f32_16x16x32_bf16 v[112:115], v[208:211], v[44:47], v[112:115]\nv_mfma_f32_16x16x32_bf16 v[116:119], v[212:215], v[44:47], v[116:119]\nv_mfma_f32_16x16x32_bf16 v[120:123], a[80:83], v[44:47], v[120:123]\nv_mfma_f32_16x16x32_bf16 v[124:127], a[84:87], v[44:47], v[124:127]\n"
"ds_read_b128 v[44:47], v33 offset:24576\ns_add_u32 m0, s52, 20480\ns_waitcnt lgkmcnt(3)\nv_mfma_f32_16x16x32_bf16 v[96:99], v[216:219], v[48:51], v[96:99]\n"
"s_add_u32 s54, s51, 32768\ns_cmp_eq_u32 s54, 163840\nv_mfma_f32_16x16x32_bf16 v[100:103], v[220:223], v[48:51], v[100:103]\nv_mfma_f32_16x16x32_bf16 v[104:107], a[88:91], v[48:51], v[104:107]\n"
"v_mfma_f32_16x16x32_bf16 v[108:111], a[92:95], v[48:51], v[108:111]\ns_cselect_b32 s54, 0, s54\ns_sub_u32 s54, s54, s51\nds_read_b128 v[48:51], v34 offset:24576\n"
"s_lshl_b32 s53, s45, 7\ns_add_u32 s53, s53, s49\ns_waitcnt lgkmcnt(3)\nv_mfma_f32_16x16x32_bf16 v[112:115], v[216:219], v[52:55], v[112:115]\n"
"buffer_load_dwordx4 v10, s[12:15], s53 offen lds\nv_mfma_f32_16x16x32_bf16 v[116:119], v[220:223], v[52:55], v[116:119]\nv_mfma_f32_16x16x32_bf16 v[120:123], a[88:91], v[52:55], v[120:123]\nv_mfma_f32_16x16x32_bf16 v[124:127], a[92:95], v[52:55], v[124:127]\n"
"ds_read_b128 v[52:55], v35 offset:24576\ns_waitcnt lgkmcnt(3)\nv_mfma_f32_16x16x32_bf16 v[96:99], v[224:227], v[40:43], v[96:99]\nv_add_u32 v32, s54, v32\n"
"v_mfma_f32_16x16x32_bf16 v[100:103], v[228:231], v[40:43], v[100:103]\nv_mfma_f32_16x16x32_bf16 v[104:107], a[96:99], v[40:43], v[104:107]\nv_mfma_f32_16x16x32_bf16 v[108:111], a[100:103], v[40:43], v[108:111]\nds_read_b128 v[40:43], v32 offset:0\n"
"s_waitcnt lgkmcnt(3)\nv_mfma_f32_16x16x32_bf16 v[112:115], v[224:227], v[44:47], v[112:115]\nv_add_u32 v33, s54, v33\nv_mfma_f32_16x16x32_bf16 v[116:119], v[228:231], v[44:47], v[116:119]\n"
"s_add_u32 m0, s52, 24576\nv_mfma_f32_16x16x32_bf16 v[120:123], a[96:99], v[44:47], v[120:123]\ns_lshl_b32 s53, s46, 7\ns_add_u32 s53, s53, s49\n"
"v_mfma_f32_16x16x32_bf16 v[124:127], a[100:103], v[44:47], v[124:127]\nds_read_b128 v[44:47], v33 offset:0\nbuffer_load_dwordx4 v10, s[12:15], s53 offen lds\ns_waitcnt lgkmcnt(3)\n"
"v_mfma_f32_16x16x32_bf16 v[96:99], v[232:235], v[48:51], v[96:99]\nv_add_u32 v34, s54, v34\nv_mfma_f32_16x16x32_bf16 v[100:103], v[236:239], v[48:51], v[100:103]\nv_mfma_f32_16x16x32_bf16 v[104:107], a[104:107], v[48:51], v[104:107]\n"
"v_mfma_f32_16x16x32_bf16 v[108:111], a[108:111], v[48:51], v[108:111]\nds_read_b128 v[48:51], v34 offset:0\ns_waitcnt lgkmcnt(3)\nv_mfma_f32_16x16x32_bf16 v[112:115], v[232:235], v[52:55], v[112:115]\n"
"v_add_u32 v35, s54, v35\nv_mfma_f32_16x16x32_bf16 v[116:119], v[236:239], v[52:55], v[116:119]\nv_mfma_f32_16x16x32_bf16 v[120:123], a[104:107], v[52:55], v[120:123]\nv_mfma_f32_16x16x32_bf16 v[124:127], a[108:111], v[52:55], v[124:127]\n"
"ds_read_b128 v[52:55], v35 offset:0\ns_add_u32 s52, s51, s32\ns_add_u32 s51, s51, s54\ns_add_u32 s49, s49, 28672\n"
"s_cmp_lt_u32 s49, 1949696\ns_cbranch_scc1 fg0_loop\ns_nop 7\nv_cvt_pk_bf16_f32 v56, v96, v97\n"
"v_cvt_pk_bf16_f32 v57, v98, v99\nv_cvt_pk_bf16_f32 v58, v100, v101\nv_cvt_pk_bf16_f32 v59, v102, v103\ns_nop 0\n"
"s_nop 0\nbuffer_store_dwordx4 v[56:59], v15, s[20:23], s50 offen sc1\nv_cvt_pk_bf16_f32 v60, v112, v113\nv_cvt_pk_bf16_f32 v61, v114, v115\n"
"v_cvt_pk_bf16_f32 v62, v116, v117\nv_cvt_pk_bf16_f32 v63, v118, v119\ns_nop 0\ns_nop 0\n"
"buffer_store_dwordx4 v[60:63], v16, s[20:23], s50 offen sc1\nv_cvt_pk_bf16_f32 v56, v104, v105\nv_cvt_pk_bf16_f32 v57, v106, v107\nv_cvt_pk_bf16_f32 v58, v108, v109\n"
"v_cvt_pk_bf16_f32 v59, v110, v111\ns_nop 0\ns_nop 0\nbuffer_store_dwordx4 v[56:59], v17, s[20:23], s50 offen sc1\n"
"v_cvt_pk_bf16_f32 v60, v120, v121\nv_cvt_pk_bf16_f32 v61, v122, v123\nv_cvt_pk_bf16_f32 v62, v124, v125\nv_cvt_pk_bf16_f32 v63, v126, v127\n"
"s_nop 0\ns_nop 0\nbuffer_store_dwordx4 v[60:63], v18, s[20:23], s50 offen sc1\ns_add_u32 s48, s28, 64\n"
"v_mov_b32 v36, s48\ns_waitcnt vmcnt(0) lgkmcnt(0)\ns_barrier\nbuffer_store_dword v36, v19, s[24:27], s33 offen sc1\n"
"fg0_end:\n"
    :  : "{s[8:23]}"(s0), "{s[24:39]}"(s1), "{v[8:23]}"(v0), "{a[128:143]}"(k0), "{a[144:159]}"(k1), "{a[160:175]}"(k2), "{a[176:191]}"(k3) : "v24", "v25", "v26", "v27", "v28", "v29", "v30", "v31", "v32", "v33", "v34", "v35", "v36", "v37", "v38", "v39", "v40", "v41", "v42", "v43", "v44", "v45", "v46", "v47", "v48", "v49", "v50", "v51", "v52", "v53", "v54", "v55", "v56", "v57", "v58", "v59", "v60", "v61", "v62", "v63", "v64", "v65", "v66", "v67", "v68", "v69", "v70", "v71", "v72", "v73", "v74", "v75", "v76", "v77", "v78", "v79", "v80", "v81", "v82", "v83", "v84", "v85", "v86", "v87", "v88", "v89", "v90", "v91", "v92", "v93", "v94", "v95", "v96", "v97", "v98", "v99", "v100", "v101", "v102", "v103", "v104", "v105", "v106", "v107", "v108", "v109", "v110", "v111", "v112", "v113", "v114", "v115", "v116", "v117", "v118", "v119", "v120", "v121", "v122", "v123", "v124", "v125", "v126", "v127", "v128", "v129", "v130", "v131", "v132", "v133", "v134", "v135", "v136", "v137", "v138", "v139", "v140", "v141", "v142", "v143", "v144", "v145", "v146", "v147", "v148", "v149", "v150", "v151", "v152", "v153", "v154", "v155", "v156", "v157", "v158", "v159", "v160", "v161", "v162", "v163", "v164", "v165", "v166", "v167", "v168", "v169", "v170", "v171", "v172", "v173", "v174", "v175", "v176", "v177", "v178", "v179", "v180", "v181", "v182", "v183", "v184", "v185", "v186", "v187", "v188", "v189", "v190", "v191", "v192", "v193", "v194", "v195", "v196", "v197", "v198", "v199", "v200", "v201", "v202", "v203", "v204", "v205", "v206", "v207", "v208", "v209", "v210", "v211", "v212", "v213", "v214", "v215", "v216", "v217", "v218", "v219", "v220", "v221", "v222", "v223", "v224", "v225", "v226", "v227", "v228", "v229", "v230", "v231", "v232", "v233", "v234", "v235", "v236", "v237", "v238", "v239", "a0", "a1", "a2", "a3", "a4", "a5", "a6", "a7", "a8", "a9", "a10", "a11", "a12", "a13", "a14", "a15", "a16", "a17", "a18", "a19", "a20", "a21", "a22", "a23", "a24", "a25", "a26", "a27", "a28", "a29", "a30", "a31", "a32", "a33", "a34", "a35", "a36", "a37", "a38", "a39", "a40", "a41", "a42", "a43", "a44", "a45", "a46", "a47", "a48", "a49", "a50", "a51", "a52", "a53", "a54", "a55", "a56", "a57", "a58", "a59", "a60", "a61", "a62", "a63", "a64", "a65", "a66", "a67", "a68", "a69", "a70", "a71", "a72", "a73", "a74", "a75", "a76", "a77", "a78", "a79", "a80", "a81", "a82", "a83", "a84", "a85", "a86", "a87", "a88", "a89", "a90", "a91", "a92", "a93", "a94", "a95", "a96", "a97", "a98", "a99", "a100", "a101", "a102", "a103", "a104", "a105", "a106", "a107", "a108", "a109", "a110", "a111", "s40", "s41", "s42", "s43", "s44", "s45", "s46", "s47", "s48", "s49", "s50", "s51", "s52", "s53", "s54", "s55", "s56", "s57", "vcc", "scc", "memory");
    return o;
}
DI unsigned gemm_asm1(const GCtx& c, u16v k0, u16v k1, u16v k2, u16v k3) {
    const u16v s0 = {RFL(c.rw1[0]), RFL(c.rw1[1]), RFL(c.rw1[2]), RFL(c.rw1[3]), RFL(c.rx[0]), RFL(c.rx[1]), RFL(c.rx[2]), RFL(c.rx[3]), RFL(c.rexp[0]), RFL(c.rexp[1]), RFL(c.rexp[2]), RFL(c.rexp[3]), RFL(c.rout[0]), RFL(c.rout[1]), RFL(c.rout[2]), RFL(c.rout[3])}, s1 = {RFL(c.rhb[0]), RFL(c.rhb[1]), RFL(c.rhb[2]), RFL(c.rhb[3]), RFL(c.tag), RFL(c.rot), RFL(c.sw1), RFL(c.mw), RFL(c.mx), RFL(c.sfg), RFL(c.stay), RFL(c.csc), RFL(c.ostr), RFL(c.p0), RFL(c.p1), RFL(c.p2)}, v0 = {c.vr0, c.vr1, c.vx, c.wa, c.xa, c.vexp, c.vimp, c.vo0, c.vo1, c.vo2, c.vo3, c.vfl, c.vpf, c.p3, c.p4, c.p5};
    unsigned o = 0;
    asm volatile(
"s_nop 4\ns_add_u32 s40, s29, 0\ns_sub_u32 s53, s40, 7\ns_cmp_ge_u32 s40, 7\n"
"s_cselect_b32 s40, s53, s40\ns_add_u32 s41, s29, 1\ns_sub_u32 s53, s41, 7\ns_cmp_ge_u32 s41, 7\n"
"s_cselect_b32 s41, s53, s41\ns_add_u32 s42, s29, 2\ns_sub_u32 s53, s42, 7\ns_cmp_ge_u32 s42, 7\n"
"s_cselect_b32 s42, s53, s42\ns_add_u32 s43, s29, 3\ns_sub_u32 s53, s43, 7\ns_cmp_ge_u32 s43, 7\n"
"s_cselect_b32 s43, s53, s43\ns_add_u32 s44, s29, 4\ns_sub_u32 s53, s44, 7\ns_cmp_ge_u32 s44, 7\n"
"s_cselect_b32 s44, s53, s44\ns_add_u32 s45, s29, 5\ns_sub_u32 s53, s45, 7\ns_cmp_ge_u32 s45, 7\n"
"s_cselect_b32 s45, s53, s45\ns_add_u32 s46, s29, 6\ns_sub_u32 s53, s46, 7\ns_cmp_ge_u32 s46, 7\n"
"s_cselect_b32 s46, s53, s46\nv_xor_b32 v24, 0, v11\nv_add_u32 v24, 65536, v24\nv_xor_b32 v25, 64, v11\n"
"v_add_u32 v25, 65536, v25\nv_xor_b32 v26, 128, v11\nv_add_u32 v26, 65536, v26\nv_xor_b32 v27, 192, v11\n"
"v_add_u32 v27, 65536, v27\nv_xor_b32 v28, 16, v11\nv_add_u32 v28, 67584, v28\nv_xor_b32 v29, 80, v11\n"
"v_add_u32 v29, 67584, v29\nv_xor_b32 v30, 144, v11\nv_add_u32 v30, 67584, v30\nv_xor_b32 v31, 208, v11\n"
"v_add_u32 v31, 67584, v31\nv_xor_b32 v32, 0, v12\nv_add_u32 v32, 0, v32\nv_xor_b32 v33, 0, v12\n"
"v_add_u32 v33, 512, v33\nv_xor_b32 v34, 64, v12\nv_add_u32 v34, 0, v34\nv_xor_b32 v35, 64, v12\n"
"v_add_u32 v35, 512, v35\ns_mov_b32 s55, 0\ns_waitcnt vmcnt(23)\ns_barrier\n"
"ds_read_b128 v[40:43], v24 offset:0\nds_read_b128 v[44:47], v28 offset:0\nds_read_b128 v[48:51], v25 offset:0\nds_read_b128 v[52:55], v29 offset:0\n"
"s_waitcnt lgkmcnt(3)\nv_mfma_f32_16x16x32_bf16 v[64:67], v[40:43], a[128:131], 0\nv_mfma_f32_16x16x32_bf16 v[68:71], v[40:43], a[160:163], 0\nds_read_b128 v[40:43], v26 offset:0\n"
"s_waitcnt lgkmcnt(3)\nv_mfma_f32_16x16x32_bf16 v[72:75], v[44:47], a[128:131], 0\nv_mfma_f32_16x16x32_bf16 v[76:79], v[44:47], a[160:163], 0\nds_read_b128 v[44:47], v30 offset:0\n"
"s_lshl_b32 s53, s43, 19\ns_add_u32 s53, s53, s30\ns_waitcnt lgkmcnt(3)\nv_mfma_f32_16x16x32_bf16 v[64:67], v[48:51], a[132:135], v[64:67]\n"
"s_add_u32 m0, s31, 32768\ns_add_u32 s53, s53, 0\nv_mfma_f32_16x16x32_bf16 v[68:71], v[48:51], a[164:167], v[68:71]\nds_read_b128 v[48:51], v27 offset:0\n"
"buffer_load_dwordx4 v8, s[8:11], s53 offen lds\ns_waitcnt lgkmcnt(3)\nv_mfma_f32_16x16x32_bf16 v[72:75], v[52:55], a[132:135], v[72:75]\nbuffer_load_dwordx4 v9, s[8:11], s53 offen offset:1024 lds\n"
"v_mfma_f32_16x16x32_bf16 v[76:79], v[52:55], a[164:167], v[76:79]\nds_read_b128 v[52:55], v31 offset:0\ns_lshl_b32 s53, s43, 19\ns_add_u32 s53, s53, s30\n"
"s_waitcnt lgkmcnt(3)\nv_mfma_f32_16x16x32_bf16 v[64:67], v[40:43], a[136:139], v[64:67]\ns_add_u32 m0, s31, 40960\ns_add_u32 s53, s53, 131072\n"
"v_mfma_f32_16x16x32_bf16 v[68:71], v[40:43], a[168:171], v[68:71]\nds_read_b128 v[40:43], v24 offset:256\nbuffer_load_dwordx4 v8, s[8:11], s53 offen lds\ns_waitcnt lgkmcnt(3)\n"
"v_mfma_f32_16x16x32_bf16 v[72:75], v[44:47], a[136:139], v[72:75]\nbuffer_load_dwordx4 v9, s[8:11], s53 offen offset:1024 lds\nv_mfma_f32_16x16x32_bf16 v[76:79], v[44:47], a[168:171], v[76:79]\nds_read_b128 v[44:47], v28 offset:256\n"
"s_waitcnt lgkmcnt(3)\nv_mfma_f32_16x16x32_bf16 v[64:67], v[48:51], a[140:143], v[64:67]\nv_mfma_f32_16x16x32_bf16 v[68:71], v[48:51], a[172:175], v[68:71]\nds_read_b128 v[48:51], v25 offset:256\n"
"s_waitcnt lgkmcnt(3)\nv_mfma_f32_16x16x32_bf16 v[72:75], v[52:55], a[140:143], v[72:75]\nv_mfma_f32_16x16x32_bf16 v[76:79], v[52:55], a[172:175], v[76:79]\nds_read_b128 v[52:55], v29 offset:256\n"
"s_waitcnt lgkmcnt(3)\nv_mfma_f32_16x16x32_bf16 v[64:67], v[40:43], a[144:147], v[64:67]\nv_mfma_f32_16x16x32_bf16 v[68:71], v[40:43], a[176:179], v[68:71]\nds_read_b128 v[40:43], v26 offset:256\n"
"s_waitcnt lgkmcnt(3)\nv_mfma_f32_16x16x32_bf16 v[72:75], v[44:47], a[144:147], v[72:75]\nv_mfma_f32_16x16x32_bf16 v[76:79], v[44:47], a[176:179], v[76:79]\nds_read_b128 v[44:47], v30 offset:256\n"
"s_waitcnt lgkmcnt(3)\nv_mfma_f32_16x16x32_bf16 v[64:67], v[48:51], a[148:151], v[64:67]\nv_mfma_f32_16x16x32_bf16 v[68:71], v[48:51], a[180:183], v[68:71]\nds_read_b128 v[48:51], v27 offset:256\n"
"s_waitcnt lgkmcnt(3)\nv_mfma_f32_16x16x32_bf16 v[72:75], v[52:55], a[148:151], v[72:75]\nv_mfma_f32_16x16x32_bf16 v[76:79], v[52:55], a[180:183], v[76:79]\nds_read_b128 v[52:55], v31 offset:256\n"
"s_waitcnt lgkmcnt(3)\nv_mfma_f32_16x16x32_bf16 v[64:67], v[40:43], a[152:155], v[64:67]\nv_mfma_f32_16x16x32_bf16 v[68:71], v[40:43], a[184:187], v[68:71]\nds_read_b128 v[40:43], v24 offset:16384\n"
"s_waitcnt lgkmcnt(3)\nv_mfma_f32_16x16x32_bf16 v[72:75], v[44:47], a[152:155], v[72:75]\nv_mfma_f32_16x16x32_bf16 v[76:79], v[44:47], a[184:187], v[76:79]\nds_read_b128 v[44:47], v28 offset:16384\n"
"s_waitcnt lgkmcnt(3)\nv_mfma_f32_16x16x32_bf16 v[64:67], v[48:51], a[156:159], v[64:67]\nv_mfma_f32_16x16x32_bf16 v[68:71], v[48:51], a[188:191], v[68:71]\nds_read_b128 v[48:51], v25 offset:16384\n"
"s_waitcnt lgkmcnt(3)\nv_mfma_f32_16x16x32_bf16 v[72:75], v[52:55], a[156:159], v[72:75]\nv_mfma_f32_16x16x32_bf16 v[76:79], v[52:55], a[188:191], v[76:79]\nds_read_b128 v[52:55], v29 offset:16384\n"
"s_waitcnt lgkmcnt(3)\nv_mfma_f32_16x16x32_bf16 v[80:83], v[40:43], a[128:131], 0\nv_mfma_f32_16x16x32_bf16 v[84:87], v[40:43], a[160:163], 0\nds_read_b128 v[40:43], v26 offset:16384\n"
"s_waitcnt lgkmcnt(3)\nv_mfma_f32_16x16x32_bf16 v[88:91], v[44:47], a[128:131], 0\nv_mfma_f32_16x16x32_bf16 v[92:95], v[44:47], a[160:163], 0\nds_read_b128 v[44:47], v30 offset:16384\n"
"v_cvt_pk_bf16_f32 v128, v64, v65\nv_cvt_pk_bf16_f32 v129, v66, v67\ns_waitcnt lgkmcnt(3)\nv_mfma_f32_16x16x32_bf16 v[80:83], v[48:51], a[132:135], v[80:83]\n"
"v_cvt_pk_bf16_f32 v130, v72, v73\nv_cvt_pk_bf16_f32 v131, v74, v75\nv_mfma_f32_16x16x32_bf16 v[84:87], v[48:51], a[164:167], v[84:87]\nds_read_b128 v[48:51], v27 offset:16384\n"
"v_cvt_pk_bf16_f32 v132, v68, v69\nv_cvt_pk_bf16_f32 v133, v70, v71\ns_waitcnt lgkmcnt(3)\nv_mfma_f32_16x16x32_bf16 v[88:91], v[52:55], a[132:135], v[88:91]\n"
"v_cvt_pk_bf16_f32 v134, v76, v77\nv_cvt_pk_bf16_f32 v135, v78, v79\nv_mfma_f32_16x16x32_bf16 v[92:95], v[52:55], a[164:167], v[92:95]\nds_read_b128 v[52:55], v31 offset:16384\n"
"s_lshl_b32 s54, s40, 11\ns_add_u32 s54, s54, 0\ns_waitcnt lgkmcnt(3)\nv_mfma_f32_16x16x32_bf16 v[80:83], v[40:43], a[136:139], v[80:83]\n"
"buffer_store_dwordx4 v[128:131], v13, s[16:19], s54 offen sc1\nv_mfma_f32_16x16x32_bf16 v[84:87], v[40:43], a[168:171], v[84:87]\nds_read_b128 v[40:43], v24 offset:16640\ns_add_u32 s54, s54, 229376\n"
"s_waitcnt lgkmcnt(3)\nv_mfma_f32_16x16x32_bf16 v[88:91], v[44:47], a[136:139], v[88:91]\nbuffer_store_dwordx4 v[132:135], v13, s[16:19], s54 offen sc1\nv_mfma_f32_16x16x32_bf16 v[92:95], v[44:47], a[168:171], v[92:95]\n"
"ds_read_b128 v[44:47], v28 offset:16640\ns_lshl_b32 s53, s43, 19\ns_add_u32 s53, s53, s30\ns_waitcnt lgkmcnt(3)\n"
"v_mfma_f32_16x16x32_bf16 v[80:83], v[48:51], a[140:143], v[80:83]\ns_add_u32 m0, s31, 49152\ns_add_u32 s53, s53, 262144\nv_mfma_f32_16x16x32_bf16 v[84:87], v[48:51], a[172:175], v[84:87]\n"
"ds_read_b128 v[48:51], v25 offset:16640\nbuffer_load_dwordx4 v8, s[8:11], s53 offen lds\ns_waitcnt lgkmcnt(3)\nv_mfma_f32_16x16x32_bf16 v[88:91], v[52:55], a[140:143], v[88:91]\n"
"buffer_load_dwordx4 v9, s[8:11], s53 offen offset:1024 lds\nv_mfma_f32_16x16x32_bf16 v[92:95], v[52:55], a[172:175], v[92:95]\nds_read_b128 v[52:55], v29 offset:16640\ns_lshl_b32 s53, s43, 19\n"
"s_add_u32 s53, s53, s30\ns_waitcnt lgkmcnt(3)\nv_mfma_f32_16x16x32_bf16 v[80:83], v[40:43], a[144:147], v[80:83]\ns_add_u32 m0, s31, 57344\n"
"s_add_u32 s53, s53, 393216\nv_mfma_f32_16x16x32_bf16 v[84:87], v[40:43], a[176:179], v[84:87]\nds_read_b128 v[40:43], v26 offset:16640\nbuffer_load_dwordx4 v8, s[8:11], s53 offen lds\n"
"s_waitcnt lgkmcnt(3)\nv_mfma_f32_16x16x32_bf16 v[88:91], v[44:47], a[144:147], v[88:91]\nbuffer_load_dwordx4 v9, s[8:11], s53 offen offset:1024 lds\nv_mfma_f32_16x16x32_bf16 v[92:95], v[44:47], a[176:179], v[92:95]\n"
"ds_read_b128 v[44:47], v30 offset:16640\ns_waitcnt lgkmcnt(3)\nv_mfma_f32_16x16x32_bf16 v[80:83], v[48:51], a[148:151], v[80:83]\nv_mfma_f32_16x16x32_bf16 v[84:87], v[48:51], a[180:183], v[84:87]\n"
"ds_read_b128 v[48:51], v27 offset:16640\ns_waitcnt lgkmcnt(3)\nv_mfma_f32_16x16x32_bf16 v[88:91], v[52:55], a[148:151], v[88:91]\nv_mfma_f32_16x16x32_bf16 v[92:95], v[52:55], a[180:183], v[92:95]\n"
"ds_read_b128 v[52:55], v31 offset:16640\ns_waitcnt lgkmcnt(3)\nv_mfma_f32_16x16x32_bf16 v[80:83], v[40:43], a[152:155], v[80:83]\nv_mfma_f32_16x16x32_bf16 v[84:87], v[40:43], a[184:187], v[84:87]\n"
"s_waitcnt lgkmcnt(2)\nv_mfma_f32_16x16x32_bf16 v[88:91], v[44:47], a[152:155], v[88:91]\nv_mfma_f32_16x16x32_bf16 v[92:95], v[44:47], a[184:187], v[92:95]\ns_waitcnt lgkmcnt(1)\n"
"v_mfma_f32_16x16x32_bf16 v[80:83], v[48:51], a[156:159], v[80:83]\nv_mfma_f32_16x16x32_bf16 v[84:87], v[48:51], a[188:191], v[84:87]\ns_waitcnt lgkmcnt(0)\nv_mfma_f32_16x16x32_bf16 v[88:91], v[52:55], a[156:159], v[88:91]\n"
"v_mfma_f32_16x16x32_bf16 v[92:95], v[52:55], a[188:191], v[92:95]\nv_add_u32 v24, 32768, v24\nv_add_u32 v25, 32768, v25\nv_add_u32 v26, 32768, v26\n"
"v_add_u32 v27, 32768, v27\nv_add_u32 v28, 32768, v28\nv_add_u32 v29, 32768, v29\nv_add_u32 v30, 32768, v30\n"
"v_add_u32 v31, 32768, v31\ns_waitcnt vmcnt(25)\ns_barrier\nds_read_b128 v[40:43], v24 offset:0\n"
"ds_read_b128 v[44:47], v28 offset:0\nds_read_b128 v[48:51], v25 offset:0\nds_read_b128 v[52:55], v29 offset:0\ns_waitcnt lgkmcnt(3)\n"
"v_mfma_f32_16x16x32_bf16 v[64:67], v[40:43], a[128:131], 0\nv_mfma_f32_16x16x32_bf16 v[68:71], v[40:43], a[160:163], 0\nds_read_b128 v[40:43], v26 offset:0\ns_waitcnt lgkmcnt(3)\n"
"v_mfma_f32_16x16x32_bf16 v[72:75], v[44:47], a[128:131], 0\nv_mfma_f32_16x16x32_bf16 v[76:79], v[44:47], a[160:163], 0\nds_read_b128 v[44:47], v30 offset:0\nv_cvt_pk_bf16_f32 v136, v80, v81\n"
"v_cvt_pk_bf16_f32 v137, v82, v83\ns_waitcnt lgkmcnt(3)\nv_mfma_f32_16x16x32_bf16 v[64:67], v[48:51], a[132:135], v[64:67]\nv_cvt_pk_bf16_f32 v138, v88, v89\n"
"v_cvt_pk_bf16_f32 v139, v90, v91\nv_mfma_f32_16x16x32_bf16 v[68:71], v[48:51], a[164:167], v[68:71]\nds_read_b128 v[48:51], v27 offset:0\nv_cvt_pk_bf16_f32 v140, v84, v85\n"
"v_cvt_pk_bf16_f32 v141, v86, v87\ns_waitcnt lgkmcnt(3)\nv_mfma_f32_16x16x32_bf16 v[72:75], v[52:55], a[132:135], v[72:75]\nv_cvt_pk_bf16_f32 v142, v92, v93\n"
"v_cvt_pk_bf16_f32 v143, v94, v95\nv_mfma_f32_16x16x32_bf16 v[76:79], v[52:55], a[164:167], v[76:79]\nds_read_b128 v[52:55], v31 offset:0\ns_lshl_b32 s54, s40, 11\n"
"s_add_u32 s54, s54, 1024\ns_waitcnt lgkmcnt(3)\nv_mfma_f32_16x16x32_bf16 v[64:67], v[40:43], a[136:139], v[64:67]\nbuffer_store_dwordx4 v[136:139], v13, s[16:19], s54 offen sc1\n"
"v_mfma_f32_16x16x32_bf16 v[68:71], v[40:43], a[168:171], v[68:71]\nds_read_b128 v[40:43], v24 offset:256\ns_add_u32 s54, s54, 229376\ns_waitcnt lgkmcnt(3)\n"
"v_mfma_f32_16x16x32_bf16 v[72:75], v[44:47], a[136:139], v[72:75]\nbuffer_store_dwordx4 v[140:143], v13, s[16:19], s54 offen sc1\nv_mfma_f32_16x16x32_bf16 v[76:79], v[44:47], a[168:171], v[76:79]\nds_read_b128 v[44:47], v28 offset:256\n"
"s_lshl_b32 s53, s44, 19\ns_add_u32 s53, s53, s30\ns_waitcnt lgkmcnt(3)\nv_mfma_f32_16x16x32_bf16 v[64:67], v[48:51], a[140:143], v[64:67]\n"
"s_add_u32 m0, s31, 65536\ns_add_u32 s53, s53, 0\nv_mfma_f32_16x16x32_bf16 v[68:71], v[48:51], a[172:175], v[68:71]\nds_read_b128 v[48:51], v25 offset:256\n"
"buffer_load_dwordx4 v8, s[8:11], s53 offen lds\ns_waitcnt lgkmcnt(3)\nv_mfma_f32_16x16x32_bf16 v[72:75], v[52:55], a[140:143], v[72:75]\nbuffer_load_dwordx4 v9, s[8:11], s53 offen offset:1024 lds\n"
"v_mfma_f32_16x16x32_bf16 v[76:79], v[52:55], a[172:175], v[76:79]\nds_read_b128 v[52:55], v29 offset:256\ns_lshl_b32 s53, s44, 19\ns_add_u32 s53, s53, s30\n"
"s_waitcnt lgkmcnt(3)\nv_mfma_f32_16x16x32_bf16 v[64:67], v[40:43], a[144:147], v[64:67]\ns_add_u32 m0, s31, 73728\ns_add_u32 s53, s53, 131072\n"
"v_mfma_f32_16x16x32_bf16 v[68:71], v[40:43], a[176:179], v[68:71]\nds_read_b128 v[40:43], v26 offset:256\nbuffer_load_dwordx4 v8, s[8:11], s53 offen lds\ns_waitcnt lgkmcnt(3)\n"
"v_mfma_f32_16x16x32_bf16 v[72:75], v[44:47], a[144:147], v[72:75]\nbuffer_load_dwordx4 v9, s[8:11], s53 offen offset:1024 lds\nv_mfma_f32_16x16x32_bf16 v[76:79], v[44:47], a[176:179], v[76:79]\nds_read_b128 v[44:47], v30 offset:256\n"
"s_waitcnt lgkmcnt(3)\nv_mfma_f32_16x16x32_bf16 v[64:67], v[48:51], a[148:151], v[64:67]\nv_mfma_f32_16x16x32_bf16 v[68:71], v[48:51], a[180:183], v[68:71]\nds_read_b128 v[48:51], v27 offset:256\n"
"s_waitcnt lgkmcnt(3)\nv_mfma_f32_16x16x32_bf16 v[72:75], v[52:55], a[148:151], v[72:75]\nv_mfma_f32_16x16x32_bf16 v[76:79], v[52:55], a[180:183], v[76:79]\nds_read_b128 v[52:55], v31 offset:256\n"
"s_waitcnt lgkmcnt(3)\nv_mfma_f32_16x16x32_bf16 v[64:67], v[40:43], a[152:155], v[64:67]\nv_mfma_f32_16x16x32_bf16 v[68:71], v[40:43], a[184:187], v[68:71]\nds_read_b128 v[40:43], v24 offset:16384\n"
"s_waitcnt lgkmcnt(3)\nv_mfma_f32_16x16x32_bf16 v[72:75], v[44:47], a[152:155], v[72:75]\nv_mfma_f32_16x16x32_bf16 v[76:79], v[44:47], a[184:187], v[76:79]\nds_read_b128 v[44:47], v28 offset:16384\n"
"s_waitcnt lgkmcnt(3)\nv_mfma_f32_16x16x32_bf16 v[64:67], v[48:51], a[156:159], v[64:67]\nv_mfma_f32_16x16x32_bf16 v[68:71], v[48:51], a[188:191], v[68:71]\nds_read_b128 v[48:51], v25 offset:16384\n"
"s_waitcnt lgkmcnt(3)\nv_mfma_f32_16x16x32_bf16 v[72:75], v[52:55], a[156:159], v[72:75]\nv_mfma_f32_16x16x32_bf16 v[76:79], v[52:55], a[188:191], v[76:79]\nds_read_b128 v[52:55], v29 offset:16384\n"
"s_waitcnt lgkmcnt(3)\nv_mfma_f32_16x16x32_bf16 v[80:83], v[40:43], a[128:131], 0\nv_mfma_f32_16x16x32_bf16 v[84:87], v[40:43], a[160:163], 0\nds_read_b128 v[40:43], v26 offset:16384\n"
"s_waitcnt lgkmcnt(3)\nv_mfma_f32_16x16x32_bf16 v[88:91], v[44:47], a[128:131], 0\nv_mfma_f32_16x16x32_bf16 v[92:95], v[44:47], a[160:163], 0\nds_read_b128 v[44:47], v30 offset:16384\n"
"v_cvt_pk_bf16_f32 v144, v64, v65\nv_cvt_pk_bf16_f32 v145, v66, v67\ns_waitcnt lgkmcnt(3)\nv_mfma_f32_16x16x32_bf16 v[80:83], v[48:51], a[132:135], v[80:83]\n"
"v_cvt_pk_bf16_f32 v146, v72, v73\nv_cvt_pk_bf16_f32 v147, v74, v75\nv_mfma_f32_16x16x32_bf16 v[84:87], v[48:51], a[164:167], v[84:87]\nds_read_b128 v[48:51], v27 offset:16384\n"
"v_cvt_pk_bf16_f32 v148, v68, v69\nv_cvt_pk_bf16_f32 v149, v70, v71\ns_waitcnt lgkmcnt(3)\nv_mfma_f32_16x16x32_bf16 v[88:91], v[52:55], a[132:135], v[88:91]\n"
"v_cvt_pk_bf16_f32 v150, v76, v77\nv_cvt_pk_bf16_f32 v151, v78, v79\nv_mfma_f32_16x16x32_bf16 v[92:95], v[52:55], a[164:167], v[92:95]\nds_read_b128 v[52:55], v31 offset:16384\n"
"s_lshl_b32 s54, s41, 11\ns_add_u32 s54, s54, 0\ns_waitcnt lgkmcnt(3)\nv_mfma_f32_16x16x32_bf16 v[80:83], v[40:43], a[136:139], v[80:83]\n"
"buffer_store_dwordx4 v[144:147], v13, s[16:19], s54 offen sc1\nv_mfma_f32_16x16x32_bf16 v[84:87], v[40:43], a[168:171], v[84:87]\nds_read_b128 v[40:43], v24 offset:16640\ns_add_u32 s54, s54, 229376\n"
"s_waitcnt lgkmcnt(3)\nv_mfma_f32_16x16x32_bf16 v[88:91], v[44:47], a[136:139], v[88:91]\nbuffer_store_dwordx4 v[148:151], v13, s[16:19], s54 offen sc1\nv_mfma_f32_16x16x32_bf16 v[92:95], v[44:47], a[168:171], v[92:95]\n"
"ds_read_b128 v[44:47], v28 offset:16640\ns_lshl_b32 s53, s44, 19\ns_add_u32 s53, s53, s30\ns_waitcnt lgkmcnt(3)\n"
"v_mfma_f32_16x16x32_bf16 v[80:83], v[48:51], a[140:143], v[80:83]\ns_add_u32 m0, s31, 81920\ns_add_u32 s53, s53, 262144\nv_mfma_f32_16x16x32_bf16 v[84:87], v[48:51], a[172:175], v[84:87]\n"
"ds_read_b128 v[48:51], v25 offset:16640\nbuffer_load_dwordx4 v8, s[8:11], s53 offen lds\ns_waitcnt lgkmcnt(3)\nv_mfma_f32_16x16x32_bf16 v[88:91], v[52:55], a[140:143], v[88:91]\n"
"buffer_load_dwordx4 v9, s[8:11], s53 offen offset:1024 lds\nv_mfma_f32_16x16x32_bf16 v[92:95], v[52:55], a[172:175], v[92:95]\nds_read_b128 v[52:55], v29 offset:16640\ns_lshl_b32 s53, s44, 19\n"
"s_add_u32 s53, s53, s30\ns_waitcnt lgkmcnt(3)\nv_mfma_f32_16x16x32_bf16 v[80:83], v[40:43], a[144:147], v[80:83]\ns_add_u32 m0, s31, 90112\n"
"s_add_u32 s53, s53, 393216\nv_mfma_f32_16x16x32_bf16 v[84:87], v[40:43], a[176:179], v[84:87]\nds_read_b128 v[40:43], v26 offset:16640\nbuffer_load_dwordx4 v8, s[8:11], s53 offen lds\n"
"s_waitcnt lgkmcnt(3)\nv_mfma_f32_16x16x32_bf16 v[88:91], v[44:47], a[144:147], v[88:91]\nbuffer_load_dwordx4 v9, s[8:11], s53 offen offset:1024 lds\nv_mfma_f32_16x16x32_bf16 v[92:95], v[44:47], a[176:179], v[92:95]\n"
"ds_read_b128 v[44:47], v30 offset:16640\ns_waitcnt lgkmcnt(3)\nv_mfma_f32_16x16x32_bf16 v[80:83], v[48:51], a[148:151], v[80:83]\nv_mfma_f32_16x16x32_bf16 v[84:87], v[48:51], a[180:183], v[84:87]\n"
"ds_read_b128 v[48:51], v27 offset:16640\ns_waitcnt lgkmcnt(3)\nv_mfma_f32_16x16x32_bf16 v[88:91], v[52:55], a[148:151], v[88:91]\nv_mfma_f32_16x16x32_bf16 v[92:95], v[52:55], a[180:183], v[92:95]\n"
"ds_read_b128 v[52:55], v31 offset:16640\ns_waitcnt lgkmcnt(3)\nv_mfma_f32_16x16x32_bf16 v[80:83], v[40:43], a[152:155], v[80:83]\nv_mfma_f32_16x16x32_bf16 v[84:87], v[40:43], a[184:187], v[84:87]\n"
"s_waitcnt lgkmcnt(2)\nv_mfma_f32_16x16x32_bf16 v[88:91], v[44:47], a[152:155], v[88:91]\nv_mfma_f32_16x16x32_bf16 v[92:95], v[44:47], a[184:187], v[92:95]\ns_waitcnt lgkmcnt(1)\n"
"v_mfma_f32_16x16x32_bf16 v[80:83], v[48:51], a[156:159], v[80:83]\nv_mfma_f32_16x16x32_bf16 v[84:87], v[48:51], a[188:191], v[84:87]\ns_waitcnt lgkmcnt(0)\nv_mfma_f32_16x16x32_bf16 v[88:91], v[52:55], a[156:159], v[88:91]\n"
"v_mfma_f32_16x16x32_bf16 v[92:95], v[52:55], a[188:191], v[92:95]\nv_add_u32 v24, 32768, v24\nv_add_u32 v25, 32768, v25\nv_add_u32 v26, 32768, v26\n"
"v_add_u32 v27, 32768, v27\nv_add_u32 v28, 32768, v28\nv_add_u32 v29, 32768, v29\nv_add_u32 v30, 32768, v30\n"
"v_add_u32 v31, 32768, v31\ns_waitcnt vmcnt(22)\ns_barrier\nds_read_b128 v[40:43], v24 offset:0\n"
"ds_read_b128 v[44:47], v28 offset:0\nds_read_b128 v[48:51], v25 offset:0\nds_read_b128 v[52:55], v29 offset:0\ns_waitcnt lgkmcnt(3)\n"
"v_mfma_f32_16x16x32_bf16 v[64:67], v[40:43], a[128:131], 0\nv_mfma_f32_16x16x32_bf16 v[68:71], v[40:43], a[160:163], 0\nds_read_b128 v[40:43], v26 offset:0\ns_waitcnt lgkmcnt(3)\n"
"v_mfma_f32_16x16x32_bf16 v[72:75], v[44:47], a[128:131], 0\nv_mfma_f32_16x16x32_bf16 v[76:79], v[44:47], a[160:163], 0\nds_read_b128 v[44:47], v30 offset:0\nv_cvt_pk_bf16_f32 v152, v80, v81\n"
"v_cvt_pk_bf16_f32 v153, v82, v83\ns_waitcnt lgkmcnt(3)\nv_mfma_f32_16x16x32_bf16 v[64:67], v[48:51], a[132:135], v[64:67]\nv_cvt_pk_bf16_f32 v154, v88, v89\n"
"v_cvt_pk_bf16_f32 v155, v90, v91\nv_mfma_f32_16x16x32_bf16 v[68:71], v[48:51], a[164:167], v[68:71]\nds_read_b128 v[48:51], v27 offset:0\nv_cvt_pk_bf16_f32 v156, v84, v85\n"
"v_cvt_pk_bf16_f32 v157, v86, v87\ns_waitcnt lgkmcnt(3)\nv_mfma_f32_16x16x32_bf16 v[72:75], v[52:55], a[132:135], v[72:75]\nv_cvt_pk_bf16_f32 v158, v92, v93\n"
"v_cvt_pk_bf16_f32 v159, v94, v95\nv_mfma_f32_16x16x32_bf16 v[76:79], v[52:55], a[164:167], v[76:79]\nds_read_b128 v[52:55], v31 offset:0\ns_lshl_b32 s54, s41, 11\n"
"s_add_u32 s54, s54, 1024\ns_waitcnt lgkmcnt(3)\nv_mfma_f32_16x16x32_bf16 v[64:67], v[40:43], a[136:139], v[64:67]\nbuffer_store_dwordx4 v[152:155], v13, s[16:19], s54 offen sc1\n"
"v_mfma_f32_16x16x32_bf16 v[68:71], v[40:43], a[168:171], v[68:71]\nds_read_b128 v[40:43], v24 offset:256\ns_add_u32 s54, s54, 229376\ns_waitcnt lgkmcnt(3)\n"
"v_mfma_f32_16x16x32_bf16 v[72:75], v[44:47], a[136:139], v[72:75]\nbuffer_store_dwordx4 v[156:159], v13, s[16:19], s54 offen sc1\nv_mfma_f32_16x16x32_bf16 v[76:79], v[44:47], a[168:171], v[76:79]\nds_read_b128 v[44:47], v28 offset:256\n"
"s_lshl_b32 s53, s45, 19\ns_add_u32 s53, s53, s30\ns_waitcnt lgkmcnt(3)\nv_mfma_f32_16x16x32_bf16 v[64:67], v[48:51], a[140:143], v[64:67]\n"
"s_add_u32 m0, s31, 98304\ns_add_u32 s53, s53, 0\nv_mfma_f32_16x16x32_bf16 v[68:71], v[48:51], a[172:175], v[68:71]\nds_read_b128 v[48:51], v25 offset:256\n"
"buffer_load_dwordx4 v8, s[8:11], s53 offen lds\ns_waitcnt lgkmcnt(3)\nv_mfma_f32_16x16x32_bf16 v[72:75], v[52:55], a[140:143], v[72:75]\nbuffer_load_dwordx4 v9, s[8:11], s53 offen offset:1024 lds\n"
"v_mfma_f32_16x16x32_bf16 v[76:79], v[52:55], a[172:175], v[76:79]\nds_read_b128 v[52:55], v29 offset:256\ns_lshl_b32 s53, s45, 19\ns_add_u32 s53, s53, s30\n"
"s_waitcnt lgkmcnt(3)\nv_mfma_f32_16x16x32_bf16 v[64:67], v[40:43], a[144:147], v[64:67]\ns_add_u32 m0, s31, 106496\ns_add_u32 s53, s53, 131072\n"
"v_mfma_f32_16x16x32_bf16 v[68:71], v[40:43], a[176:179], v[68:71]\nds_read_b128 v[40:43], v26 offset:256\nbuffer_load_dwordx4 v8, s[8:11], s53 offen lds\ns_waitcnt lgkmcnt(3)\n"
"v_mfma_f32_16x16x32_bf16 v[72:75], v[44:47], a[144:147], v[72:75]\nbuffer_load_dwordx4 v9, s[8:11], s53 offen offset:1024 lds\nv_mfma_f32_16x16x32_bf16 v[76:79], v[44:47], a[176:179], v[76:79]\nds_read_b128 v[44:47], v30 offset:256\n"
"s_waitcnt lgkmcnt(3)\nv_mfma_f32_16x16x32_bf16 v[64:67], v[48:51], a[148:151], v[64:67]\nv_mfma_f32_16x16x32_bf16 v[68:71], v[48:51], a[180:183], v[68:71]\nds_read_b128 v[48:51], v27 offset:256\n"
"s_waitcnt lgkmcnt(3)\nv_mfma_f32_16x16x32_bf16 v[72:75], v[52:55], a[148:151], v[72:75]\nv_mfma_f32_16x16x32_bf16 v[76:79], v[52:55], a[180:183], v[76:79]\nds_read_b128 v[52:55], v31 offset:256\n"
"s_waitcnt lgkmcnt(3)\nv_mfma_f32_16x16x32_bf16 v[64:67], v[40:43], a[152:155], v[64:67]\nv_mfma_f32_16x16x32_bf16 v[68:71], v[40:43], a[184:187], v[68:71]\nds_read_b128 v[40:43], v24 offset:16384\n"
"s_waitcnt lgkmcnt(3)\nv_mfma_f32_16x16x32_bf16 v[72:75], v[44:47], a[152:155], v[72:75]\nv_mfma_f32_16x16x32_bf16 v[76:79], v[44:47], a[184:187], v[76:79]\nds_read_b128 v[44:47], v28 offset:16384\n"
"s_waitcnt lgkmcnt(3)\nv_mfma_f32_16x16x32_bf16 v[64:67], v[48:51], a[156:159], v[64:67]\nv_mfma_f32_16x16x32_bf16 v[68:71], v[48:51], a[188:191], v[68:71]\nds_read_b128 v[48:51], v25 offset:16384\n"
"s_waitcnt lgkmcnt(3)\nv_mfma_f32_16x16x32_bf16 v[72:75], v[52:55], a[156:159], v[72:75]\nv_mfma_f32_16x16x32_bf16 v[76:79], v[52:55], a[188:191], v[76:79]\nds_read_b128 v[52:55], v29 offset:16384\n"
"s_waitcnt lgkmcnt(3)\nv_mfma_f32_16x16x32_bf16 v[80:83], v[40:43], a[128:131], 0\nv_mfma_f32_16x16x32_bf16 v[84:87], v[40:43], a[160:163], 0\nds_read_b128 v[40:43], v26 offset:16384\n"
"s_waitcnt lgkmcnt(3)\nv_mfma_f32_16x16x32_bf16 v[88:91], v[44:47], a[128:131], 0\nv_mfma_f32_16x16x32_bf16 v[92:95], v[44:47], a[160:163], 0\nds_read_b128 v[44:47], v30 offset:16384\n"
"v_cvt_pk_bf16_f32 v160, v64, v65\nv_cvt_pk_bf16_f32 v161, v66, v67\ns_waitcnt lgkmcnt(3)\nv_mfma_f32_16x16x32_bf16 v[80:83], v[48:51], a[132:135], v[80:83]\n"
"v_cvt_pk_bf16_f32 v162, v72, v73\nv_cvt_pk_bf16_f32 v163, v74, v75\nv_mfma_f32_16x16x32_bf16 v[84:87], v[48:51], a[164:167], v[84:87]\nds_read_b128 v[48:51], v27 offset:16384\n"
"v_cvt_pk_bf16_f32 v164, v68, v69\nv_cvt_pk_bf16_f32 v165, v70, v71\ns_waitcnt lgkmcnt(3)\nv_mfma_f32_16x16x32_bf16 v[88:91], v[52:55], a[132:135], v[88:91]\n"
"v_cvt_pk_bf16_f32 v166, v76, v77\nv_cvt_pk_bf16_f32 v167, v78, v79\nv_mfma_f32_16x16x32_bf16 v[92:95], v[52:55], a[164:167], v[92:95]\nds_read_b128 v[52:55], v31 offset:16384\n"
"s_lshl_b32 s54, s42, 11\ns_add_u32 s54, s54, 0\ns_waitcnt lgkmcnt(3)\nv_mfma_f32_16x16x32_bf16 v[80:83], v[40:43], a[136:139], v[80:83]\n"
"buffer_store_dwordx4 v[160:163], v13, s[16:19], s54 offen sc1\nv_mfma_f32_16x16x32_bf16 v[84:87], v[40:43], a[168:171], v[84:87]\nds_read_b128 v[40:43], v24 offset:16640\ns_add_u32 s54, s54, 229376\n"
"s_waitcnt lgkmcnt(3)\nv_mfma_f32_16x16x32_bf16 v[88:91], v[44:47], a[136:139], v[88:91]\nbuffer_store_dwordx4 v[164:167], v13, s[16:19], s54 offen sc1\nv_mfma_f32_16x16x32_bf16 v[92:95], v[44:47], a[168:171], v[92:95]\n"
"ds_read_b128 v[44:47], v28 offset:16640\ns_lshl_b32 s53, s45, 19\ns_add_u32 s53, s53, s30\ns_waitcnt lgkmcnt(3)\n"
"v_mfma_f32_16x16x32_bf16 v[80:83], v[48:51], a[140:143], v[80:83]\ns_add_u32 m0, s31, 114688\ns_add_u32 s53, s53, 262144\nv_mfma_f32_16x16x32_bf16 v[84:87], v[48:51], a[172:175], v[84:87]\n"
"ds_read_b128 v[48:51], v25 offset:16640\nbuffer_load_dwordx4 v8, s[8:11], s53 offen lds\ns_waitcnt lgkmcnt(3)\nv_mfma_f32_16x16x32_bf16 v[88:91], v[52:55], a[140:143], v[88:91]\n"
"buffer_load_dwordx4 v9, s[8:11], s53 offen offset:1024 lds\nv_mfma_f32_16x16x32_bf16 v[92:95], v[52:55], a[172:175], v[92:95]\nds_read_b128 v[52:55], v29 offset:16640\ns_lshl_b32 s53, s45, 19\n"
"s_add_u32 s53, s53, s30\ns_waitcnt lgkmcnt(3)\nv_mfma_f32_16x16x32_bf16 v[80:83], v[40:43], a[144:147], v[80:83]\ns_add_u32 m0, s31, 122880\n"
"s_add_u32 s53, s53, 393216\nv_mfma_f32_16x16x32_bf16 v[84:87], v[40:43], a[176:179], v[84:87]\nds_read_b128 v[40:43], v26 offset:16640\nbuffer_load_dwordx4 v8, s[8:11], s53 offen lds\n"
"s_waitcnt lgkmcnt(3)\nv_mfma_f32_16x16x32_bf16 v[88:91], v[44:47], a[144:147], v[88:91]\nbuffer_load_dwordx4 v9, s[8:11], s53 offen offset:1024 lds\nv_mfma_f32_16x16x32_bf16 v[92:95], v[44:47], a[176:179], v[92:95]\n"
"ds_read_b128 v[44:47], v30 offset:16640\ns_waitcnt lgkmcnt(3)\nv_mfma_f32_16x16x32_bf16 v[80:83], v[48:51], a[148:151], v[80:83]\nv_mfma_f32_16x16x32_bf16 v[84:87], v[48:51], a[180:183], v[84:87]\n"
"ds_read_b128 v[48:51], v27 offset:16640\ns_waitcnt lgkmcnt(3)\nv_mfma_f32_16x16x32_bf16 v[88:91], v[52:55], a[148:151], v[88:91]\nv_mfma_f32_16x16x32_bf16 v[92:95], v[52:55], a[180:183], v[92:95]\n"
"ds_read_b128 v[52:55], v31 offset:16640\ns_waitcnt lgkmcnt(3)\nv_mfma_f32_16x16x32_bf16 v[80:83], v[40:43], a[152:155], v[80:83]\nv_mfma_f32_16x16x32_bf16 v[84:87], v[40:43], a[184:187], v[84:87]\n"
"s_waitcnt lgkmcnt(2)\nv_mfma_f32_16x16x32_bf16 v[88:91], v[44:47], a[152:155], v[88:91]\nv_mfma_f32_16x16x32_bf16 v[92:95], v[44:47], a[184:187], v[92:95]\ns_waitcnt lgkmcnt(1)\n"
"v_mfma_f32_16x16x32_bf16 v[80:83], v[48:51], a[156:159], v[80:83]\nv_mfma_f32_16x16x32_bf16 v[84:87], v[48:51], a[188:191], v[84:87]\ns_waitcnt lgkmcnt(0)\nv_mfma_f32_16x16x32_bf16 v[88:91], v[52:55], a[156:159], v[88:91]\n"
"v_mfma_f32_16x16x32_bf16 v[92:95], v[52:55], a[188:191], v[92:95]\nv_add_u32 v24, -98304, v24\nv_add_u32 v25, -98304, v25\nv_add_u32 v26, -98304, v26\n"
"v_add_u32 v27, -98304, v27\nv_add_u32 v28, -98304, v28\nv_add_u32 v29, -98304, v29\nv_add_u32 v30, -98304, v30\n"
"v_add_u32 v31, -98304, v31\ns_waitcnt vmcnt(24)\ns_barrier\nds_read_b128 v[40:43], v24 offset:0\n"
"ds_read_b128 v[44:47], v28 offset:0\nds_read_b128 v[48:51], v25 offset:0\nds_read_b128 v[52:55], v29 offset:0\ns_waitcnt lgkmcnt(3)\n"
"v_mfma_f32_16x16x32_bf16 v[64:67], v[40:43], a[128:131], 0\nv_mfma_f32_16x16x32_bf16 v[68:71], v[40:43], a[160:163], 0\nds_read_b128 v[40:43], v26 offset:0\ns_waitcnt lgkmcnt(3)\n"
"v_mfma_f32_16x16x32_bf16 v[72:75], v[44:47], a[128:131], 0\nv_mfma_f32_16x16x32_bf16 v[76:79], v[44:47], a[160:163], 0\nds_read_b128 v[44:47], v30 offset:0\nv_cvt_pk_bf16_f32 v168, v80, v81\n"
"v_cvt_pk_bf16_f32 v169, v82, v83\ns_waitcnt lgkmcnt(3)\nv_mfma_f32_16x16x32_bf16 v[64:67], v[48:51], a[132:135], v[64:67]\nv_cvt_pk_bf16_f32 v170, v88, v89\n"
"v_cvt_pk_bf16_f32 v171, v90, v91\nv_mfma_f32_16x16x32_bf16 v[68:71], v[48:51], a[164:167], v[68:71]\nds_read_b128 v[48:51], v27 offset:0\nv_cvt_pk_bf16_f32 v172, v84, v85\n"
"v_cvt_pk_bf16_f32 v173, v86, v87\ns_waitcnt lgkmcnt(3)\nv_mfma_f32_16x16x32_bf16 v[72:75], v[52:55], a[132:135], v[72:75]\nv_cvt_pk_bf16_f32 v174, v92, v93\n"
"v_cvt_pk_bf16_f32 v175, v94, v95\nv_mfma_f32_16x16x32_bf16 v[76:79], v[52:55], a[164:167], v[76:79]\nds_read_b128 v[52:55], v31 offset:0\ns_lshl_b32 s54, s42, 11\n"
"s_add_u32 s54, s54, 1024\ns_waitcnt lgkmcnt(3)\nv_mfma_f32_16x16x32_bf16 v[64:67], v[40:43], a[136:139], v[64:67]\nbuffer_store_dwordx4 v[168:171], v13, s[16:19], s54 offen sc1\n"
"v_mfma_f32_16x16x32_bf16 v[68:71], v[40:43], a[168:171], v[68:71]\nds_read_b128 v[40:43], v24 offset:256\ns_add_u32 s54, s54, 229376\ns_waitcnt lgkmcnt(3)\n"
"v_mfma_f32_16x16x32_bf16 v[72:75], v[44:47], a[136:139], v[72:75]\nbuffer_store_dwordx4 v[172:175], v13, s[16:19], s54 offen sc1\nv_mfma_f32_16x16x32_bf16 v[76:79], v[44:47], a[168:171], v[76:79]\nds_read_b128 v[44:47], v28 offset:256\n"
"s_lshl_b32 s53, s46, 19\ns_add_u32 s53, s53, s30\ns_waitcnt lgkmcnt(3)\nv_mfma_f32_16x16x32_bf16 v[64:67], v[48:51], a[140:143], v[64:67]\n"
"s_add_u32 m0, s31, 131072\ns_add_u32 s53, s53, 0\nv_mfma_f32_16x16x32_bf16 v[68:71], v[48:51], a[172:175], v[68:71]\nds_read_b128 v[48:51], v25 offset:256\n"
"buffer_load_dwordx4 v8, s[8:11], s53 offen lds\ns_waitcnt lgkmcnt(3)\nv_mfma_f32_16x16x32_bf16 v[72:75], v[52:55], a[140:143], v[72:75]\nbuffer_load_dwordx4 v9, s[8:11], s53 offen offset:1024 lds\n"
"v_mfma_f32_16x16x32_bf16 v[76:79], v[52:55], a[172:175], v[76:79]\nds_read_b128 v[52:55], v29 offset:256\ns_lshl_b32 s53, s46, 19\ns_add_u32 s53, s53, s30\n"
"s_waitcnt lgkmcnt(3)\nv_mfma_f32_16x16x32_bf16 v[64:67], v[40:43], a[144:147], v[64:67]\ns_add_u32 m0, s31, 139264\ns_add_u32 s53, s53, 131072\n"
"v_mfma_f32_16x16x32_bf16 v[68:71], v[40:43], a[176:179], v[68:71]\nds_read_b128 v[40:43], v26 offset:256\nbuffer_load_dwordx4 v8, s[8:11], s53 offen lds\ns_waitcnt lgkmcnt(3)\n"
"v_mfma_f32_16x16x32_bf16 v[72:75], v[44:47], a[144:147], v[72:75]\nbuffer_load_dwordx4 v9, s[8:11], s53 offen offset:1024 lds\nv_mfma_f32_16x16x32_bf16 v[76:79], v[44:47], a[176:179], v[76:79]\nds_read_b128 v[44:47], v30 offset:256\n"
"s_waitcnt lgkmcnt(3)\nv_mfma_f32_16x16x32_bf16 v[64:67], v[48:51], a[148:151], v[64:67]\nv_mfma_f32_16x16x32_bf16 v[68:71], v[48:51], a[180:183], v[68:71]\nds_read_b128 v[48:51], v27 offset:256\n"
"s_waitcnt lgkmcnt(3)\nv_mfma_f32_16x16x32_bf16 v[72:75], v[52:55], a[148:151], v[72:75]\nv_mfma_f32_16x16x32_bf16 v[76:79], v[52:55], a[180:183], v[76:79]\nds_read_b128 v[52:55], v31 offset:256\n"
"s_waitcnt lgkmcnt(3)\nv_mfma_f32_16x16x32_bf16 v[64:67], v[40:43], a[152:155], v[64:67]\nv_mfma_f32_16x16x32_bf16 v[68:71], v[40:43], a[184:187], v[68:71]\nds_read_b128 v[40:43], v24 offset:16384\n"
"s_waitcnt lgkmcnt(3)\nv_mfma_f32_16x16x32_bf16 v[72:75], v[44:47], a[152:155], v[72:75]\nv_mfma_f32_16x16x32_bf16 v[76:79], v[44:47], a[184:187], v[76:79]\nds_read_b128 v[44:47], v28 offset:16384\n"
"s_waitcnt lgkmcnt(3)\nv_mfma_f32_16x16x32_bf16 v[64:67], v[48:51], a[156:159], v[64:67]\nv_mfma_f32_16x16x32_bf16 v[68:71], v[48:51], a[188:191], v[68:71]\nds_read_b128 v[48:51], v25 offset:16384\n"
"s_waitcnt lgkmcnt(3)\nv_mfma_f32_16x16x32_bf16 v[72:75], v[52:55], a[156:159], v[72:75]\nv_mfma_f32_16x16x32_bf16 v[76:79], v[52:55], a[188:191], v[76:79]\nds_read_b128 v[52:55], v29 offset:16384\n"
"s_waitcnt lgkmcnt(3)\nv_mfma_f32_16x16x32_bf16 v[80:83], v[40:43], a[128:131], 0\nv_mfma_f32_16x16x32_bf16 v[84:87], v[40:43], a[160:163], 0\nds_read_b128 v[40:43], v26 offset:16384\n"
"s_waitcnt lgkmcnt(3)\nv_mfma_f32_16x16x32_bf16 v[88:91], v[44:47], a[128:131], 0\nv_mfma_f32_16x16x32_bf16 v[92:95], v[44:47], a[160:163], 0\nds_read_b128 v[44:47], v30 offset:16384\n"
"v_cvt_pk_bf16_f32 v176, v64, v65\nv_cvt_pk_bf16_f32 v177, v66, v67\ns_waitcnt lgkmcnt(3)\nv_mfma_f32_16x16x32_bf16 v[80:83], v[48:51], a[132:135], v[80:83]\n"
"v_cvt_pk_bf16_f32 v178, v72, v73\nv_cvt_pk_bf16_f32 v179, v74, v75\nv_mfma_f32_16x16x32_bf16 v[84:87], v[48:51], a[164:167], v[84:87]\nds_read_b128 v[48:51], v27 offset:16384\n"
"v_cvt_pk_bf16_f32 v180, v68, v69\nv_cvt_pk_bf16_f32 v181, v70, v71\ns_waitcnt lgkmcnt(3)\nv_mfma_f32_16x16x32_bf16 v[88:91], v[52:55], a[132:135], v[88:91]\n"
"v_cvt_pk_bf16_f32 v182, v76, v77\nv_cvt_pk_bf16_f32 v183, v78, v79\nv_mfma_f32_16x16x32_bf16 v[92:95], v[52:55], a[164:167], v[92:95]\nds_read_b128 v[52:55], v31 offset:16384\n"
"s_lshl_b32 s54, s43, 11\ns_add_u32 s54, s54, 0\ns_waitcnt lgkmcnt(3)\nv_mfma_f32_16x16x32_bf16 v[80:83], v[40:43], a[136:139], v[80:83]\n"
"buffer_store_dwordx4 v[176:179], v13, s[16:19], s54 offen sc1\nv_mfma_f32_16x16x32_bf16 v[84:87], v[40:43], a[168:171], v[84:87]\nds_read_b128 v[40:43], v24 offset:16640\ns_add_u32 s54, s54, 229376\n"
"s_waitcnt lgkmcnt(3)\nv_mfma_f32_16x16x32_bf16 v[88:91], v[44:47], a[136:139], v[88:91]\nbuffer_store_dwordx4 v[180:183], v13, s[16:19], s54 offen sc1\nv_mfma_f32_16x16x32_bf16 v[92:95], v[44:47], a[168:171], v[92:95]\n"
"ds_read_b128 v[44:47], v28 offset:16640\ns_lshl_b32 s53, s46, 19\ns_add_u32 s53, s53, s30\ns_waitcnt lgkmcnt(3)\n"
"v_mfma_f32_16x16x32_bf16 v[80:83], v[48:51], a[140:143], v[80:83]\ns_add_u32 m0, s31, 147456\ns_add_u32 s53, s53, 262144\nv_mfma_f32_16x16x32_bf16 v[84:87], v[48:51], a[172:175], v[84:87]\n"
"ds_read_b128 v[48:51], v25 offset:16640\nbuffer_load_dwordx4 v8, s[8:11], s53 offen lds\ns_waitcnt lgkmcnt(3)\nv_mfma_f32_16x16x32_bf16 v[88:91], v[52:55], a[140:143], v[88:91]\n"
"buffer_load_dwordx4 v9, s[8:11], s53 offen offset:1024 lds\nv_mfma_f32_16x16x32_bf16 v[92:95], v[52:55], a[172:175], v[92:95]\nds_read_b128 v[52:55], v29 offset:16640\ns_lshl_b32 s53, s46, 19\n"
"s_add_u32 s53, s53, s30\ns_waitcnt lgkmcnt(3)\nv_mfma_f32_16x16x32_bf16 v[80:83], v[40:43], a[144:147], v[80:83]\ns_add_u32 m0, s31, 155648\n"
"s_add_u32 s53, s53, 393216\nv_mfma_f32_16x16x32_bf16 v[84:87], v[40:43], a[176:179], v[84:87]\nds_read_b128 v[40:43], v26 offset:16640\nbuffer_load_dwordx4 v8, s[8:11], s53 offen lds\n"
"s_waitcnt lgkmcnt(3)\nv_mfma_f32_16x16x32_bf16 v[88:91], v[44:47], a[144:147], v[88:91]\nbuffer_load_dwordx4 v9, s[8:11], s53 offen offset:1024 lds\nv_mfma_f32_16x16x32_bf16 v[92:95], v[44:47], a[176:179], v[92:95]\n"
"ds_read_b128 v[44:47], v30 offset:16640\ns_waitcnt lgkmcnt(3)\nv_mfma_f32_16x16x32_bf16 v[80:83], v[48:51], a[148:151], v[80:83]\nv_mfma_f32_16x16x32_bf16 v[84:87], v[48:51], a[180:183], v[84:87]\n"
"ds_read_b128 v[48:51], v27 offset:16640\ns_waitcnt lgkmcnt(3)\nv_mfma_f32_16x16x32_bf16 v[88:91], v[52:55], a[148:151], v[88:91]\nv_mfma_f32_16x16x32_bf16 v[92:95], v[52:55], a[180:183], v[92:95]\n"
"ds_read_b128 v[52:55], v31 offset:16640\ns_waitcnt lgkmcnt(3)\nv_mfma_f32_16x16x32_bf16 v[80:83], v[40:43], a[152:155], v[80:83]\nv_mfma_f32_16x16x32_bf16 v[84:87], v[40:43], a[184:187], v[84:87]\n"
"s_waitcnt lgkmcnt(2)\nv_mfma_f32_16x16x32_bf16 v[88:91], v[44:47], a[152:155], v[88:91]\nv_mfma_f32_16x16x32_bf16 v[92:95], v[44:47], a[184:187], v[92:95]\ns_waitcnt lgkmcnt(1)\n"
"v_mfma_f32_16x16x32_bf16 v[80:83], v[48:51], a[156:159], v[80:83]\nv_mfma_f32_16x16x32_bf16 v[84:87], v[48:51], a[188:191], v[84:87]\ns_waitcnt lgkmcnt(0)\nv_mfma_f32_16x16x32_bf16 v[88:91], v[52:55], a[156:159], v[88:91]\n"
"v_mfma_f32_16x16x32_bf16 v[92:95], v[52:55], a[188:191], v[92:95]\nv_add_u32 v24, 32768, v24\nv_add_u32 v25, 32768, v25\nv_add_u32 v26, 32768, v26\n"
"v_add_u32 v27, 32768, v27\nv_add_u32 v28, 32768, v28\nv_add_u32 v29, 32768, v29\nv_add_u32 v30, 32768, v30\n"
"v_add_u32 v31, 32768, v31\ns_waitcnt vmcnt(24)\ns_barrier\nds_read_b128 v[40:43], v24 offset:0\n"
"ds_read_b128 v[44:47], v28 offset:0\nds_read_b128 v[48:51], v25 offset:0\nds_read_b128 v[52:55], v29 offset:0\ns_waitcnt lgkmcnt(3)\n"
"v_mfma_f32_16x16x32_bf16 v[64:67], v[40:43], a[128:131], 0\nv_mfma_f32_16x16x32_bf16 v[68:71], v[40:43], a[160:163], 0\nds_read_b128 v[40:43], v26 offset:0\ns_waitcnt lgkmcnt(3)\n"
"v_mfma_f32_16x16x32_bf16 v[72:75], v[44:47], a[128:131], 0\nv_mfma_f32_16x16x32_bf16 v[76:79], v[44:47], a[160:163], 0\nds_read_b128 v[44:47], v30 offset:0\nv_cvt_pk_bf16_f32 v184, v80, v81\n"
"v_cvt_pk_bf16_f32 v185, v82, v83\ns_waitcnt lgkmcnt(3)\nv_mfma_f32_16x16x32_bf16 v[64:67], v[48:51], a[132:135], v[64:67]\nv_cvt_pk_bf16_f32 v186, v88, v89\n"
"v_cvt_pk_bf16_f32 v187, v90, v91\nv_mfma_f32_16x16x32_bf16 v[68:71], v[48:51], a[164:167], v[68:71]\nds_read_b128 v[48:51], v27 offset:0\nv_cvt_pk_bf16_f32 v188, v84, v85\n"
"v_cvt_pk_bf16_f32 v189, v86, v87\ns_waitcnt lgkmcnt(3)\nv_mfma_f32_16x16x32_bf16 v[72:75], v[52:55], a[132:135], v[72:75]\nv_cvt_pk_bf16_f32 v190, v92, v93\n"
"v_cvt_pk_bf16_f32 v191, v94, v95\nv_mfma_f32_16x16x32_bf16 v[76:79], v[52:55], a[164:167], v[76:79]\nds_read_b128 v[52:55], v31 offset:0\ns_lshl_b32 s54, s43, 11\n"
"s_add_u32 s54, s54, 1024\ns_waitcnt lgkmcnt(3)\nv_mfma_f32_16x16x32_bf16 v[64:67], v[40:43], a[136:139], v[64:67]\nbuffer_store_dwordx4 v[184:187], v13, s[16:19], s54 offen sc1\n"
"v_mfma_f32_16x16x32_bf16 v[68:71], v[40:43], a[168:171], v[68:71]\nds_read_b128 v[40:43], v24 offset:256\ns_add_u32 s54, s54, 229376\ns_waitcnt lgkmcnt(3)\n"
"v_mfma_f32_16x16x32_bf16 v[72:75], v[44:47], a[136:139], v[72:75]\nbuffer_store_dwordx4 v[188:191], v13, s[16:19], s54 offen sc1\nv_mfma_f32_16x16x32_bf16 v[76:79], v[44:47], a[168:171], v[76:79]\nds_read_b128 v[44:47], v28 offset:256\n"
"s_add_u32 m0, s32, 32768\ns_waitcnt lgkmcnt(3)\nv_mfma_f32_16x16x32_bf16 v[64:67], v[48:51], a[140:143], v[64:67]\ns_lshl_b32 s53, s40, 7\n"
"s_add_u32 s53, s53, 28672\nv_mfma_f32_16x16x32_bf16 v[68:71], v[48:51], a[172:175], v[68:71]\nds_read_b128 v[48:51], v25 offset:256\nbuffer_load_dwordx4 v10, s[12:15], s53 offen lds\n"
"s_waitcnt lgkmcnt(3)\nv_mfma_f32_16x16x32_bf16 v[72:75], v[52:55], a[140:143], v[72:75]\ns_add_u32 m0, s32, 36864\nv_mfma_f32_16x16x32_bf16 v[76:79], v[52:55], a[172:175], v[76:79]\n"
"ds_read_b128 v[52:55], v29 offset:256\ns_lshl_b32 s53, s41, 7\ns_add_u32 s53, s53, 28672\ns_waitcnt lgkmcnt(3)\n"
"v_mfma_f32_16x16x32_bf16 v[64:67], v[40:43], a[144:147], v[64:67]\nbuffer_load_dwordx4 v10, s[12:15], s53 offen lds\nv_mfma_f32_16x16x32_bf16 v[68:71], v[40:43], a[176:179], v[68:71]\nds_read_b128 v[40:43], v26 offset:256\n"
"s_add_u32 m0, s32, 40960\ns_waitcnt lgkmcnt(3)\nv_mfma_f32_16x16x32_bf16 v[72:75], v[44:47], a[144:147], v[72:75]\ns_lshl_b32 s53, s42, 7\n"
"s_add_u32 s53, s53, 28672\nv_mfma_f32_16x16x32_bf16 v[76:79], v[44:47], a[176:179], v[76:79]\nds_read_b128 v[44:47], v30 offset:256\nbuffer_load_dwordx4 v10, s[12:15], s53 offen lds\n"
"s_waitcnt lgkmcnt(3)\nv_mfma_f32_16x16x32_bf16 v[64:67], v[48:51], a[148:151], v[64:67]\nv_mfma_f32_16x16x32_bf16 v[68:71], v[48:51], a[180:183], v[68:71]\nds_read_b128 v[48:51], v27 offset:256\n"
"s_waitcnt lgkmcnt(3)\nv_mfma_f32_16x16x32_bf16 v[72:75], v[52:55], a[148:151], v[72:75]\nv_mfma_f32_16x16x32_bf16 v[76:79], v[52:55], a[180:183], v[76:79]\nds_read_b128 v[52:55], v31 offset:256\n"
"s_waitcnt lgkmcnt(3)\nv_mfma_f32_16x16x32_bf16 v[64:67], v[40:43], a[152:155], v[64:67]\nv_mfma_f32_16x16x32_bf16 v[68:71], v[40:43], a[184:187], v[68:71]\nds_read_b128 v[40:43], v24 offset:16384\n"
"s_waitcnt lgkmcnt(3)\nv_mfma_f32_16x16x32_bf16 v[72:75], v[44:47], a[152:155], v[72:75]\nv_mfma_f32_16x16x32_bf16 v[76:79], v[44:47], a[184:187], v[76:79]\nds_read_b128 v[44:47], v28 offset:16384\n"
"s_waitcnt lgkmcnt(3)\nv_mfma_f32_16x16x32_bf16 v[64:67], v[48:51], a[156:159], v[64:67]\nv_mfma_f32_16x16x32_bf16 v[68:71], v[48:51], a[188:191], v[68:71]\nds_read_b128 v[48:51], v25 offset:16384\n"
"s_waitcnt lgkmcnt(3)\nv_mfma_f32_16x16x32_bf16 v[72:75], v[52:55], a[156:159], v[72:75]\nv_mfma_f32_16x16x32_bf16 v[76:79], v[52:55], a[188:191], v[76:79]\nds_read_b128 v[52:55], v29 offset:16384\n"
"s_waitcnt lgkmcnt(3)\nv_mfma_f32_16x16x32_bf16 v[80:83], v[40:43], a[128:131], 0\nv_mfma_f32_16x16x32_bf16 v[84:87], v[40:43], a[160:163], 0\nds_read_b128 v[40:43], v26 offset:16384\n"
"s_waitcnt lgkmcnt(3)\nv_mfma_f32_16x16x32_bf16 v[88:91], v[44:47], a[128:131], 0\nv_mfma_f32_16x16x32_bf16 v[92:95], v[44:47], a[160:163], 0\nds_read_b128 v[44:47], v30 offset:16384\n"
"v_cvt_pk_bf16_f32 v192, v64, v65\nv_cvt_pk_bf16_f32 v193, v66, v67\ns_waitcnt lgkmcnt(3)\nv_mfma_f32_16x16x32_bf16 v[80:83], v[48:51], a[132:135], v[80:83]\n"
"v_cvt_pk_bf16_f32 v194, v72, v73\nv_cvt_pk_bf16_f32 v195, v74, v75\nv_mfma_f32_16x16x32_bf16 v[84:87], v[48:51], a[164:167], v[84:87]\nds_read_b128 v[48:51], v27 offset:16384\n"
"v_cvt_pk_bf16_f32 v196, v68, v69\nv_cvt_pk_bf16_f32 v197, v70, v71\ns_waitcnt lgkmcnt(3)\nv_mfma_f32_16x16x32_bf16 v[88:91], v[52:55], a[132:135], v[88:91]\n"
"v_cvt_pk_bf16_f32 v198, v76, v77\nv_cvt_pk_bf16_f32 v199, v78, v79\nv_mfma_f32_16x16x32_bf16 v[92:95], v[52:55], a[164:167], v[92:95]\nds_read_b128 v[52:55], v31 offset:16384\n"
"s_lshl_b32 s54, s44, 11\ns_add_u32 s54, s54, 0\ns_waitcnt lgkmcnt(3)\nv_mfma_f32_16x16x32_bf16 v[80:83], v[40:43], a[136:139], v[80:83]\n"
"buffer_store_dwordx4 v[192:195], v13, s[16:19], s54 offen sc1\nv_mfma_f32_16x16x32_bf16 v[84:87], v[40:43], a[168:171], v[84:87]\nds_read_b128 v[40:43], v24 offset:16640\ns_add_u32 s54, s54, 229376\n"
"s_waitcnt lgkmcnt(3)\nv_mfma_f32_16x16x32_bf16 v[88:91], v[44:47], a[136:139], v[88:91]\nbuffer_store_dwordx4 v[196:199], v13, s[16:19], s54 offen sc1\nv_mfma_f32_16x16x32_bf16 v[92:95], v[44:47], a[168:171], v[92:95]\n"
"ds_read_b128 v[44:47], v28 offset:16640\ns_add_u32 m0, s32, 45056\ns_waitcnt lgkmcnt(3)\nv_mfma_f32_16x16x32_bf16 v[80:83], v[48:51], a[140:143], v[80:83]\n"
"s_lshl_b32 s53, s43, 7\ns_add_u32 s53, s53, 28672\nv_mfma_f32_16x16x32_bf16 v[84:87], v[48:51], a[172:175], v[84:87]\nds_read_b128 v[48:51], v25 offset:16640\n"
"buffer_load_dwordx4 v10, s[12:15], s53 offen lds\ns_waitcnt lgkmcnt(3)\nv_mfma_f32_16x16x32_bf16 v[88:91], v[52:55], a[140:143], v[88:91]\ns_add_u32 m0, s32, 49152\n"
"v_mfma_f32_16x16x32_bf16 v[92:95], v[52:55], a[172:175], v[92:95]\nds_read_b128 v[52:55], v29 offset:16640\ns_lshl_b32 s53, s44, 7\ns_add_u32 s53, s53, 28672\n"
"s_waitcnt lgkmcnt(3)\nv_mfma_f32_16x16x32_bf16 v[80:83], v[40:43], a[144:147], v[80:83]\nbuffer_load_dwordx4 v10, s[12:15], s53 offen lds\nv_mfma_f32_16x16x32_bf16 v[84:87], v[40:43], a[176:179], v[84:87]\n"
"ds_read_b128 v[40:43], v26 offset:16640\ns_add_u32 m0, s32, 53248\ns_waitcnt lgkmcnt(3)\nv_mfma_f32_16x16x32_bf16 v[88:91], v[44:47], a[144:147], v[88:91]\n"
"s_lshl_b32 s53, s45, 7\ns_add_u32 s53, s53, 28672\nv_mfma_f32_16x16x32_bf16 v[92:95], v[44:47], a[176:179], v[92:95]\nds_read_b128 v[44:47], v30 offset:16640\n"
"buffer_load_dwordx4 v10, s[12:15], s53 offen lds\ns_waitcnt lgkmcnt(3)\nv_mfma_f32_16x16x32_bf16 v[80:83], v[48:51], a[148:151], v[80:83]\ns_add_u32 m0, s32, 57344\n"
"v_mfma_f32_16x16x32_bf16 v[84:87], v[48:51], a[180:183], v[84:87]\nds_read_b128 v[48:51], v27 offset:16640\ns_lshl_b32 s53, s46, 7\ns_add_u32 s53, s53, 28672\n"
"s_waitcnt lgkmcnt(3)\nv_mfma_f32_16x16x32_bf16 v[88:91], v[52:55], a[148:151], v[88:91]\nbuffer_load_dwordx4 v10, s[12:15], s53 offen lds\nv_mfma_f32_16x16x32_bf16 v[92:95], v[52:55], a[180:183], v[92:95]\n"
"ds_read_b128 v[52:55], v31 offset:16640\ns_waitcnt lgkmcnt(3)\nv_mfma_f32_16x16x32_bf16 v[80:83], v[40:43], a[152:155], v[80:83]\nv_mfma_f32_16x16x32_bf16 v[84:87], v[40:43], a[184:187], v[84:87]\n"
"s_waitcnt lgkmcnt(2)\nv_mfma_f32_16x16x32_bf16 v[88:91], v[44:47], a[152:155], v[88:91]\nv_mfma_f32_16x16x32_bf16 v[92:95], v[44:47], a[184:187], v[92:95]\ns_waitcnt lgkmcnt(1)\n"
"v_mfma_f32_16x16x32_bf16 v[80:83], v[48:51], a[156:159], v[80:83]\nv_mfma_f32_16x16x32_bf16 v[84:87], v[48:51], a[188:191], v[84:87]\ns_waitcnt lgkmcnt(0)\nv_mfma_f32_16x16x32_bf16 v[88:91], v[52:55], a[156:159], v[88:91]\n"
"v_mfma_f32_16x16x32_bf16 v[92:95], v[52:55], a[188:191], v[92:95]\nv_add_u32 v24, 32768, v24\nv_add_u32 v25, 32768, v25\nv_add_u32 v26, 32768, v26\n"
"v_add_u32 v27, 32768, v27\nv_add_u32 v28, 32768, v28\nv_add_u32 v29, 32768, v29\nv_add_u32 v30, 32768, v30\n"
"v_add_u32 v31, 32768, v31\ns_waitcnt vmcnt(23)\ns_barrier\nds_read_b128 v[40:43], v24 offset:0\n"
"ds_read_b128 v[44:47], v28 offset:0\nds_read_b128 v[48:51], v25 offset:0\nds_read_b128 v[52:55], v29 offset:0\ns_waitcnt lgkmcnt(3)\n"
"v_mfma_f32_16x16x32_bf16 v[64:67], v[40:43], a[128:131], 0\nv_mfma_f32_16x16x32_bf16 v[68:71], v[40:43], a[160:163], 0\nds_read_b128 v[40:43], v26 offset:0\ns_waitcnt lgkmcnt(3)\n"
"v_mfma_f32_16x16x32_bf16 v[72:75], v[44:47], a[128:131], 0\nv_mfma_f32_16x16x32_bf16 v[76:79], v[44:47], a[160:163], 0\nds_read_b128 v[44:47], v30 offset:0\nv_cvt_pk_bf16_f32 v200, v80, v81\n"
"v_cvt_pk_bf16_f32 v201, v82, v83\ns_waitcnt lgkmcnt(3)\nv_mfma_f32_16x16x32_bf16 v[64:67], v[48:51], a[132:135], v[64:67]\nv_cvt_pk_bf16_f32 v202, v88, v89\n"
"v_cvt_pk_bf16_f32 v203, v90, v91\nv_mfma_f32_16x16x32_bf16 v[68:71], v[48:51], a[164:167], v[68:71]\nds_read_b128 v[48:51], v27 offset:0\nv_cvt_pk_bf16_f32 v204, v84, v85\n"
"v_cvt_pk_bf16_f32 v205, v86, v87\ns_waitcnt lgkmcnt(3)\nv_mfma_f32_16x16x32_bf16 v[72:75], v[52:55], a[132:135], v[72:75]\nv_cvt_pk_bf16_f32 v206, v92, v93\n"
"v_cvt_pk_bf16_f32 v207, v94, v95\nv_mfma_f32_16x16x32_bf16 v[76:79], v[52:55], a[164:167], v[76:79]\nds_read_b128 v[52:55], v31 offset:0\ns_lshl_b32 s54, s44, 11\n"
"s_add_u32 s54, s54, 1024\ns_waitcnt lgkmcnt(3)\nv_mfma_f32_16x16x32_bf16 v[64:67], v[40:43], a[136:139], v[64:67]\nbuffer_store_dwordx4 v[200:203], v13, s[16:19], s54 offen sc1\n"
"v_mfma_f32_16x16x32_bf16 v[68:71], v[40:43], a[168:171], v[68:71]\nds_read_b128 v[40:43], v24 offset:256\ns_add_u32 s54, s54, 229376\ns_waitcnt lgkmcnt(3)\n"
"v_mfma_f32_16x16x32_bf16 v[72:75], v[44:47], a[136:139], v[72:75]\nbuffer_store_dwordx4 v[204:207], v13, s[16:19], s54 offen sc1\nv_mfma_f32_16x16x32_bf16 v[76:79], v[44:47], a[168:171], v[76:79]\nds_read_b128 v[44:47], v28 offset:256\n"
"s_add_u32 m0, s32, 65536\ns_waitcnt lgkmcnt(3)\nv_mfma_f32_16x16x32_bf16 v[64:67], v[48:51], a[140:143], v[64:67]\ns_lshl_b32 s53, s40, 7\n"
"s_add_u32 s53, s53, 57344\nv_mfma_f32_16x16x32_bf16 v[68:71], v[48:51], a[172:175], v[68:71]\nds_read_b128 v[48:51], v25 offset:256\nbuffer_load_dwordx4 v10, s[12:15], s53 offen lds\n"
"s_waitcnt lgkmcnt(3)\nv_mfma_f32_16x16x32_bf16 v[72:75], v[52:55], a[140:143], v[72:75]\ns_add_u32 m0, s32, 69632\nv_mfma_f32_16x16x32_bf16 v[76:79], v[52:55], a[172:175], v[76:79]\n"
"ds_read_b128 v[52:55], v29 offset:256\ns_lshl_b32 s53, s41, 7\ns_add_u32 s53, s53, 57344\ns_waitcnt lgkmcnt(3)\n"
"v_mfma_f32_16x16x32_bf16 v[64:67], v[40:43], a[144:147], v[64:67]\nbuffer_load_dwordx4 v10, s[12:15], s53 offen lds\nv_mfma_f32_16x16x32_bf16 v[68:71], v[40:43], a[176:179], v[68:71]\nds_read_b128 v[40:43], v26 offset:256\n"
"s_add_u32 m0, s32, 73728\ns_waitcnt lgkmcnt(3)\nv_mfma_f32_16x16x32_bf16 v[72:75], v[44:47], a[144:147], v[72:75]\ns_lshl_b32 s53, s42, 7\n"
"s_add_u32 s53, s53, 57344\nv_mfma_f32_16x16x32_bf16 v[76:79], v[44:47], a[176:179], v[76:79]\nds_read_b128 v[44:47], v30 offset:256\nbuffer_load_dwordx4 v10, s[12:15], s53 offen lds\n"
"s_waitcnt lgkmcnt(3)\nv_mfma_f32_16x16x32_bf16 v[64:67], v[48:51], a[148:151], v[64:67]\nv_mfma_f32_16x16x32_bf16 v[68:71], v[48:51], a[180:183], v[68:71]\nds_read_b128 v[48:51], v27 offset:256\n"
"s_waitcnt lgkmcnt(3)\nv_mfma_f32_16x16x32_bf16 v[72:75], v[52:55], a[148:151], v[72:75]\nv_mfma_f32_16x16x32_bf16 v[76:79], v[52:55], a[180:183], v[76:79]\nds_read_b128 v[52:55], v31 offset:256\n"
"s_waitcnt lgkmcnt(3)\nv_mfma_f32_16x16x32_bf16 v[64:67], v[40:43], a[152:155], v[64:67]\nv_mfma_f32_16x16x32_bf16 v[68:71], v[40:43], a[184:187], v[68:71]\nds_read_b128 v[40:43], v24 offset:16384\n"
"s_waitcnt lgkmcnt(3)\nv_mfma_f32_16x16x32_bf16 v[72:75], v[44:47], a[152:155], v[72:75]\nv_mfma_f32_16x16x32_bf16 v[76:79], v[44:47], a[184:187], v[76:79]\nds_read_b128 v[44:47], v28 offset:16384\n"
"s_waitcnt lgkmcnt(3)\nv_mfma_f32_16x16x32_bf16 v[64:67], v[48:51], a[156:159], v[64:67]\nv_mfma_f32_16x16x32_bf16 v[68:71], v[48:51], a[188:191], v[68:71]\nds_read_b128 v[48:51], v25 offset:16384\n"
"s_waitcnt lgkmcnt(3)\nv_mfma_f32_16x16x32_bf16 v[72:75], v[52:55], a[156:159], v[72:75]\nv_mfma_f32_16x16x32_bf16 v[76:79], v[52:55], a[188:191], v[76:79]\nds_read_b128 v[52:55], v29 offset:16384\n"
"s_waitcnt lgkmcnt(3)\nv_mfma_f32_16x16x32_bf16 v[80:83], v[40:43], a[128:131], 0\nv_mfma_f32_16x16x32_bf16 v[84:87], v[40:43], a[160:163], 0\nds_read_b128 v[40:43], v26 offset:16384\n"
"s_waitcnt lgkmcnt(3)\nv_mfma_f32_16x16x32_bf16 v[88:91], v[44:47], a[128:131], 0\nv_mfma_f32_16x16x32_bf16 v[92:95], v[44:47], a[160:163], 0\nds_read_b128 v[44:47], v30 offset:16384\n"
"v_cvt_pk_bf16_f32 v208, v64, v65\nv_cvt_pk_bf16_f32 v209, v66, v67\ns_waitcnt lgkmcnt(3)\nv_mfma_f32_16x16x32_bf16 v[80:83], v[48:51], a[132:135], v[80:83]\n"
"v_cvt_pk_bf16_f32 v210, v72, v73\nv_cvt_pk_bf16_f32 v211, v74, v75\nv_mfma_f32_16x16x32_bf16 v[84:87], v[48:51], a[164:167], v[84:87]\nds_read_b128 v[48:51], v27 offset:16384\n"
"v_cvt_pk_bf16_f32 v212, v68, v69\nv_cvt_pk_bf16_f32 v213, v70, v71\ns_waitcnt lgkmcnt(3)\nv_mfma_f32_16x16x32_bf16 v[88:91], v[52:55], a[132:135], v[88:91]\n"
"v_cvt_pk_bf16_f32 v214, v76, v77\nv_cvt_pk_bf16_f32 v215, v78, v79\nv_mfma_f32_16x16x32_bf16 v[92:95], v[52:55], a[164:167], v[92:95]\nds_read_b128 v[52:55], v31 offset:16384\n"
"s_lshl_b32 s54, s45, 11\ns_add_u32 s54, s54, 0\ns_waitcnt lgkmcnt(3)\nv_mfma_f32_16x16x32_bf16 v[80:83], v[40:43], a[136:139], v[80:83]\n"
"buffer_store_dwordx4 v[208:211], v13, s[16:19], s54 offen sc1\nv_mfma_f32_16x16x32_bf16 v[84:87], v[40:43], a[168:171], v[84:87]\nds_read_b128 v[40:43], v24 offset:16640\ns_add_u32 s54, s54, 229376\n"
"s_waitcnt lgkmcnt(3)\nv_mfma_f32_16x16x32_bf16 v[88:91], v[44:47], a[136:139], v[88:91]\nbuffer_store_dwordx4 v[212:215], v13, s[16:19], s54 offen sc1\nv_mfma_f32_16x16x32_bf16 v[92:95], v[44:47], a[168:171], v[92:95]\n"
"ds_read_b128 v[44:47], v28 offset:16640\ns_add_u32 m0, s32, 77824\ns_waitcnt lgkmcnt(3)\nv_mfma_f32_16x16x32_bf16 v[80:83], v[48:51], a[140:143], v[80:83]\n"
"s_lshl_b32 s53, s43, 7\ns_add_u32 s53, s53, 57344\nv_mfma_f32_16x16x32_bf16 v[84:87], v[48:51], a[172:175], v[84:87]\nds_read_b128 v[48:51], v25 offset:16640\n"
"buffer_load_dwordx4 v10, s[12:15], s53 offen lds\ns_waitcnt lgkmcnt(3)\nv_mfma_f32_16x16x32_bf16 v[88:91], v[52:55], a[140:143], v[88:91]\ns_add_u32 m0, s32, 81920\n"
"v_mfma_f32_16x16x32_bf16 v[92:95], v[52:55], a[172:175], v[92:95]\nds_read_b128 v[52:55], v29 offset:16640\ns_lshl_b32 s53, s44, 7\ns_add_u32 s53, s53, 57344\n"
"s_waitcnt lgkmcnt(3)\nv_mfma_f32_16x16x32_bf16 v[80:83], v[40:43], a[144:147], v[80:83]\nbuffer_load_dwordx4 v10, s[12:15], s53 offen lds\nv_mfma_f32_16x16x32_bf16 v[84:87], v[40:43], a[176:179], v[84:87]\n"
"ds_read_b128 v[40:43], v26 offset:16640\ns_add_u32 m0, s32, 86016\ns_waitcnt lgkmcnt(3)\nv_mfma_f32_16x16x32_bf16 v[88:91], v[44:47], a[144:147], v[88:91]\n"
"s_lshl_b32 s53, s45, 7\ns_add_u32 s53, s53, 57344\nv_mfma_f32_16x16x32_bf16 v[92:95], v[44:47], a[176:179], v[92:95]\nds_read_b128 v[44:47], v30 offset:16640\n"
"buffer_load_dwordx4 v10, s[12:15], s53 offen lds\ns_waitcnt lgkmcnt(3)\nv_mfma_f32_16x16x32_bf16 v[80:83], v[48:51], a[148:151], v[80:83]\ns_add_u32 m0, s32, 90112\n"
"v_mfma_f32_16x16x32_bf16 v[84:87], v[48:51], a[180:183], v[84:87]\nds_read_b128 v[48:51], v27 offset:16640\ns_lshl_b32 s53, s46, 7\ns_add_u32 s53, s53, 57344\n"
"s_waitcnt lgkmcnt(3)\nv_mfma_f32_16x16x32_bf16 v[88:91], v[52:55], a[148:151], v[88:91]\nbuffer_load_dwordx4 v10, s[12:15], s53 offen lds\nv_mfma_f32_16x16x32_bf16 v[92:95], v[52:55], a[180:183], v[92:95]\n"
"ds_read_b128 v[52:55], v31 offset:16640\ns_waitcnt lgkmcnt(3)\nv_mfma_f32_16x16x32_bf16 v[80:83], v[40:43], a[152:155], v[80:83]\nv_mfma_f32_16x16x32_bf16 v[84:87], v[40:43], a[184:187], v[84:87]\n"
"s_waitcnt lgkmcnt(2)\nv_mfma_f32_16x16x32_bf16 v[88:91], v[44:47], a[152:155], v[88:91]\nv_mfma_f32_16x16x32_bf16 v[92:95], v[44:47], a[184:187], v[92:95]\ns_waitcnt lgkmcnt(1)\n"
"v_mfma_f32_16x16x32_bf16 v[80:83], v[48:51], a[156:159], v[80:83]\nv_mfma_f32_16x16x32_bf16 v[84:87], v[48:51], a[188:191], v[84:87]\ns_waitcnt lgkmcnt(0)\nv_mfma_f32_16x16x32_bf16 v[88:91], v[52:55], a[156:159], v[88:91]\n"
"v_mfma_f32_16x16x32_bf16 v[92:95], v[52:55], a[188:191], v[92:95]\nv_add_u32 v24, 32768, v24\nv_add_u32 v25, 32768, v25\nv_add_u32 v26, 32768, v26\n"
"v_add_u32 v27, 32768, v27\nv_add_u32 v28, 32768, v28\nv_add_u32 v29, 32768, v29\nv_add_u32 v30, 32768, v30\n"
"v_add_u32 v31, 32768, v31\ns_waitcnt vmcnt(22)\ns_barrier\nds_read_b128 v[40:43], v24 offset:0\n"
"ds_read_b128 v[44:47], v28 offset:0\nds_read_b128 v[48:51], v25 offset:0\nds_read_b128 v[52:55], v29 offset:0\ns_waitcnt lgkmcnt(3)\n"
"v_mfma_f32_16x16x32_bf16 v[64:67], v[40:43], a[128:131], 0\nv_mfma_f32_16x16x32_bf16 v[68:71], v[40:43], a[160:163], 0\nds_read_b128 v[40:43], v26 offset:0\ns_waitcnt lgkmcnt(3)\n"
"v_mfma_f32_16x16x32_bf16 v[72:75], v[44:47], a[128:131], 0\nv_mfma_f32_16x16x32_bf16 v[76:79], v[44:47], a[160:163], 0\nds_read_b128 v[44:47], v30 offset:0\nv_cvt_pk_bf16_f32 v216, v80, v81\n"
"v_cvt_pk_bf16_f32 v217, v82, v83\ns_waitcnt lgkmcnt(3)\nv_mfma_f32_16x16x32_bf16 v[64:67], v[48:51], a[132:135], v[64:67]\nv_cvt_pk_bf16_f32 v218, v88, v89\n"
"v_cvt_pk_bf16_f32 v219, v90, v91\nv_mfma_f32_16x16x32_bf16 v[68:71], v[48:51], a[164:167], v[68:71]\nds_read_b128 v[48:51], v27 offset:0\nv_cvt_pk_bf16_f32 v220, v84, v85\n"
"v_cvt_pk_bf16_f32 v221, v86, v87\ns_waitcnt lgkmcnt(3)\nv_mfma_f32_16x16x32_bf16 v[72:75], v[52:55], a[132:135], v[72:75]\nv_cvt_pk_bf16_f32 v222, v92, v93\n"
"v_cvt_pk_bf16_f32 v223, v94, v95\nv_mfma_f32_16x16x32_bf16 v[76:79], v[52:55], a[164:167], v[76:79]\nds_read_b128 v[52:55], v31 offset:0\ns_lshl_b32 s54, s45, 11\n"
"s_add_u32 s54, s54, 1024\ns_waitcnt lgkmcnt(3)\nv_mfma_f32_16x16x32_bf16 v[64:67], v[40:43], a[136:139], v[64:67]\nbuffer_store_dwordx4 v[216:219], v13, s[16:19], s54 offen sc1\n"
"v_mfma_f32_16x16x32_bf16 v[68:71], v[40:43], a[168:171], v[68:71]\nds_read_b128 v[40:43], v24 offset:256\ns_add_u32 s54, s54, 229376\ns_waitcnt lgkmcnt(3)\n"
"v_mfma_f32_16x16x32_bf16 v[72:75], v[44:47], a[136:139], v[72:75]\nbuffer_store_dwordx4 v[220:223], v13, s[16:19], s54 offen sc1\nv_mfma_f32_16x16x32_bf16 v[76:79], v[44:47], a[168:171], v[76:79]\nds_read_b128 v[44:47], v28 offset:256\n"
"s_add_u32 m0, s32, 98304\ns_waitcnt lgkmcnt(3)\nv_mfma_f32_16x16x32_bf16 v[64:67], v[48:51], a[140:143], v[64:67]\ns_lshl_b32 s53, s40, 7\n"
"s_add_u32 s53, s53, 86016\nv_mfma_f32_16x16x32_bf16 v[68:71], v[48:51], a[172:175], v[68:71]\nds_read_b128 v[48:51], v25 offset:256\nbuffer_load_dwordx4 v10, s[12:15], s53 offen lds\n"
"s_waitcnt lgkmcnt(3)\nv_mfma_f32_16x16x32_bf16 v[72:75], v[52:55], a[140:143], v[72:75]\ns_add_u32 m0, s32, 102400\nv_mfma_f32_16x16x32_bf16 v[76:79], v[52:55], a[172:175], v[76:79]\n"
"ds_read_b128 v[52:55], v29 offset:256\ns_lshl_b32 s53, s41, 7\ns_add_u32 s53, s53, 86016\ns_waitcnt lgkmcnt(3)\n"
"v_mfma_f32_16x16x32_bf16 v[64:67], v[40:43], a[144:147], v[64:67]\nbuffer_load_dwordx4 v10, s[12:15], s53 offen lds\nv_mfma_f32_16x16x32_bf16 v[68:71], v[40:43], a[176:179], v[68:71]\nds_read_b128 v[40:43], v26 offset:256\n"
"s_add_u32 m0, s32, 106496\ns_waitcnt lgkmcnt(3)\nv_mfma_f32_16x16x32_bf16 v[72:75], v[44:47], a[144:147], v[72:75]\ns_lshl_b32 s53, s42, 7\n"
"s_add_u32 s53, s53, 86016\nv_mfma_f32_16x16x32_bf16 v[76:79], v[44:47], a[176:179], v[76:79]\nds_read_b128 v[44:47], v30 offset:256\nbuffer_load_dwordx4 v10, s[12:15], s53 offen lds\n"
"s_waitcnt lgkmcnt(3)\nv_mfma_f32_16x16x32_bf16 v[64:67], v[48:51], a[148:151], v[64:67]\nv_mfma_f32_16x16x32_bf16 v[68:71], v[48:51], a[180:183], v[68:71]\nds_read_b128 v[48:51], v27 offset:256\n"
"s_waitcnt lgkmcnt(3)\nv_mfma_f32_16x16x32_bf16 v[72:75], v[52:55], a[148:151], v[72:75]\nv_mfma_f32_16x16x32_bf16 v[76:79], v[52:55], a[180:183], v[76:79]\nds_read_b128 v[52:55], v31 offset:256\n"
"s_waitcnt lgkmcnt(3)\nv_mfma_f32_16x16x32_bf16 v[64:67], v[40:43], a[152:155], v[64:67]\nv_mfma_f32_16x16x32_bf16 v[68:71], v[40:43], a[184:187], v[68:71]\nds_read_b128 v[40:43], v24 offset:16384\n"
"s_waitcnt lgkmcnt(3)\nv_mfma_f32_16x16x32_bf16 v[72:75], v[44:47], a[152:155], v[72:75]\nv_mfma_f32_16x16x32_bf16 v[76:79], v[44:47], a[184:187], v[76:79]\nds_read_b128 v[44:47], v28 offset:16384\n"
"s_waitcnt lgkmcnt(3)\nv_mfma_f32_16x16x32_bf16 v[64:67], v[48:51], a[156:159], v[64:67]\nv_mfma_f32_16x16x32_bf16 v[68:71], v[48:51], a[188:191], v[68:71]\nds_read_b128 v[48:51], v25 offset:16384\n"
"s_waitcnt lgkmcnt(3)\nv_mfma_f32_16x16x32_bf16 v[72:75], v[52:55], a[156:159], v[72:75]\nv_mfma_f32_16x16x32_bf16 v[76:79], v[52:55], a[188:191], v[76:79]\nds_read_b128 v[52:55], v29 offset:16384\n"
"s_waitcnt lgkmcnt(3)\nv_mfma_f32_16x16x32_bf16 v[80:83], v[40:43], a[128:131], 0\nv_mfma_f32_16x16x32_bf16 v[84:87], v[40:43], a[160:163], 0\nds_read_b128 v[40:43], v26 offset:16384\n"
"s_waitcnt lgkmcnt(3)\nv_mfma_f32_16x16x32_bf16 v[88:91], v[44:47], a[128:131], 0\nv_mfma_f32_16x16x32_bf16 v[92:95], v[44:47], a[160:163], 0\nds_read_b128 v[44:47], v30 offset:16384\n"
"v_cvt_pk_bf16_f32 v224, v64, v65\nv_cvt_pk_bf16_f32 v225, v66, v67\ns_waitcnt lgkmcnt(3)\nv_mfma_f32_16x16x32_bf16 v[80:83], v[48:51], a[132:135], v[80:83]\n"
"v_cvt_pk_bf16_f32 v226, v72, v73\nv_cvt_pk_bf16_f32 v227, v74, v75\nv_mfma_f32_16x16x32_bf16 v[84:87], v[48:51], a[164:167], v[84:87]\nds_read_b128 v[48:51], v27 offset:16384\n"
"v_cvt_pk_bf16_f32 v228, v68, v69\nv_cvt_pk_bf16_f32 v229, v70, v71\ns_waitcnt lgkmcnt(3)\nv_mfma_f32_16x16x32_bf16 v[88:91], v[52:55], a[132:135], v[88:91]\n"
"v_cvt_pk_bf16_f32 v230, v76, v77\nv_cvt_pk_bf16_f32 v231, v78, v79\nv_mfma_f32_16x16x32_bf16 v[92:95], v[52:55], a[164:167], v[92:95]\nds_read_b128 v[52:55], v31 offset:16384\n"
"s_lshl_b32 s54, s46, 11\ns_add_u32 s54, s54, 0\ns_waitcnt lgkmcnt(3)\nv_mfma_f32_16x16x32_bf16 v[80:83], v[40:43], a[136:139], v[80:83]\n"
"buffer_store_dwordx4 v[224:227], v13, s[16:19], s54 offen sc1\nv_mfma_f32_16x16x32_bf16 v[84:87], v[40:43], a[168:171], v[84:87]\nds_read_b128 v[40:43], v24 offset:16640\ns_add_u32 s54, s54, 229376\n"
"s_waitcnt lgkmcnt(3)\nv_mfma_f32_16x16x32_bf16 v[88:91], v[44:47], a[136:139], v[88:91]\nbuffer_store_dwordx4 v[228:231], v13, s[16:19], s54 offen sc1\nv_mfma_f32_16x16x32_bf16 v[92:95], v[44:47], a[168:171], v[92:95]\n"
"ds_read_b128 v[44:47], v28 offset:16640\ns_add_u32 m0, s32, 110592\ns_waitcnt lgkmcnt(3)\nv_mfma_f32_16x16x32_bf16 v[80:83], v[48:51], a[140:143], v[80:83]\n"
"s_lshl_b32 s53, s43, 7\ns_add_u32 s53, s53, 86016\nv_mfma_f32_16x16x32_bf16 v[84:87], v[48:51], a[172:175], v[84:87]\nds_read_b128 v[48:51], v25 offset:16640\n"
"buffer_load_dwordx4 v10, s[12:15], s53 offen lds\ns_waitcnt lgkmcnt(3)\nv_mfma_f32_16x16x32_bf16 v[88:91], v[52:55], a[140:143], v[88:91]\ns_add_u32 m0, s32, 114688\n"
"v_mfma_f32_16x16x32_bf16 v[92:95], v[52:55], a[172:175], v[92:95]\nds_read_b128 v[52:55], v29 offset:16640\ns_lshl_b32 s53, s44, 7\ns_add_u32 s53, s53, 86016\n"
"s_waitcnt lgkmcnt(3)\nv_mfma_f32_16x16x32_bf16 v[80:83], v[40:43], a[144:147], v[80:83]\nbuffer_load_dwordx4 v10, s[12:15], s53 offen lds\nv_mfma_f32_16x16x32_bf16 v[84:87], v[40:43], a[176:179], v[84:87]\n"
"ds_read_b128 v[40:43], v26 offset:16640\ns_add_u32 m0, s32, 118784\ns_waitcnt lgkmcnt(3)\nv_mfma_f32_16x16x32_bf16 v[88:91], v[44:47], a[144:147], v[88:91]\n"
"s_lshl_b32 s53, s45, 7\ns_add_u32 s53, s53, 86016\nv_mfma_f32_16x16x32_bf16 v[92:95], v[44:47], a[176:179], v[92:95]\nds_read_b128 v[44:47], v30 offset:16640\n"
"buffer_load_dwordx4 v10, s[12:15], s53 offen lds\ns_waitcnt lgkmcnt(3)\nv_mfma_f32_16x16x32_bf16 v[80:83], v[48:51], a[148:151], v[80:83]\ns_add_u32 m0, s32, 122880\n"
"v_mfma_f32_16x16x32_bf16 v[84:87], v[48:51], a[180:183], v[84:87]\nds_read_b128 v[48:51], v27 offset:16640\ns_lshl_b32 s53, s46, 7\ns_add_u32 s53, s53, 86016\n"
"s_waitcnt lgkmcnt(3)\nv_mfma_f32_16x16x32_bf16 v[88:91], v[52:55], a[148:151], v[88:91]\nbuffer_load_dwordx4 v10, s[12:15], s53 offen lds\nv_mfma_f32_16x16x32_bf16 v[92:95], v[52:55], a[180:183], v[92:95]\n"
"ds_read_b128 v[52:55], v31 offset:16640\ns_waitcnt lgkmcnt(3)\nv_mfma_f32_16x16x32_bf16 v[80:83], v[40:43], a[152:155], v[80:83]\nv_mfma_f32_16x16x32_bf16 v[84:87], v[40:43], a[184:187], v[84:87]\n"
"s_waitcnt lgkmcnt(2)\nv_mfma_f32_16x16x32_bf16 v[88:91], v[44:47], a[152:155], v[88:91]\nv_mfma_f32_16x16x32_bf16 v[92:95], v[44:47], a[184:187], v[92:95]\ns_waitcnt lgkmcnt(1)\n"
"v_mfma_f32_16x16x32_bf16 v[80:83], v[48:51], a[156:159], v[80:83]\nv_mfma_f32_16x16x32_bf16 v[84:87], v[48:51], a[188:191], v[84:87]\ns_waitcnt lgkmcnt(0)\nv_mfma_f32_16x16x32_bf16 v[88:91], v[52:55], a[156:159], v[88:91]\n"
"v_mfma_f32_16x16x32_bf16 v[92:95], v[52:55], a[188:191], v[92:95]\ns_nop 7\nv_cvt_pk_bf16_f32 v232, v80, v81\nv_cvt_pk_bf16_f32 v233, v82, v83\n"
"v_cvt_pk_bf16_f32 v234, v88, v89\nv_cvt_pk_bf16_f32 v235, v90, v91\nv_cvt_pk_bf16_f32 v236, v84, v85\nv_cvt_pk_bf16_f32 v237, v86, v87\n"
"v_cvt_pk_bf16_f32 v238, v92, v93\nv_cvt_pk_bf16_f32 v239, v94, v95\ns_lshl_b32 s54, s46, 11\ns_add_u32 s54, s54, 1024\n"
"buffer_store_dwordx4 v[232:235], v13, s[16:19], s54 offen sc1\ns_add_u32 s54, s54, 229376\nbuffer_store_dwordx4 v[236:239], v13, s[16:19], s54 offen sc1\ns_sub_u32 s48, s28, 1\n"
"s_mov_b32 s49, 114688\ns_mov_b32 s50, 0\ns_mov_b32 s51, 0\ns_add_u32 s52, s32, 131072\n"
"ds_read_b128 v[40:43], v32 offset:0\nds_read_b128 v[44:47], v33 offset:0\nds_read_b128 v[48:51], v34 offset:0\nds_read_b128 v[52:55], v35 offset:0\n"
"s_waitcnt vmcnt(24)\ns_barrier\ns_waitcnt lgkmcnt(3)\nv_mfma_f32_16x16x32_bf16 v[64:67], v[40:43], v[128:131], 0\n"
"v_mov_b32 v36, s48\ns_add_u32 s48, s48, 1\ns_nop 0\nbuffer_store_dword v36, v19, s[24:27], s33 offen sc1\n"
"v_mfma_f32_16x16x32_bf16 v[68:71], v[40:43], v[132:135], 0\nds_read_b128 v[40:43], v32 offset:4096\ns_waitcnt lgkmcnt(3)\nv_mfma_f32_16x16x32_bf16 v[72:75], v[44:47], v[128:131], 0\n"
"v_mfma_f32_16x16x32_bf16 v[76:79], v[44:47], v[132:135], 0\nds_read_b128 v[44:47], v33 offset:4096\ns_waitcnt lgkmcnt(3)\nv_mfma_f32_16x16x32_bf16 v[64:67], v[48:51], v[136:139], v[64:67]\n"
"s_add_u32 m0, s52, 0\nv_mfma_f32_16x16x32_bf16 v[68:71], v[48:51], v[140:143], v[68:71]\nds_read_b128 v[48:51], v34 offset:4096\ns_lshl_b32 s53, s40, 7\n"
"s_add_u32 s53, s53, s49\ns_waitcnt lgkmcnt(3)\nv_mfma_f32_16x16x32_bf16 v[72:75], v[52:55], v[136:139], v[72:75]\nbuffer_load_dwordx4 v10, s[12:15], s53 offen lds\n"
"v_mfma_f32_16x16x32_bf16 v[76:79], v[52:55], v[140:143], v[76:79]\nds_read_b128 v[52:55], v35 offset:4096\ns_add_u32 m0, s52, 4096\ns_waitcnt lgkmcnt(3)\n"
"v_mfma_f32_16x16x32_bf16 v[64:67], v[40:43], v[144:147], v[64:67]\ns_lshl_b32 s53, s41, 7\ns_add_u32 s53, s53, s49\nv_mfma_f32_16x16x32_bf16 v[68:71], v[40:43], v[148:151], v[68:71]\n"
"ds_read_b128 v[40:43], v32 offset:8192\nbuffer_load_dwordx4 v10, s[12:15], s53 offen lds\ns_waitcnt lgkmcnt(3)\nv_mfma_f32_16x16x32_bf16 v[72:75], v[44:47], v[144:147], v[72:75]\n"
"s_add_u32 m0, s52, 8192\nv_mfma_f32_16x16x32_bf16 v[76:79], v[44:47], v[148:151], v[76:79]\nds_read_b128 v[44:47], v33 offset:8192\ns_lshl_b32 s53, s42, 7\n"
"s_add_u32 s53, s53, s49\ns_waitcnt lgkmcnt(3)\nv_mfma_f32_16x16x32_bf16 v[64:67], v[48:51], v[152:155], v[64:67]\nbuffer_load_dwordx4 v10, s[12:15], s53 offen lds\n"
"v_mfma_f32_16x16x32_bf16 v[68:71], v[48:51], v[156:159], v[68:71]\nds_read_b128 v[48:51], v34 offset:8192\ns_add_u32 m0, s52, 12288\ns_waitcnt lgkmcnt(3)\n"
"v_mfma_f32_16x16x32_bf16 v[72:75], v[52:55], v[152:155], v[72:75]\ns_lshl_b32 s53, s43, 7\ns_add_u32 s53, s53, s49\nv_mfma_f32_16x16x32_bf16 v[76:79], v[52:55], v[156:159], v[76:79]\n"
"ds_read_b128 v[52:55], v35 offset:8192\nbuffer_load_dwordx4 v10, s[12:15], s53 offen lds\ns_waitcnt lgkmcnt(3)\nv_mfma_f32_16x16x32_bf16 v[64:67], v[40:43], v[160:163], v[64:67]\n"
"s_add_u32 m0, s52, 16384\nv_mfma_f32_16x16x32_bf16 v[68:71], v[40:43], v[164:167], v[68:71]\nds_read_b128 v[40:43], v32 offset:12288\ns_lshl_b32 s53, s44, 7\n"
"s_add_u32 s53, s53, s49\ns_waitcnt lgkmcnt(3)\nv_mfma_f32_16x16x32_bf16 v[72:75], v[44:47], v[160:163], v[72:75]\nbuffer_load_dwordx4 v10, s[12:15], s53 offen lds\n"
"v_mfma_f32_16x16x32_bf16 v[76:79], v[44:47], v[164:167], v[76:79]\nds_read_b128 v[44:47], v33 offset:12288\ns_add_u32 m0, s52, 20480\ns_waitcnt lgkmcnt(3)\n"
"v_mfma_f32_16x16x32_bf16 v[64:67], v[48:51], v[168:171], v[64:67]\ns_lshl_b32 s53, s45, 7\ns_add_u32 s53, s53, s49\nv_mfma_f32_16x16x32_bf16 v[68:71], v[48:51], v[172:175], v[68:71]\n"
"ds_read_b128 v[48:51], v34 offset:12288\nbuffer_load_dwordx4 v10, s[12:15], s53 offen lds\ns_waitcnt lgkmcnt(3)\nv_mfma_f32_16x16x32_bf16 v[72:75], v[52:55], v[168:171], v[72:75]\n"
"s_add_u32 m0, s52, 24576\nv_mfma_f32_16x16x32_bf16 v[76:79], v[52:55], v[172:175], v[76:79]\nds_read_b128 v[52:55], v35 offset:12288\ns_lshl_b32 s53, s46, 7\n"
"s_add_u32 s53, s53, s49\ns_waitcnt lgkmcnt(3)\nv_mfma_f32_16x16x32_bf16 v[64:67], v[40:43], v[176:179], v[64:67]\nbuffer_load_dwordx4 v10, s[12:15], s53 offen lds\n"
"v_mfma_f32_16x16x32_bf16 v[68:71], v[40:43], v[180:183], v[68:71]\nds_read_b128 v[40:43], v32 offset:16384\ns_waitcnt lgkmcnt(3)\nv_mfma_f32_16x16x32_bf16 v[72:75], v[44:47], v[176:179], v[72:75]\n"
"v_mfma_f32_16x16x32_bf16 v[76:79], v[44:47], v[180:183], v[76:79]\nds_read_b128 v[44:47], v33 offset:16384\ns_waitcnt lgkmcnt(3)\nv_mfma_f32_16x16x32_bf16 v[64:67], v[48:51], v[184:187], v[64:67]\n"
"v_mfma_f32_16x16x32_bf16 v[68:71], v[48:51], v[188:191], v[68:71]\nds_read_b128 v[48:51], v34 offset:16384\ns_waitcnt lgkmcnt(3)\nv_mfma_f32_16x16x32_bf16 v[72:75], v[52:55], v[184:187], v[72:75]\n"
"v_mfma_f32_16x16x32_bf16 v[76:79], v[52:55], v[188:191], v[76:79]\nds_read_b128 v[52:55], v35 offset:16384\ns_waitcnt lgkmcnt(3)\nv_mfma_f32_16x16x32_bf16 v[64:67], v[40:43], v[192:195], v[64:67]\n"
"v_mfma_f32_16x16x32_bf16 v[68:71], v[40:43], v[196:199], v[68:71]\nds_read_b128 v[40:43], v32 offset:20480\ns_waitcnt lgkmcnt(3)\nv_mfma_f32_16x16x32_bf16 v[72:75], v[44:47], v[192:195], v[72:75]\n"
"v_mfma_f32_16x16x32_bf16 v[76:79], v[44:47], v[196:199], v[76:79]\nds_read_b128 v[44:47], v33 offset:20480\ns_waitcnt lgkmcnt(3)\nv_mfma_f32_16x16x32_bf16 v[64:67], v[48:51], v[200:203], v[64:67]\n"
"v_mfma_f32_16x16x32_bf16 v[68:71], v[48:51], v[204:207], v[68:71]\nds_read_b128 v[48:51], v34 offset:20480\ns_waitcnt lgkmcnt(3)\nv_mfma_f32_16x16x32_bf16 v[72:75], v[52:55], v[200:203], v[72:75]\n"
"v_mfma_f32_16x16x32_bf16 v[76:79], v[52:55], v[204:207], v[76:79]\nds_read_b128 v[52:55], v35 offset:20480\ns_waitcnt lgkmcnt(3)\nv_mfma_f32_16x16x32_bf16 v[64:67], v[40:43], v[208:211], v[64:67]\n"
"v_mfma_f32_16x16x32_bf16 v[68:71], v[40:43], v[212:215], v[68:71]\nds_read_b128 v[40:43], v32 offset:24576\ns_waitcnt lgkmcnt(3)\nv_mfma_f32_16x16x32_bf16 v[72:75], v[44:47], v[208:211], v[72:75]\n"
"v_mfma_f32_16x16x32_bf16 v[76:79], v[44:47], v[212:215], v[76:79]\nds_read_b128 v[44:47], v33 offset:24576\ns_waitcnt lgkmcnt(3)\nv_mfma_f32_16x16x32_bf16 v[64:67], v[48:51], v[216:219], v[64:67]\n"
"s_add_u32 s54, s51, 32768\ns_cmp_eq_u32 s54, 163840\nv_mfma_f32_16x16x32_bf16 v[68:71], v[48:51], v[220:223], v[68:71]\ns_cselect_b32 s54, 0, s54\n"
"s_sub_u32 s54, s54, s51\nds_read_b128 v[48:51], v34 offset:24576\ns_waitcnt lgkmcnt(3)\nv_mfma_f32_16x16x32_bf16 v[72:75], v[52:55], v[216:219], v[72:75]\n"
"v_mfma_f32_16x16x32_bf16 v[76:79], v[52:55], v[220:223], v[76:79]\nds_read_b128 v[52:55], v35 offset:24576\ns_waitcnt lgkmcnt(3)\nv_mfma_f32_16x16x32_bf16 v[64:67], v[40:43], v[224:227], v[64:67]\n"
"v_add_u32 v32, s54, v32\nv_mfma_f32_16x16x32_bf16 v[68:71], v[40:43], v[228:231], v[68:71]\nds_read_b128 v[40:43], v32 offset:0\ns_waitcnt lgkmcnt(3)\n"
"v_mfma_f32_16x16x32_bf16 v[72:75], v[44:47], v[224:227], v[72:75]\nv_add_u32 v33, s54, v33\nv_mfma_f32_16x16x32_bf16 v[76:79], v[44:47], v[228:231], v[76:79]\nds_read_b128 v[44:47], v33 offset:0\n"
"s_waitcnt lgkmcnt(3)\nv_mfma_f32_16x16x32_bf16 v[64:67], v[48:51], v[232:235], v[64:67]\nv_add_u32 v34, s54, v34\nv_mfma_f32_16x16x32_bf16 v[68:71], v[48:51], v[236:239], v[68:71]\n"
"ds_read_b128 v[48:51], v34 offset:0\ns_waitcnt lgkmcnt(3)\nv_mfma_f32_16x16x32_bf16 v[72:75], v[52:55], v[232:235], v[72:75]\nv_add_u32 v35, s54, v35\n"
"v_mfma_f32_16x16x32_bf16 v[76:79], v[52:55], v[236:239], v[76:79]\nds_read_b128 v[52:55], v35 offset:0\ns_add_u32 s52, s51, s32\ns_add_u32 s51, s51, s54\n"
"s_add_u32 s49, s49, 28672\ns_waitcnt vmcnt(8)\ns_barrier\ns_waitcnt lgkmcnt(3)\n"
"v_mfma_f32_16x16x32_bf16 v[96:99], v[40:43], v[128:131], 0\nv_mov_b32 v36, s48\ns_add_u32 s48, s48, 1\ns_nop 0\n"
"buffer_store_dword v36, v19, s[24:27], 0 offen sc1\nv_mfma_f32_16x16x32_bf16 v[100:103], v[40:43], v[132:135], 0\nds_read_b128 v[40:43], v32 offset:4096\ns_waitcnt lgkmcnt(3)\n"
"v_mfma_f32_16x16x32_bf16 v[104:107], v[44:47], v[128:131], 0\nv_mfma_f32_16x16x32_bf16 v[108:111], v[44:47], v[132:135], 0\nds_read_b128 v[44:47], v33 offset:4096\nv_cvt_pk_bf16_f32 v56, v64, v65\n"
"s_waitcnt lgkmcnt(3)\nv_mfma_f32_16x16x32_bf16 v[96:99], v[48:51], v[136:139], v[96:99]\nv_cvt_pk_bf16_f32 v57, v66, v67\nv_mfma_f32_16x16x32_bf16 v[100:103], v[48:51], v[140:143], v[100:103]\n"
"ds_read_b128 v[48:51], v34 offset:4096\nv_cvt_pk_bf16_f32 v58, v72, v73\ns_waitcnt lgkmcnt(3)\nv_mfma_f32_16x16x32_bf16 v[104:107], v[52:55], v[136:139], v[104:107]\n"
"v_cvt_pk_bf16_f32 v59, v74, v75\nv_mfma_f32_16x16x32_bf16 v[108:111], v[52:55], v[140:143], v[108:111]\nds_read_b128 v[52:55], v35 offset:4096\ns_waitcnt lgkmcnt(3)\n"
"v_mfma_f32_16x16x32_bf16 v[96:99], v[40:43], v[144:147], v[96:99]\nv_mfma_f32_16x16x32_bf16 v[100:103], v[40:43], v[148:151], v[100:103]\nds_read_b128 v[40:43], v32 offset:8192\nbuffer_store_dwordx4 v[56:59], v15, s[20:23], s50 offen sc1\n"
"s_waitcnt lgkmcnt(3)\nv_mfma_f32_16x16x32_bf16 v[104:107], v[44:47], v[144:147], v[104:107]\nv_cvt_pk_bf16_f32 v60, v68, v69\nv_mfma_f32_16x16x32_bf16 v[108:111], v[44:47], v[148:151], v[108:111]\n"
"ds_read_b128 v[44:47], v33 offset:8192\nv_cvt_pk_bf16_f32 v61, v70, v71\ns_waitcnt lgkmcnt(3)\nv_mfma_f32_16x16x32_bf16 v[96:99], v[48:51], v[152:155], v[96:99]\n"
"v_cvt_pk_bf16_f32 v62, v76, v77\nv_mfma_f32_16x16x32_bf16 v[100:103], v[48:51], v[156:159], v[100:103]\nds_read_b128 v[48:51], v34 offset:8192\nv_cvt_pk_bf16_f32 v63, v78, v79\n"
"s_waitcnt lgkmcnt(3)\nv_mfma_f32_16x16x32_bf16 v[104:107], v[52:55], v[152:155], v[104:107]\nv_mfma_f32_16x16x32_bf16 v[108:111], v[52:55], v[156:159], v[108:111]\nds_read_b128 v[52:55], v35 offset:8192\n"
"s_waitcnt lgkmcnt(3)\nv_mfma_f32_16x16x32_bf16 v[96:99], v[40:43], v[160:163], v[96:99]\nbuffer_store_dwordx4 v[60:63], v16, s[20:23], s50 offen sc1\nv_mfma_f32_16x16x32_bf16 v[100:103], v[40:43], v[164:167], v[100:103]\n"
"ds_read_b128 v[40:43], v32 offset:12288\ns_add_u32 s50, s50, s36\ns_waitcnt lgkmcnt(3)\nv_mfma_f32_16x16x32_bf16 v[104:107], v[44:47], v[160:163], v[104:107]\n"
"s_add_u32 m0, s52, 0\nv_mfma_f32_16x16x32_bf16 v[108:111], v[44:47], v[164:167], v[108:111]\nds_read_b128 v[44:47], v33 offset:12288\ns_lshl_b32 s53, s40, 7\n"
"s_add_u32 s53, s53, s49\ns_waitcnt lgkmcnt(3)\nv_mfma_f32_16x16x32_bf16 v[96:99], v[48:51], v[168:171], v[96:99]\nbuffer_load_dwordx4 v10, s[12:15], s53 offen lds\n"
"v_mfma_f32_16x16x32_bf16 v[100:103], v[48:51], v[172:175], v[100:103]\nds_read_b128 v[48:51], v34 offset:12288\ns_add_u32 m0, s52, 4096\ns_waitcnt lgkmcnt(3)\n"
"v_mfma_f32_16x16x32_bf16 v[104:107], v[52:55], v[168:171], v[104:107]\ns_lshl_b32 s53, s41, 7\ns_add_u32 s53, s53, s49\nv_mfma_f32_16x16x32_bf16 v[108:111], v[52:55], v[172:175], v[108:111]\n"
"ds_read_b128 v[52:55], v35 offset:12288\nbuffer_load_dwordx4 v10, s[12:15], s53 offen lds\ns_waitcnt lgkmcnt(3)\nv_mfma_f32_16x16x32_bf16 v[96:99], v[40:43], v[176:179], v[96:99]\n"
"s_add_u32 m0, s52, 8192\nv_mfma_f32_16x16x32_bf16 v[100:103], v[40:43], v[180:183], v[100:103]\nds_read_b128 v[40:43], v32 offset:16384\ns_lshl_b32 s53, s42, 7\n"
"s_add_u32 s53, s53, s49\ns_waitcnt lgkmcnt(3)\nv_mfma_f32_16x16x32_bf16 v[104:107], v[44:47], v[176:179], v[104:107]\nbuffer_load_dwordx4 v10, s[12:15], s53 offen lds\n"
"v_mfma_f32_16x16x32_bf16 v[108:111], v[44:47], v[180:183], v[108:111]\nds_read_b128 v[44:47], v33 offset:16384\ns_add_u32 m0, s52, 12288\ns_waitcnt lgkmcnt(3)\n"
"v_mfma_f32_16x16x32_bf16 v[96:99], v[48:51], v[184:187], v[96:99]\ns_lshl_b32 s53, s43, 7\ns_add_u32 s53, s53, s49\nv_mfma_f32_16x16x32_bf16 v[100:103], v[48:51], v[188:191], v[100:103]\n"
"ds_read_b128 v[48:51], v34 offset:16384\nbuffer_load_dwordx4 v10, s[12:15], s53 offen lds\ns_waitcnt lgkmcnt(3)\nv_mfma_f32_16x16x32_bf16 v[104:107], v[52:55], v[184:187], v[104:107]\n"
"s_add_u32 m0, s52, 16384\nv_mfma_f32_16x16x32_bf16 v[108:111], v[52:55], v[188:191], v[108:111]\nds_read_b128 v[52:55], v35 offset:16384\ns_lshl_b32 s53, s44, 7\n"
"s_add_u32 s53, s53, s49\ns_waitcnt lgkmcnt(3)\nv_mfma_f32_16x16x32_bf16 v[96:99], v[40:43], v[192:195], v[96:99]\nbuffer_load_dwordx4 v10, s[12:15], s53 offen lds\n"
"v_mfma_f32_16x16x32_bf16 v[100:103], v[40:43], v[196:199], v[100:103]\nds_read_b128 v[40:43], v32 offset:20480\ns_add_u32 m0, s52, 20480\ns_waitcnt lgkmcnt(3)\n"
"v_mfma_f32_16x16x32_bf16 v[104:107], v[44:47], v[192:195], v[104:107]\ns_lshl_b32 s53, s45, 7\ns_add_u32 s53, s53, s49\nv_mfma_f32_16x16x32_bf16 v[108:111], v[44:47], v[196:199], v[108:111]\n"
"ds_read_b128 v[44:47], v33 offset:20480\nbuffer_load_dwordx4 v10, s[12:15], s53 offen lds\ns_waitcnt lgkmcnt(3)\nv_mfma_f32_16x16x32_bf16 v[96:99], v[48:51], v[200:203], v[96:99]\n"
"s_add_u32 m0, s52, 24576\nv_mfma_f32_16x16x32_bf16 v[100:103], v[48:51], v[204:207], v[100:103]\nds_read_b128 v[48:51], v34 offset:20480\ns_lshl_b32 s53, s46, 7\n"
"s_add_u32 s53, s53, s49\ns_waitcnt lgkmcnt(3)\nv_mfma_f32_16x16x32_bf16 v[104:107], v[52:55], v[200:203], v[104:107]\nbuffer_load_dwordx4 v10, s[12:15], s53 offen lds\n"
"v_mfma_f32_16x16x32_bf16 v[108:111], v[52:55], v[204:207], v[108:111]\nds_read_b128 v[52:55], v35 offset:20480\ns_waitcnt lgkmcnt(3)\nv_mfma_f32_16x16x32_bf16 v[96:99], v[40:43], v[208:211], v[96:99]\n"
"v_mfma_f32_16x16x32_bf16 v[100:103], v[40:43], v[212:215], v[100:103]\nds_read_b128 v[40:43], v32 offset:24576\ns_waitcnt lgkmcnt(3)\nv_mfma_f32_16x16x32_bf16 v[104:107], v[44:47], v[208:211], v[104:107]\n"
"v_mfma_f32_16x16x32_bf16 v[108:111], v[44:47], v[212:215], v[108:111]\nds_read_b128 v[44:47], v33 offset:24576\ns_waitcnt lgkmcnt(3)\nv_mfma_f32_16x16x32_bf16 v[96:99], v[48:51], v[216:219], v[96:99]\n"
"s_add_u32 s54, s51, 32768\ns_cmp_eq_u32 s54, 163840\nv_mfma_f32_16x16x32_bf16 v[100:103], v[48:51], v[220:223], v[100:103]\ns_cselect_b32 s54, 0, s54\n"
"s_sub_u32 s54, s54, s51\nds_read_b128 v[48:51], v34 offset:24576\ns_waitcnt lgkmcnt(3)\nv_mfma_f32_16x16x32_bf16 v[104:107], v[52:55], v[216:219], v[104:107]\n"
"v_mfma_f32_16x16x32_bf16 v[108:111], v[52:55], v[220:223], v[108:111]\nds_read_b128 v[52:55], v35 offset:24576\ns_waitcnt lgkmcnt(3)\nv_mfma_f32_16x16x32_bf16 v[96:99], v[40:43], v[224:227], v[96:99]\n"
"v_add_u32 v32, s54, v32\nv_mfma_f32_16x16x32_bf16 v[100:103], v[40:43], v[228:231], v[100:103]\nds_read_b128 v[40:43], v32 offset:0\ns_waitcnt lgkmcnt(3)\n"
"v_mfma_f32_16x16x32_bf16 v[104:107], v[44:47], v[224:227], v[104:107]\nv_add_u32 v33, s54, v33\nv_mfma_f32_16x16x32_bf16 v[108:111], v[44:47], v[228:231], v[108:111]\nds_read_b128 v[44:47], v33 offset:0\n"
"s_waitcnt lgkmcnt(3)\nv_mfma_f32_16x16x32_bf16 v[96:99], v[48:51], v[232:235], v[96:99]\nv_add_u32 v34, s54, v34\nv_mfma_f32_16x16x32_bf16 v[100:103], v[48:51], v[236:239], v[100:103]\n"
"ds_read_b128 v[48:51], v34 offset:0\ns_waitcnt lgkmcnt(3)\nv_mfma_f32_16x16x32_bf16 v[104:107], v[52:55], v[232:235], v[104:107]\nv_add_u32 v35, s54, v35\n"
"v_mfma_f32_16x16x32_bf16 v[108:111], v[52:55], v[236:239], v[108:111]\nds_read_b128 v[52:55], v35 offset:0\ns_add_u32 s52, s51, s32\ns_add_u32 s51, s51, s54\n"
"s_add_u32 s49, s49, 28672\ns_waitcnt vmcnt(7)\ns_barrier\ns_waitcnt lgkmcnt(3)\n"
"v_mfma_f32_16x16x32_bf16 v[64:67], v[40:43], v[128:131], 0\nv_mov_b32 v36, s48\ns_add_u32 s48, s48, 1\ns_nop 0\n"
"buffer_store_dword v36, v19, s[24:27], s33 offen sc1\nbuffer_load_dword v37, v20, s[24:27], 0 offen sc1\nv_mfma_f32_16x16x32_bf16 v[68:71], v[40:43], v[132:135], 0\nds_read_b128 v[40:43], v32 offset:4096\n"
"s_waitcnt lgkmcnt(3)\nv_mfma_f32_16x16x32_bf16 v[72:75], v[44:47], v[128:131], 0\nv_mfma_f32_16x16x32_bf16 v[76:79], v[44:47], v[132:135], 0\nds_read_b128 v[44:47], v33 offset:4096\n"
"v_cvt_pk_bf16_f32 v56, v96, v97\ns_waitcnt lgkmcnt(3)\nv_mfma_f32_16x16x32_bf16 v[64:67], v[48:51], v[136:139], v[64:67]\nv_cvt_pk_bf16_f32 v57, v98, v99\n"
"v_mfma_f32_16x16x32_bf16 v[68:71], v[48:51], v[140:143], v[68:71]\nds_read_b128 v[48:51], v34 offset:4096\nv_cvt_pk_bf16_f32 v58, v104, v105\ns_waitcnt lgkmcnt(3)\n"
"v_mfma_f32_16x16x32_bf16 v[72:75], v[52:55], v[136:139], v[72:75]\nv_cvt_pk_bf16_f32 v59, v106, v107\nv_mfma_f32_16x16x32_bf16 v[76:79], v[52:55], v[140:143], v[76:79]\nds_read_b128 v[52:55], v35 offset:4096\n"
"s_waitcnt lgkmcnt(3)\nv_mfma_f32_16x16x32_bf16 v[64:67], v[40:43], v[144:147], v[64:67]\nv_mfma_f32_16x16x32_bf16 v[68:71], v[40:43], v[148:151], v[68:71]\nds_read_b128 v[40:43], v32 offset:8192\n"
"buffer_store_dwordx4 v[56:59], v15, s[20:23], s50 offen sc1\ns_waitcnt lgkmcnt(3)\nv_mfma_f32_16x16x32_bf16 v[72:75], v[44:47], v[144:147], v[72:75]\nv_cvt_pk_bf16_f32 v60, v100, v101\n"
"v_mfma_f32_16x16x32_bf16 v[76:79], v[44:47], v[148:151], v[76:79]\nds_read_b128 v[44:47], v33 offset:8192\nv_cvt_pk_bf16_f32 v61, v102, v103\ns_waitcnt lgkmcnt(3)\n"
"v_mfma_f32_16x16x32_bf16 v[64:67], v[48:51], v[152:155], v[64:67]\nv_cvt_pk_bf16_f32 v62, v108, v109\nv_mfma_f32_16x16x32_bf16 v[68:71], v[48:51], v[156:159], v[68:71]\nds_read_b128 v[48:51], v34 offset:8192\n"
"v_cvt_pk_bf16_f32 v63, v110, v111\ns_waitcnt lgkmcnt(3)\nv_mfma_f32_16x16x32_bf16 v[72:75], v[52:55], v[152:155], v[72:75]\nv_mfma_f32_16x16x32_bf16 v[76:79], v[52:55], v[156:159], v[76:79]\n"
"ds_read_b128 v[52:55], v35 offset:8192\ns_waitcnt lgkmcnt(3)\nv_mfma_f32_16x16x32_bf16 v[64:67], v[40:43], v[160:163], v[64:67]\nbuffer_store_dwordx4 v[60:63], v16, s[20:23], s50 offen sc1\n"
"v_mfma_f32_16x16x32_bf16 v[68:71], v[40:43], v[164:167], v[68:71]\nds_read_b128 v[40:43], v32 offset:12288\ns_add_u32 s50, s50, s36\ns_waitcnt lgkmcnt(3)\n"
"v_mfma_f32_16x16x32_bf16 v[72:75], v[44:47], v[160:163], v[72:75]\ns_add_u32 m0, s52, 0\nv_mfma_f32_16x16x32_bf16 v[76:79], v[44:47], v[164:167], v[76:79]\nds_read_b128 v[44:47], v33 offset:12288\n"
"s_lshl_b32 s53, s40, 7\ns_add_u32 s53, s53, s49\ns_waitcnt lgkmcnt(3)\nv_mfma_f32_16x16x32_bf16 v[64:67], v[48:51], v[168:171], v[64:67]\n"
"buffer_load_dwordx4 v10, s[12:15], s53 offen lds\nv_mfma_f32_16x16x32_bf16 v[68:71], v[48:51], v[172:175], v[68:71]\nds_read_b128 v[48:51], v34 offset:12288\ns_add_u32 m0, s52, 4096\n"
"s_waitcnt lgkmcnt(3)\nv_mfma_f32_16x16x32_bf16 v[72:75], v[52:55], v[168:171], v[72:75]\ns_lshl_b32 s53, s41, 7\ns_add_u32 s53, s53, s49\n"
"v_mfma_f32_16x16x32_bf16 v[76:79], v[52:55], v[172:175], v[76:79]\nds_read_b128 v[52:55], v35 offset:12288\nbuffer_load_dwordx4 v10, s[12:15], s53 offen lds\ns_waitcnt lgkmcnt(3)\n"
"v_mfma_f32_16x16x32_bf16 v[64:67], v[40:43], v[176:179], v[64:67]\ns_add_u32 m0, s52, 8192\nv_mfma_f32_16x16x32_bf16 v[68:71], v[40:43], v[180:183], v[68:71]\nds_read_b128 v[40:43], v32 offset:16384\n"
"s_lshl_b32 s53, s42, 7\ns_add_u32 s53, s53, s49\ns_waitcnt lgkmcnt(3)\nv_mfma_f32_16x16x32_bf16 v[72:75], v[44:47], v[176:179], v[72:75]\n"
"buffer_load_dwordx4 v10, s[12:15], s53 offen lds\nv_mfma_f32_16x16x32_bf16 v[76:79], v[44:47], v[180:183], v[76:79]\nds_read_b128 v[44:47], v33 offset:16384\ns_add_u32 m0, s52, 12288\n"
"s_waitcnt lgkmcnt(3)\nv_mfma_f32_16x16x32_bf16 v[64:67], v[48:51], v[184:187], v[64:67]\ns_lshl_b32 s53, s43, 7\ns_add_u32 s53, s53, s49\n"
"v_mfma_f32_16x16x32_bf16 v[68:71], v[48:51], v[188:191], v[68:71]\nds_read_b128 v[48:51], v34 offset:16384\nbuffer_load_dwordx4 v10, s[12:15], s53 offen lds\ns_waitcnt lgkmcnt(3)\n"
"v_mfma_f32_16x16x32_bf16 v[72:75], v[52:55], v[184:187], v[72:75]\ns_add_u32 m0, s52, 16384\nv_mfma_f32_16x16x32_bf16 v[76:79], v[52:55], v[188:191], v[76:79]\nds_read_b128 v[52:55], v35 offset:16384\n"
"s_lshl_b32 s53, s44, 7\ns_add_u32 s53, s53, s49\ns_waitcnt lgkmcnt(3)\nv_mfma_f32_16x16x32_bf16 v[64:67], v[40:43], v[192:195], v[64:67]\n"
"buffer_load_dwordx4 v10, s[12:15], s53 offen lds\nv_mfma_f32_16x16x32_bf16 v[68:71], v[40:43], v[196:199], v[68:71]\nds_read_b128 v[40:43], v32 offset:20480\ns_add_u32 m0, s52, 20480\n"
"s_waitcnt lgkmcnt(3)\nv_mfma_f32_16x16x32_bf16 v[72:75], v[44:47], v[192:195], v[72:75]\ns_lshl_b32 s53, s45, 7\ns_add_u32 s53, s53, s49\n"
"v_mfma_f32_16x16x32_bf16 v[76:79], v[44:47], v[196:199], v[76:79]\nds_read_b128 v[44:47], v33 offset:20480\nbuffer_load_dwordx4 v10, s[12:15], s53 offen lds\ns_waitcnt lgkmcnt(3)\n"
"v_mfma_f32_16x16x32_bf16 v[64:67], v[48:51], v[200:203], v[64:67]\ns_add_u32 m0, s52, 24576\nv_mfma_f32_16x16x32_bf16 v[68:71], v[48:51], v[204:207], v[68:71]\nds_read_b128 v[48:51], v34 offset:20480\n"
"s_lshl_b32 s53, s46, 7\ns_add_u32 s53, s53, s49\ns_waitcnt lgkmcnt(3)\nv_mfma_f32_16x16x32_bf16 v[72:75], v[52:55], v[200:203], v[72:75]\n"
"buffer_load_dwordx4 v10, s[12:15], s53 offen lds\nv_mfma_f32_16x16x32_bf16 v[76:79], v[52:55], v[204:207], v[76:79]\nds_read_b128 v[52:55], v35 offset:20480\ns_waitcnt lgkmcnt(3)\n"
"v_mfma_f32_16x16x32_bf16 v[64:67], v[40:43], v[208:211], v[64:67]\nv_mfma_f32_16x16x32_bf16 v[68:71], v[40:43], v[212:215], v[68:71]\nds_read_b128 v[40:43], v32 offset:24576\ns_waitcnt lgkmcnt(3)\n"
"v_mfma_f32_16x16x32_bf16 v[72:75], v[44:47], v[208:211], v[72:75]\nv_mfma_f32_16x16x32_bf16 v[76:79], v[44:47], v[212:215], v[76:79]\nds_read_b128 v[44:47], v33 offset:24576\ns_waitcnt lgkmcnt(3)\n"
"v_mfma_f32_16x16x32_bf16 v[64:67], v[48:51], v[216:219], v[64:67]\ns_add_u32 s54, s51, 32768\ns_cmp_eq_u32 s54, 163840\nv_mfma_f32_16x16x32_bf16 v[68:71], v[48:51], v[220:223], v[68:71]\n"
"s_cselect_b32 s54, 0, s54\ns_sub_u32 s54, s54, s51\nds_read_b128 v[48:51], v34 offset:24576\ns_waitcnt lgkmcnt(3)\n"
"v_mfma_f32_16x16x32_bf16 v[72:75], v[52:55], v[216:219], v[72:75]\nv_mfma_f32_16x16x32_bf16 v[76:79], v[52:55], v[220:223], v[76:79]\nds_read_b128 v[52:55], v35 offset:24576\ns_waitcnt lgkmcnt(3)\n"
"v_mfma_f32_16x16x32_bf16 v[64:67], v[40:43], v[224:227], v[64:67]\nv_add_u32 v32, s54, v32\nv_mfma_f32_16x16x32_bf16 v[68:71], v[40:43], v[228:231], v[68:71]\nds_read_b128 v[40:43], v32 offset:0\n"
"s_waitcnt lgkmcnt(3)\nv_mfma_f32_16x16x32_bf16 v[72:75], v[44:47], v[224:227], v[72:75]\nv_add_u32 v33, s54, v33\nv_mfma_f32_16x16x32_bf16 v[76:79], v[44:47], v[228:231], v[76:79]\n"
"ds_read_b128 v[44:47], v33 offset:0\ns_waitcnt lgkmcnt(3)\nv_mfma_f32_16x16x32_bf16 v[64:67], v[48:51], v[232:235], v[64:67]\nv_add_u32 v34, s54, v34\n"
"v_mfma_f32_16x16x32_bf16 v[68:71], v[48:51], v[236:239], v[68:71]\nds_read_b128 v[48:51], v34 offset:0\ns_waitcnt lgkmcnt(3)\nv_mfma_f32_16x16x32_bf16 v[72:75], v[52:55], v[232:235], v[72:75]\n"
"v_add_u32 v35, s54, v35\nv_mfma_f32_16x16x32_bf16 v[76:79], v[52:55], v[236:239], v[76:79]\nds_read_b128 v[52:55], v35 offset:0\ns_add_u32 s52, s51, s32\n"
"s_add_u32 s51, s51, s54\ns_add_u32 s49, s49, 28672\ns_waitcnt vmcnt(7)\nfg1_pf:\n"
"v_cmp_ne_u32 vcc, s28, v37\ns_cbranch_vccz fg1_pfok\ns_add_u32 s55, s55, 1\ns_cmp_gt_u32 s55, 262144\n"
"s_cbranch_scc0 fg1_pf2\ns_endpgm\nfg1_pf2:\ns_sleep 2\n"
"buffer_load_dword v37, v20, s[24:27], 0 offen sc1\ns_waitcnt vmcnt(0)\ns_branch fg1_pf\nfg1_pfok:\n"
"s_barrier\ns_waitcnt lgkmcnt(3)\nv_mfma_f32_16x16x32_bf16 v[96:99], v[40:43], v[128:131], 0\nv_mov_b32 v36, s48\n"
"s_add_u32 s48, s48, 1\ns_nop 0\nbuffer_store_dword v36, v19, s[24:27], s33 offen sc1\nv_mfma_f32_16x16x32_bf16 v[100:103], v[40:43], v[132:135], 0\n"
"ds_read_b128 v[40:43], v32 offset:4096\ns_waitcnt lgkmcnt(3)\nv_mfma_f32_16x16x32_bf16 v[104:107], v[44:47], v[128:131], 0\nv_mfma_f32_16x16x32_bf16 v[108:111], v[44:47], v[132:135], 0\n"
"ds_read_b128 v[44:47], v33 offset:4096\nv_cvt_pk_bf16_f32 v56, v64, v65\ns_waitcnt lgkmcnt(3)\nv_mfma_f32_16x16x32_bf16 v[96:99], v[48:51], v[136:139], v[96:99]\n"
"v_cvt_pk_bf16_f32 v57, v66, v67\nv_mfma_f32_16x16x32_bf16 v[100:103], v[48:51], v[140:143], v[100:103]\nds_read_b128 v[48:51], v34 offset:4096\nv_cvt_pk_bf16_f32 v58, v72, v73\n"
"s_waitcnt lgkmcnt(3)\nv_mfma_f32_16x16x32_bf16 v[104:107], v[52:55], v[136:139], v[104:107]\nv_cvt_pk_bf16_f32 v59, v74, v75\nv_mfma_f32_16x16x32_bf16 v[108:111], v[52:55], v[140:143], v[108:111]\n"
"ds_read_b128 v[52:55], v35 offset:4096\ns_waitcnt lgkmcnt(3)\nv_mfma_f32_16x16x32_bf16 v[96:99], v[40:43], v[144:147], v[96:99]\nv_mfma_f32_16x16x32_bf16 v[100:103], v[40:43], v[148:151], v[100:103]\n"
"ds_read_b128 v[40:43], v32 offset:8192\nbuffer_store_dwordx4 v[56:59], v15, s[20:23], s50 offen sc1\ns_waitcnt lgkmcnt(3)\nv_mfma_f32_16x16x32_bf16 v[104:107], v[44:47], v[144:147], v[104:107]\n"
"v_cvt_pk_bf16_f32 v60, v68, v69\nv_mfma_f32_16x16x32_bf16 v[108:111], v[44:47], v[148:151], v[108:111]\nds_read_b128 v[44:47], v33 offset:8192\nv_cvt_pk_bf16_f32 v61, v70, v71\n"
"s_waitcnt lgkmcnt(3)\nv_mfma_f32_16x16x32_bf16 v[96:99], v[48:51], v[152:155], v[96:99]\nv_cvt_pk_bf16_f32 v62, v76, v77\nv_mfma_f32_16x16x32_bf16 v[100:103], v[48:51], v[156:159], v[100:103]\n"
"ds_read_b128 v[48:51], v34 offset:8192\nv_cvt_pk_bf16_f32 v63, v78, v79\ns_waitcnt lgkmcnt(3)\nv_mfma_f32_16x16x32_bf16 v[104:107], v[52:55], v[152:155], v[104:107]\n"
"v_mfma_f32_16x16x32_bf16 v[108:111], v[52:55], v[156:159], v[108:111]\nds_read_b128 v[52:55], v35 offset:8192\ns_waitcnt lgkmcnt(3)\nv_mfma_f32_16x16x32_bf16 v[96:99], v[40:43], v[160:163], v[96:99]\n"
"buffer_store_dwordx4 v[60:63], v16, s[20:23], s50 offen sc1\nv_mfma_f32_16x16x32_bf16 v[100:103], v[40:43], v[164:167], v[100:103]\nds_read_b128 v[40:43], v32 offset:12288\ns_add_u32 s50, s50, s36\n"
"s_waitcnt lgkmcnt(3)\nv_mfma_f32_16x16x32_bf16 v[104:107], v[44:47], v[160:163], v[104:107]\ns_add_u32 m0, s52, 0\nv_mfma_f32_16x16x32_bf16 v[108:111], v[44:47], v[164:167], v[108:111]\n"
"ds_read_b128 v[44:47], v33 offset:12288\ns_lshl_b32 s53, s40, 7\ns_add_u32 s53, s53, s49\ns_waitcnt lgkmcnt(3)\n"
"v_mfma_f32_16x16x32_bf16 v[96:99], v[48:51], v[168:171], v[96:99]\nbuffer_load_dwordx4 v10, s[12:15], s53 offen lds\nv_mfma_f32_16x16x32_bf16 v[100:103], v[48:51], v[172:175], v[100:103]\nds_read_b128 v[48:51], v34 offset:12288\n"
"s_add_u32 m0, s52, 4096\ns_waitcnt lgkmcnt(3)\nv_mfma_f32_16x16x32_bf16 v[104:107], v[52:55], v[168:171], v[104:107]\ns_lshl_b32 s53, s41, 7\n"
"s_add_u32 s53, s53, s49\nv_mfma_f32_16x16x32_bf16 v[108:111], v[52:55], v[172:175], v[108:111]\nds_read_b128 v[52:55], v35 offset:12288\nbuffer_load_dwordx4 v10, s[12:15], s53 offen lds\n"
"s_waitcnt lgkmcnt(3)\nv_mfma_f32_16x16x32_bf16 v[96:99], v[40:43], v[176:179], v[96:99]\ns_add_u32 m0, s52, 8192\nv_mfma_f32_16x16x32_bf16 v[100:103], v[40:43], v[180:183], v[100:103]\n"
"ds_read_b128 v[40:43], v32 offset:16384\ns_lshl_b32 s53, s42, 7\ns_add_u32 s53, s53, s49\ns_waitcnt lgkmcnt(3)\n"
"v_mfma_f32_16x16x32_bf16 v[104:107], v[44:47], v[176:179], v[104:107]\nbuffer_load_dwordx4 v10, s[12:15], s53 offen lds\nv_mfma_f32_16x16x32_bf16 v[108:111], v[44:47], v[180:183], v[108:111]\nds_read_b128 v[44:47], v33 offset:16384\n"
"s_add_u32 m0, s52, 12288\ns_waitcnt lgkmcnt(3)\nv_mfma_f32_16x16x32_bf16 v[96:99], v[48:51], v[184:187], v[96:99]\ns_lshl_b32 s53, s43, 7\n"
"s_add_u32 s53, s53, s49\nv_mfma_f32_16x16x32_bf16 v[100:103], v[48:51], v[188:191], v[100:103]\nds_read_b128 v[48:51], v34 offset:16384\nbuffer_load_dwordx4 v10, s[12:15], s53 offen lds\n"
"s_waitcnt lgkmcnt(3)\nv_mfma_f32_16x16x32_bf16 v[104:107], v[52:55], v[184:187], v[104:107]\ns_add_u32 m0, s52, 16384\nv_mfma_f32_16x16x32_bf16 v[108:111], v[52:55], v[188:191], v[108:111]\n"
"ds_read_b128 v[52:55], v35 offset:16384\ns_lshl_b32 s53, s44, 7\ns_add_u32 s53, s53, s49\ns_waitcnt lgkmcnt(3)\n"
"v_mfma_f32_16x16x32_bf16 v[96:99], v[40:43], v[192:195], v[96:99]\nbuffer_load_dwordx4 v10, s[12:15], s53 offen lds\nv_mfma_f32_16x16x32_bf16 v[100:103], v[40:43], v[196:199], v[100:103]\nds_read_b128 v[40:43], v32 offset:20480\n"
"s_add_u32 m0, s52, 20480\ns_waitcnt lgkmcnt(3)\nv_mfma_f32_16x16x32_bf16 v[104:107], v[44:47], v[192:195], v[104:107]\ns_lshl_b32 s53, s45, 7\n"
"s_add_u32 s53, s53, s49\nv_mfma_f32_16x16x32_bf16 v[108:111], v[44:47], v[196:199], v[108:111]\nds_read_b128 v[44:47], v33 offset:20480\nbuffer_load_dwordx4 v10, s[12:15], s53 offen lds\n"
"s_waitcnt lgkmcnt(3)\nv_mfma_f32_16x16x32_bf16 v[96:99], v[48:51], v[200:203], v[96:99]\ns_add_u32 m0, s52, 24576\nv_mfma_f32_16x16x32_bf16 v[100:103], v[48:51], v[204:207], v[100:103]\n"
"ds_read_b128 v[48:51], v34 offset:20480\ns_lshl_b32 s53, s46, 7\ns_add_u32 s53, s53, s49\ns_waitcnt lgkmcnt(3)\n"
"v_mfma_f32_16x16x32_bf16 v[104:107], v[52:55], v[200:203], v[104:107]\nbuffer_load_dwordx4 v10, s[12:15], s53 offen lds\nv_mfma_f32_16x16x32_bf16 v[108:111], v[52:55], v[204:207], v[108:111]\nds_read_b128 v[52:55], v35 offset:20480\n"
"s_lshl_b32 s53, s40, 11\ns_add_u32 s53, s53, 0\ns_waitcnt lgkmcnt(3)\nv_mfma_f32_16x16x32_bf16 v[96:99], v[40:43], v[208:211], v[96:99]\n"
"buffer_load_dwordx4 a[0:3], v14, s[16:19], s53 offen sc1\nv_mfma_f32_16x16x32_bf16 v[100:103], v[40:43], v[212:215], v[100:103]\nds_read_b128 v[40:43], v32 offset:24576\ns_add_u32 s53, s53, 229376\n"
"buffer_load_dwordx4 a[4:7], v14, s[16:19], s53 offen sc1\ns_waitcnt lgkmcnt(3)\nv_mfma_f32_16x16x32_bf16 v[104:107], v[44:47], v[208:211], v[104:107]\ns_lshl_b32 s53, s40, 11\n"
"s_add_u32 s53, s53, 1024\nv_mfma_f32_16x16x32_bf16 v[108:111], v[44:47], v[212:215], v[108:111]\nds_read_b128 v[44:47], v33 offset:24576\nbuffer_load_dwordx4 a[8:11], v14, s[16:19], s53 offen sc1\n"
"s_waitcnt lgkmcnt(3)\nv_mfma_f32_16x16x32_bf16 v[96:99], v[48:51], v[216:219], v[96:99]\ns_add_u32 s54, s51, 32768\ns_cmp_eq_u32 s54, 163840\n"
"v_mfma_f32_16x16x32_bf16 v[100:103], v[48:51], v[220:223], v[100:103]\ns_cselect_b32 s54, 0, s54\ns_sub_u32 s54, s54, s51\nds_read_b128 v[48:51], v34 offset:24576\n"
"s_add_u32 s53, s53, 229376\nbuffer_load_dwordx4 a[12:15], v14, s[16:19], s53 offen sc1\ns_waitcnt lgkmcnt(3)\nv_mfma_f32_16x16x32_bf16 v[104:107], v[52:55], v[216:219], v[104:107]\n"
"s_lshl_b32 s53, s41, 11\ns_add_u32 s53, s53, 0\nv_mfma_f32_16x16x32_bf16 v[108:111], v[52:55], v[220:223], v[108:111]\nds_read_b128 v[52:55], v35 offset:24576\n"
"buffer_load_dwordx4 a[16:19], v14, s[16:19], s53 offen sc1\ns_waitcnt lgkmcnt(3)\nv_mfma_f32_16x16x32_bf16 v[96:99], v[40:43], v[224:227], v[96:99]\nv_add_u32 v32, s54, v32\n"
"s_add_u32 s53, s53, 229376\nbuffer_load_dwordx4 a[20:23], v14, s[16:19], s53 offen sc1\nv_mfma_f32_16x16x32_bf16 v[100:103], v[40:43], v[228:231], v[100:103]\nds_read_b128 v[40:43], v32 offset:0\n"
"s_lshl_b32 s53, s41, 11\ns_add_u32 s53, s53, 1024\ns_waitcnt lgkmcnt(3)\nv_mfma_f32_16x16x32_bf16 v[104:107], v[44:47], v[224:227], v[104:107]\n"
"v_add_u32 v33, s54, v33\nbuffer_load_dwordx4 a[24:27], v14, s[16:19], s53 offen sc1\nv_mfma_f32_16x16x32_bf16 v[108:111], v[44:47], v[228:231], v[108:111]\nds_read_b128 v[44:47], v33 offset:0\n"
"s_add_u32 s53, s53, 229376\nbuffer_load_dwordx4 a[28:31], v14, s[16:19], s53 offen sc1\ns_waitcnt lgkmcnt(3)\nv_mfma_f32_16x16x32_bf16 v[96:99], v[48:51], v[232:235], v[96:99]\n"
"v_add_u32 v34, s54, v34\ns_lshl_b32 s53, s42, 11\ns_add_u32 s53, s53, 0\nv_mfma_f32_16x16x32_bf16 v[100:103], v[48:51], v[236:239], v[100:103]\n"
"ds_read_b128 v[48:51], v34 offset:0\nbuffer_load_dwordx4 a[32:35], v14, s[16:19], s53 offen sc1\ns_waitcnt lgkmcnt(3)\nv_mfma_f32_16x16x32_bf16 v[104:107], v[52:55], v[232:235], v[104:107]\n"
"v_add_u32 v35, s54, v35\ns_add_u32 s53, s53, 229376\nbuffer_load_dwordx4 a[36:39], v14, s[16:19], s53 offen sc1\nv_mfma_f32_16x16x32_bf16 v[108:111], v[52:55], v[236:239], v[108:111]\n"
"ds_read_b128 v[52:55], v35 offset:0\ns_add_u32 s52, s51, s32\ns_add_u32 s51, s51, s54\ns_add_u32 s49, s49, 28672\n"
"s_waitcnt vmcnt(17)\ns_barrier\ns_waitcnt lgkmcnt(3)\nv_mfma_f32_16x16x32_bf16 v[64:67], v[40:43], v[128:131], 0\n"
"v_mov_b32 v36, s48\ns_add_u32 s48, s48, 1\ns_nop 0\nbuffer_store_dword v36, v19, s[24:27], s33 offen sc1\n"
"v_mfma_f32_16x16x32_bf16 v[68:71], v[40:43], v[132:135], 0\nds_read_b128 v[40:43], v32 offset:4096\ns_waitcnt lgkmcnt(3)\nv_mfma_f32_16x16x32_bf16 v[72:75], v[44:47], v[128:131], 0\n"
"v_mfma_f32_16x16x32_bf16 v[76:79], v[44:47], v[132:135], 0\nds_read_b128 v[44:47], v33 offset:4096\nv_cvt_pk_bf16_f32 v56, v96, v97\ns_waitcnt lgkmcnt(3)\n"
"v_mfma_f32_16x16x32_bf16 v[64:67], v[48:51], v[136:139], v[64:67]\nv_cvt_pk_bf16_f32 v57, v98, v99\nv_mfma_f32_16x16x32_bf16 v[68:71], v[48:51], v[140:143], v[68:71]\nds_read_b128 v[48:51], v34 offset:4096\n"
"v_cvt_pk_bf16_f32 v58, v104, v105\ns_waitcnt lgkmcnt(3)\nv_mfma_f32_16x16x32_bf16 v[72:75], v[52:55], v[136:139], v[72:75]\nv_cvt_pk_bf16_f32 v59, v106, v107\n"
"v_mfma_f32_16x16x32_bf16 v[76:79], v[52:55], v[140:143], v[76:79]\nds_read_b128 v[52:55], v35 offset:4096\ns_waitcnt lgkmcnt(3)\nv_mfma_f32_16x16x32_bf16 v[64:67], v[40:43], v[144:147], v[64:67]\n"
"v_mfma_f32_16x16x32_bf16 v[68:71], v[40:43], v[148:151], v[68:71]\nds_read_b128 v[40:43], v32 offset:8192\nbuffer_store_dwordx4 v[56:59], v15, s[20:23], s50 offen sc1\ns_waitcnt lgkmcnt(3)\n"
"v_mfma_f32_16x16x32_bf16 v[72:75], v[44:47], v[144:147], v[72:75]\nv_cvt_pk_bf16_f32 v60, v100, v101\nv_mfma_f32_16x16x32_bf16 v[76:79], v[44:47], v[148:151], v[76:79]\nds_read_b128 v[44:47], v33 offset:8192\n"
"v_cvt_pk_bf16_f32 v61, v102, v103\ns_waitcnt lgkmcnt(3)\nv_mfma_f32_16x16x32_bf16 v[64:67], v[48:51], v[152:155], v[64:67]\nv_cvt_pk_bf16_f32 v62, v108, v109\n"
"v_mfma_f32_16x16x32_bf16 v[68:71], v[48:51], v[156:159], v[68:71]\nds_read_b128 v[48:51], v34 offset:8192\nv_cvt_pk_bf16_f32 v63, v110, v111\ns_waitcnt lgkmcnt(3)\n"
"v_mfma_f32_16x16x32_bf16 v[72:75], v[52:55], v[152:155], v[72:75]\nv_mfma_f32_16x16x32_bf16 v[76:79], v[52:55], v[156:159], v[76:79]\nds_read_b128 v[52:55], v35 offset:8192\ns_waitcnt lgkmcnt(3)\n"
"v_mfma_f32_16x16x32_bf16 v[64:67], v[40:43], v[160:163], v[64:67]\nbuffer_store_dwordx4 v[60:63], v16, s[20:23], s50 offen sc1\nv_mfma_f32_16x16x32_bf16 v[68:71], v[40:43], v[164:167], v[68:71]\nds_read_b128 v[40:43], v32 offset:12288\n"
"s_add_u32 s50, s50, s36\ns_waitcnt lgkmcnt(3)\nv_mfma_f32_16x16x32_bf16 v[72:75], v[44:47], v[160:163], v[72:75]\ns_add_u32 m0, s52, 0\n"
"v_mfma_f32_16x16x32_bf16 v[76:79], v[44:47], v[164:167], v[76:79]\nds_read_b128 v[44:47], v33 offset:12288\ns_lshl_b32 s53, s40, 7\ns_add_u32 s53, s53, s49\n"
"s_waitcnt lgkmcnt(3)\nv_mfma_f32_16x16x32_bf16 v[64:67], v[48:51], v[168:171], v[64:67]\nbuffer_load_dwordx4 v10, s[12:15], s53 offen lds\nv_mfma_f32_16x16x32_bf16 v[68:71], v[48:51], v[172:175], v[68:71]\n"
"ds_read_b128 v[48:51], v34 offset:12288\ns_add_u32 m0, s52, 4096\ns_waitcnt lgkmcnt(3)\nv_mfma_f32_16x16x32_bf16 v[72:75], v[52:55], v[168:171], v[72:75]\n"
"s_lshl_b32 s53, s41, 7\ns_add_u32 s53, s53, s49\nv_mfma_f32_16x16x32_bf16 v[76:79], v[52:55], v[172:175], v[76:79]\nds_read_b128 v[52:55], v35 offset:12288\n"
"buffer_load_dwordx4 v10, s[12:15], s53 offen lds\ns_waitcnt lgkmcnt(3)\nv_mfma_f32_16x16x32_bf16 v[64:67], v[40:43], v[176:179], v[64:67]\ns_add_u32 m0, s52, 8192\n"
"v_mfma_f32_16x16x32_bf16 v[68:71], v[40:43], v[180:183], v[68:71]\nds_read_b128 v[40:43], v32 offset:16384\ns_lshl_b32 s53, s42, 7\ns_add_u32 s53, s53, s49\n"
"s_waitcnt lgkmcnt(3)\nv_mfma_f32_16x16x32_bf16 v[72:75], v[44:47], v[176:179], v[72:75]\nbuffer_load_dwordx4 v10, s[12:15], s53 offen lds\nv_mfma_f32_16x16x32_bf16 v[76:79], v[44:47], v[180:183], v[76:79]\n"
"ds_read_b128 v[44:47], v33 offset:16384\ns_add_u32 m0, s52, 12288\ns_waitcnt lgkmcnt(3)\nv_mfma_f32_16x16x32_bf16 v[64:67], v[48:51], v[184:187], v[64:67]\n"
"s_lshl_b32 s53, s43, 7\ns_add_u32 s53, s53, s49\nv_mfma_f32_16x16x32_bf16 v[68:71], v[48:51], v[188:191], v[68:71]\nds_read_b128 v[48:51], v34 offset:16384\n"
"buffer_load_dwordx4 v10, s[12:15], s53 offen lds\ns_waitcnt lgkmcnt(3)\nv_mfma_f32_16x16x32_bf16 v[72:75], v[52:55], v[184:187], v[72:75]\ns_add_u32 m0, s52, 16384\n"
"v_mfma_f32_16x16x32_bf16 v[76:79], v[52:55], v[188:191], v[76:79]\nds_read_b128 v[52:55], v35 offset:16384\ns_lshl_b32 s53, s44, 7\ns_add_u32 s53, s53, s49\n"
"s_waitcnt lgkmcnt(3)\nv_mfma_f32_16x16x32_bf16 v[64:67], v[40:43], v[192:195], v[64:67]\nbuffer_load_dwordx4 v10, s[12:15], s53 offen lds\nv_mfma_f32_16x16x32_bf16 v[68:71], v[40:43], v[196:199], v[68:71]\n"
"ds_read_b128 v[40:43], v32 offset:20480\ns_add_u32 m0, s52, 20480\ns_waitcnt lgkmcnt(3)\nv_mfma_f32_16x16x32_bf16 v[72:75], v[44:47], v[192:195], v[72:75]\n"
"s_lshl_b32 s53, s45, 7\ns_add_u32 s53, s53, s49\nv_mfma_f32_16x16x32_bf16 v[76:79], v[44:47], v[196:199], v[76:79]\nds_read_b128 v[44:47], v33 offset:20480\n"
"buffer_load_dwordx4 v10, s[12:15], s53 offen lds\ns_waitcnt lgkmcnt(3)\nv_mfma_f32_16x16x32_bf16 v[64:67], v[48:51], v[200:203], v[64:67]\ns_add_u32 m0, s52, 24576\n"
"v_mfma_f32_16x16x32_bf16 v[68:71], v[48:51], v[204:207], v[68:71]\nds_read_b128 v[48:51], v34 offset:20480\ns_lshl_b32 s53, s46, 7\ns_add_u32 s53, s53, s49\n"
"s_waitcnt lgkmcnt(3)\nv_mfma_f32_16x16x32_bf16 v[72:75], v[52:55], v[200:203], v[72:75]\nbuffer_load_dwordx4 v10, s[12:15], s53 offen lds\nv_mfma_f32_16x16x32_bf16 v[76:79], v[52:55], v[204:207], v[76:79]\n"
"ds_read_b128 v[52:55], v35 offset:20480\ns_lshl_b32 s53, s42, 11\ns_add_u32 s53, s53, 1024\ns_waitcnt lgkmcnt(3)\n"
"v_mfma_f32_16x16x32_bf16 v[64:67], v[40:43], v[208:211], v[64:67]\nbuffer_load_dwordx4 a[40:43], v14, s[16:19], s53 offen sc1\nv_mfma_f32_16x16x32_bf16 v[68:71], v[40:43], v[212:215], v[68:71]\nds_read_b128 v[40:43], v32 offset:24576\n"
"s_add_u32 s53, s53, 229376\nbuffer_load_dwordx4 a[44:47], v14, s[16:19], s53 offen sc1\ns_waitcnt lgkmcnt(3)\nv_mfma_f32_16x16x32_bf16 v[72:75], v[44:47], v[208:211], v[72:75]\n"
"s_lshl_b32 s53, s43, 11\ns_add_u32 s53, s53, 0\nv_mfma_f32_16x16x32_bf16 v[76:79], v[44:47], v[212:215], v[76:79]\nds_read_b128 v[44:47], v33 offset:24576\n"
"buffer_load_dwordx4 a[48:51], v14, s[16:19], s53 offen sc1\ns_waitcnt lgkmcnt(3)\nv_mfma_f32_16x16x32_bf16 v[64:67], v[48:51], v[216:219], v[64:67]\ns_add_u32 s54, s51, 32768\n"
"s_cmp_eq_u32 s54, 163840\nv_mfma_f32_16x16x32_bf16 v[68:71], v[48:51], v[220:223], v[68:71]\ns_cselect_b32 s54, 0, s54\ns_sub_u32 s54, s54, s51\n"
"ds_read_b128 v[48:51], v34 offset:24576\ns_add_u32 s53, s53, 229376\nbuffer_load_dwordx4 a[52:55], v14, s[16:19], s53 offen sc1\ns_waitcnt lgkmcnt(3)\n"
"v_mfma_f32_16x16x32_bf16 v[72:75], v[52:55], v[216:219], v[72:75]\ns_lshl_b32 s53, s43, 11\ns_add_u32 s53, s53, 1024\nv_mfma_f32_16x16x32_bf16 v[76:79], v[52:55], v[220:223], v[76:79]\n"
"ds_read_b128 v[52:55], v35 offset:24576\nbuffer_load_dwordx4 a[56:59], v14, s[16:19], s53 offen sc1\ns_waitcnt lgkmcnt(3)\nv_mfma_f32_16x16x32_bf16 v[64:67], v[40:43], v[224:227], v[64:67]\n"
"v_add_u32 v32, s54, v32\ns_add_u32 s53, s53, 229376\nbuffer_load_dwordx4 a[60:63], v14, s[16:19], s53 offen sc1\nv_mfma_f32_16x16x32_bf16 v[68:71], v[40:43], v[228:231], v[68:71]\n"
"ds_read_b128 v[40:43], v32 offset:0\ns_lshl_b32 s53, s44, 11\ns_add_u32 s53, s53, 0\ns_waitcnt lgkmcnt(3)\n"
"v_mfma_f32_16x16x32_bf16 v[72:75], v[44:47], v[224:227], v[72:75]\nv_add_u32 v33, s54, v33\nbuffer_load_dwordx4 a[64:67], v14, s[16:19], s53 offen sc1\nv_mfma_f32_16x16x32_bf16 v[76:79], v[44:47], v[228:231], v[76:79]\n"
"ds_read_b128 v[44:47], v33 offset:0\ns_add_u32 s53, s53, 229376\nbuffer_load_dwordx4 a[68:71], v14, s[16:19], s53 offen sc1\ns_waitcnt lgkmcnt(3)\n"
"v_mfma_f32_16x16x32_bf16 v[64:67], v[48:51], v[232:235], v[64:67]\nv_add_u32 v34, s54, v34\ns_lshl_b32 s53, s44, 11\ns_add_u32 s53, s53, 1024\n"
"v_mfma_f32_16x16x32_bf16 v[68:71], v[48:51], v[236:239], v[68:71]\nds_read_b128 v[48:51], v34 offset:0\nbuffer_load_dwordx4 a[72:75], v14, s[16:19], s53 offen sc1\ns_waitcnt lgkmcnt(3)\n"
"v_mfma_f32_16x16x32_bf16 v[72:75], v[52:55], v[232:235], v[72:75]\nv_add_u32 v35, s54, v35\ns_add_u32 s53, s53, 229376\nbuffer_load_dwordx4 a[76:79], v14, s[16:19], s53 offen sc1\n"
"v_mfma_f32_16x16x32_bf16 v[76:79], v[52:55], v[236:239], v[76:79]\nds_read_b128 v[52:55], v35 offset:0\ns_add_u32 s52, s51, s32\ns_add_u32 s51, s51, s54\n"
"s_add_u32 s49, s49, 28672\ns_waitcnt vmcnt(17)\ns_barrier\ns_waitcnt lgkmcnt(3)\n"
"v_mfma_f32_16x16x32_bf16 v[96:99], v[40:43], v[128:131], 0\nv_mov_b32 v36, s48\ns_add_u32 s48, s48, 1\ns_nop 0\n"
"buffer_store_dword v36, v19, s[24:27], s33 offen sc1\nv_mfma_f32_16x16x32_bf16 v[100:103], v[40:43], v[132:135], 0\nds_read_b128 v[40:43], v32 offset:4096\ns_waitcnt lgkmcnt(3)\n"
"v_mfma_f32_16x16x32_bf16 v[104:107], v[44:47], v[128:131], 0\nv_mfma_f32_16x16x32_bf16 v[108:111], v[44:47], v[132:135], 0\nds_read_b128 v[44:47], v33 offset:4096\nv_cvt_pk_bf16_f32 v56, v64, v65\n"
"s_waitcnt lgkmcnt(3)\nv_mfma_f32_16x16x32_bf16 v[96:99], v[48:51], v[136:139], v[96:99]\nv_cvt_pk_bf16_f32 v57, v66, v67\nv_mfma_f32_16x16x32_bf16 v[100:103], v[48:51], v[140:143], v[100:103]\n"
"ds_read_b128 v[48:51], v34 offset:4096\nv_cvt_pk_bf16_f32 v58, v72, v73\ns_waitcnt lgkmcnt(3)\nv_mfma_f32_16x16x32_bf16 v[104:107], v[52:55], v[136:139], v[104:107]\n"
"v_cvt_pk_bf16_f32 v59, v74, v75\nv_mfma_f32_16x16x32_bf16 v[108:111], v[52:55], v[140:143], v[108:111]\nds_read_b128 v[52:55], v35 offset:4096\ns_waitcnt lgkmcnt(3)\n"
"v_mfma_f32_16x16x32_bf16 v[96:99], v[40:43], v[144:147], v[96:99]\nv_mfma_f32_16x16x32_bf16 v[100:103], v[40:43], v[148:151], v[100:103]\nds_read_b128 v[40:43], v32 offset:8192\nbuffer_store_dwordx4 v[56:59], v15, s[20:23], s50 offen sc1\n"
"s_waitcnt lgkmcnt(3)\nv_mfma_f32_16x16x32_bf16 v[104:107], v[44:47], v[144:147], v[104:107]\nv_cvt_pk_bf16_f32 v60, v68, v69\nv_mfma_f32_16x16x32_bf16 v[108:111], v[44:47], v[148:151], v[108:111]\n"
"ds_read_b128 v[44:47], v33 offset:8192\nv_cvt_pk_bf16_f32 v61, v70, v71\ns_waitcnt lgkmcnt(3)\nv_mfma_f32_16x16x32_bf16 v[96:99], v[48:51], v[152:155], v[96:99]\n"
"v_cvt_pk_bf16_f32 v62, v76, v77\nv_mfma_f32_16x16x32_bf16 v[100:103], v[48:51], v[156:159], v[100:103]\nds_read_b128 v[48:51], v34 offset:8192\nv_cvt_pk_bf16_f32 v63, v78, v79\n"
"s_waitcnt lgkmcnt(3)\nv_mfma_f32_16x16x32_bf16 v[104:107], v[52:55], v[152:155], v[104:107]\nv_mfma_f32_16x16x32_bf16 v[108:111], v[52:55], v[156:159], v[108:111]\nds_read_b128 v[52:55], v35 offset:8192\n"
"s_waitcnt lgkmcnt(3)\nv_mfma_f32_16x16x32_bf16 v[96:99], v[40:43], v[160:163], v[96:99]\nbuffer_store_dwordx4 v[60:63], v16, s[20:23], s50 offen sc1\nv_mfma_f32_16x16x32_bf16 v[100:103], v[40:43], v[164:167], v[100:103]\n"
"ds_read_b128 v[40:43], v32 offset:12288\ns_add_u32 s50, s50, s36\ns_waitcnt lgkmcnt(3)\nv_mfma_f32_16x16x32_bf16 v[104:107], v[44:47], v[160:163], v[104:107]\n"
"s_add_u32 m0, s52, 0\nv_mfma_f32_16x16x32_bf16 v[108:111], v[44:47], v[164:167], v[108:111]\nds_read_b128 v[44:47], v33 offset:12288\ns_lshl_b32 s53, s40, 7\n"
"s_add_u32 s53, s53, s49\ns_waitcnt lgkmcnt(3)\nv_mfma_f32_16x16x32_bf16 v[96:99], v[48:51], v[168:171], v[96:99]\nbuffer_load_dwordx4 v10, s[12:15], s53 offen lds\n"
"v_mfma_f32_16x16x32_bf16 v[100:103], v[48:51], v[172:175], v[100:103]\nds_read_b128 v[48:51], v34 offset:12288\ns_add_u32 m0, s52, 4096\ns_waitcnt lgkmcnt(3)\n"
"v_mfma_f32_16x16x32_bf16 v[104:107], v[52:55], v[168:171], v[104:107]\ns_lshl_b32 s53, s41, 7\ns_add_u32 s53, s53, s49\nv_mfma_f32_16x16x32_bf16 v[108:111], v[52:55], v[172:175], v[108:111]\n"
"ds_read_b128 v[52:55], v35 offset:12288\nbuffer_load_dwordx4 v10, s[12:15], s53 offen lds\ns_waitcnt lgkmcnt(3)\nv_mfma_f32_16x16x32_bf16 v[96:99], v[40:43], v[176:179], v[96:99]\n"
"s_add_u32 m0, s52, 8192\nv_mfma_f32_16x16x32_bf16 v[100:103], v[40:43], v[180:183], v[100:103]\nds_read_b128 v[40:43], v32 offset:16384\ns_lshl_b32 s53, s42, 7\n"
"s_add_u32 s53, s53, s49\ns_waitcnt lgkmcnt(3)\nv_mfma_f32_16x16x32_bf16 v[104:107], v[44:47], v[176:179], v[104:107]\nbuffer_load_dwordx4 v10, s[12:15], s53 offen lds\n"
"v_mfma_f32_16x16x32_bf16 v[108:111], v[44:47], v[180:183], v[108:111]\nds_read_b128 v[44:47], v33 offset:16384\ns_add_u32 m0, s52, 12288\ns_waitcnt lgkmcnt(3)\n"
"v_mfma_f32_16x16x32_bf16 v[96:99], v[48:51], v[184:187], v[96:99]\ns_lshl_b32 s53, s43, 7\ns_add_u32 s53, s53, s49\nv_mfma_f32_16x16x32_bf16 v[100:103], v[48:51], v[188:191], v[100:103]\n"
"ds_read_b128 v[48:51], v34 offset:16384\nbuffer_load_dwordx4 v10, s[12:15], s53 offen lds\ns_waitcnt lgkmcnt(3)\nv_mfma_f32_16x16x32_bf16 v[104:107], v[52:55], v[184:187], v[104:107]\n"
"s_add_u32 m0, s52, 16384\nv_mfma_f32_16x16x32_bf16 v[108:111], v[52:55], v[188:191], v[108:111]\nds_read_b128 v[52:55], v35 offset:16384\ns_lshl_b32 s53, s44, 7\n"
"s_add_u32 s53, s53, s49\ns_waitcnt lgkmcnt(3)\nv_mfma_f32_16x16x32_bf16 v[96:99], v[40:43], v[192:195], v[96:99]\nbuffer_load_dwordx4 v10, s[12:15], s53 offen lds\n"
"v_mfma_f32_16x16x32_bf16 v[100:103], v[40:43], v[196:199], v[100:103]\nds_read_b128 v[40:43], v32 offset:20480\ns_add_u32 m0, s52, 20480\ns_waitcnt lgkmcnt(3)\n"
"v_mfma_f32_16x16x32_bf16 v[104:107], v[44:47], v[192:195], v[104:107]\ns_lshl_b32 s53, s45, 7\ns_add_u32 s53, s53, s49\nv_mfma_f32_16x16x32_bf16 v[108:111], v[44:47], v[196:199], v[108:111]\n"
"ds_read_b128 v[44:47], v33 offset:20480\nbuffer_load_dwordx4 v10, s[12:15], s53 offen lds\ns_waitcnt lgkmcnt(3)\nv_mfma_f32_16x16x32_bf16 v[96:99], v[48:51], v[200:203], v[96:99]\n"
"s_add_u32 m0, s52, 24576\nv_mfma_f32_16x16x32_bf16 v[100:103], v[48:51], v[204:207], v[100:103]\nds_read_b128 v[48:51], v34 offset:20480\ns_lshl_b32 s53, s46, 7\n"
"s_add_u32 s53, s53, s49\ns_waitcnt lgkmcnt(3)\nv_mfma_f32_16x16x32_bf16 v[104:107], v[52:55], v[200:203], v[104:107]\nbuffer_load_dwordx4 v10, s[12:15], s53 offen lds\n"
"v_mfma_f32_16x16x32_bf16 v[108:111], v[52:55], v[204:207], v[108:111]\nds_read_b128 v[52:55], v35 offset:20480\ns_lshl_b32 s53, s45, 11\ns_add_u32 s53, s53, 0\n"
"s_waitcnt lgkmcnt(3)\nv_mfma_f32_16x16x32_bf16 v[96:99], v[40:43], v[208:211], v[96:99]\nbuffer_load_dwordx4 a[80:83], v14, s[16:19], s53 offen sc1\nv_mfma_f32_16x16x32_bf16 v[100:103], v[40:43], v[212:215], v[100:103]\n"
"ds_read_b128 v[40:43], v32 offset:24576\ns_add_u32 s53, s53, 229376\nbuffer_load_dwordx4 a[84:87], v14, s[16:19], s53 offen sc1\ns_waitcnt lgkmcnt(3)\n"
"v_mfma_f32_16x16x32_bf16 v[104:107], v[44:47], v[208:211], v[104:107]\ns_lshl_b32 s53, s45, 11\ns_add_u32 s53, s53, 1024\nv_mfma_f32_16x16x32_bf16 v[108:111], v[44:47], v[212:215], v[108:111]\n"
"ds_read_b128 v[44:47], v33 offset:24576\nbuffer_load_dwordx4 a[88:91], v14, s[16:19], s53 offen sc1\ns_waitcnt lgkmcnt(3)\nv_mfma_f32_16x16x32_bf16 v[96:99], v[48:51], v[216:219], v[96:99]\n"
"s_add_u32 s54, s51, 32768\ns_cmp_eq_u32 s54, 163840\nv_mfma_f32_16x16x32_bf16 v[100:103], v[48:51], v[220:223], v[100:103]\ns_cselect_b32 s54, 0, s54\n"
"s_sub_u32 s54, s54, s51\nds_read_b128 v[48:51], v34 offset:24576\ns_add_u32 s53, s53, 229376\nbuffer_load_dwordx4 a[92:95], v14, s[16:19], s53 offen sc1\n"
"s_waitcnt lgkmcnt(3)\nv_mfma_f32_16x16x32_bf16 v[104:107], v[52:55], v[216:219], v[104:107]\ns_lshl_b32 s53, s46, 11\ns_add_u32 s53, s53, 0\n"
"v_mfma_f32_16x16x32_bf16 v[108:111], v[52:55], v[220:223], v[108:111]\nds_read_b128 v[52:55], v35 offset:24576\nbuffer_load_dwordx4 a[96:99], v14, s[16:19], s53 offen sc1\ns_waitcnt lgkmcnt(3)\n"
"v_mfma_f32_16x16x32_bf16 v[96:99], v[40:43], v[224:227], v[96:99]\nv_add_u32 v32, s54, v32\ns_add_u32 s53, s53, 229376\nbuffer_load_dwordx4 a[100:103], v14, s[16:19], s53 offen sc1\n"
"v_mfma_f32_16x16x32_bf16 v[100:103], v[40:43], v[228:231], v[100:103]\nds_read_b128 v[40:43], v32 offset:0\ns_lshl_b32 s53, s46, 11\ns_add_u32 s53, s53, 1024\n"
"s_waitcnt lgkmcnt(3)\nv_mfma_f32_16x16x32_bf16 v[104:107], v[44:47], v[224:227], v[104:107]\nv_add_u32 v33, s54, v33\nbuffer_load_dwordx4 a[104:107], v14, s[16:19], s53 offen sc1\n"
"v_mfma_f32_16x16x32_bf16 v[108:111], v[44:47], v[228:231], v[108:111]\nds_read_b128 v[44:47], v33 offset:0\ns_add_u32 s53, s53, 229376\nbuffer_load_dwordx4 a[108:111], v14, s[16:19], s53 offen sc1\n"
"s_waitcnt lgkmcnt(3)\nv_mfma_f32_16x16x32_bf16 v[96:99], v[48:51], v[232:235], v[96:99]\nv_add_u32 v34, s54, v34\nv_mfma_f32_16x16x32_bf16 v[100:103], v[48:51], v[236:239], v[100:103]\n"
"ds_read_b128 v[48:51], v34 offset:0\ns_waitcnt lgkmcnt(3)\nv_mfma_f32_16x16x32_bf16 v[104:107], v[52:55], v[232:235], v[104:107]\nv_add_u32 v35, s54, v35\n"
"v_mfma_f32_16x16x32_bf16 v[108:111], v[52:55], v[236:239], v[108:111]\nds_read_b128 v[52:55], v35 offset:0\ns_add_u32 s52, s51, s32\ns_add_u32 s51, s51, s54\n"
"s_add_u32 s49, s49, 28672\ns_cmp_lg_u32 s34, 0\ns_cbranch_scc1 fg1_stay\ns_nop 7\n"
"v_cvt_pk_bf16_f32 v56, v96, v97\nv_cvt_pk_bf16_f32 v57, v98, v99\nv_cvt_pk_bf16_f32 v58, v104, v105\nv_cvt_pk_bf16_f32 v59, v106, v107\n"
"s_nop 0\ns_nop 0\nbuffer_store_dwordx4 v[56:59], v15, s[20:23], s50 offen sc1\nv_cvt_pk_bf16_f32 v60, v100, v101\n"
"v_cvt_pk_bf16_f32 v61, v102, v103\nv_cvt_pk_bf16_f32 v62, v108, v109\nv_cvt_pk_bf16_f32 v63, v110, v111\ns_nop 0\n"
"s_nop 0\nbuffer_store_dwordx4 v[60:63], v16, s[20:23], s50 offen sc1\ns_add_u32 s48, s28, 64\nv_mov_b32 v36, s48\n"
"s_waitcnt vmcnt(0) lgkmcnt(0)\ns_barrier\nbuffer_store_dword v36, v19, s[24:27], s33 offen sc1\ns_branch fg1_end\n"
"fg1_stay:\ns_waitcnt vmcnt(0)\ns_barrier\ns_waitcnt lgkmcnt(3)\n"
"v_mfma_f32_16x16x32_bf16 v[64:67], v[40:43], v[128:131], 0\nv_mov_b32 v36, s48\ns_add_u32 s48, s48, 1\ns_nop 0\n"
"buffer_store_dword v36, v19, s[24:27], s33 offen sc1\nv_mfma_f32_16x16x32_bf16 v[68:71], v[40:43], v[132:135], 0\nv_mfma_f32_16x16x32_bf16 v[72:75], v[40:43], a[0:3], 0\nv_mfma_f32_16x16x32_bf16 v[76:79], v[40:43], a[4:7], 0\n"
"ds_read_b128 v[40:43], v32 offset:4096\nv_cvt_pk_bf16_f32 v56, v96, v97\ns_waitcnt lgkmcnt(3)\nv_mfma_f32_16x16x32_bf16 v[80:83], v[44:47], v[128:131], 0\n"
"v_cvt_pk_bf16_f32 v57, v98, v99\nv_mfma_f32_16x16x32_bf16 v[84:87], v[44:47], v[132:135], 0\nv_cvt_pk_bf16_f32 v58, v104, v105\nv_mfma_f32_16x16x32_bf16 v[88:91], v[44:47], a[0:3], 0\n"
"v_cvt_pk_bf16_f32 v59, v106, v107\nv_mfma_f32_16x16x32_bf16 v[92:95], v[44:47], a[4:7], 0\nds_read_b128 v[44:47], v33 offset:4096\ns_waitcnt lgkmcnt(3)\n"
"v_mfma_f32_16x16x32_bf16 v[64:67], v[48:51], v[136:139], v[64:67]\nv_mfma_f32_16x16x32_bf16 v[68:71], v[48:51], v[140:143], v[68:71]\nbuffer_store_dwordx4 v[56:59], v15, s[20:23], s50 offen sc1\nv_mfma_f32_16x16x32_bf16 v[72:75], v[48:51], a[8:11], v[72:75]\n"
"v_cvt_pk_bf16_f32 v60, v100, v101\nv_mfma_f32_16x16x32_bf16 v[76:79], v[48:51], a[12:15], v[76:79]\nds_read_b128 v[48:51], v34 offset:4096\nv_cvt_pk_bf16_f32 v61, v102, v103\n"
"s_waitcnt lgkmcnt(3)\nv_mfma_f32_16x16x32_bf16 v[80:83], v[52:55], v[136:139], v[80:83]\nv_cvt_pk_bf16_f32 v62, v108, v109\nv_mfma_f32_16x16x32_bf16 v[84:87], v[52:55], v[140:143], v[84:87]\n"
"v_cvt_pk_bf16_f32 v63, v110, v111\nv_mfma_f32_16x16x32_bf16 v[88:91], v[52:55], a[8:11], v[88:91]\nv_mfma_f32_16x16x32_bf16 v[92:95], v[52:55], a[12:15], v[92:95]\nds_read_b128 v[52:55], v35 offset:4096\n"
"s_waitcnt lgkmcnt(3)\nv_mfma_f32_16x16x32_bf16 v[64:67], v[40:43], v[144:147], v[64:67]\nbuffer_store_dwordx4 v[60:63], v16, s[20:23], s50 offen sc1\nv_mfma_f32_16x16x32_bf16 v[68:71], v[40:43], v[148:151], v[68:71]\n"
"s_add_u32 s50, s50, s36\nv_mfma_f32_16x16x32_bf16 v[72:75], v[40:43], a[16:19], v[72:75]\ns_add_u32 m0, s52, 0\nv_mfma_f32_16x16x32_bf16 v[76:79], v[40:43], a[20:23], v[76:79]\n"
"ds_read_b128 v[40:43], v32 offset:8192\ns_lshl_b32 s53, s40, 7\ns_add_u32 s53, s53, s49\ns_waitcnt lgkmcnt(3)\n"
"v_mfma_f32_16x16x32_bf16 v[80:83], v[44:47], v[144:147], v[80:83]\nbuffer_load_dwordx4 v10, s[12:15], s53 offen lds\nv_mfma_f32_16x16x32_bf16 v[84:87], v[44:47], v[148:151], v[84:87]\nv_mfma_f32_16x16x32_bf16 v[88:91], v[44:47], a[16:19], v[88:91]\n"
"v_mfma_f32_16x16x32_bf16 v[92:95], v[44:47], a[20:23], v[92:95]\nds_read_b128 v[44:47], v33 offset:8192\ns_waitcnt lgkmcnt(3)\nv_mfma_f32_16x16x32_bf16 v[64:67], v[48:51], v[152:155], v[64:67]\n"
"v_mfma_f32_16x16x32_bf16 v[68:71], v[48:51], v[156:159], v[68:71]\nv_mfma_f32_16x16x32_bf16 v[72:75], v[48:51], a[24:27], v[72:75]\nv_mfma_f32_16x16x32_bf16 v[76:79], v[48:51], a[28:31], v[76:79]\nds_read_b128 v[48:51], v34 offset:8192\n"
"s_waitcnt lgkmcnt(3)\nv_mfma_f32_16x16x32_bf16 v[80:83], v[52:55], v[152:155], v[80:83]\nv_mfma_f32_16x16x32_bf16 v[84:87], v[52:55], v[156:159], v[84:87]\ns_add_u32 m0, s52, 4096\n"
"v_mfma_f32_16x16x32_bf16 v[88:91], v[52:55], a[24:27], v[88:91]\ns_lshl_b32 s53, s41, 7\ns_add_u32 s53, s53, s49\nv_mfma_f32_16x16x32_bf16 v[92:95], v[52:55], a[28:31], v[92:95]\n"
"ds_read_b128 v[52:55], v35 offset:8192\nbuffer_load_dwordx4 v10, s[12:15], s53 offen lds\ns_waitcnt lgkmcnt(3)\nv_mfma_f32_16x16x32_bf16 v[64:67], v[40:43], v[160:163], v[64:67]\n"
"v_mfma_f32_16x16x32_bf16 v[68:71], v[40:43], v[164:167], v[68:71]\nv_mfma_f32_16x16x32_bf16 v[72:75], v[40:43], a[32:35], v[72:75]\nv_mfma_f32_16x16x32_bf16 v[76:79], v[40:43], a[36:39], v[76:79]\nds_read_b128 v[40:43], v32 offset:12288\n"
"s_waitcnt lgkmcnt(3)\nv_mfma_f32_16x16x32_bf16 v[80:83], v[44:47], v[160:163], v[80:83]\nv_mfma_f32_16x16x32_bf16 v[84:87], v[44:47], v[164:167], v[84:87]\nv_mfma_f32_16x16x32_bf16 v[88:91], v[44:47], a[32:35], v[88:91]\n"
"v_mfma_f32_16x16x32_bf16 v[92:95], v[44:47], a[36:39], v[92:95]\nds_read_b128 v[44:47], v33 offset:12288\ns_waitcnt lgkmcnt(3)\nv_mfma_f32_16x16x32_bf16 v[64:67], v[48:51], v[168:171], v[64:67]\n"
"s_add_u32 m0, s52, 8192\nv_mfma_f32_16x16x32_bf16 v[68:71], v[48:51], v[172:175], v[68:71]\ns_lshl_b32 s53, s42, 7\ns_add_u32 s53, s53, s49\n"
"v_mfma_f32_16x16x32_bf16 v[72:75], v[48:51], a[40:43], v[72:75]\nbuffer_load_dwordx4 v10, s[12:15], s53 offen lds\nv_mfma_f32_16x16x32_bf16 v[76:79], v[48:51], a[44:47], v[76:79]\nds_read_b128 v[48:51], v34 offset:12288\n"
"s_waitcnt lgkmcnt(3)\nv_mfma_f32_16x16x32_bf16 v[80:83], v[52:55], v[168:171], v[80:83]\nv_mfma_f32_16x16x32_bf16 v[84:87], v[52:55], v[172:175], v[84:87]\nv_mfma_f32_16x16x32_bf16 v[88:91], v[52:55], a[40:43], v[88:91]\n"
"v_mfma_f32_16x16x32_bf16 v[92:95], v[52:55], a[44:47], v[92:95]\nds_read_b128 v[52:55], v35 offset:12288\ns_waitcnt lgkmcnt(3)\nv_mfma_f32_16x16x32_bf16 v[64:67], v[40:43], v[176:179], v[64:67]\n"
"v_mfma_f32_16x16x32_bf16 v[68:71], v[40:43], v[180:183], v[68:71]\nv_mfma_f32_16x16x32_bf16 v[72:75], v[40:43], a[48:51], v[72:75]\nv_mfma_f32_16x16x32_bf16 v[76:79], v[40:43], a[52:55], v[76:79]\nds_read_b128 v[40:43], v32 offset:16384\n"
"s_add_u32 m0, s52, 12288\ns_waitcnt lgkmcnt(3)\nv_mfma_f32_16x16x32_bf16 v[80:83], v[44:47], v[176:179], v[80:83]\ns_lshl_b32 s53, s43, 7\n"
"s_add_u32 s53, s53, s49\nv_mfma_f32_16x16x32_bf16 v[84:87], v[44:47], v[180:183], v[84:87]\nbuffer_load_dwordx4 v10, s[12:15], s53 offen lds\nv_mfma_f32_16x16x32_bf16 v[88:91], v[44:47], a[48:51], v[88:91]\n"
"v_mfma_f32_16x16x32_bf16 v[92:95], v[44:47], a[52:55], v[92:95]\nds_read_b128 v[44:47], v33 offset:16384\ns_waitcnt lgkmcnt(3)\nv_mfma_f32_16x16x32_bf16 v[64:67], v[48:51], v[184:187], v[64:67]\n"
"v_mfma_f32_16x16x32_bf16 v[68:71], v[48:51], v[188:191], v[68:71]\nv_mfma_f32_16x16x32_bf16 v[72:75], v[48:51], a[56:59], v[72:75]\nv_mfma_f32_16x16x32_bf16 v[76:79], v[48:51], a[60:63], v[76:79]\nds_read_b128 v[48:51], v34 offset:16384\n"
"s_waitcnt lgkmcnt(3)\nv_mfma_f32_16x16x32_bf16 v[80:83], v[52:55], v[184:187], v[80:83]\nv_mfma_f32_16x16x32_bf16 v[84:87], v[52:55], v[188:191], v[84:87]\nv_mfma_f32_16x16x32_bf16 v[88:91], v[52:55], a[56:59], v[88:91]\n"
"s_add_u32 m0, s52, 16384\nv_mfma_f32_16x16x32_bf16 v[92:95], v[52:55], a[60:63], v[92:95]\nds_read_b128 v[52:55], v35 offset:16384\ns_lshl_b32 s53, s44, 7\n"
"s_add_u32 s53, s53, s49\ns_waitcnt lgkmcnt(3)\nv_mfma_f32_16x16x32_bf16 v[64:67], v[40:43], v[192:195], v[64:67]\nbuffer_load_dwordx4 v10, s[12:15], s53 offen lds\n"
"v_mfma_f32_16x16x32_bf16 v[68:71], v[40:43], v[196:199], v[68:71]\nv_mfma_f32_16x16x32_bf16 v[72:75], v[40:43], a[64:67], v[72:75]\nv_mfma_f32_16x16x32_bf16 v[76:79], v[40:43], a[68:71], v[76:79]\nds_read_b128 v[40:43], v32 offset:20480\n"
"s_waitcnt lgkmcnt(3)\nv_mfma_f32_16x16x32_bf16 v[80:83], v[44:47], v[192:195], v[80:83]\nv_mfma_f32_16x16x32_bf16 v[84:87], v[44:47], v[196:199], v[84:87]\nv_mfma_f32_16x16x32_bf16 v[88:91], v[44:47], a[64:67], v[88:91]\n"
"v_mfma_f32_16x16x32_bf16 v[92:95], v[44:47], a[68:71], v[92:95]\nds_read_b128 v[44:47], v33 offset:20480\ns_waitcnt lgkmcnt(3)\nv_mfma_f32_16x16x32_bf16 v[64:67], v[48:51], v[200:203], v[64:67]\n"
"v_mfma_f32_16x16x32_bf16 v[68:71], v[48:51], v[204:207], v[68:71]\ns_add_u32 m0, s52, 20480\nv_mfma_f32_16x16x32_bf16 v[72:75], v[48:51], a[72:75], v[72:75]\ns_lshl_b32 s53, s45, 7\n"
"s_add_u32 s53, s53, s49\nv_mfma_f32_16x16x32_bf16 v[76:79], v[48:51], a[76:79], v[76:79]\nds_read_b128 v[48:51], v34 offset:20480\nbuffer_load_dwordx4 v10, s[12:15], s53 offen lds\n"
"s_waitcnt lgkmcnt(3)\nv_mfma_f32_16x16x32_bf16 v[80:83], v[52:55], v[200:203], v[80:83]\nv_mfma_f32_16x16x32_bf16 v[84:87], v[52:55], v[204:207], v[84:87]\nv_mfma_f32_16x16x32_bf16 v[88:91], v[52:55], a[72:75], v[88:91]\n"
"v_mfma_f32_16x16x32_bf16 v[92:95], v[52:55], a[76:79], v[92:95]\nds_read_b128 v[52:55], v35 offset:20480\ns_waitcnt lgkmcnt(3)\nv_mfma_f32_16x16x32_bf16 v[64:67], v[40:43], v[208:211], v[64:67]\n"
"v_mfma_f32_16x16x32_bf16 v[68:71], v[40:43], v[212:215], v[68:71]\nv_mfma_f32_16x16x32_bf16 v[72:75], v[40:43], a[80:83], v[72:75]\nv_mfma_f32_16x16x32_bf16 v[76:79], v[40:43], a[84:87], v[76:79]\nds_read_b128 v[40:43], v32 offset:24576\n"
"s_waitcnt lgkmcnt(3)\nv_mfma_f32_16x16x32_bf16 v[80:83], v[44:47], v[208:211], v[80:83]\ns_add_u32 m0, s52, 24576\nv_mfma_f32_16x16x32_bf16 v[84:87], v[44:47], v[212:215], v[84:87]\n"
"s_lshl_b32 s53, s46, 7\ns_add_u32 s53, s53, s49\nv_mfma_f32_16x16x32_bf16 v[88:91], v[44:47], a[80:83], v[88:91]\nbuffer_load_dwordx4 v10, s[12:15], s53 offen lds\n"
"v_mfma_f32_16x16x32_bf16 v[92:95], v[44:47], a[84:87], v[92:95]\nds_read_b128 v[44:47], v33 offset:24576\ns_waitcnt lgkmcnt(3)\nv_mfma_f32_16x16x32_bf16 v[64:67], v[48:51], v[216:219], v[64:67]\n"
"s_add_u32 s54, s51, 32768\ns_cmp_eq_u32 s54, 163840\nv_mfma_f32_16x16x32_bf16 v[68:71], v[48:51], v[220:223], v[68:71]\nv_mfma_f32_16x16x32_bf16 v[72:75], v[48:51], a[88:91], v[72:75]\n"
"v_mfma_f32_16x16x32_bf16 v[76:79], v[48:51], a[92:95], v[76:79]\ns_cselect_b32 s54, 0, s54\ns_sub_u32 s54, s54, s51\nds_read_b128 v[48:51], v34 offset:24576\n"
"s_waitcnt lgkmcnt(3)\nv_mfma_f32_16x16x32_bf16 v[80:83], v[52:55], v[216:219], v[80:83]\nv_mfma_f32_16x16x32_bf16 v[84:87], v[52:55], v[220:223], v[84:87]\nv_mfma_f32_16x16x32_bf16 v[88:91], v[52:55], a[88:91], v[88:91]\n"
"v_mfma_f32_16x16x32_bf16 v[92:95], v[52:55], a[92:95], v[92:95]\nds_read_b128 v[52:55], v35 offset:24576\ns_waitcnt lgkmcnt(3)\nv_mfma_f32_16x16x32_bf16 v[64:67], v[40:43], v[224:227], v[64:67]\n"
"v_add_u32 v32, s54, v32\nv_mfma_f32_16x16x32_bf16 v[68:71], v[40:43], v[228:231], v[68:71]\nv_mfma_f32_16x16x32_bf16 v[72:75], v[40:43], a[96:99], v[72:75]\nv_mfma_f32_16x16x32_bf16 v[76:79], v[40:43], a[100:103], v[76:79]\n"
"ds_read_b128 v[40:43], v32 offset:0\ns_waitcnt lgkmcnt(3)\nv_mfma_f32_16x16x32_bf16 v[80:83], v[44:47], v[224:227], v[80:83]\nv_add_u32 v33, s54, v33\n"
"v_mfma_f32_16x16x32_bf16 v[84:87], v[44:47], v[228:231], v[84:87]\nv_mfma_f32_16x16x32_bf16 v[88:91], v[44:47], a[96:99], v[88:91]\nv_mfma_f32_16x16x32_bf16 v[92:95], v[44:47], a[100:103], v[92:95]\nds_read_b128 v[44:47], v33 offset:0\n"
"s_waitcnt lgkmcnt(3)\nv_mfma_f32_16x16x32_bf16 v[64:67], v[48:51], v[232:235], v[64:67]\nv_add_u32 v34, s54, v34\nv_mfma_f32_16x16x32_bf16 v[68:71], v[48:51], v[236:239], v[68:71]\n"
"v_mfma_f32_16x16x32_bf16 v[72:75], v[48:51], a[104:107], v[72:75]\nv_mfma_f32_16x16x32_bf16 v[76:79], v[48:51], a[108:111], v[76:79]\nds_read_b128 v[48:51], v34 offset:0\ns_waitcnt lgkmcnt(3)\n"
"v_mfma_f32_16x16x32_bf16 v[80:83], v[52:55], v[232:235], v[80:83]\nv_add_u32 v35, s54, v35\nv_mfma_f32_16x16x32_bf16 v[84:87], v[52:55], v[236:239], v[84:87]\nv_mfma_f32_16x16x32_bf16 v[88:91], v[52:55], a[104:107], v[88:91]\n"
"v_mfma_f32_16x16x32_bf16 v[92:95], v[52:55], a[108:111], v[92:95]\nds_read_b128 v[52:55], v35 offset:0\ns_add_u32 s52, s51, s32\ns_add_u32 s51, s51, s54\n"
"s_add_u32 s49, s49, 28672\ns_waitcnt vmcnt(7)\ns_barrier\ns_waitcnt lgkmcnt(3)\n"
"v_mfma_f32_16x16x32_bf16 v[96:99], v[40:43], v[128:131], 0\nv_mov_b32 v36, s48\ns_add_u32 s48, s48, 1\ns_nop 0\n"
"buffer_store_dword v36, v19, s[24:27], s33 offen sc1\nv_mfma_f32_16x16x32_bf16 v[100:103], v[40:43], v[132:135], 0\nv_mfma_f32_16x16x32_bf16 v[104:107], v[40:43], a[0:3], 0\nv_mfma_f32_16x16x32_bf16 v[108:111], v[40:43], a[4:7], 0\n"
"ds_read_b128 v[40:43], v32 offset:4096\nv_cvt_pk_bf16_f32 v56, v64, v65\ns_waitcnt lgkmcnt(3)\nv_mfma_f32_16x16x32_bf16 v[112:115], v[44:47], v[128:131], 0\n"
"v_cvt_pk_bf16_f32 v57, v66, v67\nv_mfma_f32_16x16x32_bf16 v[116:119], v[44:47], v[132:135], 0\nv_cvt_pk_bf16_f32 v58, v80, v81\nv_mfma_f32_16x16x32_bf16 v[120:123], v[44:47], a[0:3], 0\n"
"v_cvt_pk_bf16_f32 v59, v82, v83\nv_mfma_f32_16x16x32_bf16 v[124:127], v[44:47], a[4:7], 0\nds_read_b128 v[44:47], v33 offset:4096\ns_waitcnt lgkmcnt(3)\n"
"v_mfma_f32_16x16x32_bf16 v[96:99], v[48:51], v[136:139], v[96:99]\nv_mfma_f32_16x16x32_bf16 v[100:103], v[48:51], v[140:143], v[100:103]\nbuffer_store_dwordx4 v[56:59], v15, s[20:23], s50 offen sc1\nv_mfma_f32_16x16x32_bf16 v[104:107], v[48:51], a[8:11], v[104:107]\n"
"v_cvt_pk_bf16_f32 v60, v68, v69\nv_mfma_f32_16x16x32_bf16 v[108:111], v[48:51], a[12:15], v[108:111]\nds_read_b128 v[48:51], v34 offset:4096\nv_cvt_pk_bf16_f32 v61, v70, v71\n"
"s_waitcnt lgkmcnt(3)\nv_mfma_f32_16x16x32_bf16 v[112:115], v[52:55], v[136:139], v[112:115]\nv_cvt_pk_bf16_f32 v62, v84, v85\nv_mfma_f32_16x16x32_bf16 v[116:119], v[52:55], v[140:143], v[116:119]\n"
"v_cvt_pk_bf16_f32 v63, v86, v87\nv_mfma_f32_16x16x32_bf16 v[120:123], v[52:55], a[8:11], v[120:123]\nv_mfma_f32_16x16x32_bf16 v[124:127], v[52:55], a[12:15], v[124:127]\nds_read_b128 v[52:55], v35 offset:4096\n"
"s_waitcnt lgkmcnt(3)\nv_mfma_f32_16x16x32_bf16 v[96:99], v[40:43], v[144:147], v[96:99]\nbuffer_store_dwordx4 v[60:63], v16, s[20:23], s50 offen sc1\nv_mfma_f32_16x16x32_bf16 v[100:103], v[40:43], v[148:151], v[100:103]\n"
"v_cvt_pk_bf16_f32 v56, v72, v73\nv_mfma_f32_16x16x32_bf16 v[104:107], v[40:43], a[16:19], v[104:107]\nv_cvt_pk_bf16_f32 v57, v74, v75\nv_mfma_f32_16x16x32_bf16 v[108:111], v[40:43], a[20:23], v[108:111]\n"
"ds_read_b128 v[40:43], v32 offset:8192\nv_cvt_pk_bf16_f32 v58, v88, v89\ns_waitcnt lgkmcnt(3)\nv_mfma_f32_16x16x32_bf16 v[112:115], v[44:47], v[144:147], v[112:115]\n"
"v_cvt_pk_bf16_f32 v59, v90, v91\nv_mfma_f32_16x16x32_bf16 v[116:119], v[44:47], v[148:151], v[116:119]\nv_mfma_f32_16x16x32_bf16 v[120:123], v[44:47], a[16:19], v[120:123]\nv_mfma_f32_16x16x32_bf16 v[124:127], v[44:47], a[20:23], v[124:127]\n"
"ds_read_b128 v[44:47], v33 offset:8192\nbuffer_store_dwordx4 v[56:59], v17, s[20:23], s50 offen sc1\ns_waitcnt lgkmcnt(3)\nv_mfma_f32_16x16x32_bf16 v[96:99], v[48:51], v[152:155], v[96:99]\n"
"v_cvt_pk_bf16_f32 v60, v76, v77\nv_mfma_f32_16x16x32_bf16 v[100:103], v[48:51], v[156:159], v[100:103]\nv_cvt_pk_bf16_f32 v61, v78, v79\nv_mfma_f32_16x16x32_bf16 v[104:107], v[48:51], a[24:27], v[104:107]\n"
"v_cvt_pk_bf16_f32 v62, v92, v93\nv_mfma_f32_16x16x32_bf16 v[108:111], v[48:51], a[28:31], v[108:111]\nds_read_b128 v[48:51], v34 offset:8192\nv_cvt_pk_bf16_f32 v63, v94, v95\n"
"s_waitcnt lgkmcnt(3)\nv_mfma_f32_16x16x32_bf16 v[112:115], v[52:55], v[152:155], v[112:115]\nv_mfma_f32_16x16x32_bf16 v[116:119], v[52:55], v[156:159], v[116:119]\nv_mfma_f32_16x16x32_bf16 v[120:123], v[52:55], a[24:27], v[120:123]\n"
"buffer_store_dwordx4 v[60:63], v18, s[20:23], s50 offen sc1\nv_mfma_f32_16x16x32_bf16 v[124:127], v[52:55], a[28:31], v[124:127]\nds_read_b128 v[52:55], v35 offset:8192\ns_add_u32 s50, s50, s36\n"
"s_waitcnt lgkmcnt(3)\nv_mfma_f32_16x16x32_bf16 v[96:99], v[40:43], v[160:163], v[96:99]\ns_add_u32 m0, s52, 0\nv_mfma_f32_16x16x32_bf16 v[100:103], v[40:43], v[164:167], v[100:103]\n"
"s_lshl_b32 s53, s40, 7\ns_add_u32 s53, s53, s49\nv_mfma_f32_16x16x32_bf16 v[104:107], v[40:43], a[32:35], v[104:107]\nbuffer_load_dwordx4 v10, s[12:15], s53 offen lds\n"
"v_mfma_f32_16x16x32_bf16 v[108:111], v[40:43], a[36:39], v[108:111]\nds_read_b128 v[40:43], v32 offset:12288\ns_waitcnt lgkmcnt(3)\nv_mfma_f32_16x16x32_bf16 v[112:115], v[44:47], v[160:163], v[112:115]\n"
"v_mfma_f32_16x16x32_bf16 v[116:119], v[44:47], v[164:167], v[116:119]\nv_mfma_f32_16x16x32_bf16 v[120:123], v[44:47], a[32:35], v[120:123]\nv_mfma_f32_16x16x32_bf16 v[124:127], v[44:47], a[36:39], v[124:127]\nds_read_b128 v[44:47], v33 offset:12288\n"
"s_waitcnt lgkmcnt(3)\nv_mfma_f32_16x16x32_bf16 v[96:99], v[48:51], v[168:171], v[96:99]\nv_mfma_f32_16x16x32_bf16 v[100:103], v[48:51], v[172:175], v[100:103]\nv_mfma_f32_16x16x32_bf16 v[104:107], v[48:51], a[40:43], v[104:107]\n"
"v_mfma_f32_16x16x32_bf16 v[108:111], v[48:51], a[44:47], v[108:111]\nds_read_b128 v[48:51], v34 offset:12288\ns_add_u32 m0, s52, 4096\ns_waitcnt lgkmcnt(3)\n"
"v_mfma_f32_16x16x32_bf16 v[112:115], v[52:55], v[168:171], v[112:115]\ns_lshl_b32 s53, s41, 7\ns_add_u32 s53, s53, s49\nv_mfma_f32_16x16x32_bf16 v[116:119], v[52:55], v[172:175], v[116:119]\n"
"buffer_load_dwordx4 v10, s[12:15], s53 offen lds\nv_mfma_f32_16x16x32_bf16 v[120:123], v[52:55], a[40:43], v[120:123]\nv_mfma_f32_16x16x32_bf16 v[124:127], v[52:55], a[44:47], v[124:127]\nds_read_b128 v[52:55], v35 offset:12288\n"
"s_waitcnt lgkmcnt(3)\nv_mfma_f32_16x16x32_bf16 v[96:99], v[40:43], v[176:179], v[96:99]\nv_mfma_f32_16x16x32_bf16 v[100:103], v[40:43], v[180:183], v[100:103]\nv_mfma_f32_16x16x32_bf16 v[104:107], v[40:43], a[48:51], v[104:107]\n"
"v_mfma_f32_16x16x32_bf16 v[108:111], v[40:43], a[52:55], v[108:111]\nds_read_b128 v[40:43], v32 offset:16384\ns_waitcnt lgkmcnt(3)\nv_mfma_f32_16x16x32_bf16 v[112:115], v[44:47], v[176:179], v[112:115]\n"
"v_mfma_f32_16x16x32_bf16 v[116:119], v[44:47], v[180:183], v[116:119]\nv_mfma_f32_16x16x32_bf16 v[120:123], v[44:47], a[48:51], v[120:123]\ns_add_u32 m0, s52, 8192\nv_mfma_f32_16x16x32_bf16 v[124:127], v[44:47], a[52:55], v[124:127]\n"
"ds_read_b128 v[44:47], v33 offset:16384\ns_lshl_b32 s53, s42, 7\ns_add_u32 s53, s53, s49\ns_waitcnt lgkmcnt(3)\n"
"v_mfma_f32_16x16x32_bf16 v[96:99], v[48:51], v[184:187], v[96:99]\nbuffer_load_dwordx4 v10, s[12:15], s53 offen lds\nv_mfma_f32_16x16x32_bf16 v[100:103], v[48:51], v[188:191], v[100:103]\nv_mfma_f32_16x16x32_bf16 v[104:107], v[48:51], a[56:59], v[104:107]\n"
"v_mfma_f32_16x16x32_bf16 v[108:111], v[48:51], a[60:63], v[108:111]\nds_read_b128 v[48:51], v34 offset:16384\ns_waitcnt lgkmcnt(3)\nv_mfma_f32_16x16x32_bf16 v[112:115], v[52:55], v[184:187], v[112:115]\n"
"v_mfma_f32_16x16x32_bf16 v[116:119], v[52:55], v[188:191], v[116:119]\nv_mfma_f32_16x16x32_bf16 v[120:123], v[52:55], a[56:59], v[120:123]\nv_mfma_f32_16x16x32_bf16 v[124:127], v[52:55], a[60:63], v[124:127]\nds_read_b128 v[52:55], v35 offset:16384\n"
"s_waitcnt lgkmcnt(3)\nv_mfma_f32_16x16x32_bf16 v[96:99], v[40:43], v[192:195], v[96:99]\nv_mfma_f32_16x16x32_bf16 v[100:103], v[40:43], v[196:199], v[100:103]\ns_add_u32 m0, s52, 12288\n"
"v_mfma_f32_16x16x32_bf16 v[104:107], v[40:43], a[64:67], v[104:107]\ns_lshl_b32 s53, s43, 7\ns_add_u32 s53, s53, s49\nv_mfma_f32_16x16x32_bf16 v[108:111], v[40:43], a[68:71], v[108:111]\n"
"ds_read_b128 v[40:43], v32 offset:20480\nbuffer_load_dwordx4 v10, s[12:15], s53 offen lds\ns_waitcnt lgkmcnt(3)\nv_mfma_f32_16x16x32_bf16 v[112:115], v[44:47], v[192:195], v[112:115]\n"
"v_mfma_f32_16x16x32_bf16 v[116:119], v[44:47], v[196:199], v[116:119]\nv_mfma_f32_16x16x32_bf16 v[120:123], v[44:47], a[64:67], v[120:123]\nv_mfma_f32_16x16x32_bf16 v[124:127], v[44:47], a[68:71], v[124:127]\nds_read_b128 v[44:47], v33 offset:20480\n"
"s_waitcnt lgkmcnt(3)\nv_mfma_f32_16x16x32_bf16 v[96:99], v[48:51], v[200:203], v[96:99]\nv_mfma_f32_16x16x32_bf16 v[100:103], v[48:51], v[204:207], v[100:103]\nv_mfma_f32_16x16x32_bf16 v[104:107], v[48:51], a[72:75], v[104:107]\n"
"v_mfma_f32_16x16x32_bf16 v[108:111], v[48:51], a[76:79], v[108:111]\nds_read_b128 v[48:51], v34 offset:20480\ns_waitcnt lgkmcnt(3)\nv_mfma_f32_16x16x32_bf16 v[112:115], v[52:55], v[200:203], v[112:115]\n"
"s_add_u32 m0, s52, 16384\nv_mfma_f32_16x16x32_bf16 v[116:119], v[52:55], v[204:207], v[116:119]\ns_lshl_b32 s53, s44, 7\ns_add_u32 s53, s53, s49\n"
"v_mfma_f32_16x16x32_bf16 v[120:123], v[52:55], a[72:75], v[120:123]\nbuffer_load_dwordx4 v10, s[12:15], s53 offen lds\nv_mfma_f32_16x16x32_bf16 v[124:127], v[52:55], a[76:79], v[124:127]\nds_read_b128 v[52:55], v35 offset:20480\n"
"s_waitcnt lgkmcnt(3)\nv_mfma_f32_16x16x32_bf16 v[96:99], v[40:43], v[208:211], v[96:99]\nv_mfma_f32_16x16x32_bf16 v[100:103], v[40:43], v[212:215], v[100:103]\nv_mfma_f32_16x16x32_bf16 v[104:107], v[40:43], a[80:83], v[104:107]\n"
"v_mfma_f32_16x16x32_bf16 v[108:111], v[40:43], a[84:87], v[108:111]\nds_read_b128 v[40:43], v32 offset:24576\ns_waitcnt lgkmcnt(3)\nv_mfma_f32_16x16x32_bf16 v[112:115], v[44:47], v[208:211], v[112:115]\n"
"v_mfma_f32_16x16x32_bf16 v[116:119], v[44:47], v[212:215], v[116:119]\nv_mfma_f32_16x16x32_bf16 v[120:123], v[44:47], a[80:83], v[120:123]\nv_mfma_f32_16x16x32_bf16 v[124:127], v[44:47], a[84:87], v[124:127]\nds_read_b128 v[44:47], v33 offset:24576\n"
"s_add_u32 m0, s52, 20480\ns_waitcnt lgkmcnt(3)\nv_mfma_f32_16x16x32_bf16 v[96:99], v[48:51], v[216:219], v[96:99]\ns_add_u32 s54, s51, 32768\n"
"s_cmp_eq_u32 s54, 163840\nv_mfma_f32_16x16x32_bf16 v[100:103], v[48:51], v[220:223], v[100:103]\nv_mfma_f32_16x16x32_bf16 v[104:107], v[48:51], a[88:91], v[104:107]\nv_mfma_f32_16x16x32_bf16 v[108:111], v[48:51], a[92:95], v[108:111]\n"
"s_cselect_b32 s54, 0, s54\ns_sub_u32 s54, s54, s51\nds_read_b128 v[48:51], v34 offset:24576\ns_lshl_b32 s53, s45, 7\n"
"s_add_u32 s53, s53, s49\ns_waitcnt lgkmcnt(3)\nv_mfma_f32_16x16x32_bf16 v[112:115], v[52:55], v[216:219], v[112:115]\nbuffer_load_dwordx4 v10, s[12:15], s53 offen lds\n"
"v_mfma_f32_16x16x32_bf16 v[116:119], v[52:55], v[220:223], v[116:119]\nv_mfma_f32_16x16x32_bf16 v[120:123], v[52:55], a[88:91], v[120:123]\nv_mfma_f32_16x16x32_bf16 v[124:127], v[52:55], a[92:95], v[124:127]\nds_read_b128 v[52:55], v35 offset:24576\n"
"s_waitcnt lgkmcnt(3)\nv_mfma_f32_16x16x32_bf16 v[96:99], v[40:43], v[224:227], v[96:99]\nv_add_u32 v32, s54, v32\nv_mfma_f32_16x16x32_bf16 v[100:103], v[40:43], v[228:231], v[100:103]\n"
"v_mfma_f32_16x16x32_bf16 v[104:107], v[40:43], a[96:99], v[104:107]\nv_mfma_f32_16x16x32_bf16 v[108:111], v[40:43], a[100:103], v[108:111]\nds_read_b128 v[40:43], v32 offset:0\ns_waitcnt lgkmcnt(3)\n"
"v_mfma_f32_16x16x32_bf16 v[112:115], v[44:47], v[224:227], v[112:115]\nv_add_u32 v33, s54, v33\nv_mfma_f32_16x16x32_bf16 v[116:119], v[44:47], v[228:231], v[116:119]\ns_add_u32 m0, s52, 24576\n"
"v_mfma_f32_16x16x32_bf16 v[120:123], v[44:47], a[96:99], v[120:123]\ns_lshl_b32 s53, s46, 7\ns_add_u32 s53, s53, s49\nv_mfma_f32_16x16x32_bf16 v[124:127], v[44:47], a[100:103], v[124:127]\n"
"ds_read_b128 v[44:47], v33 offset:0\nbuffer_load_dwordx4 v10, s[12:15], s53 offen lds\ns_waitcnt lgkmcnt(3)\nv_mfma_f32_16x16x32_bf16 v[96:99], v[48:51], v[232:235], v[96:99]\n"
"v_add_u32 v34, s54, v34\nv_mfma_f32_16x16x32_bf16 v[100:103], v[48:51], v[236:239], v[100:103]\nv_mfma_f32_16x16x32_bf16 v[104:107], v[48:51], a[104:107], v[104:107]\nv_mfma_f32_16x16x32_bf16 v[108:111], v[48:51], a[108:111], v[108:111]\n"
"ds_read_b128 v[48:51], v34 offset:0\ns_waitcnt lgkmcnt(3)\nv_mfma_f32_16x16x32_bf16 v[112:115], v[52:55], v[232:235], v[112:115]\nv_add_u32 v35, s54, v35\n"
"v_mfma_f32_16x16x32_bf16 v[116:119], v[52:55], v[236:239], v[116:119]\nv_mfma_f32_16x16x32_bf16 v[120:123], v[52:55], a[104:107], v[120:123]\nv_mfma_f32_16x16x32_bf16 v[124:127], v[52:55], a[108:111], v[124:127]\nds_read_b128 v[52:55], v35 offset:0\n"
"s_add_u32 s52, s51, s32\ns_add_u32 s51, s51, s54\ns_add_u32 s49, s49, 28672\n.p2align 6\n"
"fg1_loop:\ns_waitcnt vmcnt(7)\ns_barrier\ns_waitcnt lgkmcnt(3)\n"
"v_mfma_f32_16x16x32_bf16 v[64:67], v[40:43], v[128:131], 0\nv_mov_b32 v36, s48\ns_add_u32 s48, s48, 1\ns_nop 0\n"
"buffer_store_dword v36, v19, s[24:27], s33 offen sc1\nv_mfma_f32_16x16x32_bf16 v[68:71], v[40:43], v[132:135], 0\nv_mfma_f32_16x16x32_bf16 v[72:75], v[40:43], a[0:3], 0\nv_mfma_f32_16x16x32_bf16 v[76:79], v[40:43], a[4:7], 0\n"
"ds_read_b128 v[40:43], v32 offset:4096\nv_cvt_pk_bf16_f32 v56, v96, v97\ns_waitcnt lgkmcnt(3)\nv_mfma_f32_16x16x32_bf16 v[80:83], v[44:47], v[128:131], 0\n"
"v_cvt_pk_bf16_f32 v57, v98, v99\nv_mfma_f32_16x16x32_bf16 v[84:87], v[44:47], v[132:135], 0\nv_cvt_pk_bf16_f32 v58, v112, v113\nv_mfma_f32_16x16x32_bf16 v[88:91], v[44:47], a[0:3], 0\n"
"v_cvt_pk_bf16_f32 v59, v114, v115\nv_mfma_f32_16x16x32_bf16 v[92:95], v[44:47], a[4:7], 0\nds_read_b128 v[44:47], v33 offset:4096\ns_waitcnt lgkmcnt(3)\n"
"v_mfma_f32_16x16x32_bf16 v[64:67], v[48:51], v[136:139], v[64:67]\nv_mfma_f32_16x16x32_bf16 v[68:71], v[48:51], v[140:143], v[68:71]\nbuffer_store_dwordx4 v[56:59], v15, s[20:23], s50 offen sc1\nv_mfma_f32_16x16x32_bf16 v[72:75], v[48:51], a[8:11], v[72:75]\n"
"v_cvt_pk_bf16_f32 v60, v100, v101\nv_mfma_f32_16x16x32_bf16 v[76:79], v[48:51], a[12:15], v[76:79]\nds_read_b128 v[48:51], v34 offset:4096\nv_cvt_pk_bf16_f32 v61, v102, v103\n"
"s_waitcnt lgkmcnt(3)\nv_mfma_f32_16x16x32_bf16 v[80:83], v[52:55], v[136:139], v[80:83]\nv_cvt_pk_bf16_f32 v62, v116, v117\nv_mfma_f32_16x16x32_bf16 v[84:87], v[52:55], v[140:143], v[84:87]\n"
"v_cvt_pk_bf16_f32 v63, v118, v119\nv_mfma_f32_16x16x32_bf16 v[88:91], v[52:55], a[8:11], v[88:91]\nv_mfma_f32_16x16x32_bf16 v[92:95], v[52:55], a[12:15], v[92:95]\nds_read_b128 v[52:55], v35 offset:4096\n"
"s_waitcnt lgkmcnt(3)\nv_mfma_f32_16x16x32_bf16 v[64:67], v[40:43], v[144:147], v[64:67]\nbuffer_store_dwordx4 v[60:63], v16, s[20:23], s50 offen sc1\nv_mfma_f32_16x16x32_bf16 v[68:71], v[40:43], v[148:151], v[68:71]\n"
"v_cvt_pk_bf16_f32 v56, v104, v105\nv_mfma_f32_16x16x32_bf16 v[72:75], v[40:43], a[16:19], v[72:75]\nv_cvt_pk_bf16_f32 v57, v106, v107\nv_mfma_f32_16x16x32_bf16 v[76:79], v[40:43], a[20:23], v[76:79]\n"
"ds_read_b128 v[40:43], v32 offset:8192\nv_cvt_pk_bf16_f32 v58, v120, v121\ns_waitcnt lgkmcnt(3)\nv_mfma_f32_16x16x32_bf16 v[80:83], v[44:47], v[144:147], v[80:83]\n"
"v_cvt_pk_bf16_f32 v59, v122, v123\nv_mfma_f32_16x16x32_bf16 v[84:87], v[44:47], v[148:151], v[84:87]\nv_mfma_f32_16x16x32_bf16 v[88:91], v[44:47], a[16:19], v[88:91]\nv_mfma_f32_16x16x32_bf16 v[92:95], v[44:47], a[20:23], v[92:95]\n"
"ds_read_b128 v[44:47], v33 offset:8192\nbuffer_store_dwordx4 v[56:59], v17, s[20:23], s50 offen sc1\ns_waitcnt lgkmcnt(3)\nv_mfma_f32_16x16x32_bf16 v[64:67], v[48:51], v[152:155], v[64:67]\n"
"v_cvt_pk_bf16_f32 v60, v108, v109\nv_mfma_f32_16x16x32_bf16 v[68:71], v[48:51], v[156:159], v[68:71]\nv_cvt_pk_bf16_f32 v61, v110, v111\nv_mfma_f32_16x16x32_bf16 v[72:75], v[48:51], a[24:27], v[72:75]\n"
"v_cvt_pk_bf16_f32 v62, v124, v125\nv_mfma_f32_16x16x32_bf16 v[76:79], v[48:51], a[28:31], v[76:79]\nds_read_b128 v[48:51], v34 offset:8192\nv_cvt_pk_bf16_f32 v63, v126, v127\n"
"s_waitcnt lgkmcnt(3)\nv_mfma_f32_16x16x32_bf16 v[80:83], v[52:55], v[152:155], v[80:83]\nv_mfma_f32_16x16x32_bf16 v[84:87], v[52:55], v[156:159], v[84:87]\nv_mfma_f32_16x16x32_bf16 v[88:91], v[52:55], a[24:27], v[88:91]\n"
"buffer_store_dwordx4 v[60:63], v18, s[20:23], s50 offen sc1\nv_mfma_f32_16x16x32_bf16 v[92:95], v[52:55], a[28:31], v[92:95]\nds_read_b128 v[52:55], v35 offset:8192\ns_add_u32 s50, s50, s36\n"
"s_waitcnt lgkmcnt(3)\nv_mfma_f32_16x16x32_bf16 v[64:67], v[40:43], v[160:163], v[64:67]\ns_add_u32 m0, s52, 0\nv_mfma_f32_16x16x32_bf16 v[68:71], v[40:43], v[164:167], v[68:71]\n"
"s_lshl_b32 s53, s40, 7\ns_add_u32 s53, s53, s49\nv_mfma_f32_16x16x32_bf16 v[72:75], v[40:43], a[32:35], v[72:75]\nbuffer_load_dwordx4 v10, s[12:15], s53 offen lds\n"
"v_mfma_f32_16x16x32_bf16 v[76:79], v[40:43], a[36:39], v[76:79]\nds_read_b128 v[40:43], v32 offset:12288\ns_waitcnt lgkmcnt(3)\nv_mfma_f32_16x16x32_bf16 v[80:83], v[44:47], v[160:163], v[80:83]\n"
"v_mfma_f32_16x16x32_bf16 v[84:87], v[44:47], v[164:167], v[84:87]\nv_mfma_f32_16x16x32_bf16 v[88:91], v[44:47], a[32:35], v[88:91]\nv_mfma_f32_16x16x32_bf16 v[92:95], v[44:47], a[36:39], v[92:95]\nds_read_b128 v[44:47], v33 offset:12288\n"
"s_waitcnt lgkmcnt(3)\nv_mfma_f32_16x16x32_bf16 v[64:67], v[48:51], v[168:171], v[64:67]\nv_mfma_f32_16x16x32_bf16 v[68:71], v[48:51], v[172:175], v[68:71]\nv_mfma_f32_16x16x32_bf16 v[72:75], v[48:51], a[40:43], v[72:75]\n"
"v_mfma_f32_16x16x32_bf16 v[76:79], v[48:51], a[44:47], v[76:79]\nds_read_b128 v[48:51], v34 offset:12288\ns_add_u32 m0, s52, 4096\ns_waitcnt lgkmcnt(3)\n"
"v_mfma_f32_16x16x32_bf16 v[80:83], v[52:55], v[168:171], v[80:83]\ns_lshl_b32 s53, s41, 7\ns_add_u32 s53, s53, s49\nv_mfma_f32_16x16x32_bf16 v[84:87], v[52:55], v[172:175], v[84:87]\n"
"buffer_load_dwordx4 v10, s[12:15], s53 offen lds\nv_mfma_f32_16x16x32_bf16 v[88:91], v[52:55], a[40:43], v[88:91]\nv_mfma_f32_16x16x32_bf16 v[92:95], v[52:55], a[44:47], v[92:95]\nds_read_b128 v[52:55], v35 offset:12288\n"
"s_waitcnt lgkmcnt(3)\nv_mfma_f32_16x16x32_bf16 v[64:67], v[40:43], v[176:179], v[64:67]\nv_mfma_f32_16x16x32_bf16 v[68:71], v[40:43], v[180:183], v[68:71]\nv_mfma_f32_16x16x32_bf16 v[72:75], v[40:43], a[48:51], v[72:75]\n"
"v_mfma_f32_16x16x32_bf16 v[76:79], v[40:43], a[52:55], v[76:79]\nds_read_b128 v[40:43], v32 offset:16384\ns_waitcnt lgkmcnt(3)\nv_mfma_f32_16x16x32_bf16 v[80:83], v[44:47], v[176:179], v[80:83]\n"
"v_mfma_f32_16x16x32_bf16 v[84:87], v[44:47], v[180:183], v[84:87]\nv_mfma_f32_16x16x32_bf16 v[88:91], v[44:47], a[48:51], v[88:91]\ns_add_u32 m0, s52, 8192\nv_mfma_f32_16x16x32_bf16 v[92:95], v[44:47], a[52:55], v[92:95]\n"
"ds_read_b128 v[44:47], v33 offset:16384\ns_lshl_b32 s53, s42, 7\ns_add_u32 s53, s53, s49\ns_waitcnt lgkmcnt(3)\n"
"v_mfma_f32_16x16x32_bf16 v[64:67], v[48:51], v[184:187], v[64:67]\nbuffer_load_dwordx4 v10, s[12:15], s53 offen lds\nv_mfma_f32_16x16x32_bf16 v[68:71], v[48:51], v[188:191], v[68:71]\nv_mfma_f32_16x16x32_bf16 v[72:75], v[48:51], a[56:59], v[72:75]\n"
"v_mfma_f32_16x16x32_bf16 v[76:79], v[48:51], a[60:63], v[76:79]\nds_read_b128 v[48:51], v34 offset:16384\ns_waitcnt lgkmcnt(3)\nv_mfma_f32_16x16x32_bf16 v[80:83], v[52:55], v[184:187], v[80:83]\n"
"v_mfma_f32_16x16x32_bf16 v[84:87], v[52:55], v[188:191], v[84:87]\nv_mfma_f32_16x16x32_bf16 v[88:91], v[52:55], a[56:59], v[88:91]\nv_mfma_f32_16x16x32_bf16 v[92:95], v[52:55], a[60:63], v[92:95]\nds_read_b128 v[52:55], v35 offset:16384\n"
"s_waitcnt lgkmcnt(3)\nv_mfma_f32_16x16x32_bf16 v[64:67], v[40:43], v[192:195], v[64:67]\nv_mfma_f32_16x16x32_bf16 v[68:71], v[40:43], v[196:199], v[68:71]\ns_add_u32 m0, s52, 12288\n"
"v_mfma_f32_16x16x32_bf16 v[72:75], v[40:43], a[64:67], v[72:75]\ns_lshl_b32 s53, s43, 7\ns_add_u32 s53, s53, s49\nv_mfma_f32_16x16x32_bf16 v[76:79], v[40:43], a[68:71], v[76:79]\n"
"ds_read_b128 v[40:43], v32 offset:20480\nbuffer_load_dwordx4 v10, s[12:15], s53 offen lds\ns_waitcnt lgkmcnt(3)\nv_mfma_f32_16x16x32_bf16 v[80:83], v[44:47], v[192:195], v[80:83]\n"
"v_mfma_f32_16x16x32_bf16 v[84:87], v[44:47], v[196:199], v[84:87]\nv_mfma_f32_16x16x32_bf16 v[88:91], v[44:47], a[64:67], v[88:91]\nv_mfma_f32_16x16x32_bf16 v[92:95], v[44:47], a[68:71], v[92:95]\nds_read_b128 v[44:47], v33 offset:20480\n"
"s_waitcnt lgkmcnt(3)\nv_mfma_f32_16x16x32_bf16 v[64:67], v[48:51], v[200:203], v[64:67]\nv_mfma_f32_16x16x32_bf16 v[68:71], v[48:51], v[204:207], v[68:71]\nv_mfma_f32_16x16x32_bf16 v[72:75], v[48:51], a[72:75], v[72:75]\n"
"v_mfma_f32_16x16x32_bf16 v[76:79], v[48:51], a[76:79], v[76:79]\nds_read_b128 v[48:51], v34 offset:20480\ns_waitcnt lgkmcnt(3)\nv_mfma_f32_16x16x32_bf16 v[80:83], v[52:55], v[200:203], v[80:83]\n"
"s_add_u32 m0, s52, 16384\nv_mfma_f32_16x16x32_bf16 v[84:87], v[52:55], v[204:207], v[84:87]\ns_lshl_b32 s53, s44, 7\ns_add_u32 s53, s53, s49\n"
"v_mfma_f32_16x16x32_bf16 v[88:91], v[52:55], a[72:75], v[88:91]\nbuffer_load_dwordx4 v10, s[12:15], s53 offen lds\nv_mfma_f32_16x16x32_bf16 v[92:95], v[52:55], a[76:79], v[92:95]\nds_read_b128 v[52:55], v35 offset:20480\n"
"s_waitcnt lgkmcnt(3)\nv_mfma_f32_16x16x32_bf16 v[64:67], v[40:43], v[208:211], v[64:67]\nv_mfma_f32_16x16x32_bf16 v[68:71], v[40:43], v[212:215], v[68:71]\nv_mfma_f32_16x16x32_bf16 v[72:75], v[40:43], a[80:83], v[72:75]\n"
"v_mfma_f32_16x16x32_bf16 v[76:79], v[40:43], a[84:87], v[76:79]\nds_read_b128 v[40:43], v32 offset:24576\ns_waitcnt lgkmcnt(3)\nv_mfma_f32_16x16x32_bf16 v[80:83], v[44:47], v[208:211], v[80:83]\n"
"v_mfma_f32_16x16x32_bf16 v[84:87], v[44:47], v[212:215], v[84:87]\nv_mfma_f32_16x16x32_bf16 v[88:91], v[44:47], a[80:83], v[88:91]\nv_mfma_f32_16x16x32_bf16 v[92:95], v[44:47], a[84:87], v[92:95]\nds_read_b128 v[44:47], v33 offset:24576\n"
"s_add_u32 m0, s52, 20480\ns_waitcnt lgkmcnt(3)\nv_mfma_f32_16x16x32_bf16 v[64:67], v[48:51], v[216:219], v[64:67]\ns_add_u32 s54, s51, 32768\n"
"s_cmp_eq_u32 s54, 163840\nv_mfma_f32_16x16x32_bf16 v[68:71], v[48:51], v[220:223], v[68:71]\nv_mfma_f32_16x16x32_bf16 v[72:75], v[48:51], a[88:91], v[72:75]\nv_mfma_f32_16x16x32_bf16 v[76:79], v[48:51], a[92:95], v[76:79]\n"
"s_cselect_b32 s54, 0, s54\ns_sub_u32 s54, s54, s51\nds_read_b128 v[48:51], v34 offset:24576\ns_lshl_b32 s53, s45, 7\n"
"s_add_u32 s53, s53, s49\ns_waitcnt lgkmcnt(3)\nv_mfma_f32_16x16x32_bf16 v[80:83], v[52:55], v[216:219], v[80:83]\nbuffer_load_dwordx4 v10, s[12:15], s53 offen lds\n"
"v_mfma_f32_16x16x32_bf16 v[84:87], v[52:55], v[220:223], v[84:87]\nv_mfma_f32_16x16x32_bf16 v[88:91], v[52:55], a[88:91], v[88:91]\nv_mfma_f32_16x16x32_bf16 v[92:95], v[52:55], a[92:95], v[92:95]\nds_read_b128 v[52:55], v35 offset:24576\n"
"s_waitcnt lgkmcnt(3)\nv_mfma_f32_16x16x32_bf16 v[64:67], v[40:43], v[224:227], v[64:67]\nv_add_u32 v32, s54, v32\nv_mfma_f32_16x16x32_bf16 v[68:71], v[40:43], v[228:231], v[68:71]\n"
"v_mfma_f32_16x16x32_bf16 v[72:75], v[40:43], a[96:99], v[72:75]\nv_mfma_f32_16x16x32_bf16 v[76:79], v[40:43], a[100:103], v[76:79]\nds_read_b128 v[40:43], v32 offset:0\ns_waitcnt lgkmcnt(3)\n"
"v_mfma_f32_16x16x32_bf16 v[80:83], v[44:47], v[224:227], v[80:83]\nv_add_u32 v33, s54, v33\nv_mfma_f32_16x16x32_bf16 v[84:87], v[44:47], v[228:231], v[84:87]\ns_add_u32 m0, s52, 24576\n"
"v_mfma_f32_16x16x32_bf16 v[88:91], v[44:47], a[96:99], v[88:91]\ns_lshl_b32 s53, s46, 7\ns_add_u32 s53, s53, s49\nv_mfma_f32_16x16x32_bf16 v[92:95], v[44:47], a[100:103], v[92:95]\n"
"ds_read_b128 v[44:47], v33 offset:0\nbuffer_load_dwordx4 v10, s[12:15], s53 offen lds\ns_waitcnt lgkmcnt(3)\nv_mfma_f32_16x16x32_bf16 v[64:67], v[48:51], v[232:235], v[64:67]\n"
"v_add_u32 v34, s54, v34\nv_mfma_f32_16x16x32_bf16 v[68:71], v[48:51], v[236:239], v[68:71]\nv_mfma_f32_16x16x32_bf16 v[72:75], v[48:51], a[104:107], v[72:75]\nv_mfma_f32_16x16x32_bf16 v[76:79], v[48:51], a[108:111], v[76:79]\n"
"ds_read_b128 v[48:51], v34 offset:0\ns_waitcnt lgkmcnt(3)\nv_mfma_f32_16x16x32_bf16 v[80:83], v[52:55], v[232:235], v[80:83]\nv_add_u32 v35, s54, v35\n"
"v_mfma_f32_16x16x32_bf16 v[84:87], v[52:55], v[236:239], v[84:87]\nv_mfma_f32_16x16x32_bf16 v[88:91], v[52:55], a[104:107], v[88:91]\nv_mfma_f32_16x16x32_bf16 v[92:95], v[52:55], a[108:111], v[92:95]\nds_read_b128 v[52:55], v35 offset:0\n"
"s_add_u32 s52, s51, s32\ns_add_u32 s51, s51, s54\ns_add_u32 s49, s49, 28672\ns_waitcnt vmcnt(7)\n"
"s_barrier\ns_waitcnt lgkmcnt(3)\nv_mfma_f32_16x16x32_bf16 v[96:99], v[40:43], v[128:131], 0\nv_mov_b32 v36, s48\n"
"s_add_u32 s48, s48, 1\ns_nop 0\nbuffer_store_dword v36, v19, s[24:27], s33 offen sc1\nv_mfma_f32_16x16x32_bf16 v[100:103], v[40:43], v[132:135], 0\n"
"v_mfma_f32_16x16x32_bf16 v[104:107], v[40:43], a[0:3], 0\nv_mfma_f32_16x16x32_bf16 v[108:111], v[40:43], a[4:7], 0\nds_read_b128 v[40:43], v32 offset:4096\nv_cvt_pk_bf16_f32 v56, v64, v65\n"
"s_waitcnt lgkmcnt(3)\nv_mfma_f32_16x16x32_bf16 v[112:115], v[44:47], v[128:131], 0\nv_cvt_pk_bf16_f32 v57, v66, v67\nv_mfma_f32_16x16x32_bf16 v[116:119], v[44:47], v[132:135], 0\n"
"v_cvt_pk_bf16_f32 v58, v80, v81\nv_mfma_f32_16x16x32_bf16 v[120:123], v[44:47], a[0:3], 0\nv_cvt_pk_bf16_f32 v59, v82, v83\nv_mfma_f32_16x16x32_bf16 v[124:127], v[44:47], a[4:7], 0\n"
"ds_read_b128 v[44:47], v33 offset:4096\ns_waitcnt lgkmcnt(3)\nv_mfma_f32_16x16x32_bf16 v[96:99], v[48:51], v[136:139], v[96:99]\nv_mfma_f32_16x16x32_bf16 v[100:103], v[48:51], v[140:143], v[100:103]\n"
"buffer_store_dwordx4 v[56:59], v15, s[20:23], s50 offen sc1\nv_mfma_f32_16x16x32_bf16 v[104:107], v[48:51], a[8:11], v[104:107]\nv_cvt_pk_bf16_f32 v60, v68, v69\nv_mfma_f32_16x16x32_bf16 v[108:111], v[48:51], a[12:15], v[108:111]\n"
"ds_read_b128 v[48:51], v34 offset:4096\nv_cvt_pk_bf16_f32 v61, v70, v71\ns_waitcnt lgkmcnt(3)\nv_mfma_f32_16x16x32_bf16 v[112:115], v[52:55], v[136:139], v[112:115]\n"
"v_cvt_pk_bf16_f32 v62, v84, v85\nv_mfma_f32_16x16x32_bf16 v[116:119], v[52:55], v[140:143], v[116:119]\nv_cvt_pk_bf16_f32 v63, v86, v87\nv_mfma_f32_16x16x32_bf16 v[120:123], v[52:55], a[8:11], v[120:123]\n"
"v_mfma_f32_16x16x32_bf16 v[124:127], v[52:55], a[12:15], v[124:127]\nds_read_b128 v[52:55], v35 offset:4096\ns_waitcnt lgkmcnt(3)\nv_mfma_f32_16x16x32_bf16 v[96:99], v[40:43], v[144:147], v[96:99]\n"
"buffer_store_dwordx4 v[60:63], v16, s[20:23], s50 offen sc1\nv_mfma_f32_16x16x32_bf16 v[100:103], v[40:43], v[148:151], v[100:103]\nv_cvt_pk_bf16_f32 v56, v72, v73\nv_mfma_f32_16x16x32_bf16 v[104:107], v[40:43], a[16:19], v[104:107]\n"
"v_cvt_pk_bf16_f32 v57, v74, v75\nv_mfma_f32_16x16x32_bf16 v[108:111], v[40:43], a[20:23], v[108:111]\nds_read_b128 v[40:43], v32 offset:8192\nv_cvt_pk_bf16_f32 v58, v88, v89\n"
"s_waitcnt lgkmcnt(3)\nv_mfma_f32_16x16x32_bf16 v[112:115], v[44:47], v[144:147], v[112:115]\nv_cvt_pk_bf16_f32 v59, v90, v91\nv_mfma_f32_16x16x32_bf16 v[116:119], v[44:47], v[148:151], v[116:119]\n"
"v_mfma_f32_16x16x32_bf16 v[120:123], v[44:47], a[16:19], v[120:123]\nv_mfma_f32_16x16x32_bf16 v[124:127], v[44:47], a[20:23], v[124:127]\nds_read_b128 v[44:47], v33 offset:8192\nbuffer_store_dwordx4 v[56:59], v17, s[20:23], s50 offen sc1\n"
"s_waitcnt lgkmcnt(3)\nv_mfma_f32_16x16x32_bf16 v[96:99], v[48:51], v[152:155], v[96:99]\nv_cvt_pk_bf16_f32 v60, v76, v77\nv_mfma_f32_16x16x32_bf16 v[100:103], v[48:51], v[156:159], v[100:103]\n"
"v_cvt_pk_bf16_f32 v61, v78, v79\nv_mfma_f32_16x16x32_bf16 v[104:107], v[48:51], a[24:27], v[104:107]\nv_cvt_pk_bf16_f32 v62, v92, v93\nv_mfma_f32_16x16x32_bf16 v[108:111], v[48:51], a[28:31], v[108:111]\n"
"ds_read_b128 v[48:51], v34 offset:8192\nv_cvt_pk_bf16_f32 v63, v94, v95\ns_waitcnt lgkmcnt(3)\nv_mfma_f32_16x16x32_bf16 v[112:115], v[52:55], v[152:155], v[112:115]\n"
"v_mfma_f32_16x16x32_bf16 v[116:119], v[52:55], v[156:159], v[116:119]\nv_mfma_f32_16x16x32_bf16 v[120:123], v[52:55], a[24:27], v[120:123]\nbuffer_store_dwordx4 v[60:63], v18, s[20:23], s50 offen sc1\nv_mfma_f32_16x16x32_bf16 v[124:127], v[52:55], a[28:31], v[124:127]\n"
"ds_read_b128 v[52:55], v35 offset:8192\ns_add_u32 s50, s50, s36\ns_waitcnt lgkmcnt(3)\nv_mfma_f32_16x16x32_bf16 v[96:99], v[40:43], v[160:163], v[96:99]\n"
"s_add_u32 m0, s52, 0\nv_mfma_f32_16x16x32_bf16 v[100:103], v[40:43], v[164:167], v[100:103]\ns_lshl_b32 s53, s40, 7\ns_add_u32 s53, s53, s49\n"
"v_mfma_f32_16x16x32_bf16 v[104:107], v[40:43], a[32:35], v[104:107]\nbuffer_load_dwordx4 v10, s[12:15], s53 offen lds\nv_mfma_f32_16x16x32_bf16 v[108:111], v[40:43], a[36:39], v[108:111]\nds_read_b128 v[40:43], v32 offset:12288\n"
"s_waitcnt lgkmcnt(3)\nv_mfma_f32_16x16x32_bf16 v[112:115], v[44:47], v[160:163], v[112:115]\nv_mfma_f32_16x16x32_bf16 v[116:119], v[44:47], v[164:167], v[116:119]\nv_mfma_f32_16x16x32_bf16 v[120:123], v[44:47], a[32:35], v[120:123]\n"
"v_mfma_f32_16x16x32_bf16 v[124:127], v[44:47], a[36:39], v[124:127]\nds_read_b128 v[44:47], v33 offset:12288\ns_waitcnt lgkmcnt(3)\nv_mfma_f32_16x16x32_bf16 v[96:99], v[48:51], v[168:171], v[96:99]\n"
"v_mfma_f32_16x16x32_bf16 v[100:103], v[48:51], v[172:175], v[100:103]\nv_mfma_f32_16x16x32_bf16 v[104:107], v[48:51], a[40:43], v[104:107]\nv_mfma_f32_16x16x32_bf16 v[108:111], v[48:51], a[44:47], v[108:111]\nds_read_b128 v[48:51], v34 offset:12288\n"
"s_add_u32 m0, s52, 4096\ns_waitcnt lgkmcnt(3)\nv_mfma_f32_16x16x32_bf16 v[112:115], v[52:55], v[168:171], v[112:115]\ns_lshl_b32 s53, s41, 7\n"
"s_add_u32 s53, s53, s49\nv_mfma_f32_16x16x32_bf16 v[116:119], v[52:55], v[172:175], v[116:119]\nbuffer_load_dwordx4 v10, s[12:15], s53 offen lds\nv_mfma_f32_16x16x32_bf16 v[120:123], v[52:55], a[40:43], v[120:123]\n"
"v_mfma_f32_16x16x32_bf16 v[124:127], v[52:55], a[44:47], v[124:127]\nds_read_b128 v[52:55], v35 offset:12288\ns_waitcnt lgkmcnt(3)\nv_mfma_f32_16x16x32_bf16 v[96:99], v[40:43], v[176:179], v[96:99]\n"
"v_mfma_f32_16x16x32_bf16 v[100:103], v[40:43], v[180:183], v[100:103]\nv_mfma_f32_16x16x32_bf16 v[104:107], v[40:43], a[48:51], v[104:107]\nv_mfma_f32_16x16x32_bf16 v[108:111], v[40:43], a[52:55], v[108:111]\nds_read_b128 v[40:43], v32 offset:16384\n"
"s_waitcnt lgkmcnt(3)\nv_mfma_f32_16x16x32_bf16 v[112:115], v[44:47], v[176:179], v[112:115]\nv_mfma_f32_16x16x32_bf16 v[116:119], v[44:47], v[180:183], v[116:119]\nv_mfma_f32_16x16x32_bf16 v[120:123], v[44:47], a[48:51], v[120:123]\n"
"s_add_u32 m0, s52, 8192\nv_mfma_f32_16x16x32_bf16 v[124:127], v[44:47], a[52:55], v[124:127]\nds_read_b128 v[44:47], v33 offset:16384\ns_lshl_b32 s53, s42, 7\n"
"s_add_u32 s53, s53, s49\ns_waitcnt lgkmcnt(3)\nv_mfma_f32_16x16x32_bf16 v[96:99], v[48:51], v[184:187], v[96:99]\nbuffer_load_dwordx4 v10, s[12:15], s53 offen lds\n"
"v_mfma_f32_16x16x32_bf16 v[100:103], v[48:51], v[188:191], v[100:103]\nv_mfma_f32_16x16x32_bf16 v[104:107], v[48:51], a[56:59], v[104:107]\nv_mfma_f32_16x16x32_bf16 v[108:111], v[48:51], a[60:63], v[108:111]\nds_read_b128 v[48:51], v34 offset:16384\n"
"s_waitcnt lgkmcnt(3)\nv_mfma_f32_16x16x32_bf16 v[112:115], v[52:55], v[184:187], v[112:115]\nv_mfma_f32_16x16x32_bf16 v[116:119], v[52:55], v[188:191], v[116:119]\nv_mfma_f32_16x16x32_bf16 v[120:123], v[52:55], a[56:59], v[120:123]\n"
"v_mfma_f32_16x16x32_bf16 v[124:127], v[52:55], a[60:63], v[124:127]\nds_read_b128 v[52:55], v35 offset:16384\ns_waitcnt lgkmcnt(3)\nv_mfma_f32_16x16x32_bf16 v[96:99], v[40:43], v[192:195], v[96:99]\n"
"v_mfma_f32_16x16x32_bf16 v[100:103], v[40:43], v[196:199], v[100:103]\ns_add_u32 m0, s52, 12288\nv_mfma_f32_16x16x32_bf16 v[104:107], v[40:43], a[64:67], v[104:107]\ns_lshl_b32 s53, s43, 7\n"
"s_add_u32 s53, s53, s49\nv_mfma_f32_16x16x32_bf16 v[108:111], v[40:43], a[68:71], v[108:111]\nds_read_b128 v[40:43], v32 offset:20480\nbuffer_load_dwordx4 v10, s[12:15], s53 offen lds\n"
"s_waitcnt lgkmcnt(3)\nv_mfma_f32_16x16x32_bf16 v[112:115], v[44:47], v[192:195], v[112:115]\nv_mfma_f32_16x16x32_bf16 v[116:119], v[44:47], v[196:199], v[116:119]\nv_mfma_f32_16x16x32_bf16 v[120:123], v[44:47], a[64:67], v[120:123]\n"
"v_mfma_f32_16x16x32_bf16 v[124:127], v[44:47], a[68:71], v[124:127]\nds_read_b128 v[44:47], v33 offset:20480\ns_waitcnt lgkmcnt(3)\nv_mfma_f32_16x16x32_bf16 v[96:99], v[48:51], v[200:203], v[96:99]\n"
"v_mfma_f32_16x16x32_bf16 v[100:103], v[48:51], v[204:207], v[100:103]\nv_mfma_f32_16x16x32_bf16 v[104:107], v[48:51], a[72:75], v[104:107]\nv_mfma_f32_16x16x32_bf16 v[108:111], v[48:51], a[76:79], v[108:111]\nds_read_b128 v[48:51], v34 offset:20480\n"
"s_waitcnt lgkmcnt(3)\nv_mfma_f32_16x16x32_bf16 v[112:115], v[52:55], v[200:203], v[112:115]\ns_add_u32 m0, s52, 16384\nv_mfma_f32_16x16x32_bf16 v[116:119], v[52:55], v[204:207], v[116:119]\n"
"s_lshl_b32 s53, s44, 7\ns_add_u32 s53, s53, s49\nv_mfma_f32_16x16x32_bf16 v[120:123], v[52:55], a[72:75], v[120:123]\nbuffer_load_dwordx4 v10, s[12:15], s53 offen lds\n"
"v_mfma_f32_16x16x32_bf16 v[124:127], v[52:55], a[76:79], v[124:127]\nds_read_b128 v[52:55], v35 offset:20480\ns_waitcnt lgkmcnt(3)\nv_mfma_f32_16x16x32_bf16 v[96:99], v[40:43], v[208:211], v[96:99]\n"
"v_mfma_f32_16x16x32_bf16 v[100:103], v[40:43], v[212:215], v[100:103]\nv_mfma_f32_16x16x32_bf16 v[104:107], v[40:43], a[80:83], v[104:107]\nv_mfma_f32_16x16x32_bf16 v[108:111], v[40:43], a[84:87], v[108:111]\nds_read_b128 v[40:43], v32 offset:24576\n"
"s_waitcnt lgkmcnt(3)\nv_mfma_f32_16x16x32_bf16 v[112:115], v[44:47], v[208:211], v[112:115]\nv_mfma_f32_16x16x32_bf16 v[116:119], v[44:47], v[212:215], v[116:119]\nv_mfma_f32_16x16x32_bf16 v[120:123], v[44:47], a[80:83], v[120:123]\n"
"v_mfma_f32_16x16x32_bf16 v[124:127], v[44:47], a[84:87], v[124:127]\nds_read_b128 v[44:47], v33 offset:24576\ns_add_u32 m0, s52, 20480\ns_waitcnt lgkmcnt(3)\n"
"v_mfma_f32_16x16x32_bf16 v[96:99], v[48:51], v[216:219], v[96:99]\ns_add_u32 s54, s51, 32768\ns_cmp_eq_u32 s54, 163840\nv_mfma_f32_16x16x32_bf16 v[100:103], v[48:51], v[220:223], v[100:103]\n"
"v_mfma_f32_16x16x32_bf16 v[104:107], v[48:51], a[88:91], v[104:107]\nv_mfma_f32_16x16x32_bf16 v[108:111], v[48:51], a[92:95], v[108:111]\ns_cselect_b32 s54, 0, s54\ns_sub_u32 s54, s54, s51\n"
"ds_read_b128 v[48:51], v34 offset:24576\ns_lshl_b32 s53, s45, 7\ns_add_u32 s53, s53, s49\ns_waitcnt lgkmcnt(3)\n"
"v_mfma_f32_16x16x32_bf16 v[112:115], v[52:55], v[216:219], v[112:115]\nbuffer_load_dwordx4 v10, s[12:15], s53 offen lds\nv_mfma_f32_16x16x32_bf16 v[116:119], v[52:55], v[220:223], v[116:119]\nv_mfma_f32_16x16x32_bf16 v[120:123], v[52:55], a[88:91], v[120:123]\n"
"v_mfma_f32_16x16x32_bf16 v[124:127], v[52:55], a[92:95], v[124:127]\nds_read_b128 v[52:55], v35 offset:24576\ns_waitcnt lgkmcnt(3)\nv_mfma_f32_16x16x32_bf16 v[96:99], v[40:43], v[224:227], v[96:99]\n"
"v_add_u32 v32, s54, v32\nv_mfma_f32_16x16x32_bf16 v[100:103], v[40:43], v[228:231], v[100:103]\nv_mfma_f32_16x16x32_bf16 v[104:107], v[40:43], a[96:99], v[104:107]\nv_mfma_f32_16x16x32_bf16 v[108:111], v[40:43], a[100:103], v[108:111]\n"
"ds_read_b128 v[40:43], v32 offset:0\ns_waitcnt lgkmcnt(3)\nv_mfma_f32_16x16x32_bf16 v[112:115], v[44:47], v[224:227], v[112:115]\nv_add_u32 v33, s54, v33\n"
"v_mfma_f32_16x16x32_bf16 v[116:119], v[44:47], v[228:231], v[116:119]\ns_add_u32 m0, s52, 24576\nv_mfma_f32_16x16x32_bf16 v[120:123], v[44:47], a[96:99], v[120:123]\ns_lshl_b32 s53, s46, 7\n"
"s_add_u32 s53, s53, s49\nv_mfma_f32_16x16x32_bf16 v[124:127], v[44:47], a[100:103], v[124:127]\nds_read_b128 v[44:47], v33 offset:0\nbuffer_load_dwordx4 v10, s[12:15], s53 offen lds\n"
"s_waitcnt lgkmcnt(3)\nv_mfma_f32_16x16x32_bf16 v[96:99], v[48:51], v[232:235], v[96:99]\nv_add_u32 v34, s54, v34\nv_mfma_f32_16x16x32_bf16 v[100:103], v[48:51], v[236:239], v[100:103]\n"
"v_mfma_f32_16x16x32_bf16 v[104:107], v[48:51], a[104:107], v[104:107]\nv_mfma_f32_16x16x32_bf16 v[108:111], v[48:51], a[108:111], v[108:111]\nds_read_b128 v[48:51], v34 offset:0\ns_waitcnt lgkmcnt(3)\n"
"v_mfma_f32_16x16x32_bf16 v[112:115], v[52:55], v[232:235], v[112:115]\nv_add_u32 v35, s54, v35\nv_mfma_f32_16x16x32_bf16 v[116:119], v[52:55], v[236:239], v[116:119]\nv_mfma_f32_16x16x32_bf16 v[120:123], v[52:55], a[104:107], v[120:123]\n"
"v_mfma_f32_16x16x32_bf16 v[124:127], v[52:55], a[108:111], v[124:127]\nds_read_b128 v[52:55], v35 offset:0\ns_add_u32 s52, s51, s32\ns_add_u32 s51, s51, s54\n"
"s_add_u32 s49, s49, 28672\ns_cmp_lt_u32 s49, 1949696\ns_cbranch_scc1 fg1_loop\ns_nop 7\n"
"v_cvt_pk_bf16_f32 v56, v96, v97\nv_cvt_pk_bf16_f32 v57, v98, v99\nv_cvt_pk_bf16_f32 v58, v112, v113\nv_cvt_pk_bf16_f32 v59, v114, v115\n"
"s_nop 0\ns_nop 0\nbuffer_store_dwordx4 v[56:59], v15, s[20:23], s50 offen sc1\nv_cvt_pk_bf16_f32 v60, v100, v101\n"
"v_cvt_pk_bf16_f32 v61, v102, v103\nv_cvt_pk_bf16_f32 v62, v116, v117\nv_cvt_pk_bf16_f32 v63, v118, v119\ns_nop 0\n"
"s_nop 0\nbuffer_store_dwordx4 v[60:63], v16, s[20:23], s50 offen sc1\nv_cvt_pk_bf16_f32 v56, v104, v105\nv_cvt_pk_bf16_f32 v57, v106, v107\n"
"v_cvt_pk_bf16_f32 v58, v120, v121\nv_cvt_pk_bf16_f32 v59, v122, v123\ns_nop 0\ns_nop 0\n"
"buffer_store_dwordx4 v[56:59], v17, s[20:23], s50 offen sc1\nv_cvt_pk_bf16_f32 v60, v108, v109\nv_cvt_pk_bf16_f32 v61, v110, v111\nv_cvt_pk_bf16_f32 v62, v124, v125\n"
"v_cvt_pk_bf16_f32 v63, v126, v127\ns_nop 0\ns_nop 0\nbuffer_store_dwordx4 v[60:63], v18, s[20:23], s50 offen sc1\n"
"s_add_u32 s48, s28, 64\nv_mov_b32 v36, s48\ns_waitcnt vmcnt(0) lgkmcnt(0)\ns_barrier\n"
"buffer_store_dword v36, v19, s[24:27], s33 offen sc1\nfg1_end:\n"
    :  : "{s[8:23]}"(s0), "{s[24:39]}"(s1), "{v[8:23]}"(v0), "{a[128:143]}"(k0), "{a[144:159]}"(k1), "{a[160:175]}"(k2), "{a[176:191]}"(k3) : "v24", "v25", "v26", "v27", "v28", "v29", "v30", "v31", "v32", "v33", "v34", "v35", "v36", "v37", "v38", "v39", "v40", "v41", "v42", "v43", "v44", "v45", "v46", "v47", "v48", "v49", "v50", "v51", "v52", "v53", "v54", "v55", "v56", "v57", "v58", "v59", "v60", "v61", "v62", "v63", "v64", "v65", "v66", "v67", "v68", "v69", "v70", "v71", "v72", "v73", "v74", "v75", "v76", "v77", "v78", "v79", "v80", "v81", "v82", "v83", "v84", "v85", "v86", "v87", "v88", "v89", "v90", "v91", "v92", "v93", "v94", "v95", "v96", "v97", "v98", "v99", "v100", "v101", "v102", "v103", "v104", "v105", "v106", "v107", "v108", "v109", "v110", "v111", "v112", "v113", "v114", "v115", "v116", "v117", "v118", "v119", "v120", "v121", "v122", "v123", "v124", "v125", "v126", "v127", "v128", "v129", "v130", "v131", "v132", "v133", "v134", "v135", "v136", "v137", "v138", "v139", "v140", "v141", "v142", "v143", "v144", "v145", "v146", "v147", "v148", "v149", "v150", "v151", "v152", "v153", "v154", "v155", "v156", "v157", "v158", "v159", "v160", "v161", "v162", "v163", "v164", "v165", "v166", "v167", "v168", "v169", "v170", "v171", "v172", "v173", "v174", "v175", "v176", "v177", "v178", "v179", "v180", "v181", "v182", "v183", "v184", "v185", "v186", "v187", "v188", "v189", "v190", "v191", "v192", "v193", "v194", "v195", "v196", "v197", "v198", "v199", "v200", "v201", "v202", "v203", "v204", "v205", "v206", "v207", "v208", "v209", "v210", "v211", "v212", "v213", "v214", "v215", "v216", "v217", "v218", "v219", "v220", "v221", "v222", "v223", "v224", "v225", "v226", "v227", "v228", "v229", "v230", "v231", "v232", "v233", "v234", "v235", "v236", "v237", "v238", "v239", "a0", "a1", "a2", "a3", "a4", "a5", "a6", "a7", "a8", "a9", "a10", "a11", "a12", "a13", "a14", "a15", "a16", "a17", "a18", "a19", "a20", "a21", "a22", "a23", "a24", "a25", "a26", "a27", "a28", "a29", "a30", "a31", "a32", "a33", "a34", "a35", "a36", "a37", "a38", "a39", "a40", "a41", "a42", "a43", "a44", "a45", "a46", "a47", "a48", "a49", "a50", "a51", "a52", "a53", "a54", "a55", "a56", "a57", "a58", "a59", "a60", "a61", "a62", "a63", "a64", "a65", "a66", "a67", "a68", "a69", "a70", "a71", "a72", "a73", "a74", "a75", "a76", "a77", "a78", "a79", "a80", "a81", "a82", "a83", "a84", "a85", "a86", "a87", "a88", "a89", "a90", "a91", "a92", "a93", "a94", "a95", "a96", "a97", "a98", "a99", "a100", "a101", "a102", "a103", "a104", "a105", "a106", "a107", "a108", "a109", "a110", "a111", "s40", "s41", "s42", "s43", "s44", "s45", "s46", "s47", "s48", "s49", "s50", "s51", "s52", "s53", "s54", "s55", "s56", "s57", "vcc", "scc", "memory");
    return o;
}
struct OCtx { u4 r; unsigned sg, ssx, sr, su, sum, nt, hx, p0, p1, p2, p3, p4; unsigned vg, vsx, vu, vum, vx, vt, vpx, vpm, vpr, vo, p5, p6, p7, p8, p9, pa; };
DI void o_asm(const OCtx& c) {
    const u16v s0 = {RFL(c.r[0]), RFL(c.r[1]), RFL(c.r[2]), RFL(c.r[3]), RFL(c.sg), RFL(c.ssx), RFL(c.sr), RFL(c.su), RFL(c.sum), RFL(c.nt), RFL(c.hx), RFL(c.p0), RFL(c.p1), RFL(c.p2), RFL(c.p3), RFL(c.p4)}, v0 = {c.vg, c.vsx, c.vu, c.vum, c.vx, c.vt, c.vpx, c.vpm, c.vpr, c.vo, c.p5, c.p6, c.p7, c.p8, c.p9, c.pa};
    asm volatile(
"s_nop 4\ns_xor_b32 s72, s18, 0\ns_mul_i32 s72, s72, 1835008\ns_add_u32 s72, s72, s12\n"
"buffer_load_dwordx4 a[0:3], v8, s[8:11], s72 offen offset:0 sc1\nbuffer_load_dwordx4 a[4:7], v8, s[8:11], s72 offen offset:512 sc1\nbuffer_load_dwordx4 a[8:11], v8, s[8:11], s72 offen offset:1024 sc1\nbuffer_load_dwordx4 a[12:15], v8, s[8:11], s72 offen offset:1536 sc1\n"
"buffer_load_dwordx4 a[16:19], v8, s[8:11], s72 offen offset:2048 sc1\nbuffer_load_dwordx4 a[20:23], v8, s[8:11], s72 offen offset:2560 sc1\nbuffer_load_dwordx4 a[24:27], v8, s[8:11], s72 offen offset:3072 sc1\nbuffer_load_dwordx4 a[28:31], v8, s[8:11], s72 offen offset:3584 sc1\n"
"s_add_u32 s72, s72, 4096\nbuffer_load_dwordx4 a[32:35], v8, s[8:11], s72 offen offset:0 sc1\nbuffer_load_dwordx4 a[36:39], v8, s[8:11], s72 offen offset:512 sc1\nbuffer_load_dwordx4 a[40:43], v8, s[8:11], s72 offen offset:1024 sc1\n"
"buffer_load_dwordx4 a[44:47], v8, s[8:11], s72 offen offset:1536 sc1\nbuffer_load_dwordx4 a[48:51], v8, s[8:11], s72 offen offset:2048 sc1\nbuffer_load_dwordx4 a[52:55], v8, s[8:11], s72 offen offset:2560 sc1\nbuffer_load_dwordx4 a[56:59], v8, s[8:11], s72 offen offset:3072 sc1\n"
"buffer_load_dwordx4 a[60:63], v8, s[8:11], s72 offen offset:3584 sc1\ns_add_u32 s72, s72, 4096\nbuffer_load_dwordx4 a[64:67], v8, s[8:11], s72 offen offset:0 sc1\nbuffer_load_dwordx4 a[68:71], v8, s[8:11], s72 offen offset:512 sc1\n"
"buffer_load_dwordx4 a[72:75], v8, s[8:11], s72 offen offset:1024 sc1\nbuffer_load_dwordx4 a[76:79], v8, s[8:11], s72 offen offset:1536 sc1\nbuffer_load_dwordx4 a[80:83], v8, s[8:11], s72 offen offset:2048 sc1\nbuffer_load_dwordx4 a[84:87], v8, s[8:11], s72 offen offset:2560 sc1\n"
"buffer_load_dwordx4 a[88:91], v8, s[8:11], s72 offen offset:3072 sc1\nbuffer_load_dwordx4 a[92:95], v8, s[8:11], s72 offen offset:3584 sc1\ns_add_u32 s72, s72, 4096\nbuffer_load_dwordx4 a[96:99], v8, s[8:11], s72 offen offset:0 sc1\n"
"buffer_load_dwordx4 a[100:103], v8, s[8:11], s72 offen offset:512 sc1\nbuffer_load_dwordx4 a[104:107], v8, s[8:11], s72 offen offset:1024 sc1\nbuffer_load_dwordx4 a[108:111], v8, s[8:11], s72 offen offset:1536 sc1\ns_xor_b32 s73, s18, 0\n"
"s_lshl_b32 s73, s73, 18\ns_add_u32 s73, s73, s13\nbuffer_load_dwordx4 v[64:67], v9, s[8:11], s73 offen sc1\ns_add_u32 s73, s73, 65536\n"
"buffer_load_dwordx4 v[68:71], v9, s[8:11], s73 offen sc1\ns_add_u32 s73, s73, 65536\nbuffer_load_dwordx4 v[72:75], v9, s[8:11], s73 offen sc1\ns_add_u32 s73, s73, 65536\n"
"buffer_load_dwordx4 v[76:79], v9, s[8:11], s73 offen sc1\ns_xor_b32 s72, s18, 1\ns_mul_i32 s72, s72, 1835008\ns_add_u32 s72, s72, s12\n"
"buffer_load_dwordx4 a[112:115], v8, s[8:11], s72 offen offset:0 sc1\nbuffer_load_dwordx4 a[116:119], v8, s[8:11], s72 offen offset:512 sc1\nbuffer_load_dwordx4 a[120:123], v8, s[8:11], s72 offen offset:1024 sc1\nbuffer_load_dwordx4 a[124:127], v8, s[8:11], s72 offen offset:1536 sc1\n"
"buffer_load_dwordx4 a[128:131], v8, s[8:11], s72 offen offset:2048 sc1\nbuffer_load_dwordx4 a[132:135], v8, s[8:11], s72 offen offset:2560 sc1\nbuffer_load_dwordx4 a[136:139], v8, s[8:11], s72 offen offset:3072 sc1\nbuffer_load_dwordx4 a[140:143], v8, s[8:11], s72 offen offset:3584 sc1\n"
"s_add_u32 s72, s72, 4096\nbuffer_load_dwordx4 a[144:147], v8, s[8:11], s72 offen offset:0 sc1\nbuffer_load_dwordx4 a[148:151], v8, s[8:11], s72 offen offset:512 sc1\nbuffer_load_dwordx4 a[152:155], v8, s[8:11], s72 offen offset:1024 sc1\n"
"buffer_load_dwordx4 a[156:159], v8, s[8:11], s72 offen offset:1536 sc1\nbuffer_load_dwordx4 a[160:163], v8, s[8:11], s72 offen offset:2048 sc1\nbuffer_load_dwordx4 a[164:167], v8, s[8:11], s72 offen offset:2560 sc1\nbuffer_load_dwordx4 a[168:171], v8, s[8:11], s72 offen offset:3072 sc1\n"
"buffer_load_dwordx4 a[172:175], v8, s[8:11], s72 offen offset:3584 sc1\ns_add_u32 s72, s72, 4096\nbuffer_load_dwordx4 a[176:179], v8, s[8:11], s72 offen offset:0 sc1\nbuffer_load_dwordx4 a[180:183], v8, s[8:11], s72 offen offset:512 sc1\n"
"buffer_load_dwordx4 a[184:187], v8, s[8:11], s72 offen offset:1024 sc1\nbuffer_load_dwordx4 a[188:191], v8, s[8:11], s72 offen offset:1536 sc1\nbuffer_load_dwordx4 a[192:195], v8, s[8:11], s72 offen offset:2048 sc1\nbuffer_load_dwordx4 a[196:199], v8, s[8:11], s72 offen offset:2560 sc1\n"
"buffer_load_dwordx4 a[200:203], v8, s[8:11], s72 offen offset:3072 sc1\nbuffer_load_dwordx4 a[204:207], v8, s[8:11], s72 offen offset:3584 sc1\ns_add_u32 s72, s72, 4096\nbuffer_load_dwordx4 a[208:211], v8, s[8:11], s72 offen offset:0 sc1\n"
"buffer_load_dwordx4 a[212:215], v8, s[8:11], s72 offen offset:512 sc1\nbuffer_load_dwordx4 a[216:219], v8, s[8:11], s72 offen offset:1024 sc1\nbuffer_load_dwordx4 a[220:223], v8, s[8:11], s72 offen offset:1536 sc1\ns_add_u32 s76, s15, 0\n"
"v_xor_b32 v24, 0, v12\ns_add_u32 s77, s15, 4096\nv_xor_b32 v25, 32, v12\ns_add_u32 s78, s15, 8192\n"
"v_xor_b32 v26, 64, v12\ns_add_u32 s79, s15, 12288\nv_xor_b32 v27, 96, v12\ns_mov_b32 s80, s16\n"
"v_cmp_gt_i32 s[40:41], 0, v13\nv_cmp_gt_i32 s[42:43], 1, v13\nv_cmp_gt_i32 s[44:45], 2, v13\nv_cmp_gt_i32 s[46:47], 3, v13\n"
"v_cmp_gt_i32 s[48:49], 4, v13\nv_cmp_gt_i32 s[50:51], 5, v13\nv_cmp_gt_i32 s[52:53], 6, v13\nv_cmp_gt_i32 s[54:55], 7, v13\n"
"v_cmp_gt_i32 s[56:57], 16, v13\nv_cmp_gt_i32 s[58:59], 17, v13\nv_cmp_gt_i32 s[60:61], 18, v13\nv_cmp_gt_i32 s[62:63], 19, v13\n"
"v_cmp_gt_i32 s[64:65], 20, v13\nv_cmp_gt_i32 s[66:67], 21, v13\nv_cmp_gt_i32 s[68:69], 22, v13\nv_cmp_gt_i32 s[70:71], 23, v13\n"
"s_barrier\nds_read_b128 v[128:131], v24 offset:0\nds_read_b128 v[132:135], v25 offset:0\nds_read_b128 v[136:139], v26 offset:0\n"
"ds_read_b128 v[140:143], v27 offset:0\nds_read_b128 v[144:147], v24 offset:4096\nds_read_b128 v[148:151], v25 offset:4096\nds_read_b128 v[152:155], v26 offset:4096\n"
"ds_read_b128 v[156:159], v27 offset:4096\nds_read_b128 v[160:163], v24 offset:8192\nds_read_b128 v[164:167], v25 offset:8192\nds_read_b128 v[168:171], v26 offset:8192\n"
"ds_read_b128 v[172:175], v27 offset:8192\ns_waitcnt lgkmcnt(0)\nds_read_b128 v[176:179], v24 offset:12288\nds_read_b128 v[180:183], v25 offset:12288\n"
"ds_read_b128 v[184:187], v26 offset:12288\nds_read_b128 v[188:191], v27 offset:12288\nds_read_b128 v[192:195], v24 offset:16384\nds_read_b128 v[196:199], v25 offset:16384\n"
"ds_read_b128 v[200:203], v26 offset:16384\nds_read_b128 v[204:207], v27 offset:16384\nds_read_b128 v[208:211], v24 offset:20480\nds_read_b128 v[212:215], v25 offset:20480\n"
"ds_read_b128 v[216:219], v26 offset:20480\nds_read_b128 v[220:223], v27 offset:20480\ns_waitcnt lgkmcnt(0)\nds_read_b128 v[224:227], v24 offset:24576\n"
"ds_read_b128 v[228:231], v25 offset:24576\nds_read_b128 v[232:235], v26 offset:24576\nds_read_b128 v[236:239], v27 offset:24576\nv_mov_b32 v31, 0xff800000\n"
"s_waitcnt lgkmcnt(0)\ns_waitcnt vmcnt(59)\nv_mfma_f32_32x32x16_bf16 v[40:55], a[0:3], v[128:131], 0\ns_xor_b32 s73, s18, 1\n"
"s_lshl_b32 s73, s73, 18\ns_add_u32 s73, s73, s13\nbuffer_load_dwordx4 v[80:83], v9, s[8:11], s73 offen sc1\ns_add_u32 s73, s73, 65536\n"
"buffer_load_dwordx4 v[84:87], v9, s[8:11], s73 offen sc1\ns_add_u32 s73, s73, 65536\nbuffer_load_dwordx4 v[88:91], v9, s[8:11], s73 offen sc1\ns_add_u32 s73, s73, 65536\n"
"buffer_load_dwordx4 v[92:95], v9, s[8:11], s73 offen sc1\ns_xor_b32 s72, s18, 2\ns_mul_i32 s72, s72, 1835008\ns_add_u32 s72, s72, s12\n"
"buffer_load_dwordx4 a[0:3], v8, s[8:11], s72 offen offset:0 sc1\ns_waitcnt vmcnt(63)\nv_mfma_f32_32x32x16_bf16 v[40:55], a[4:7], v[132:135], v[40:55]\nbuffer_load_dwordx4 a[4:7], v8, s[8:11], s72 offen offset:512 sc1\n"
"s_waitcnt vmcnt(63)\nv_mfma_f32_32x32x16_bf16 v[40:55], a[8:11], v[136:139], v[40:55]\nbuffer_load_dwordx4 a[8:11], v8, s[8:11], s72 offen offset:1024 sc1\ns_waitcnt vmcnt(63)\n"
"v_mfma_f32_32x32x16_bf16 v[40:55], a[12:15], v[140:143], v[40:55]\nbuffer_load_dwordx4 a[12:15], v8, s[8:11], s72 offen offset:1536 sc1\ns_waitcnt vmcnt(63)\nv_mfma_f32_32x32x16_bf16 v[40:55], a[16:19], v[144:147], v[40:55]\n"
"buffer_load_dwordx4 a[16:19], v8, s[8:11], s72 offen offset:2048 sc1\ns_waitcnt vmcnt(63)\nv_mfma_f32_32x32x16_bf16 v[40:55], a[20:23], v[148:151], v[40:55]\nbuffer_load_dwordx4 a[20:23], v8, s[8:11], s72 offen offset:2560 sc1\n"
"s_waitcnt vmcnt(63)\nv_mfma_f32_32x32x16_bf16 v[40:55], a[24:27], v[152:155], v[40:55]\nbuffer_load_dwordx4 a[24:27], v8, s[8:11], s72 offen offset:3072 sc1\ns_waitcnt vmcnt(63)\n"
"v_mfma_f32_32x32x16_bf16 v[40:55], a[28:31], v[156:159], v[40:55]\nbuffer_load_dwordx4 a[28:31], v8, s[8:11], s72 offen offset:3584 sc1\ns_waitcnt vmcnt(63)\nv_mfma_f32_32x32x16_bf16 v[40:55], a[32:35], v[160:163], v[40:55]\n"
"s_add_u32 s72, s72, 4096\nbuffer_load_dwordx4 a[32:35], v8, s[8:11], s72 offen offset:0 sc1\ns_waitcnt vmcnt(63)\nv_mfma_f32_32x32x16_bf16 v[40:55], a[36:39], v[164:167], v[40:55]\n"
"buffer_load_dwordx4 a[36:39], v8, s[8:11], s72 offen offset:512 sc1\ns_waitcnt vmcnt(63)\nv_mfma_f32_32x32x16_bf16 v[40:55], a[40:43], v[168:171], v[40:55]\nbuffer_load_dwordx4 a[40:43], v8, s[8:11], s72 offen offset:1024 sc1\n"
"s_waitcnt vmcnt(63)\nv_mfma_f32_32x32x16_bf16 v[40:55], a[44:47], v[172:175], v[40:55]\nbuffer_load_dwordx4 a[44:47], v8, s[8:11], s72 offen offset:1536 sc1\ns_waitcnt vmcnt(63)\n"
"v_mfma_f32_32x32x16_bf16 v[40:55], a[48:51], v[176:179], v[40:55]\nbuffer_load_dwordx4 a[48:51], v8, s[8:11], s72 offen offset:2048 sc1\ns_waitcnt vmcnt(63)\nv_mfma_f32_32x32x16_bf16 v[40:55], a[52:55], v[180:183], v[40:55]\n"
"buffer_load_dwordx4 a[52:55], v8, s[8:11], s72 offen offset:2560 sc1\ns_waitcnt vmcnt(63)\nv_mfma_f32_32x32x16_bf16 v[40:55], a[56:59], v[184:187], v[40:55]\nbuffer_load_dwordx4 a[56:59], v8, s[8:11], s72 offen offset:3072 sc1\n"
"s_waitcnt vmcnt(63)\nv_mfma_f32_32x32x16_bf16 v[40:55], a[60:63], v[188:191], v[40:55]\nbuffer_load_dwordx4 a[60:63], v8, s[8:11], s72 offen offset:3584 sc1\ns_waitcnt vmcnt(63)\n"
"v_mfma_f32_32x32x16_bf16 v[40:55], a[64:67], v[192:195], v[40:55]\ns_add_u32 s72, s72, 4096\nbuffer_load_dwordx4 a[64:67], v8, s[8:11], s72 offen offset:0 sc1\ns_waitcnt vmcnt(63)\n"
"v_mfma_f32_32x32x16_bf16 v[40:55], a[68:71], v[196:199], v[40:55]\nbuffer_load_dwordx4 a[68:71], v8, s[8:11], s72 offen offset:512 sc1\ns_waitcnt vmcnt(63)\nv_mfma_f32_32x32x16_bf16 v[40:55], a[72:75], v[200:203], v[40:55]\n"
"buffer_load_dwordx4 a[72:75], v8, s[8:11], s72 offen offset:1024 sc1\ns_waitcnt vmcnt(63)\nv_mfma_f32_32x32x16_bf16 v[40:55], a[76:79], v[204:207], v[40:55]\nbuffer_load_dwordx4 a[76:79], v8, s[8:11], s72 offen offset:1536 sc1\n"
"s_waitcnt vmcnt(63)\nv_mfma_f32_32x32x16_bf16 v[40:55], a[80:83], v[208:211], v[40:55]\nbuffer_load_dwordx4 a[80:83], v8, s[8:11], s72 offen offset:2048 sc1\ns_waitcnt vmcnt(63)\n"
"v_mfma_f32_32x32x16_bf16 v[40:55], a[84:87], v[212:215], v[40:55]\nbuffer_load_dwordx4 a[84:87], v8, s[8:11], s72 offen offset:2560 sc1\ns_waitcnt vmcnt(63)\nv_mfma_f32_32x32x16_bf16 v[40:55], a[88:91], v[216:219], v[40:55]\n"
"buffer_load_dwordx4 a[88:91], v8, s[8:11], s72 offen offset:3072 sc1\ns_waitcnt vmcnt(63)\nv_mfma_f32_32x32x16_bf16 v[40:55], a[92:95], v[220:223], v[40:55]\nbuffer_load_dwordx4 a[92:95], v8, s[8:11], s72 offen offset:3584 sc1\n"
"s_waitcnt vmcnt(63)\nv_mfma_f32_32x32x16_bf16 v[40:55], a[96:99], v[224:227], v[40:55]\ns_add_u32 s72, s72, 4096\nbuffer_load_dwordx4 a[96:99], v8, s[8:11], s72 offen offset:0 sc1\n"
"s_waitcnt vmcnt(63)\nv_mfma_f32_32x32x16_bf16 v[40:55], a[100:103], v[228:231], v[40:55]\nbuffer_load_dwordx4 a[100:103], v8, s[8:11], s72 offen offset:512 sc1\ns_waitcnt vmcnt(63)\n"
"v_mfma_f32_32x32x16_bf16 v[40:55], a[104:107], v[232:235], v[40:55]\nbuffer_load_dwordx4 a[104:107], v8, s[8:11], s72 offen offset:1024 sc1\ns_waitcnt vmcnt(63)\nv_mfma_f32_32x32x16_bf16 v[40:55], a[108:111], v[236:239], v[40:55]\n"
"buffer_load_dwordx4 a[108:111], v8, s[8:11], s72 offen offset:1536 sc1\ns_waitcnt vmcnt(60)\nv_add_f32 v64, v64, v68\nv_add_f32 v72, v72, v76\n"
"v_add_f32 v65, v65, v69\nv_add_f32 v73, v73, v77\nv_add_f32 v66, v66, v70\nv_add_f32 v74, v74, v78\n"
"v_add_f32 v67, v67, v71\nv_add_f32 v75, v75, v79\nv_add_f32 v64, v64, v72\nv_add_f32 v65, v65, v73\n"
"v_add_f32 v66, v66, v74\nv_add_f32 v67, v67, v75\nv_max3_f32 v28, v64, v65, v66\nv_max_f32 v28, v28, v67\n"
"v_cndmask_b32 v40, v40, v31, s[40:41]\nv_cndmask_b32 v41, v41, v31, s[42:43]\nv_cndmask_b32 v42, v42, v31, s[44:45]\nv_cndmask_b32 v43, v43, v31, s[46:47]\n"
"v_cndmask_b32 v44, v44, v31, s[48:49]\nv_cndmask_b32 v45, v45, v31, s[50:51]\nv_cndmask_b32 v46, v46, v31, s[52:53]\nv_cndmask_b32 v47, v47, v31, s[54:55]\n"
"v_cndmask_b32 v48, v48, v31, s[56:57]\nv_cndmask_b32 v49, v49, v31, s[58:59]\nv_cndmask_b32 v50, v50, v31, s[60:61]\nv_cndmask_b32 v51, v51, v31, s[62:63]\n"
"v_cndmask_b32 v52, v52, v31, s[64:65]\nv_cndmask_b32 v53, v53, v31, s[66:67]\nv_cndmask_b32 v54, v54, v31, s[68:69]\nv_cndmask_b32 v55, v55, v31, s[70:71]\n"
"v_max3_f32 v28, v28, v40, v41\nv_max3_f32 v28, v28, v42, v43\nv_max3_f32 v28, v28, v44, v45\nv_max3_f32 v28, v28, v46, v47\n"
"v_max3_f32 v28, v28, v48, v49\nv_max3_f32 v28, v28, v50, v51\nv_max3_f32 v28, v28, v52, v53\nv_max3_f32 v28, v28, v54, v55\n"
"v_mov_b32 v30, v28\ns_nop 1\nv_permlane32_swap_b32 v28, v30\nv_max_f32 v28, v28, v30\n"
"v_sub_f32 v40, v40, v28\nv_sub_f32 v41, v41, v28\nv_sub_f32 v42, v42, v28\nv_sub_f32 v43, v43, v28\n"
"v_sub_f32 v44, v44, v28\nv_sub_f32 v45, v45, v28\nv_sub_f32 v46, v46, v28\nv_sub_f32 v47, v47, v28\n"
"v_sub_f32 v48, v48, v28\nv_sub_f32 v49, v49, v28\nv_sub_f32 v50, v50, v28\nv_sub_f32 v51, v51, v28\n"
"v_sub_f32 v52, v52, v28\nv_sub_f32 v53, v53, v28\nv_sub_f32 v54, v54, v28\nv_sub_f32 v55, v55, v28\n"
"v_sub_f32 v64, v64, v28\nv_sub_f32 v65, v65, v28\nv_sub_f32 v66, v66, v28\nv_sub_f32 v67, v67, v28\n"
"v_exp_f32 v40, v40\nv_exp_f32 v41, v41\nv_exp_f32 v42, v42\nv_exp_f32 v43, v43\n"
"v_exp_f32 v44, v44\nv_exp_f32 v45, v45\nv_exp_f32 v46, v46\nv_exp_f32 v47, v47\n"
"v_exp_f32 v48, v48\nv_exp_f32 v49, v49\nv_exp_f32 v50, v50\nv_exp_f32 v51, v51\n"
"v_exp_f32 v52, v52\nv_exp_f32 v53, v53\nv_exp_f32 v54, v54\nv_exp_f32 v55, v55\n"
"v_exp_f32 v64, v64\nv_exp_f32 v65, v65\nv_exp_f32 v66, v66\nv_exp_f32 v67, v67\n"
"v_add_f32 v29, v40, v41\nv_add_f32 v30, v42, v43\nv_add_f32 v29, v29, v44\nv_add_f32 v30, v30, v45\n"
"v_add_f32 v29, v29, v46\nv_add_f32 v30, v30, v47\nv_add_f32 v29, v29, v48\nv_add_f32 v30, v30, v49\n"
"v_add_f32 v29, v29, v50\nv_add_f32 v30, v30, v51\nv_add_f32 v29, v29, v52\nv_add_f32 v30, v30, v53\n"
"v_add_f32 v29, v29, v54\nv_add_f32 v30, v30, v55\nv_add_f32 v29, v29, v64\nv_add_f32 v30, v30, v65\n"
"v_add_f32 v29, v29, v66\nv_add_f32 v30, v30, v67\nv_add_f32 v29, v29, v30\nv_mov_b32 v30, v29\n"
"s_nop 1\nv_permlane32_swap_b32 v29, v30\nv_add_f32 v29, v29, v30\nv_rcp_f32 v29, v29\n"
"s_nop 0\nv_mul_f32 v40, v40, v29\nv_mul_f32 v41, v41, v29\nv_mul_f32 v42, v42, v29\n"
"v_mul_f32 v43, v43, v29\nv_mul_f32 v44, v44, v29\nv_mul_f32 v45, v45, v29\nv_mul_f32 v46, v46, v29\n"
"v_mul_f32 v47, v47, v29\nv_mul_f32 v48, v48, v29\nv_mul_f32 v49, v49, v29\nv_mul_f32 v50, v50, v29\n"
"v_mul_f32 v51, v51, v29\nv_mul_f32 v52, v52, v29\nv_mul_f32 v53, v53, v29\nv_mul_f32 v54, v54, v29\n"
"v_mul_f32 v55, v55, v29\nv_mul_f32 v64, v64, v29\nv_mul_f32 v65, v65, v29\nv_mul_f32 v66, v66, v29\n"
"v_mul_f32 v67, v67, v29\nv_cvt_pk_bf16_f32 v56, v40, v41\nv_cvt_pk_bf16_f32 v57, v42, v43\nv_cvt_pk_bf16_f32 v58, v44, v45\n"
"v_cvt_pk_bf16_f32 v59, v46, v47\nv_cvt_pk_bf16_f32 v60, v48, v49\nv_cvt_pk_bf16_f32 v61, v50, v51\nv_cvt_pk_bf16_f32 v62, v52, v53\n"
"v_cvt_pk_bf16_f32 v63, v54, v55\nv_cvt_pk_bf16_f32 v32, v64, v65\nv_cvt_pk_bf16_f32 v36, v66, v67\nv_xor_b32 v30, 0, v14\n"
"ds_write_b128 v30, v[56:59]\nds_write_b128 v30, v[60:63] offset:1024\ns_waitcnt vmcnt(59)\nv_mfma_f32_32x32x16_bf16 v[40:55], a[112:115], v[128:131], 0\n"
"s_xor_b32 s73, s18, 2\ns_lshl_b32 s73, s73, 18\ns_add_u32 s73, s73, s13\nbuffer_load_dwordx4 v[96:99], v9, s[8:11], s73 offen sc1\n"
"s_add_u32 s73, s73, 65536\nbuffer_load_dwordx4 v[100:103], v9, s[8:11], s73 offen sc1\ns_add_u32 s73, s73, 65536\nbuffer_load_dwordx4 v[104:107], v9, s[8:11], s73 offen sc1\n"
"s_add_u32 s73, s73, 65536\nbuffer_load_dwordx4 v[108:111], v9, s[8:11], s73 offen sc1\ns_xor_b32 s72, s18, 3\ns_mul_i32 s72, s72, 1835008\n"
"s_add_u32 s72, s72, s12\nbuffer_load_dwordx4 a[112:115], v8, s[8:11], s72 offen offset:0 sc1\ns_waitcnt vmcnt(63)\nv_mfma_f32_32x32x16_bf16 v[40:55], a[116:119], v[132:135], v[40:55]\n"
"buffer_load_dwordx4 a[116:119], v8, s[8:11], s72 offen offset:512 sc1\ns_waitcnt vmcnt(63)\nv_mfma_f32_32x32x16_bf16 v[40:55], a[120:123], v[136:139], v[40:55]\nbuffer_load_dwordx4 a[120:123], v8, s[8:11], s72 offen offset:1024 sc1\n"
"s_waitcnt vmcnt(63)\nv_mfma_f32_32x32x16_bf16 v[40:55], a[124:127], v[140:143], v[40:55]\nbuffer_load_dwordx4 a[124:127], v8, s[8:11], s72 offen offset:1536 sc1\ns_waitcnt vmcnt(63)\n"
"v_mfma_f32_32x32x16_bf16 v[40:55], a[128:131], v[144:147], v[40:55]\nbuffer_load_dwordx4 a[128:131], v8, s[8:11], s72 offen offset:2048 sc1\ns_waitcnt vmcnt(63)\nv_mfma_f32_32x32x16_bf16 v[40:55], a[132:135], v[148:151], v[40:55]\n"
"buffer_load_dwordx4 a[132:135], v8, s[8:11], s72 offen offset:2560 sc1\ns_waitcnt vmcnt(63)\nv_mfma_f32_32x32x16_bf16 v[40:55], a[136:139], v[152:155], v[40:55]\nbuffer_load_dwordx4 a[136:139], v8, s[8:11], s72 offen offset:3072 sc1\n"
"s_waitcnt vmcnt(63)\nv_mfma_f32_32x32x16_bf16 v[40:55], a[140:143], v[156:159], v[40:55]\nbuffer_load_dwordx4 a[140:143], v8, s[8:11], s72 offen offset:3584 sc1\ns_waitcnt vmcnt(63)\n"
"v_mfma_f32_32x32x16_bf16 v[40:55], a[144:147], v[160:163], v[40:55]\ns_add_u32 s72, s72, 4096\nbuffer_load_dwordx4 a[144:147], v8, s[8:11], s72 offen offset:0 sc1\ns_waitcnt vmcnt(63)\n"
"v_mfma_f32_32x32x16_bf16 v[40:55], a[148:151], v[164:167], v[40:55]\nbuffer_load_dwordx4 a[148:151], v8, s[8:11], s72 offen offset:512 sc1\ns_waitcnt vmcnt(63)\nv_mfma_f32_32x32x16_bf16 v[40:55], a[152:155], v[168:171], v[40:55]\n"
"buffer_load_dwordx4 a[152:155], v8, s[8:11], s72 offen offset:1024 sc1\ns_waitcnt vmcnt(63)\nv_mfma_f32_32x32x16_bf16 v[40:55], a[156:159], v[172:175], v[40:55]\nbuffer_load_dwordx4 a[156:159], v8, s[8:11], s72 offen offset:1536 sc1\n"
"s_waitcnt vmcnt(63)\nv_mfma_f32_32x32x16_bf16 v[40:55], a[160:163], v[176:179], v[40:55]\nbuffer_load_dwordx4 a[160:163], v8, s[8:11], s72 offen offset:2048 sc1\ns_waitcnt vmcnt(63)\n"
"v_mfma_f32_32x32x16_bf16 v[40:55], a[164:167], v[180:183], v[40:55]\nbuffer_load_dwordx4 a[164:167], v8, s[8:11], s72 offen offset:2560 sc1\ns_waitcnt vmcnt(63)\nv_mfma_f32_32x32x16_bf16 v[40:55], a[168:171], v[184:187], v[40:55]\n"
"buffer_load_dwordx4 a[168:171], v8, s[8:11], s72 offen offset:3072 sc1\ns_waitcnt vmcnt(63)\nv_mfma_f32_32x32x16_bf16 v[40:55], a[172:175], v[188:191], v[40:55]\nbuffer_load_dwordx4 a[172:175], v8, s[8:11], s72 offen offset:3584 sc1\n"
"s_waitcnt vmcnt(63)\nv_mfma_f32_32x32x16_bf16 v[40:55], a[176:179], v[192:195], v[40:55]\ns_add_u32 s72, s72, 4096\nbuffer_load_dwordx4 a[176:179], v8, s[8:11], s72 offen offset:0 sc1\n"
"s_waitcnt vmcnt(63)\nv_mfma_f32_32x32x16_bf16 v[40:55], a[180:183], v[196:199], v[40:55]\nbuffer_load_dwordx4 a[180:183], v8, s[8:11], s72 offen offset:512 sc1\ns_waitcnt vmcnt(63)\n"
"v_mfma_f32_32x32x16_bf16 v[40:55], a[184:187], v[200:203], v[40:55]\nbuffer_load_dwordx4 a[184:187], v8, s[8:11], s72 offen offset:1024 sc1\ns_waitcnt vmcnt(63)\nv_mfma_f32_32x32x16_bf16 v[40:55], a[188:191], v[204:207], v[40:55]\n"
"buffer_load_dwordx4 a[188:191], v8, s[8:11], s72 offen offset:1536 sc1\ns_waitcnt vmcnt(63)\nv_mfma_f32_32x32x16_bf16 v[40:55], a[192:195], v[208:211], v[40:55]\nbuffer_load_dwordx4 a[192:195], v8, s[8:11], s72 offen offset:2048 sc1\n"
"s_waitcnt vmcnt(63)\nv_mfma_f32_32x32x16_bf16 v[40:55], a[196:199], v[212:215], v[40:55]\nbuffer_load_dwordx4 a[196:199], v8, s[8:11], s72 offen offset:2560 sc1\ns_waitcnt vmcnt(63)\n"
"v_mfma_f32_32x32x16_bf16 v[40:55], a[200:203], v[216:219], v[40:55]\nbuffer_load_dwordx4 a[200:203], v8, s[8:11], s72 offen offset:3072 sc1\ns_waitcnt vmcnt(63)\nv_mfma_f32_32x32x16_bf16 v[40:55], a[204:207], v[220:223], v[40:55]\n"
"buffer_load_dwordx4 a[204:207], v8, s[8:11], s72 offen offset:3584 sc1\ns_waitcnt vmcnt(63)\nv_mfma_f32_32x32x16_bf16 v[40:55], a[208:211], v[224:227], v[40:55]\ns_add_u32 s72, s72, 4096\n"
"buffer_load_dwordx4 a[208:211], v8, s[8:11], s72 offen offset:0 sc1\ns_waitcnt vmcnt(63)\nv_mfma_f32_32x32x16_bf16 v[40:55], a[212:215], v[228:231], v[40:55]\nbuffer_load_dwordx4 a[212:215], v8, s[8:11], s72 offen offset:512 sc1\n"
"s_waitcnt vmcnt(63)\nv_mfma_f32_32x32x16_bf16 v[40:55], a[216:219], v[232:235], v[40:55]\nbuffer_load_dwordx4 a[216:219], v8, s[8:11], s72 offen offset:1024 sc1\ns_waitcnt vmcnt(63)\n"
"v_mfma_f32_32x32x16_bf16 v[40:55], a[220:223], v[236:239], v[40:55]\nbuffer_load_dwordx4 a[220:223], v8, s[8:11], s72 offen offset:1536 sc1\ns_waitcnt vmcnt(60)\nv_add_f32 v80, v80, v84\n"
"v_add_f32 v88, v88, v92\nv_add_f32 v81, v81, v85\nv_add_f32 v89, v89, v93\nv_add_f32 v82, v82, v86\n"
"v_add_f32 v90, v90, v94\nv_add_f32 v83, v83, v87\nv_add_f32 v91, v91, v95\nv_add_f32 v80, v80, v88\n"
"v_add_f32 v81, v81, v89\nv_add_f32 v82, v82, v90\nv_add_f32 v83, v83, v91\nv_max3_f32 v28, v80, v81, v82\n"
"v_max_f32 v28, v28, v83\nv_cndmask_b32 v40, v40, v31, s[40:41]\nv_cndmask_b32 v41, v41, v31, s[42:43]\nv_cndmask_b32 v42, v42, v31, s[44:45]\n"
"v_cndmask_b32 v43, v43, v31, s[46:47]\nv_cndmask_b32 v44, v44, v31, s[48:49]\nv_cndmask_b32 v45, v45, v31, s[50:51]\nv_cndmask_b32 v46, v46, v31, s[52:53]\n"
"v_cndmask_b32 v47, v47, v31, s[54:55]\nv_cndmask_b32 v48, v48, v31, s[56:57]\nv_cndmask_b32 v49, v49, v31, s[58:59]\nv_cndmask_b32 v50, v50, v31, s[60:61]\n"
"v_cndmask_b32 v51, v51, v31, s[62:63]\nv_cndmask_b32 v52, v52, v31, s[64:65]\nv_cndmask_b32 v53, v53, v31, s[66:67]\nv_cndmask_b32 v54, v54, v31, s[68:69]\n"
"v_cndmask_b32 v55, v55, v31, s[70:71]\nv_max3_f32 v28, v28, v40, v41\nv_max3_f32 v28, v28, v42, v43\nv_max3_f32 v28, v28, v44, v45\n"
"v_max3_f32 v28, v28, v46, v47\nv_max3_f32 v28, v28, v48, v49\nv_max3_f32 v28, v28, v50, v51\nv_max3_f32 v28, v28, v52, v53\n"
"v_max3_f32 v28, v28, v54, v55\nv_mov_b32 v30, v28\ns_nop 1\nv_permlane32_swap_b32 v28, v30\n"
"v_max_f32 v28, v28, v30\nv_sub_f32 v40, v40, v28\nv_sub_f32 v41, v41, v28\nv_sub_f32 v42, v42, v28\n"
"v_sub_f32 v43, v43, v28\nv_sub_f32 v44, v44, v28\nv_sub_f32 v45, v45, v28\nv_sub_f32 v46, v46, v28\n"
"v_sub_f32 v47, v47, v28\nv_sub_f32 v48, v48, v28\nv_sub_f32 v49, v49, v28\nv_sub_f32 v50, v50, v28\n"
"v_sub_f32 v51, v51, v28\nv_sub_f32 v52, v52, v28\nv_sub_f32 v53, v53, v28\nv_sub_f32 v54, v54, v28\n"
"v_sub_f32 v55, v55, v28\nv_sub_f32 v80, v80, v28\nv_sub_f32 v81, v81, v28\nv_sub_f32 v82, v82, v28\n"
"v_sub_f32 v83, v83, v28\nv_exp_f32 v40, v40\nv_exp_f32 v41, v41\nv_exp_f32 v42, v42\n"
"v_exp_f32 v43, v43\nv_exp_f32 v44, v44\nv_exp_f32 v45, v45\nv_exp_f32 v46, v46\n"
"v_exp_f32 v47, v47\nv_exp_f32 v48, v48\nv_exp_f32 v49, v49\nv_exp_f32 v50, v50\n"
"v_exp_f32 v51, v51\nv_exp_f32 v52, v52\nv_exp_f32 v53, v53\nv_exp_f32 v54, v54\n"
"v_exp_f32 v55, v55\nv_exp_f32 v80, v80\nv_exp_f32 v81, v81\nv_exp_f32 v82, v82\n"
"v_exp_f32 v83, v83\nv_add_f32 v29, v40, v41\nv_add_f32 v30, v42, v43\nv_add_f32 v29, v29, v44\n"
"v_add_f32 v30, v30, v45\nv_add_f32 v29, v29, v46\nv_add_f32 v30, v30, v47\nv_add_f32 v29, v29, v48\n"
"v_add_f32 v30, v30, v49\nv_add_f32 v29, v29, v50\nv_add_f32 v30, v30, v51\nv_add_f32 v29, v29, v52\n"
"v_add_f32 v30, v30, v53\nv_add_f32 v29, v29, v54\nv_add_f32 v30, v30, v55\nv_add_f32 v29, v29, v80\n"
"v_add_f32 v30, v30, v81\nv_add_f32 v29, v29, v82\nv_add_f32 v30, v30, v83\nv_add_f32 v29, v29, v30\n"
"v_mov_b32 v30, v29\ns_nop 1\nv_permlane32_swap_b32 v29, v30\nv_add_f32 v29, v29, v30\n"
"v_rcp_f32 v29, v29\ns_nop 0\nv_mul_f32 v40, v40, v29\nv_mul_f32 v41, v41, v29\n"
"v_mul_f32 v42, v42, v29\nv_mul_f32 v43, v43, v29\nv_mul_f32 v44, v44, v29\nv_mul_f32 v45, v45, v29\n"
"v_mul_f32 v46, v46, v29\nv_mul_f32 v47, v47, v29\nv_mul_f32 v48, v48, v29\nv_mul_f32 v49, v49, v29\n"
"v_mul_f32 v50, v50, v29\nv_mul_f32 v51, v51, v29\nv_mul_f32 v52, v52, v29\nv_mul_f32 v53, v53, v29\n"
"v_mul_f32 v54, v54, v29\nv_mul_f32 v55, v55, v29\nv_mul_f32 v80, v80, v29\nv_mul_f32 v81, v81, v29\n"
"v_mul_f32 v82, v82, v29\nv_mul_f32 v83, v83, v29\nv_cvt_pk_bf16_f32 v56, v40, v41\nv_cvt_pk_bf16_f32 v57, v42, v43\n"
"v_cvt_pk_bf16_f32 v58, v44, v45\nv_cvt_pk_bf16_f32 v59, v46, v47\nv_cvt_pk_bf16_f32 v60, v48, v49\nv_cvt_pk_bf16_f32 v61, v50, v51\n"
"v_cvt_pk_bf16_f32 v62, v52, v53\nv_cvt_pk_bf16_f32 v63, v54, v55\nv_cvt_pk_bf16_f32 v33, v80, v81\nv_cvt_pk_bf16_f32 v37, v82, v83\n"
"v_xor_b32 v30, 2048, v14\nds_write_b128 v30, v[56:59]\nds_write_b128 v30, v[60:63] offset:1024\ns_waitcnt vmcnt(59)\n"
"v_mfma_f32_32x32x16_bf16 v[40:55], a[0:3], v[128:131], 0\ns_xor_b32 s73, s18, 3\ns_lshl_b32 s73, s73, 18\ns_add_u32 s73, s73, s13\n"
"buffer_load_dwordx4 v[112:115], v9, s[8:11], s73 offen sc1\ns_add_u32 s73, s73, 65536\nbuffer_load_dwordx4 v[116:119], v9, s[8:11], s73 offen sc1\ns_add_u32 s73, s73, 65536\n"
"buffer_load_dwordx4 v[120:123], v9, s[8:11], s73 offen sc1\ns_add_u32 s73, s73, 65536\nbuffer_load_dwordx4 v[124:127], v9, s[8:11], s73 offen sc1\nbuffer_load_dwordx4 a[0:3], v10, s[8:11], s76 offen offset:0 sc1\n"
"s_waitcnt vmcnt(63)\nv_mfma_f32_32x32x16_bf16 v[40:55], a[4:7], v[132:135], v[40:55]\nbuffer_load_dwordx4 a[4:7], v10, s[8:11], s76 offen offset:512 sc1\ns_waitcnt vmcnt(63)\n"
"v_mfma_f32_32x32x16_bf16 v[40:55], a[8:11], v[136:139], v[40:55]\nbuffer_load_dwordx4 a[8:11], v10, s[8:11], s76 offen offset:1024 sc1\ns_waitcnt vmcnt(63)\nv_mfma_f32_32x32x16_bf16 v[40:55], a[12:15], v[140:143], v[40:55]\n"
"buffer_load_dwordx4 a[12:15], v10, s[8:11], s76 offen offset:1536 sc1\ns_waitcnt vmcnt(63)\nv_mfma_f32_32x32x16_bf16 v[40:55], a[16:19], v[144:147], v[40:55]\nbuffer_load_dwordx4 a[16:19], v10, s[8:11], s76 offen offset:2048 sc1\n"
"s_waitcnt vmcnt(63)\nv_mfma_f32_32x32x16_bf16 v[40:55], a[20:23], v[148:151], v[40:55]\nbuffer_load_dwordx4 a[20:23], v10, s[8:11], s76 offen offset:2560 sc1\ns_waitcnt vmcnt(63)\n"
"v_mfma_f32_32x32x16_bf16 v[40:55], a[24:27], v[152:155], v[40:55]\nbuffer_load_dwordx4 a[24:27], v10, s[8:11], s76 offen offset:3072 sc1\ns_waitcnt vmcnt(63)\nv_mfma_f32_32x32x16_bf16 v[40:55], a[28:31], v[156:159], v[40:55]\n"
"buffer_load_dwordx4 a[28:31], v10, s[8:11], s76 offen offset:3584 sc1\ns_waitcnt vmcnt(63)\nv_mfma_f32_32x32x16_bf16 v[40:55], a[32:35], v[160:163], v[40:55]\nbuffer_load_dwordx4 a[32:35], v10, s[8:11], s77 offen offset:0 sc1\n"
"s_waitcnt vmcnt(63)\nv_mfma_f32_32x32x16_bf16 v[40:55], a[36:39], v[164:167], v[40:55]\nbuffer_load_dwordx4 a[36:39], v10, s[8:11], s77 offen offset:512 sc1\ns_waitcnt vmcnt(63)\n"
"v_mfma_f32_32x32x16_bf16 v[40:55], a[40:43], v[168:171], v[40:55]\nbuffer_load_dwordx4 a[40:43], v10, s[8:11], s77 offen offset:1024 sc1\ns_waitcnt vmcnt(63)\nv_mfma_f32_32x32x16_bf16 v[40:55], a[44:47], v[172:175], v[40:55]\n"
"buffer_load_dwordx4 a[44:47], v10, s[8:11], s77 offen offset:1536 sc1\ns_waitcnt vmcnt(63)\nv_mfma_f32_32x32x16_bf16 v[40:55], a[48:51], v[176:179], v[40:55]\nbuffer_load_dwordx4 a[48:51], v10, s[8:11], s77 offen offset:2048 sc1\n"
"s_waitcnt vmcnt(63)\nv_mfma_f32_32x32x16_bf16 v[40:55], a[52:55], v[180:183], v[40:55]\nbuffer_load_dwordx4 a[52:55], v10, s[8:11], s77 offen offset:2560 sc1\ns_waitcnt vmcnt(63)\n"
"v_mfma_f32_32x32x16_bf16 v[40:55], a[56:59], v[184:187], v[40:55]\nbuffer_load_dwordx4 a[56:59], v10, s[8:11], s77 offen offset:3072 sc1\ns_waitcnt vmcnt(63)\nv_mfma_f32_32x32x16_bf16 v[40:55], a[60:63], v[188:191], v[40:55]\n"
"buffer_load_dwordx4 a[60:63], v10, s[8:11], s77 offen offset:3584 sc1\ns_waitcnt vmcnt(63)\nv_mfma_f32_32x32x16_bf16 v[40:55], a[64:67], v[192:195], v[40:55]\nbuffer_load_dwordx4 a[64:67], v10, s[8:11], s78 offen offset:0 sc1\n"
"s_waitcnt vmcnt(63)\nv_mfma_f32_32x32x16_bf16 v[40:55], a[68:71], v[196:199], v[40:55]\nbuffer_load_dwordx4 a[68:71], v10, s[8:11], s78 offen offset:512 sc1\ns_waitcnt vmcnt(63)\n"
"v_mfma_f32_32x32x16_bf16 v[40:55], a[72:75], v[200:203], v[40:55]\nbuffer_load_dwordx4 a[72:75], v10, s[8:11], s78 offen offset:1024 sc1\ns_waitcnt vmcnt(63)\nv_mfma_f32_32x32x16_bf16 v[40:55], a[76:79], v[204:207], v[40:55]\n"
"buffer_load_dwordx4 a[76:79], v10, s[8:11], s78 offen offset:1536 sc1\ns_waitcnt vmcnt(63)\nv_mfma_f32_32x32x16_bf16 v[40:55], a[80:83], v[208:211], v[40:55]\nbuffer_load_dwordx4 a[80:83], v10, s[8:11], s78 offen offset:2048 sc1\n"
"s_waitcnt vmcnt(63)\nv_mfma_f32_32x32x16_bf16 v[40:55], a[84:87], v[212:215], v[40:55]\nbuffer_load_dwordx4 a[84:87], v10, s[8:11], s78 offen offset:2560 sc1\ns_waitcnt vmcnt(63)\n"
"v_mfma_f32_32x32x16_bf16 v[40:55], a[88:91], v[216:219], v[40:55]\nbuffer_load_dwordx4 a[88:91], v10, s[8:11], s78 offen offset:3072 sc1\ns_waitcnt vmcnt(63)\nv_mfma_f32_32x32x16_bf16 v[40:55], a[92:95], v[220:223], v[40:55]\n"
"buffer_load_dwordx4 a[92:95], v10, s[8:11], s78 offen offset:3584 sc1\ns_waitcnt vmcnt(63)\nv_mfma_f32_32x32x16_bf16 v[40:55], a[96:99], v[224:227], v[40:55]\nbuffer_load_dwordx4 a[96:99], v10, s[8:11], s79 offen offset:0 sc1\n"
"s_waitcnt vmcnt(63)\nv_mfma_f32_32x32x16_bf16 v[40:55], a[100:103], v[228:231], v[40:55]\nbuffer_load_dwordx4 a[100:103], v10, s[8:11], s79 offen offset:512 sc1\ns_waitcnt vmcnt(63)\n"
"v_mfma_f32_32x32x16_bf16 v[40:55], a[104:107], v[232:235], v[40:55]\nbuffer_load_dwordx4 a[104:107], v10, s[8:11], s79 offen offset:1024 sc1\ns_waitcnt vmcnt(63)\nv_mfma_f32_32x32x16_bf16 v[40:55], a[108:111], v[236:239], v[40:55]\n"
"buffer_load_dwordx4 a[108:111], v10, s[8:11], s79 offen offset:1536 sc1\ns_waitcnt vmcnt(60)\nv_add_f32 v96, v96, v100\nv_add_f32 v104, v104, v108\n"
"v_add_f32 v97, v97, v101\nv_add_f32 v105, v105, v109\nv_add_f32 v98, v98, v102\nv_add_f32 v106, v106, v110\n"
"v_add_f32 v99, v99, v103\nv_add_f32 v107, v107, v111\nv_add_f32 v96, v96, v104\nv_add_f32 v97, v97, v105\n"
"v_add_f32 v98, v98, v106\nv_add_f32 v99, v99, v107\nv_max3_f32 v28, v96, v97, v98\nv_max_f32 v28, v28, v99\n"
"v_cndmask_b32 v40, v40, v31, s[40:41]\nv_cndmask_b32 v41, v41, v31, s[42:43]\nv_cndmask_b32 v42, v42, v31, s[44:45]\nv_cndmask_b32 v43, v43, v31, s[46:47]\n"
"v_cndmask_b32 v44, v44, v31, s[48:49]\nv_cndmask_b32 v45, v45, v31, s[50:51]\nv_cndmask_b32 v46, v46, v31, s[52:53]\nv_cndmask_b32 v47, v47, v31, s[54:55]\n"
"v_cndmask_b32 v48, v48, v31, s[56:57]\nv_cndmask_b32 v49, v49, v31, s[58:59]\nv_cndmask_b32 v50, v50, v31, s[60:61]\nv_cndmask_b32 v51, v51, v31, s[62:63]\n"
"v_cndmask_b32 v52, v52, v31, s[64:65]\nv_cndmask_b32 v53, v53, v31, s[66:67]\nv_cndmask_b32 v54, v54, v31, s[68:69]\nv_cndmask_b32 v55, v55, v31, s[70:71]\n"
"v_max3_f32 v28, v28, v40, v41\nv_max3_f32 v28, v28, v42, v43\nv_max3_f32 v28, v28, v44, v45\nv_max3_f32 v28, v28, v46, v47\n"
"v_max3_f32 v28, v28, v48, v49\nv_max3_f32 v28, v28, v50, v51\nv_max3_f32 v28, v28, v52, v53\nv_max3_f32 v28, v28, v54, v55\n"
"v_mov_b32 v30, v28\ns_nop 1\nv_permlane32_swap_b32 v28, v30\nv_max_f32 v28, v28, v30\n"
"v_sub_f32 v40, v40, v28\nv_sub_f32 v41, v41, v28\nv_sub_f32 v42, v42, v28\nv_sub_f32 v43, v43, v28\n"
"v_sub_f32 v44, v44, v28\nv_sub_f32 v45, v45, v28\nv_sub_f32 v46, v46, v28\nv_sub_f32 v47, v47, v28\n"
"v_sub_f32 v48, v48, v28\nv_sub_f32 v49, v49, v28\nv_sub_f32 v50, v50, v28\nv_sub_f32 v51, v51, v28\n"
"v_sub_f32 v52, v52, v28\nv_sub_f32 v53, v53, v28\nv_sub_f32 v54, v54, v28\nv_sub_f32 v55, v55, v28\n"
"v_sub_f32 v96, v96, v28\nv_sub_f32 v97, v97, v28\nv_sub_f32 v98, v98, v28\nv_sub_f32 v99, v99, v28\n"
"v_exp_f32 v40, v40\nv_exp_f32 v41, v41\nv_exp_f32 v42, v42\nv_exp_f32 v43, v43\n"
"v_exp_f32 v44, v44\nv_exp_f32 v45, v45\nv_exp_f32 v46, v46\nv_exp_f32 v47, v47\n"
"v_exp_f32 v48, v48\nv_exp_f32 v49, v49\nv_exp_f32 v50, v50\nv_exp_f32 v51, v51\n"
"v_exp_f32 v52, v52\nv_exp_f32 v53, v53\nv_exp_f32 v54, v54\nv_exp_f32 v55, v55\n"
"v_exp_f32 v96, v96\nv_exp_f32 v97, v97\nv_exp_f32 v98, v98\nv_exp_f32 v99, v99\n"
"v_add_f32 v29, v40, v41\nv_add_f32 v30, v42, v43\nv_add_f32 v29, v29, v44\nv_add_f32 v30, v30, v45\n"
"v_add_f32 v29, v29, v46\nv_add_f32 v30, v30, v47\nv_add_f32 v29, v29, v48\nv_add_f32 v30, v30, v49\n"
"v_add_f32 v29, v29, v50\nv_add_f32 v30, v30, v51\nv_add_f32 v29, v29, v52\nv_add_f32 v30, v30, v53\n"
"v_add_f32 v29, v29, v54\nv_add_f32 v30, v30, v55\nv_add_f32 v29, v29, v96\nv_add_f32 v30, v30, v97\n"
"v_add_f32 v29, v29, v98\nv_add_f32 v30, v30, v99\nv_add_f32 v29, v29, v30\nv_mov_b32 v30, v29\n"
"s_nop 1\nv_permlane32_swap_b32 v29, v30\nv_add_f32 v29, v29, v30\nv_rcp_f32 v29, v29\n"
"s_nop 0\nv_mul_f32 v40, v40, v29\nv_mul_f32 v41, v41, v29\nv_mul_f32 v42, v42, v29\n"
"v_mul_f32 v43, v43, v29\nv_mul_f32 v44, v44, v29\nv_mul_f32 v45, v45, v29\nv_mul_f32 v46, v46, v29\n"
"v_mul_f32 v47, v47, v29\nv_mul_f32 v48, v48, v29\nv_mul_f32 v49, v49, v29\nv_mul_f32 v50, v50, v29\n"
"v_mul_f32 v51, v51, v29\nv_mul_f32 v52, v52, v29\nv_mul_f32 v53, v53, v29\nv_mul_f32 v54, v54, v29\n"
"v_mul_f32 v55, v55, v29\nv_mul_f32 v96, v96, v29\nv_mul_f32 v97, v97, v29\nv_mul_f32 v98, v98, v29\n"
"v_mul_f32 v99, v99, v29\nv_cvt_pk_bf16_f32 v56, v40, v41\nv_cvt_pk_bf16_f32 v57, v42, v43\nv_cvt_pk_bf16_f32 v58, v44, v45\n"
"v_cvt_pk_bf16_f32 v59, v46, v47\nv_cvt_pk_bf16_f32 v60, v48, v49\nv_cvt_pk_bf16_f32 v61, v50, v51\nv_cvt_pk_bf16_f32 v62, v52, v53\n"
"v_cvt_pk_bf16_f32 v63, v54, v55\nv_cvt_pk_bf16_f32 v34, v96, v97\nv_cvt_pk_bf16_f32 v38, v98, v99\nv_xor_b32 v30, 4096, v14\n"
"ds_write_b128 v30, v[56:59]\nds_write_b128 v30, v[60:63] offset:1024\ns_waitcnt vmcnt(59)\nv_mfma_f32_32x32x16_bf16 v[40:55], a[112:115], v[128:131], 0\n"
"buffer_load_dwordx4 a[112:115], v10, s[8:11], s79 offen offset:2048 sc1\ns_waitcnt vmcnt(59)\nv_mfma_f32_32x32x16_bf16 v[40:55], a[116:119], v[132:135], v[40:55]\nbuffer_load_dwordx4 a[116:119], v10, s[8:11], s79 offen offset:2560 sc1\n"
"s_waitcnt vmcnt(59)\nv_mfma_f32_32x32x16_bf16 v[40:55], a[120:123], v[136:139], v[40:55]\nbuffer_load_dwordx4 a[120:123], v10, s[8:11], s79 offen offset:3072 sc1\ns_waitcnt vmcnt(59)\n"
"v_mfma_f32_32x32x16_bf16 v[40:55], a[124:127], v[140:143], v[40:55]\nbuffer_load_dwordx4 a[124:127], v10, s[8:11], s79 offen offset:3584 sc1\ns_waitcnt vmcnt(59)\nv_mfma_f32_32x32x16_bf16 v[40:55], a[128:131], v[144:147], v[40:55]\n"
"buffer_load_dwordx4 a[128:131], v11, s[8:11], s80 offen offset:0 sc1\ns_waitcnt vmcnt(59)\nv_mfma_f32_32x32x16_bf16 v[40:55], a[132:135], v[148:151], v[40:55]\nbuffer_load_dwordx4 a[132:135], v11, s[8:11], s80 offen offset:256 sc1\n"
"s_waitcnt vmcnt(59)\nv_mfma_f32_32x32x16_bf16 v[40:55], a[136:139], v[152:155], v[40:55]\nbuffer_load_dwordx4 a[136:139], v11, s[8:11], s80 offen offset:1024 sc1\ns_waitcnt vmcnt(59)\n"
"v_mfma_f32_32x32x16_bf16 v[40:55], a[140:143], v[156:159], v[40:55]\nbuffer_load_dwordx4 a[140:143], v11, s[8:11], s80 offen offset:1280 sc1\ns_waitcnt vmcnt(59)\nv_mfma_f32_32x32x16_bf16 v[40:55], a[144:147], v[160:163], v[40:55]\n"
"buffer_load_dwordx4 a[144:147], v11, s[8:11], s80 offen offset:2048 sc1\ns_waitcnt vmcnt(59)\nv_mfma_f32_32x32x16_bf16 v[40:55], a[148:151], v[164:167], v[40:55]\nbuffer_load_dwordx4 a[148:151], v11, s[8:11], s80 offen offset:2304 sc1\n"
"s_waitcnt vmcnt(59)\nv_mfma_f32_32x32x16_bf16 v[40:55], a[152:155], v[168:171], v[40:55]\nbuffer_load_dwordx4 a[152:155], v11, s[8:11], s80 offen offset:3072 sc1\ns_waitcnt vmcnt(59)\n"
"v_mfma_f32_32x32x16_bf16 v[40:55], a[156:159], v[172:175], v[40:55]\nbuffer_load_dwordx4 a[156:159], v11, s[8:11], s80 offen offset:3328 sc1\ns_waitcnt vmcnt(59)\nv_mfma_f32_32x32x16_bf16 v[40:55], a[160:163], v[176:179], v[40:55]\n"
"s_waitcnt vmcnt(58)\nv_mfma_f32_32x32x16_bf16 v[40:55], a[164:167], v[180:183], v[40:55]\ns_waitcnt vmcnt(57)\nv_mfma_f32_32x32x16_bf16 v[40:55], a[168:171], v[184:187], v[40:55]\n"
"s_waitcnt vmcnt(56)\nv_mfma_f32_32x32x16_bf16 v[40:55], a[172:175], v[188:191], v[40:55]\ns_waitcnt vmcnt(55)\nv_mfma_f32_32x32x16_bf16 v[40:55], a[176:179], v[192:195], v[40:55]\n"
"s_waitcnt vmcnt(54)\nv_mfma_f32_32x32x16_bf16 v[40:55], a[180:183], v[196:199], v[40:55]\ns_waitcnt vmcnt(53)\nv_mfma_f32_32x32x16_bf16 v[40:55], a[184:187], v[200:203], v[40:55]\n"
"s_waitcnt vmcnt(52)\nv_mfma_f32_32x32x16_bf16 v[40:55], a[188:191], v[204:207], v[40:55]\ns_waitcnt vmcnt(51)\nv_mfma_f32_32x32x16_bf16 v[40:55], a[192:195], v[208:211], v[40:55]\n"
"s_waitcnt vmcnt(50)\nv_mfma_f32_32x32x16_bf16 v[40:55], a[196:199], v[212:215], v[40:55]\ns_waitcnt vmcnt(49)\nv_mfma_f32_32x32x16_bf16 v[40:55], a[200:203], v[216:219], v[40:55]\n"
"s_waitcnt vmcnt(48)\nv_mfma_f32_32x32x16_bf16 v[40:55], a[204:207], v[220:223], v[40:55]\ns_waitcnt vmcnt(47)\nv_mfma_f32_32x32x16_bf16 v[40:55], a[208:211], v[224:227], v[40:55]\n"
"s_waitcnt vmcnt(46)\nv_mfma_f32_32x32x16_bf16 v[40:55], a[212:215], v[228:231], v[40:55]\ns_waitcnt vmcnt(45)\nv_mfma_f32_32x32x16_bf16 v[40:55], a[216:219], v[232:235], v[40:55]\n"
"s_waitcnt vmcnt(44)\nv_mfma_f32_32x32x16_bf16 v[40:55], a[220:223], v[236:239], v[40:55]\ns_waitcnt vmcnt(40)\nv_add_f32 v112, v112, v116\n"
"v_add_f32 v120, v120, v124\nv_add_f32 v113, v113, v117\nv_add_f32 v121, v121, v125\nv_add_f32 v114, v114, v118\n"
"v_add_f32 v122, v122, v126\nv_add_f32 v115, v115, v119\nv_add_f32 v123, v123, v127\nv_add_f32 v112, v112, v120\n"
"v_add_f32 v113, v113, v121\nv_add_f32 v114, v114, v122\nv_add_f32 v115, v115, v123\nv_max3_f32 v28, v112, v113, v114\n"
"v_max_f32 v28, v28, v115\nv_cndmask_b32 v40, v40, v31, s[40:41]\nv_cndmask_b32 v41, v41, v31, s[42:43]\nv_cndmask_b32 v42, v42, v31, s[44:45]\n"
"v_cndmask_b32 v43, v43, v31, s[46:47]\nv_cndmask_b32 v44, v44, v31, s[48:49]\nv_cndmask_b32 v45, v45, v31, s[50:51]\nv_cndmask_b32 v46, v46, v31, s[52:53]\n"
"v_cndmask_b32 v47, v47, v31, s[54:55]\nv_cndmask_b32 v48, v48, v31, s[56:57]\nv_cndmask_b32 v49, v49, v31, s[58:59]\nv_cndmask_b32 v50, v50, v31, s[60:61]\n"
"v_cndmask_b32 v51, v51, v31, s[62:63]\nv_cndmask_b32 v52, v52, v31, s[64:65]\nv_cndmask_b32 v53, v53, v31, s[66:67]\nv_cndmask_b32 v54, v54, v31, s[68:69]\n"
"v_cndmask_b32 v55, v55, v31, s[70:71]\nv_max3_f32 v28, v28, v40, v41\nv_max3_f32 v28, v28, v42, v43\nv_max3_f32 v28, v28, v44, v45\n"
"v_max3_f32 v28, v28, v46, v47\nv_max3_f32 v28, v28, v48, v49\nv_max3_f32 v28, v28, v50, v51\nv_max3_f32 v28, v28, v52, v53\n"
"v_max3_f32 v28, v28, v54, v55\nv_mov_b32 v30, v28\ns_nop 1\nv_permlane32_swap_b32 v28, v30\n"
"v_max_f32 v28, v28, v30\nv_sub_f32 v40, v40, v28\nv_sub_f32 v41, v41, v28\nv_sub_f32 v42, v42, v28\n"
"v_sub_f32 v43, v43, v28\nv_sub_f32 v44, v44, v28\nv_sub_f32 v45, v45, v28\nv_sub_f32 v46, v46, v28\n"
"v_sub_f32 v47, v47, v28\nv_sub_f32 v48, v48, v28\nv_sub_f32 v49, v49, v28\nv_sub_f32 v50, v50, v28\n"
"v_sub_f32 v51, v51, v28\nv_sub_f32 v52, v52, v28\nv_sub_f32 v53, v53, v28\nv_sub_f32 v54, v54, v28\n"
"v_sub_f32 v55, v55, v28\nv_sub_f32 v112, v112, v28\nv_sub_f32 v113, v113, v28\nv_sub_f32 v114, v114, v28\n"
"v_sub_f32 v115, v115, v28\nv_exp_f32 v40, v40\nv_exp_f32 v41, v41\nv_exp_f32 v42, v42\n"
"v_exp_f32 v43, v43\nv_exp_f32 v44, v44\nv_exp_f32 v45, v45\nv_exp_f32 v46, v46\n"
"v_exp_f32 v47, v47\nv_exp_f32 v48, v48\nv_exp_f32 v49, v49\nv_exp_f32 v50, v50\n"
"v_exp_f32 v51, v51\nv_exp_f32 v52, v52\nv_exp_f32 v53, v53\nv_exp_f32 v54, v54\n"
"v_exp_f32 v55, v55\nv_exp_f32 v112, v112\nv_exp_f32 v113, v113\nv_exp_f32 v114, v114\n"
"v_exp_f32 v115, v115\nv_add_f32 v29, v40, v41\nv_add_f32 v30, v42, v43\nv_add_f32 v29, v29, v44\n"
"v_add_f32 v30, v30, v45\nv_add_f32 v29, v29, v46\nv_add_f32 v30, v30, v47\nv_add_f32 v29, v29, v48\n"
"v_add_f32 v30, v30, v49\nv_add_f32 v29, v29, v50\nv_add_f32 v30, v30, v51\nv_add_f32 v29, v29, v52\n"
"v_add_f32 v30, v30, v53\nv_add_f32 v29, v29, v54\nv_add_f32 v30, v30, v55\nv_add_f32 v29, v29, v112\n"
"v_add_f32 v30, v30, v113\nv_add_f32 v29, v29, v114\nv_add_f32 v30, v30, v115\nv_add_f32 v29, v29, v30\n"
"v_mov_b32 v30, v29\ns_nop 1\nv_permlane32_swap_b32 v29, v30\nv_add_f32 v29, v29, v30\n"
"v_rcp_f32 v29, v29\ns_nop 0\nv_mul_f32 v40, v40, v29\nv_mul_f32 v41, v41, v29\n"
"v_mul_f32 v42, v42, v29\nv_mul_f32 v43, v43, v29\nv_mul_f32 v44, v44, v29\nv_mul_f32 v45, v45, v29\n"
"v_mul_f32 v46, v46, v29\nv_mul_f32 v47, v47, v29\nv_mul_f32 v48, v48, v29\nv_mul_f32 v49, v49, v29\n"
"v_mul_f32 v50, v50, v29\nv_mul_f32 v51, v51, v29\nv_mul_f32 v52, v52, v29\nv_mul_f32 v53, v53, v29\n"
"v_mul_f32 v54, v54, v29\nv_mul_f32 v55, v55, v29\nv_mul_f32 v112, v112, v29\nv_mul_f32 v113, v113, v29\n"
"v_mul_f32 v114, v114, v29\nv_mul_f32 v115, v115, v29\nv_cvt_pk_bf16_f32 v56, v40, v41\nv_cvt_pk_bf16_f32 v57, v42, v43\n"
"v_cvt_pk_bf16_f32 v58, v44, v45\nv_cvt_pk_bf16_f32 v59, v46, v47\nv_cvt_pk_bf16_f32 v60, v48, v49\nv_cvt_pk_bf16_f32 v61, v50, v51\n"
"v_cvt_pk_bf16_f32 v62, v52, v53\nv_cvt_pk_bf16_f32 v63, v54, v55\nv_cvt_pk_bf16_f32 v35, v112, v113\nv_cvt_pk_bf16_f32 v39, v114, v115\n"
"v_xor_b32 v30, 6144, v14\nds_write_b128 v30, v[56:59]\nds_write_b128 v30, v[60:63] offset:1024\nds_write_b128 v15, v[32:35]\n"
"ds_write_b128 v15, v[36:39] offset:1024\ns_waitcnt lgkmcnt(0)\ns_barrier\nds_read_b128 v[96:99], v16 offset:0\n"
"ds_read_b128 v[100:103], v16 offset:1024\nds_read_b128 v[104:107], v16 offset:2048\nds_read_b128 v[108:111], v16 offset:3072\nds_read_b128 v[112:115], v16 offset:4096\n"
"ds_read_b128 v[116:119], v16 offset:5120\nds_read_b128 v[120:123], v16 offset:6144\nds_read_b128 v[124:127], v16 offset:7168\nds_read_b128 v[128:131], v16 offset:8192\n"
"ds_read_b128 v[132:135], v16 offset:9216\nds_read_b128 v[136:139], v16 offset:10240\nds_read_b128 v[140:143], v16 offset:11264\ns_waitcnt lgkmcnt(0)\n"
"ds_read_b128 v[144:147], v16 offset:12288\nds_read_b128 v[148:151], v16 offset:13312\nds_read_b128 v[152:155], v16 offset:14336\nds_read_b128 v[156:159], v16 offset:15360\n"
"ds_read_b128 v[160:163], v16 offset:16384\nds_read_b128 v[164:167], v16 offset:17408\nds_read_b128 v[168:171], v16 offset:18432\nds_read_b128 v[172:175], v16 offset:19456\n"
"ds_read_b128 v[176:179], v16 offset:20480\nds_read_b128 v[180:183], v16 offset:21504\nds_read_b128 v[184:187], v16 offset:22528\nds_read_b128 v[188:191], v16 offset:23552\n"
"s_waitcnt lgkmcnt(0)\nds_read_b128 v[192:195], v16 offset:24576\nds_read_b128 v[196:199], v16 offset:25600\nds_read_b128 v[200:203], v16 offset:26624\n"
"ds_read_b128 v[204:207], v16 offset:27648\nds_read_b128 v[208:211], v16 offset:28672\nds_read_b128 v[212:215], v16 offset:29696\nds_read_b128 v[216:219], v16 offset:30720\n"
"ds_read_b128 v[220:223], v16 offset:31744\nds_read_b128 v[224:227], v16 offset:32768\nds_read_b128 v[228:231], v16 offset:33792\nds_read_b128 v[232:235], v16 offset:34816\n"
"ds_read_b128 v[236:239], v16 offset:35840\ns_waitcnt lgkmcnt(0)\nds_read_b128 v[240:243], v16 offset:36864\nds_read_b128 v[244:247], v16 offset:37888\n"
"ds_read_b128 v[248:251], v16 offset:38912\nds_read_b128 v[252:255], v16 offset:39936\nv_mov_b32 v24, v17\ns_sub_u32 s72, s17, 1\n"
"s_waitcnt lgkmcnt(0)\nfo_loop:\ns_add_u32 s76, s76, 8388608\ns_add_u32 s77, s77, 8388608\n"
"s_add_u32 s78, s78, 8388608\ns_add_u32 s79, s79, 8388608\ns_add_u32 s80, s80, 2097152\ns_waitcnt vmcnt(39)\n"
"v_mfma_f32_32x32x16_bf16 v[40:55], a[0:3], v[96:99], 0\nbuffer_load_dwordx4 a[0:3], v10, s[8:11], s76 offen offset:0 sc1\ns_waitcnt vmcnt(39)\nv_mfma_f32_32x32x16_bf16 v[40:55], a[4:7], v[100:103], v[40:55]\n"
"buffer_load_dwordx4 a[4:7], v10, s[8:11], s76 offen offset:512 sc1\ns_waitcnt vmcnt(39)\nv_mfma_f32_32x32x16_bf16 v[40:55], a[8:11], v[104:107], v[40:55]\nbuffer_load_dwordx4 a[8:11], v10, s[8:11], s76 offen offset:1024 sc1\n"
"s_waitcnt vmcnt(39)\nv_mfma_f32_32x32x16_bf16 v[40:55], a[12:15], v[108:111], v[40:55]\nbuffer_load_dwordx4 a[12:15], v10, s[8:11], s76 offen offset:1536 sc1\ns_waitcnt vmcnt(39)\n"
"v_mfma_f32_32x32x16_bf16 v[40:55], a[16:19], v[112:115], v[40:55]\nbuffer_load_dwordx4 a[16:19], v10, s[8:11], s76 offen offset:2048 sc1\ns_waitcnt vmcnt(39)\nv_mfma_f32_32x32x16_bf16 v[40:55], a[20:23], v[116:119], v[40:55]\n"
"buffer_load_dwordx4 a[20:23], v10, s[8:11], s76 offen offset:2560 sc1\ns_waitcnt vmcnt(39)\nv_mfma_f32_32x32x16_bf16 v[40:55], a[24:27], v[120:123], v[40:55]\nbuffer_load_dwordx4 a[24:27], v10, s[8:11], s76 offen offset:3072 sc1\n"
"s_waitcnt vmcnt(39)\nv_mfma_f32_32x32x16_bf16 v[40:55], a[28:31], v[124:127], v[40:55]\nbuffer_load_dwordx4 a[28:31], v10, s[8:11], s76 offen offset:3584 sc1\ns_waitcnt vmcnt(39)\n"
"v_mfma_f32_32x32x16_bf16 v[40:55], a[32:35], v[128:131], v[40:55]\nbuffer_load_dwordx4 a[32:35], v10, s[8:11], s77 offen offset:0 sc1\ns_waitcnt vmcnt(39)\nv_mfma_f32_32x32x16_bf16 v[40:55], a[36:39], v[132:135], v[40:55]\n"
"buffer_load_dwordx4 a[36:39], v10, s[8:11], s77 offen offset:512 sc1\ns_waitcnt vmcnt(39)\nv_mfma_f32_32x32x16_bf16 v[40:55], a[40:43], v[136:139], v[40:55]\nbuffer_load_dwordx4 a[40:43], v10, s[8:11], s77 offen offset:1024 sc1\n"
"s_waitcnt vmcnt(39)\nv_mfma_f32_32x32x16_bf16 v[40:55], a[44:47], v[140:143], v[40:55]\nbuffer_load_dwordx4 a[44:47], v10, s[8:11], s77 offen offset:1536 sc1\ns_waitcnt vmcnt(39)\n"
"v_mfma_f32_32x32x16_bf16 v[40:55], a[48:51], v[144:147], v[40:55]\nbuffer_load_dwordx4 a[48:51], v10, s[8:11], s77 offen offset:2048 sc1\ns_waitcnt vmcnt(39)\nv_mfma_f32_32x32x16_bf16 v[40:55], a[52:55], v[148:151], v[40:55]\n"
"buffer_load_dwordx4 a[52:55], v10, s[8:11], s77 offen offset:2560 sc1\ns_waitcnt vmcnt(39)\nv_mfma_f32_32x32x16_bf16 v[40:55], a[56:59], v[152:155], v[40:55]\nbuffer_load_dwordx4 a[56:59], v10, s[8:11], s77 offen offset:3072 sc1\n"
"s_waitcnt vmcnt(39)\nv_mfma_f32_32x32x16_bf16 v[40:55], a[60:63], v[156:159], v[40:55]\nbuffer_load_dwordx4 a[60:63], v10, s[8:11], s77 offen offset:3584 sc1\ns_waitcnt vmcnt(39)\n"
"v_mfma_f32_32x32x16_bf16 v[40:55], a[64:67], v[160:163], v[40:55]\nbuffer_load_dwordx4 a[64:67], v10, s[8:11], s78 offen offset:0 sc1\ns_waitcnt vmcnt(39)\nv_mfma_f32_32x32x16_bf16 v[40:55], a[68:71], v[164:167], v[40:55]\n"
"buffer_load_dwordx4 a[68:71], v10, s[8:11], s78 offen offset:512 sc1\ns_waitcnt vmcnt(39)\nv_mfma_f32_32x32x16_bf16 v[40:55], a[72:75], v[168:171], v[40:55]\nbuffer_load_dwordx4 a[72:75], v10, s[8:11], s78 offen offset:1024 sc1\n"
"s_waitcnt vmcnt(39)\nv_mfma_f32_32x32x16_bf16 v[40:55], a[76:79], v[172:175], v[40:55]\nbuffer_load_dwordx4 a[76:79], v10, s[8:11], s78 offen offset:1536 sc1\ns_waitcnt vmcnt(39)\n"
"v_mfma_f32_32x32x16_bf16 v[40:55], a[80:83], v[176:179], v[40:55]\nbuffer_load_dwordx4 a[80:83], v10, s[8:11], s78 offen offset:2048 sc1\ns_waitcnt vmcnt(39)\nv_mfma_f32_32x32x16_bf16 v[40:55], a[84:87], v[180:183], v[40:55]\n"
"buffer_load_dwordx4 a[84:87], v10, s[8:11], s78 offen offset:2560 sc1\ns_waitcnt vmcnt(39)\nv_mfma_f32_32x32x16_bf16 v[40:55], a[88:91], v[184:187], v[40:55]\nbuffer_load_dwordx4 a[88:91], v10, s[8:11], s78 offen offset:3072 sc1\n"
"s_waitcnt vmcnt(39)\nv_mfma_f32_32x32x16_bf16 v[40:55], a[92:95], v[188:191], v[40:55]\nbuffer_load_dwordx4 a[92:95], v10, s[8:11], s78 offen offset:3584 sc1\ns_waitcnt vmcnt(39)\n"
"v_mfma_f32_32x32x16_bf16 v[40:55], a[96:99], v[192:195], v[40:55]\nbuffer_load_dwordx4 a[96:99], v10, s[8:11], s79 offen offset:0 sc1\ns_waitcnt vmcnt(39)\nv_mfma_f32_32x32x16_bf16 v[40:55], a[100:103], v[196:199], v[40:55]\n"
"buffer_load_dwordx4 a[100:103], v10, s[8:11], s79 offen offset:512 sc1\ns_waitcnt vmcnt(39)\nv_mfma_f32_32x32x16_bf16 v[40:55], a[104:107], v[200:203], v[40:55]\nbuffer_load_dwordx4 a[104:107], v10, s[8:11], s79 offen offset:1024 sc1\n"
"s_waitcnt vmcnt(39)\nv_mfma_f32_32x32x16_bf16 v[40:55], a[108:111], v[204:207], v[40:55]\nbuffer_load_dwordx4 a[108:111], v10, s[8:11], s79 offen offset:1536 sc1\ns_waitcnt vmcnt(39)\n"
"v_mfma_f32_32x32x16_bf16 v[40:55], a[112:115], v[208:211], v[40:55]\nbuffer_load_dwordx4 a[112:115], v10, s[8:11], s79 offen offset:2048 sc1\ns_waitcnt vmcnt(39)\nv_mfma_f32_32x32x16_bf16 v[40:55], a[116:119], v[212:215], v[40:55]\n"
"buffer_load_dwordx4 a[116:119], v10, s[8:11], s79 offen offset:2560 sc1\ns_waitcnt vmcnt(39)\nv_mfma_f32_32x32x16_bf16 v[40:55], a[120:123], v[216:219], v[40:55]\nbuffer_load_dwordx4 a[120:123], v10, s[8:11], s79 offen offset:3072 sc1\n"
"s_waitcnt vmcnt(39)\nv_mfma_f32_32x32x16_bf16 v[40:55], a[124:127], v[220:223], v[40:55]\nbuffer_load_dwordx4 a[124:127], v10, s[8:11], s79 offen offset:3584 sc1\ns_waitcnt vmcnt(39)\n"
"v_mfma_f32_32x32x16_bf16 v[40:55], a[128:131], v[224:227], v[40:55]\nbuffer_load_dwordx4 a[128:131], v11, s[8:11], s80 offen offset:0 sc1\ns_waitcnt vmcnt(39)\nv_mfma_f32_32x32x16_bf16 v[40:55], a[132:135], v[228:231], v[40:55]\n"
"buffer_load_dwordx4 a[132:135], v11, s[8:11], s80 offen offset:256 sc1\ns_waitcnt vmcnt(39)\nv_mfma_f32_32x32x16_bf16 v[40:55], a[136:139], v[232:235], v[40:55]\nbuffer_load_dwordx4 a[136:139], v11, s[8:11], s80 offen offset:1024 sc1\n"
"s_waitcnt vmcnt(39)\nv_mfma_f32_32x32x16_bf16 v[40:55], a[140:143], v[236:239], v[40:55]\nbuffer_load_dwordx4 a[140:143], v11, s[8:11], s80 offen offset:1280 sc1\ns_waitcnt vmcnt(39)\n"
"v_mfma_f32_32x32x16_bf16 v[40:55], a[144:147], v[240:243], v[40:55]\nbuffer_load_dwordx4 a[144:147], v11, s[8:11], s80 offen offset:2048 sc1\ns_waitcnt vmcnt(39)\nv_mfma_f32_32x32x16_bf16 v[40:55], a[148:151], v[244:247], v[40:55]\n"
"buffer_load_dwordx4 a[148:151], v11, s[8:11], s80 offen offset:2304 sc1\ns_waitcnt vmcnt(39)\nv_mfma_f32_32x32x16_bf16 v[40:55], a[152:155], v[248:251], v[40:55]\nbuffer_load_dwordx4 a[152:155], v11, s[8:11], s80 offen offset:3072 sc1\n"
"s_waitcnt vmcnt(39)\nv_mfma_f32_32x32x16_bf16 v[40:55], a[156:159], v[252:255], v[40:55]\nbuffer_load_dwordx4 a[156:159], v11, s[8:11], s80 offen offset:3328 sc1\ns_nop 11\n"
"v_cvt_pk_bf16_f32 v56, v40, v41\nv_cvt_pk_bf16_f32 v57, v42, v43\nv_cvt_pk_bf16_f32 v58, v44, v45\nv_cvt_pk_bf16_f32 v59, v46, v47\n"
"v_cvt_pk_bf16_f32 v60, v48, v49\nv_cvt_pk_bf16_f32 v61, v50, v51\nv_cvt_pk_bf16_f32 v62, v52, v53\nv_cvt_pk_bf16_f32 v63, v54, v55\n"
"ds_write_b128 v24, v[56:59]\nds_write_b128 v24, v[60:63] offset:32\nv_add_u32 v24, 256, v24\ns_sub_u32 s72, s72, 1\n"
"s_cmp_lg_u32 s72, 0\ns_cbranch_scc1 fo_loop\ns_waitcnt vmcnt(39)\nv_mfma_f32_32x32x16_bf16 v[40:55], a[0:3], v[96:99], 0\n"
"s_waitcnt vmcnt(38)\nv_mfma_f32_32x32x16_bf16 v[40:55], a[4:7], v[100:103], v[40:55]\ns_waitcnt vmcnt(37)\nv_mfma_f32_32x32x16_bf16 v[40:55], a[8:11], v[104:107], v[40:55]\n"
"s_waitcnt vmcnt(36)\nv_mfma_f32_32x32x16_bf16 v[40:55], a[12:15], v[108:111], v[40:55]\ns_waitcnt vmcnt(35)\nv_mfma_f32_32x32x16_bf16 v[40:55], a[16:19], v[112:115], v[40:55]\n"
"s_waitcnt vmcnt(34)\nv_mfma_f32_32x32x16_bf16 v[40:55], a[20:23], v[116:119], v[40:55]\ns_waitcnt vmcnt(33)\nv_mfma_f32_32x32x16_bf16 v[40:55], a[24:27], v[120:123], v[40:55]\n"
"s_waitcnt vmcnt(32)\nv_mfma_f32_32x32x16_bf16 v[40:55], a[28:31], v[124:127], v[40:55]\ns_waitcnt vmcnt(31)\nv_mfma_f32_32x32x16_bf16 v[40:55], a[32:35], v[128:131], v[40:55]\n"
"s_waitcnt vmcnt(30)\nv_mfma_f32_32x32x16_bf16 v[40:55], a[36:39], v[132:135], v[40:55]\ns_waitcnt vmcnt(29)\nv_mfma_f32_32x32x16_bf16 v[40:55], a[40:43], v[136:139], v[40:55]\n"
"s_waitcnt vmcnt(28)\nv_mfma_f32_32x32x16_bf16 v[40:55], a[44:47], v[140:143], v[40:55]\ns_waitcnt vmcnt(27)\nv_mfma_f32_32x32x16_bf16 v[40:55], a[48:51], v[144:147], v[40:55]\n"
"s_waitcnt vmcnt(26)\nv_mfma_f32_32x32x16_bf16 v[40:55], a[52:55], v[148:151], v[40:55]\ns_waitcnt vmcnt(25)\nv_mfma_f32_32x32x16_bf16 v[40:55], a[56:59], v[152:155], v[40:55]\n"
"s_waitcnt vmcnt(24)\nv_mfma_f32_32x32x16_bf16 v[40:55], a[60:63], v[156:159], v[40:55]\ns_waitcnt vmcnt(23)\nv_mfma_f32_32x32x16_bf16 v[40:55], a[64:67], v[160:163], v[40:55]\n"
"s_waitcnt vmcnt(22)\nv_mfma_f32_32x32x16_bf16 v[40:55], a[68:71], v[164:167], v[40:55]\ns_waitcnt vmcnt(21)\nv_mfma_f32_32x32x16_bf16 v[40:55], a[72:75], v[168:171], v[40:55]\n"
"s_waitcnt vmcnt(20)\nv_mfma_f32_32x32x16_bf16 v[40:55], a[76:79], v[172:175], v[40:55]\ns_waitcnt vmcnt(19)\nv_mfma_f32_32x32x16_bf16 v[40:55], a[80:83], v[176:179], v[40:55]\n"
"s_waitcnt vmcnt(18)\nv_mfma_f32_32x32x16_bf16 v[40:55], a[84:87], v[180:183], v[40:55]\ns_waitcnt vmcnt(17)\nv_mfma_f32_32x32x16_bf16 v[40:55], a[88:91], v[184:187], v[40:55]\n"
"s_waitcnt vmcnt(16)\nv_mfma_f32_32x32x16_bf16 v[40:55], a[92:95], v[188:191], v[40:55]\ns_waitcnt vmcnt(15)\nv_mfma_f32_32x32x16_bf16 v[40:55], a[96:99], v[192:195], v[40:55]\n"
"s_waitcnt vmcnt(14)\nv_mfma_f32_32x32x16_bf16 v[40:55], a[100:103], v[196:199], v[40:55]\ns_waitcnt vmcnt(13)\nv_mfma_f32_32x32x16_bf16 v[40:55], a[104:107], v[200:203], v[40:55]\n"
"s_waitcnt vmcnt(12)\nv_mfma_f32_32x32x16_bf16 v[40:55], a[108:111], v[204:207], v[40:55]\ns_waitcnt vmcnt(11)\nv_mfma_f32_32x32x16_bf16 v[40:55], a[112:115], v[208:211], v[40:55]\n"
"s_waitcnt vmcnt(10)\nv_mfma_f32_32x32x16_bf16 v[40:55], a[116:119], v[212:215], v[40:55]\ns_waitcnt vmcnt(9)\nv_mfma_f32_32x32x16_bf16 v[40:55], a[120:123], v[216:219], v[40:55]\n"
"s_waitcnt vmcnt(8)\nv_mfma_f32_32x32x16_bf16 v[40:55], a[124:127], v[220:223], v[40:55]\ns_waitcnt vmcnt(7)\nv_mfma_f32_32x32x16_bf16 v[40:55], a[128:131], v[224:227], v[40:55]\n"
"s_waitcnt vmcnt(6)\nv_mfma_f32_32x32x16_bf16 v[40:55], a[132:135], v[228:231], v[40:55]\ns_waitcnt vmcnt(5)\nv_mfma_f32_32x32x16_bf16 v[40:55], a[136:139], v[232:235], v[40:55]\n"
"s_waitcnt vmcnt(4)\nv_mfma_f32_32x32x16_bf16 v[40:55], a[140:143], v[236:239], v[40:55]\ns_waitcnt vmcnt(3)\nv_mfma_f32_32x32x16_bf16 v[40:55], a[144:147], v[240:243], v[40:55]\n"
"s_waitcnt vmcnt(2)\nv_mfma_f32_32x32x16_bf16 v[40:55], a[148:151], v[244:247], v[40:55]\ns_waitcnt vmcnt(1)\nv_mfma_f32_32x32x16_bf16 v[40:55], a[152:155], v[248:251], v[40:55]\n"
"s_waitcnt vmcnt(0)\nv_mfma_f32_32x32x16_bf16 v[40:55], a[156:159], v[252:255], v[40:55]\ns_nop 11\nv_cvt_pk_bf16_f32 v56, v40, v41\n"
"v_cvt_pk_bf16_f32 v57, v42, v43\nv_cvt_pk_bf16_f32 v58, v44, v45\nv_cvt_pk_bf16_f32 v59, v46, v47\nv_cvt_pk_bf16_f32 v60, v48, v49\n"
"v_cvt_pk_bf16_f32 v61, v50, v51\nv_cvt_pk_bf16_f32 v62, v52, v53\nv_cvt_pk_bf16_f32 v63, v54, v55\nds_write_b128 v24, v[56:59]\n"
"ds_write_b128 v24, v[60:63] offset:32\nv_add_u32 v24, 256, v24\n"
    :: "{s[8:23]}"(s0), "{v[8:23]}"(v0) : "v24", "v25", "v26", "v27", "v28", "v29", "v30", "v31", "v32", "v33", "v34", "v35", "v36", "v37", "v38", "v39", "v40", "v41", "v42", "v43", "v44", "v45", "v46", "v47", "v48", "v49", "v50", "v51", "v52", "v53", "v54", "v55", "v56", "v57", "v58", "v59", "v60", "v61", "v62", "v63", "v64", "v65", "v66", "v67", "v68", "v69", "v70", "v71", "v72", "v73", "v74", "v75", "v76", "v77", "v78", "v79", "v80", "v81", "v82", "v83", "v84", "v85", "v86", "v87", "v88", "v89", "v90", "v91", "v92", "v93", "v94", "v95", "v96", "v97", "v98", "v99", "v100", "v101", "v102", "v103", "v104", "v105", "v106", "v107", "v108", "v109", "v110", "v111", "v112", "v113", "v114", "v115", "v116", "v117", "v118", "v119", "v120", "v121", "v122", "v123", "v124", "v125", "v126", "v127", "v128", "v129", "v130", "v131", "v132", "v133", "v134", "v135", "v136", "v137", "v138", "v139", "v140", "v141", "v142", "v143", "v144", "v145", "v146", "v147", "v148", "v149", "v150", "v151", "v152", "v153", "v154", "v155", "v156", "v157", "v158", "v159", "v160", "v161", "v162", "v163", "v164", "v165", "v166", "v167", "v168", "v169", "v170", "v171", "v172", "v173", "v174", "v175", "v176", "v177", "v178", "v179", "v180", "v181", "v182", "v183", "v184", "v185", "v186", "v187", "v188", "v189", "v190", "v191", "v192", "v193", "v194", "v195", "v196", "v197", "v198", "v199", "v200", "v201", "v202", "v203", "v204", "v205", "v206", "v207", "v208", "v209", "v210", "v211", "v212", "v213", "v214", "v215", "v216", "v217", "v218", "v219", "v220", "v221", "v222", "v223", "v224", "v225", "v226", "v227", "v228", "v229", "v230", "v231", "v232", "v233", "v234", "v235", "v236", "v237", "v238", "v239", "v240", "v241", "v242", "v243", "v244", "v245", "v246", "v247", "v248", "v249", "v250", "v251", "v252", "v253", "v254", "v255", "a0", "a1", "a2", "a3", "a4", "a5", "a6", "a7", "a8", "a9", "a10", "a11", "a12", "a13", "a14", "a15", "a16", "a17", "a18", "a19", "a20", "a21", "a22", "a23", "a24", "a25", "a26", "a27", "a28", "a29", "a30", "a31", "a32", "a33", "a34", "a35", "a36", "a37", "a38", "a39", "a40", "a41", "a42", "a43", "a44", "a45", "a46", "a47", "a48", "a49", "a50", "a51", "a52", "a53", "a54", "a55", "a56", "a57", "a58", "a59", "a60", "a61", "a62", "a63", "a64", "a65", "a66", "a67", "a68", "a69", "a70", "a71", "a72", "a73", "a74", "a75", "a76", "a77", "a78", "a79", "a80", "a81", "a82", "a83", "a84", "a85", "a86", "a87", "a88", "a89", "a90", "a91", "a92", "a93", "a94", "a95", "a96", "a97", "a98", "a99", "a100", "a101", "a102", "a103", "a104", "a105", "a106", "a107", "a108", "a109", "a110", "a111", "a112", "a113", "a114", "a115", "a116", "a117", "a118", "a119", "a120", "a121", "a122", "a123", "a124", "a125", "a126", "a127", "a128", "a129", "a130", "a131", "a132", "a133", "a134", "a135", "a136", "a137", "a138", "a139", "a140", "a141", "a142", "a143", "a144", "a145", "a146", "a147", "a148", "a149", "a150", "a151", "a152", "a153", "a154", "a155", "a156", "a157", "a158", "a159", "a160", "a161", "a162", "a163", "a164", "a165", "a166", "a167", "a168", "a169", "a170", "a171", "a172", "a173", "a174", "a175", "a176", "a177", "a178", "a179", "a180", "a181", "a182", "a183", "a184", "a185", "a186", "a187", "a188", "a189", "a190", "a191", "a192", "a193", "a194", "a195", "a196", "a197", "a198", "a199", "a200", "a201", "a202", "a203", "a204", "a205", "a206", "a207", "a208", "a209", "a210", "a211", "a212", "a213", "a214", "a215", "a216", "a217", "a218", "a219", "a220", "a221", "a222", "a223", "s40", "s41", "s42", "s43", "s44", "s45", "s46", "s47", "s48", "s49", "s50", "s51", "s52", "s53", "s54", "s55", "s56", "s57", "s58", "s59", "s60", "s61", "s62", "s63", "s64", "s65", "s66", "s67", "s68", "s69", "s70", "s71", "s72", "s73", "s74", "s75", "s76", "s77", "s78", "s79", "s80", "s81", "vcc", "scc", "memory");
}
DI u4 pack8(const f16v& x, int s, float sc) { return (u4){pk2(x[8 * s] * sc, x[8 * s + 1] * sc), pk2(x[8 * s + 2] * sc, x[8 * s + 3] * sc), pk2(x[8 * s + 4] * sc, x[8 * s + 5] * sc), pk2(x[8 * s + 6] * sc, x[8 * s + 7] * sc)}; }
DI u4 pack8(const f16v& x, int s) { return (u4){pk2(x[8 * s], x[8 * s + 1]), pk2(x[8 * s + 2], x[8 * s + 3]), pk2(x[8 * s + 4], x[8 * s + 5]), pk2(x[8 * s + 6], x[8 * s + 7])}; }
DI void dma_pair(u4 srd, int v0, int v1, int soff, unsigned m) {
    asm volatile("s_mov_b32 m0, %4\n\ts_nop 0\n\tbuffer_load_dwordx4 %0, %2, %3 offen lds\n\tbuffer_load_dwordx4 %1, %2, %3 offen offset:1024 lds" :: "v"(v0), "v"(v1), "s"(srd), "s"(soff), "s"(m) : "memory");
}
DI void dma_one(u4 srd, int v0, int soff, unsigned m) {
    asm volatile("s_mov_b32 m0, %3\n\ts_nop 0\n\tbuffer_load_dwordx4 %0, %1, %2 offen lds" :: "v"(v0), "s"(srd), "s"(soff), "s"(m) : "memory");
}
DI void mV_va(f16v& c, u4 x, u4 y) { asm volatile("v_mfma_f32_32x32x16_bf16 %0, %1, %2, %0" : "+v"(c) : "v"(x), "a"(y)); }
DI void mV_va0(f16v& c, u4 x, u4 y) { asm volatile("v_mfma_f32_32x32x16_bf16 %0, %1, %2, 0" : "=&v"(c) : "v"(x), "a"(y)); }
DI void mV_av(f16v& c, u4 x, u4 y) { asm volatile("v_mfma_f32_32x32x16_bf16 %0, %1, %2, %0" : "+v"(c) : "a"(x), "v"(y)); }
DI void mV_av0(f16v& c, u4 x, u4 y) { asm volatile("v_mfma_f32_32x32x16_bf16 %0, %1, %2, 0" : "=&v"(c) : "a"(x), "v"(y)); }
DI void mV_vv(f16v& c, u4 x, u4 y) { asm volatile("v_mfma_f32_32x32x16_bf16 %0, %1, %2, %0" : "+v"(c) : "v"(x), "v"(y)); }
DI void mV_vv0(f16v& c, u4 x, u4 y) { asm volatile("v_mfma_f32_32x32x16_bf16 %0, %1, %2, 0" : "=&v"(c) : "v"(x), "v"(y)); }
template <int N, int PD, class RD, class MM, class EX> DI void pipe(RD rd, MM mm, EX ex) {
    u4 f[PD];
#pragma unroll
    for (int j = 0; j < PD; ++j) f[j] = rd(j);
    SB();
#pragma unroll
    for (int j = 0; j < N; ++j) { mm(j, f[j % PD]); if (j + PD < N) f[j % PD] = rd(j + PD); ex(j); SB(); }
}
DI int swzr(int row) { return ((row >> 2) & 1) | ((row & 3) << 1) | (row & 8); }
DI int swap23(int x) { return (x & ~12) | ((x & 4) << 1) | ((x & 8) >> 1); }
#define SLICE 32768
#define MFW32(x, y) asm volatile("s_nop 11" : "+v"(x), "+v"(y))
struct SideCtx {
    char LAS* sm; unsigned smb; int lane, wave;
    int vr0, vr1, vc0, vc1;
};
DI SideCtx make_ctx(int wave) {
    SideCtx c; c.sm = (char LAS*)smem_; c.smb = (unsigned)(size_t)c.sm; c.lane = fresh_lane(); c.wave = wave;
    asm volatile("" : "+s"(c.wave), "+s"(c.smb));
    const int l = c.lane;
    { int row = 4 * c.wave + (l >> 5); c.vr0 = row * 8192 + (((l & 31) ^ swzr(row)) * 16); row += 2; c.vr1 = row * 8192 + (((l & 31) ^ swzr(row)) * 16) - 1024; }
    { int row = l >> 3; c.vc0 = row * 896 + (((l & 7) ^ (((row >> 1) & 1) << 2)) * 16); c.vc1 = c.vc0 + 8 * 896 - 1024; }
    return c;
}
DI void dma_rows_q(const SideCtx& c, u4 srd, int h, int r0, int slot, int g) { dma_pair(srd, c.vr0, c.vr1, (r0 + 16 * g) * 8192 + h * 512, c.smb + slot * SLICE + (8 * g + 2 * c.wave) * 1024); }
DI void dma_cols_q(const SideCtx& c, u4 srd, int h, int c0, int slot, int g) { const int k = 8 * c.wave + 2 * g; dma_pair(srd, c.vc0, c.vc1, (h * 256 + 8 * k) * 896 + c0 * 2, c.smb + slot * SLICE + k * 1024); }
DI void dma_rows(const SideCtx& c, u4 srd, int h, int r0, int slot) {
#pragma unroll
    for (int g = 0; g < 4; ++g) dma_rows_q(c, srd, h, r0, slot, g);
}
DI void dma_cols(const SideCtx& c, u4 srd, int h, int c0, int slot) {
#pragma unroll
    for (int g = 0; g < 4; ++g) dma_cols_q(c, srd, h, c0, slot, g);
}
DI void set_flag(const SideCtx& c, u4 rhb, int off, unsigned tag) {
    waitvm<0>(); bar();
    if (c.wave == 0) sst32(tag, rhb, c.lane == 0 ? off : OOB, 0);
}

DI void slp(int n) {
#pragma nounroll
    for (int i = 0; i < n; ++i) asm volatile("s_sleep 1");
}
DI int xsrc(int wave, int lane, int k) { const int n = 64 * (7 * (wave & 1) + k) + lane, row = (n * 1171) >> 16, pos = n - 56 * row; return (row + 16 * (wave >> 1)) * 896 + ((pos ^ ((row >> 1) & 7)) * 16); }
DI int xrd(int row, int hi, int k) { return row * 896 + (((2 * k + hi) ^ ((row >> 1) & 7)) * 16); }
#define G_ROT 0
#define WSL(u) ((((u) + 1) & 3) + 1)

DI int xswz(int type, int row) { return type ? ((row >> 1) & 1) | (((row >> 3) & 3) << 1) : (row >> 1) & 7; }
DI int xsrc8(int type, int wave, int lane) { const int row = 8 * wave + (lane >> 3); return row * 896 + (((lane & 7) ^ xswz(type, row)) * 16); }
DI int rot7(int u, int sub) { const int x = u + G_ROT * sub; return x >= 7 ? x - 7 : x; }
DI void gemm_first(int wave, int type, int h, int sub) {
    CArgs& a = get_args();
    const SideCtx c = make_ctx(wave);
    h = __builtin_amdgcn_readfirstlane(h); sub = __builtin_amdgcn_readfirstlane(sub); asm volatile("" : "+s"(h), "+s"(sub));
    const u4 rw1 = mksrd(type ? a.Wv : a.Wk, 448 * 8192), rw2 = mksrd(type ? a.Wo : a.Wq, 448 * 8192), rx = mksrd(a.X, 2048 * 896);
    const int c1 = sub == 3 ? 384 : sub * 128 + 64;
    if (type) { dma_cols(c, rw2, h, sub * 128, WSL(2)); dma_cols(c, rw2, h, c1, WSL(3)); } else { dma_rows(c, rw2, h, sub * 128, WSL(2)); dma_rows(c, rw2, h, c1, WSL(3)); }
    dma_rows(c, rw1, h, 64 * rot7(0, sub), WSL(0)); dma_rows(c, rw1, h, 64 * rot7(1, sub), WSL(1));
    const int vx = xsrc8(type, c.wave, c.lane);
#pragma unroll
    for (int k = 0; k < 7; ++k) dma_one(rx, vx, rot7(k, sub) * 128, c.smb + k * 4096 + c.wave * 1024);
}
template <int TYPE>
DI void gemm_role(unsigned tag, int vb, int wave, int h, int sub) {
    CArgs& a = get_args();
    const SideCtx c = make_ctx(wave);
    const int lane = c.lane, g = lane >> 4, n = lane & 15;
    h = __builtin_amdgcn_readfirstlane(h); sub = __builtin_amdgcn_readfirstlane(sub); asm volatile("" : "+s"(h), "+s"(sub));
    const int B = 4 * sub + c.wave, prow = 8 * (n >> 2) + (n & 3);
    GCtx x;
    x.rw1 = mksrd(TYPE ? a.Wv : a.Wk, 448 * 8192); x.rx = mksrd(a.X, 2048 * 896); x.rhb = mksrd(a.HB, HB_SZ);
    x.rexp = mksrd(TYPE ? a.WvoF : a.WqkF, 28 * 16 * 14336); x.rout = mksrd(TYPE ? a.UF : a.GF, 16 * 64 * 28672);
    x.tag = tag; x.rot = rot7(0, sub); x.sw1 = h * 512; x.mw = c.smb + c.wave * 2048; x.mx = c.smb + c.wave * 1024;
    x.sfg = O_FG - O_FW + (h * 2 + TYPE - TYPE * 16 - h) * 256; x.stay = sub < 2; x.csc = __float_as_uint(CSCALE); x.ostr = TYPE ? 16384 : 28672;
    x.p0 = x.p1 = x.p2 = x.p3 = x.p4 = x.p5 = 0;
    x.vr0 = c.vr0; x.vr1 = c.vr1; x.vx = xsrc8(TYPE, c.wave, lane);
    x.wa = c.smb + prow * 512 + ((g ^ swzr(prow)) * 16);
    { const int row = TYPE ? prow : n; x.xa = c.smb + row * 128 + ((g ^ xswz(TYPE, row)) * 16); }
    auto wof = [&](int blk) { return (TYPE ? 2 * blk * 16 + h : h * 28 + 2 * blk) * 14336 + g * 256; };
    auto oof = [&](int blk, int i) { return blk > 13 ? OOB : (TYPE ? (2 * blk + i) * 1048576 + h * 1024 : h * 64 * 28672 + i * 14336 + blk * 1024) + lane * 16; };
    x.vexp = B > 13 ? OOB : wof(B) + (TYPE ? n : 8 * ((n >> 2) & 1) + 4 * (n >> 3) + (n & 3)) * 16;
    x.vimp = B > 5 ? OOB : wof(B + 8) + (TYPE ? n * 16 : (n >> 3) * 14336 + (8 * ((n >> 2) & 1) + (n & 3)) * 16);
    x.vo0 = oof(B, 0); x.vo1 = oof(B, 1); x.vo2 = oof(B + 8, 0); x.vo3 = oof(B + 8, 1);
    x.vfl = (c.wave == 0 && lane == 0) ? O_FW + ((TYPE * 16 + h) * 4 + sub) * 64 : OOB;
    x.vpf = O_FW + ((TYPE * 16 + h) * 4 + (sub | 2)) * 64;
    u4 kf[16];
    waitvm<23>(); bar();
    PSTAMP(1);
    const int kb = ((c.wave >> 1) ? WSL(3) : WSL(2)) * SLICE;
#pragma unroll
    for (int i = 0; i < 16; ++i) {
        const int ct = i >> 3, ks = i & 7;
        if (TYPE) {
            const int r = n >> 2, p = n & 3, ch = (4 * (c.wave & 1) + 2 * ct + (p >> 1)) ^ (((r >> 1) & 1) << 2);
            const char LAS* q = c.sm + kb + (8 * g + r) * 128 + ch * 16 + 8 * (p & 1) + ks * 4096;
            const u2 lo = trread(q), hi = trread(q + 512); kf[i] = (u4){lo[0], lo[1], hi[0], hi[1]};
        } else {
            const int row = prow + 4 * ct;
            kf[i] = *(const u4 LAS*)(c.sm + kb + (c.wave & 1) * 16384 + row * 512 + (((4 * (ks & 3) + g) ^ swzr(row)) * 16) + (ks >> 2) * 256);
        }
        if (B > 13) kf[i] = (u4){0u, 0u, 0u, 0u};
    }
    bar();
    dma_rows(c, x.rw1, h, 64 * rot7(2, sub), WSL(2));
#define KF4(i) (u16v){kf[i][0], kf[i][1], kf[i][2], kf[i][3], kf[i + 1][0], kf[i + 1][1], kf[i + 1][2], kf[i + 1][3], kf[i + 2][0], kf[i + 2][1], kf[i + 2][2], kf[i + 2][3], kf[i + 3][0], kf[i + 3][1], kf[i + 3][2], kf[i + 3][3]}
    const unsigned tw = TYPE ? gemm_asm1(x, KF4(0), KF4(4), KF4(8), KF4(12)) : gemm_asm0(x, KF4(0), KF4(4), KF4(8), KF4(12));
}

DI void o_task(unsigned tag, int wave, int s) {
    {
        CArgs& a = get_args();
        const SideCtx c = make_ctx(wave);
        const int lane = c.lane, hi = lane >> 5, l31 = lane & 31, w = c.wave, pc = swap23(l31);
        s = __builtin_amdgcn_readfirstlane(s); asm volatile("" : "+s"(s));
        const u4 rhb = mksrd(a.HB, HB_SZ), rx = mksrd(a.X, 2048 * 896);
        bar();
        const int vx = xsrc8(0, w, lane);
#pragma unroll
        for (int k = 0; k < 7; ++k) dma_one(rx, vx, s * 28672 + k * 128, c.smb + k * 4096 + w * 1024);
        wait_flags(rhb, lane < 16 ? O_FH + (16 * w + lane) * 128 : OOB, tag + s + 2);
        OCtx x;
        x.r = mksrd(a.HB, 0x7000000);
        x.sg = (unsigned)((char*)a.GF - a.HB) + s * 28672; x.ssx = (unsigned)((char*)a.SXM - a.HB) + s * 1024; x.hx = 4 * w + 2 * (w & 1);
        x.su = (unsigned)((char*)a.UF - a.HB) + s * 16384 + w * 2097152; x.sum = (unsigned)((char*)a.UM - a.HB) + s * 4096 + w * 524288; x.nt = w < 2 ? 4 : 3;
        x.sr = x.p0 = x.p1 = x.p2 = x.p3 = x.p4 = x.p5 = x.p6 = x.p7 = x.p8 = x.p9 = x.pa = 0;
        x.vg = (pc >> 4) * 14336 + hi * 256 + (pc & 15) * 16;
        x.vsx = (l31 >> 4) * 512 + hi * 256 + (l31 & 15) * 16;
        x.vu = (pc >> 4) * 1048576 + hi * 256 + (pc & 15) * 16; x.vum = (pc >> 4) * 262144 + hi * 512 + (pc & 15) * 16;
        x.vx = c.smb + l31 * 128 + ((hi ^ xswz(0, l31)) * 16); x.vt = l31 - 8 * hi;
        x.vpx = c.smb + 32768 + x.hx * 2048 + lane * 16; x.vpm = c.smb + 65536 + w * 2048 + lane * 16; x.vpr = c.smb + 32768 + lane * 16;
        x.vo = c.smb + 73728 + l31 * 912 + w * 64 + hi * 16;
        o_asm(x);
    }
    bar();
    const int tid = wave * 64 + fresh_lane();
    const u4 ro = mksrd(get_args().out, 2048 * 1792);
#pragma unroll
    for (int k = 0; k < 7; ++k) sst128(*(const u4 LAS*)(smem_ + 73728 + (tid >> 3) * 912 + (tid & 7) * 16 + k * 128), ro, (tid >> 3) * 1792 + (tid & 7) * 16, s * 32 * 1792 + 896 + k * 128);
}

DI void e_task(unsigned tag, int wave, int s, int hg) {
    CArgs& a = get_args();
    const SideCtx c = make_ctx(wave);
    const int lane = c.lane, hi = lane >> 5, l31 = lane & 31;
    s = __builtin_amdgcn_readfirstlane(s); hg = __builtin_amdgcn_readfirstlane(hg); asm volatile("" : "+s"(s), "+s"(hg));
    const int h = 4 * hg + c.wave;
    const u4 rhb = mksrd(a.HB, HB_SZ), rg = mksrd(a.GF, 16 * 64 * 28672), rx = mksrd(a.X, 2048 * 896);
    bar();
#pragma unroll
    for (int k = 0; k < 7; ++k) dma_one(rx, xsrc(c.wave, lane, k), s * 28672, c.smb + (7 * c.wave + k) * 1024);
    wait_flags(rhb, lane < 4 ? O_FG + (h * 8 + lane) * 64 : OOB, tag + s + 1);
    u4 gf[28];
    const int key = swap23(l31), o_g = (key >> 4) * 14336 + hi * 256 + (key & 15) * 16;
#pragma unroll
    for (int k = 0; k < 28; ++k) asm volatile("buffer_load_dwordx4 %0, %1, %2, %3 offen sc1" : "=a"(gf[k]) : "v"(o_g), "s"(rg), "s"((h * 64 + s) * 28672 + k * 512) : "memory");
    bar();
    f16v sx;
    pipe<28, 4>([&](int k) { return *(const u4 LAS*)(c.sm + xrd(l31, hi, k & 3) + (k >> 2) * 128); },
                [&](int k, u4 f) { if (k == 0) WAITV(21, "+a"(gf[0]), "+a"(gf[1]), "+a"(gf[2]), "+a"(gf[3]), "+a"(gf[4]), "+a"(gf[5]), "+a"(gf[6]));
                                   if (k == 7) WAITV(14, "+a"(gf[7]), "+a"(gf[8]), "+a"(gf[9]), "+a"(gf[10]), "+a"(gf[11]), "+a"(gf[12]), "+a"(gf[13]));
                                   if (k == 14) WAITV(7, "+a"(gf[14]), "+a"(gf[15]), "+a"(gf[16]), "+a"(gf[17]), "+a"(gf[18]), "+a"(gf[19]), "+a"(gf[20]));
                                   if (k == 21) WAITV(0, "+a"(gf[21]), "+a"(gf[22]), "+a"(gf[23]), "+a"(gf[24]), "+a"(gf[25]), "+a"(gf[26]), "+a"(gf[27]));
                                   if (k == 0) mV_av0(sx, gf[k], f); else mV_av(sx, gf[k], f); },
                [&](int) {});
    asm volatile("s_nop 11" : "+v"(sx));
    float m = -3.0e38f;
#pragma unroll
    for (int j = 0; j < 16; ++j) { if (16 * (j >> 3) + 8 * hi + (j & 7) > l31) sx[j] = -INFINITY; m = fmaxf(m, sx[j]); }
    m = x32max(m);
    float z = 0.f;
#pragma unroll
    for (int j = 0; j < 16; ++j) { sx[j] = __builtin_amdgcn_exp2f(sx[j] - m); z += sx[j]; }
    z = x32add(z);
    const u4 rex = mksrd(a.EX, 64 * 16 * 2048), rmz = mksrd(a.MZX, 64 * 16 * 256);
    sst128(pack8(sx, 0), rex, lane * 16, (s * 16 + h) * 2048); sst128(pack8(sx, 1), rex, lane * 16, (s * 16 + h) * 2048 + 1024);
    sst64((u2){__float_as_uint(m), __float_as_uint(z)}, rmz, lane < 32 ? lane * 8 : OOB, (s * 16 + h) * 256);
    set_flag(c, rhb, O_FE + (s * 4 + hg) * 64, tag);
}

DI void x_task(unsigned tag, int wave, int s, int cb) {
    CArgs& a = get_args();
    const SideCtx c = make_ctx(wave);
    const int lane = c.lane, hi = lane >> 5, l31 = lane & 31, w = c.wave;
    s = __builtin_amdgcn_readfirstlane(s); cb = __builtin_amdgcn_readfirstlane(cb); asm volatile("" : "+s"(s), "+s"(cb));
    const u4 rhb = mksrd(a.HB, HB_SZ);
    const u4 ru = mksrd(a.UF, 16 * 64 * 28672), rex = mksrd(a.EX, 64 * 16 * 2048), rmz = mksrd(a.MZX, 64 * 16 * 256);
    const u4 rum = mksrd(a.UM, 28 * 64 * 4096), rsx = mksrd(a.SXM, 64 * 65536);
    wait_flags(rhb, lane < 8 ? O_FG + (((4 * w + (lane & 3)) * 2 + 1) * 4 + ((cb >> 1) & (lane < 4 ? 3 : 1))) * 64 : lane == 8 ? O_FE + (s * 4 + w) * 64 : OOB, lane < 8 ? tag + s + 1 : tag);
    u4 uf[4][2][2], ex[4][2]; u2 mz[4];
    const int o_u = (l31 >> 4) * 1048576 + hi * 256 + (l31 & 15) * 16;
#pragma unroll
    for (int k = 0; k < 4; ++k) {
        const int h = 4 * w + (k ^ (2 * (w & 1))), sh = s * 16 + h;
#pragma unroll
        for (int sg = 0; sg < 2; ++sg) {
            ex[k][sg] = sld128(rex, lane * 16, sh * 2048 + sg * 1024);
#pragma unroll
            for (int nt = 0; nt < 2; ++nt) uf[k][sg][nt] = sld128(ru, o_u, (4 * cb + 2 * nt) * 1048576 + sh * 1024 + sg * 512);
        }
        mz[k] = sld64(rmz, l31 * 8, sh * 256);
    }
    waitvm<0>();
    f16v rr[4][2];
#pragma unroll
    for (int k = 0; k < 4; ++k) {
        asm volatile("" : "+v"(mz[k]), "+v"(ex[k][0]), "+v"(ex[k][1]), "+v"(uf[k][0][0]), "+v"(uf[k][0][1]), "+v"(uf[k][1][0]), "+v"(uf[k][1][1]));
        mV_vv0(rr[k][0], uf[k][0][0], ex[k][0]); mV_vv0(rr[k][1], uf[k][0][1], ex[k][0]);
        mV_vv(rr[k][0], uf[k][1][0], ex[k][1]); mV_vv(rr[k][1], uf[k][1][1], ex[k][1]);
        MFW32(rr[k][0], rr[k][1]);
    }
    wait_flags(rhb, lane < 16 ? O_FH + (16 * w + lane) * 128 : s < 62 ? OOB : lane < 32 ? O_FH + (16 * w + lane - 16) * 128 + 64 : lane < 36 ? O_FP + ((4 * cb + lane - 32) * 4 + w) * 64 : OOB, tag + s + 2);
    u4 sp[4][4], um[2][2];
    const int o_sx = (l31 >> 4) * 512 + hi * 256 + (l31 & 15) * 16, o_um = (l31 >> 4) * 262144 + hi * 512 + (l31 & 15) * 16;
#pragma unroll
    for (int k = 0; k < 16; ++k) sp[k >> 2][k & 3] = sld128(rsx, o_sx, ((4 * w + ((k >> 2) ^ (2 * (w & 1)))) * 4 + (k & 3)) * 65536 + s * 1024);
#pragma unroll
    for (int k = 0; k < 4; ++k) um[k >> 1][k & 1] = sld128(rum, o_um, (4 * cb + 2 * (k & 1)) * 262144 + s * 4096 + w * 1024 + (k >> 1) * 256);
    u2 pm[4]; float scx[4];
#pragma unroll
    for (int k = 0; k < 4; ++k) {
        if (k == 0) WAITV(16, "+v"(sp[0][0]), "+v"(sp[0][1]), "+v"(sp[0][2]), "+v"(sp[0][3]));
        if (k == 1) WAITV(12, "+v"(sp[1][0]), "+v"(sp[1][1]), "+v"(sp[1][2]), "+v"(sp[1][3]));
        if (k == 2) WAITV(8, "+v"(sp[2][0]), "+v"(sp[2][1]), "+v"(sp[2][2]), "+v"(sp[2][3]));
        if (k == 3) WAITV(4, "+v"(sp[3][0]), "+v"(sp[3][1]), "+v"(sp[3][2]), "+v"(sp[3][3]));
        const f4 sc = (__builtin_bit_cast(f4, sp[k][0]) + __builtin_bit_cast(f4, sp[k][1])) + (__builtin_bit_cast(f4, sp[k][2]) + __builtin_bit_cast(f4, sp[k][3]));
        const float mx = __uint_as_float(mz[k][0]);
        float m = fmaxf(x32max(fmaxf(fmaxf(sc[0], sc[1]), fmaxf(sc[2], sc[3]))), mx);
        float e[4];
#pragma unroll
        for (int j = 0; j < 4; ++j) e[j] = __builtin_amdgcn_exp2f(sc[j] - m);
        const float cx = __builtin_amdgcn_exp2f(mx - m);
        const float z = x32add((e[0] + e[1]) + (e[2] + e[3])) + cx * __uint_as_float(mz[k][1]);
        const float inv = __builtin_amdgcn_rcpf(z);
        scx[k] = cx * inv;
        pm[k] = (u2){pk2(e[0] * inv, e[1] * inv), pk2(e[2] * inv, e[3] * inv)};
    }
    WAITV(0, "+v"(um[0][0]), "+v"(um[0][1]), "+v"(um[1][0]), "+v"(um[1][1]));
    f16v d0, d1;
    {
        u4 p0 = {pm[0][0], pm[1][0], pm[2][0], pm[3][0]}, p1 = {pm[0][1], pm[1][1], pm[2][1], pm[3][1]};
        asm volatile("s_nop 1\n\tv_mfma_f32_32x32x16_bf16 %0, %2, %6, 0\n\tv_mfma_f32_32x32x16_bf16 %1, %3, %6, 0\n\tv_mfma_f32_32x32x16_bf16 %0, %4, %7, %0\n\tv_mfma_f32_32x32x16_bf16 %1, %5, %7, %1"
                     : "=&v"(d0), "=&v"(d1) : "v"(um[0][0]), "v"(um[0][1]), "v"(um[1][0]), "v"(um[1][1]), "v"(p0), "v"(p1));
    }
    f16v a0 = (rr[0][0] * scx[0] + rr[1][0] * scx[1]) + (rr[2][0] * scx[2] + rr[3][0] * scx[3]);
    f16v a1 = (rr[0][1] * scx[0] + rr[1][1] * scx[1]) + (rr[2][1] * scx[2] + rr[3][1] * scx[3]);
    asm volatile("s_nop 11" : "+v"(d0), "+v"(d1), "+v"(a0), "+v"(a1));
    a0 += d0; a1 += d1;
    bar();
#pragma unroll
    for (int g = 0; g < 4; ++g) {
        *(f4 LAS*)(c.sm + w * 8704 + l31 * 272 + (8 * g + 4 * hi) * 4) = (f4){a0[4 * g], a0[4 * g + 1], a0[4 * g + 2], a0[4 * g + 3]};
        *(f4 LAS*)(c.sm + w * 8704 + l31 * 272 + 128 + (8 * g + 4 * hi) * 4) = (f4){a1[4 * g], a1[4 * g + 1], a1[4 * g + 2], a1[4 * g + 3]};
    }
    bar();
    {
        const int t = w * 8 + (lane >> 3), c8 = lane & 7;
        f4 s0 = {0.f, 0.f, 0.f, 0.f}, s1 = s0;
#pragma unroll
        for (int p = 0; p < 4; ++p) { s0 += *(const f4 LAS*)(c.sm + p * 8704 + t * 272 + c8 * 32); s1 += *(const f4 LAS*)(c.sm + p * 8704 + t * 272 + c8 * 32 + 16); }
        sst128((u4){pk2(s0[0], s0[1]), pk2(s0[2], s0[3]), pk2(s1[0], s1[1]), pk2(s1[2], s1[3])}, mksrd(a.out, 2048 * 1792), t * 1792 + c8 * 16, s * 32 * 1792 + 896 + cb * 128);
    }
}

#define PA_UT 0
#define PA_TU 32896
#define PA_KH (PA_TU + 33280)
#define PA_KL (PA_KH + 7680)
#define PA_KT (PA_KL + 7680)
#define PA_RED (PA_KT + 24576)
typedef __attribute__((ext_vector_type(2))) _Float16 h2;
typedef __attribute__((ext_vector_type(8))) _Float16 h8;
DI unsigned pkh(float a, float b) { f2 v = {a, b}; return __builtin_bit_cast(unsigned, __builtin_convertvector(v, h2)); }
DI void parta_task(unsigned tag, int wave, int task) {
    CArgs& a = get_args();
    const SideCtx c = make_ctx(wave);
    char LAS* sm = c.sm;
    const int lane = c.lane, w = c.wave, tid = w * 64 + lane, hi = lane >> 5, l31 = lane & 31, l15 = lane & 15, kk = lane >> 4;
    task = __builtin_amdgcn_readfirstlane(task); asm volatile("" : "+s"(task));
    const int d0 = task * 8;
    const u4 rpa = mksrd(a.PA, 56 * 32768);
    bar();
    float scl[2];
#pragma unroll
    for (int g = 0; g < 2; ++g) {
        const int d = d0 + 4 * g + w;
        const float dt = bfr(expf(bf2f(a.Alogdt[d])));
        const u2 x0 = *(const u2*)(a.AlogA + d * 256 + lane * 4), x1 = *(const u2*)(a.Aim + d * 256 + lane * 4), x2 = *(const u2*)(a.ACr + d * 256 + lane * 4), x3 = *(const u2*)(a.ACi + d * 256 + lane * 4);
        f4 z[4]; unsigned mx = 0;
#pragma unroll
        for (int i = 0; i < 4; ++i) {
            auto el = [&](u2 x) { return bf2f((u16)(x[i >> 1] >> (16 * (i & 1)))); };
            z[i] = (f4){-bfr(expf(el(x0))) * dt * LOG2E, el(x1) * dt * 0.15915494309189535f, 2.f * el(x2) * dt, 2.f * el(x3) * dt};
            mx = max(mx, max(__float_as_uint(z[i][2]) & 0x7f800000u, __float_as_uint(z[i][3]) & 0x7f800000u));
        }
#pragma unroll
        for (int o = 1; o < 64; o <<= 1) mx = max(mx, (unsigned)__shfl_xor((int)mx, o));
        mx = min(max(mx, 0x00800000u), 0x7e800000u);
        scl[g] = __uint_as_float(mx);
        const float inv = __uint_as_float(0x7f000000u - mx);
#pragma unroll
        for (int i = 0; i < 4; ++i) { z[i][2] *= inv; z[i][3] *= inv; *(f4 LAS*)(sm + PA_RED + w * 9216 + g * 4096 + (i * 64 + lane) * 16) = z[i]; }
    }
    const u4 dsk4 = *(const u4*)(a.ADsk + d0);
    u4 uv[8];
    {
        const u4 ru = mksrd(a.Au, 2048 * 896);
        asm volatile("s_waitcnt vmcnt(0)\n\ts_nop 4" ::: "memory");
#pragma unroll
        for (int i = 0; i < 8; ++i) asm volatile("buffer_load_dwordx4 %0, %1, %2, %3 offen" : "=v"(uv[i]) : "v"(tid * 7168), "s"(ru), "s"(i * 896 + d0 * 2) : "memory");
    }
    if (tid < 8) *(u4 LAS*)(sm + PA_UT + tid * 4112 + 4096) = (u4){0u, 0u, 0u, 0u};
    if (tid < 32) *(u4 LAS*)(sm + PA_TU + ((tid >> 2) * 260 + 256 + (tid & 3)) * 16) = (u4){0u, 0u, 0u, 0u};
    for (int i = tid; i < 2 * 31 * 5; i += 256) { const int x = i / 155, r = i % 155; *(u4 LAS*)(sm + (x ? PA_KL : PA_KH) + r * 16) = (u4){0u, 0u, 0u, 0u}; }
    float qpos[4];
    qpos[0] = (float)l15;
    qpos[1] = l15 < 8 ? (float)(256 + 2 * l15) : (float)(512 + ((l15 - 8) >> 2) * 256 + (l15 & 3) * 4);
    qpos[2] = l15 < 8 ? (float)(1024 + (l15 >> 1) * 256 + (l15 & 1) * 8) : 2048.f;
    qpos[3] = (float)(16 * l15);
#pragma nounroll
    for (int g = 0; g < 2; ++g) {
        const char LAS* pt = sm + PA_RED + w * 9216 + g * 4096 + kk * 16;
        f4 acc[3] = {{0.f, 0.f, 0.f, 0.f}, {0.f, 0.f, 0.f, 0.f}, {0.f, 0.f, 0.f, 0.f}};
#pragma nounroll
        for (int st = 0; st < 16; ++st) {
            u4 op[4];
#pragma unroll
            for (int i = 0; i < 4; ++i) {
                const f4 z = *(const f4 LAS*)(pt + st * 256 + i * 64);
#pragma unroll
                for (int t = 0; t < 4; ++t) {
                    const float ex = __builtin_amdgcn_exp2f(z[0] * qpos[t]), rv = __builtin_amdgcn_fractf(z[1] * qpos[t]);
                    const float er = ex * __builtin_amdgcn_cosf(rv), ei = ex * __builtin_amdgcn_sinf(rv);
                    op[t][i] = t < 3 ? pkh(er, ei) : pkh(z[2] * er - z[3] * ei, -(z[2] * ei + z[3] * er));
                }
            }
#pragma unroll
            for (int t = 0; t < 3; ++t) acc[t] = __builtin_amdgcn_mfma_f32_16x16x32_f16(__builtin_bit_cast(h8, op[3]), __builtin_bit_cast(h8, op[t]), acc[t], 0, 0, 0);
        }
        const float sc = g ? scl[1] : scl[0];
#pragma unroll
        for (int t = 0; t < 3; ++t) *(f4 LAS*)(sm + PA_KT + (4 * g + w) * 3072 + ((t * 16 + l15) * 16 + 4 * kk) * 4) = acc[t] * sc;
    }
    WAITV(0, "+v"(uv[0]), "+v"(uv[1]), "+v"(uv[2]), "+v"(uv[3]), "+v"(uv[4]), "+v"(uv[5]), "+v"(uv[6]), "+v"(uv[7]));
#pragma unroll
    for (int ch = 0; ch < 8; ++ch) {
        u4 o;
#pragma unroll
        for (int j = 0; j < 4; ++j) o[j] = __builtin_amdgcn_perm(uv[6 - 2 * j][ch >> 1], uv[7 - 2 * j][ch >> 1], (ch & 1) ? 0x07060302u : 0x05040100u);
        *(u4 LAS*)(sm + PA_UT + ch * 4112 + (255 - tid) * 16) = o;
    }
    int toff[8];
#pragma unroll
    for (int j = 0; j < 8; ++j) {
        const int p = (int)bfr((float)(tid * 8 + j));
        int col, ar = (p & 255) >> 4;
        if (p < 256) col = p & 15; else if (p < 512) col = 16 + ((p & 15) >> 1); else if (p < 1024) col = 24 + ((p - 512) >> 8) * 4 + ((p & 15) >> 2);
        else if (p < 2048) col = 32 + ((p - 1024) >> 8) * 2 + ((p & 15) >> 3); else col = 40;
        toff[j] = (col * 16 + ar) * 4;
    }
    bar();
    {
#pragma nounroll
        for (int ch = 0; ch < 8; ++ch) {
            {
                const u4 x0 = *(const u4 LAS*)(sm + PA_UT + ch * 4112 + tid * 16), x1 = *(const u4 LAS*)(sm + PA_UT + ch * 4112 + tid * 16 + 16);
                float kv[8];
#pragma unroll
                for (int j = 0; j < 8; ++j) kv[j] = *(const float LAS*)(sm + PA_KT + ch * 3072 + toff[j]);
                const unsigned r[8] = {x0[0], x0[1], x0[2], x0[3], x1[0], x1[1], x1[2], x1[3]};
#pragma unroll
                for (int cp = 0; cp < 8; ++cp) {
                    u4 o;
#pragma unroll
                    for (int j = 0; j < 4; ++j) o[j] = (cp & 1) ? __builtin_amdgcn_alignbit(r[(cp >> 1) + j + 1], r[(cp >> 1) + j], 16) : r[(cp >> 1) + j];
                    *(u4 LAS*)(sm + PA_TU + (cp * 260 + tid) * 16) = o;
                }
                u4 h4, l4;
#pragma unroll
                for (int j = 0; j < 4; ++j) { h4[j] = pk2(kv[2 * j], kv[2 * j + 1]);
                    l4[j] = pk2(kv[2 * j] - __uint_as_float(h4[j] << 16), kv[2 * j + 1] - __uint_as_float(h4[j] & 0xffff0000u)); }
                const int o = (31 + (tid >> 2)) * 80 + (tid & 3) * 16;
                *(u4 LAS*)(sm + PA_KH + o) = h4; *(u4 LAS*)(sm + PA_KL + o) = l4;
            }
            bar();
            {
                f16v y0, y1;
                const int m00 = 2047 - l31 + 8 * hi, cp = m00 & 7;
                const char LAS* ab = sm + PA_TU + (cp * 260 + (m00 >> 3)) * 16 - 63 * 64;
                const char LAS* bb = sm + PA_KH + l31 * 80 + hi * 16;
                const char LAS* a1w = ab - w * 64; const char LAS* b1w = bb - w * 80;
                const char LAS* a2w = ab + w * 64; const char LAS* b2w = bb + w * 80;
                u4 fr[2][4];
                auto rdg = [&](int g, int sl) __attribute__((always_inline)) {
                    const int i = (g & 15) >> 1, ks = g & 1;
                    if (g < 16) { fr[sl][0] = *(const u4 LAS*)(b1w + (31 - 4 * i) * 80 + ks * 32); fr[sl][2] = *(const u4 LAS*)(a1w + (63 - 4 * i) * 64 + ks * 32);
                                  fr[sl][3] = *(const u4 LAS*)(a1w + (31 - 4 * i) * 64 + ks * 32); fr[sl][1] = *(const u4 LAS*)(b1w + 7680 + (31 - 4 * i) * 80 + ks * 32); }
                    else { fr[sl][0] = *(const u4 LAS*)(b2w + (32 + 4 * i) * 80 + ks * 32); fr[sl][3] = *(const u4 LAS*)(a2w + (32 + 4 * i) * 64 + ks * 32);
                           fr[sl][1] = *(const u4 LAS*)(b2w + 7680 + (32 + 4 * i) * 80 + ks * 32); }
                };
                rdg(0, 0); SB();
#pragma unroll
                for (int g = 0; g < 32; ++g) {
                    const int sl = g & 1;
                    if (g + 1 < 32) rdg(g + 1, sl ^ 1);
                    SB();
                    if (g == 0) { mV_vv0(y0, fr[sl][2], fr[sl][0]); mV_vv0(y1, fr[sl][3], fr[sl][0]); }
                    else { if (g < 16) mV_vv(y0, fr[sl][2], fr[sl][0]); mV_vv(y1, fr[sl][3], fr[sl][0]); }
                    if (g < 16) mV_vv(y0, fr[sl][2], fr[sl][1]);
                    mV_vv(y1, fr[sl][3], fr[sl][1]);
                    SB();
                }
                MFW32(y0, y1);
#pragma unroll
                for (int g = 0; g < 4; ++g) {
                    *(f4 LAS*)(sm + PA_RED + w * 9216 + l31 * 144 + (8 * g + 4 * hi) * 4) = (f4){y0[4 * g], y0[4 * g + 1], y0[4 * g + 2], y0[4 * g + 3]};
                    *(f4 LAS*)(sm + PA_RED + w * 9216 + (32 + l31) * 144 + (8 * g + 4 * hi) * 4) = (f4){y1[4 * g], y1[4 * g + 1], y1[4 * g + 2], y1[4 * g + 3]};
                }
            }
            bar();
            {
                f4 s0 = {0.f, 0.f, 0.f, 0.f}, s1 = s0;
#pragma unroll
                for (int p = 0; p < 4; ++p) { const char LAS* q_ = sm + PA_RED + p * 9216 + (tid >> 2) * 144 + (tid & 3) * 32; s0 += *(const f4 LAS*)q_; s1 += *(const f4 LAS*)(q_ + 16); }
                const u4 uu = *(const u4 LAS*)(sm + PA_UT + ch * 4112 + (255 - tid) * 16);
                const unsigned dw = (ch & 4) ? ((ch & 2) ? dsk4[3] : dsk4[2]) : ((ch & 2) ? dsk4[1] : dsk4[0]);
                const float dsk = __uint_as_float((ch & 1) ? dw & 0xffff0000u : dw << 16);
                float yv[8];
#pragma unroll
                for (int j = 0; j < 8; ++j) yv[j] = bfr(j < 4 ? s0[j & 3] : s1[j & 3]) + bfr(__uint_as_float((j & 1) ? uu[3 - (j >> 1)] << 16 : uu[3 - (j >> 1)] & 0xffff0000u) * dsk);
                sst128((u4){pk2(yv[0], yv[1]), pk2(yv[2], yv[3]), pk2(yv[4], yv[5]), pk2(yv[6], yv[7])}, rpa, tid * 16, (d0 + ch) * 4096);
            }
        }
    }
    set_flag(c, mksrd(a.HB, HB_SZ), O_FA + task * 64, tag);
}
DI void pa_out(unsigned tag, int wave, int t) {
    CArgs& a = get_args();
    const SideCtx c = make_ctx(wave);
    const int lane = c.lane, tid = c.wave * 64 + lane;
    t = __builtin_amdgcn_readfirstlane(t); asm volatile("" : "+s"(t));
    const u4 rpa = mksrd(a.PA, 56 * 32768), ro = mksrd(a.out, 2048 * 1792);
    wait_flags(mksrd(a.HB, HB_SZ), lane < 8 ? O_FA + ((t & ~7) + lane) * 64 : OOB, tag);
    u4 v[8];
#pragma unroll
    for (int i = 0; i < 8; ++i) v[i] = sld128(rpa, (tid >> 5) * 4096 + (tid & 31) * 16, ((t >> 3) * 64 + i * 8) * 4096 + (t & 7) * 512);
    WAITV(0, "+v"(v[0]), "+v"(v[1]), "+v"(v[2]), "+v"(v[3]), "+v"(v[4]), "+v"(v[5]), "+v"(v[6]), "+v"(v[7]));
    bar();
#pragma unroll
    for (int i = 0; i < 8; ++i) *(u4 LAS*)(c.sm + (i * 8 + (tid >> 5)) * 528 + (tid & 31) * 16) = v[i];
    bar();
#pragma unroll
    for (int k = 0; k < 8; ++k) {
        const char LAS* p = c.sm + (tid & 7) * 8 * 528 + ((tid >> 3) + 32 * k) * 2;
        u4 o;
#pragma unroll
        for (int j = 0; j < 4; ++j) o[j] = (unsigned)*(const u16 LAS*)(p + (2 * j) * 528) | ((unsigned)*(const u16 LAS*)(p + (2 * j + 1) * 528) << 16);
        sst128(o, ro, (tid >> 3) * 1792 + (tid & 7) * 16, ((t & 7) * 256 + 32 * k) * 1792 + (t >> 3) * 128);
    }
}
#define P_R 56
#define P_EPS 88
#define P_DM 4
#define P_XOR 4
#define L_SCR 28672

DI void chain_role(unsigned tag, int vb, int wave) {
    CArgs& a = get_args();
    const int nx = vb & 3, id = vb >> 3, W = wave;
    const int lane = fresh_lane(), q = lane >> 4, n = lane & 15;
    const u4 r = mksrd(a.HB, HB_SZ);
    for (int i = W * 64 + lane; i < 1792; i += 256) *(u4 LAS*)(smem_ + i * 16) = (u4){0u, 0u, 0u, 0u};
    wait_flags(r, O_RDY + lane * 64, tag); wait_flags(r, O_RDY + 4096 + lane * 64, tag);
    unsigned win, t0;
    {
        unsigned sp = 0;
#pragma nounroll
        for (;;) {
            u4 v = sld128(r, O_PAR, 0); WAITV(0, "+v"(v));
            win = __builtin_amdgcn_readfirstlane(v[0]) ^ tag; t0 = __builtin_amdgcn_readfirstlane(v[1]);
            if (win <= 3u && (t0 ^ tag) == (unsigned)__builtin_amdgcn_readfirstlane(v[2])) break;
            asm volatile("s_sleep 4" ::: "memory");
            if (++sp > SPIN_MAX) __builtin_amdgcn_endpgm();
        }
    }
    const int wA = win * 8192, wB = wA ^ 16384, wF = 524288 + wA;
    auto copy_off = [&](int g) { return (2 + (g >> 1)) * 32768 + ((g & 1) ? wA : wB); };
    Ctx c;
    c.r = r; c.rts = r; c.t0 = t0; c.per = P_A + P_B; c.R = P_R; c.tol = 4; c.tag = tag; c.dm = P_DM; c.des = P_EPS; c.role = (id < 16 ? 0 : id < 20 ? 4 : 8) + W; c.tf0 = 0;
    c.idx = (lane & 1) ? 0xEEEE : 0x4444; c.vl16 = lane * 16;
    c.vg = c.vg3 = c.vtl = c.vtr = c.vsel = c.vraw = c.vfo = c.ve = c.vs = c.vrw = c.vrr = c.vr1 = c.vr2 = c.vdbg = 0;
    c.vtb = lane < 28 ? O_ANN + lane * 128 : OOB;
    c.vts = 32768 + W * 4096 + (STAMPS ? lane * 4 + (lane < 16 ? 0 : 32768) : 0);
    PSTAMP(1);
    if (id < 16) {
        const int hb = nx * 16 + id, h = hb >> 2, part = hb & 3;
        c.rw = mksrd(a.WqkF, 16 * 28 * 14336); c.sw = (h * 28 + part * 7) * 14336;
        c.rx = mksrd(a.SXM, 64 * 65536); c.sx = hb * 65536 + W * 512;
        c.rg = mksrd(a.X, 2048 * 896); c.sg = 0;
        c.vm = copy_off(nx) + (W >> 1) * 4096 + lane * 16;
        c.vtl = q * 1088 + n * 8 + (W >> 1) * 512;
        const int r_ = (lane >> 2) & 3, c_ = lane & 3;
        const int rec = (h >> 1) * 1024 + part * 256 + (h & 1) * 128;
        const int raw = (h >> 2) * 32768 + wF + (h & 3) * 2048 + part * 512;
        c.vfl = lane == 0 ? O_FH + hb * 128 + W * 64 : OOB; c.vpub = OOB;
        if (W < 2) {
            c.vg = (16 * W + (n & 7)) * 896 + (112 * part + 32 * (q >> 1) + 16 * (n >> 3) + 8 * (q & 1)) * 2; c.vg3 = (q >= 2 && n >= 8) ? OOB : c.vg + 128;
            c.vtr = c_ * 1088 + (8 * q + r_) * 8;
            c.vpub = (q >> 1) * 256 + n * 16 + (q & 1) * 8;
        } else if (W == 2) {
            c.vraw = lane < 16 ? (lane < 8 ? wA : wB) + rec + (lane & 7) * 16 : lane < 32 ? raw + (lane - 16) * 16 : id < 14 ? copy_off(nx) + id * 512 + (lane - 32) * 16 : OOB;
        } else {
            c.vg = copy_off(nx) + (7 * part + 2 * (q >> 1) + (n >> 3)) * 256 + (2 * (n & 7) + (q & 1)) * 16; c.vg3 = (q >= 2 && n >= 8) ? OOB : c.vg + 1024;
            c.vtr = c_ * 1088 + (16 * (q >> 1) + 4 * (r_ >> 1) + 2 * (q & 1) + (r_ & 1)) * 8;
            c.vpub = (n < 8 ? wA : wB) + rec + (n & 7) * 16 + q * 4;
            c.vraw = n < 8 ? raw + n * 32 + q * 8 : OOB;
        }
        wait_flags(r, lane < 4 ? O_FW + (h * 4 + lane) * 64 : OOB, tag);
    } else if (id < 20) {
        const int h = nx * 4 + id - 16;
        c.rw = r; c.sw = 0; c.rx = r; c.sx = 0;
        c.rg = mksrd(a.GF, 16 * 64 * 28672); c.sg = h * 64 * 28672 + W * 4096;
        c.vm = copy_off(nx) + W * 2048 + lane * 16;
        c.vtl = W * 1280 + 2 * q * 160 + n * 4;
        c.vtr = ((lane >> 2) & 7) * 160 + (8 * W + 2 * (lane & 3)) * 4;
        const int rec = 32768 + (h >> 1) * 1024 + W * 256 + (h & 1) * 128;
        const int raw = (h >> 2) * 32768 + wF + (h & 3) * 2048 + W * 512 + 256;
        c.vpub = (lane < 32 ? wA : wB) + rec + (lane & 31) * 4;
        c.vraw = lane < 32 ? raw + lane * 8 : OOB;
        c.vsel = lane < 16 ? (lane < 8 ? wA : wB) + rec + (lane & 7) * 16 : lane < 32 ? raw + (lane - 16) * 16 : OOB;
        c.vfl = O_FG + (h * 8 + (lane & 7)) * 64;
        wait_flags(r, c.vfl, tag + 3);
    } else {
        const int pid = nx * 7 + id - 20;
        c.rw = mksrd(a.WvoF, 28 * 16 * 14336); c.sw = (pid * 16 + 4 * W) * 14336;
        c.rg = mksrd(a.UF, 16 * 64 * 28672); c.sg = pid * 1048576 + W * 4096;
        c.rx = mksrd(a.UM, 28 * 64 * 4096); c.sx = pid * 262144 + W * 1024;
        c.tf0 = P_A;
        c.vm = copy_off(nx) + W * 2048 + lane * 16;
        c.vpub = copy_off(W) + pid * 256 + lane * 4;
        c.vs = ((nx & 1) ? wA : wB) + W * 2048 + lane * 16; c.ve = c.vs + 32768;
        auto qb = [](int x) { return x * 256 + (x & 1) * 32 + (x >> 1) * 128; };
        c.vrw = 8192 + W * 1280 + qb(q) + n * 8;
        c.vrr = 8192 + qb(lane & 3) + ((lane >> 3) + 8 * ((lane >> 2) & 1)) * 8;
        c.vr1 = W * 32768 + wF + (n >> 3) * 2048 + (n & 7) * 32 + q * 8;
        c.vr2 = W * 32768 + wF + (n >> 3) * 2048 + q * 512 + 256 + (n & 7) * 32;
        c.vg = O_FG + (((4 * W + (lane & 3)) * 2 + 1) * 4 + (pid >> 3)) * 64;
        c.vfl = lane == 0 ? O_FP + (pid * 4 + W) * 64 : OOB;
        c.vsel = (pid == 0 && lane == 0) ? O_EPOCH : OOB;
        c.vfo = lane == 0 ? O_ANN + pid * 128 + W * 4 : OOB;
        sst32(san(*(const unsigned*)(a.mem0 + (lane >> 3) * 448 + pid * 16 + 4 * (lane & 3) + 2 * ((lane >> 2) & 1))), r, c.vpub, 3 * 131072);
        wait_flags(r, lane < 4 ? O_FW + ((16 + 4 * W + lane) * 4 + (pid >> 3)) * 64 : OOB, tag);
    }
    PSTAMP(2);
    asm volatile("s_waitcnt lgkmcnt(0)" ::: "memory");
    chain_asm(c);
}

__global__ __launch_bounds__(256, 1) void fused_kernel(Args a_) {
    CArgs& a = get_args();
    const int wave = __builtin_amdgcn_readfirstlane(threadIdx.x >> 6);
    const u4 rhb = mksrd(a.HB, HB_SZ);
    unsigned ep = 0;
    if (wave == 0) ep = sld32(rhb, O_EPOCH, 0);
    else {
        u64 pc; unsigned len;
        asm volatile("s_getpc_b64 %0\nfk_code_lo:\n\ts_max_i32 %1, fk_code_hi-fk_code_lo, 0" : "=s"(pc), "=s"(len) :: "scc");
        const u4 rc = mksrd((const void*)pc, len & ~15u);
        const int vo = (((blockIdx.x >> 3) * 3 + wave - 1) * 2) * 1024 + (threadIdx.x & 63) * 16;
#pragma unroll
        for (int i = 0; i < 2; ++i) dma_one(rc, vo + i * 1024, 0, (unsigned)(size_t)(char LAS*)smem_ + L_SCR + wave * 1024);
    }
    auto first = [&](int vb_) __attribute__((always_inline)) {
        const int res = vb_ & 7, idx = vb_ >> 3;
        if (res >= 4) gemm_first(wave, idx >> 4, 4 * (res - 4) + ((idx >> 2) & 3), idx & 3); else waitvm<0>();
    };
    const int xcc = (__builtin_amdgcn_s_getreg((31 << 11) | 20) & 7) ^ P_XOR;
    int vb = xcc + (blockIdx.x & ~7);
    unsigned tag;
#pragma nounroll
    for (int pass = 0;; ++pass) {
        first(vb);
        if (pass) break;
        if (wave == 0) {
            WAITV(39, "+v"(ep));
            if (threadIdx.x == 0) {
                const unsigned tg = ep + 0x9E3779B4u;
                unsigned slot = (unsigned)xcc * 32u + (blockIdx.x >> 3);
                for (int k = 0; k < 256; ++k, slot = (slot + 1u) & 255u)
                    if (__hip_atomic_exchange((unsigned*)(a.HB + O_CNT) + slot * 32u, tg, __ATOMIC_RELAXED, __HIP_MEMORY_SCOPE_AGENT) != tg) break;
                *(u2 LAS*)(smem_ + L_SCR) = (u2){(slot >> 5) + 8u * (slot & 31u), tg};
            }
        }
        bar();
        const int got = __builtin_amdgcn_readfirstlane(*(const unsigned LAS*)(smem_ + L_SCR));
        tag = __builtin_amdgcn_readfirstlane(*(const unsigned LAS*)(smem_ + L_SCR + 4));
        if (got == vb) break;
        vb = got; waitvm<0>(); bar();
    }
    vb = __builtin_amdgcn_readfirstlane(vb); tag = __builtin_amdgcn_readfirstlane(tag);
    const int res = vb & 7, idx = vb >> 3;
    if (res < 4) {
        const int lane = fresh_lane(), nb = res * 32 + idx;
        sst128((u4){FF, FF, FF, FF}, rhb, lane * 16, nb * 8192 + wave * 2048); sst128((u4){FF, FF, FF, FF}, rhb, lane * 16, nb * 8192 + wave * 2048 + 1024);
        if (nb == 0) sst128((u4){0u, 0u, 0u, 0u}, rhb, lane * 16, O_ANN + wave * 1024);
        waitvm<0>(); bar();
        if (wave == 0) sst32(tag, rhb, lane == 0 ? O_RDY + nb * 64 : OOB, 0);
        if (idx < 27) chain_role(tag, vb, wave);
        if (nb == 63 && wave == 0) {
            u4 bs = {1u << 20, 1u << 20, 1u << 20, 1u << 20};
#pragma nounroll
            for (int rep = 0; rep < 4; ++rep) {
                sst32(rep, rhb, lane < 4 ? O_PROBE + lane * 8192 : OOB, 0); waitvm<0>();
#pragma unroll
                for (int k = 0; k < 4; ++k) {
                    const unsigned c0 = now32();
                    unsigned v = sld32(rhb, lane == 0 ? O_PROBE + k * 8192 : OOB, 0); WAITV(0, "+v"(v));
                    bs[k] = min(bs[k], (now32() - c0) * 4 + k);
                }
            }
            const unsigned t0 = rt_now();
            sst128((u4){(min(min(bs[0], bs[1]), min(bs[2], bs[3])) & 3u) ^ tag, t0, t0 ^ tag, 0u}, rhb, lane == 0 ? O_PAR : OOB, 0);
        }
    } else {
        if (idx >> 4) gemm_role<1>(tag, vb, wave, 4 * (res - 4) + ((idx >> 2) & 3), idx & 3); else gemm_role<0>(tag, vb, wave, 4 * (res - 4) + ((idx >> 2) & 3), idx & 3);
        PSTAMP(3);
    }
    const int f = res < 4 ? res * 5 + idx - 27 : ((idx & 2) ? 20 : 84) + (idx >> 2) * 8 + (idx & 1) * 4 + (res - 4);
    if (f < 56) { parta_task(tag, wave, f); PSTAMP(5); }
    if (f < 20) return;
#pragma nounroll
    for (int it = 0; it < 6; ++it) {
        int t = 0;
        if (f < 56) { const int i = f - 20; t = it == 0 ? (i < 20 ? 0x1000 + 28 + i : 0x4000 + i - 20) : it == 1 ? (i < 20 ? 0 : 0x4000 + i - 4) : it == 2 ? (i < 20 ? 0 : 0x4000 + i + 12) : it == 3 ? (i >= 20 && i < 28 ? 0x4000 + i + 28 : 0) : it == 4 ? (i >= 16 ? 0x3000 + 28 + i - 16 : 0) : 0; }
        else if (f < 84) { const int i = f - 56; t = it == 0 ? 0x1000 + i : it < 4 ? (i + 28 * (it - 1) < 64 ? 0x2000 + i + 28 * (it - 1) : 0) : it == 4 ? 0x3000 + i : 0; }
        else t = it == 0 ? 0x3000 + 48 + f - 84 : 0;
        const int k = t >> 12, j = t & 4095;
        if (k == 1) o_task(tag, wave, j); else if (k == 2) e_task(tag, wave, 48 + (j >> 2), j & 3); else if (k == 3) x_task(tag, wave, 48 + j / 7, j % 7); else if (k == 4) pa_out(tag, wave, j);
    }
    PSTAMP(6);
}
__device__ __attribute__((used, noinline)) void fk_tail() { asm volatile("fk_code_hi:"); }

extern "C" void kernel_launch(void* const* d_in, const int* in_sizes, int n_in, void* d_out, int out_size, void* d_ws, size_t ws_size, hipStream_t stream) {
    Args a;
    a.Au = (const u16*)d_in[0]; a.AlogA = (const u16*)d_in[1]; a.Aim = (const u16*)d_in[2]; a.ACr = (const u16*)d_in[3]; a.ACi = (const u16*)d_in[4];
    a.Alogdt = (const u16*)d_in[5]; a.ADsk = (const u16*)d_in[6]; a.X = (const u16*)d_in[7]; a.mem0 = (const u16*)d_in[8];
    a.Wq = (const u16*)d_in[9]; a.Wk = (const u16*)d_in[10]; a.Wv = (const u16*)d_in[11]; a.Wo = (const u16*)d_in[12];
    a.out = (u16*)d_out;
    char* base = (char*)(((size_t)d_ws + (2u << 20) - 1) & ~(size_t)((2u << 20) - 1));
    a.HB = base;
    size_t off = HB_SZ;
    auto alloc = [&](size_t n) { char* p = base + off; off += (n + 4095) & ~(size_t)4095; return p; };
    a.TS = (unsigned*)alloc(8u << 20);
    a.WvoF = (u16*)alloc(28 * 16 * 14336); a.WqkF = (u16*)alloc(28 * 16 * 14336); a.GF = (u16*)alloc((size_t)16 * 64 * 28672); a.UF = (u16*)alloc((size_t)16 * 64 * 28672);
    a.EX = (u16*)alloc(64 * 16 * 2048); a.UM = (u16*)alloc(28 * 64 * 4096); a.MZX = (float*)alloc(64 * 16 * 256); a.SXM = (float*)alloc(64 * 65536); a.PA = (u16*)alloc(56 * 32768);
    (void)hipFuncSetAttribute((const void*)fused_kernel, hipFuncAttributeMaxDynamicSharedMemorySize, 160 * 1024);
    fused_kernel<<<256, 256, 160 * 1024, stream>>>(a);
}
```

```cpp
#define P_A 208
#define P_B 264
#include <hip/hip_runtime.h>
#include <stdint.h>
typedef unsigned short u16;
typedef unsigned long long u64;
typedef __attribute__((ext_vector_type(2))) __bf16 bf2;
typedef __attribute__((ext_vector_type(2))) float f2;
typedef __attribute__((ext_vector_type(4))) float f4;
typedef __attribute__((ext_vector_type(16))) float f16v;
typedef __attribute__((ext_vector_type(2))) unsigned u2;
typedef __attribute__((ext_vector_type(4))) unsigned u4;
typedef __attribute__((ext_vector_type(16))) unsigned u16v;
typedef __attribute__((ext_vector_type(4))) short s4;
#define LAS __attribute__((address_space(3)))
#define DI __device__ __forceinline__
#define SB() __builtin_amdgcn_sched_barrier(0)
#define PIN(x) asm volatile("" : "+v"(x))
#define WAITV(n, ...) asm volatile("s_waitcnt vmcnt(" #n ")" : __VA_ARGS__ :: "memory")
#define LOG2E 1.4426950408889634f
#define CSCALE (LOG2E * 0.0625f)
#define FF 0xFFFFFFFFu
#define OOB 0x7f000000
#define SPIN_MAX (1u << 18)

#define HB_SZ (2 << 20)
#define O_CTL (1 << 20)
#define O_ANN O_CTL
#define O_PAR (O_CTL + 4096)
#define O_FW (O_CTL + 8192)
#define O_FG (O_CTL + 24576)
#define O_FE (O_CTL + 32768)
#define O_FH (O_CTL + 49152)
#define O_FP (O_CTL + 57344)
#define O_FA (O_CTL + 65536)
#define O_CNT (O_CTL + 73728)
#define O_EPOCH (O_CNT + 32768)
#define O_PROBE (O_CTL + 131072)

struct Args {
    const u16 *Au, *AlogA, *Aim, *ACr, *ACi, *Alogdt, *ADsk, *X, *mem0, *Wq, *Wk, *Wv, *Wo;
    u16* out;
    char* HB;
    u16 *WvoF, *WqkF, *GF, *UF, *EX, *UM, *PA; float *MZX, *SXM;
    unsigned* TS;
};
typedef const Args __attribute__((address_space(4))) CArgs;
DI CArgs& get_args() { CArgs* p = (CArgs*)__builtin_amdgcn_kernarg_segment_ptr(); asm volatile("" : "+s"(p)); return *p; }
extern __shared__ __attribute__((aligned(16))) char smem_[];

DI unsigned pk2(float a, float b) { f2 v = {a, b}; return __builtin_bit_cast(unsigned, __builtin_convertvector(v, bf2)); }
DI float bfr(float a) { return __uint_as_float(pk2(a, a) & 0xffff0000u); }
DI float bf2f(u16 x) { return __uint_as_float((unsigned)x << 16); }
DI unsigned san(unsigned x) { return x < 0xFFFFFFFEu ? x : 0xFFFFFFFEu; }
DI void bar() { asm volatile("s_waitcnt lgkmcnt(0)\n\ts_barrier" ::: "memory"); SB(); }
DI float x32max(float a) { auto r = __builtin_amdgcn_permlane32_swap(__float_as_uint(a), __float_as_uint(a), false, false); return fmaxf(__uint_as_float(r[0]), __uint_as_float(r[1])); }
DI float x32add(float a) { auto r = __builtin_amdgcn_permlane32_swap(__float_as_uint(a), __float_as_uint(a), false, false); return __uint_as_float(r[0]) + __uint_as_float(r[1]); }
DI u2 trread(const char LAS* p) { return __builtin_bit_cast(u2, __builtin_amdgcn_ds_read_tr16_b64_v4i16((LAS s4*)p)); }
DI u4 mksrd(const void* p, unsigned bytes) { u64 x = (u64)p; return (u4){(unsigned)x, (unsigned)(x >> 32) & 0xffffu, bytes, 0x00020000u}; }
DI unsigned now32() { u64 t; asm volatile("s_memtime %0\n\ts_waitcnt lgkmcnt(0)" : "=s"(t) :: "memory"); return (unsigned)t; }
DI unsigned rt_now() { u64 t; asm volatile("s_memrealtime %0\n\ts_waitcnt lgkmcnt(0)" : "=s"(t) :: "memory"); return (unsigned)t; }
DI u4 ld128(u4 srd, int voff, int soff) { u4 v; asm volatile("buffer_load_dwordx4 %0, %1, %2, %3 offen sc1" : "=v"(v) : "v"(voff), "s"(srd), "s"(soff) : "memory"); return v; }
DI void st128(u4 v, u4 srd, int voff, int soff) { asm volatile("buffer_store_dwordx4 %0, %1, %2, %3 offen sc1\n\ts_nop 1" :: "v"(v), "v"(voff), "s"(srd), "s"(soff) : "memory"); }
DI void st32(unsigned v, u4 srd, int voff, int soff) { asm volatile("buffer_store_dword %0, %1, %2, %3 offen sc1" :: "v"(v), "v"(voff), "s"(srd), "s"(soff) : "memory"); }
DI u4 sld128(u4 srd, int voff, int soff) { u4 v; asm volatile("s_nop 4\n\tbuffer_load_dwordx4 %0, %1, %2, %3 offen sc1" : "=v"(v) : "v"(voff), "s"(srd), "s"(soff) : "memory"); return v; }
DI u2 sld64(u4 srd, int voff, int soff) { u2 v; asm volatile("s_nop 4\n\tbuffer_load_dwordx2 %0, %1, %2, %3 offen sc1" : "=v"(v) : "v"(voff), "s"(srd), "s"(soff) : "memory"); return v; }
DI unsigned sld32(u4 srd, int voff, int soff) { unsigned v; asm volatile("s_nop 4\n\tbuffer_load_dword %0, %1, %2, %3 offen sc1" : "=v"(v) : "v"(voff), "s"(srd), "s"(soff) : "memory"); return v; }
DI void sst128(u4 v, u4 srd, int voff, int soff) { asm volatile("s_nop 4\n\tbuffer_store_dwordx4 %0, %1, %2, %3 offen sc1\n\ts_nop 1" :: "v"(v), "v"(voff), "s"(srd), "s"(soff) : "memory"); }
DI void sst64(u2 v, u4 srd, int voff, int soff) { asm volatile("s_nop 4\n\tbuffer_store_dwordx2 %0, %1, %2, %3 offen sc1" :: "v"(v), "v"(voff), "s"(srd), "s"(soff) : "memory"); }
DI void sst32(unsigned v, u4 srd, int voff, int soff) { asm volatile("s_nop 4\n\tbuffer_store_dword %0, %1, %2, %3 offen sc1" :: "v"(v), "v"(voff), "s"(srd), "s"(soff) : "memory"); }
template <int N> DI void waitvm() { asm volatile("s_waitcnt vmcnt(%0)" :: "i"(N) : "memory"); }
DI int fresh_lane() { int l; asm volatile("v_mbcnt_lo_u32_b32 %0, -1, 0\n\tv_mbcnt_hi_u32_b32 %0, -1, %0" : "=v"(l)); __builtin_assume(l >= 0 && l < 64); return l; }
#define W_SLP 8
template <int SLP = W_SLP>
DI void wait_flags(u4 rhb, int voff_or_oob, unsigned want) {
    unsigned sp = 0;
#pragma nounroll
    for (;;) {
        unsigned v = sld32(rhb, voff_or_oob, 0); WAITV(0, "+v"(v));
        if (__all((v - want <= 72u) | (voff_or_oob >= OOB))) break;
        unsigned k = __builtin_amdgcn_readfirstlane(want - v);
        k = k > 64u ? (SLP < W_SLP ? 1u : 2u) : k;
        asm volatile("s_sleep %0" :: "i"(SLP) : "memory");
#pragma nounroll
        for (unsigned i = 1; i < k; ++i) asm volatile("s_sleep 30" ::: "memory");
        if (++sp > SPIN_MAX / 16) __builtin_amdgcn_endpgm();
    }
}
#define PSTAMP(k) do { } while (0)
#define RFL(x) (unsigned)__builtin_amdgcn_readfirstlane(x)
#define STAMPS 0
struct Ctx { u4 r, rw, rg, rx, rts; unsigned t0, tf0, per, R, tol, tag, sw, sg, sx, dm, des, role; unsigned idx, vl16, vm, vg, vg3, vtl, vtr, vsel, vpub, vraw, vfl, vfo, ve, vs, vrw, vrr, vr1, vr2, vts, vdbg, vtb; };
DI void chain_asm(const Ctx& c) {
    const u16v s0 = {RFL(c.r[0]), RFL(c.r[1]), RFL(c.r[2]), RFL(c.r[3]), RFL(c.rw[0]), RFL(c.rw[1]), RFL(c.rw[2]), RFL(c.rw[3]), RFL(c.rg[0]), RFL(c.rg[1]), RFL(c.rg[2]), RFL(c.rg[3]), RFL(c.rx[0]), RFL(c.rx[1]), RFL(c.rx[2]), RFL(c.rx[3])}, s1 = {RFL(c.rts[0]), RFL(c.rts[1]), RFL(c.rts[2]), RFL(c.rts[3]), RFL(c.t0), RFL(c.tf0), RFL(c.per), RFL(c.R), RFL(c.tol), RFL(c.tag), RFL(c.sw), RFL(c.sg), RFL(c.sx), RFL(c.dm), RFL(c.des), RFL(c.role)}, v0 = {c.idx, c.vl16, c.vm, c.vg, c.vg3, c.vtl, c.vtr, c.vsel, c.vpub, c.vraw, c.vfl, c.vfo, c.ve, c.vs, c.vrw, c.vrr}, v1 = {c.vr1, c.vr2, c.vts, c.vdbg, c.vtb, 0u, 0u, 0u, 0u, 0u, 0u, 0u, 0u, 0u, 0u, 0u};
    asm volatile(
"s_nop 4\ns_cmp_eq_u32 s39, 0\ns_cbranch_scc1 fk_jh0\ns_cmp_eq_u32 s39, 1\n"
"s_cbranch_scc1 fk_jh1\ns_cmp_eq_u32 s39, 2\ns_cbranch_scc1 fk_jh2\ns_cmp_eq_u32 s39, 3\n"
"s_cbranch_scc1 fk_jh3\ns_cmp_eq_u32 s39, 4\ns_cbranch_scc1 fk_jx0\ns_cmp_eq_u32 s39, 5\n"
"s_cbranch_scc1 fk_jx1\ns_cmp_eq_u32 s39, 6\ns_cbranch_scc1 fk_jx2\ns_cmp_eq_u32 s39, 7\n"
"s_cbranch_scc1 fk_jx3\ns_cmp_eq_u32 s39, 8\ns_cbranch_scc1 fk_jp0\ns_cmp_eq_u32 s39, 9\n"
"s_cbranch_scc1 fk_jp1\ns_cmp_eq_u32 s39, 10\ns_cbranch_scc1 fk_jp2\ns_cmp_eq_u32 s39, 11\n"
"s_cbranch_scc1 fk_jp3\ns_endpgm\nfk_jh0:\ns_branch fk_h0\n"
"fk_jh1:\ns_branch fk_h1\nfk_jh2:\ns_branch fk_h2\n"
"fk_jh3:\ns_branch fk_h3\nfk_jx0:\ns_branch fk_x0\n"
"fk_jx1:\ns_branch fk_x1\nfk_jx2:\ns_branch fk_x2\n"
"fk_jx3:\ns_branch fk_x3\nfk_jp0:\ns_branch fk_p0\n"
"fk_jp1:\ns_branch fk_p1\nfk_jp2:\ns_branch fk_p2\n"
"fk_jp3:\ns_branch fk_p3\nfk_h0:\ns_mov_b32 s40, 0\n"
"s_mov_b32 s41, 0\ns_mov_b32 s42, 262144\ns_mov_b32 s43, 393216\ns_mov_b32 s44, s28\n"
"s_mov_b32 s45, 0\ns_mov_b32 s46, 0\ns_mov_b32 s92, 0\ns_mov_b32 s49, 0\n"
"s_mov_b32 s53, 0\ns_mov_b32 s50, s35\ns_mov_b32 s51, s36\nv_mov_b32 v131, v13\n"
"v_mov_b32 v132, v14\nv_mov_b32 v134, 24576\nv_mov_b32 v115, 0\nv_mov_b32 v120, -1\n"
"v_mov_b32 v121, -1\nv_mov_b32 v122, -1\nv_mov_b32 v123, -1\ns_add_u32 s47, s34, 0\n"
"s_and_b32 s52, s47, 1\ns_mul_i32 s52, s52, -14208\ns_mul_i32 s48, s47, 14336\ns_add_u32 s48, s48, s52\n"
"s_add_u32 s47, s48, 0\nbuffer_load_dwordx4 a[0:3], v15, s[12:15], s47 offen sc1\nbuffer_load_dwordx4 a[4:7], v15, s[12:15], s47 offen offset:1024 sc1\ns_add_u32 s47, s48, 2048\n"
"buffer_load_dwordx4 a[8:11], v15, s[12:15], s47 offen sc1\nbuffer_load_dwordx4 a[12:15], v15, s[12:15], s47 offen offset:1024 sc1\ns_add_u32 s47, s48, 4096\nbuffer_load_dwordx4 a[16:19], v15, s[12:15], s47 offen sc1\n"
"buffer_load_dwordx4 a[20:23], v15, s[12:15], s47 offen offset:1024 sc1\ns_add_u32 s47, s48, 6144\nbuffer_load_dwordx4 a[24:27], v15, s[12:15], s47 offen sc1\nbuffer_load_dwordx4 a[28:31], v15, s[12:15], s47 offen offset:1024 sc1\n"
"s_add_u32 s47, s34, 1\ns_and_b32 s52, s47, 1\ns_mul_i32 s52, s52, -14208\ns_mul_i32 s48, s47, 14336\n"
"s_add_u32 s48, s48, s52\ns_add_u32 s47, s48, 0\nbuffer_load_dwordx4 a[32:35], v15, s[12:15], s47 offen sc1\nbuffer_load_dwordx4 a[36:39], v15, s[12:15], s47 offen offset:1024 sc1\n"
"s_add_u32 s47, s48, 2048\nbuffer_load_dwordx4 a[40:43], v15, s[12:15], s47 offen sc1\nbuffer_load_dwordx4 a[44:47], v15, s[12:15], s47 offen offset:1024 sc1\ns_add_u32 s47, s48, 4096\n"
"buffer_load_dwordx4 a[48:51], v15, s[12:15], s47 offen sc1\nbuffer_load_dwordx4 a[52:55], v15, s[12:15], s47 offen offset:1024 sc1\ns_add_u32 s47, s48, 6144\nbuffer_load_dwordx4 a[56:59], v15, s[12:15], s47 offen sc1\n"
"buffer_load_dwordx4 a[60:63], v15, s[12:15], s47 offen offset:1024 sc1\ns_add_u32 s47, s34, 2\ns_and_b32 s52, s47, 1\ns_mul_i32 s52, s52, -14208\n"
"s_mul_i32 s48, s47, 14336\ns_add_u32 s48, s48, s52\ns_add_u32 s47, s48, 0\nbuffer_load_dwordx4 a[64:67], v15, s[12:15], s47 offen sc1\n"
"buffer_load_dwordx4 a[68:71], v15, s[12:15], s47 offen offset:1024 sc1\ns_add_u32 s47, s48, 2048\nbuffer_load_dwordx4 a[72:75], v15, s[12:15], s47 offen sc1\nbuffer_load_dwordx4 a[76:79], v15, s[12:15], s47 offen offset:1024 sc1\n"
"s_add_u32 s47, s48, 4096\nbuffer_load_dwordx4 a[80:83], v15, s[12:15], s47 offen sc1\nbuffer_load_dwordx4 a[84:87], v15, s[12:15], s47 offen offset:1024 sc1\ns_add_u32 s47, s48, 6144\n"
"buffer_load_dwordx4 a[88:91], v15, s[12:15], s47 offen sc1\nbuffer_load_dwordx4 a[92:95], v15, s[12:15], s47 offen offset:1024 sc1\ns_add_u32 s47, s34, 3\ns_and_b32 s52, s47, 1\n"
"s_mul_i32 s52, s52, -14208\ns_mul_i32 s48, s47, 14336\ns_add_u32 s48, s48, s52\ns_add_u32 s47, s48, 0\n"
"buffer_load_dwordx4 a[96:99], v15, s[12:15], s47 offen sc1\nbuffer_load_dwordx4 a[100:103], v15, s[12:15], s47 offen offset:1024 sc1\ns_add_u32 s47, s48, 2048\nbuffer_load_dwordx4 a[104:107], v15, s[12:15], s47 offen sc1\n"
"buffer_load_dwordx4 a[108:111], v15, s[12:15], s47 offen offset:1024 sc1\ns_add_u32 s47, s48, 4096\nbuffer_load_dwordx4 a[112:115], v15, s[12:15], s47 offen sc1\nbuffer_load_dwordx4 a[116:119], v15, s[12:15], s47 offen offset:1024 sc1\n"
"s_add_u32 s47, s48, 6144\nbuffer_load_dwordx4 a[120:123], v15, s[12:15], s47 offen sc1\nbuffer_load_dwordx4 a[124:127], v15, s[12:15], s47 offen offset:1024 sc1\ns_add_u32 s47, s50, 7168\n"
"buffer_load_dwordx4 v[72:75], v11, s[16:19], s50 offen\nbuffer_load_dwordx4 v[76:79], v12, s[16:19], s50 offen\nbuffer_load_dwordx4 v[80:83], v11, s[16:19], s47 offen\nbuffer_load_dwordx4 v[84:87], v12, s[16:19], s47 offen\n"
"s_add_u32 s50, s50, 28672\ns_waitcnt vmcnt(0)\ns_barrier\ns_memrealtime s[54:55]\n"
"s_waitcnt lgkmcnt(0)\ns_add_u32 s44, s54, 16\ns_mov_b32 s45, 8\n.p2align 6\n"
"top_h0:\nv_mov_b32 v56, 0\nv_mov_b32 v57, 0\nv_mov_b32 v58, 0\n"
"v_mov_b32 v59, 0\nv_mov_b32 v60, 0\nv_mov_b32 v61, 0\nv_mov_b32 v62, 0\n"
"v_mov_b32 v63, 0\nv_mov_b32 v64, 0\nv_mov_b32 v65, 0\nv_mov_b32 v66, 0\n"
"v_mov_b32 v67, 0\nv_mov_b32 v68, 0\nv_mov_b32 v69, 0\nv_mov_b32 v70, 0\n"
"v_mov_b32 v71, 0\nv_mov_b32 v128, 0\ns_mul_i32 s48, s45, 6\ns_and_b32 s48, s48, -4\n"
"s_getpc_b64 s[60:61]\nspc_h0:\ns_sub_u32 s48, sgo_h0-spc_h0, s48\ns_add_u32 s60, s60, s48\n"
"s_addc_u32 s61, s61, 0\nsf_h0:\ns_memrealtime s[54:55]\ns_nop 2\n"
"s_memrealtime s[56:57]\ns_nop 2\ns_memrealtime s[58:59]\ns_waitcnt lgkmcnt(4)\n"
"s_sub_u32 s47, s54, s44\ns_add_u32 s48, s47, 12\ns_cmp_ge_u32 s48, 12\ns_cbranch_scc1 so_h0\n"
"s_memrealtime s[54:55]\ns_waitcnt lgkmcnt(4)\ns_cmp_ge_u32 s56, s44\ns_cbranch_scc1 sx_h0\n"
"s_memrealtime s[56:57]\ns_waitcnt lgkmcnt(4)\ns_cmp_ge_u32 s58, s44\ns_cbranch_scc1 sx_h0\n"
"s_memrealtime s[58:59]\ns_waitcnt lgkmcnt(4)\ns_cmp_ge_u32 s54, s44\ns_cbranch_scc1 sx_h0\n"
"s_memrealtime s[54:55]\ns_waitcnt lgkmcnt(4)\ns_cmp_ge_u32 s56, s44\ns_cbranch_scc1 sx_h0\n"
"s_memrealtime s[56:57]\ns_waitcnt lgkmcnt(4)\ns_cmp_ge_u32 s58, s44\ns_cbranch_scc1 sx_h0\n"
"s_memrealtime s[58:59]\ns_waitcnt lgkmcnt(4)\ns_cmp_ge_u32 s54, s44\ns_cbranch_scc1 sx_h0\n"
"s_memrealtime s[54:55]\ns_waitcnt lgkmcnt(4)\ns_cmp_ge_u32 s56, s44\ns_cbranch_scc1 sx_h0\n"
"s_memrealtime s[56:57]\ns_waitcnt lgkmcnt(4)\ns_cmp_ge_u32 s58, s44\ns_cbranch_scc1 sx_h0\n"
"s_memrealtime s[58:59]\ns_waitcnt lgkmcnt(4)\ns_cmp_ge_u32 s54, s44\ns_cbranch_scc1 sx_h0\n"
"s_memrealtime s[54:55]\ns_waitcnt lgkmcnt(4)\ns_cmp_ge_u32 s56, s44\ns_cbranch_scc1 sx_h0\n"
"s_memrealtime s[56:57]\ns_waitcnt lgkmcnt(4)\ns_cmp_ge_u32 s58, s44\ns_cbranch_scc1 sx_h0\n"
"s_memrealtime s[58:59]\ns_waitcnt lgkmcnt(4)\ns_cmp_ge_u32 s54, s44\ns_cbranch_scc1 sx_h0\n"
"s_memrealtime s[54:55]\ns_waitcnt lgkmcnt(4)\ns_cmp_ge_u32 s56, s44\ns_cbranch_scc1 sx_h0\n"
"s_memrealtime s[56:57]\ns_waitcnt lgkmcnt(4)\ns_cmp_ge_u32 s58, s44\ns_cbranch_scc1 sx_h0\n"
"s_memrealtime s[58:59]\ns_waitcnt lgkmcnt(4)\ns_cmp_ge_u32 s54, s44\ns_cbranch_scc1 sx_h0\n"
"s_memrealtime s[54:55]\ns_waitcnt lgkmcnt(4)\ns_cmp_ge_u32 s56, s44\ns_cbranch_scc1 sx_h0\n"
"s_memrealtime s[56:57]\ns_branch sx_h0\nso_h0:\ns_waitcnt lgkmcnt(0)\n"
"s_cmp_lt_i32 s47, 0\ns_cbranch_scc1 sf_h0\ns_branch sgo_h0\nsx_h0:\n"
"s_setpc_b64 s[60:61]\ns_nop 0\ns_nop 0\ns_nop 0\n"
"s_nop 0\ns_nop 0\ns_nop 0\ns_nop 0\n"
"s_nop 0\ns_nop 0\ns_nop 0\ns_nop 0\n"
"s_nop 0\ns_nop 0\ns_nop 0\ns_nop 0\n"
"s_nop 0\ns_nop 0\ns_nop 0\ns_nop 0\n"
"s_nop 0\ns_nop 0\ns_nop 0\ns_nop 0\n"
"s_nop 0\nsgo_h0:\nbuffer_load_dwordx4 v[40:43], v10, s[8:11], s43 offen offset:0 sc1\nbuffer_load_dwordx4 v[44:47], v10, s[8:11], s43 offen offset:1024 sc1\n"
"s_nop 15\ns_nop 3\nbuffer_load_dwordx4 v[48:51], v10, s[8:11], s43 offen offset:2048 sc1\ns_nop 13\n"
"buffer_load_dwordx4 v[52:55], v10, s[8:11], s43 offen offset:3072 sc1\nlanded_h0:\ns_waitcnt vmcnt(3)\nv_smfmac_f32_16x16x64_bf16 v[56:59], v[40:43], a[0:7], v8\n"
"v_max3_u32 v128, v128, v40, v41\nv_smfmac_f32_16x16x64_bf16 v[60:63], v[40:43], a[32:39], v8\nv_max3_u32 v128, v128, v42, v43\nv_smfmac_f32_16x16x64_bf16 v[64:67], v[40:43], a[64:71], v8\n"
"v_smfmac_f32_16x16x64_bf16 v[68:71], v[40:43], a[96:103], v8\ns_waitcnt vmcnt(2)\nv_smfmac_f32_16x16x64_bf16 v[56:59], v[44:47], a[8:15], v8\nv_max3_u32 v128, v128, v44, v45\n"
"v_smfmac_f32_16x16x64_bf16 v[60:63], v[44:47], a[40:47], v8\nv_max3_u32 v128, v128, v46, v47\nv_smfmac_f32_16x16x64_bf16 v[64:67], v[44:47], a[72:79], v8\nv_smfmac_f32_16x16x64_bf16 v[68:71], v[44:47], a[104:111], v8\n"
"s_waitcnt vmcnt(1)\nv_smfmac_f32_16x16x64_bf16 v[56:59], v[48:51], a[16:23], v8\nv_max3_u32 v128, v128, v48, v49\nv_smfmac_f32_16x16x64_bf16 v[60:63], v[48:51], a[48:55], v8\n"
"v_max3_u32 v128, v128, v50, v51\nv_smfmac_f32_16x16x64_bf16 v[64:67], v[48:51], a[80:87], v8\nv_smfmac_f32_16x16x64_bf16 v[68:71], v[48:51], a[112:119], v8\ns_waitcnt vmcnt(0)\n"
"v_smfmac_f32_16x16x64_bf16 v[56:59], v[52:55], a[24:31], v8\nv_max3_u32 v128, v128, v52, v53\nv_smfmac_f32_16x16x64_bf16 v[60:63], v[52:55], a[56:63], v8\nv_max3_u32 v128, v128, v54, v55\n"
"v_smfmac_f32_16x16x64_bf16 v[64:67], v[52:55], a[88:95], v8\nv_cmp_eq_u32 vcc, -1, v128\nv_smfmac_f32_16x16x64_bf16 v[68:71], v[52:55], a[120:127], v8\nv_add_f32 v129, v56, v57\n"
"v_add_f32 v130, v58, v59\nv_cvt_pk_bf16_f32 v112, v129, v130\nv_add_f32 v129, v60, v61\nv_add_f32 v130, v62, v63\n"
"v_cvt_pk_bf16_f32 v113, v129, v130\nv_add_f32 v129, v64, v65\nv_add_f32 v130, v66, v67\nv_cvt_pk_bf16_f32 v114, v129, v130\n"
"v_add_f32 v129, v68, v69\nv_add_f32 v130, v70, v71\nv_cvt_pk_bf16_f32 v115, v129, v130\nds_write2_b64 v131, v[112:113], v[114:115] offset0:0 offset1:16\n"
"s_cbranch_vccnz retry_h0\nv_mov_b32 v129, 0\ns_nop 1\nv_mov_b32 v129, 0\n"
"s_nop 1\ns_waitcnt lgkmcnt(0)\ns_barrier\ns_nop 9\n"
"ds_read_b64_tr_b16 v[88:89], v132 offset:0\nds_read_b64_tr_b16 v[90:91], v132 offset:32\nds_read_b64_tr_b16 v[92:93], v132 offset:256\nds_read_b64_tr_b16 v[94:95], v132 offset:288\n"
"ds_read_b64_tr_b16 v[96:97], v132 offset:512\nds_read_b64_tr_b16 v[98:99], v132 offset:544\nds_read_b64_tr_b16 v[100:101], v132 offset:768\nds_read_b64_tr_b16 v[102:103], v132 offset:800\n"
"s_waitcnt lgkmcnt(6)\nv_mfma_f32_16x16x32_bf16 v[104:107], v[88:91], v[72:75], 0\nv_mfma_f32_16x16x32_bf16 v[108:111], v[88:91], v[80:83], 0\ns_waitcnt lgkmcnt(4)\n"
"v_mfma_f32_16x16x32_bf16 v[104:107], v[92:95], v[76:79], v[104:107]\nv_mfma_f32_16x16x32_bf16 v[108:111], v[92:95], v[84:87], v[108:111]\ns_waitcnt lgkmcnt(2)\nv_mfma_f32_16x16x32_bf16 v[104:107], v[96:99], v[72:75], v[104:107]\n"
"v_mfma_f32_16x16x32_bf16 v[108:111], v[96:99], v[80:83], v[108:111]\ns_waitcnt lgkmcnt(0)\nv_mfma_f32_16x16x32_bf16 v[104:107], v[100:103], v[76:79], v[104:107]\nv_mfma_f32_16x16x32_bf16 v[108:111], v[100:103], v[84:87], v[108:111]\n"
"s_nop 7\nv_add_f32_dpp v124, v106, v104 row_ror:8 row_mask:0xf bank_mask:0x3\nv_add_f32_dpp v124, v108, v110 row_ror:8 row_mask:0xf bank_mask:0xc\nv_add_f32_dpp v125, v107, v105 row_ror:8 row_mask:0xf bank_mask:0x3\n"
"v_add_f32_dpp v125, v109, v111 row_ror:8 row_mask:0xf bank_mask:0xc\ns_add_u32 s47, s50, 7168\nbuffer_load_dwordx4 v[72:75], v11, s[16:19], s50 offen\nbuffer_load_dwordx4 v[76:79], v12, s[16:19], s50 offen\n"
"buffer_load_dwordx4 v[80:83], v11, s[16:19], s47 offen\nbuffer_load_dwordx4 v[84:87], v12, s[16:19], s47 offen\ns_add_u32 s50, s50, 28672\nbuffer_store_dwordx2 v[124:125], v16, s[20:23], s51 offen sc1\n"
"s_add_u32 s51, s51, 1024\ns_add_u32 s47, s40, s33\nv_mov_b32 v135, s47\nbuffer_store_dword v135, v18, s[8:11], 0 offen sc1\n"
"ds_read_b32 v133, v134\nv_xor_b32 v131, 8192, v131\nv_xor_b32 v132, 8192, v132\ns_waitcnt lgkmcnt(0)\n"
"v_readfirstlane_b32 s47, v133\ns_sub_u32 s47, s47, s40\ns_cmp_eq_u32 s47, 1\ns_cbranch_scc1 adopt_h0\n"
"back_h0:\ns_add_u32 s40, s40, 1\ns_mov_b32 s43, s41\ns_add_u32 s41, s41, 131072\n"
"s_and_b32 s41, s41, 524287\ns_xor_b32 s42, s41, 262144\ns_and_b32 s47, s46, 15\ns_mul_i32 s48, s47, 8\n"
"s_add_u32 s45, s45, s48\ns_add_u32 s45, s45, s30\ns_lshr_b32 s48, s45, 4\ns_lshl_b32 s48, s48, 2\n"
"s_add_u32 s44, s44, s48\ns_and_b32 s45, s45, 15\ns_add_u32 s92, s92, 1\ns_waitcnt lgkmcnt(0)\n"
"s_cmp_lt_u32 s40, 64\ns_cbranch_scc1 top_h0\ns_add_u32 s47, s33, 64\nv_mov_b32 v135, s47\n"
"s_waitcnt vmcnt(0)\nbuffer_store_dword v135, v18, s[8:11], 0 offen sc1\ns_branch end_h0\nretry2_h0:\n"
"s_sleep 3\nretry_h0:\ns_add_u32 s53, s53, 1\ns_add_u32 s49, s49, 1\n"
"s_cmp_gt_u32 s49, 65536\ns_cbranch_scc1 end_h0\nbuffer_load_dwordx4 v[40:43], v10, s[8:11], s43 offen offset:0 sc1\nbuffer_load_dwordx4 v[44:47], v10, s[8:11], s43 offen offset:1024 sc1\n"
"buffer_load_dwordx4 v[48:51], v10, s[8:11], s43 offen offset:2048 sc1\nbuffer_load_dwordx4 v[52:55], v10, s[8:11], s43 offen offset:3072 sc1\nv_mov_b32 v128, 0\ns_add_u32 s47, s40, 1\n"
"v_mov_b32 v133, s47\nds_write_b32 v134, v133\ns_waitcnt vmcnt(0)\nv_max3_u32 v128, v128, v40, v41\n"
"v_max3_u32 v128, v128, v42, v43\nv_max3_u32 v128, v128, v44, v45\nv_max3_u32 v128, v128, v46, v47\nv_max3_u32 v128, v128, v48, v49\n"
"v_max3_u32 v128, v128, v50, v51\nv_max3_u32 v128, v128, v52, v53\nv_max3_u32 v128, v128, v54, v55\nv_cmp_eq_u32 vcc, -1, v128\n"
"v_mov_b32 v56, 0\nv_mov_b32 v57, 0\nv_mov_b32 v58, 0\nv_mov_b32 v59, 0\n"
"v_mov_b32 v60, 0\nv_mov_b32 v61, 0\nv_mov_b32 v62, 0\nv_mov_b32 v63, 0\n"
"v_mov_b32 v64, 0\nv_mov_b32 v65, 0\nv_mov_b32 v66, 0\nv_mov_b32 v67, 0\n"
"v_mov_b32 v68, 0\nv_mov_b32 v69, 0\nv_mov_b32 v70, 0\nv_mov_b32 v71, 0\n"
"s_cbranch_vccnz retry2_h0\ns_branch landed_h0\nadopt_h0:\nbuffer_load_dwordx4 v[116:119], v28, s[8:11], 0 offen sc1\n"
"s_lshl_b32 s52, s40, 26\ns_or_b32 s47, s52, 0x3ffffff\ns_waitcnt vmcnt(0)\nv_cmp_ge_u32 vcc, s47, v116\n"
"s_nop 1\nv_cndmask_b32 v116, 0, v116, vcc\nv_cmp_ge_u32 vcc, s47, v117\ns_nop 1\n"
"v_cndmask_b32 v117, 0, v117, vcc\nv_cmp_ge_u32 vcc, s47, v118\ns_nop 1\nv_cndmask_b32 v118, 0, v118, vcc\n"
"v_cmp_ge_u32 vcc, s47, v119\ns_nop 1\nv_cndmask_b32 v119, 0, v119, vcc\nv_max3_u32 v133, v116, v117, v118\n"
"v_max_u32 v133, v133, v119\ns_nop 1\nv_max_u32_dpp v133, v133, v133 row_ror:8 row_mask:0xf bank_mask:0xf\ns_nop 1\n"
"v_max_u32_dpp v133, v133, v133 row_ror:4 row_mask:0xf bank_mask:0xf\ns_nop 1\nv_max_u32_dpp v133, v133, v133 row_ror:2 row_mask:0xf bank_mask:0xf\ns_nop 1\n"
"v_max_u32_dpp v133, v133, v133 row_ror:1 row_mask:0xf bank_mask:0xf\ns_nop 0\nv_readlane_b32 s47, v133, 0\nv_readlane_b32 s48, v133, 16\n"
"s_max_u32 s48, s48, s47\ns_cmp_le_u32 s48, s52\ns_cbranch_scc1 back_h0\ns_bfe_u32 s52, s48, 1179656\n"
"s_add_u32 s44, s52, s28\ns_mov_b32 s45, 0\ns_and_b32 s46, s48, 255\ns_mov_b32 s92, 0\n"
"s_branch back_h0\nend_h0:\ns_branch fk_done\nfk_h1:\n"
"s_mov_b32 s40, 0\ns_mov_b32 s41, 0\ns_mov_b32 s42, 262144\ns_mov_b32 s43, 393216\n"
"s_mov_b32 s44, s28\ns_mov_b32 s45, 0\ns_mov_b32 s46, 0\ns_mov_b32 s92, 0\n"
"s_mov_b32 s49, 0\ns_mov_b32 s53, 0\ns_mov_b32 s50, s35\ns_mov_b32 s51, s36\n"
"v_mov_b32 v131, v13\nv_mov_b32 v132, v14\nv_mov_b32 v134, 24576\nv_mov_b32 v115, 0\n"
"v_mov_b32 v120, -1\nv_mov_b32 v121, -1\nv_mov_b32 v122, -1\nv_mov_b32 v123, -1\n"
"s_add_u32 s47, s34, 4\ns_and_b32 s52, s47, 1\ns_mul_i32 s52, s52, -14208\ns_mul_i32 s48, s47, 14336\n"
"s_add_u32 s48, s48, s52\ns_add_u32 s47, s48, 2048\nbuffer_load_dwordx4 a[8:11], v15, s[12:15], s47 offen sc1\nbuffer_load_dwordx4 a[12:15], v15, s[12:15], s47 offen offset:1024 sc1\n"
"s_add_u32 s47, s48, 0\nbuffer_load_dwordx4 a[0:3], v15, s[12:15], s47 offen sc1\nbuffer_load_dwordx4 a[4:7], v15, s[12:15], s47 offen offset:1024 sc1\ns_add_u32 s47, s48, 6144\n"
"buffer_load_dwordx4 a[24:27], v15, s[12:15], s47 offen sc1\nbuffer_load_dwordx4 a[28:31], v15, s[12:15], s47 offen offset:1024 sc1\ns_add_u32 s47, s48, 4096\nbuffer_load_dwordx4 a[16:19], v15, s[12:15], s47 offen sc1\n"
"buffer_load_dwordx4 a[20:23], v15, s[12:15], s47 offen offset:1024 sc1\ns_add_u32 s47, s34, 5\ns_and_b32 s52, s47, 1\ns_mul_i32 s52, s52, -14208\n"
"s_mul_i32 s48, s47, 14336\ns_add_u32 s48, s48, s52\ns_add_u32 s47, s48, 2048\nbuffer_load_dwordx4 a[40:43], v15, s[12:15], s47 offen sc1\n"
"buffer_load_dwordx4 a[44:47], v15, s[12:15], s47 offen offset:1024 sc1\ns_add_u32 s47, s48, 0\nbuffer_load_dwordx4 a[32:35], v15, s[12:15], s47 offen sc1\nbuffer_load_dwordx4 a[36:39], v15, s[12:15], s47 offen offset:1024 sc1\n"
"s_add_u32 s47, s48, 6144\nbuffer_load_dwordx4 a[56:59], v15, s[12:15], s47 offen sc1\nbuffer_load_dwordx4 a[60:63], v15, s[12:15], s47 offen offset:1024 sc1\ns_add_u32 s47, s48, 4096\n"
"buffer_load_dwordx4 a[48:51], v15, s[12:15], s47 offen sc1\nbuffer_load_dwordx4 a[52:55], v15, s[12:15], s47 offen offset:1024 sc1\ns_add_u32 s47, s34, 6\ns_and_b32 s52, s47, 1\n"
"s_mul_i32 s52, s52, -14208\ns_mul_i32 s48, s47, 14336\ns_add_u32 s48, s48, s52\ns_add_u32 s47, s48, 2048\n"
"buffer_load_dwordx4 a[72:75], v15, s[12:15], s47 offen sc1\nbuffer_load_dwordx4 a[76:79], v15, s[12:15], s47 offen offset:1024 sc1\ns_add_u32 s47, s48, 0\nbuffer_load_dwordx4 a[64:67], v15, s[12:15], s47 offen sc1\n"
"buffer_load_dwordx4 a[68:71], v15, s[12:15], s47 offen offset:1024 sc1\ns_add_u32 s47, s48, 6144\nbuffer_load_dwordx4 a[88:91], v15, s[12:15], s47 offen sc1\nbuffer_load_dwordx4 a[92:95], v15, s[12:15], s47 offen offset:1024 sc1\n"
"s_add_u32 s47, s48, 4096\nbuffer_load_dwordx4 a[80:83], v15, s[12:15], s47 offen sc1\nbuffer_load_dwordx4 a[84:87], v15, s[12:15], s47 offen offset:1024 sc1\ns_add_u32 s47, s50, 7168\n"
"buffer_load_dwordx4 v[72:75], v11, s[16:19], s50 offen\nbuffer_load_dwordx4 v[76:79], v12, s[16:19], s50 offen\nbuffer_load_dwordx4 v[80:83], v11, s[16:19], s47 offen\nbuffer_load_dwordx4 v[84:87], v12, s[16:19], s47 offen\n"
"s_add_u32 s50, s50, 28672\ns_waitcnt vmcnt(0)\ns_barrier\ns_memrealtime s[54:55]\n"
"s_waitcnt lgkmcnt(0)\ns_add_u32 s44, s54, 16\ns_mov_b32 s45, 8\n.p2align 6\n"
"top_h1:\nv_mov_b32 v56, 0\nv_mov_b32 v57, 0\nv_mov_b32 v58, 0\n"
"v_mov_b32 v59, 0\nv_mov_b32 v60, 0\nv_mov_b32 v61, 0\nv_mov_b32 v62, 0\n"
"v_mov_b32 v63, 0\nv_mov_b32 v64, 0\nv_mov_b32 v65, 0\nv_mov_b32 v66, 0\n"
"v_mov_b32 v67, 0\nv_mov_b32 v128, 0\ns_mul_i32 s48, s45, 6\ns_and_b32 s48, s48, -4\n"
"s_getpc_b64 s[60:61]\nspc_h1:\ns_sub_u32 s48, sgo_h1-spc_h1, s48\ns_add_u32 s60, s60, s48\n"
"s_addc_u32 s61, s61, 0\nsf_h1:\ns_memrealtime s[54:55]\ns_nop 2\n"
"s_memrealtime s[56:57]\ns_nop 2\ns_memrealtime s[58:59]\ns_waitcnt lgkmcnt(4)\n"
"s_sub_u32 s47, s54, s44\ns_add_u32 s48, s47, 12\ns_cmp_ge_u32 s48, 12\ns_cbranch_scc1 so_h1\n"
"s_memrealtime s[54:55]\ns_waitcnt lgkmcnt(4)\ns_cmp_ge_u32 s56, s44\ns_cbranch_scc1 sx_h1\n"
"s_memrealtime s[56:57]\ns_waitcnt lgkmcnt(4)\ns_cmp_ge_u32 s58, s44\ns_cbranch_scc1 sx_h1\n"
"s_memrealtime s[58:59]\ns_waitcnt lgkmcnt(4)\ns_cmp_ge_u32 s54, s44\ns_cbranch_scc1 sx_h1\n"
"s_memrealtime s[54:55]\ns_waitcnt lgkmcnt(4)\ns_cmp_ge_u32 s56, s44\ns_cbranch_scc1 sx_h1\n"
"s_memrealtime s[56:57]\ns_waitcnt lgkmcnt(4)\ns_cmp_ge_u32 s58, s44\ns_cbranch_scc1 sx_h1\n"
"s_memrealtime s[58:59]\ns_waitcnt lgkmcnt(4)\ns_cmp_ge_u32 s54, s44\ns_cbranch_scc1 sx_h1\n"
"s_memrealtime s[54:55]\ns_waitcnt lgkmcnt(4)\ns_cmp_ge_u32 s56, s44\ns_cbranch_scc1 sx_h1\n"
"s_memrealtime s[56:57]\ns_waitcnt lgkmcnt(4)\ns_cmp_ge_u32 s58, s44\ns_cbranch_scc1 sx_h1\n"
"s_memrealtime s[58:59]\ns_waitcnt lgkmcnt(4)\ns_cmp_ge_u32 s54, s44\ns_cbranch_scc1 sx_h1\n"
"s_memrealtime s[54:55]\ns_waitcnt lgkmcnt(4)\ns_cmp_ge_u32 s56, s44\ns_cbranch_scc1 sx_h1\n"
"s_memrealtime s[56:57]\ns_waitcnt lgkmcnt(4)\ns_cmp_ge_u32 s58, s44\ns_cbranch_scc1 sx_h1\n"
"s_memrealtime s[58:59]\ns_waitcnt lgkmcnt(4)\ns_cmp_ge_u32 s54, s44\ns_cbranch_scc1 sx_h1\n"
"s_memrealtime s[54:55]\ns_waitcnt lgkmcnt(4)\ns_cmp_ge_u32 s56, s44\ns_cbranch_scc1 sx_h1\n"
"s_memrealtime s[56:57]\ns_waitcnt lgkmcnt(4)\ns_cmp_ge_u32 s58, s44\ns_cbranch_scc1 sx_h1\n"
"s_memrealtime s[58:59]\ns_waitcnt lgkmcnt(4)\ns_cmp_ge_u32 s54, s44\ns_cbranch_scc1 sx_h1\n"
"s_memrealtime s[54:55]\ns_waitcnt lgkmcnt(4)\ns_cmp_ge_u32 s56, s44\ns_cbranch_scc1 sx_h1\n"
"s_memrealtime s[56:57]\ns_branch sx_h1\nso_h1:\ns_waitcnt lgkmcnt(0)\n"
"s_cmp_lt_i32 s47, 0\ns_cbranch_scc1 sf_h1\ns_branch sgo_h1\nsx_h1:\n"
"s_setpc_b64 s[60:61]\ns_nop 0\ns_nop 0\ns_nop 0\n"
"s_nop 0\ns_nop 0\ns_nop 0\ns_nop 0\n"
"s_nop 0\ns_nop 0\ns_nop 0\ns_nop 0\n"
"s_nop 0\ns_nop 0\ns_nop 0\ns_nop 0\n"
"s_nop 0\ns_nop 0\ns_nop 0\ns_nop 0\n"
"s_nop 0\ns_nop 0\ns_nop 0\ns_nop 0\n"
"s_nop 0\nsgo_h1:\ns_nop 4\nbuffer_load_dwordx4 v[40:43], v10, s[8:11], s43 offen offset:1024 sc1\n"
"s_nop 4\nbuffer_load_dwordx4 v[44:47], v10, s[8:11], s43 offen offset:0 sc1\ns_nop 13\nbuffer_load_dwordx4 v[48:51], v10, s[8:11], s43 offen offset:3072 sc1\n"
"s_nop 13\nbuffer_load_dwordx4 v[52:55], v10, s[8:11], s43 offen offset:2048 sc1\nlanded_h1:\ns_waitcnt vmcnt(3)\n"
"v_smfmac_f32_16x16x64_bf16 v[56:59], v[40:43], a[8:15], v8\nv_max3_u32 v128, v128, v40, v41\nv_smfmac_f32_16x16x64_bf16 v[60:63], v[40:43], a[40:47], v8\nv_max3_u32 v128, v128, v42, v43\n"
"v_smfmac_f32_16x16x64_bf16 v[64:67], v[40:43], a[72:79], v8\ns_waitcnt vmcnt(2)\nv_smfmac_f32_16x16x64_bf16 v[56:59], v[44:47], a[0:7], v8\nv_max3_u32 v128, v128, v44, v45\n"
"v_smfmac_f32_16x16x64_bf16 v[60:63], v[44:47], a[32:39], v8\nv_max3_u32 v128, v128, v46, v47\nv_smfmac_f32_16x16x64_bf16 v[64:67], v[44:47], a[64:71], v8\ns_waitcnt vmcnt(1)\n"
"v_smfmac_f32_16x16x64_bf16 v[56:59], v[48:51], a[24:31], v8\nv_max3_u32 v128, v128, v48, v49\nv_smfmac_f32_16x16x64_bf16 v[60:63], v[48:51], a[56:63], v8\nv_max3_u32 v128, v128, v50, v51\n"
"v_smfmac_f32_16x16x64_bf16 v[64:67], v[48:51], a[88:95], v8\ns_waitcnt vmcnt(0)\nv_smfmac_f32_16x16x64_bf16 v[56:59], v[52:55], a[16:23], v8\nv_max3_u32 v128, v128, v52, v53\n"
"v_smfmac_f32_16x16x64_bf16 v[60:63], v[52:55], a[48:55], v8\nv_max3_u32 v128, v128, v54, v55\nv_smfmac_f32_16x16x64_bf16 v[64:67], v[52:55], a[80:87], v8\nv_add_f32 v129, v56, v57\n"
"v_add_f32 v130, v58, v59\nv_cvt_pk_bf16_f32 v112, v129, v130\nv_add_f32 v129, v60, v61\nv_add_f32 v130, v62, v63\n"
"v_cvt_pk_bf16_f32 v113, v129, v130\nv_cmp_eq_u32 vcc, -1, v128\nv_add_f32 v129, v64, v65\nv_add_f32 v130, v66, v67\n"
"v_cvt_pk_bf16_f32 v114, v129, v130\nds_write2_b64 v131, v[112:113], v[114:115] offset0:32 offset1:48\ns_cbranch_vccnz retry_h1\nv_mov_b32 v129, 0\n"
"s_nop 1\nv_mov_b32 v129, 0\ns_nop 1\ns_waitcnt lgkmcnt(0)\n"
"s_barrier\ns_nop 9\nds_read_b64_tr_b16 v[88:89], v132 offset:0\nds_read_b64_tr_b16 v[90:91], v132 offset:32\n"
"ds_read_b64_tr_b16 v[92:93], v132 offset:256\nds_read_b64_tr_b16 v[94:95], v132 offset:288\nds_read_b64_tr_b16 v[96:97], v132 offset:512\nds_read_b64_tr_b16 v[98:99], v132 offset:544\n"
"ds_read_b64_tr_b16 v[100:101], v132 offset:768\nds_read_b64_tr_b16 v[102:103], v132 offset:800\ns_waitcnt lgkmcnt(6)\nv_mfma_f32_16x16x32_bf16 v[104:107], v[88:91], v[72:75], 0\n"
"v_mfma_f32_16x16x32_bf16 v[108:111], v[88:91], v[80:83], 0\ns_waitcnt lgkmcnt(4)\nv_mfma_f32_16x16x32_bf16 v[104:107], v[92:95], v[76:79], v[104:107]\nv_mfma_f32_16x16x32_bf16 v[108:111], v[92:95], v[84:87], v[108:111]\n"
"s_waitcnt lgkmcnt(2)\nv_mfma_f32_16x16x32_bf16 v[104:107], v[96:99], v[72:75], v[104:107]\nv_mfma_f32_16x16x32_bf16 v[108:111], v[96:99], v[80:83], v[108:111]\ns_waitcnt lgkmcnt(0)\n"
"v_mfma_f32_16x16x32_bf16 v[104:107], v[100:103], v[76:79], v[104:107]\nv_mfma_f32_16x16x32_bf16 v[108:111], v[100:103], v[84:87], v[108:111]\ns_nop 7\nv_add_f32_dpp v124, v106, v104 row_ror:8 row_mask:0xf bank_mask:0x3\n"
"v_add_f32_dpp v124, v108, v110 row_ror:8 row_mask:0xf bank_mask:0xc\nv_add_f32_dpp v125, v107, v105 row_ror:8 row_mask:0xf bank_mask:0x3\nv_add_f32_dpp v125, v109, v111 row_ror:8 row_mask:0xf bank_mask:0xc\ns_add_u32 s47, s50, 7168\n"
"buffer_load_dwordx4 v[72:75], v11, s[16:19], s50 offen\nbuffer_load_dwordx4 v[76:79], v12, s[16:19], s50 offen\nbuffer_load_dwordx4 v[80:83], v11, s[16:19], s47 offen\nbuffer_load_dwordx4 v[84:87], v12, s[16:19], s47 offen\n"
"s_add_u32 s50, s50, 28672\nbuffer_store_dwordx2 v[124:125], v16, s[20:23], s51 offen sc1\ns_add_u32 s51, s51, 1024\nds_read_b32 v133, v134\n"
"v_xor_b32 v131, 8192, v131\nv_xor_b32 v132, 8192, v132\ns_waitcnt lgkmcnt(0)\nv_readfirstlane_b32 s47, v133\n"
"s_sub_u32 s47, s47, s40\ns_cmp_eq_u32 s47, 1\ns_cbranch_scc1 adopt_h1\nback_h1:\n"
"s_add_u32 s40, s40, 1\ns_mov_b32 s43, s41\ns_add_u32 s41, s41, 131072\ns_and_b32 s41, s41, 524287\n"
"s_xor_b32 s42, s41, 262144\ns_and_b32 s47, s46, 15\ns_mul_i32 s48, s47, 8\ns_add_u32 s45, s45, s48\n"
"s_add_u32 s45, s45, s30\ns_lshr_b32 s48, s45, 4\ns_lshl_b32 s48, s48, 2\ns_add_u32 s44, s44, s48\n"
"s_and_b32 s45, s45, 15\ns_add_u32 s92, s92, 1\ns_waitcnt lgkmcnt(0)\ns_cmp_lt_u32 s40, 64\n"
"s_cbranch_scc1 top_h1\ns_add_u32 s47, s33, 64\nv_mov_b32 v135, s47\ns_waitcnt vmcnt(0)\n"
"buffer_store_dword v135, v18, s[8:11], 0 offen sc1\ns_branch end_h1\nretry2_h1:\ns_sleep 3\n"
"retry_h1:\ns_add_u32 s53, s53, 1\ns_add_u32 s49, s49, 1\ns_cmp_gt_u32 s49, 65536\n"
"s_cbranch_scc1 end_h1\nbuffer_load_dwordx4 v[40:43], v10, s[8:11], s43 offen offset:1024 sc1\nbuffer_load_dwordx4 v[44:47], v10, s[8:11], s43 offen offset:0 sc1\nbuffer_load_dwordx4 v[48:51], v10, s[8:11], s43 offen offset:3072 sc1\n"
"buffer_load_dwordx4 v[52:55], v10, s[8:11], s43 offen offset:2048 sc1\nv_mov_b32 v128, 0\ns_add_u32 s47, s40, 1\nv_mov_b32 v133, s47\n"
"ds_write_b32 v134, v133\ns_waitcnt vmcnt(0)\nv_max3_u32 v128, v128, v40, v41\nv_max3_u32 v128, v128, v42, v43\n"
"v_max3_u32 v128, v128, v44, v45\nv_max3_u32 v128, v128, v46, v47\nv_max3_u32 v128, v128, v48, v49\nv_max3_u32 v128, v128, v50, v51\n"
"v_max3_u32 v128, v128, v52, v53\nv_max3_u32 v128, v128, v54, v55\nv_cmp_eq_u32 vcc, -1, v128\nv_mov_b32 v56, 0\n"
"v_mov_b32 v57, 0\nv_mov_b32 v58, 0\nv_mov_b32 v59, 0\nv_mov_b32 v60, 0\n"
"v_mov_b32 v61, 0\nv_mov_b32 v62, 0\nv_mov_b32 v63, 0\nv_mov_b32 v64, 0\n"
"v_mov_b32 v65, 0\nv_mov_b32 v66, 0\nv_mov_b32 v67, 0\ns_cbranch_vccnz retry2_h1\n"
"s_branch landed_h1\nadopt_h1:\nbuffer_load_dwordx4 v[116:119], v28, s[8:11], 0 offen sc1\ns_lshl_b32 s52, s40, 26\n"
"s_or_b32 s47, s52, 0x3ffffff\ns_waitcnt vmcnt(0)\nv_cmp_ge_u32 vcc, s47, v116\ns_nop 1\n"
"v_cndmask_b32 v116, 0, v116, vcc\nv_cmp_ge_u32 vcc, s47, v117\ns_nop 1\nv_cndmask_b32 v117, 0, v117, vcc\n"
"v_cmp_ge_u32 vcc, s47, v118\ns_nop 1\nv_cndmask_b32 v118, 0, v118, vcc\nv_cmp_ge_u32 vcc, s47, v119\n"
"s_nop 1\nv_cndmask_b32 v119, 0, v119, vcc\nv_max3_u32 v133, v116, v117, v118\nv_max_u32 v133, v133, v119\n"
"s_nop 1\nv_max_u32_dpp v133, v133, v133 row_ror:8 row_mask:0xf bank_mask:0xf\ns_nop 1\nv_max_u32_dpp v133, v133, v133 row_ror:4 row_mask:0xf bank_mask:0xf\n"
"s_nop 1\nv_max_u32_dpp v133, v133, v133 row_ror:2 row_mask:0xf bank_mask:0xf\ns_nop 1\nv_max_u32_dpp v133, v133, v133 row_ror:1 row_mask:0xf bank_mask:0xf\n"
"s_nop 0\nv_readlane_b32 s47, v133, 0\nv_readlane_b32 s48, v133, 16\ns_max_u32 s48, s48, s47\n"
"s_cmp_le_u32 s48, s52\ns_cbranch_scc1 back_h1\ns_bfe_u32 s52, s48, 1179656\ns_add_u32 s44, s52, s28\n"
"s_mov_b32 s45, 0\ns_and_b32 s46, s48, 255\ns_mov_b32 s92, 0\ns_branch back_h1\n"
"end_h1:\ns_branch fk_done\nfk_h2:\ns_mov_b32 s40, 0\n"
"s_mov_b32 s41, 0\ns_mov_b32 s42, 262144\ns_mov_b32 s43, 393216\ns_mov_b32 s44, s28\n"
"s_mov_b32 s45, 0\ns_mov_b32 s46, 0\ns_mov_b32 s92, 0\ns_mov_b32 s49, 0\n"
"s_mov_b32 s53, 0\ns_mov_b32 s50, s35\ns_mov_b32 s51, s36\nv_mov_b32 v127, v13\n"
"v_mov_b32 v128, v14\nv_mov_b32 v130, 24576\nv_mov_b32 v111, 0\nv_mov_b32 v116, -1\n"
"v_mov_b32 v117, -1\nv_mov_b32 v118, -1\nv_mov_b32 v119, -1\ns_add_u32 s47, s34, 0\n"
"s_and_b32 s52, s47, 1\ns_mul_i32 s52, s52, -14208\ns_mul_i32 s48, s47, 14336\ns_add_u32 s48, s48, s52\n"
"s_add_u32 s47, s48, 8192\nbuffer_load_dwordx4 a[0:3], v15, s[12:15], s47 offen sc1\nbuffer_load_dwordx4 a[4:7], v15, s[12:15], s47 offen offset:1024 sc1\ns_add_u32 s47, s48, 10240\n"
"buffer_load_dwordx4 a[8:11], v15, s[12:15], s47 offen sc1\nbuffer_load_dwordx4 a[12:15], v15, s[12:15], s47 offen offset:1024 sc1\ns_add_u32 s47, s48, 12288\nbuffer_load_dwordx4 a[16:19], v15, s[12:15], s47 offen sc1\n"
"buffer_load_dwordx4 a[20:23], v15, s[12:15], s47 offen offset:1024 sc1\ns_add_u32 s47, s34, 1\ns_and_b32 s52, s47, 1\ns_mul_i32 s52, s52, -14208\n"
"s_mul_i32 s48, s47, 14336\ns_add_u32 s48, s48, s52\ns_add_u32 s47, s48, 8192\nbuffer_load_dwordx4 a[32:35], v15, s[12:15], s47 offen sc1\n"
"buffer_load_dwordx4 a[36:39], v15, s[12:15], s47 offen offset:1024 sc1\ns_add_u32 s47, s48, 10240\nbuffer_load_dwordx4 a[40:43], v15, s[12:15], s47 offen sc1\nbuffer_load_dwordx4 a[44:47], v15, s[12:15], s47 offen offset:1024 sc1\n"
"s_add_u32 s47, s48, 12288\nbuffer_load_dwordx4 a[48:51], v15, s[12:15], s47 offen sc1\nbuffer_load_dwordx4 a[52:55], v15, s[12:15], s47 offen offset:1024 sc1\ns_add_u32 s47, s34, 2\n"
"s_and_b32 s52, s47, 1\ns_mul_i32 s52, s52, -14208\ns_mul_i32 s48, s47, 14336\ns_add_u32 s48, s48, s52\n"
"s_add_u32 s47, s48, 8192\nbuffer_load_dwordx4 a[64:67], v15, s[12:15], s47 offen sc1\nbuffer_load_dwordx4 a[68:71], v15, s[12:15], s47 offen offset:1024 sc1\ns_add_u32 s47, s48, 10240\n"
"buffer_load_dwordx4 a[72:75], v15, s[12:15], s47 offen sc1\nbuffer_load_dwordx4 a[76:79], v15, s[12:15], s47 offen offset:1024 sc1\ns_add_u32 s47, s48, 12288\nbuffer_load_dwordx4 a[80:83], v15, s[12:15], s47 offen sc1\n"
"buffer_load_dwordx4 a[84:87], v15, s[12:15], s47 offen offset:1024 sc1\ns_add_u32 s47, s34, 3\ns_and_b32 s52, s47, 1\ns_mul_i32 s52, s52, -14208\n"
"s_mul_i32 s48, s47, 14336\ns_add_u32 s48, s48, s52\ns_add_u32 s47, s48, 8192\nbuffer_load_dwordx4 a[96:99], v15, s[12:15], s47 offen sc1\n"
"buffer_load_dwordx4 a[100:103], v15, s[12:15], s47 offen offset:1024 sc1\ns_add_u32 s47, s48, 10240\nbuffer_load_dwordx4 a[104:107], v15, s[12:15], s47 offen sc1\nbuffer_load_dwordx4 a[108:111], v15, s[12:15], s47 offen offset:1024 sc1\n"
"s_add_u32 s47, s48, 12288\nbuffer_load_dwordx4 a[112:115], v15, s[12:15], s47 offen sc1\nbuffer_load_dwordx4 a[116:119], v15, s[12:15], s47 offen offset:1024 sc1\ns_waitcnt vmcnt(0)\n"
"s_barrier\ns_memrealtime s[54:55]\ns_waitcnt lgkmcnt(0)\ns_add_u32 s44, s54, 16\n"
"s_mov_b32 s45, 8\n.p2align 6\ntop_h2:\nv_mov_b32 v52, 0\n"
"v_mov_b32 v53, 0\nv_mov_b32 v54, 0\nv_mov_b32 v55, 0\nv_mov_b32 v56, 0\n"
"v_mov_b32 v57, 0\nv_mov_b32 v58, 0\nv_mov_b32 v59, 0\nv_mov_b32 v60, 0\n"
"v_mov_b32 v61, 0\nv_mov_b32 v62, 0\nv_mov_b32 v63, 0\nv_mov_b32 v64, 0\n"
"v_mov_b32 v65, 0\nv_mov_b32 v66, 0\nv_mov_b32 v67, 0\nv_mov_b32 v124, 0\n"
"s_mul_i32 s48, s45, 6\ns_and_b32 s48, s48, -4\ns_getpc_b64 s[60:61]\nspc_h2:\n"
"s_sub_u32 s48, sgo_h2-spc_h2, s48\ns_add_u32 s60, s60, s48\ns_addc_u32 s61, s61, 0\nsf_h2:\n"
"s_memrealtime s[54:55]\ns_nop 2\ns_memrealtime s[56:57]\ns_nop 2\n"
"s_memrealtime s[58:59]\ns_waitcnt lgkmcnt(4)\ns_sub_u32 s47, s54, s44\ns_add_u32 s48, s47, 12\n"
"s_cmp_ge_u32 s48, 12\ns_cbranch_scc1 so_h2\ns_memrealtime s[54:55]\ns_waitcnt lgkmcnt(4)\n"
"s_cmp_ge_u32 s56, s44\ns_cbranch_scc1 sx_h2\ns_memrealtime s[56:57]\ns_waitcnt lgkmcnt(4)\n"
"s_cmp_ge_u32 s58, s44\ns_cbranch_scc1 sx_h2\ns_memrealtime s[58:59]\ns_waitcnt lgkmcnt(4)\n"
"s_cmp_ge_u32 s54, s44\ns_cbranch_scc1 sx_h2\ns_memrealtime s[54:55]\ns_waitcnt lgkmcnt(4)\n"
"s_cmp_ge_u32 s56, s44\ns_cbranch_scc1 sx_h2\ns_memrealtime s[56:57]\ns_waitcnt lgkmcnt(4)\n"
"s_cmp_ge_u32 s58, s44\ns_cbranch_scc1 sx_h2\ns_memrealtime s[58:59]\ns_waitcnt lgkmcnt(4)\n"
"s_cmp_ge_u32 s54, s44\ns_cbranch_scc1 sx_h2\ns_memrealtime s[54:55]\ns_waitcnt lgkmcnt(4)\n"
"s_cmp_ge_u32 s56, s44\ns_cbranch_scc1 sx_h2\ns_memrealtime s[56:57]\ns_waitcnt lgkmcnt(4)\n"
"s_cmp_ge_u32 s58, s44\ns_cbranch_scc1 sx_h2\ns_memrealtime s[58:59]\ns_waitcnt lgkmcnt(4)\n"
"s_cmp_ge_u32 s54, s44\ns_cbranch_scc1 sx_h2\ns_memrealtime s[54:55]\ns_waitcnt lgkmcnt(4)\n"
"s_cmp_ge_u32 s56, s44\ns_cbranch_scc1 sx_h2\ns_memrealtime s[56:57]\ns_waitcnt lgkmcnt(4)\n"
"s_cmp_ge_u32 s58, s44\ns_cbranch_scc1 sx_h2\ns_memrealtime s[58:59]\ns_waitcnt lgkmcnt(4)\n"
"s_cmp_ge_u32 s54, s44\ns_cbranch_scc1 sx_h2\ns_memrealtime s[54:55]\ns_waitcnt lgkmcnt(4)\n"
"s_cmp_ge_u32 s56, s44\ns_cbranch_scc1 sx_h2\ns_memrealtime s[56:57]\ns_waitcnt lgkmcnt(4)\n"
"s_cmp_ge_u32 s58, s44\ns_cbranch_scc1 sx_h2\ns_memrealtime s[58:59]\ns_waitcnt lgkmcnt(4)\n"
"s_cmp_ge_u32 s54, s44\ns_cbranch_scc1 sx_h2\ns_memrealtime s[54:55]\ns_waitcnt lgkmcnt(4)\n"
"s_cmp_ge_u32 s56, s44\ns_cbranch_scc1 sx_h2\ns_memrealtime s[56:57]\ns_branch sx_h2\n"
"so_h2:\ns_waitcnt lgkmcnt(0)\ns_cmp_lt_i32 s47, 0\ns_cbranch_scc1 sf_h2\n"
"s_branch sgo_h2\nsx_h2:\ns_setpc_b64 s[60:61]\ns_nop 0\n"
"s_nop 0\ns_nop 0\ns_nop 0\ns_nop 0\n"
"s_nop 0\ns_nop 0\ns_nop 0\ns_nop 0\n"
"s_nop 0\ns_nop 0\ns_nop 0\ns_nop 0\n"
"s_nop 0\ns_nop 0\ns_nop 0\ns_nop 0\n"
"s_nop 0\ns_nop 0\ns_nop 0\ns_nop 0\n"
"s_nop 0\ns_nop 0\ns_nop 0\nsgo_h2:\n"
"s_nop 8\nbuffer_load_dwordx4 v[40:43], v10, s[8:11], s43 offen offset:0 sc1\ns_nop 13\nbuffer_load_dwordx4 v[44:47], v10, s[8:11], s43 offen offset:1024 sc1\n"
"s_nop 13\nbuffer_load_dwordx4 v[48:51], v10, s[8:11], s43 offen offset:2048 sc1\nlanded_h2:\ns_waitcnt vmcnt(2)\n"
"v_smfmac_f32_16x16x64_bf16 v[52:55], v[40:43], a[0:7], v8\nv_max3_u32 v124, v124, v40, v41\nv_smfmac_f32_16x16x64_bf16 v[56:59], v[40:43], a[32:39], v8\nv_max3_u32 v124, v124, v42, v43\n"
"v_smfmac_f32_16x16x64_bf16 v[60:63], v[40:43], a[64:71], v8\nv_smfmac_f32_16x16x64_bf16 v[64:67], v[40:43], a[96:103], v8\ns_waitcnt vmcnt(1)\nv_smfmac_f32_16x16x64_bf16 v[52:55], v[44:47], a[8:15], v8\n"
"v_max3_u32 v124, v124, v44, v45\nv_smfmac_f32_16x16x64_bf16 v[56:59], v[44:47], a[40:47], v8\nv_max3_u32 v124, v124, v46, v47\nv_smfmac_f32_16x16x64_bf16 v[60:63], v[44:47], a[72:79], v8\n"
"v_smfmac_f32_16x16x64_bf16 v[64:67], v[44:47], a[104:111], v8\ns_waitcnt vmcnt(0)\nv_smfmac_f32_16x16x64_bf16 v[52:55], v[48:51], a[16:23], v8\nv_max3_u32 v124, v124, v48, v49\n"
"v_smfmac_f32_16x16x64_bf16 v[56:59], v[48:51], a[48:55], v8\nv_max3_u32 v124, v124, v50, v51\nv_smfmac_f32_16x16x64_bf16 v[60:63], v[48:51], a[80:87], v8\nv_cmp_eq_u32 vcc, -1, v124\n"
"v_smfmac_f32_16x16x64_bf16 v[64:67], v[48:51], a[112:119], v8\nv_add_f32 v125, v52, v53\nv_add_f32 v126, v54, v55\nv_cvt_pk_bf16_f32 v108, v125, v126\n"
"v_add_f32 v125, v56, v57\nv_add_f32 v126, v58, v59\nv_cvt_pk_bf16_f32 v109, v125, v126\nv_add_f32 v125, v60, v61\n"
"v_add_f32 v126, v62, v63\nv_cvt_pk_bf16_f32 v110, v125, v126\nv_add_f32 v125, v64, v65\nv_add_f32 v126, v66, v67\n"
"v_cvt_pk_bf16_f32 v111, v125, v126\nds_write2_b64 v127, v[108:109], v[110:111] offset0:0 offset1:16\ns_cbranch_vccnz retry_h2\nv_mov_b32 v125, 0\n"
"s_nop 1\nv_mov_b32 v125, 0\ns_nop 1\ns_waitcnt lgkmcnt(0)\n"
"s_barrier\nv_mov_b32 v125, 0\ns_nop 1\nv_mov_b32 v125, 0\n"
"s_nop 1\nv_mov_b32 v125, 0\ns_nop 1\nv_mov_b32 v125, 0\n"
"s_nop 1\nv_mov_b32 v125, 0\ns_nop 1\nv_mov_b32 v125, 0\n"
"s_nop 1\nv_mov_b32 v125, 0\ns_nop 1\nv_mov_b32 v125, 0\n"
"s_nop 1\nv_mov_b32 v125, 0\ns_nop 1\nv_mov_b32 v125, 0\n"
"s_nop 1\nv_mov_b32 v125, 0\ns_nop 1\nv_mov_b32 v125, 0\n"
"s_nop 1\nv_mov_b32 v125, 0\ns_nop 1\nv_mov_b32 v125, 0\n"
"s_nop 1\nv_mov_b32 v125, 0\ns_nop 1\nv_mov_b32 v125, 0\n"
"s_nop 1\nv_mov_b32 v125, 0\ns_nop 1\nv_mov_b32 v125, 0\n"
"s_nop 1\nv_mov_b32 v125, 0\ns_nop 1\nv_mov_b32 v125, 0\n"
"s_nop 1\nv_mov_b32 v125, 0\ns_nop 1\nv_mov_b32 v125, 0\n"
"buffer_store_dwordx4 v[116:119], v17, s[8:11], s42 offen sc1\nds_read_b32 v129, v130\nv_xor_b32 v127, 8192, v127\nv_xor_b32 v128, 8192, v128\n"
"s_waitcnt lgkmcnt(0)\nv_readfirstlane_b32 s47, v129\ns_sub_u32 s47, s47, s40\ns_cmp_eq_u32 s47, 1\n"
"s_cbranch_scc1 adopt_h2\nback_h2:\ns_add_u32 s40, s40, 1\ns_mov_b32 s43, s41\n"
"s_add_u32 s41, s41, 131072\ns_and_b32 s41, s41, 524287\ns_xor_b32 s42, s41, 262144\ns_and_b32 s47, s46, 15\n"
"s_mul_i32 s48, s47, 8\ns_add_u32 s45, s45, s48\ns_add_u32 s45, s45, s30\ns_lshr_b32 s48, s45, 4\n"
"s_lshl_b32 s48, s48, 2\ns_add_u32 s44, s44, s48\ns_and_b32 s45, s45, 15\ns_add_u32 s92, s92, 1\n"
"s_waitcnt lgkmcnt(0)\ns_cmp_lt_u32 s40, 64\ns_cbranch_scc1 top_h2\ns_branch end_h2\n"
"retry2_h2:\ns_sleep 3\nretry_h2:\ns_add_u32 s53, s53, 1\n"
"s_add_u32 s49, s49, 1\ns_cmp_gt_u32 s49, 65536\ns_cbranch_scc1 end_h2\nbuffer_load_dwordx4 v[40:43], v10, s[8:11], s43 offen offset:0 sc1\n"
"buffer_load_dwordx4 v[44:47], v10, s[8:11], s43 offen offset:1024 sc1\nbuffer_load_dwordx4 v[48:51], v10, s[8:11], s43 offen offset:2048 sc1\nv_mov_b32 v124, 0\ns_add_u32 s47, s40, 1\n"
"v_mov_b32 v129, s47\nds_write_b32 v130, v129\ns_waitcnt vmcnt(0)\nv_max3_u32 v124, v124, v40, v41\n"
"v_max3_u32 v124, v124, v42, v43\nv_max3_u32 v124, v124, v44, v45\nv_max3_u32 v124, v124, v46, v47\nv_max3_u32 v124, v124, v48, v49\n"
"v_max3_u32 v124, v124, v50, v51\nv_cmp_eq_u32 vcc, -1, v124\nv_mov_b32 v52, 0\nv_mov_b32 v53, 0\n"
"v_mov_b32 v54, 0\nv_mov_b32 v55, 0\nv_mov_b32 v56, 0\nv_mov_b32 v57, 0\n"
"v_mov_b32 v58, 0\nv_mov_b32 v59, 0\nv_mov_b32 v60, 0\nv_mov_b32 v61, 0\n"
"v_mov_b32 v62, 0\nv_mov_b32 v63, 0\nv_mov_b32 v64, 0\nv_mov_b32 v65, 0\n"
"v_mov_b32 v66, 0\nv_mov_b32 v67, 0\ns_cbranch_vccnz retry2_h2\ns_branch landed_h2\n"
"adopt_h2:\nbuffer_load_dwordx4 v[112:115], v28, s[8:11], 0 offen sc1\ns_lshl_b32 s52, s40, 26\ns_or_b32 s47, s52, 0x3ffffff\n"
"s_waitcnt vmcnt(0)\nv_cmp_ge_u32 vcc, s47, v112\ns_nop 1\nv_cndmask_b32 v112, 0, v112, vcc\n"
"v_cmp_ge_u32 vcc, s47, v113\ns_nop 1\nv_cndmask_b32 v113, 0, v113, vcc\nv_cmp_ge_u32 vcc, s47, v114\n"
"s_nop 1\nv_cndmask_b32 v114, 0, v114, vcc\nv_cmp_ge_u32 vcc, s47, v115\ns_nop 1\n"
"v_cndmask_b32 v115, 0, v115, vcc\nv_max3_u32 v129, v112, v113, v114\nv_max_u32 v129, v129, v115\ns_nop 1\n"
"v_max_u32_dpp v129, v129, v129 row_ror:8 row_mask:0xf bank_mask:0xf\ns_nop 1\nv_max_u32_dpp v129, v129, v129 row_ror:4 row_mask:0xf bank_mask:0xf\ns_nop 1\n"
"v_max_u32_dpp v129, v129, v129 row_ror:2 row_mask:0xf bank_mask:0xf\ns_nop 1\nv_max_u32_dpp v129, v129, v129 row_ror:1 row_mask:0xf bank_mask:0xf\ns_nop 0\n"
"v_readlane_b32 s47, v129, 0\nv_readlane_b32 s48, v129, 16\ns_max_u32 s48, s48, s47\ns_cmp_le_u32 s48, s52\n"
"s_cbranch_scc1 back_h2\ns_bfe_u32 s52, s48, 1179656\ns_add_u32 s44, s52, s28\ns_mov_b32 s45, 0\n"
"s_and_b32 s46, s48, 255\ns_mov_b32 s92, 0\ns_branch back_h2\nend_h2:\n"
"s_branch fk_done\nfk_h3:\ns_mov_b32 s40, 0\ns_mov_b32 s41, 0\n"
"s_mov_b32 s42, 262144\ns_mov_b32 s43, 393216\ns_mov_b32 s44, s28\ns_mov_b32 s45, 0\n"
"s_mov_b32 s46, 0\ns_mov_b32 s92, 0\ns_mov_b32 s49, 0\ns_mov_b32 s53, 0\n"
"s_mov_b32 s50, s35\ns_mov_b32 s51, s36\nv_mov_b32 v127, v13\nv_mov_b32 v128, v14\n"
"v_mov_b32 v130, 24576\nv_mov_b32 v111, 0\nv_mov_b32 v116, -1\nv_mov_b32 v117, -1\n"
"v_mov_b32 v118, -1\nv_mov_b32 v119, -1\ns_add_u32 s47, s34, 4\ns_and_b32 s52, s47, 1\n"
"s_mul_i32 s52, s52, -14208\ns_mul_i32 s48, s47, 14336\ns_add_u32 s48, s48, s52\ns_add_u32 s47, s48, 10240\n"
"buffer_load_dwordx4 a[8:11], v15, s[12:15], s47 offen sc1\nbuffer_load_dwordx4 a[12:15], v15, s[12:15], s47 offen offset:1024 sc1\ns_add_u32 s47, s48, 8192\nbuffer_load_dwordx4 a[0:3], v15, s[12:15], s47 offen sc1\n"
"buffer_load_dwordx4 a[4:7], v15, s[12:15], s47 offen offset:1024 sc1\ns_add_u32 s47, s48, 12288\nbuffer_load_dwordx4 a[16:19], v15, s[12:15], s47 offen sc1\nbuffer_load_dwordx4 a[20:23], v15, s[12:15], s47 offen offset:1024 sc1\n"
"s_add_u32 s47, s34, 5\ns_and_b32 s52, s47, 1\ns_mul_i32 s52, s52, -14208\ns_mul_i32 s48, s47, 14336\n"
"s_add_u32 s48, s48, s52\ns_add_u32 s47, s48, 10240\nbuffer_load_dwordx4 a[40:43], v15, s[12:15], s47 offen sc1\nbuffer_load_dwordx4 a[44:47], v15, s[12:15], s47 offen offset:1024 sc1\n"
"s_add_u32 s47, s48, 8192\nbuffer_load_dwordx4 a[32:35], v15, s[12:15], s47 offen sc1\nbuffer_load_dwordx4 a[36:39], v15, s[12:15], s47 offen offset:1024 sc1\ns_add_u32 s47, s48, 12288\n"
"buffer_load_dwordx4 a[48:51], v15, s[12:15], s47 offen sc1\nbuffer_load_dwordx4 a[52:55], v15, s[12:15], s47 offen offset:1024 sc1\ns_add_u32 s47, s34, 6\ns_and_b32 s52, s47, 1\n"
"s_mul_i32 s52, s52, -14208\ns_mul_i32 s48, s47, 14336\ns_add_u32 s48, s48, s52\ns_add_u32 s47, s48, 10240\n"
"buffer_load_dwordx4 a[72:75], v15, s[12:15], s47 offen sc1\nbuffer_load_dwordx4 a[76:79], v15, s[12:15], s47 offen offset:1024 sc1\ns_add_u32 s47, s48, 8192\nbuffer_load_dwordx4 a[64:67], v15, s[12:15], s47 offen sc1\n"
"buffer_load_dwordx4 a[68:71], v15, s[12:15], s47 offen offset:1024 sc1\ns_add_u32 s47, s48, 12288\nbuffer_load_dwordx4 a[80:83], v15, s[12:15], s47 offen sc1\nbuffer_load_dwordx4 a[84:87], v15, s[12:15], s47 offen offset:1024 sc1\n"
"v_lshlrev_b32 v114, 2, v9\ns_mov_b32 s52, 1064960\nbuffer_load_dword v112, v114, s[8:11], s52 offen sc1\ns_add_u32 s52, s52, 4096\n"
"buffer_load_dword v113, v114, s[8:11], s52 offen sc1\ns_waitcnt vmcnt(0)\nar_h3:\nv_xor_b32 v112, s33, v112\n"
"v_xor_b32 v113, s33, v113\nv_or_b32 v112, v112, v113\nv_cmp_ne_u32 vcc, 0, v112\ns_cbranch_vccz arok_h3\n"
"s_add_u32 s53, s53, 0\ns_add_u32 s49, s49, 1\ns_cmp_gt_u32 s49, 65536\ns_cbranch_scc1 end_h3\n"
"s_sleep 2\nv_lshlrev_b32 v114, 2, v9\ns_mov_b32 s52, 1064960\nbuffer_load_dword v112, v114, s[8:11], s52 offen sc1\n"
"s_add_u32 s52, s52, 4096\nbuffer_load_dword v113, v114, s[8:11], s52 offen sc1\ns_waitcnt vmcnt(0)\ns_branch ar_h3\n"
"arok_h3:\ns_barrier\ns_memrealtime s[54:55]\ns_waitcnt lgkmcnt(0)\n"
"s_add_u32 s44, s54, 16\ns_mov_b32 s45, 8\n.p2align 6\ntop_h3:\n"
"v_mov_b32 v52, 0\nv_mov_b32 v53, 0\nv_mov_b32 v54, 0\nv_mov_b32 v55, 0\n"
"v_mov_b32 v56, 0\nv_mov_b32 v57, 0\nv_mov_b32 v58, 0\nv_mov_b32 v59, 0\n"
"v_mov_b32 v60, 0\nv_mov_b32 v61, 0\nv_mov_b32 v62, 0\nv_mov_b32 v63, 0\n"
"v_mov_b32 v124, 0\ns_mul_i32 s48, s45, 6\ns_and_b32 s48, s48, -4\ns_getpc_b64 s[60:61]\n"
"spc_h3:\ns_sub_u32 s48, sgo_h3-spc_h3, s48\ns_add_u32 s60, s60, s48\ns_addc_u32 s61, s61, 0\n"
"sf_h3:\ns_memrealtime s[54:55]\ns_nop 2\ns_memrealtime s[56:57]\n"
"s_nop 2\ns_memrealtime s[58:59]\ns_waitcnt lgkmcnt(4)\ns_sub_u32 s47, s54, s44\n"
"s_add_u32 s48, s47, 12\ns_cmp_ge_u32 s48, 12\ns_cbranch_scc1 so_h3\ns_memrealtime s[54:55]\n"
"s_waitcnt lgkmcnt(4)\ns_cmp_ge_u32 s56, s44\ns_cbranch_scc1 sx_h3\ns_memrealtime s[56:57]\n"
"s_waitcnt lgkmcnt(4)\ns_cmp_ge_u32 s58, s44\ns_cbranch_scc1 sx_h3\ns_memrealtime s[58:59]\n"
"s_waitcnt lgkmcnt(4)\ns_cmp_ge_u32 s54, s44\ns_cbranch_scc1 sx_h3\ns_memrealtime s[54:55]\n"
"s_waitcnt lgkmcnt(4)\ns_cmp_ge_u32 s56, s44\ns_cbranch_scc1 sx_h3\ns_memrealtime s[56:57]\n"
"s_waitcnt lgkmcnt(4)\ns_cmp_ge_u32 s58, s44\ns_cbranch_scc1 sx_h3\ns_memrealtime s[58:59]\n"
"s_waitcnt lgkmcnt(4)\ns_cmp_ge_u32 s54, s44\ns_cbranch_scc1 sx_h3\ns_memrealtime s[54:55]\n"
"s_waitcnt lgkmcnt(4)\ns_cmp_ge_u32 s56, s44\ns_cbranch_scc1 sx_h3\ns_memrealtime s[56:57]\n"
"s_waitcnt lgkmcnt(4)\ns_cmp_ge_u32 s58, s44\ns_cbranch_scc1 sx_h3\ns_memrealtime s[58:59]\n"
"s_waitcnt lgkmcnt(4)\ns_cmp_ge_u32 s54, s44\ns_cbranch_scc1 sx_h3\ns_memrealtime s[54:55]\n"
"s_waitcnt lgkmcnt(4)\ns_cmp_ge_u32 s56, s44\ns_cbranch_scc1 sx_h3\ns_memrealtime s[56:57]\n"
"s_waitcnt lgkmcnt(4)\ns_cmp_ge_u32 s58, s44\ns_cbranch_scc1 sx_h3\ns_memrealtime s[58:59]\n"
"s_waitcnt lgkmcnt(4)\ns_cmp_ge_u32 s54, s44\ns_cbranch_scc1 sx_h3\ns_memrealtime s[54:55]\n"
"s_waitcnt lgkmcnt(4)\ns_cmp_ge_u32 s56, s44\ns_cbranch_scc1 sx_h3\ns_memrealtime s[56:57]\n"
"s_waitcnt lgkmcnt(4)\ns_cmp_ge_u32 s58, s44\ns_cbranch_scc1 sx_h3\ns_memrealtime s[58:59]\n"
"s_waitcnt lgkmcnt(4)\ns_cmp_ge_u32 s54, s44\ns_cbranch_scc1 sx_h3\ns_memrealtime s[54:55]\n"
"s_waitcnt lgkmcnt(4)\ns_cmp_ge_u32 s56, s44\ns_cbranch_scc1 sx_h3\ns_memrealtime s[56:57]\n"
"s_branch sx_h3\nso_h3:\ns_waitcnt lgkmcnt(0)\ns_cmp_lt_i32 s47, 0\n"
"s_cbranch_scc1 sf_h3\ns_branch sgo_h3\nsx_h3:\ns_setpc_b64 s[60:61]\n"
"s_nop 0\ns_nop 0\ns_nop 0\ns_nop 0\n"
"s_nop 0\ns_nop 0\ns_nop 0\ns_nop 0\n"
"s_nop 0\ns_nop 0\ns_nop 0\ns_nop 0\n"
"s_nop 0\ns_nop 0\ns_nop 0\ns_nop 0\n"
"s_nop 0\ns_nop 0\ns_nop 0\ns_nop 0\n"
"s_nop 0\ns_nop 0\ns_nop 0\ns_nop 0\n"
"sgo_h3:\ns_nop 15\ns_nop 2\nbuffer_load_dwordx4 v[40:43], v10, s[8:11], s43 offen offset:1024 sc1\n"
"s_nop 13\nbuffer_load_dwordx4 v[44:47], v10, s[8:11], s43 offen offset:0 sc1\ns_nop 13\nbuffer_load_dwordx4 v[48:51], v10, s[8:11], s43 offen offset:2048 sc1\n"
"s_nop 3\nbuffer_load_dwordx4 v[68:71], v11, s[8:11], s43 offen sc1\nbuffer_load_dwordx4 v[72:75], v12, s[8:11], s43 offen sc1\nv_mov_b32 v125, 0\n"
"s_nop 1\nv_mov_b32 v125, 0\ns_nop 1\nv_mov_b32 v125, 0\n"
"s_nop 1\nv_mov_b32 v125, 0\ns_nop 1\nv_mov_b32 v125, 0\n"
"s_nop 1\nv_mov_b32 v125, 0\ns_nop 1\nv_mov_b32 v125, 0\n"
"s_nop 1\nv_mov_b32 v125, 0\ns_nop 1\nv_mov_b32 v125, 0\n"
"s_nop 1\nv_mov_b32 v125, 0\ns_nop 1\nv_mov_b32 v125, 0\n"
"s_nop 1\nv_mov_b32 v125, 0\ns_nop 1\nv_mov_b32 v125, 0\n"
"s_nop 1\nv_mov_b32 v125, 0\ns_nop 1\nv_mov_b32 v125, 0\n"
"s_nop 1\nv_mov_b32 v125, 0\ns_nop 1\nv_mov_b32 v125, 0\n"
"s_nop 0\nlanded_h3:\ns_waitcnt vmcnt(4)\nv_smfmac_f32_16x16x64_bf16 v[52:55], v[40:43], a[8:15], v8\n"
"v_max3_u32 v124, v124, v40, v41\nv_smfmac_f32_16x16x64_bf16 v[56:59], v[40:43], a[40:47], v8\nv_max3_u32 v124, v124, v42, v43\nv_smfmac_f32_16x16x64_bf16 v[60:63], v[40:43], a[72:79], v8\n"
"s_waitcnt vmcnt(3)\nv_smfmac_f32_16x16x64_bf16 v[52:55], v[44:47], a[0:7], v8\nv_max3_u32 v124, v124, v44, v45\nv_smfmac_f32_16x16x64_bf16 v[56:59], v[44:47], a[32:39], v8\n"
"v_max3_u32 v124, v124, v46, v47\nv_smfmac_f32_16x16x64_bf16 v[60:63], v[44:47], a[64:71], v8\ns_waitcnt vmcnt(2)\nv_smfmac_f32_16x16x64_bf16 v[52:55], v[48:51], a[16:23], v8\n"
"v_max3_u32 v124, v124, v48, v49\nv_smfmac_f32_16x16x64_bf16 v[56:59], v[48:51], a[48:55], v8\nv_max3_u32 v124, v124, v50, v51\nv_smfmac_f32_16x16x64_bf16 v[60:63], v[48:51], a[80:87], v8\n"
"v_add_f32 v125, v52, v53\nv_add_f32 v126, v54, v55\nv_cvt_pk_bf16_f32 v108, v125, v126\nv_add_f32 v125, v56, v57\n"
"v_add_f32 v126, v58, v59\nv_cvt_pk_bf16_f32 v109, v125, v126\nv_cmp_eq_u32 vcc, -1, v124\nv_add_f32 v125, v60, v61\n"
"v_add_f32 v126, v62, v63\nv_cvt_pk_bf16_f32 v110, v125, v126\nds_write2_b64 v127, v[108:109], v[110:111] offset0:32 offset1:48\ns_cbranch_vccnz retry_h3\n"
"v_mov_b32 v125, 0\ns_nop 1\nv_mov_b32 v125, 0\ns_nop 1\n"
"s_waitcnt lgkmcnt(0)\ns_barrier\nds_read_b64_tr_b16 v[84:85], v128 offset:0\nds_read_b64_tr_b16 v[86:87], v128 offset:64\n"
"ds_read_b64_tr_b16 v[88:89], v128 offset:256\nds_read_b64_tr_b16 v[90:91], v128 offset:320\nds_read_b64_tr_b16 v[92:93], v128 offset:512\nds_read_b64_tr_b16 v[94:95], v128 offset:576\n"
"ds_read_b64_tr_b16 v[96:97], v128 offset:768\nds_read_b64_tr_b16 v[98:99], v128 offset:832\nmb_h3:\ns_waitcnt vmcnt(0)\n"
"v_max3_u32 v124, v124, v68, v69\nv_max3_u32 v124, v124, v70, v71\nv_max3_u32 v124, v124, v72, v73\nv_max3_u32 v124, v124, v74, v75\n"
"v_cmp_eq_u32 vcc, -1, v124\ns_waitcnt lgkmcnt(6)\nv_mfma_f32_16x16x32_bf16 v[100:103], v[84:87], v[68:71], 0\ns_waitcnt lgkmcnt(4)\n"
"v_mfma_f32_16x16x32_bf16 v[100:103], v[88:91], v[72:75], v[100:103]\ns_waitcnt lgkmcnt(2)\nv_mfma_f32_16x16x32_bf16 v[100:103], v[92:95], v[68:71], v[100:103]\ns_waitcnt lgkmcnt(0)\n"
"v_mfma_f32_16x16x32_bf16 v[100:103], v[96:99], v[72:75], v[100:103]\ns_cbranch_vccnz mbr_h3\ns_nop 6\nv_add_f32_dpp v120, v102, v100 row_ror:8 row_mask:0xf bank_mask:0x3\n"
"v_add_f32_dpp v120, v100, v102 row_ror:8 row_mask:0xf bank_mask:0xc\nv_add_f32_dpp v121, v103, v101 row_ror:8 row_mask:0xf bank_mask:0x3\nv_add_f32_dpp v121, v101, v103 row_ror:8 row_mask:0xf bank_mask:0xc\nv_cvt_pk_f16_f32 v125, v120, v121\n"
"buffer_store_dword v125, v16, s[8:11], s41 offen sc1\ns_nop 15\ns_nop 15\ns_nop 15\n"
"buffer_store_dwordx2 v[120:121], v17, s[8:11], s41 offen sc1\nds_read_b32 v129, v130\nv_xor_b32 v127, 8192, v127\nv_xor_b32 v128, 8192, v128\n"
"s_waitcnt lgkmcnt(0)\nv_readfirstlane_b32 s47, v129\ns_sub_u32 s47, s47, s40\ns_cmp_eq_u32 s47, 1\n"
"s_cbranch_scc1 adopt_h3\nback_h3:\ns_add_u32 s40, s40, 1\ns_mov_b32 s43, s41\n"
"s_add_u32 s41, s41, 131072\ns_and_b32 s41, s41, 524287\ns_xor_b32 s42, s41, 262144\ns_and_b32 s47, s46, 15\n"
"s_mul_i32 s48, s47, 8\ns_add_u32 s45, s45, s48\ns_add_u32 s45, s45, s30\ns_lshr_b32 s48, s45, 4\n"
"s_lshl_b32 s48, s48, 2\ns_add_u32 s44, s44, s48\ns_and_b32 s45, s45, 15\ns_add_u32 s92, s92, 1\n"
"s_waitcnt lgkmcnt(0)\ns_cmp_lt_u32 s40, 64\ns_cbranch_scc1 top_h3\ns_branch end_h3\n"
"retry2_h3:\ns_sleep 3\nretry_h3:\ns_add_u32 s53, s53, 1\n"
"s_add_u32 s49, s49, 1\ns_cmp_gt_u32 s49, 65536\ns_cbranch_scc1 end_h3\nbuffer_load_dwordx4 v[40:43], v10, s[8:11], s43 offen offset:1024 sc1\n"
"buffer_load_dwordx4 v[44:47], v10, s[8:11], s43 offen offset:0 sc1\nbuffer_load_dwordx4 v[48:51], v10, s[8:11], s43 offen offset:2048 sc1\nbuffer_load_dwordx4 v[68:71], v11, s[8:11], s43 offen sc1\nbuffer_load_dwordx4 v[72:75], v12, s[8:11], s43 offen sc1\n"
"v_mov_b32 v124, 0\ns_add_u32 s47, s40, 1\nv_mov_b32 v129, s47\nds_write_b32 v130, v129\n"
"s_waitcnt vmcnt(0)\nv_max3_u32 v124, v124, v40, v41\nv_max3_u32 v124, v124, v42, v43\nv_max3_u32 v124, v124, v44, v45\n"
"v_max3_u32 v124, v124, v46, v47\nv_max3_u32 v124, v124, v48, v49\nv_max3_u32 v124, v124, v50, v51\nv_cmp_eq_u32 vcc, -1, v124\n"
"v_mov_b32 v52, 0\nv_mov_b32 v53, 0\nv_mov_b32 v54, 0\nv_mov_b32 v55, 0\n"
"v_mov_b32 v56, 0\nv_mov_b32 v57, 0\nv_mov_b32 v58, 0\nv_mov_b32 v59, 0\n"
"v_mov_b32 v60, 0\nv_mov_b32 v61, 0\nv_mov_b32 v62, 0\nv_mov_b32 v63, 0\n"
"s_cbranch_vccnz retry2_h3\ns_branch landed_h3\nadopt_h3:\nbuffer_load_dwordx4 v[112:115], v28, s[8:11], 0 offen sc1\n"
"s_lshl_b32 s52, s40, 26\ns_or_b32 s47, s52, 0x3ffffff\ns_waitcnt vmcnt(0)\nv_cmp_ge_u32 vcc, s47, v112\n"
"s_nop 1\nv_cndmask_b32 v112, 0, v112, vcc\nv_cmp_ge_u32 vcc, s47, v113\ns_nop 1\n"
"v_cndmask_b32 v113, 0, v113, vcc\nv_cmp_ge_u32 vcc, s47, v114\ns_nop 1\nv_cndmask_b32 v114, 0, v114, vcc\n"
"v_cmp_ge_u32 vcc, s47, v115\ns_nop 1\nv_cndmask_b32 v115, 0, v115, vcc\nv_max3_u32 v129, v112, v113, v114\n"
"v_max_u32 v129, v129, v115\ns_nop 1\nv_max_u32_dpp v129, v129, v129 row_ror:8 row_mask:0xf bank_mask:0xf\ns_nop 1\n"
"v_max_u32_dpp v129, v129, v129 row_ror:4 row_mask:0xf bank_mask:0xf\ns_nop 1\nv_max_u32_dpp v129, v129, v129 row_ror:2 row_mask:0xf bank_mask:0xf\ns_nop 1\n"
"v_max_u32_dpp v129, v129, v129 row_ror:1 row_mask:0xf bank_mask:0xf\ns_nop 0\nv_readlane_b32 s47, v129, 0\nv_readlane_b32 s48, v129, 16\n"
"s_max_u32 s48, s48, s47\ns_cmp_le_u32 s48, s52\ns_cbranch_scc1 back_h3\ns_bfe_u32 s52, s48, 1179656\n"
"s_add_u32 s44, s52, s28\ns_mov_b32 s45, 0\ns_and_b32 s46, s48, 255\ns_mov_b32 s92, 0\n"
"s_branch back_h3\nmbr_h3:\ns_add_u32 s53, s53, 1\ns_add_u32 s49, s49, 1\n"
"s_cmp_gt_u32 s49, 65536\ns_cbranch_scc1 end_h3\nbuffer_load_dwordx4 v[68:71], v11, s[8:11], s43 offen sc1\nbuffer_load_dwordx4 v[72:75], v12, s[8:11], s43 offen sc1\n"
"v_mov_b32 v124, 0\ns_branch mb_h3\nend_h3:\ns_branch fk_done\n"
"fk_x0:\ns_mov_b32 s40, 0\ns_mov_b32 s41, 0\ns_mov_b32 s42, 262144\n"
"s_mov_b32 s43, 393216\ns_mov_b32 s44, s28\ns_mov_b32 s45, 0\ns_mov_b32 s46, 0\n"
"s_mov_b32 s92, 0\ns_mov_b32 s49, 0\ns_mov_b32 s53, 0\ns_mov_b32 s50, s35\n"
"s_mov_b32 s51, s36\nv_mov_b32 v81, v13\nv_mov_b32 v82, v14\nv_mov_b32 v84, 24576\n"
"s_add_u32 s51, s50, 14336\nv_mov_b32 v64, -1\nv_mov_b32 v65, -1\nv_mov_b32 v66, -1\n"
"v_mov_b32 v67, -1\nbuffer_load_dwordx4 a[0:3], v9, s[16:19], s50 offen offset:0 sc1\nbuffer_load_dwordx4 a[4:7], v9, s[16:19], s50 offen offset:1024 sc1\nbuffer_load_dwordx4 a[8:11], v9, s[16:19], s50 offen offset:2048 sc1\n"
"buffer_load_dwordx4 a[12:15], v9, s[16:19], s50 offen offset:3072 sc1\nbuffer_load_dwordx4 a[16:19], v9, s[16:19], s51 offen offset:0 sc1\nbuffer_load_dwordx4 a[20:23], v9, s[16:19], s51 offen offset:1024 sc1\nbuffer_load_dwordx4 a[24:27], v9, s[16:19], s51 offen offset:2048 sc1\n"
"buffer_load_dwordx4 a[28:31], v9, s[16:19], s51 offen offset:3072 sc1\ns_add_u32 s50, s50, 28672\ns_add_u32 s51, s51, 28672\nbuffer_load_dword v85, v18, s[8:11], 0 offen sc1\n"
"s_waitcnt vmcnt(0)\nf0_x0:\ns_add_u32 s47, s40, 2\ns_min_u32 s47, s47, 64\n"
"s_add_u32 s47, s47, s33\nv_subrev_u32 v85, s47, v85\nv_cmp_lt_u32 vcc, 64, v85\ns_cbranch_vccz f0ok_x0\n"
"s_add_u32 s53, s53, 0\ns_add_u32 s49, s49, 1\ns_cmp_gt_u32 s49, 65536\ns_cbranch_scc1 end_x0\n"
"s_sleep 2\nbuffer_load_dword v85, v18, s[8:11], 0 offen sc1\ns_waitcnt vmcnt(0)\ns_branch f0_x0\n"
"f0ok_x0:\ns_barrier\ns_memrealtime s[54:55]\ns_waitcnt lgkmcnt(0)\n"
"s_add_u32 s44, s54, 16\ns_mov_b32 s45, 8\n.p2align 6\ntop_x0:\n"
"v_mov_b32 v48, 0\nv_mov_b32 v49, 0\nv_mov_b32 v50, 0\nv_mov_b32 v51, 0\n"
"v_mov_b32 v52, 0\nv_mov_b32 v53, 0\nv_mov_b32 v54, 0\nv_mov_b32 v55, 0\n"
"v_mov_b32 v78, 0\ns_mul_i32 s48, s45, 6\ns_and_b32 s48, s48, -4\ns_getpc_b64 s[60:61]\n"
"spc_x0:\ns_sub_u32 s48, sgo_x0-spc_x0, s48\ns_add_u32 s60, s60, s48\ns_addc_u32 s61, s61, 0\n"
"sf_x0:\ns_memrealtime s[54:55]\ns_nop 2\ns_memrealtime s[56:57]\n"
"s_nop 2\ns_memrealtime s[58:59]\ns_waitcnt lgkmcnt(4)\ns_sub_u32 s47, s54, s44\n"
"s_add_u32 s48, s47, 12\ns_cmp_ge_u32 s48, 12\ns_cbranch_scc1 so_x0\ns_memrealtime s[54:55]\n"
"s_waitcnt lgkmcnt(4)\ns_cmp_ge_u32 s56, s44\ns_cbranch_scc1 sx_x0\ns_memrealtime s[56:57]\n"
"s_waitcnt lgkmcnt(4)\ns_cmp_ge_u32 s58, s44\ns_cbranch_scc1 sx_x0\ns_memrealtime s[58:59]\n"
"s_waitcnt lgkmcnt(4)\ns_cmp_ge_u32 s54, s44\ns_cbranch_scc1 sx_x0\ns_memrealtime s[54:55]\n"
"s_waitcnt lgkmcnt(4)\ns_cmp_ge_u32 s56, s44\ns_cbranch_scc1 sx_x0\ns_memrealtime s[56:57]\n"
"s_waitcnt lgkmcnt(4)\ns_cmp_ge_u32 s58, s44\ns_cbranch_scc1 sx_x0\ns_memrealtime s[58:59]\n"
"s_waitcnt lgkmcnt(4)\ns_cmp_ge_u32 s54, s44\ns_cbranch_scc1 sx_x0\ns_memrealtime s[54:55]\n"
"s_waitcnt lgkmcnt(4)\ns_cmp_ge_u32 s56, s44\ns_cbranch_scc1 sx_x0\ns_memrealtime s[56:57]\n"
"s_waitcnt lgkmcnt(4)\ns_cmp_ge_u32 s58, s44\ns_cbranch_scc1 sx_x0\ns_memrealtime s[58:59]\n"
"s_waitcnt lgkmcnt(4)\ns_cmp_ge_u32 s54, s44\ns_cbranch_scc1 sx_x0\ns_memrealtime s[54:55]\n"
"s_waitcnt lgkmcnt(4)\ns_cmp_ge_u32 s56, s44\ns_cbranch_scc1 sx_x0\ns_memrealtime s[56:57]\n"
"s_waitcnt lgkmcnt(4)\ns_cmp_ge_u32 s58, s44\ns_cbranch_scc1 sx_x0\ns_memrealtime s[58:59]\n"
"s_waitcnt lgkmcnt(4)\ns_cmp_ge_u32 s54, s44\ns_cbranch_scc1 sx_x0\ns_memrealtime s[54:55]\n"
"s_waitcnt lgkmcnt(4)\ns_cmp_ge_u32 s56, s44\ns_cbranch_scc1 sx_x0\ns_memrealtime s[56:57]\n"
"s_waitcnt lgkmcnt(4)\ns_cmp_ge_u32 s58, s44\ns_cbranch_scc1 sx_x0\ns_memrealtime s[58:59]\n"
"s_waitcnt lgkmcnt(4)\ns_cmp_ge_u32 s54, s44\ns_cbranch_scc1 sx_x0\ns_memrealtime s[54:55]\n"
"s_waitcnt lgkmcnt(4)\ns_cmp_ge_u32 s56, s44\ns_cbranch_scc1 sx_x0\ns_memrealtime s[56:57]\n"
"s_branch sx_x0\nso_x0:\ns_waitcnt lgkmcnt(0)\ns_cmp_lt_i32 s47, 0\n"
"s_cbranch_scc1 sf_x0\ns_branch sgo_x0\nsx_x0:\ns_setpc_b64 s[60:61]\n"
"s_nop 0\ns_nop 0\ns_nop 0\ns_nop 0\n"
"s_nop 0\ns_nop 0\ns_nop 0\ns_nop 0\n"
"s_nop 0\ns_nop 0\ns_nop 0\ns_nop 0\n"
"s_nop 0\ns_nop 0\ns_nop 0\ns_nop 0\n"
"s_nop 0\ns_nop 0\ns_nop 0\ns_nop 0\n"
"s_nop 0\ns_nop 0\ns_nop 0\ns_nop 0\n"
"sgo_x0:\nbuffer_load_dwordx4 v[40:43], v10, s[8:11], s43 offen offset:0 sc1\nbuffer_load_dwordx4 v[44:47], v10, s[8:11], s43 offen offset:1024 sc1\nlanded_x0:\n"
"s_waitcnt vmcnt(1)\nv_smfmac_f32_16x16x64_bf16 v[48:51], v[40:43], a[0:7], v8\nv_max3_u32 v78, v78, v40, v41\nv_smfmac_f32_16x16x64_bf16 v[52:55], v[40:43], a[16:23], v8\n"
"v_max3_u32 v78, v78, v42, v43\ns_waitcnt vmcnt(0)\nv_smfmac_f32_16x16x64_bf16 v[48:51], v[44:47], a[8:15], v8\nv_max3_u32 v78, v78, v44, v45\n"
"v_smfmac_f32_16x16x64_bf16 v[52:55], v[44:47], a[24:31], v8\nv_max3_u32 v78, v78, v46, v47\nv_cmp_eq_u32 vcc, -1, v78\ns_nop 0\n"
"v_add_f32 v56, v48, v49\nv_add_f32 v57, v50, v51\ns_nop 1\nv_add_f32 v58, v52, v53\n"
"v_add_f32 v59, v54, v55\nds_write2_b32 v81, v56, v57 offset0:0 offset1:40\nds_write2_b32 v81, v58, v59 offset0:16 offset1:56\ns_cbranch_vccnz retry_x0\n"
"v_mov_b32 v79, 0\ns_nop 1\nv_mov_b32 v79, 0\ns_nop 1\n"
"v_mov_b32 v79, 0\ns_waitcnt lgkmcnt(0)\ns_barrier\nds_read_b64 v[68:69], v82 offset:0\n"
"ds_read_b64 v[70:71], v82 offset:1280\nds_read_b64 v[72:73], v82 offset:2560\nds_read_b64 v[74:75], v82 offset:3840\nv_mov_b32 v79, 0\n"
"s_nop 1\nv_mov_b32 v79, 0\ns_nop 1\nv_mov_b32 v79, 0\n"
"s_nop 1\ns_waitcnt lgkmcnt(2)\nv_pk_add_f32 v[68:69], v[68:69], v[70:71]\ns_waitcnt lgkmcnt(0)\n"
"v_pk_add_f32 v[72:73], v[72:73], v[74:75]\nv_pk_add_f32 v[68:69], v[68:69], v[72:73]\nv_exp_f32 v79, v68\nv_exp_f32 v80, v69\n"
"s_nop 0\nv_cvt_pk_bf16_f32 v79, v79, v80\nbuffer_store_dword v79, v16, s[8:11], s41 offen sc1\ns_barrier\n"
"buffer_load_dwordx4 a[0:3], v9, s[16:19], s50 offen offset:0 sc1\ns_nop 15\ns_nop 7\nbuffer_load_dwordx4 a[4:7], v9, s[16:19], s50 offen offset:1024 sc1\n"
"s_nop 15\ns_nop 7\nbuffer_load_dwordx4 a[8:11], v9, s[16:19], s50 offen offset:2048 sc1\ns_nop 15\n"
"s_nop 7\nbuffer_load_dwordx4 a[12:15], v9, s[16:19], s50 offen offset:3072 sc1\ns_nop 15\ns_nop 7\n"
"buffer_load_dwordx4 a[16:19], v9, s[16:19], s51 offen offset:0 sc1\ns_nop 15\ns_nop 7\nbuffer_load_dwordx4 a[20:23], v9, s[16:19], s51 offen offset:1024 sc1\n"
"s_nop 15\ns_nop 7\nbuffer_load_dwordx4 a[24:27], v9, s[16:19], s51 offen offset:2048 sc1\ns_nop 15\n"
"s_nop 7\nbuffer_load_dwordx4 a[28:31], v9, s[16:19], s51 offen offset:3072 sc1\ns_nop 15\ns_nop 7\n"
"s_add_u32 s50, s50, 28672\ns_add_u32 s51, s51, 28672\nbuffer_store_dwordx2 v[68:69], v17, s[8:11], s41 offen sc1\nbuffer_store_dwordx4 v[64:67], v15, s[8:11], s42 offen sc1\n"
"ds_read_b32 v83, v84\nv_xor_b32 v81, 8192, v81\nv_xor_b32 v82, 8192, v82\ns_waitcnt lgkmcnt(0)\n"
"v_readfirstlane_b32 s47, v83\ns_sub_u32 s47, s47, s40\ns_cmp_eq_u32 s47, 1\ns_cbranch_scc1 adopt_x0\n"
"back_x0:\ns_add_u32 s40, s40, 1\ns_mov_b32 s43, s41\ns_add_u32 s41, s41, 131072\n"
"s_and_b32 s41, s41, 524287\ns_xor_b32 s42, s41, 262144\ns_and_b32 s47, s46, 15\ns_mul_i32 s48, s47, 8\n"
"s_add_u32 s45, s45, s48\ns_add_u32 s45, s45, s30\ns_lshr_b32 s48, s45, 4\ns_lshl_b32 s48, s48, 2\n"
"s_add_u32 s44, s44, s48\ns_and_b32 s45, s45, 15\ns_add_u32 s92, s92, 1\ns_waitcnt lgkmcnt(0)\n"
"s_cmp_lt_u32 s40, 63\ns_cbranch_scc1 top_x0\ns_branch end_x0\nretry2_x0:\n"
"s_sleep 3\nretry_x0:\ns_add_u32 s53, s53, 1\ns_add_u32 s49, s49, 1\n"
"s_cmp_gt_u32 s49, 65536\ns_cbranch_scc1 end_x0\nbuffer_load_dwordx4 v[40:43], v10, s[8:11], s43 offen offset:0 sc1\nbuffer_load_dwordx4 v[44:47], v10, s[8:11], s43 offen offset:1024 sc1\n"
"v_mov_b32 v78, 0\ns_add_u32 s47, s40, 1\nv_mov_b32 v83, s47\nds_write_b32 v84, v83\n"
"s_waitcnt vmcnt(0)\nv_max3_u32 v78, v78, v40, v41\nv_max3_u32 v78, v78, v42, v43\nv_max3_u32 v78, v78, v44, v45\n"
"v_max3_u32 v78, v78, v46, v47\nv_cmp_eq_u32 vcc, -1, v78\nv_mov_b32 v48, 0\nv_mov_b32 v49, 0\n"
"v_mov_b32 v50, 0\nv_mov_b32 v51, 0\nv_mov_b32 v52, 0\nv_mov_b32 v53, 0\n"
"v_mov_b32 v54, 0\nv_mov_b32 v55, 0\ns_cbranch_vccnz retry2_x0\ns_branch landed_x0\n"
"adopt_x0:\nbuffer_load_dwordx4 v[60:63], v28, s[8:11], 0 offen sc1\ns_lshl_b32 s52, s40, 26\ns_or_b32 s47, s52, 0x3ffffff\n"
"s_waitcnt vmcnt(0)\nv_cmp_ge_u32 vcc, s47, v60\ns_nop 1\nv_cndmask_b32 v60, 0, v60, vcc\n"
"v_cmp_ge_u32 vcc, s47, v61\ns_nop 1\nv_cndmask_b32 v61, 0, v61, vcc\nv_cmp_ge_u32 vcc, s47, v62\n"
"s_nop 1\nv_cndmask_b32 v62, 0, v62, vcc\nv_cmp_ge_u32 vcc, s47, v63\ns_nop 1\n"
"v_cndmask_b32 v63, 0, v63, vcc\nv_max3_u32 v83, v60, v61, v62\nv_max_u32 v83, v83, v63\ns_nop 1\n"
"v_max_u32_dpp v83, v83, v83 row_ror:8 row_mask:0xf bank_mask:0xf\ns_nop 1\nv_max_u32_dpp v83, v83, v83 row_ror:4 row_mask:0xf bank_mask:0xf\ns_nop 1\n"
"v_max_u32_dpp v83, v83, v83 row_ror:2 row_mask:0xf bank_mask:0xf\ns_nop 1\nv_max_u32_dpp v83, v83, v83 row_ror:1 row_mask:0xf bank_mask:0xf\ns_nop 0\n"
"v_readlane_b32 s47, v83, 0\nv_readlane_b32 s48, v83, 16\ns_max_u32 s48, s48, s47\ns_cmp_le_u32 s48, s52\n"
"s_cbranch_scc1 back_x0\ns_bfe_u32 s52, s48, 1179656\ns_add_u32 s44, s52, s28\ns_mov_b32 s45, 0\n"
"s_and_b32 s46, s48, 255\ns_mov_b32 s92, 0\ns_branch back_x0\nend_x0:\n"
"s_branch fk_done\nfk_x1:\ns_mov_b32 s40, 0\ns_mov_b32 s41, 0\n"
"s_mov_b32 s42, 262144\ns_mov_b32 s43, 393216\ns_mov_b32 s44, s28\ns_mov_b32 s45, 0\n"
"s_mov_b32 s46, 0\ns_mov_b32 s92, 0\ns_mov_b32 s49, 0\ns_mov_b32 s53, 0\n"
"s_mov_b32 s50, s35\ns_mov_b32 s51, s36\nv_mov_b32 v81, v13\nv_mov_b32 v82, v14\n"
"v_mov_b32 v84, 24576\ns_add_u32 s51, s50, 14336\nv_mov_b32 v64, -1\nv_mov_b32 v65, -1\n"
"v_mov_b32 v66, -1\nv_mov_b32 v67, -1\nbuffer_load_dwordx4 a[8:11], v9, s[16:19], s50 offen offset:2048 sc1\nbuffer_load_dwordx4 a[12:15], v9, s[16:19], s50 offen offset:3072 sc1\n"
"buffer_load_dwordx4 a[0:3], v9, s[16:19], s50 offen offset:0 sc1\nbuffer_load_dwordx4 a[4:7], v9, s[16:19], s50 offen offset:1024 sc1\nbuffer_load_dwordx4 a[24:27], v9, s[16:19], s51 offen offset:2048 sc1\nbuffer_load_dwordx4 a[28:31], v9, s[16:19], s51 offen offset:3072 sc1\n"
"buffer_load_dwordx4 a[16:19], v9, s[16:19], s51 offen offset:0 sc1\nbuffer_load_dwordx4 a[20:23], v9, s[16:19], s51 offen offset:1024 sc1\ns_add_u32 s50, s50, 28672\ns_add_u32 s51, s51, 28672\n"
"buffer_load_dword v85, v18, s[8:11], 0 offen sc1\ns_waitcnt vmcnt(0)\nf0_x1:\ns_add_u32 s47, s40, 2\n"
"s_min_u32 s47, s47, 64\ns_add_u32 s47, s47, s33\nv_subrev_u32 v85, s47, v85\nv_cmp_lt_u32 vcc, 64, v85\n"
"s_cbranch_vccz f0ok_x1\ns_add_u32 s53, s53, 0\ns_add_u32 s49, s49, 1\ns_cmp_gt_u32 s49, 65536\n"
"s_cbranch_scc1 end_x1\ns_sleep 2\nbuffer_load_dword v85, v18, s[8:11], 0 offen sc1\ns_waitcnt vmcnt(0)\n"
"s_branch f0_x1\nf0ok_x1:\ns_barrier\ns_memrealtime s[54:55]\n"
"s_waitcnt lgkmcnt(0)\ns_add_u32 s44, s54, 16\ns_mov_b32 s45, 8\n.p2align 6\n"
"top_x1:\nv_mov_b32 v48, 0\nv_mov_b32 v49, 0\nv_mov_b32 v50, 0\n"
"v_mov_b32 v51, 0\nv_mov_b32 v52, 0\nv_mov_b32 v53, 0\nv_mov_b32 v54, 0\n"
"v_mov_b32 v55, 0\nv_mov_b32 v78, 0\ns_mul_i32 s48, s45, 6\ns_and_b32 s48, s48, -4\n"
"s_getpc_b64 s[60:61]\nspc_x1:\ns_sub_u32 s48, sgo_x1-spc_x1, s48\ns_add_u32 s60, s60, s48\n"
"s_addc_u32 s61, s61, 0\nsf_x1:\ns_memrealtime s[54:55]\ns_nop 2\n"
"s_memrealtime s[56:57]\ns_nop 2\ns_memrealtime s[58:59]\ns_waitcnt lgkmcnt(4)\n"
"s_sub_u32 s47, s54, s44\ns_add_u32 s48, s47, 12\ns_cmp_ge_u32 s48, 12\ns_cbranch_scc1 so_x1\n"
"s_memrealtime s[54:55]\ns_waitcnt lgkmcnt(4)\ns_cmp_ge_u32 s56, s44\ns_cbranch_scc1 sx_x1\n"
"s_memrealtime s[56:57]\ns_waitcnt lgkmcnt(4)\ns_cmp_ge_u32 s58, s44\ns_cbranch_scc1 sx_x1\n"
"s_memrealtime s[58:59]\ns_waitcnt lgkmcnt(4)\ns_cmp_ge_u32 s54, s44\ns_cbranch_scc1 sx_x1\n"
"s_memrealtime s[54:55]\ns_waitcnt lgkmcnt(4)\ns_cmp_ge_u32 s56, s44\ns_cbranch_scc1 sx_x1\n"
"s_memrealtime s[56:57]\ns_waitcnt lgkmcnt(4)\ns_cmp_ge_u32 s58, s44\ns_cbranch_scc1 sx_x1\n"
"s_memrealtime s[58:59]\ns_waitcnt lgkmcnt(4)\ns_cmp_ge_u32 s54, s44\ns_cbranch_scc1 sx_x1\n"
"s_memrealtime s[54:55]\ns_waitcnt lgkmcnt(4)\ns_cmp_ge_u32 s56, s44\ns_cbranch_scc1 sx_x1\n"
"s_memrealtime s[56:57]\ns_waitcnt lgkmcnt(4)\ns_cmp_ge_u32 s58, s44\ns_cbranch_scc1 sx_x1\n"
"s_memrealtime s[58:59]\ns_waitcnt lgkmcnt(4)\ns_cmp_ge_u32 s54, s44\ns_cbranch_scc1 sx_x1\n"
"s_memrealtime s[54:55]\ns_waitcnt lgkmcnt(4)\ns_cmp_ge_u32 s56, s44\ns_cbranch_scc1 sx_x1\n"
"s_memrealtime s[56:57]\ns_waitcnt lgkmcnt(4)\ns_cmp_ge_u32 s58, s44\ns_cbranch_scc1 sx_x1\n"
"s_memrealtime s[58:59]\ns_waitcnt lgkmcnt(4)\ns_cmp_ge_u32 s54, s44\ns_cbranch_scc1 sx_x1\n"
"s_memrealtime s[54:55]\ns_waitcnt lgkmcnt(4)\ns_cmp_ge_u32 s56, s44\ns_cbranch_scc1 sx_x1\n"
"s_memrealtime s[56:57]\ns_waitcnt lgkmcnt(4)\ns_cmp_ge_u32 s58, s44\ns_cbranch_scc1 sx_x1\n"
"s_memrealtime s[58:59]\ns_waitcnt lgkmcnt(4)\ns_cmp_ge_u32 s54, s44\ns_cbranch_scc1 sx_x1\n"
"s_memrealtime s[54:55]\ns_waitcnt lgkmcnt(4)\ns_cmp_ge_u32 s56, s44\ns_cbranch_scc1 sx_x1\n"
"s_memrealtime s[56:57]\ns_branch sx_x1\nso_x1:\ns_waitcnt lgkmcnt(0)\n"
"s_cmp_lt_i32 s47, 0\ns_cbranch_scc1 sf_x1\ns_branch sgo_x1\nsx_x1:\n"
"s_setpc_b64 s[60:61]\ns_nop 0\ns_nop 0\ns_nop 0\n"
"s_nop 0\ns_nop 0\ns_nop 0\ns_nop 0\n"
"s_nop 0\ns_nop 0\ns_nop 0\ns_nop 0\n"
"s_nop 0\ns_nop 0\ns_nop 0\ns_nop 0\n"
"s_nop 0\ns_nop 0\ns_nop 0\ns_nop 0\n"
"s_nop 0\ns_nop 0\ns_nop 0\ns_nop 0\n"
"s_nop 0\nsgo_x1:\ns_nop 5\nbuffer_load_dwordx4 v[40:43], v10, s[8:11], s43 offen offset:1024 sc1\n"
"buffer_load_dwordx4 v[44:47], v10, s[8:11], s43 offen offset:0 sc1\nlanded_x1:\ns_waitcnt vmcnt(1)\nv_smfmac_f32_16x16x64_bf16 v[48:51], v[40:43], a[8:15], v8\n"
"v_max3_u32 v78, v78, v40, v41\nv_smfmac_f32_16x16x64_bf16 v[52:55], v[40:43], a[24:31], v8\nv_max3_u32 v78, v78, v42, v43\ns_waitcnt vmcnt(0)\n"
"v_smfmac_f32_16x16x64_bf16 v[48:51], v[44:47], a[0:7], v8\nv_max3_u32 v78, v78, v44, v45\nv_smfmac_f32_16x16x64_bf16 v[52:55], v[44:47], a[16:23], v8\nv_max3_u32 v78, v78, v46, v47\n"
"v_cmp_eq_u32 vcc, -1, v78\ns_nop 0\nv_add_f32 v56, v48, v49\nv_add_f32 v57, v50, v51\n"
"s_nop 1\nv_add_f32 v58, v52, v53\nv_add_f32 v59, v54, v55\nds_write2_b32 v81, v56, v57 offset0:0 offset1:40\n"
"ds_write2_b32 v81, v58, v59 offset0:16 offset1:56\ns_cbranch_vccnz retry_x1\nv_mov_b32 v79, 0\ns_nop 1\n"
"v_mov_b32 v79, 0\ns_nop 1\nv_mov_b32 v79, 0\ns_waitcnt lgkmcnt(0)\n"
"s_barrier\nds_read_b64 v[68:69], v82 offset:0\nds_read_b64 v[70:71], v82 offset:1280\nds_read_b64 v[72:73], v82 offset:2560\n"
"ds_read_b64 v[74:75], v82 offset:3840\nv_mov_b32 v79, 0\ns_nop 1\nv_mov_b32 v79, 0\n"
"s_nop 1\nv_mov_b32 v79, 0\ns_nop 1\ns_waitcnt lgkmcnt(2)\n"
"v_pk_add_f32 v[68:69], v[68:69], v[70:71]\ns_waitcnt lgkmcnt(0)\nv_pk_add_f32 v[72:73], v[72:73], v[74:75]\nv_pk_add_f32 v[68:69], v[68:69], v[72:73]\n"
"v_exp_f32 v79, v68\nv_exp_f32 v80, v69\ns_nop 0\nv_cvt_pk_bf16_f32 v79, v79, v80\n"
"buffer_store_dword v79, v16, s[8:11], s41 offen sc1\ns_barrier\nbuffer_load_dwordx4 a[8:11], v9, s[16:19], s50 offen offset:2048 sc1\ns_nop 15\n"
"s_nop 7\nbuffer_load_dwordx4 a[12:15], v9, s[16:19], s50 offen offset:3072 sc1\ns_nop 15\ns_nop 7\n"
"buffer_load_dwordx4 a[0:3], v9, s[16:19], s50 offen offset:0 sc1\ns_nop 15\ns_nop 7\nbuffer_load_dwordx4 a[4:7], v9, s[16:19], s50 offen offset:1024 sc1\n"
"s_nop 15\ns_nop 7\nbuffer_load_dwordx4 a[24:27], v9, s[16:19], s51 offen offset:2048 sc1\ns_nop 15\n"
"s_nop 7\nbuffer_load_dwordx4 a[28:31], v9, s[16:19], s51 offen offset:3072 sc1\ns_nop 15\ns_nop 7\n"
"buffer_load_dwordx4 a[16:19], v9, s[16:19], s51 offen offset:0 sc1\ns_nop 15\ns_nop 7\nbuffer_load_dwordx4 a[20:23], v9, s[16:19], s51 offen offset:1024 sc1\n"
"s_nop 15\ns_nop 7\ns_add_u32 s50, s50, 28672\ns_add_u32 s51, s51, 28672\n"
"buffer_store_dwordx2 v[68:69], v17, s[8:11], s41 offen sc1\nbuffer_store_dwordx4 v[64:67], v15, s[8:11], s42 offen sc1\nds_read_b32 v83, v84\nv_xor_b32 v81, 8192, v81\n"
"v_xor_b32 v82, 8192, v82\ns_waitcnt lgkmcnt(0)\nv_readfirstlane_b32 s47, v83\ns_sub_u32 s47, s47, s40\n"
"s_cmp_eq_u32 s47, 1\ns_cbranch_scc1 adopt_x1\nback_x1:\ns_add_u32 s40, s40, 1\n"
"s_mov_b32 s43, s41\ns_add_u32 s41, s41, 131072\ns_and_b32 s41, s41, 524287\ns_xor_b32 s42, s41, 262144\n"
"s_and_b32 s47, s46, 15\ns_mul_i32 s48, s47, 8\ns_add_u32 s45, s45, s48\ns_add_u32 s45, s45, s30\n"
"s_lshr_b32 s48, s45, 4\ns_lshl_b32 s48, s48, 2\ns_add_u32 s44, s44, s48\ns_and_b32 s45, s45, 15\n"
"s_add_u32 s92, s92, 1\ns_waitcnt lgkmcnt(0)\ns_cmp_lt_u32 s40, 63\ns_cbranch_scc1 top_x1\n"
"s_branch end_x1\nretry2_x1:\ns_sleep 3\nretry_x1:\n"
"s_add_u32 s53, s53, 1\ns_add_u32 s49, s49, 1\ns_cmp_gt_u32 s49, 65536\ns_cbranch_scc1 end_x1\n"
"buffer_load_dwordx4 v[40:43], v10, s[8:11], s43 offen offset:1024 sc1\nbuffer_load_dwordx4 v[44:47], v10, s[8:11], s43 offen offset:0 sc1\nv_mov_b32 v78, 0\ns_add_u32 s47, s40, 1\n"
"v_mov_b32 v83, s47\nds_write_b32 v84, v83\ns_waitcnt vmcnt(0)\nv_max3_u32 v78, v78, v40, v41\n"
"v_max3_u32 v78, v78, v42, v43\nv_max3_u32 v78, v78, v44, v45\nv_max3_u32 v78, v78, v46, v47\nv_cmp_eq_u32 vcc, -1, v78\n"
"v_mov_b32 v48, 0\nv_mov_b32 v49, 0\nv_mov_b32 v50, 0\nv_mov_b32 v51, 0\n"
"v_mov_b32 v52, 0\nv_mov_b32 v53, 0\nv_mov_b32 v54, 0\nv_mov_b32 v55, 0\n"
"s_cbranch_vccnz retry2_x1\ns_branch landed_x1\nadopt_x1:\nbuffer_load_dwordx4 v[60:63], v28, s[8:11], 0 offen sc1\n"
"s_lshl_b32 s52, s40, 26\ns_or_b32 s47, s52, 0x3ffffff\ns_waitcnt vmcnt(0)\nv_cmp_ge_u32 vcc, s47, v60\n"
"s_nop 1\nv_cndmask_b32 v60, 0, v60, vcc\nv_cmp_ge_u32 vcc, s47, v61\ns_nop 1\n"
"v_cndmask_b32 v61, 0, v61, vcc\nv_cmp_ge_u32 vcc, s47, v62\ns_nop 1\nv_cndmask_b32 v62, 0, v62, vcc\n"
"v_cmp_ge_u32 vcc, s47, v63\ns_nop 1\nv_cndmask_b32 v63, 0, v63, vcc\nv_max3_u32 v83, v60, v61, v62\n"
"v_max_u32 v83, v83, v63\ns_nop 1\nv_max_u32_dpp v83, v83, v83 row_ror:8 row_mask:0xf bank_mask:0xf\ns_nop 1\n"
"v_max_u32_dpp v83, v83, v83 row_ror:4 row_mask:0xf bank_mask:0xf\ns_nop 1\nv_max_u32_dpp v83, v83, v83 row_ror:2 row_mask:0xf bank_mask:0xf\ns_nop 1\n"
"v_max_u32_dpp v83, v83, v83 row_ror:1 row_mask:0xf bank_mask:0xf\ns_nop 0\nv_readlane_b32 s47, v83, 0\nv_readlane_b32 s48, v83, 16\n"
"s_max_u32 s48, s48, s47\ns_cmp_le_u32 s48, s52\ns_cbranch_scc1 back_x1\ns_bfe_u32 s52, s48, 1179656\n"
"s_add_u32 s44, s52, s28\ns_mov_b32 s45, 0\ns_and_b32 s46, s48, 255\ns_mov_b32 s92, 0\n"
"s_branch back_x1\nend_x1:\ns_branch fk_done\nfk_x2:\n"
"s_mov_b32 s40, 0\ns_mov_b32 s41, 0\ns_mov_b32 s42, 262144\ns_mov_b32 s43, 393216\n"
"s_mov_b32 s44, s28\ns_mov_b32 s45, 0\ns_mov_b32 s46, 0\ns_mov_b32 s92, 0\n"
"s_mov_b32 s49, 0\ns_mov_b32 s53, 0\ns_mov_b32 s50, s35\ns_mov_b32 s51, s36\n"
"v_mov_b32 v81, v13\nv_mov_b32 v82, v14\nv_mov_b32 v84, 24576\ns_add_u32 s51, s50, 14336\n"
"v_mov_b32 v64, -1\nv_mov_b32 v65, -1\nv_mov_b32 v66, -1\nv_mov_b32 v67, -1\n"
"buffer_load_dwordx4 a[0:3], v9, s[16:19], s50 offen offset:0 sc1\nbuffer_load_dwordx4 a[4:7], v9, s[16:19], s50 offen offset:1024 sc1\nbuffer_load_dwordx4 a[8:11], v9, s[16:19], s50 offen offset:2048 sc1\nbuffer_load_dwordx4 a[12:15], v9, s[16:19], s50 offen offset:3072 sc1\n"
"buffer_load_dwordx4 a[16:19], v9, s[16:19], s51 offen offset:0 sc1\nbuffer_load_dwordx4 a[20:23], v9, s[16:19], s51 offen offset:1024 sc1\nbuffer_load_dwordx4 a[24:27], v9, s[16:19], s51 offen offset:2048 sc1\nbuffer_load_dwordx4 a[28:31], v9, s[16:19], s51 offen offset:3072 sc1\n"
"s_add_u32 s50, s50, 28672\ns_add_u32 s51, s51, 28672\nbuffer_load_dword v85, v18, s[8:11], 0 offen sc1\ns_waitcnt vmcnt(0)\n"
"f0_x2:\ns_add_u32 s47, s40, 2\ns_min_u32 s47, s47, 64\ns_add_u32 s47, s47, s33\n"
"v_subrev_u32 v85, s47, v85\nv_cmp_lt_u32 vcc, 64, v85\ns_cbranch_vccz f0ok_x2\ns_add_u32 s53, s53, 0\n"
"s_add_u32 s49, s49, 1\ns_cmp_gt_u32 s49, 65536\ns_cbranch_scc1 end_x2\ns_sleep 2\n"
"buffer_load_dword v85, v18, s[8:11], 0 offen sc1\ns_waitcnt vmcnt(0)\ns_branch f0_x2\nf0ok_x2:\n"
"s_barrier\ns_memrealtime s[54:55]\ns_waitcnt lgkmcnt(0)\ns_add_u32 s44, s54, 16\n"
"s_mov_b32 s45, 8\n.p2align 6\ntop_x2:\nv_mov_b32 v48, 0\n"
"v_mov_b32 v49, 0\nv_mov_b32 v50, 0\nv_mov_b32 v51, 0\nv_mov_b32 v52, 0\n"
"v_mov_b32 v53, 0\nv_mov_b32 v54, 0\nv_mov_b32 v55, 0\nv_mov_b32 v78, 0\n"
"s_mul_i32 s48, s45, 6\ns_and_b32 s48, s48, -4\ns_getpc_b64 s[60:61]\nspc_x2:\n"
"s_sub_u32 s48, sgo_x2-spc_x2, s48\ns_add_u32 s60, s60, s48\ns_addc_u32 s61, s61, 0\nsf_x2:\n"
"s_memrealtime s[54:55]\ns_nop 2\ns_memrealtime s[56:57]\ns_nop 2\n"
"s_memrealtime s[58:59]\ns_waitcnt lgkmcnt(4)\ns_sub_u32 s47, s54, s44\ns_add_u32 s48, s47, 12\n"
"s_cmp_ge_u32 s48, 12\ns_cbranch_scc1 so_x2\ns_memrealtime s[54:55]\ns_waitcnt lgkmcnt(4)\n"
"s_cmp_ge_u32 s56, s44\ns_cbranch_scc1 sx_x2\ns_memrealtime s[56:57]\ns_waitcnt lgkmcnt(4)\n"
"s_cmp_ge_u32 s58, s44\ns_cbranch_scc1 sx_x2\ns_memrealtime s[58:59]\ns_waitcnt lgkmcnt(4)\n"
"s_cmp_ge_u32 s54, s44\ns_cbranch_scc1 sx_x2\ns_memrealtime s[54:55]\ns_waitcnt lgkmcnt(4)\n"
"s_cmp_ge_u32 s56, s44\ns_cbranch_scc1 sx_x2\ns_memrealtime s[56:57]\ns_waitcnt lgkmcnt(4)\n"
"s_cmp_ge_u32 s58, s44\ns_cbranch_scc1 sx_x2\ns_memrealtime s[58:59]\ns_waitcnt lgkmcnt(4)\n"
"s_cmp_ge_u32 s54, s44\ns_cbranch_scc1 sx_x2\ns_memrealtime s[54:55]\ns_waitcnt lgkmcnt(4)\n"
"s_cmp_ge_u32 s56, s44\ns_cbranch_scc1 sx_x2\ns_memrealtime s[56:57]\ns_waitcnt lgkmcnt(4)\n"
"s_cmp_ge_u32 s58, s44\ns_cbranch_scc1 sx_x2\ns_memrealtime s[58:59]\ns_waitcnt lgkmcnt(4)\n"
"s_cmp_ge_u32 s54, s44\ns_cbranch_scc1 sx_x2\ns_memrealtime s[54:55]\ns_waitcnt lgkmcnt(4)\n"
"s_cmp_ge_u32 s56, s44\ns_cbranch_scc1 sx_x2\ns_memrealtime s[56:57]\ns_waitcnt lgkmcnt(4)\n"
"s_cmp_ge_u32 s58, s44\ns_cbranch_scc1 sx_x2\ns_memrealtime s[58:59]\ns_waitcnt lgkmcnt(4)\n"
"s_cmp_ge_u32 s54, s44\ns_cbranch_scc1 sx_x2\ns_memrealtime s[54:55]\ns_waitcnt lgkmcnt(4)\n"
"s_cmp_ge_u32 s56, s44\ns_cbranch_scc1 sx_x2\ns_memrealtime s[56:57]\ns_waitcnt lgkmcnt(4)\n"
"s_cmp_ge_u32 s58, s44\ns_cbranch_scc1 sx_x2\ns_memrealtime s[58:59]\ns_waitcnt lgkmcnt(4)\n"
"s_cmp_ge_u32 s54, s44\ns_cbranch_scc1 sx_x2\ns_memrealtime s[54:55]\ns_waitcnt lgkmcnt(4)\n"
"s_cmp_ge_u32 s56, s44\ns_cbranch_scc1 sx_x2\ns_memrealtime s[56:57]\ns_branch sx_x2\n"
"so_x2:\ns_waitcnt lgkmcnt(0)\ns_cmp_lt_i32 s47, 0\ns_cbranch_scc1 sf_x2\n"
"s_branch sgo_x2\nsx_x2:\ns_setpc_b64 s[60:61]\ns_nop 0\n"
"s_nop 0\ns_nop 0\ns_nop 0\ns_nop 0\n"
"s_nop 0\ns_nop 0\ns_nop 0\ns_nop 0\n"
"s_nop 0\ns_nop 0\ns_nop 0\ns_nop 0\n"
"s_nop 0\ns_nop 0\ns_nop 0\ns_nop 0\n"
"s_nop 0\ns_nop 0\ns_nop 0\ns_nop 0\n"
"s_nop 0\ns_nop 0\ns_nop 0\nsgo_x2:\n"
"s_nop 11\nbuffer_load_dwordx4 v[40:43], v10, s[8:11], s43 offen offset:0 sc1\nbuffer_load_dwordx4 v[44:47], v10, s[8:11], s43 offen offset:1024 sc1\nv_mov_b32 v79, 0\n"
"s_nop 1\nv_mov_b32 v79, 0\ns_nop 1\nv_mov_b32 v79, 0\n"
"s_nop 1\nv_mov_b32 v79, 0\ns_nop 1\nv_mov_b32 v79, 0\n"
"s_nop 1\nv_mov_b32 v79, 0\ns_nop 1\nv_mov_b32 v79, 0\n"
"s_nop 1\nv_mov_b32 v79, 0\ns_nop 1\nv_mov_b32 v79, 0\n"
"s_nop 1\nv_mov_b32 v79, 0\ns_nop 1\nv_mov_b32 v79, 0\n"
"s_nop 1\nv_mov_b32 v79, 0\ns_nop 1\nv_mov_b32 v79, 0\n"
"s_nop 1\nv_mov_b32 v79, 0\ns_nop 1\nv_mov_b32 v79, 0\n"
"s_nop 1\nv_mov_b32 v79, 0\ns_nop 1\nv_mov_b32 v79, 0\n"
"s_nop 1\nv_mov_b32 v79, 0\ns_nop 1\nv_mov_b32 v79, 0\n"
"s_nop 1\nv_mov_b32 v79, 0\ns_nop 1\nv_mov_b32 v79, 0\n"
"s_nop 1\nv_mov_b32 v79, 0\ns_nop 1\nv_mov_b32 v79, 0\n"
"s_nop 1\nv_mov_b32 v79, 0\ns_nop 1\nv_mov_b32 v79, 0\n"
"s_nop 1\nv_mov_b32 v79, 0\ns_nop 1\nv_mov_b32 v79, 0\n"
"s_nop 1\nv_mov_b32 v79, 0\ns_nop 1\nv_mov_b32 v79, 0\n"
"landed_x2:\ns_waitcnt vmcnt(1)\nv_smfmac_f32_16x16x64_bf16 v[48:51], v[40:43], a[0:7], v8\nv_max3_u32 v78, v78, v40, v41\n"
"v_smfmac_f32_16x16x64_bf16 v[52:55], v[40:43], a[16:23], v8\nv_max3_u32 v78, v78, v42, v43\ns_waitcnt vmcnt(0)\nv_smfmac_f32_16x16x64_bf16 v[48:51], v[44:47], a[8:15], v8\n"
"v_max3_u32 v78, v78, v44, v45\nv_smfmac_f32_16x16x64_bf16 v[52:55], v[44:47], a[24:31], v8\nv_max3_u32 v78, v78, v46, v47\nv_cmp_eq_u32 vcc, -1, v78\n"
"s_nop 0\nv_add_f32 v56, v48, v49\nv_add_f32 v57, v50, v51\ns_nop 1\n"
"v_add_f32 v58, v52, v53\nv_add_f32 v59, v54, v55\nds_write2_b32 v81, v56, v57 offset0:0 offset1:40\nds_write2_b32 v81, v58, v59 offset0:16 offset1:56\n"
"s_cbranch_vccnz retry_x2\nv_mov_b32 v79, 0\ns_nop 1\nv_mov_b32 v79, 0\n"
"s_nop 1\nv_mov_b32 v79, 0\ns_waitcnt lgkmcnt(0)\ns_barrier\n"
"ds_read_b64 v[68:69], v82 offset:0\nds_read_b64 v[70:71], v82 offset:1280\nds_read_b64 v[72:73], v82 offset:2560\nds_read_b64 v[74:75], v82 offset:3840\n"
"v_mov_b32 v79, 0\ns_nop 1\nv_mov_b32 v79, 0\ns_nop 1\n"
"v_mov_b32 v79, 0\ns_nop 1\ns_waitcnt lgkmcnt(2)\nv_pk_add_f32 v[68:69], v[68:69], v[70:71]\n"
"s_waitcnt lgkmcnt(0)\nv_pk_add_f32 v[72:73], v[72:73], v[74:75]\nv_pk_add_f32 v[68:69], v[68:69], v[72:73]\nv_exp_f32 v79, v68\n"
"v_exp_f32 v80, v69\ns_nop 0\nv_cvt_pk_bf16_f32 v79, v79, v80\nbuffer_store_dword v79, v16, s[8:11], s41 offen sc1\n"
"s_barrier\nbuffer_load_dwordx4 a[0:3], v9, s[16:19], s50 offen offset:0 sc1\ns_nop 15\ns_nop 7\n"
"buffer_load_dwordx4 a[4:7], v9, s[16:19], s50 offen offset:1024 sc1\ns_nop 15\ns_nop 7\nbuffer_load_dwordx4 a[8:11], v9, s[16:19], s50 offen offset:2048 sc1\n"
"s_nop 15\ns_nop 7\nbuffer_load_dwordx4 a[12:15], v9, s[16:19], s50 offen offset:3072 sc1\ns_nop 15\n"
"s_nop 7\nbuffer_load_dwordx4 a[16:19], v9, s[16:19], s51 offen offset:0 sc1\ns_nop 15\ns_nop 7\n"
"buffer_load_dwordx4 a[20:23], v9, s[16:19], s51 offen offset:1024 sc1\ns_nop 15\ns_nop 7\nbuffer_load_dwordx4 a[24:27], v9, s[16:19], s51 offen offset:2048 sc1\n"
"s_nop 15\ns_nop 7\nbuffer_load_dwordx4 a[28:31], v9, s[16:19], s51 offen offset:3072 sc1\ns_nop 15\n"
"s_nop 7\ns_add_u32 s50, s50, 28672\ns_add_u32 s51, s51, 28672\nbuffer_store_dwordx2 v[68:69], v17, s[8:11], s41 offen sc1\n"
"buffer_store_dwordx4 v[64:67], v15, s[8:11], s42 offen sc1\nds_read_b32 v83, v84\nv_xor_b32 v81, 8192, v81\nv_xor_b32 v82, 8192, v82\n"
"s_waitcnt lgkmcnt(0)\nv_readfirstlane_b32 s47, v83\ns_sub_u32 s47, s47, s40\ns_cmp_eq_u32 s47, 1\n"
"s_cbranch_scc1 adopt_x2\nback_x2:\ns_add_u32 s40, s40, 1\ns_mov_b32 s43, s41\n"
"s_add_u32 s41, s41, 131072\ns_and_b32 s41, s41, 524287\ns_xor_b32 s42, s41, 262144\ns_and_b32 s47, s46, 15\n"
"s_mul_i32 s48, s47, 8\ns_add_u32 s45, s45, s48\ns_add_u32 s45, s45, s30\ns_lshr_b32 s48, s45, 4\n"
"s_lshl_b32 s48, s48, 2\ns_add_u32 s44, s44, s48\ns_and_b32 s45, s45, 15\ns_add_u32 s92, s92, 1\n"
"s_waitcnt lgkmcnt(0)\ns_cmp_lt_u32 s40, 63\ns_cbranch_scc1 top_x2\ns_branch end_x2\n"
"retry2_x2:\ns_sleep 3\nretry_x2:\ns_add_u32 s53, s53, 1\n"
"s_add_u32 s49, s49, 1\ns_cmp_gt_u32 s49, 65536\ns_cbranch_scc1 end_x2\nbuffer_load_dwordx4 v[40:43], v10, s[8:11], s43 offen offset:0 sc1\n"
"buffer_load_dwordx4 v[44:47], v10, s[8:11], s43 offen offset:1024 sc1\nv_mov_b32 v78, 0\ns_add_u32 s47, s40, 1\nv_mov_b32 v83, s47\n"
"ds_write_b32 v84, v83\ns_waitcnt vmcnt(0)\nv_max3_u32 v78, v78, v40, v41\nv_max3_u32 v78, v78, v42, v43\n"
"v_max3_u32 v78, v78, v44, v45\nv_max3_u32 v78, v78, v46, v47\nv_cmp_eq_u32 vcc, -1, v78\nv_mov_b32 v48, 0\n"
"v_mov_b32 v49, 0\nv_mov_b32 v50, 0\nv_mov_b32 v51, 0\nv_mov_b32 v52, 0\n"
"v_mov_b32 v53, 0\nv_mov_b32 v54, 0\nv_mov_b32 v55, 0\ns_cbranch_vccnz retry2_x2\n"
"s_branch landed_x2\nadopt_x2:\nbuffer_load_dwordx4 v[60:63], v28, s[8:11], 0 offen sc1\ns_lshl_b32 s52, s40, 26\n"
"s_or_b32 s47, s52, 0x3ffffff\ns_waitcnt vmcnt(0)\nv_cmp_ge_u32 vcc, s47, v60\ns_nop 1\n"
"v_cndmask_b32 v60, 0, v60, vcc\nv_cmp_ge_u32 vcc, s47, v61\ns_nop 1\nv_cndmask_b32 v61, 0, v61, vcc\n"
"v_cmp_ge_u32 vcc, s47, v62\ns_nop 1\nv_cndmask_b32 v62, 0, v62, vcc\nv_cmp_ge_u32 vcc, s47, v63\n"
"s_nop 1\nv_cndmask_b32 v63, 0, v63, vcc\nv_max3_u32 v83, v60, v61, v62\nv_max_u32 v83, v83, v63\n"
"s_nop 1\nv_max_u32_dpp v83, v83, v83 row_ror:8 row_mask:0xf bank_mask:0xf\ns_nop 1\nv_max_u32_dpp v83, v83, v83 row_ror:4 row_mask:0xf bank_mask:0xf\n"
"s_nop 1\nv_max_u32_dpp v83, v83, v83 row_ror:2 row_mask:0xf bank_mask:0xf\ns_nop 1\nv_max_u32_dpp v83, v83, v83 row_ror:1 row_mask:0xf bank_mask:0xf\n"
"s_nop 0\nv_readlane_b32 s47, v83, 0\nv_readlane_b32 s48, v83, 16\ns_max_u32 s48, s48, s47\n"
"s_cmp_le_u32 s48, s52\ns_cbranch_scc1 back_x2\ns_bfe_u32 s52, s48, 1179656\ns_add_u32 s44, s52, s28\n"
"s_mov_b32 s45, 0\ns_and_b32 s46, s48, 255\ns_mov_b32 s92, 0\ns_branch back_x2\n"
"end_x2:\ns_branch fk_done\nfk_x3:\ns_mov_b32 s40, 0\n"
"s_mov_b32 s41, 0\ns_mov_b32 s42, 262144\ns_mov_b32 s43, 393216\ns_mov_b32 s44, s28\n"
"s_mov_b32 s45, 0\ns_mov_b32 s46, 0\ns_mov_b32 s92, 0\ns_mov_b32 s49, 0\n"
"s_mov_b32 s53, 0\ns_mov_b32 s50, s35\ns_mov_b32 s51, s36\nv_mov_b32 v77, v13\n"
"v_mov_b32 v78, v14\nv_mov_b32 v80, 24576\ns_add_u32 s51, s50, 14336\nv_mov_b32 v60, -1\n"
"v_mov_b32 v61, -1\nv_mov_b32 v62, -1\nv_mov_b32 v63, -1\nbuffer_load_dwordx4 a[0:3], v9, s[16:19], s50 offen offset:0 sc1\n"
"buffer_load_dwordx4 a[4:7], v9, s[16:19], s50 offen offset:1024 sc1\nbuffer_load_dwordx4 a[16:19], v9, s[16:19], s51 offen offset:0 sc1\nbuffer_load_dwordx4 a[20:23], v9, s[16:19], s51 offen offset:1024 sc1\ns_add_u32 s50, s50, 28672\n"
"s_add_u32 s51, s51, 28672\nv_lshlrev_b32 v58, 2, v9\ns_mov_b32 s52, 1064960\nbuffer_load_dword v56, v58, s[8:11], s52 offen sc1\n"
"s_add_u32 s52, s52, 4096\nbuffer_load_dword v57, v58, s[8:11], s52 offen sc1\nbuffer_load_dword v81, v18, s[8:11], 0 offen sc1\ns_waitcnt vmcnt(0)\n"
"ar_x3:\nv_xor_b32 v56, s33, v56\nv_xor_b32 v57, s33, v57\nv_or_b32 v56, v56, v57\n"
"v_cmp_ne_u32 vcc, 0, v56\ns_cbranch_vccz arok_x3\ns_add_u32 s53, s53, 0\ns_add_u32 s49, s49, 1\n"
"s_cmp_gt_u32 s49, 65536\ns_cbranch_scc1 end_x3\ns_sleep 2\nv_lshlrev_b32 v58, 2, v9\n"
"s_mov_b32 s52, 1064960\nbuffer_load_dword v56, v58, s[8:11], s52 offen sc1\ns_add_u32 s52, s52, 4096\nbuffer_load_dword v57, v58, s[8:11], s52 offen sc1\n"
"s_waitcnt vmcnt(0)\ns_branch ar_x3\narok_x3:\nf0_x3:\n"
"s_add_u32 s47, s40, 2\ns_min_u32 s47, s47, 64\ns_add_u32 s47, s47, s33\nv_subrev_u32 v81, s47, v81\n"
"v_cmp_lt_u32 vcc, 64, v81\ns_cbranch_vccz f0ok_x3\ns_add_u32 s53, s53, 0\ns_add_u32 s49, s49, 1\n"
"s_cmp_gt_u32 s49, 65536\ns_cbranch_scc1 end_x3\ns_sleep 2\nbuffer_load_dword v81, v18, s[8:11], 0 offen sc1\n"
"s_waitcnt vmcnt(0)\ns_branch f0_x3\nf0ok_x3:\ns_barrier\n"
"s_memrealtime s[54:55]\ns_waitcnt lgkmcnt(0)\ns_add_u32 s44, s54, 16\ns_mov_b32 s45, 8\n"
".p2align 6\ntop_x3:\nv_mov_b32 v44, 0\nv_mov_b32 v45, 0\n"
"v_mov_b32 v46, 0\nv_mov_b32 v47, 0\nv_mov_b32 v48, 0\nv_mov_b32 v49, 0\n"
"v_mov_b32 v50, 0\nv_mov_b32 v51, 0\nv_mov_b32 v74, 0\ns_mul_i32 s48, s45, 6\n"
"s_and_b32 s48, s48, -4\ns_getpc_b64 s[60:61]\nspc_x3:\ns_sub_u32 s48, sgo_x3-spc_x3, s48\n"
"s_add_u32 s60, s60, s48\ns_addc_u32 s61, s61, 0\nsf_x3:\ns_memrealtime s[54:55]\n"
"s_nop 2\ns_memrealtime s[56:57]\ns_nop 2\ns_memrealtime s[58:59]\n"
"s_waitcnt lgkmcnt(4)\ns_sub_u32 s47, s54, s44\ns_add_u32 s48, s47, 12\ns_cmp_ge_u32 s48, 12\n"
"s_cbranch_scc1 so_x3\ns_memrealtime s[54:55]\ns_waitcnt lgkmcnt(4)\ns_cmp_ge_u32 s56, s44\n"
"s_cbranch_scc1 sx_x3\ns_memrealtime s[56:57]\ns_waitcnt lgkmcnt(4)\ns_cmp_ge_u32 s58, s44\n"
"s_cbranch_scc1 sx_x3\ns_memrealtime s[58:59]\ns_waitcnt lgkmcnt(4)\ns_cmp_ge_u32 s54, s44\n"
"s_cbranch_scc1 sx_x3\ns_memrealtime s[54:55]\ns_waitcnt lgkmcnt(4)\ns_cmp_ge_u32 s56, s44\n"
"s_cbranch_scc1 sx_x3\ns_memrealtime s[56:57]\ns_waitcnt lgkmcnt(4)\ns_cmp_ge_u32 s58, s44\n"
"s_cbranch_scc1 sx_x3\ns_memrealtime s[58:59]\ns_waitcnt lgkmcnt(4)\ns_cmp_ge_u32 s54, s44\n"
"s_cbranch_scc1 sx_x3\ns_memrealtime s[54:55]\ns_waitcnt lgkmcnt(4)\ns_cmp_ge_u32 s56, s44\n"
"s_cbranch_scc1 sx_x3\ns_memrealtime s[56:57]\ns_waitcnt lgkmcnt(4)\ns_cmp_ge_u32 s58, s44\n"
"s_cbranch_scc1 sx_x3\ns_memrealtime s[58:59]\ns_waitcnt lgkmcnt(4)\ns_cmp_ge_u32 s54, s44\n"
"s_cbranch_scc1 sx_x3\ns_memrealtime s[54:55]\ns_waitcnt lgkmcnt(4)\ns_cmp_ge_u32 s56, s44\n"
"s_cbranch_scc1 sx_x3\ns_memrealtime s[56:57]\ns_waitcnt lgkmcnt(4)\ns_cmp_ge_u32 s58, s44\n"
"s_cbranch_scc1 sx_x3\ns_memrealtime s[58:59]\ns_waitcnt lgkmcnt(4)\ns_cmp_ge_u32 s54, s44\n"
"s_cbranch_scc1 sx_x3\ns_memrealtime s[54:55]\ns_waitcnt lgkmcnt(4)\ns_cmp_ge_u32 s56, s44\n"
"s_cbranch_scc1 sx_x3\ns_memrealtime s[56:57]\ns_waitcnt lgkmcnt(4)\ns_cmp_ge_u32 s58, s44\n"
"s_cbranch_scc1 sx_x3\ns_memrealtime s[58:59]\ns_waitcnt lgkmcnt(4)\ns_cmp_ge_u32 s54, s44\n"
"s_cbranch_scc1 sx_x3\ns_memrealtime s[54:55]\ns_waitcnt lgkmcnt(4)\ns_cmp_ge_u32 s56, s44\n"
"s_cbranch_scc1 sx_x3\ns_memrealtime s[56:57]\ns_branch sx_x3\nso_x3:\n"
"s_waitcnt lgkmcnt(0)\ns_cmp_lt_i32 s47, 0\ns_cbranch_scc1 sf_x3\ns_branch sgo_x3\n"
"sx_x3:\ns_setpc_b64 s[60:61]\ns_nop 0\ns_nop 0\n"
"s_nop 0\ns_nop 0\ns_nop 0\ns_nop 0\n"
"s_nop 0\ns_nop 0\ns_nop 0\ns_nop 0\n"
"s_nop 0\ns_nop 0\ns_nop 0\ns_nop 0\n"
"s_nop 0\ns_nop 0\ns_nop 0\ns_nop 0\n"
"s_nop 0\ns_nop 0\ns_nop 0\ns_nop 0\n"
"s_nop 0\ns_nop 0\nsgo_x3:\ns_nop 15\n"
"s_nop 1\nbuffer_load_dwordx4 v[40:43], v10, s[8:11], s43 offen offset:0 sc1\nv_mov_b32 v75, 0\ns_nop 1\n"
"v_mov_b32 v75, 0\ns_nop 1\nv_mov_b32 v75, 0\ns_nop 1\n"
"v_mov_b32 v75, 0\ns_nop 1\nv_mov_b32 v75, 0\ns_nop 1\n"
"v_mov_b32 v75, 0\ns_nop 1\nv_mov_b32 v75, 0\ns_nop 1\n"
"v_mov_b32 v75, 0\ns_nop 1\nv_mov_b32 v75, 0\ns_nop 1\n"
"v_mov_b32 v75, 0\ns_nop 1\nv_mov_b32 v75, 0\ns_nop 1\n"
"v_mov_b32 v75, 0\ns_nop 1\nv_mov_b32 v75, 0\ns_nop 1\n"
"v_mov_b32 v75, 0\ns_nop 1\nv_mov_b32 v75, 0\ns_nop 1\n"
"v_mov_b32 v75, 0\ns_nop 1\nv_mov_b32 v75, 0\ns_nop 1\n"
"v_mov_b32 v75, 0\ns_nop 1\nv_mov_b32 v75, 0\ns_nop 1\n"
"v_mov_b32 v75, 0\ns_nop 1\nv_mov_b32 v75, 0\ns_nop 1\n"
"v_mov_b32 v75, 0\ns_nop 1\nv_mov_b32 v75, 0\ns_nop 1\n"
"v_mov_b32 v75, 0\ns_nop 1\nv_mov_b32 v75, 0\ns_nop 1\n"
"v_mov_b32 v75, 0\ns_nop 1\nv_mov_b32 v75, 0\ns_nop 1\n"
"v_mov_b32 v75, 0\ns_nop 1\nv_mov_b32 v75, 0\ns_nop 1\n"
"v_mov_b32 v75, 0\ns_nop 1\nv_mov_b32 v75, 0\ns_nop 1\n"
"v_mov_b32 v75, 0\ns_nop 0\nlanded_x3:\ns_waitcnt vmcnt(0)\n"
"v_smfmac_f32_16x16x64_bf16 v[44:47], v[40:43], a[0:7], v8\nv_max3_u32 v74, v74, v40, v41\nv_smfmac_f32_16x16x64_bf16 v[48:51], v[40:43], a[16:23], v8\nv_max3_u32 v74, v74, v42, v43\n"
"v_cmp_eq_u32 vcc, -1, v74\ns_nop 0\nv_add_f32 v52, v44, v45\nv_add_f32 v53, v46, v47\n"
"s_nop 1\nv_add_f32 v54, v48, v49\nv_add_f32 v55, v50, v51\nds_write2_b32 v77, v52, v53 offset0:0 offset1:40\n"
"ds_write2_b32 v77, v54, v55 offset0:16 offset1:56\ns_cbranch_vccnz retry_x3\nv_mov_b32 v75, 0\ns_nop 1\n"
"v_mov_b32 v75, 0\ns_nop 1\nv_mov_b32 v75, 0\ns_waitcnt lgkmcnt(0)\n"
"s_barrier\nds_read_b64 v[64:65], v78 offset:0\nds_read_b64 v[66:67], v78 offset:1280\nds_read_b64 v[68:69], v78 offset:2560\n"
"ds_read_b64 v[70:71], v78 offset:3840\nv_mov_b32 v75, 0\ns_nop 1\nv_mov_b32 v75, 0\n"
"s_nop 1\nv_mov_b32 v75, 0\ns_nop 1\ns_waitcnt lgkmcnt(2)\n"
"v_pk_add_f32 v[64:65], v[64:65], v[66:67]\ns_waitcnt lgkmcnt(0)\nv_pk_add_f32 v[68:69], v[68:69], v[70:71]\nv_pk_add_f32 v[64:65], v[64:65], v[68:69]\n"
"v_exp_f32 v75, v64\nv_exp_f32 v76, v65\ns_nop 0\nv_cvt_pk_bf16_f32 v75, v75, v76\n"
"buffer_store_dword v75, v16, s[8:11], s41 offen sc1\ns_barrier\nbuffer_load_dword v81, v18, s[8:11], 0 offen sc1\nbuffer_load_dwordx4 a[0:3], v9, s[16:19], s50 offen offset:0 sc1\n"
"s_nop 15\ns_nop 7\nbuffer_load_dwordx4 a[4:7], v9, s[16:19], s50 offen offset:1024 sc1\ns_nop 15\n"
"s_nop 7\nbuffer_load_dwordx4 a[16:19], v9, s[16:19], s51 offen offset:0 sc1\ns_nop 15\ns_nop 7\n"
"buffer_load_dwordx4 a[20:23], v9, s[16:19], s51 offen offset:1024 sc1\ns_nop 15\ns_nop 7\ns_add_u32 s50, s50, 28672\n"
"s_add_u32 s51, s51, 28672\nbuffer_store_dwordx2 v[64:65], v17, s[8:11], s41 offen sc1\nbuffer_store_dwordx4 v[60:63], v15, s[8:11], s42 offen sc1\nds_read_b32 v79, v80\n"
"v_xor_b32 v77, 8192, v77\nv_xor_b32 v78, 8192, v78\ns_waitcnt lgkmcnt(0)\nv_readfirstlane_b32 s47, v79\n"
"s_sub_u32 s47, s47, s40\ns_cmp_eq_u32 s47, 1\ns_cbranch_scc1 adopt_x3\nback_x3:\n"
"s_waitcnt vmcnt(6)\nfl_x3:\ns_add_u32 s47, s40, 4\ns_min_u32 s47, s47, 64\n"
"s_add_u32 s47, s47, s33\nv_subrev_u32 v81, s47, v81\nv_cmp_lt_u32 vcc, 64, v81\ns_cbranch_vccz flok_x3\n"
"s_add_u32 s53, s53, 0\ns_add_u32 s49, s49, 1\ns_cmp_gt_u32 s49, 65536\ns_cbranch_scc1 end_x3\n"
"s_sleep 2\nbuffer_load_dword v81, v18, s[8:11], 0 offen sc1\ns_waitcnt vmcnt(0)\ns_branch fl_x3\n"
"flok_x3:\ns_add_u32 s40, s40, 1\ns_mov_b32 s43, s41\ns_add_u32 s41, s41, 131072\n"
"s_and_b32 s41, s41, 524287\ns_xor_b32 s42, s41, 262144\ns_and_b32 s47, s46, 15\ns_mul_i32 s48, s47, 8\n"
"s_add_u32 s45, s45, s48\ns_add_u32 s45, s45, s30\ns_lshr_b32 s48, s45, 4\ns_lshl_b32 s48, s48, 2\n"
"s_add_u32 s44, s44, s48\ns_and_b32 s45, s45, 15\ns_add_u32 s92, s92, 1\ns_waitcnt lgkmcnt(0)\n"
"s_cmp_lt_u32 s40, 63\ns_cbranch_scc1 top_x3\ns_branch end_x3\nretry2_x3:\n"
"s_sleep 3\nretry_x3:\ns_add_u32 s53, s53, 1\ns_add_u32 s49, s49, 1\n"
"s_cmp_gt_u32 s49, 65536\ns_cbranch_scc1 end_x3\nbuffer_load_dwordx4 v[40:43], v10, s[8:11], s43 offen offset:0 sc1\nv_mov_b32 v74, 0\n"
"s_add_u32 s47, s40, 1\nv_mov_b32 v79, s47\nds_write_b32 v80, v79\ns_waitcnt vmcnt(0)\n"
"v_max3_u32 v74, v74, v40, v41\nv_max3_u32 v74, v74, v42, v43\nv_cmp_eq_u32 vcc, -1, v74\nv_mov_b32 v44, 0\n"
"v_mov_b32 v45, 0\nv_mov_b32 v46, 0\nv_mov_b32 v47, 0\nv_mov_b32 v48, 0\n"
"v_mov_b32 v49, 0\nv_mov_b32 v50, 0\nv_mov_b32 v51, 0\ns_cbranch_vccnz retry2_x3\n"
"s_branch landed_x3\nadopt_x3:\nbuffer_load_dwordx4 v[56:59], v28, s[8:11], 0 offen sc1\ns_lshl_b32 s52, s40, 26\n"
"s_or_b32 s47, s52, 0x3ffffff\ns_waitcnt vmcnt(0)\nv_cmp_ge_u32 vcc, s47, v56\ns_nop 1\n"
"v_cndmask_b32 v56, 0, v56, vcc\nv_cmp_ge_u32 vcc, s47, v57\ns_nop 1\nv_cndmask_b32 v57, 0, v57, vcc\n"
"v_cmp_ge_u32 vcc, s47, v58\ns_nop 1\nv_cndmask_b32 v58, 0, v58, vcc\nv_cmp_ge_u32 vcc, s47, v59\n"
"s_nop 1\nv_cndmask_b32 v59, 0, v59, vcc\nv_max3_u32 v79, v56, v57, v58\nv_max_u32 v79, v79, v59\n"
"s_nop 1\nv_max_u32_dpp v79, v79, v79 row_ror:8 row_mask:0xf bank_mask:0xf\ns_nop 1\nv_max_u32_dpp v79, v79, v79 row_ror:4 row_mask:0xf bank_mask:0xf\n"
"s_nop 1\nv_max_u32_dpp v79, v79, v79 row_ror:2 row_mask:0xf bank_mask:0xf\ns_nop 1\nv_max_u32_dpp v79, v79, v79 row_ror:1 row_mask:0xf bank_mask:0xf\n"
"s_nop 0\nv_readlane_b32 s47, v79, 0\nv_readlane_b32 s48, v79, 16\ns_max_u32 s48, s48, s47\n"
"s_cmp_le_u32 s48, s52\ns_cbranch_scc1 back_x3\ns_bfe_u32 s52, s48, 1179656\ns_add_u32 s44, s52, s28\n"
"s_mov_b32 s45, 0\ns_and_b32 s46, s48, 255\ns_mov_b32 s92, 0\ns_branch back_x3\n"
"end_x3:\ns_branch fk_done\nfk_p0:\ns_mov_b32 s40, 0\n"
"s_mov_b32 s41, 0\ns_mov_b32 s42, 262144\ns_mov_b32 s43, 393216\ns_mov_b32 s44, s28\n"
"s_mov_b32 s45, 0\ns_mov_b32 s46, 0\ns_mov_b32 s92, 0\ns_mov_b32 s49, 0\n"
"s_mov_b32 s53, 0\ns_mov_b32 s50, s35\ns_mov_b32 s51, s36\nv_mov_b32 v222, 24576\n"
"s_mov_b32 s62, 0x00ff00ff\ns_mov_b32 s63, 0x00ff00ff\nv_cndmask_b32 v223, 0, -1, s[62:63]\nv_not_b32 v224, v223\n"
"v_bfe_u32 v219, v9, 4, 1\nv_lshlrev_b32 v220, 4, v219\nv_mov_b32 v218, 0x3c00\nv_lshlrev_b32 v218, v220, v218\n"
"v_bfe_u32 v220, v9, 5, 3\nv_cmp_eq_u32 vcc, 0, v220\ns_nop 1\nv_cndmask_b32 v168, 0, v218, vcc\n"
"v_cmp_eq_u32 vcc, 2, v220\ns_nop 1\nv_cndmask_b32 v169, 0, v218, vcc\nv_cmp_eq_u32 vcc, 4, v220\n"
"s_nop 1\nv_cndmask_b32 v170, 0, v218, vcc\nv_cmp_eq_u32 vcc, 6, v220\ns_nop 1\n"
"v_cndmask_b32 v171, 0, v218, vcc\nv_cmp_eq_u32 vcc, 1, v220\ns_nop 1\nv_cndmask_b32 v172, 0, v218, vcc\n"
"v_cmp_eq_u32 vcc, 3, v220\ns_nop 1\nv_cndmask_b32 v173, 0, v218, vcc\nv_cmp_eq_u32 vcc, 5, v220\n"
"s_nop 1\nv_cndmask_b32 v174, 0, v218, vcc\nv_cmp_eq_u32 vcc, 7, v220\ns_nop 1\n"
"v_cndmask_b32 v175, 0, v218, vcc\nv_mov_b32 v218, 0x3f803f80\nv_cmp_eq_u32 vcc, 0, v219\ns_nop 1\n"
"v_cndmask_b32 v176, 0, v218, vcc\nv_cndmask_b32 v177, 0, v218, vcc\nv_cndmask_b32 v178, 0, v218, vcc\nv_cndmask_b32 v179, 0, v218, vcc\n"
"v_cndmask_b32 v214, 0, v218, vcc\nv_cmp_eq_u32 vcc, 1, v219\ns_nop 1\nv_cndmask_b32 v180, 0, v218, vcc\n"
"v_cndmask_b32 v181, 0, v218, vcc\nv_cndmask_b32 v182, 0, v218, vcc\nv_cndmask_b32 v183, 0, v218, vcc\nv_cndmask_b32 v215, 0, v218, vcc\n"
"s_add_u32 s47, s34, 0\nbuffer_load_dwordx4 a[0:3], v9, s[12:15], s47 offen sc1\nbuffer_load_dwordx4 a[4:7], v9, s[12:15], s47 offen offset:1024 sc1\ns_add_u32 s47, s34, 2048\n"
"buffer_load_dwordx4 a[8:11], v9, s[12:15], s47 offen sc1\nbuffer_load_dwordx4 a[12:15], v9, s[12:15], s47 offen offset:1024 sc1\ns_add_u32 s47, s34, 4096\nbuffer_load_dwordx4 a[16:19], v9, s[12:15], s47 offen sc1\n"
"buffer_load_dwordx4 a[20:23], v9, s[12:15], s47 offen offset:1024 sc1\ns_add_u32 s47, s34, 6144\nbuffer_load_dwordx4 a[24:27], v9, s[12:15], s47 offen sc1\nbuffer_load_dwordx4 a[28:31], v9, s[12:15], s47 offen offset:1024 sc1\n"
"s_add_u32 s47, s34, 8192\nbuffer_load_dwordx4 a[32:35], v9, s[12:15], s47 offen sc1\nbuffer_load_dwordx4 a[36:39], v9, s[12:15], s47 offen offset:1024 sc1\ns_add_u32 s47, s34, 10240\n"
"buffer_load_dwordx4 a[40:43], v9, s[12:15], s47 offen sc1\nbuffer_load_dwordx4 a[44:47], v9, s[12:15], s47 offen offset:1024 sc1\ns_add_u32 s47, s34, 12288\nbuffer_load_dwordx4 a[48:51], v9, s[12:15], s47 offen sc1\n"
"buffer_load_dwordx4 a[52:55], v9, s[12:15], s47 offen offset:1024 sc1\ns_add_u32 s47, s34, 14336\nbuffer_load_dwordx4 a[56:59], v9, s[12:15], s47 offen sc1\nbuffer_load_dwordx4 a[60:63], v9, s[12:15], s47 offen offset:1024 sc1\n"
"s_add_u32 s47, s34, 16384\nbuffer_load_dwordx4 a[64:67], v9, s[12:15], s47 offen sc1\nbuffer_load_dwordx4 a[68:71], v9, s[12:15], s47 offen offset:1024 sc1\ns_add_u32 s47, s34, 18432\n"
"buffer_load_dwordx4 a[72:75], v9, s[12:15], s47 offen sc1\nbuffer_load_dwordx4 a[76:79], v9, s[12:15], s47 offen offset:1024 sc1\ns_add_u32 s47, s34, 20480\nbuffer_load_dwordx4 a[80:83], v9, s[12:15], s47 offen sc1\n"
"buffer_load_dwordx4 a[84:87], v9, s[12:15], s47 offen offset:1024 sc1\ns_add_u32 s47, s34, 22528\nbuffer_load_dwordx4 a[88:91], v9, s[12:15], s47 offen sc1\nbuffer_load_dwordx4 a[92:95], v9, s[12:15], s47 offen offset:1024 sc1\n"
"s_add_u32 s47, s34, 24576\nbuffer_load_dwordx4 a[96:99], v9, s[12:15], s47 offen sc1\nbuffer_load_dwordx4 a[100:103], v9, s[12:15], s47 offen offset:1024 sc1\ns_add_u32 s47, s34, 26624\n"
"buffer_load_dwordx4 a[104:107], v9, s[12:15], s47 offen sc1\nbuffer_load_dwordx4 a[108:111], v9, s[12:15], s47 offen offset:1024 sc1\ns_add_u32 s47, s34, 28672\nbuffer_load_dwordx4 a[112:115], v9, s[12:15], s47 offen sc1\n"
"buffer_load_dwordx4 a[116:119], v9, s[12:15], s47 offen offset:1024 sc1\ns_add_u32 s47, s34, 30720\nbuffer_load_dwordx4 a[120:123], v9, s[12:15], s47 offen sc1\nbuffer_load_dwordx4 a[124:127], v9, s[12:15], s47 offen offset:1024 sc1\n"
"s_add_u32 s47, s34, 32768\nbuffer_load_dwordx4 a[128:131], v9, s[12:15], s47 offen sc1\nbuffer_load_dwordx4 a[132:135], v9, s[12:15], s47 offen offset:1024 sc1\ns_add_u32 s47, s34, 34816\n"
"buffer_load_dwordx4 a[136:139], v9, s[12:15], s47 offen sc1\nbuffer_load_dwordx4 a[140:143], v9, s[12:15], s47 offen offset:1024 sc1\ns_add_u32 s47, s34, 36864\nbuffer_load_dwordx4 a[144:147], v9, s[12:15], s47 offen sc1\n"
"buffer_load_dwordx4 a[148:151], v9, s[12:15], s47 offen offset:1024 sc1\ns_add_u32 s47, s34, 38912\nbuffer_load_dwordx4 a[152:155], v9, s[12:15], s47 offen sc1\nbuffer_load_dwordx4 a[156:159], v9, s[12:15], s47 offen offset:1024 sc1\n"
"s_add_u32 s47, s34, 40960\nbuffer_load_dwordx4 a[160:163], v9, s[12:15], s47 offen sc1\nbuffer_load_dwordx4 a[164:167], v9, s[12:15], s47 offen offset:1024 sc1\ns_add_u32 s47, s34, 43008\n"
"buffer_load_dwordx4 a[168:171], v9, s[12:15], s47 offen sc1\nbuffer_load_dwordx4 a[172:175], v9, s[12:15], s47 offen offset:1024 sc1\ns_add_u32 s47, s34, 45056\nbuffer_load_dwordx4 a[176:179], v9, s[12:15], s47 offen sc1\n"
"buffer_load_dwordx4 a[180:183], v9, s[12:15], s47 offen offset:1024 sc1\ns_add_u32 s47, s34, 47104\nbuffer_load_dwordx4 a[184:187], v9, s[12:15], s47 offen sc1\nbuffer_load_dwordx4 a[188:191], v9, s[12:15], s47 offen offset:1024 sc1\n"
"s_add_u32 s47, s34, 49152\nbuffer_load_dwordx4 a[192:195], v9, s[12:15], s47 offen sc1\nbuffer_load_dwordx4 a[196:199], v9, s[12:15], s47 offen offset:1024 sc1\ns_add_u32 s47, s34, 51200\n"
"buffer_load_dwordx4 a[200:203], v9, s[12:15], s47 offen sc1\nbuffer_load_dwordx4 a[204:207], v9, s[12:15], s47 offen offset:1024 sc1\ns_add_u32 s47, s34, 53248\nbuffer_load_dwordx4 a[208:211], v9, s[12:15], s47 offen sc1\n"
"buffer_load_dwordx4 a[212:215], v9, s[12:15], s47 offen offset:1024 sc1\ns_add_u32 s47, s34, 55296\nbuffer_load_dwordx4 a[216:219], v9, s[12:15], s47 offen sc1\nbuffer_load_dwordx4 a[220:223], v9, s[12:15], s47 offen offset:1024 sc1\n"
"v_lshlrev_b32 v166, 2, v9\ns_mov_b32 s52, 1064960\nbuffer_load_dword v164, v166, s[8:11], s52 offen sc1\ns_add_u32 s52, s52, 4096\n"
"buffer_load_dword v165, v166, s[8:11], s52 offen sc1\nbuffer_load_dword v218, v11, s[8:11], 0 offen sc1\ns_mov_b64 s[94:95], 1\ns_barrier\n"
"s_waitcnt vmcnt(0)\nar_p0:\nv_xor_b32 v164, s33, v164\nv_xor_b32 v165, s33, v165\n"
"v_or_b32 v164, v164, v165\nv_cmp_ne_u32 vcc, 0, v164\ns_cbranch_vccz arok_p0\ns_add_u32 s53, s53, 0\n"
"s_add_u32 s49, s49, 1\ns_cmp_gt_u32 s49, 65536\ns_cbranch_scc1 end_p0\ns_sleep 2\n"
"v_lshlrev_b32 v166, 2, v9\ns_mov_b32 s52, 1064960\nbuffer_load_dword v164, v166, s[8:11], s52 offen sc1\ns_add_u32 s52, s52, 4096\n"
"buffer_load_dword v165, v166, s[8:11], s52 offen sc1\ns_waitcnt vmcnt(0)\ns_branch ar_p0\narok_p0:\n"
"fl_p0:\ns_add_u32 s47, s40, 2\ns_min_u32 s47, s47, 64\ns_add_u32 s47, s47, s33\n"
"v_subrev_u32 v218, s47, v218\nv_cmp_lt_u32 vcc, 64, v218\ns_cbranch_vccz flok_p0\ns_add_u32 s53, s53, 0\n"
"s_add_u32 s49, s49, 1\ns_cmp_gt_u32 s49, 65536\ns_cbranch_scc1 end_p0\ns_sleep 2\n"
"buffer_load_dword v218, v11, s[8:11], 0 offen sc1\ns_waitcnt vmcnt(0)\ns_branch fl_p0\nflok_p0:\n"
"buffer_load_dwordx4 v[100:103], v9, s[16:19], s50 offen offset:0 sc1\nbuffer_load_dwordx4 v[104:107], v9, s[16:19], s50 offen offset:1024 sc1\nbuffer_load_dwordx4 v[108:111], v9, s[16:19], s50 offen offset:2048 sc1\nbuffer_load_dwordx4 v[112:115], v9, s[16:19], s50 offen offset:3072 sc1\n"
"s_add_u32 s50, s50, 16384\ns_memrealtime s[54:55]\ns_waitcnt lgkmcnt(0)\ns_add_u32 s44, s54, 12\n"
"s_mov_b32 s45, 8\n.p2align 6\ntop_p0:\nv_mov_b32 v68, 0\n"
"v_mov_b32 v69, 0\nv_mov_b32 v70, 0\nv_mov_b32 v71, 0\nv_mov_b32 v72, 0\n"
"v_mov_b32 v73, 0\nv_mov_b32 v74, 0\nv_mov_b32 v75, 0\nv_mov_b32 v76, 0\n"
"v_mov_b32 v77, 0\nv_mov_b32 v78, 0\nv_mov_b32 v79, 0\nv_mov_b32 v80, 0\n"
"v_mov_b32 v81, 0\nv_mov_b32 v82, 0\nv_mov_b32 v83, 0\nv_mov_b32 v218, 0\n"
"s_add_u32 s52, s44, s37\nm_p0:\ns_memrealtime s[54:55]\ns_waitcnt lgkmcnt(0)\n"
"s_sub_u32 s47, s54, s52\ns_cmp_lt_i32 s47, 0\ns_cbranch_scc1 m_p0\ntop2_p0:\n"
"buffer_load_dwordx4 v[40:43], v10, s[8:11], s43 offen offset:0 sc1\nbuffer_load_dwordx4 v[44:47], v10, s[8:11], s43 offen offset:1024 sc1\ns_and_b32 s47, s46, 15\ns_mul_i32 s47, s47, 4\n"
"s_add_u32 s47, s47, s29\ns_add_u32 s47, s47, s45\ns_and_b32 s91, s47, 15\ns_lshr_b32 s47, s47, 4\n"
"s_lshl_b32 s47, s47, 2\ns_add_u32 s90, s44, s47\ns_mul_i32 s48, s91, 6\ns_and_b32 s48, s48, -4\n"
"s_getpc_b64 s[60:61]\nspc_p0:\ns_sub_u32 s48, sgo_p0-spc_p0, s48\ns_add_u32 s60, s60, s48\n"
"s_addc_u32 s61, s61, 0\ns_waitcnt vmcnt(1)\nds_write_b128 v9, v[40:43] offset:0\nv_max3_u32 v218, v218, v40, v41\n"
"v_max3_u32 v218, v218, v42, v43\ns_waitcnt vmcnt(0)\nds_write_b128 v9, v[44:47] offset:1024\nv_max3_u32 v218, v218, v44, v45\n"
"v_max3_u32 v218, v218, v46, v47\nv_cmp_eq_u32 vcc, -1, v218\nv_smfmac_f32_16x16x64_bf16 v[68:71], v[40:43], a[0:7], v8\nv_smfmac_f32_16x16x64_bf16 v[72:75], v[40:43], a[56:63], v8\n"
"s_cbranch_vccnz retry1_p0\ns_waitcnt lgkmcnt(0)\ns_barrier\nds_read_b32 v221, v222\n"
"ds_read_b128 v[48:51], v9 offset:2048\nds_read_b128 v[52:55], v9 offset:3072\nds_read_b128 v[56:59], v9 offset:4096\nds_read_b128 v[60:63], v9 offset:5120\n"
"ds_read_b128 v[64:67], v9 offset:6144\nv_smfmac_f32_16x16x64_bf16 v[76:79], v[40:43], a[112:119], v8\nv_smfmac_f32_16x16x64_bf16 v[80:83], v[40:43], a[168:175], v8\nv_smfmac_f32_16x16x64_bf16 v[68:71], v[44:47], a[8:15], v8\n"
"v_smfmac_f32_16x16x64_bf16 v[72:75], v[44:47], a[64:71], v8\nv_smfmac_f32_16x16x64_bf16 v[76:79], v[44:47], a[120:127], v8\nv_smfmac_f32_16x16x64_bf16 v[80:83], v[44:47], a[176:183], v8\ns_waitcnt lgkmcnt(0)\n"
"v_readfirstlane_b32 s47, v221\ns_sub_u32 s47, s47, s40\ns_cmp_eq_u32 s47, 1\ns_cbranch_scc1 adopt_p0\n"
"back_p0:\ns_cmp_eq_u32 s40, 63\ns_cbranch_scc1 last_p0\nsf_p0:\n"
"s_memrealtime s[54:55]\ns_nop 2\ns_memrealtime s[56:57]\ns_nop 2\n"
"s_memrealtime s[58:59]\ns_waitcnt lgkmcnt(4)\ns_sub_u32 s47, s54, s90\ns_add_u32 s48, s47, 12\n"
"s_cmp_ge_u32 s48, 12\ns_cbranch_scc1 so_p0\ns_memrealtime s[54:55]\ns_waitcnt lgkmcnt(4)\n"
"s_cmp_ge_u32 s56, s90\ns_cbranch_scc1 sx_p0\ns_memrealtime s[56:57]\ns_waitcnt lgkmcnt(4)\n"
"s_cmp_ge_u32 s58, s90\ns_cbranch_scc1 sx_p0\ns_memrealtime s[58:59]\ns_waitcnt lgkmcnt(4)\n"
"s_cmp_ge_u32 s54, s90\ns_cbranch_scc1 sx_p0\ns_memrealtime s[54:55]\ns_waitcnt lgkmcnt(4)\n"
"s_cmp_ge_u32 s56, s90\ns_cbranch_scc1 sx_p0\ns_memrealtime s[56:57]\ns_waitcnt lgkmcnt(4)\n"
"s_cmp_ge_u32 s58, s90\ns_cbranch_scc1 sx_p0\ns_memrealtime s[58:59]\ns_waitcnt lgkmcnt(4)\n"
"s_cmp_ge_u32 s54, s90\ns_cbranch_scc1 sx_p0\ns_memrealtime s[54:55]\ns_waitcnt lgkmcnt(4)\n"
"s_cmp_ge_u32 s56, s90\ns_cbranch_scc1 sx_p0\ns_memrealtime s[56:57]\ns_waitcnt lgkmcnt(4)\n"
"s_cmp_ge_u32 s58, s90\ns_cbranch_scc1 sx_p0\ns_memrealtime s[58:59]\ns_waitcnt lgkmcnt(4)\n"
"s_cmp_ge_u32 s54, s90\ns_cbranch_scc1 sx_p0\ns_memrealtime s[54:55]\ns_waitcnt lgkmcnt(4)\n"
"s_cmp_ge_u32 s56, s90\ns_cbranch_scc1 sx_p0\ns_memrealtime s[56:57]\ns_waitcnt lgkmcnt(4)\n"
"s_cmp_ge_u32 s58, s90\ns_cbranch_scc1 sx_p0\ns_memrealtime s[58:59]\ns_waitcnt lgkmcnt(4)\n"
"s_cmp_ge_u32 s54, s90\ns_cbranch_scc1 sx_p0\ns_memrealtime s[54:55]\ns_waitcnt lgkmcnt(4)\n"
"s_cmp_ge_u32 s56, s90\ns_cbranch_scc1 sx_p0\ns_memrealtime s[56:57]\ns_waitcnt lgkmcnt(4)\n"
"s_cmp_ge_u32 s58, s90\ns_cbranch_scc1 sx_p0\ns_memrealtime s[58:59]\ns_waitcnt lgkmcnt(4)\n"
"s_cmp_ge_u32 s54, s90\ns_cbranch_scc1 sx_p0\ns_memrealtime s[54:55]\ns_waitcnt lgkmcnt(4)\n"
"s_cmp_ge_u32 s56, s90\ns_cbranch_scc1 sx_p0\ns_memrealtime s[56:57]\ns_branch sx_p0\n"
"so_p0:\ns_waitcnt lgkmcnt(0)\ns_cmp_lt_i32 s47, 0\ns_cbranch_scc1 sf_p0\n"
"s_cmp_gt_i32 s47, s32\ns_cbranch_scc1 late_p0\ns_branch sgo_p0\nsx_p0:\n"
"s_setpc_b64 s[60:61]\ns_nop 0\ns_nop 0\ns_nop 0\n"
"s_nop 0\ns_nop 0\ns_nop 0\ns_nop 0\n"
"s_nop 0\ns_nop 0\ns_nop 0\ns_nop 0\n"
"s_nop 0\ns_nop 0\ns_nop 0\ns_nop 0\n"
"s_nop 0\ns_nop 0\ns_nop 0\ns_nop 0\n"
"s_nop 0\ns_nop 0\ns_nop 0\ns_nop 0\n"
"s_nop 0\nsgo_p0:\nbuffer_load_dwordx4 v[116:119], v20, s[8:11], s41 offen offset:0 sc1\nbuffer_load_dwordx4 v[120:123], v20, s[8:11], s41 offen offset:1024 sc1\n"
"v_smfmac_f32_16x16x64_bf16 v[68:71], v[48:51], a[16:23], v8\nv_mov_b32 v84, v100\nv_smfmac_f32_16x16x64_bf16 v[72:75], v[48:51], a[72:79], v8\nv_mov_b32 v85, v101\n"
"v_smfmac_f32_16x16x64_bf16 v[76:79], v[48:51], a[128:135], v8\nv_mov_b32 v86, v102\nv_smfmac_f32_16x16x64_bf16 v[80:83], v[48:51], a[184:191], v8\nv_mov_b32 v87, v103\n"
"v_smfmac_f32_16x16x64_bf16 v[68:71], v[52:55], a[24:31], v8\nv_mov_b32 v88, v104\nv_smfmac_f32_16x16x64_bf16 v[72:75], v[52:55], a[80:87], v8\nv_mov_b32 v89, v105\n"
"v_smfmac_f32_16x16x64_bf16 v[76:79], v[52:55], a[136:143], v8\nv_mov_b32 v90, v106\nv_smfmac_f32_16x16x64_bf16 v[80:83], v[52:55], a[192:199], v8\nv_mov_b32 v91, v107\n"
"v_smfmac_f32_16x16x64_bf16 v[68:71], v[56:59], a[32:39], v8\nv_mov_b32 v92, v108\nv_smfmac_f32_16x16x64_bf16 v[72:75], v[56:59], a[88:95], v8\nv_mov_b32 v93, v109\n"
"buffer_load_dwordx4 v[124:127], v21, s[8:11], s41 offen offset:0 sc1\nbuffer_load_dwordx4 v[128:131], v21, s[8:11], s41 offen offset:1024 sc1\nv_smfmac_f32_16x16x64_bf16 v[76:79], v[56:59], a[144:151], v8\nv_mov_b32 v94, v110\n"
"v_smfmac_f32_16x16x64_bf16 v[80:83], v[56:59], a[200:207], v8\nv_mov_b32 v95, v111\nv_smfmac_f32_16x16x64_bf16 v[68:71], v[60:63], a[40:47], v8\nv_mov_b32 v96, v112\n"
"v_smfmac_f32_16x16x64_bf16 v[72:75], v[60:63], a[96:103], v8\nv_mov_b32 v97, v113\nv_smfmac_f32_16x16x64_bf16 v[76:79], v[60:63], a[152:159], v8\nv_mov_b32 v98, v114\n"
"v_smfmac_f32_16x16x64_bf16 v[80:83], v[60:63], a[208:215], v8\nv_mov_b32 v99, v115\nv_smfmac_f32_16x16x64_bf16 v[68:71], v[64:67], a[48:55], v8\nv_smfmac_f32_16x16x64_bf16 v[72:75], v[64:67], a[104:111], v8\n"
"v_smfmac_f32_16x16x64_bf16 v[76:79], v[64:67], a[160:167], v8\nv_smfmac_f32_16x16x64_bf16 v[80:83], v[64:67], a[216:223], v8\nv_add_f32 v219, v68, v69\nv_add_f32 v220, v70, v71\n"
"v_cvt_pk_bf16_f32 v156, v219, v220\nv_add_f32 v219, v72, v73\nv_add_f32 v220, v74, v75\nv_cvt_pk_bf16_f32 v157, v219, v220\n"
"v_add_f32 v219, v76, v77\nv_add_f32 v220, v78, v79\nv_cvt_pk_bf16_f32 v158, v219, v220\nv_add_f32 v219, v80, v81\n"
"v_add_f32 v220, v82, v83\nv_cvt_pk_bf16_f32 v159, v219, v220\nbuffer_load_dwordx4 v[100:103], v9, s[16:19], s50 offen offset:0 sc1\nbuffer_load_dwordx4 v[104:107], v9, s[16:19], s50 offen offset:1024 sc1\n"
"buffer_load_dwordx4 v[108:111], v9, s[16:19], s50 offen offset:2048 sc1\nbuffer_load_dwordx4 v[112:115], v9, s[16:19], s50 offen offset:3072 sc1\ns_add_u32 s50, s50, 16384\nbuffer_store_dwordx4 v[156:159], v9, s[20:23], s51 offen sc1\n"
"s_waitcnt vmcnt(8)\nv_mfma_f32_16x16x32_bf16 v[132:135], v[84:87], v[116:119], 0\nv_mfma_f32_16x16x32_bf16 v[136:139], v[88:91], v[116:119], 0\nv_mfma_f32_16x16x32_bf16 v[148:151], v[176:179], v[116:119], 0\n"
"s_waitcnt vmcnt(7)\nv_mfma_f32_16x16x32_bf16 v[140:143], v[92:95], v[120:123], 0\nv_mfma_f32_16x16x32_bf16 v[144:147], v[96:99], v[120:123], 0\nv_mfma_f32_16x16x32_bf16 v[148:151], v[180:183], v[120:123], v[148:151]\n"
"v_cndmask_b32 v132, v136, v132, s[62:63]\nv_cndmask_b32 v133, v137, v133, s[62:63]\nv_cndmask_b32 v134, v138, v134, s[62:63]\nv_cndmask_b32 v135, v139, v135, s[62:63]\n"
"s_waitcnt vmcnt(6)\nv_mfma_f32_16x16x32_f16 v[152:155], v[168:171], v[124:127], 0\ns_waitcnt vmcnt(5)\nv_mfma_f32_16x16x32_f16 v[152:155], v[172:175], v[128:131], v[152:155]\n"
"v_cndmask_b32 v140, v144, v140, s[62:63]\nv_cndmask_b32 v141, v145, v141, s[62:63]\nv_cndmask_b32 v142, v146, v142, s[62:63]\nv_cndmask_b32 v143, v147, v143, s[62:63]\n"
"s_nop 2\nv_exp_f32 v152, v152\nv_exp_f32 v153, v153\nv_exp_f32 v154, v154\n"
"v_exp_f32 v155, v155\nv_cvt_pk_bf16_f32 v200, v152, v153\nv_cvt_pk_bf16_f32 v201, v154, v155\nv_and_b32 v160, v200, v223\n"
"v_and_b32 v161, v200, v224\nv_mfma_f32_16x16x16_bf16 v[148:151], v[214:215], v[200:201], v[148:151]\nv_and_b32 v162, v201, v223\nv_and_b32 v163, v201, v224\n"
"v_mfma_f32_16x16x16_bf16 v[132:135], v[156:157], v[160:161], v[132:135]\ns_nop 0\nv_mfma_f32_16x16x16_bf16 v[140:143], v[158:159], v[162:163], v[140:143]\nv_rcp_f32 v202, v148\n"
"v_rcp_f32 v203, v149\nv_mul_f32 v132, v132, v202\nv_mul_f32 v133, v133, v202\nv_mul_f32 v134, v134, v202\n"
"v_mul_f32 v135, v135, v202\nv_fmac_f32 v132, v140, v203\nv_fmac_f32 v133, v141, v203\nv_fmac_f32 v134, v142, v203\n"
"v_fmac_f32 v135, v143, v203\nv_add_f32_dpp v204, v132, v132 row_ror:8 row_mask:0xf bank_mask:0x3\nv_add_f32_dpp v205, v133, v133 row_ror:8 row_mask:0xf bank_mask:0x3\nv_add_f32_dpp v204, v134, v134 row_ror:8 row_mask:0xf bank_mask:0xc\n"
"v_add_f32_dpp v205, v135, v135 row_ror:8 row_mask:0xf bank_mask:0xc\nv_subrev_u32 v219, 0x2f800000, v148\nv_subrev_u32 v220, 0x2f800000, v149\nv_max_u32 v219, v219, v220\n"
"v_cmp_le_u32 vcc, 0x20000000, v219\nds_write_b64 v22, v[204:205]\ns_or_b64 s[56:57], vcc, s[94:95]\ns_cbranch_scc1 slow_p0\n"
"join_p0:\nv_mov_b32 v219, 0\ns_nop 1\nv_mov_b32 v219, 0\n"
"s_waitcnt lgkmcnt(0)\ns_barrier\nds_read_b64 v[206:207], v23 offset:0\nds_read_b64 v[208:209], v23 offset:1280\n"
"ds_read_b64 v[210:211], v23 offset:2560\nds_read_b64 v[212:213], v23 offset:3840\nds_read_b32 v221, v222\nv_mov_b32 v219, 0\n"
"s_nop 1\nv_mov_b32 v219, 0\ns_nop 1\nv_mov_b32 v219, 0\n"
"s_waitcnt lgkmcnt(3)\nv_pk_add_f32 v[206:207], v[206:207], v[208:209]\ns_waitcnt lgkmcnt(1)\nv_pk_add_f32 v[210:211], v[210:211], v[212:213]\n"
"v_pk_add_f32 v[206:207], v[206:207], v[210:211]\nv_cvt_pk_bf16_f32 v220, v206, v207\nbuffer_store_dword v220, v16, s[8:11], s41 offen sc1\ns_add_u32 s51, s51, 4096\n"
"s_add_u32 s40, s40, 1\ns_mov_b32 s43, s41\ns_add_u32 s41, s41, 131072\ns_and_b32 s41, s41, 524287\n"
"s_xor_b32 s42, s41, 262144\ns_and_b32 s47, s46, 15\ns_mul_i32 s48, s47, 8\ns_add_u32 s45, s45, s48\n"
"s_add_u32 s45, s45, s30\ns_lshr_b32 s48, s45, 4\ns_lshl_b32 s48, s48, 2\ns_add_u32 s44, s44, s48\n"
"s_and_b32 s45, s45, 15\ns_add_u32 s92, s92, 1\ns_waitcnt lgkmcnt(0)\ns_cmp_eq_u32 s40, 1\n"
"s_cbranch_scc1 one_p0\nv_readfirstlane_b32 s47, v221\ns_sub_u32 s47, s47, s40\ns_cmp_eq_u32 s47, 1\n"
"s_cbranch_scc1 rec_p0\ns_branch top_p0\none_p0:\nbuffer_load_dword v218, v11, s[8:11], 0 offen sc1\n"
"s_waitcnt vmcnt(0)\nf1_p0:\ns_add_u32 s47, s40, 2\ns_min_u32 s47, s47, 64\n"
"s_add_u32 s47, s47, s33\nv_subrev_u32 v218, s47, v218\nv_cmp_lt_u32 vcc, 64, v218\ns_cbranch_vccz f1ok_p0\n"
"s_add_u32 s53, s53, 0\ns_add_u32 s49, s49, 1\ns_cmp_gt_u32 s49, 65536\ns_cbranch_scc1 end_p0\n"
"s_sleep 2\nbuffer_load_dword v218, v11, s[8:11], 0 offen sc1\ns_waitcnt vmcnt(0)\ns_branch f1_p0\n"
"f1ok_p0:\ns_branch top_p0\nlast_p0:\nv_smfmac_f32_16x16x64_bf16 v[68:71], v[48:51], a[16:23], v8\n"
"v_smfmac_f32_16x16x64_bf16 v[72:75], v[48:51], a[72:79], v8\nv_smfmac_f32_16x16x64_bf16 v[76:79], v[48:51], a[128:135], v8\nv_smfmac_f32_16x16x64_bf16 v[80:83], v[48:51], a[184:191], v8\nv_smfmac_f32_16x16x64_bf16 v[68:71], v[52:55], a[24:31], v8\n"
"v_smfmac_f32_16x16x64_bf16 v[72:75], v[52:55], a[80:87], v8\nv_smfmac_f32_16x16x64_bf16 v[76:79], v[52:55], a[136:143], v8\nv_smfmac_f32_16x16x64_bf16 v[80:83], v[52:55], a[192:199], v8\nv_smfmac_f32_16x16x64_bf16 v[68:71], v[56:59], a[32:39], v8\n"
"v_smfmac_f32_16x16x64_bf16 v[72:75], v[56:59], a[88:95], v8\nv_smfmac_f32_16x16x64_bf16 v[76:79], v[56:59], a[144:151], v8\nv_smfmac_f32_16x16x64_bf16 v[80:83], v[56:59], a[200:207], v8\nv_smfmac_f32_16x16x64_bf16 v[68:71], v[60:63], a[40:47], v8\n"
"v_smfmac_f32_16x16x64_bf16 v[72:75], v[60:63], a[96:103], v8\nv_smfmac_f32_16x16x64_bf16 v[76:79], v[60:63], a[152:159], v8\nv_smfmac_f32_16x16x64_bf16 v[80:83], v[60:63], a[208:215], v8\nv_smfmac_f32_16x16x64_bf16 v[68:71], v[64:67], a[48:55], v8\n"
"v_smfmac_f32_16x16x64_bf16 v[72:75], v[64:67], a[104:111], v8\nv_smfmac_f32_16x16x64_bf16 v[76:79], v[64:67], a[160:167], v8\nv_smfmac_f32_16x16x64_bf16 v[80:83], v[64:67], a[216:223], v8\nv_add_f32 v219, v68, v69\n"
"v_add_f32 v220, v70, v71\nv_cvt_pk_bf16_f32 v156, v219, v220\nv_add_f32 v219, v72, v73\nv_add_f32 v220, v74, v75\n"
"v_cvt_pk_bf16_f32 v157, v219, v220\nv_add_f32 v219, v76, v77\nv_add_f32 v220, v78, v79\nv_cvt_pk_bf16_f32 v158, v219, v220\n"
"v_add_f32 v219, v80, v81\nv_add_f32 v220, v82, v83\nv_cvt_pk_bf16_f32 v159, v219, v220\nbuffer_store_dwordx4 v[156:159], v9, s[20:23], s51 offen sc1\n"
"s_add_u32 s47, s33, 64\nv_mov_b32 v219, s47\ns_waitcnt vmcnt(0)\nbuffer_store_dword v219, v18, s[8:11], 0 offen sc1\n"
"v_mov_b32 v220, s33\nbuffer_store_dword v220, v15, s[8:11], 0 offen sc1\ns_branch end_p0\nretry1_p0:\n"
"s_add_u32 s53, s53, 256\ns_add_u32 s49, s49, 1\ns_cmp_gt_u32 s49, 65536\ns_cbranch_scc1 end_p0\n"
"s_add_u32 s47, s40, 1\nv_mov_b32 v221, s47\nds_write_b32 v222, v221\nrec_p0:\n"
"s_sleep 4\nv_mov_b32 v68, 0\nv_mov_b32 v69, 0\nv_mov_b32 v70, 0\n"
"v_mov_b32 v71, 0\nv_mov_b32 v72, 0\nv_mov_b32 v73, 0\nv_mov_b32 v74, 0\n"
"v_mov_b32 v75, 0\nv_mov_b32 v76, 0\nv_mov_b32 v77, 0\nv_mov_b32 v78, 0\n"
"v_mov_b32 v79, 0\nv_mov_b32 v80, 0\nv_mov_b32 v81, 0\nv_mov_b32 v82, 0\n"
"v_mov_b32 v83, 0\nv_mov_b32 v218, 0\ns_branch top2_p0\nadopt_p0:\n"
"buffer_load_dwordx4 v[164:167], v28, s[8:11], 0 offen sc1\ns_lshl_b32 s52, s40, 26\ns_or_b32 s47, s52, 0x3ffffff\ns_waitcnt vmcnt(0)\n"
"v_cmp_ge_u32 vcc, s47, v164\ns_nop 1\nv_cndmask_b32 v164, 0, v164, vcc\nv_cmp_ge_u32 vcc, s47, v165\n"
"s_nop 1\nv_cndmask_b32 v165, 0, v165, vcc\nv_cmp_ge_u32 vcc, s47, v166\ns_nop 1\n"
"v_cndmask_b32 v166, 0, v166, vcc\nv_cmp_ge_u32 vcc, s47, v167\ns_nop 1\nv_cndmask_b32 v167, 0, v167, vcc\n"
"v_max3_u32 v221, v164, v165, v166\nv_max_u32 v221, v221, v167\ns_nop 1\nv_max_u32_dpp v221, v221, v221 row_ror:8 row_mask:0xf bank_mask:0xf\n"
"s_nop 1\nv_max_u32_dpp v221, v221, v221 row_ror:4 row_mask:0xf bank_mask:0xf\ns_nop 1\nv_max_u32_dpp v221, v221, v221 row_ror:2 row_mask:0xf bank_mask:0xf\n"
"s_nop 1\nv_max_u32_dpp v221, v221, v221 row_ror:1 row_mask:0xf bank_mask:0xf\ns_nop 0\nv_readlane_b32 s47, v221, 0\n"
"v_readlane_b32 s48, v221, 16\ns_max_u32 s48, s48, s47\ns_cmp_le_u32 s48, s52\ns_cbranch_scc1 back2_p0\n"
"s_bfe_u32 s52, s48, 1179656\ns_add_u32 s44, s52, s28\ns_mov_b32 s45, 0\ns_and_b32 s46, s48, 255\n"
"s_mov_b32 s92, 0\ns_branch back2_p0\nback2_p0:\ns_and_b32 s47, s46, 15\n"
"s_mul_i32 s47, s47, 4\ns_add_u32 s47, s47, s29\ns_add_u32 s47, s47, s45\ns_and_b32 s91, s47, 15\n"
"s_lshr_b32 s47, s47, 4\ns_lshl_b32 s47, s47, 2\ns_add_u32 s90, s44, s47\ns_mul_i32 s48, s91, 6\n"
"s_and_b32 s48, s48, -4\ns_getpc_b64 s[60:61]\nspc2_p0:\ns_sub_u32 s48, sgo_p0-spc2_p0, s48\n"
"s_add_u32 s60, s60, s48\ns_addc_u32 s61, s61, -1\ns_branch back_p0\nlate_p0:\n"
"s_add_u32 s53, s53, 16777216\ns_mov_b64 s[94:95], 1\ns_add_u32 s52, s90, 20\nlw_p0:\n"
"s_memrealtime s[54:55]\ns_waitcnt lgkmcnt(0)\ns_sub_u32 s47, s54, s52\ns_cmp_lt_i32 s47, 0\n"
"s_cbranch_scc1 lw_p0\ns_branch sgo_p0\nslow_p0:\ns_mov_b64 s[94:95], 0\n"
"s_cbranch_vccnz slow2_p0\ns_memrealtime s[54:55]\ns_add_u32 s52, s40, 1\ns_lshl_b32 s52, s52, 26\n"
"s_waitcnt lgkmcnt(0)\ns_sub_u32 s47, s54, s28\ns_add_u32 s47, s47, s31\ns_min_u32 s47, s47, 0x3ffff\n"
"s_lshl_b32 s47, s47, 8\ns_or_b32 s47, s47, s52\ns_sub_u32 s52, s92, 1\ns_cmp_lt_u32 s52, 6\n"
"s_cselect_b32 s48, 1, 0\ns_cmp_ge_u32 s40, 5\ns_cselect_b32 s48, s48, 0\ns_add_u32 s48, s48, s46\n"
"s_min_u32 s48, s48, 15\ns_or_b32 s47, s47, s48\nv_mov_b32 v225, s47\nbuffer_store_dword v225, v19, s[8:11], 0 offen sc1\n"
"s_add_u32 s47, s40, 2\nv_mov_b32 v221, s47\nds_write_b32 v222, v221\ns_branch join_p0\n"
"slow2_p0:\nv_max3_u32 v218, v124, v125, v126\nv_max_u32 v218, v218, v127\nv_max3_u32 v218, v218, v128, v129\n"
"v_max3_u32 v218, v218, v130, v131\nv_max3_u32 v218, v218, v116, v117\nv_max3_u32 v218, v218, v118, v119\nv_max3_u32 v218, v218, v120, v121\n"
"v_max3_u32 v218, v218, v122, v123\nv_cmp_eq_u32 vcc, -1, v218\ns_cbranch_vccz exact_p0\ns_branch spin_p0\n"
"spin2_p0:\ns_sleep 3\nspin_p0:\ns_add_u32 s53, s53, 1\n"
"s_add_u32 s49, s49, 1\ns_cmp_gt_u32 s49, 65536\ns_cbranch_scc1 end_p0\nbuffer_load_dwordx4 v[116:119], v20, s[8:11], s41 offen offset:0 sc1\n"
"buffer_load_dwordx4 v[120:123], v20, s[8:11], s41 offen offset:1024 sc1\nbuffer_load_dwordx4 v[124:127], v21, s[8:11], s41 offen offset:0 sc1\nbuffer_load_dwordx4 v[128:131], v21, s[8:11], s41 offen offset:1024 sc1\ns_waitcnt vmcnt(0)\n"
"v_max3_u32 v218, v124, v125, v126\nv_max_u32 v218, v218, v127\nv_max3_u32 v218, v218, v128, v129\nv_max3_u32 v218, v218, v130, v131\n"
"v_max3_u32 v218, v218, v116, v117\nv_max3_u32 v218, v218, v118, v119\nv_max3_u32 v218, v218, v120, v121\nv_max3_u32 v218, v218, v122, v123\n"
"v_cmp_eq_u32 vcc, -1, v218\ns_cbranch_vccnz spin2_p0\ns_memrealtime s[54:55]\ns_add_u32 s52, s40, 1\n"
"s_lshl_b32 s52, s52, 26\ns_waitcnt lgkmcnt(0)\ns_sub_u32 s47, s54, s28\ns_add_u32 s47, s47, s38\n"
"s_min_u32 s47, s47, 0x3ffff\ns_lshl_b32 s47, s47, 8\ns_or_b32 s47, s47, s52\ns_sub_u32 s52, s92, 1\n"
"s_cmp_lt_u32 s52, 6\ns_cselect_b32 s48, 1, 0\ns_cmp_ge_u32 s40, 5\ns_cselect_b32 s48, s48, 0\n"
"s_add_u32 s48, s48, s46\ns_min_u32 s48, s48, 15\ns_or_b32 s47, s47, s48\nv_mov_b32 v225, s47\n"
"buffer_store_dword v225, v19, s[8:11], 0 offen sc1\ns_add_u32 s47, s40, 2\nv_mov_b32 v221, s47\nds_write_b32 v222, v221\n"
"v_mfma_f32_16x16x32_bf16 v[132:135], v[84:87], v[116:119], 0\nv_mfma_f32_16x16x32_bf16 v[136:139], v[88:91], v[116:119], 0\nv_mfma_f32_16x16x32_bf16 v[148:151], v[176:179], v[116:119], 0\nv_mfma_f32_16x16x32_bf16 v[140:143], v[92:95], v[120:123], 0\n"
"v_mfma_f32_16x16x32_bf16 v[144:147], v[96:99], v[120:123], 0\nv_mfma_f32_16x16x32_bf16 v[148:151], v[180:183], v[120:123], v[148:151]\nv_cndmask_b32 v132, v136, v132, s[62:63]\nv_cndmask_b32 v133, v137, v133, s[62:63]\n"
"v_cndmask_b32 v134, v138, v134, s[62:63]\nv_cndmask_b32 v135, v139, v135, s[62:63]\nv_mfma_f32_16x16x32_f16 v[152:155], v[168:171], v[124:127], 0\nv_mfma_f32_16x16x32_f16 v[152:155], v[172:175], v[128:131], v[152:155]\n"
"v_cndmask_b32 v140, v144, v140, s[62:63]\nv_cndmask_b32 v141, v145, v141, s[62:63]\nv_cndmask_b32 v142, v146, v142, s[62:63]\nv_cndmask_b32 v143, v147, v143, s[62:63]\n"
"s_nop 2\nv_exp_f32 v152, v152\nv_exp_f32 v153, v153\nv_exp_f32 v154, v154\n"
"v_exp_f32 v155, v155\nv_cvt_pk_bf16_f32 v200, v152, v153\nv_cvt_pk_bf16_f32 v201, v154, v155\nv_and_b32 v160, v200, v223\n"
"v_and_b32 v161, v200, v224\nv_mfma_f32_16x16x16_bf16 v[148:151], v[214:215], v[200:201], v[148:151]\nv_and_b32 v162, v201, v223\nv_and_b32 v163, v201, v224\n"
"v_mfma_f32_16x16x16_bf16 v[132:135], v[156:157], v[160:161], v[132:135]\ns_nop 0\nv_mfma_f32_16x16x16_bf16 v[140:143], v[158:159], v[162:163], v[140:143]\nv_rcp_f32 v202, v148\n"
"v_rcp_f32 v203, v149\nv_mul_f32 v132, v132, v202\nv_mul_f32 v133, v133, v202\nv_mul_f32 v134, v134, v202\n"
"v_mul_f32 v135, v135, v202\nv_fmac_f32 v132, v140, v203\nv_fmac_f32 v133, v141, v203\nv_fmac_f32 v134, v142, v203\n"
"v_fmac_f32 v135, v143, v203\nv_add_f32_dpp v204, v132, v132 row_ror:8 row_mask:0xf bank_mask:0x3\nv_add_f32_dpp v205, v133, v133 row_ror:8 row_mask:0xf bank_mask:0x3\nv_add_f32_dpp v204, v134, v134 row_ror:8 row_mask:0xf bank_mask:0xc\n"
"v_add_f32_dpp v205, v135, v135 row_ror:8 row_mask:0xf bank_mask:0xc\nv_subrev_u32 v219, 0x2f800000, v148\nv_subrev_u32 v220, 0x2f800000, v149\nv_max_u32 v219, v219, v220\n"
"v_cmp_le_u32 vcc, 0x20000000, v219\nds_write_b64 v22, v[204:205]\ns_cbranch_vccz join_p0\nexact_p0:\n"
"s_add_u32 s53, s53, 65536\nex0_p0:\ns_add_u32 s47, s41, 0\nbuffer_load_dwordx2 v[184:185], v24, s[8:11], s47 offen offset:0 sc1\n"
"buffer_load_dwordx2 v[186:187], v24, s[8:11], s47 offen offset:512 sc1\nbuffer_load_dwordx2 v[188:189], v24, s[8:11], s47 offen offset:1024 sc1\nbuffer_load_dwordx2 v[190:191], v24, s[8:11], s47 offen offset:1536 sc1\nbuffer_load_dwordx4 v[192:195], v25, s[8:11], s47 offen sc1\n"
"buffer_load_dwordx4 v[196:199], v25, s[8:11], s47 offen offset:16 sc1\nv_mov_b32 v218, 0\ns_waitcnt vmcnt(0)\nv_max3_u32 v218, v218, v184, v185\n"
"v_max3_u32 v218, v218, v186, v187\nv_max3_u32 v218, v218, v188, v189\nv_max3_u32 v218, v218, v190, v191\nv_max3_u32 v218, v218, v192, v193\n"
"v_max3_u32 v218, v218, v194, v195\nv_max3_u32 v218, v218, v196, v197\nv_max3_u32 v218, v218, v198, v199\nv_cmp_eq_u32 vcc, -1, v218\n"
"s_cbranch_vccz exok0_p0\ns_add_u32 s53, s53, 0\ns_add_u32 s49, s49, 1\ns_cmp_gt_u32 s49, 65536\n"
"s_cbranch_scc1 end_p0\ns_branch ex0_p0\nexok0_p0:\nv_add_f32 v184, v184, v186\n"
"v_add_f32 v188, v188, v190\nv_add_f32 v184, v184, v188\nv_add_f32 v185, v185, v187\nv_add_f32 v189, v189, v191\n"
"v_add_f32 v185, v185, v189\nv_max_f32 v225, v184, v185\nv_max3_f32 v225, v225, v192, v193\nv_max3_f32 v225, v225, v194, v195\n"
"v_max3_f32 v225, v225, v196, v197\nv_max3_f32 v225, v225, v198, v199\nv_mov_b32 v219, v225\ns_nop 1\n"
"v_permlane16_swap_b32 v225, v219\nv_max_f32 v225, v225, v219\nv_mov_b32 v219, v225\ns_nop 1\n"
"v_permlane32_swap_b32 v225, v219\nv_max_f32 v225, v225, v219\nv_sub_f32 v184, v184, v225\nv_exp_f32 v152, v184\n"
"v_sub_f32 v185, v185, v225\nv_exp_f32 v153, v185\nv_sub_f32 v192, v192, v225\nv_exp_f32 v192, v192\n"
"v_sub_f32 v193, v193, v225\nv_exp_f32 v193, v193\nv_sub_f32 v194, v194, v225\nv_exp_f32 v194, v194\n"
"v_sub_f32 v195, v195, v225\nv_exp_f32 v195, v195\nv_sub_f32 v196, v196, v225\nv_exp_f32 v196, v196\n"
"v_sub_f32 v197, v197, v225\nv_exp_f32 v197, v197\nv_sub_f32 v198, v198, v225\nv_exp_f32 v198, v198\n"
"v_sub_f32 v199, v199, v225\nv_exp_f32 v199, v199\ns_nop 0\nv_cvt_pk_bf16_f32 v116, v192, v193\n"
"v_cvt_pk_bf16_f32 v117, v194, v195\nv_cvt_pk_bf16_f32 v118, v196, v197\nv_cvt_pk_bf16_f32 v119, v198, v199\nex1_p0:\n"
"s_add_u32 s47, s41, 4096\nbuffer_load_dwordx2 v[184:185], v24, s[8:11], s47 offen offset:0 sc1\nbuffer_load_dwordx2 v[186:187], v24, s[8:11], s47 offen offset:512 sc1\nbuffer_load_dwordx2 v[188:189], v24, s[8:11], s47 offen offset:1024 sc1\n"
"buffer_load_dwordx2 v[190:191], v24, s[8:11], s47 offen offset:1536 sc1\nbuffer_load_dwordx4 v[192:195], v25, s[8:11], s47 offen sc1\nbuffer_load_dwordx4 v[196:199], v25, s[8:11], s47 offen offset:16 sc1\nv_mov_b32 v218, 0\n"
"s_waitcnt vmcnt(0)\nv_max3_u32 v218, v218, v184, v185\nv_max3_u32 v218, v218, v186, v187\nv_max3_u32 v218, v218, v188, v189\n"
"v_max3_u32 v218, v218, v190, v191\nv_max3_u32 v218, v218, v192, v193\nv_max3_u32 v218, v218, v194, v195\nv_max3_u32 v218, v218, v196, v197\n"
"v_max3_u32 v218, v218, v198, v199\nv_cmp_eq_u32 vcc, -1, v218\ns_cbranch_vccz exok1_p0\ns_add_u32 s53, s53, 0\n"
"s_add_u32 s49, s49, 1\ns_cmp_gt_u32 s49, 65536\ns_cbranch_scc1 end_p0\ns_branch ex1_p0\n"
"exok1_p0:\nv_add_f32 v184, v184, v186\nv_add_f32 v188, v188, v190\nv_add_f32 v184, v184, v188\n"
"v_add_f32 v185, v185, v187\nv_add_f32 v189, v189, v191\nv_add_f32 v185, v185, v189\nv_max_f32 v225, v184, v185\n"
"v_max3_f32 v225, v225, v192, v193\nv_max3_f32 v225, v225, v194, v195\nv_max3_f32 v225, v225, v196, v197\nv_max3_f32 v225, v225, v198, v199\n"
"v_mov_b32 v219, v225\ns_nop 1\nv_permlane16_swap_b32 v225, v219\nv_max_f32 v225, v225, v219\n"
"v_mov_b32 v219, v225\ns_nop 1\nv_permlane32_swap_b32 v225, v219\nv_max_f32 v225, v225, v219\n"
"v_sub_f32 v184, v184, v225\nv_exp_f32 v154, v184\nv_sub_f32 v185, v185, v225\nv_exp_f32 v155, v185\n"
"v_sub_f32 v192, v192, v225\nv_exp_f32 v192, v192\nv_sub_f32 v193, v193, v225\nv_exp_f32 v193, v193\n"
"v_sub_f32 v194, v194, v225\nv_exp_f32 v194, v194\nv_sub_f32 v195, v195, v225\nv_exp_f32 v195, v195\n"
"v_sub_f32 v196, v196, v225\nv_exp_f32 v196, v196\nv_sub_f32 v197, v197, v225\nv_exp_f32 v197, v197\n"
"v_sub_f32 v198, v198, v225\nv_exp_f32 v198, v198\nv_sub_f32 v199, v199, v225\nv_exp_f32 v199, v199\n"
"s_nop 0\nv_cvt_pk_bf16_f32 v120, v192, v193\nv_cvt_pk_bf16_f32 v121, v194, v195\nv_cvt_pk_bf16_f32 v122, v196, v197\n"
"v_cvt_pk_bf16_f32 v123, v198, v199\ns_memrealtime s[54:55]\ns_add_u32 s52, s40, 1\ns_lshl_b32 s52, s52, 26\n"
"s_waitcnt lgkmcnt(0)\ns_sub_u32 s47, s54, s28\ns_add_u32 s47, s47, s38\ns_min_u32 s47, s47, 0x3ffff\n"
"s_lshl_b32 s47, s47, 8\ns_or_b32 s47, s47, s52\ns_sub_u32 s52, s92, 1\ns_cmp_lt_u32 s52, 6\n"
"s_cselect_b32 s48, 1, 0\ns_cmp_ge_u32 s40, 5\ns_cselect_b32 s48, s48, 0\ns_add_u32 s48, s48, s46\n"
"s_min_u32 s48, s48, 15\ns_or_b32 s47, s47, s48\nv_mov_b32 v225, s47\nbuffer_store_dword v225, v19, s[8:11], 0 offen sc1\n"
"s_add_u32 s47, s40, 2\nv_mov_b32 v221, s47\nds_write_b32 v222, v221\nv_mfma_f32_16x16x32_bf16 v[132:135], v[84:87], v[116:119], 0\n"
"v_mfma_f32_16x16x32_bf16 v[136:139], v[88:91], v[116:119], 0\nv_mfma_f32_16x16x32_bf16 v[148:151], v[176:179], v[116:119], 0\nv_mfma_f32_16x16x32_bf16 v[140:143], v[92:95], v[120:123], 0\nv_mfma_f32_16x16x32_bf16 v[144:147], v[96:99], v[120:123], 0\n"
"v_mfma_f32_16x16x32_bf16 v[148:151], v[180:183], v[120:123], v[148:151]\nv_cndmask_b32 v132, v136, v132, s[62:63]\nv_cndmask_b32 v133, v137, v133, s[62:63]\nv_cndmask_b32 v134, v138, v134, s[62:63]\n"
"v_cndmask_b32 v135, v139, v135, s[62:63]\ns_nop 0\nv_cndmask_b32 v140, v144, v140, s[62:63]\nv_cndmask_b32 v141, v145, v141, s[62:63]\n"
"v_cndmask_b32 v142, v146, v142, s[62:63]\nv_cndmask_b32 v143, v147, v143, s[62:63]\nv_cvt_pk_bf16_f32 v200, v152, v153\nv_cvt_pk_bf16_f32 v201, v154, v155\n"
"v_and_b32 v160, v200, v223\nv_and_b32 v161, v200, v224\nv_mfma_f32_16x16x16_bf16 v[148:151], v[214:215], v[200:201], v[148:151]\nv_and_b32 v162, v201, v223\n"
"v_and_b32 v163, v201, v224\nv_mfma_f32_16x16x16_bf16 v[132:135], v[156:157], v[160:161], v[132:135]\ns_nop 0\nv_mfma_f32_16x16x16_bf16 v[140:143], v[158:159], v[162:163], v[140:143]\n"
"v_rcp_f32 v202, v148\nv_rcp_f32 v203, v149\nv_mul_f32 v132, v132, v202\nv_mul_f32 v133, v133, v202\n"
"v_mul_f32 v134, v134, v202\nv_mul_f32 v135, v135, v202\nv_fmac_f32 v132, v140, v203\nv_fmac_f32 v133, v141, v203\n"
"v_fmac_f32 v134, v142, v203\nv_fmac_f32 v135, v143, v203\nv_add_f32_dpp v204, v132, v132 row_ror:8 row_mask:0xf bank_mask:0x3\nv_add_f32_dpp v205, v133, v133 row_ror:8 row_mask:0xf bank_mask:0x3\n"
"v_add_f32_dpp v204, v134, v134 row_ror:8 row_mask:0xf bank_mask:0xc\nv_add_f32_dpp v205, v135, v135 row_ror:8 row_mask:0xf bank_mask:0xc\nds_write_b64 v22, v[204:205]\ns_branch join_p0\n"
"end_p0:\ns_branch fk_done\nfk_p1:\ns_mov_b32 s40, 0\n"
"s_mov_b32 s41, 0\ns_mov_b32 s42, 262144\ns_mov_b32 s43, 393216\ns_mov_b32 s44, s28\n"
"s_mov_b32 s45, 0\ns_mov_b32 s46, 0\ns_mov_b32 s92, 0\ns_mov_b32 s49, 0\n"
"s_mov_b32 s53, 0\ns_mov_b32 s50, s35\ns_mov_b32 s51, s36\nv_mov_b32 v222, 24576\n"
"s_mov_b32 s62, 0x00ff00ff\ns_mov_b32 s63, 0x00ff00ff\nv_cndmask_b32 v223, 0, -1, s[62:63]\nv_not_b32 v224, v223\n"
"v_bfe_u32 v219, v9, 4, 1\nv_lshlrev_b32 v220, 4, v219\nv_mov_b32 v218, 0x3c00\nv_lshlrev_b32 v218, v220, v218\n"
"v_bfe_u32 v220, v9, 5, 3\nv_cmp_eq_u32 vcc, 0, v220\ns_nop 1\nv_cndmask_b32 v168, 0, v218, vcc\n"
"v_cmp_eq_u32 vcc, 2, v220\ns_nop 1\nv_cndmask_b32 v169, 0, v218, vcc\nv_cmp_eq_u32 vcc, 4, v220\n"
"s_nop 1\nv_cndmask_b32 v170, 0, v218, vcc\nv_cmp_eq_u32 vcc, 6, v220\ns_nop 1\n"
"v_cndmask_b32 v171, 0, v218, vcc\nv_cmp_eq_u32 vcc, 1, v220\ns_nop 1\nv_cndmask_b32 v172, 0, v218, vcc\n"
"v_cmp_eq_u32 vcc, 3, v220\ns_nop 1\nv_cndmask_b32 v173, 0, v218, vcc\nv_cmp_eq_u32 vcc, 5, v220\n"
"s_nop 1\nv_cndmask_b32 v174, 0, v218, vcc\nv_cmp_eq_u32 vcc, 7, v220\ns_nop 1\n"
"v_cndmask_b32 v175, 0, v218, vcc\nv_mov_b32 v218, 0x3f803f80\nv_cmp_eq_u32 vcc, 0, v219\ns_nop 1\n"
"v_cndmask_b32 v176, 0, v218, vcc\nv_cndmask_b32 v177, 0, v218, vcc\nv_cndmask_b32 v178, 0, v218, vcc\nv_cndmask_b32 v179, 0, v218, vcc\n"
"v_cndmask_b32 v214, 0, v218, vcc\nv_cmp_eq_u32 vcc, 1, v219\ns_nop 1\nv_cndmask_b32 v180, 0, v218, vcc\n"
"v_cndmask_b32 v181, 0, v218, vcc\nv_cndmask_b32 v182, 0, v218, vcc\nv_cndmask_b32 v183, 0, v218, vcc\nv_cndmask_b32 v215, 0, v218, vcc\n"
"s_add_u32 s47, s34, 28672\nbuffer_load_dwordx4 a[0:3], v9, s[12:15], s47 offen sc1\nbuffer_load_dwordx4 a[4:7], v9, s[12:15], s47 offen offset:1024 sc1\ns_add_u32 s47, s34, 30720\n"
"buffer_load_dwordx4 a[8:11], v9, s[12:15], s47 offen sc1\nbuffer_load_dwordx4 a[12:15], v9, s[12:15], s47 offen offset:1024 sc1\ns_add_u32 s47, s34, 32768\nbuffer_load_dwordx4 a[16:19], v9, s[12:15], s47 offen sc1\n"
"buffer_load_dwordx4 a[20:23], v9, s[12:15], s47 offen offset:1024 sc1\ns_add_u32 s47, s34, 34816\nbuffer_load_dwordx4 a[24:27], v9, s[12:15], s47 offen sc1\nbuffer_load_dwordx4 a[28:31], v9, s[12:15], s47 offen offset:1024 sc1\n"
"s_add_u32 s47, s34, 36864\nbuffer_load_dwordx4 a[32:35], v9, s[12:15], s47 offen sc1\nbuffer_load_dwordx4 a[36:39], v9, s[12:15], s47 offen offset:1024 sc1\ns_add_u32 s47, s34, 38912\n"
"buffer_load_dwordx4 a[40:43], v9, s[12:15], s47 offen sc1\nbuffer_load_dwordx4 a[44:47], v9, s[12:15], s47 offen offset:1024 sc1\ns_add_u32 s47, s34, 40960\nbuffer_load_dwordx4 a[48:51], v9, s[12:15], s47 offen sc1\n"
"buffer_load_dwordx4 a[52:55], v9, s[12:15], s47 offen offset:1024 sc1\ns_add_u32 s47, s34, 43008\nbuffer_load_dwordx4 a[56:59], v9, s[12:15], s47 offen sc1\nbuffer_load_dwordx4 a[60:63], v9, s[12:15], s47 offen offset:1024 sc1\n"
"s_add_u32 s47, s34, 45056\nbuffer_load_dwordx4 a[64:67], v9, s[12:15], s47 offen sc1\nbuffer_load_dwordx4 a[68:71], v9, s[12:15], s47 offen offset:1024 sc1\ns_add_u32 s47, s34, 47104\n"
"buffer_load_dwordx4 a[72:75], v9, s[12:15], s47 offen sc1\nbuffer_load_dwordx4 a[76:79], v9, s[12:15], s47 offen offset:1024 sc1\ns_add_u32 s47, s34, 49152\nbuffer_load_dwordx4 a[80:83], v9, s[12:15], s47 offen sc1\n"
"buffer_load_dwordx4 a[84:87], v9, s[12:15], s47 offen offset:1024 sc1\ns_add_u32 s47, s34, 51200\nbuffer_load_dwordx4 a[88:91], v9, s[12:15], s47 offen sc1\nbuffer_load_dwordx4 a[92:95], v9, s[12:15], s47 offen offset:1024 sc1\n"
"s_add_u32 s47, s34, 53248\nbuffer_load_dwordx4 a[96:99], v9, s[12:15], s47 offen sc1\nbuffer_load_dwordx4 a[100:103], v9, s[12:15], s47 offen offset:1024 sc1\ns_add_u32 s47, s34, 55296\n"
"buffer_load_dwordx4 a[104:107], v9, s[12:15], s47 offen sc1\nbuffer_load_dwordx4 a[108:111], v9, s[12:15], s47 offen offset:1024 sc1\ns_add_u32 s47, s34, 0\nbuffer_load_dwordx4 a[112:115], v9, s[12:15], s47 offen sc1\n"
"buffer_load_dwordx4 a[116:119], v9, s[12:15], s47 offen offset:1024 sc1\ns_add_u32 s47, s34, 2048\nbuffer_load_dwordx4 a[120:123], v9, s[12:15], s47 offen sc1\nbuffer_load_dwordx4 a[124:127], v9, s[12:15], s47 offen offset:1024 sc1\n"
"s_add_u32 s47, s34, 4096\nbuffer_load_dwordx4 a[128:131], v9, s[12:15], s47 offen sc1\nbuffer_load_dwordx4 a[132:135], v9, s[12:15], s47 offen offset:1024 sc1\ns_add_u32 s47, s34, 6144\n"
"buffer_load_dwordx4 a[136:139], v9, s[12:15], s47 offen sc1\nbuffer_load_dwordx4 a[140:143], v9, s[12:15], s47 offen offset:1024 sc1\ns_add_u32 s47, s34, 8192\nbuffer_load_dwordx4 a[144:147], v9, s[12:15], s47 offen sc1\n"
"buffer_load_dwordx4 a[148:151], v9, s[12:15], s47 offen offset:1024 sc1\ns_add_u32 s47, s34, 10240\nbuffer_load_dwordx4 a[152:155], v9, s[12:15], s47 offen sc1\nbuffer_load_dwordx4 a[156:159], v9, s[12:15], s47 offen offset:1024 sc1\n"
"s_add_u32 s47, s34, 12288\nbuffer_load_dwordx4 a[160:163], v9, s[12:15], s47 offen sc1\nbuffer_load_dwordx4 a[164:167], v9, s[12:15], s47 offen offset:1024 sc1\ns_add_u32 s47, s34, 14336\n"
"buffer_load_dwordx4 a[168:171], v9, s[12:15], s47 offen sc1\nbuffer_load_dwordx4 a[172:175], v9, s[12:15], s47 offen offset:1024 sc1\ns_add_u32 s47, s34, 16384\nbuffer_load_dwordx4 a[176:179], v9, s[12:15], s47 offen sc1\n"
"buffer_load_dwordx4 a[180:183], v9, s[12:15], s47 offen offset:1024 sc1\ns_add_u32 s47, s34, 18432\nbuffer_load_dwordx4 a[184:187], v9, s[12:15], s47 offen sc1\nbuffer_load_dwordx4 a[188:191], v9, s[12:15], s47 offen offset:1024 sc1\n"
"s_add_u32 s47, s34, 20480\nbuffer_load_dwordx4 a[192:195], v9, s[12:15], s47 offen sc1\nbuffer_load_dwordx4 a[196:199], v9, s[12:15], s47 offen offset:1024 sc1\ns_add_u32 s47, s34, 22528\n"
"buffer_load_dwordx4 a[200:203], v9, s[12:15], s47 offen sc1\nbuffer_load_dwordx4 a[204:207], v9, s[12:15], s47 offen offset:1024 sc1\ns_add_u32 s47, s34, 24576\nbuffer_load_dwordx4 a[208:211], v9, s[12:15], s47 offen sc1\n"
"buffer_load_dwordx4 a[212:215], v9, s[12:15], s47 offen offset:1024 sc1\ns_add_u32 s47, s34, 26624\nbuffer_load_dwordx4 a[216:219], v9, s[12:15], s47 offen sc1\nbuffer_load_dwordx4 a[220:223], v9, s[12:15], s47 offen offset:1024 sc1\n"
"v_lshlrev_b32 v166, 2, v9\ns_mov_b32 s52, 1064960\nbuffer_load_dword v164, v166, s[8:11], s52 offen sc1\ns_add_u32 s52, s52, 4096\n"
"buffer_load_dword v165, v166, s[8:11], s52 offen sc1\nbuffer_load_dword v218, v11, s[8:11], 0 offen sc1\ns_mov_b64 s[94:95], 1\ns_barrier\n"
"s_waitcnt vmcnt(0)\nar_p1:\nv_xor_b32 v164, s33, v164\nv_xor_b32 v165, s33, v165\n"
"v_or_b32 v164, v164, v165\nv_cmp_ne_u32 vcc, 0, v164\ns_cbranch_vccz arok_p1\ns_add_u32 s53, s53, 0\n"
"s_add_u32 s49, s49, 1\ns_cmp_gt_u32 s49, 65536\ns_cbranch_scc1 end_p1\ns_sleep 2\n"
"v_lshlrev_b32 v166, 2, v9\ns_mov_b32 s52, 1064960\nbuffer_load_dword v164, v166, s[8:11], s52 offen sc1\ns_add_u32 s52, s52, 4096\n"
"buffer_load_dword v165, v166, s[8:11], s52 offen sc1\ns_waitcnt vmcnt(0)\ns_branch ar_p1\narok_p1:\n"
"fl_p1:\ns_add_u32 s47, s40, 2\ns_min_u32 s47, s47, 64\ns_add_u32 s47, s47, s33\n"
"v_subrev_u32 v218, s47, v218\nv_cmp_lt_u32 vcc, 64, v218\ns_cbranch_vccz flok_p1\ns_add_u32 s53, s53, 0\n"
"s_add_u32 s49, s49, 1\ns_cmp_gt_u32 s49, 65536\ns_cbranch_scc1 end_p1\ns_sleep 2\n"
"buffer_load_dword v218, v11, s[8:11], 0 offen sc1\ns_waitcnt vmcnt(0)\ns_branch fl_p1\nflok_p1:\n"
"buffer_load_dwordx4 v[100:103], v9, s[16:19], s50 offen offset:2048 sc1\nbuffer_load_dwordx4 v[104:107], v9, s[16:19], s50 offen offset:3072 sc1\nbuffer_load_dwordx4 v[108:111], v9, s[16:19], s50 offen offset:0 sc1\nbuffer_load_dwordx4 v[112:115], v9, s[16:19], s50 offen offset:1024 sc1\n"
"s_add_u32 s50, s50, 16384\ns_memrealtime s[54:55]\ns_waitcnt lgkmcnt(0)\ns_add_u32 s44, s54, 12\n"
"s_mov_b32 s45, 8\n.p2align 6\ntop_p1:\nv_mov_b32 v68, 0\n"
"v_mov_b32 v69, 0\nv_mov_b32 v70, 0\nv_mov_b32 v71, 0\nv_mov_b32 v72, 0\n"
"v_mov_b32 v73, 0\nv_mov_b32 v74, 0\nv_mov_b32 v75, 0\nv_mov_b32 v76, 0\n"
"v_mov_b32 v77, 0\nv_mov_b32 v78, 0\nv_mov_b32 v79, 0\nv_mov_b32 v80, 0\n"
"v_mov_b32 v81, 0\nv_mov_b32 v82, 0\nv_mov_b32 v83, 0\nv_mov_b32 v218, 0\n"
"s_add_u32 s52, s44, s37\nm_p1:\ns_memrealtime s[54:55]\ns_waitcnt lgkmcnt(0)\n"
"s_sub_u32 s47, s54, s52\ns_cmp_lt_i32 s47, 0\ns_cbranch_scc1 m_p1\ntop2_p1:\n"
"buffer_load_dwordx4 v[52:55], v10, s[8:11], s43 offen offset:1024 sc1\nbuffer_load_dwordx4 v[48:51], v10, s[8:11], s43 offen offset:0 sc1\ns_and_b32 s47, s46, 15\ns_mul_i32 s47, s47, 4\n"
"s_add_u32 s47, s47, s29\ns_add_u32 s47, s47, s45\ns_and_b32 s91, s47, 15\ns_lshr_b32 s47, s47, 4\n"
"s_lshl_b32 s47, s47, 2\ns_add_u32 s90, s44, s47\ns_mul_i32 s48, s91, 6\ns_and_b32 s48, s48, -4\n"
"s_getpc_b64 s[60:61]\nspc_p1:\ns_sub_u32 s48, sgo_p1-spc_p1, s48\ns_add_u32 s60, s60, s48\n"
"s_addc_u32 s61, s61, 0\ns_waitcnt vmcnt(1)\nds_write_b128 v9, v[52:55] offset:3072\nv_max3_u32 v218, v218, v52, v53\n"
"v_max3_u32 v218, v218, v54, v55\ns_waitcnt vmcnt(0)\nds_write_b128 v9, v[48:51] offset:2048\nv_max3_u32 v218, v218, v48, v49\n"
"v_max3_u32 v218, v218, v50, v51\nv_cmp_eq_u32 vcc, -1, v218\nv_smfmac_f32_16x16x64_bf16 v[68:71], v[52:55], a[24:31], v8\nv_smfmac_f32_16x16x64_bf16 v[72:75], v[52:55], a[80:87], v8\n"
"s_cbranch_vccnz retry1_p1\ns_waitcnt lgkmcnt(0)\ns_barrier\nds_read_b32 v221, v222\n"
"ds_read_b128 v[56:59], v9 offset:4096\nds_read_b128 v[60:63], v9 offset:5120\nds_read_b128 v[64:67], v9 offset:6144\nds_read_b128 v[40:43], v9 offset:0\n"
"ds_read_b128 v[44:47], v9 offset:1024\nv_smfmac_f32_16x16x64_bf16 v[76:79], v[52:55], a[136:143], v8\nv_smfmac_f32_16x16x64_bf16 v[80:83], v[52:55], a[192:199], v8\nv_smfmac_f32_16x16x64_bf16 v[68:71], v[48:51], a[16:23], v8\n"
"v_smfmac_f32_16x16x64_bf16 v[72:75], v[48:51], a[72:79], v8\nv_smfmac_f32_16x16x64_bf16 v[76:79], v[48:51], a[128:135], v8\nv_smfmac_f32_16x16x64_bf16 v[80:83], v[48:51], a[184:191], v8\ns_waitcnt lgkmcnt(0)\n"
"v_readfirstlane_b32 s47, v221\ns_sub_u32 s47, s47, s40\ns_cmp_eq_u32 s47, 1\ns_cbranch_scc1 adopt_p1\n"
"back_p1:\ns_cmp_eq_u32 s40, 63\ns_cbranch_scc1 last_p1\nsf_p1:\n"
"s_memrealtime s[54:55]\ns_nop 2\ns_memrealtime s[56:57]\ns_nop 2\n"
"s_memrealtime s[58:59]\ns_waitcnt lgkmcnt(4)\ns_sub_u32 s47, s54, s90\ns_add_u32 s48, s47, 12\n"
"s_cmp_ge_u32 s48, 12\ns_cbranch_scc1 so_p1\ns_memrealtime s[54:55]\ns_waitcnt lgkmcnt(4)\n"
"s_cmp_ge_u32 s56, s90\ns_cbranch_scc1 sx_p1\ns_memrealtime s[56:57]\ns_waitcnt lgkmcnt(4)\n"
"s_cmp_ge_u32 s58, s90\ns_cbranch_scc1 sx_p1\ns_memrealtime s[58:59]\ns_waitcnt lgkmcnt(4)\n"
"s_cmp_ge_u32 s54, s90\ns_cbranch_scc1 sx_p1\ns_memrealtime s[54:55]\ns_waitcnt lgkmcnt(4)\n"
"s_cmp_ge_u32 s56, s90\ns_cbranch_scc1 sx_p1\ns_memrealtime s[56:57]\ns_waitcnt lgkmcnt(4)\n"
"s_cmp_ge_u32 s58, s90\ns_cbranch_scc1 sx_p1\ns_memrealtime s[58:59]\ns_waitcnt lgkmcnt(4)\n"
"s_cmp_ge_u32 s54, s90\ns_cbranch_scc1 sx_p1\ns_memrealtime s[54:55]\ns_waitcnt lgkmcnt(4)\n"
"s_cmp_ge_u32 s56, s90\ns_cbranch_scc1 sx_p1\ns_memrealtime s[56:57]\ns_waitcnt lgkmcnt(4)\n"
"s_cmp_ge_u32 s58, s90\ns_cbranch_scc1 sx_p1\ns_memrealtime s[58:59]\ns_waitcnt lgkmcnt(4)\n"
"s_cmp_ge_u32 s54, s90\ns_cbranch_scc1 sx_p1\ns_memrealtime s[54:55]\ns_waitcnt lgkmcnt(4)\n"
"s_cmp_ge_u32 s56, s90\ns_cbranch_scc1 sx_p1\ns_memrealtime s[56:57]\ns_waitcnt lgkmcnt(4)\n"
"s_cmp_ge_u32 s58, s90\ns_cbranch_scc1 sx_p1\ns_memrealtime s[58:59]\ns_waitcnt lgkmcnt(4)\n"
"s_cmp_ge_u32 s54, s90\ns_cbranch_scc1 sx_p1\ns_memrealtime s[54:55]\ns_waitcnt lgkmcnt(4)\n"
"s_cmp_ge_u32 s56, s90\ns_cbranch_scc1 sx_p1\ns_memrealtime s[56:57]\ns_waitcnt lgkmcnt(4)\n"
"s_cmp_ge_u32 s58, s90\ns_cbranch_scc1 sx_p1\ns_memrealtime s[58:59]\ns_waitcnt lgkmcnt(4)\n"
"s_cmp_ge_u32 s54, s90\ns_cbranch_scc1 sx_p1\ns_memrealtime s[54:55]\ns_waitcnt lgkmcnt(4)\n"
"s_cmp_ge_u32 s56, s90\ns_cbranch_scc1 sx_p1\ns_memrealtime s[56:57]\ns_branch sx_p1\n"
"so_p1:\ns_waitcnt lgkmcnt(0)\ns_cmp_lt_i32 s47, 0\ns_cbranch_scc1 sf_p1\n"
"s_cmp_gt_i32 s47, s32\ns_cbranch_scc1 late_p1\ns_branch sgo_p1\nsx_p1:\n"
"s_setpc_b64 s[60:61]\ns_nop 0\ns_nop 0\ns_nop 0\n"
"s_nop 0\ns_nop 0\ns_nop 0\ns_nop 0\n"
"s_nop 0\ns_nop 0\ns_nop 0\ns_nop 0\n"
"s_nop 0\ns_nop 0\ns_nop 0\ns_nop 0\n"
"s_nop 0\ns_nop 0\ns_nop 0\ns_nop 0\n"
"s_nop 0\ns_nop 0\ns_nop 0\ns_nop 0\n"
"s_nop 0\nsgo_p1:\ns_nop 5\nbuffer_load_dwordx4 v[116:119], v20, s[8:11], s41 offen offset:1024 sc1\n"
"buffer_load_dwordx4 v[120:123], v20, s[8:11], s41 offen offset:0 sc1\nv_smfmac_f32_16x16x64_bf16 v[68:71], v[56:59], a[32:39], v8\nv_mov_b32 v84, v100\nv_smfmac_f32_16x16x64_bf16 v[72:75], v[56:59], a[88:95], v8\n"
"v_mov_b32 v85, v101\nv_smfmac_f32_16x16x64_bf16 v[76:79], v[56:59], a[144:151], v8\nv_mov_b32 v86, v102\nv_smfmac_f32_16x16x64_bf16 v[80:83], v[56:59], a[200:207], v8\n"
"v_mov_b32 v87, v103\nv_smfmac_f32_16x16x64_bf16 v[68:71], v[60:63], a[40:47], v8\nv_mov_b32 v88, v104\nv_smfmac_f32_16x16x64_bf16 v[72:75], v[60:63], a[96:103], v8\n"
"v_mov_b32 v89, v105\nv_smfmac_f32_16x16x64_bf16 v[76:79], v[60:63], a[152:159], v8\nv_mov_b32 v90, v106\nv_smfmac_f32_16x16x64_bf16 v[80:83], v[60:63], a[208:215], v8\n"
"v_mov_b32 v91, v107\nv_smfmac_f32_16x16x64_bf16 v[68:71], v[64:67], a[48:55], v8\nv_mov_b32 v92, v108\nv_smfmac_f32_16x16x64_bf16 v[72:75], v[64:67], a[104:111], v8\n"
"v_mov_b32 v93, v109\nbuffer_load_dwordx4 v[124:127], v21, s[8:11], s41 offen offset:1024 sc1\nbuffer_load_dwordx4 v[128:131], v21, s[8:11], s41 offen offset:0 sc1\nv_smfmac_f32_16x16x64_bf16 v[76:79], v[64:67], a[160:167], v8\n"
"v_mov_b32 v94, v110\nv_smfmac_f32_16x16x64_bf16 v[80:83], v[64:67], a[216:223], v8\nv_mov_b32 v95, v111\nv_smfmac_f32_16x16x64_bf16 v[68:71], v[40:43], a[0:7], v8\n"
"v_mov_b32 v96, v112\nv_smfmac_f32_16x16x64_bf16 v[72:75], v[40:43], a[56:63], v8\nv_mov_b32 v97, v113\nv_smfmac_f32_16x16x64_bf16 v[76:79], v[40:43], a[112:119], v8\n"
"v_mov_b32 v98, v114\nv_smfmac_f32_16x16x64_bf16 v[80:83], v[40:43], a[168:175], v8\nv_mov_b32 v99, v115\nv_smfmac_f32_16x16x64_bf16 v[68:71], v[44:47], a[8:15], v8\n"
"v_smfmac_f32_16x16x64_bf16 v[72:75], v[44:47], a[64:71], v8\nv_smfmac_f32_16x16x64_bf16 v[76:79], v[44:47], a[120:127], v8\nv_smfmac_f32_16x16x64_bf16 v[80:83], v[44:47], a[176:183], v8\nv_add_f32 v219, v68, v69\n"
"v_add_f32 v220, v70, v71\nv_cvt_pk_bf16_f32 v156, v219, v220\nv_add_f32 v219, v72, v73\nv_add_f32 v220, v74, v75\n"
"v_cvt_pk_bf16_f32 v157, v219, v220\nv_add_f32 v219, v76, v77\nv_add_f32 v220, v78, v79\nv_cvt_pk_bf16_f32 v158, v219, v220\n"
"v_add_f32 v219, v80, v81\nv_add_f32 v220, v82, v83\nv_cvt_pk_bf16_f32 v159, v219, v220\nbuffer_load_dwordx4 v[100:103], v9, s[16:19], s50 offen offset:2048 sc1\n"
"buffer_load_dwordx4 v[104:107], v9, s[16:19], s50 offen offset:3072 sc1\nbuffer_load_dwordx4 v[108:111], v9, s[16:19], s50 offen offset:0 sc1\nbuffer_load_dwordx4 v[112:115], v9, s[16:19], s50 offen offset:1024 sc1\ns_add_u32 s50, s50, 16384\n"
"buffer_store_dwordx4 v[156:159], v9, s[20:23], s51 offen sc1\ns_waitcnt vmcnt(8)\nv_mfma_f32_16x16x32_bf16 v[132:135], v[84:87], v[116:119], 0\nv_mfma_f32_16x16x32_bf16 v[136:139], v[88:91], v[116:119], 0\n"
"v_mfma_f32_16x16x32_bf16 v[148:151], v[176:179], v[116:119], 0\ns_waitcnt vmcnt(7)\nv_mfma_f32_16x16x32_bf16 v[140:143], v[92:95], v[120:123], 0\nv_mfma_f32_16x16x32_bf16 v[144:147], v[96:99], v[120:123], 0\n"
"v_mfma_f32_16x16x32_bf16 v[148:151], v[180:183], v[120:123], v[148:151]\nv_cndmask_b32 v132, v136, v132, s[62:63]\nv_cndmask_b32 v133, v137, v133, s[62:63]\nv_cndmask_b32 v134, v138, v134, s[62:63]\n"
"v_cndmask_b32 v135, v139, v135, s[62:63]\ns_waitcnt vmcnt(6)\nv_mfma_f32_16x16x32_f16 v[152:155], v[168:171], v[124:127], 0\ns_waitcnt vmcnt(5)\n"
"v_mfma_f32_16x16x32_f16 v[152:155], v[172:175], v[128:131], v[152:155]\nv_cndmask_b32 v140, v144, v140, s[62:63]\nv_cndmask_b32 v141, v145, v141, s[62:63]\nv_cndmask_b32 v142, v146, v142, s[62:63]\n"
"v_cndmask_b32 v143, v147, v143, s[62:63]\ns_nop 2\nv_exp_f32 v152, v152\nv_exp_f32 v153, v153\n"
"v_exp_f32 v154, v154\nv_exp_f32 v155, v155\nv_cvt_pk_bf16_f32 v200, v152, v153\nv_cvt_pk_bf16_f32 v201, v154, v155\n"
"v_and_b32 v160, v200, v223\nv_and_b32 v161, v200, v224\nv_mfma_f32_16x16x16_bf16 v[148:151], v[214:215], v[200:201], v[148:151]\nv_and_b32 v162, v201, v223\n"
"v_and_b32 v163, v201, v224\nv_mfma_f32_16x16x16_bf16 v[132:135], v[156:157], v[160:161], v[132:135]\ns_nop 0\nv_mfma_f32_16x16x16_bf16 v[140:143], v[158:159], v[162:163], v[140:143]\n"
"v_rcp_f32 v202, v148\nv_rcp_f32 v203, v149\nv_mul_f32 v132, v132, v202\nv_mul_f32 v133, v133, v202\n"
"v_mul_f32 v134, v134, v202\nv_mul_f32 v135, v135, v202\nv_fmac_f32 v132, v140, v203\nv_fmac_f32 v133, v141, v203\n"
"v_fmac_f32 v134, v142, v203\nv_fmac_f32 v135, v143, v203\nv_add_f32_dpp v204, v132, v132 row_ror:8 row_mask:0xf bank_mask:0x3\nv_add_f32_dpp v205, v133, v133 row_ror:8 row_mask:0xf bank_mask:0x3\n"
"v_add_f32_dpp v204, v134, v134 row_ror:8 row_mask:0xf bank_mask:0xc\nv_add_f32_dpp v205, v135, v135 row_ror:8 row_mask:0xf bank_mask:0xc\nv_subrev_u32 v219, 0x2f800000, v148\nv_subrev_u32 v220, 0x2f800000, v149\n"
"v_max_u32 v219, v219, v220\nv_cmp_le_u32 vcc, 0x20000000, v219\nds_write_b64 v22, v[204:205]\ns_or_b64 s[56:57], vcc, s[94:95]\n"
"s_cbranch_scc1 slow_p1\njoin_p1:\nv_mov_b32 v219, 0\ns_nop 1\n"
"v_mov_b32 v219, 0\ns_waitcnt lgkmcnt(0)\ns_barrier\nds_read_b64 v[206:207], v23 offset:0\n"
"ds_read_b64 v[208:209], v23 offset:1280\nds_read_b64 v[210:211], v23 offset:2560\nds_read_b64 v[212:213], v23 offset:3840\nds_read_b32 v221, v222\n"
"v_mov_b32 v219, 0\ns_nop 1\nv_mov_b32 v219, 0\ns_nop 1\n"
"v_mov_b32 v219, 0\ns_waitcnt lgkmcnt(3)\nv_pk_add_f32 v[206:207], v[206:207], v[208:209]\ns_waitcnt lgkmcnt(1)\n"
"v_pk_add_f32 v[210:211], v[210:211], v[212:213]\nv_pk_add_f32 v[206:207], v[206:207], v[210:211]\nv_cvt_pk_bf16_f32 v220, v206, v207\nbuffer_store_dword v220, v16, s[8:11], s41 offen sc1\n"
"s_add_u32 s51, s51, 4096\ns_add_u32 s40, s40, 1\ns_mov_b32 s43, s41\ns_add_u32 s41, s41, 131072\n"
"s_and_b32 s41, s41, 524287\ns_xor_b32 s42, s41, 262144\ns_and_b32 s47, s46, 15\ns_mul_i32 s48, s47, 8\n"
"s_add_u32 s45, s45, s48\ns_add_u32 s45, s45, s30\ns_lshr_b32 s48, s45, 4\ns_lshl_b32 s48, s48, 2\n"
"s_add_u32 s44, s44, s48\ns_and_b32 s45, s45, 15\ns_add_u32 s92, s92, 1\ns_waitcnt lgkmcnt(0)\n"
"s_cmp_eq_u32 s40, 1\ns_cbranch_scc1 one_p1\nv_readfirstlane_b32 s47, v221\ns_sub_u32 s47, s47, s40\n"
"s_cmp_eq_u32 s47, 1\ns_cbranch_scc1 rec_p1\ns_branch top_p1\none_p1:\n"
"buffer_load_dword v218, v11, s[8:11], 0 offen sc1\ns_waitcnt vmcnt(0)\nf1_p1:\ns_add_u32 s47, s40, 2\n"
"s_min_u32 s47, s47, 64\ns_add_u32 s47, s47, s33\nv_subrev_u32 v218, s47, v218\nv_cmp_lt_u32 vcc, 64, v218\n"
"s_cbranch_vccz f1ok_p1\ns_add_u32 s53, s53, 0\ns_add_u32 s49, s49, 1\ns_cmp_gt_u32 s49, 65536\n"
"s_cbranch_scc1 end_p1\ns_sleep 2\nbuffer_load_dword v218, v11, s[8:11], 0 offen sc1\ns_waitcnt vmcnt(0)\n"
"s_branch f1_p1\nf1ok_p1:\ns_branch top_p1\nlast_p1:\n"
"v_smfmac_f32_16x16x64_bf16 v[68:71], v[56:59], a[32:39], v8\nv_smfmac_f32_16x16x64_bf16 v[72:75], v[56:59], a[88:95], v8\nv_smfmac_f32_16x16x64_bf16 v[76:79], v[56:59], a[144:151], v8\nv_smfmac_f32_16x16x64_bf16 v[80:83], v[56:59], a[200:207], v8\n"
"v_smfmac_f32_16x16x64_bf16 v[68:71], v[60:63], a[40:47], v8\nv_smfmac_f32_16x16x64_bf16 v[72:75], v[60:63], a[96:103], v8\nv_smfmac_f32_16x16x64_bf16 v[76:79], v[60:63], a[152:159], v8\nv_smfmac_f32_16x16x64_bf16 v[80:83], v[60:63], a[208:215], v8\n"
"v_smfmac_f32_16x16x64_bf16 v[68:71], v[64:67], a[48:55], v8\nv_smfmac_f32_16x16x64_bf16 v[72:75], v[64:67], a[104:111], v8\nv_smfmac_f32_16x16x64_bf16 v[76:79], v[64:67], a[160:167], v8\nv_smfmac_f32_16x16x64_bf16 v[80:83], v[64:67], a[216:223], v8\n"
"v_smfmac_f32_16x16x64_bf16 v[68:71], v[40:43], a[0:7], v8\nv_smfmac_f32_16x16x64_bf16 v[72:75], v[40:43], a[56:63], v8\nv_smfmac_f32_16x16x64_bf16 v[76:79], v[40:43], a[112:119], v8\nv_smfmac_f32_16x16x64_bf16 v[80:83], v[40:43], a[168:175], v8\n"
"v_smfmac_f32_16x16x64_bf16 v[68:71], v[44:47], a[8:15], v8\nv_smfmac_f32_16x16x64_bf16 v[72:75], v[44:47], a[64:71], v8\nv_smfmac_f32_16x16x64_bf16 v[76:79], v[44:47], a[120:127], v8\nv_smfmac_f32_16x16x64_bf16 v[80:83], v[44:47], a[176:183], v8\n"
"v_add_f32 v219, v68, v69\nv_add_f32 v220, v70, v71\nv_cvt_pk_bf16_f32 v156, v219, v220\nv_add_f32 v219, v72, v73\n"
"v_add_f32 v220, v74, v75\nv_cvt_pk_bf16_f32 v157, v219, v220\nv_add_f32 v219, v76, v77\nv_add_f32 v220, v78, v79\n"
"v_cvt_pk_bf16_f32 v158, v219, v220\nv_add_f32 v219, v80, v81\nv_add_f32 v220, v82, v83\nv_cvt_pk_bf16_f32 v159, v219, v220\n"
"buffer_store_dwordx4 v[156:159], v9, s[20:23], s51 offen sc1\ns_add_u32 s47, s33, 64\nv_mov_b32 v219, s47\ns_waitcnt vmcnt(0)\n"
"buffer_store_dword v219, v18, s[8:11], 0 offen sc1\ns_branch end_p1\nretry1_p1:\ns_add_u32 s53, s53, 256\n"
"s_add_u32 s49, s49, 1\ns_cmp_gt_u32 s49, 65536\ns_cbranch_scc1 end_p1\ns_add_u32 s47, s40, 1\n"
"v_mov_b32 v221, s47\nds_write_b32 v222, v221\nrec_p1:\ns_sleep 4\n"
"v_mov_b32 v68, 0\nv_mov_b32 v69, 0\nv_mov_b32 v70, 0\nv_mov_b32 v71, 0\n"
"v_mov_b32 v72, 0\nv_mov_b32 v73, 0\nv_mov_b32 v74, 0\nv_mov_b32 v75, 0\n"
"v_mov_b32 v76, 0\nv_mov_b32 v77, 0\nv_mov_b32 v78, 0\nv_mov_b32 v79, 0\n"
"v_mov_b32 v80, 0\nv_mov_b32 v81, 0\nv_mov_b32 v82, 0\nv_mov_b32 v83, 0\n"
"v_mov_b32 v218, 0\ns_branch top2_p1\nadopt_p1:\nbuffer_load_dwordx4 v[164:167], v28, s[8:11], 0 offen sc1\n"
"s_lshl_b32 s52, s40, 26\ns_or_b32 s47, s52, 0x3ffffff\ns_waitcnt vmcnt(0)\nv_cmp_ge_u32 vcc, s47, v164\n"
"s_nop 1\nv_cndmask_b32 v164, 0, v164, vcc\nv_cmp_ge_u32 vcc, s47, v165\ns_nop 1\n"
"v_cndmask_b32 v165, 0, v165, vcc\nv_cmp_ge_u32 vcc, s47, v166\ns_nop 1\nv_cndmask_b32 v166, 0, v166, vcc\n"
"v_cmp_ge_u32 vcc, s47, v167\ns_nop 1\nv_cndmask_b32 v167, 0, v167, vcc\nv_max3_u32 v221, v164, v165, v166\n"
"v_max_u32 v221, v221, v167\ns_nop 1\nv_max_u32_dpp v221, v221, v221 row_ror:8 row_mask:0xf bank_mask:0xf\ns_nop 1\n"
"v_max_u32_dpp v221, v221, v221 row_ror:4 row_mask:0xf bank_mask:0xf\ns_nop 1\nv_max_u32_dpp v221, v221, v221 row_ror:2 row_mask:0xf bank_mask:0xf\ns_nop 1\n"
"v_max_u32_dpp v221, v221, v221 row_ror:1 row_mask:0xf bank_mask:0xf\ns_nop 0\nv_readlane_b32 s47, v221, 0\nv_readlane_b32 s48, v221, 16\n"
"s_max_u32 s48, s48, s47\ns_cmp_le_u32 s48, s52\ns_cbranch_scc1 back2_p1\ns_bfe_u32 s52, s48, 1179656\n"
"s_add_u32 s44, s52, s28\ns_mov_b32 s45, 0\ns_and_b32 s46, s48, 255\ns_mov_b32 s92, 0\n"
"s_branch back2_p1\nback2_p1:\ns_and_b32 s47, s46, 15\ns_mul_i32 s47, s47, 4\n"
"s_add_u32 s47, s47, s29\ns_add_u32 s47, s47, s45\ns_and_b32 s91, s47, 15\ns_lshr_b32 s47, s47, 4\n"
"s_lshl_b32 s47, s47, 2\ns_add_u32 s90, s44, s47\ns_mul_i32 s48, s91, 6\ns_and_b32 s48, s48, -4\n"
"s_getpc_b64 s[60:61]\nspc2_p1:\ns_sub_u32 s48, sgo_p1-spc2_p1, s48\ns_add_u32 s60, s60, s48\n"
"s_addc_u32 s61, s61, -1\ns_branch back_p1\nlate_p1:\ns_add_u32 s53, s53, 16777216\n"
"s_mov_b64 s[94:95], 1\ns_add_u32 s52, s90, 20\nlw_p1:\ns_memrealtime s[54:55]\n"
"s_waitcnt lgkmcnt(0)\ns_sub_u32 s47, s54, s52\ns_cmp_lt_i32 s47, 0\ns_cbranch_scc1 lw_p1\n"
"s_branch sgo_p1\nslow_p1:\ns_mov_b64 s[94:95], 0\ns_cbranch_vccnz slow2_p1\n"
"s_memrealtime s[54:55]\ns_add_u32 s52, s40, 1\ns_lshl_b32 s52, s52, 26\ns_waitcnt lgkmcnt(0)\n"
"s_sub_u32 s47, s54, s28\ns_add_u32 s47, s47, s31\ns_min_u32 s47, s47, 0x3ffff\ns_lshl_b32 s47, s47, 8\n"
"s_or_b32 s47, s47, s52\ns_sub_u32 s52, s92, 1\ns_cmp_lt_u32 s52, 6\ns_cselect_b32 s48, 1, 0\n"
"s_cmp_ge_u32 s40, 5\ns_cselect_b32 s48, s48, 0\ns_add_u32 s48, s48, s46\ns_min_u32 s48, s48, 15\n"
"s_or_b32 s47, s47, s48\nv_mov_b32 v225, s47\nbuffer_store_dword v225, v19, s[8:11], 0 offen sc1\ns_add_u32 s47, s40, 2\n"
"v_mov_b32 v221, s47\nds_write_b32 v222, v221\ns_branch join_p1\nslow2_p1:\n"
"v_max3_u32 v218, v124, v125, v126\nv_max_u32 v218, v218, v127\nv_max3_u32 v218, v218, v128, v129\nv_max3_u32 v218, v218, v130, v131\n"
"v_max3_u32 v218, v218, v116, v117\nv_max3_u32 v218, v218, v118, v119\nv_max3_u32 v218, v218, v120, v121\nv_max3_u32 v218, v218, v122, v123\n"
"v_cmp_eq_u32 vcc, -1, v218\ns_cbranch_vccz exact_p1\ns_branch spin_p1\nspin2_p1:\n"
"s_sleep 3\nspin_p1:\ns_add_u32 s53, s53, 1\ns_add_u32 s49, s49, 1\n"
"s_cmp_gt_u32 s49, 65536\ns_cbranch_scc1 end_p1\nbuffer_load_dwordx4 v[116:119], v20, s[8:11], s41 offen offset:1024 sc1\nbuffer_load_dwordx4 v[120:123], v20, s[8:11], s41 offen offset:0 sc1\n"
"buffer_load_dwordx4 v[124:127], v21, s[8:11], s41 offen offset:1024 sc1\nbuffer_load_dwordx4 v[128:131], v21, s[8:11], s41 offen offset:0 sc1\ns_waitcnt vmcnt(0)\nv_max3_u32 v218, v124, v125, v126\n"
"v_max_u32 v218, v218, v127\nv_max3_u32 v218, v218, v128, v129\nv_max3_u32 v218, v218, v130, v131\nv_max3_u32 v218, v218, v116, v117\n"
"v_max3_u32 v218, v218, v118, v119\nv_max3_u32 v218, v218, v120, v121\nv_max3_u32 v218, v218, v122, v123\nv_cmp_eq_u32 vcc, -1, v218\n"
"s_cbranch_vccnz spin2_p1\ns_memrealtime s[54:55]\ns_add_u32 s52, s40, 1\ns_lshl_b32 s52, s52, 26\n"
"s_waitcnt lgkmcnt(0)\ns_sub_u32 s47, s54, s28\ns_add_u32 s47, s47, s38\ns_min_u32 s47, s47, 0x3ffff\n"
"s_lshl_b32 s47, s47, 8\ns_or_b32 s47, s47, s52\ns_sub_u32 s52, s92, 1\ns_cmp_lt_u32 s52, 6\n"
"s_cselect_b32 s48, 1, 0\ns_cmp_ge_u32 s40, 5\ns_cselect_b32 s48, s48, 0\ns_add_u32 s48, s48, s46\n"
"s_min_u32 s48, s48, 15\ns_or_b32 s47, s47, s48\nv_mov_b32 v225, s47\nbuffer_store_dword v225, v19, s[8:11], 0 offen sc1\n"
"s_add_u32 s47, s40, 2\nv_mov_b32 v221, s47\nds_write_b32 v222, v221\nv_mfma_f32_16x16x32_bf16 v[132:135], v[84:87], v[116:119], 0\n"
"v_mfma_f32_16x16x32_bf16 v[136:139], v[88:91], v[116:119], 0\nv_mfma_f32_16x16x32_bf16 v[148:151], v[176:179], v[116:119], 0\nv_mfma_f32_16x16x32_bf16 v[140:143], v[92:95], v[120:123], 0\nv_mfma_f32_16x16x32_bf16 v[144:147], v[96:99], v[120:123], 0\n"
"v_mfma_f32_16x16x32_bf16 v[148:151], v[180:183], v[120:123], v[148:151]\nv_cndmask_b32 v132, v136, v132, s[62:63]\nv_cndmask_b32 v133, v137, v133, s[62:63]\nv_cndmask_b32 v134, v138, v134, s[62:63]\n"
"v_cndmask_b32 v135, v139, v135, s[62:63]\nv_mfma_f32_16x16x32_f16 v[152:155], v[168:171], v[124:127], 0\nv_mfma_f32_16x16x32_f16 v[152:155], v[172:175], v[128:131], v[152:155]\nv_cndmask_b32 v140, v144, v140, s[62:63]\n"
"v_cndmask_b32 v141, v145, v141, s[62:63]\nv_cndmask_b32 v142, v146, v142, s[62:63]\nv_cndmask_b32 v143, v147, v143, s[62:63]\ns_nop 2\n"
"v_exp_f32 v152, v152\nv_exp_f32 v153, v153\nv_exp_f32 v154, v154\nv_exp_f32 v155, v155\n"
"v_cvt_pk_bf16_f32 v200, v152, v153\nv_cvt_pk_bf16_f32 v201, v154, v155\nv_and_b32 v160, v200, v223\nv_and_b32 v161, v200, v224\n"
"v_mfma_f32_16x16x16_bf16 v[148:151], v[214:215], v[200:201], v[148:151]\nv_and_b32 v162, v201, v223\nv_and_b32 v163, v201, v224\nv_mfma_f32_16x16x16_bf16 v[132:135], v[156:157], v[160:161], v[132:135]\n"
"s_nop 0\nv_mfma_f32_16x16x16_bf16 v[140:143], v[158:159], v[162:163], v[140:143]\nv_rcp_f32 v202, v148\nv_rcp_f32 v203, v149\n"
"v_mul_f32 v132, v132, v202\nv_mul_f32 v133, v133, v202\nv_mul_f32 v134, v134, v202\nv_mul_f32 v135, v135, v202\n"
"v_fmac_f32 v132, v140, v203\nv_fmac_f32 v133, v141, v203\nv_fmac_f32 v134, v142, v203\nv_fmac_f32 v135, v143, v203\n"
"v_add_f32_dpp v204, v132, v132 row_ror:8 row_mask:0xf bank_mask:0x3\nv_add_f32_dpp v205, v133, v133 row_ror:8 row_mask:0xf bank_mask:0x3\nv_add_f32_dpp v204, v134, v134 row_ror:8 row_mask:0xf bank_mask:0xc\nv_add_f32_dpp v205, v135, v135 row_ror:8 row_mask:0xf bank_mask:0xc\n"
"v_subrev_u32 v219, 0x2f800000, v148\nv_subrev_u32 v220, 0x2f800000, v149\nv_max_u32 v219, v219, v220\nv_cmp_le_u32 vcc, 0x20000000, v219\n"
"ds_write_b64 v22, v[204:205]\ns_cbranch_vccz join_p1\nexact_p1:\ns_add_u32 s53, s53, 65536\n"
"ex0_p1:\ns_add_u32 s47, s41, 4096\nbuffer_load_dwordx2 v[184:185], v24, s[8:11], s47 offen offset:0 sc1\nbuffer_load_dwordx2 v[186:187], v24, s[8:11], s47 offen offset:512 sc1\n"
"buffer_load_dwordx2 v[188:189], v24, s[8:11], s47 offen offset:1024 sc1\nbuffer_load_dwordx2 v[190:191], v24, s[8:11], s47 offen offset:1536 sc1\nbuffer_load_dwordx4 v[192:195], v25, s[8:11], s47 offen sc1\nbuffer_load_dwordx4 v[196:199], v25, s[8:11], s47 offen offset:16 sc1\n"
"v_mov_b32 v218, 0\ns_waitcnt vmcnt(0)\nv_max3_u32 v218, v218, v184, v185\nv_max3_u32 v218, v218, v186, v187\n"
"v_max3_u32 v218, v218, v188, v189\nv_max3_u32 v218, v218, v190, v191\nv_max3_u32 v218, v218, v192, v193\nv_max3_u32 v218, v218, v194, v195\n"
"v_max3_u32 v218, v218, v196, v197\nv_max3_u32 v218, v218, v198, v199\nv_cmp_eq_u32 vcc, -1, v218\ns_cbranch_vccz exok0_p1\n"
"s_add_u32 s53, s53, 0\ns_add_u32 s49, s49, 1\ns_cmp_gt_u32 s49, 65536\ns_cbranch_scc1 end_p1\n"
"s_branch ex0_p1\nexok0_p1:\nv_add_f32 v184, v184, v186\nv_add_f32 v188, v188, v190\n"
"v_add_f32 v184, v184, v188\nv_add_f32 v185, v185, v187\nv_add_f32 v189, v189, v191\nv_add_f32 v185, v185, v189\n"
"v_max_f32 v225, v184, v185\nv_max3_f32 v225, v225, v192, v193\nv_max3_f32 v225, v225, v194, v195\nv_max3_f32 v225, v225, v196, v197\n"
"v_max3_f32 v225, v225, v198, v199\nv_mov_b32 v219, v225\ns_nop 1\nv_permlane16_swap_b32 v225, v219\n"
"v_max_f32 v225, v225, v219\nv_mov_b32 v219, v225\ns_nop 1\nv_permlane32_swap_b32 v225, v219\n"
"v_max_f32 v225, v225, v219\nv_sub_f32 v184, v184, v225\nv_exp_f32 v152, v184\nv_sub_f32 v185, v185, v225\n"
"v_exp_f32 v153, v185\nv_sub_f32 v192, v192, v225\nv_exp_f32 v192, v192\nv_sub_f32 v193, v193, v225\n"
"v_exp_f32 v193, v193\nv_sub_f32 v194, v194, v225\nv_exp_f32 v194, v194\nv_sub_f32 v195, v195, v225\n"
"v_exp_f32 v195, v195\nv_sub_f32 v196, v196, v225\nv_exp_f32 v196, v196\nv_sub_f32 v197, v197, v225\n"
"v_exp_f32 v197, v197\nv_sub_f32 v198, v198, v225\nv_exp_f32 v198, v198\nv_sub_f32 v199, v199, v225\n"
"v_exp_f32 v199, v199\ns_nop 0\nv_cvt_pk_bf16_f32 v116, v192, v193\nv_cvt_pk_bf16_f32 v117, v194, v195\n"
"v_cvt_pk_bf16_f32 v118, v196, v197\nv_cvt_pk_bf16_f32 v119, v198, v199\nex1_p1:\ns_add_u32 s47, s41, 0\n"
"buffer_load_dwordx2 v[184:185], v24, s[8:11], s47 offen offset:0 sc1\nbuffer_load_dwordx2 v[186:187], v24, s[8:11], s47 offen offset:512 sc1\nbuffer_load_dwordx2 v[188:189], v24, s[8:11], s47 offen offset:1024 sc1\nbuffer_load_dwordx2 v[190:191], v24, s[8:11], s47 offen offset:1536 sc1\n"
"buffer_load_dwordx4 v[192:195], v25, s[8:11], s47 offen sc1\nbuffer_load_dwordx4 v[196:199], v25, s[8:11], s47 offen offset:16 sc1\nv_mov_b32 v218, 0\ns_waitcnt vmcnt(0)\n"
"v_max3_u32 v218, v218, v184, v185\nv_max3_u32 v218, v218, v186, v187\nv_max3_u32 v218, v218, v188, v189\nv_max3_u32 v218, v218, v190, v191\n"
"v_max3_u32 v218, v218, v192, v193\nv_max3_u32 v218, v218, v194, v195\nv_max3_u32 v218, v218, v196, v197\nv_max3_u32 v218, v218, v198, v199\n"
"v_cmp_eq_u32 vcc, -1, v218\ns_cbranch_vccz exok1_p1\ns_add_u32 s53, s53, 0\ns_add_u32 s49, s49, 1\n"
"s_cmp_gt_u32 s49, 65536\ns_cbranch_scc1 end_p1\ns_branch ex1_p1\nexok1_p1:\n"
"v_add_f32 v184, v184, v186\nv_add_f32 v188, v188, v190\nv_add_f32 v184, v184, v188\nv_add_f32 v185, v185, v187\n"
"v_add_f32 v189, v189, v191\nv_add_f32 v185, v185, v189\nv_max_f32 v225, v184, v185\nv_max3_f32 v225, v225, v192, v193\n"
"v_max3_f32 v225, v225, v194, v195\nv_max3_f32 v225, v225, v196, v197\nv_max3_f32 v225, v225, v198, v199\nv_mov_b32 v219, v225\n"
"s_nop 1\nv_permlane16_swap_b32 v225, v219\nv_max_f32 v225, v225, v219\nv_mov_b32 v219, v225\n"
"s_nop 1\nv_permlane32_swap_b32 v225, v219\nv_max_f32 v225, v225, v219\nv_sub_f32 v184, v184, v225\n"
"v_exp_f32 v154, v184\nv_sub_f32 v185, v185, v225\nv_exp_f32 v155, v185\nv_sub_f32 v192, v192, v225\n"
"v_exp_f32 v192, v192\nv_sub_f32 v193, v193, v225\nv_exp_f32 v193, v193\nv_sub_f32 v194, v194, v225\n"
"v_exp_f32 v194, v194\nv_sub_f32 v195, v195, v225\nv_exp_f32 v195, v195\nv_sub_f32 v196, v196, v225\n"
"v_exp_f32 v196, v196\nv_sub_f32 v197, v197, v225\nv_exp_f32 v197, v197\nv_sub_f32 v198, v198, v225\n"
"v_exp_f32 v198, v198\nv_sub_f32 v199, v199, v225\nv_exp_f32 v199, v199\ns_nop 0\n"
"v_cvt_pk_bf16_f32 v120, v192, v193\nv_cvt_pk_bf16_f32 v121, v194, v195\nv_cvt_pk_bf16_f32 v122, v196, v197\nv_cvt_pk_bf16_f32 v123, v198, v199\n"
"s_memrealtime s[54:55]\ns_add_u32 s52, s40, 1\ns_lshl_b32 s52, s52, 26\ns_waitcnt lgkmcnt(0)\n"
"s_sub_u32 s47, s54, s28\ns_add_u32 s47, s47, s38\ns_min_u32 s47, s47, 0x3ffff\ns_lshl_b32 s47, s47, 8\n"
"s_or_b32 s47, s47, s52\ns_sub_u32 s52, s92, 1\ns_cmp_lt_u32 s52, 6\ns_cselect_b32 s48, 1, 0\n"
"s_cmp_ge_u32 s40, 5\ns_cselect_b32 s48, s48, 0\ns_add_u32 s48, s48, s46\ns_min_u32 s48, s48, 15\n"
"s_or_b32 s47, s47, s48\nv_mov_b32 v225, s47\nbuffer_store_dword v225, v19, s[8:11], 0 offen sc1\ns_add_u32 s47, s40, 2\n"
"v_mov_b32 v221, s47\nds_write_b32 v222, v221\nv_mfma_f32_16x16x32_bf16 v[132:135], v[84:87], v[116:119], 0\nv_mfma_f32_16x16x32_bf16 v[136:139], v[88:91], v[116:119], 0\n"
"v_mfma_f32_16x16x32_bf16 v[148:151], v[176:179], v[116:119], 0\nv_mfma_f32_16x16x32_bf16 v[140:143], v[92:95], v[120:123], 0\nv_mfma_f32_16x16x32_bf16 v[144:147], v[96:99], v[120:123], 0\nv_mfma_f32_16x16x32_bf16 v[148:151], v[180:183], v[120:123], v[148:151]\n"
"v_cndmask_b32 v132, v136, v132, s[62:63]\nv_cndmask_b32 v133, v137, v133, s[62:63]\nv_cndmask_b32 v134, v138, v134, s[62:63]\nv_cndmask_b32 v135, v139, v135, s[62:63]\n"
"s_nop 0\nv_cndmask_b32 v140, v144, v140, s[62:63]\nv_cndmask_b32 v141, v145, v141, s[62:63]\nv_cndmask_b32 v142, v146, v142, s[62:63]\n"
"v_cndmask_b32 v143, v147, v143, s[62:63]\nv_cvt_pk_bf16_f32 v200, v152, v153\nv_cvt_pk_bf16_f32 v201, v154, v155\nv_and_b32 v160, v200, v223\n"
"v_and_b32 v161, v200, v224\nv_mfma_f32_16x16x16_bf16 v[148:151], v[214:215], v[200:201], v[148:151]\nv_and_b32 v162, v201, v223\nv_and_b32 v163, v201, v224\n"
"v_mfma_f32_16x16x16_bf16 v[132:135], v[156:157], v[160:161], v[132:135]\ns_nop 0\nv_mfma_f32_16x16x16_bf16 v[140:143], v[158:159], v[162:163], v[140:143]\nv_rcp_f32 v202, v148\n"
"v_rcp_f32 v203, v149\nv_mul_f32 v132, v132, v202\nv_mul_f32 v133, v133, v202\nv_mul_f32 v134, v134, v202\n"
"v_mul_f32 v135, v135, v202\nv_fmac_f32 v132, v140, v203\nv_fmac_f32 v133, v141, v203\nv_fmac_f32 v134, v142, v203\n"
"v_fmac_f32 v135, v143, v203\nv_add_f32_dpp v204, v132, v132 row_ror:8 row_mask:0xf bank_mask:0x3\nv_add_f32_dpp v205, v133, v133 row_ror:8 row_mask:0xf bank_mask:0x3\nv_add_f32_dpp v204, v134, v134 row_ror:8 row_mask:0xf bank_mask:0xc\n"
"v_add_f32_dpp v205, v135, v135 row_ror:8 row_mask:0xf bank_mask:0xc\nds_write_b64 v22, v[204:205]\ns_branch join_p1\nend_p1:\n"
"s_branch fk_done\nfk_p2:\ns_mov_b32 s40, 0\ns_mov_b32 s41, 0\n"
"s_mov_b32 s42, 262144\ns_mov_b32 s43, 393216\ns_mov_b32 s44, s28\ns_mov_b32 s45, 0\n"
"s_mov_b32 s46, 0\ns_mov_b32 s92, 0\ns_mov_b32 s49, 0\ns_mov_b32 s53, 0\n"
"s_mov_b32 s50, s35\ns_mov_b32 s51, s36\nv_mov_b32 v222, 24576\ns_mov_b32 s62, 0x00ff00ff\n"
"s_mov_b32 s63, 0x00ff00ff\nv_cndmask_b32 v223, 0, -1, s[62:63]\nv_not_b32 v224, v223\nv_bfe_u32 v219, v9, 4, 1\n"
"v_lshlrev_b32 v220, 4, v219\nv_mov_b32 v218, 0x3c00\nv_lshlrev_b32 v218, v220, v218\nv_bfe_u32 v220, v9, 5, 3\n"
"v_cmp_eq_u32 vcc, 0, v220\ns_nop 1\nv_cndmask_b32 v168, 0, v218, vcc\nv_cmp_eq_u32 vcc, 2, v220\n"
"s_nop 1\nv_cndmask_b32 v169, 0, v218, vcc\nv_cmp_eq_u32 vcc, 4, v220\ns_nop 1\n"
"v_cndmask_b32 v170, 0, v218, vcc\nv_cmp_eq_u32 vcc, 6, v220\ns_nop 1\nv_cndmask_b32 v171, 0, v218, vcc\n"
"v_cmp_eq_u32 vcc, 1, v220\ns_nop 1\nv_cndmask_b32 v172, 0, v218, vcc\nv_cmp_eq_u32 vcc, 3, v220\n"
"s_nop 1\nv_cndmask_b32 v173, 0, v218, vcc\nv_cmp_eq_u32 vcc, 5, v220\ns_nop 1\n"
"v_cndmask_b32 v174, 0, v218, vcc\nv_cmp_eq_u32 vcc, 7, v220\ns_nop 1\nv_cndmask_b32 v175, 0, v218, vcc\n"
"v_mov_b32 v218, 0x3f803f80\nv_cmp_eq_u32 vcc, 0, v219\ns_nop 1\nv_cndmask_b32 v176, 0, v218, vcc\n"
"v_cndmask_b32 v177, 0, v218, vcc\nv_cndmask_b32 v178, 0, v218, vcc\nv_cndmask_b32 v179, 0, v218, vcc\nv_cndmask_b32 v214, 0, v218, vcc\n"
"v_cmp_eq_u32 vcc, 1, v219\ns_nop 1\nv_cndmask_b32 v180, 0, v218, vcc\nv_cndmask_b32 v181, 0, v218, vcc\n"
"v_cndmask_b32 v182, 0, v218, vcc\nv_cndmask_b32 v183, 0, v218, vcc\nv_cndmask_b32 v215, 0, v218, vcc\ns_add_u32 s47, s34, 0\n"
"buffer_load_dwordx4 a[0:3], v9, s[12:15], s47 offen sc1\nbuffer_load_dwordx4 a[4:7], v9, s[12:15], s47 offen offset:1024 sc1\ns_add_u32 s47, s34, 2048\nbuffer_load_dwordx4 a[8:11], v9, s[12:15], s47 offen sc1\n"
"buffer_load_dwordx4 a[12:15], v9, s[12:15], s47 offen offset:1024 sc1\ns_add_u32 s47, s34, 4096\nbuffer_load_dwordx4 a[16:19], v9, s[12:15], s47 offen sc1\nbuffer_load_dwordx4 a[20:23], v9, s[12:15], s47 offen offset:1024 sc1\n"
"s_add_u32 s47, s34, 6144\nbuffer_load_dwordx4 a[24:27], v9, s[12:15], s47 offen sc1\nbuffer_load_dwordx4 a[28:31], v9, s[12:15], s47 offen offset:1024 sc1\ns_add_u32 s47, s34, 8192\n"
"buffer_load_dwordx4 a[32:35], v9, s[12:15], s47 offen sc1\nbuffer_load_dwordx4 a[36:39], v9, s[12:15], s47 offen offset:1024 sc1\ns_add_u32 s47, s34, 10240\nbuffer_load_dwordx4 a[40:43], v9, s[12:15], s47 offen sc1\n"
"buffer_load_dwordx4 a[44:47], v9, s[12:15], s47 offen offset:1024 sc1\ns_add_u32 s47, s34, 12288\nbuffer_load_dwordx4 a[48:51], v9, s[12:15], s47 offen sc1\nbuffer_load_dwordx4 a[52:55], v9, s[12:15], s47 offen offset:1024 sc1\n"
"s_add_u32 s47, s34, 14336\nbuffer_load_dwordx4 a[56:59], v9, s[12:15], s47 offen sc1\nbuffer_load_dwordx4 a[60:63], v9, s[12:15], s47 offen offset:1024 sc1\ns_add_u32 s47, s34, 16384\n"
"buffer_load_dwordx4 a[64:67], v9, s[12:15], s47 offen sc1\nbuffer_load_dwordx4 a[68:71], v9, s[12:15], s47 offen offset:1024 sc1\ns_add_u32 s47, s34, 18432\nbuffer_load_dwordx4 a[72:75], v9, s[12:15], s47 offen sc1\n"
"buffer_load_dwordx4 a[76:79], v9, s[12:15], s47 offen offset:1024 sc1\ns_add_u32 s47, s34, 20480\nbuffer_load_dwordx4 a[80:83], v9, s[12:15], s47 offen sc1\nbuffer_load_dwordx4 a[84:87], v9, s[12:15], s47 offen offset:1024 sc1\n"
"s_add_u32 s47, s34, 22528\nbuffer_load_dwordx4 a[88:91], v9, s[12:15], s47 offen sc1\nbuffer_load_dwordx4 a[92:95], v9, s[12:15], s47 offen offset:1024 sc1\ns_add_u32 s47, s34, 24576\n"
"buffer_load_dwordx4 a[96:99], v9, s[12:15], s47 offen sc1\nbuffer_load_dwordx4 a[100:103], v9, s[12:15], s47 offen offset:1024 sc1\ns_add_u32 s47, s34, 26624\nbuffer_load_dwordx4 a[104:107], v9, s[12:15], s47 offen sc1\n"
"buffer_load_dwordx4 a[108:111], v9, s[12:15], s47 offen offset:1024 sc1\ns_add_u32 s47, s34, 28672\nbuffer_load_dwordx4 a[112:115], v9, s[12:15], s47 offen sc1\nbuffer_load_dwordx4 a[116:119], v9, s[12:15], s47 offen offset:1024 sc1\n"
"s_add_u32 s47, s34, 30720\nbuffer_load_dwordx4 a[120:123], v9, s[12:15], s47 offen sc1\nbuffer_load_dwordx4 a[124:127], v9, s[12:15], s47 offen offset:1024 sc1\ns_add_u32 s47, s34, 32768\n"
"buffer_load_dwordx4 a[128:131], v9, s[12:15], s47 offen sc1\nbuffer_load_dwordx4 a[132:135], v9, s[12:15], s47 offen offset:1024 sc1\ns_add_u32 s47, s34, 34816\nbuffer_load_dwordx4 a[136:139], v9, s[12:15], s47 offen sc1\n"
"buffer_load_dwordx4 a[140:143], v9, s[12:15], s47 offen offset:1024 sc1\ns_add_u32 s47, s34, 36864\nbuffer_load_dwordx4 a[144:147], v9, s[12:15], s47 offen sc1\nbuffer_load_dwordx4 a[148:151], v9, s[12:15], s47 offen offset:1024 sc1\n"
"s_add_u32 s47, s34, 38912\nbuffer_load_dwordx4 a[152:155], v9, s[12:15], s47 offen sc1\nbuffer_load_dwordx4 a[156:159], v9, s[12:15], s47 offen offset:1024 sc1\ns_add_u32 s47, s34, 40960\n"
"buffer_load_dwordx4 a[160:163], v9, s[12:15], s47 offen sc1\nbuffer_load_dwordx4 a[164:167], v9, s[12:15], s47 offen offset:1024 sc1\ns_add_u32 s47, s34, 43008\nbuffer_load_dwordx4 a[168:171], v9, s[12:15], s47 offen sc1\n"
"buffer_load_dwordx4 a[172:175], v9, s[12:15], s47 offen offset:1024 sc1\ns_add_u32 s47, s34, 45056\nbuffer_load_dwordx4 a[176:179], v9, s[12:15], s47 offen sc1\nbuffer_load_dwordx4 a[180:183], v9, s[12:15], s47 offen offset:1024 sc1\n"
"s_add_u32 s47, s34, 47104\nbuffer_load_dwordx4 a[184:187], v9, s[12:15], s47 offen sc1\nbuffer_load_dwordx4 a[188:191], v9, s[12:15], s47 offen offset:1024 sc1\ns_add_u32 s47, s34, 49152\n"
"buffer_load_dwordx4 a[192:195], v9, s[12:15], s47 offen sc1\nbuffer_load_dwordx4 a[196:199], v9, s[12:15], s47 offen offset:1024 sc1\ns_add_u32 s47, s34, 51200\nbuffer_load_dwordx4 a[200:203], v9, s[12:15], s47 offen sc1\n"
"buffer_load_dwordx4 a[204:207], v9, s[12:15], s47 offen offset:1024 sc1\ns_add_u32 s47, s34, 53248\nbuffer_load_dwordx4 a[208:211], v9, s[12:15], s47 offen sc1\nbuffer_load_dwordx4 a[212:215], v9, s[12:15], s47 offen offset:1024 sc1\n"
"s_add_u32 s47, s34, 55296\nbuffer_load_dwordx4 a[216:219], v9, s[12:15], s47 offen sc1\nbuffer_load_dwordx4 a[220:223], v9, s[12:15], s47 offen offset:1024 sc1\nv_lshlrev_b32 v166, 2, v9\n"
"s_mov_b32 s52, 1064960\nbuffer_load_dword v164, v166, s[8:11], s52 offen sc1\ns_add_u32 s52, s52, 4096\nbuffer_load_dword v165, v166, s[8:11], s52 offen sc1\n"
"buffer_load_dword v218, v11, s[8:11], 0 offen sc1\ns_mov_b64 s[94:95], 1\ns_barrier\ns_waitcnt vmcnt(0)\n"
"ar_p2:\nv_xor_b32 v164, s33, v164\nv_xor_b32 v165, s33, v165\nv_or_b32 v164, v164, v165\n"
"v_cmp_ne_u32 vcc, 0, v164\ns_cbranch_vccz arok_p2\ns_add_u32 s53, s53, 0\ns_add_u32 s49, s49, 1\n"
"s_cmp_gt_u32 s49, 65536\ns_cbranch_scc1 end_p2\ns_sleep 2\nv_lshlrev_b32 v166, 2, v9\n"
"s_mov_b32 s52, 1064960\nbuffer_load_dword v164, v166, s[8:11], s52 offen sc1\ns_add_u32 s52, s52, 4096\nbuffer_load_dword v165, v166, s[8:11], s52 offen sc1\n"
"s_waitcnt vmcnt(0)\ns_branch ar_p2\narok_p2:\nfl_p2:\n"
"s_add_u32 s47, s40, 2\ns_min_u32 s47, s47, 64\ns_add_u32 s47, s47, s33\nv_subrev_u32 v218, s47, v218\n"
"v_cmp_lt_u32 vcc, 64, v218\ns_cbranch_vccz flok_p2\ns_add_u32 s53, s53, 0\ns_add_u32 s49, s49, 1\n"
"s_cmp_gt_u32 s49, 65536\ns_cbranch_scc1 end_p2\ns_sleep 2\nbuffer_load_dword v218, v11, s[8:11], 0 offen sc1\n"
"s_waitcnt vmcnt(0)\ns_branch fl_p2\nflok_p2:\nbuffer_load_dwordx4 v[100:103], v9, s[16:19], s50 offen offset:0 sc1\n"
"buffer_load_dwordx4 v[104:107], v9, s[16:19], s50 offen offset:1024 sc1\nbuffer_load_dwordx4 v[108:111], v9, s[16:19], s50 offen offset:2048 sc1\nbuffer_load_dwordx4 v[112:115], v9, s[16:19], s50 offen offset:3072 sc1\ns_add_u32 s50, s50, 16384\n"
"s_memrealtime s[54:55]\ns_waitcnt lgkmcnt(0)\ns_add_u32 s44, s54, 12\ns_mov_b32 s45, 8\n"
".p2align 6\ntop_p2:\nv_mov_b32 v68, 0\nv_mov_b32 v69, 0\n"
"v_mov_b32 v70, 0\nv_mov_b32 v71, 0\nv_mov_b32 v72, 0\nv_mov_b32 v73, 0\n"
"v_mov_b32 v74, 0\nv_mov_b32 v75, 0\nv_mov_b32 v76, 0\nv_mov_b32 v77, 0\n"
"v_mov_b32 v78, 0\nv_mov_b32 v79, 0\nv_mov_b32 v80, 0\nv_mov_b32 v81, 0\n"
"v_mov_b32 v82, 0\nv_mov_b32 v83, 0\nv_mov_b32 v218, 0\ns_add_u32 s52, s44, s37\n"
"m_p2:\ns_memrealtime s[54:55]\ns_waitcnt lgkmcnt(0)\ns_sub_u32 s47, s54, s52\n"
"s_cmp_lt_i32 s47, 0\ns_cbranch_scc1 m_p2\ntop2_p2:\nbuffer_load_dwordx4 v[56:59], v10, s[8:11], s43 offen offset:0 sc1\n"
"buffer_load_dwordx4 v[60:63], v10, s[8:11], s43 offen offset:1024 sc1\ns_and_b32 s47, s46, 15\ns_mul_i32 s47, s47, 4\ns_add_u32 s47, s47, s29\n"
"s_add_u32 s47, s47, s45\ns_and_b32 s91, s47, 15\ns_lshr_b32 s47, s47, 4\ns_lshl_b32 s47, s47, 2\n"
"s_add_u32 s90, s44, s47\ns_mul_i32 s48, s91, 6\ns_and_b32 s48, s48, -4\ns_getpc_b64 s[60:61]\n"
"spc_p2:\ns_sub_u32 s48, sgo_p2-spc_p2, s48\ns_add_u32 s60, s60, s48\ns_addc_u32 s61, s61, 0\n"
"s_waitcnt vmcnt(1)\nds_write_b128 v9, v[56:59] offset:4096\nv_max3_u32 v218, v218, v56, v57\nv_max3_u32 v218, v218, v58, v59\n"
"s_waitcnt vmcnt(0)\nds_write_b128 v9, v[60:63] offset:5120\nv_max3_u32 v218, v218, v60, v61\nv_max3_u32 v218, v218, v62, v63\n"
"v_cmp_eq_u32 vcc, -1, v218\nv_smfmac_f32_16x16x64_bf16 v[68:71], v[56:59], a[32:39], v8\nv_smfmac_f32_16x16x64_bf16 v[72:75], v[56:59], a[88:95], v8\ns_cbranch_vccnz retry1_p2\n"
"s_waitcnt lgkmcnt(0)\ns_barrier\nds_read_b32 v221, v222\nds_read_b128 v[64:67], v9 offset:6144\n"
"ds_read_b128 v[40:43], v9 offset:0\nds_read_b128 v[44:47], v9 offset:1024\nds_read_b128 v[48:51], v9 offset:2048\nds_read_b128 v[52:55], v9 offset:3072\n"
"v_smfmac_f32_16x16x64_bf16 v[76:79], v[56:59], a[144:151], v8\nv_smfmac_f32_16x16x64_bf16 v[80:83], v[56:59], a[200:207], v8\nv_smfmac_f32_16x16x64_bf16 v[68:71], v[60:63], a[40:47], v8\nv_smfmac_f32_16x16x64_bf16 v[72:75], v[60:63], a[96:103], v8\n"
"v_smfmac_f32_16x16x64_bf16 v[76:79], v[60:63], a[152:159], v8\nv_smfmac_f32_16x16x64_bf16 v[80:83], v[60:63], a[208:215], v8\ns_waitcnt lgkmcnt(0)\nv_readfirstlane_b32 s47, v221\n"
"s_sub_u32 s47, s47, s40\ns_cmp_eq_u32 s47, 1\ns_cbranch_scc1 adopt_p2\nback_p2:\n"
"s_cmp_eq_u32 s40, 63\ns_cbranch_scc1 last_p2\nsf_p2:\ns_memrealtime s[54:55]\n"
"s_nop 2\ns_memrealtime s[56:57]\ns_nop 2\ns_memrealtime s[58:59]\n"
"s_waitcnt lgkmcnt(4)\ns_sub_u32 s47, s54, s90\ns_add_u32 s48, s47, 12\ns_cmp_ge_u32 s48, 12\n"
"s_cbranch_scc1 so_p2\ns_memrealtime s[54:55]\ns_waitcnt lgkmcnt(4)\ns_cmp_ge_u32 s56, s90\n"
"s_cbranch_scc1 sx_p2\ns_memrealtime s[56:57]\ns_waitcnt lgkmcnt(4)\ns_cmp_ge_u32 s58, s90\n"
"s_cbranch_scc1 sx_p2\ns_memrealtime s[58:59]\ns_waitcnt lgkmcnt(4)\ns_cmp_ge_u32 s54, s90\n"
"s_cbranch_scc1 sx_p2\ns_memrealtime s[54:55]\ns_waitcnt lgkmcnt(4)\ns_cmp_ge_u32 s56, s90\n"
"s_cbranch_scc1 sx_p2\ns_memrealtime s[56:57]\ns_waitcnt lgkmcnt(4)\ns_cmp_ge_u32 s58, s90\n"
"s_cbranch_scc1 sx_p2\ns_memrealtime s[58:59]\ns_waitcnt lgkmcnt(4)\ns_cmp_ge_u32 s54, s90\n"
"s_cbranch_scc1 sx_p2\ns_memrealtime s[54:55]\ns_waitcnt lgkmcnt(4)\ns_cmp_ge_u32 s56, s90\n"
"s_cbranch_scc1 sx_p2\ns_memrealtime s[56:57]\ns_waitcnt lgkmcnt(4)\ns_cmp_ge_u32 s58, s90\n"
"s_cbranch_scc1 sx_p2\ns_memrealtime s[58:59]\ns_waitcnt lgkmcnt(4)\ns_cmp_ge_u32 s54, s90\n"
"s_cbranch_scc1 sx_p2\ns_memrealtime s[54:55]\ns_waitcnt lgkmcnt(4)\ns_cmp_ge_u32 s56, s90\n"
"s_cbranch_scc1 sx_p2\ns_memrealtime s[56:57]\ns_waitcnt lgkmcnt(4)\ns_cmp_ge_u32 s58, s90\n"
"s_cbranch_scc1 sx_p2\ns_memrealtime s[58:59]\ns_waitcnt lgkmcnt(4)\ns_cmp_ge_u32 s54, s90\n"
"s_cbranch_scc1 sx_p2\ns_memrealtime s[54:55]\ns_waitcnt lgkmcnt(4)\ns_cmp_ge_u32 s56, s90\n"
"s_cbranch_scc1 sx_p2\ns_memrealtime s[56:57]\ns_waitcnt lgkmcnt(4)\ns_cmp_ge_u32 s58, s90\n"
"s_cbranch_scc1 sx_p2\ns_memrealtime s[58:59]\ns_waitcnt lgkmcnt(4)\ns_cmp_ge_u32 s54, s90\n"
"s_cbranch_scc1 sx_p2\ns_memrealtime s[54:55]\ns_waitcnt lgkmcnt(4)\ns_cmp_ge_u32 s56, s90\n"
"s_cbranch_scc1 sx_p2\ns_memrealtime s[56:57]\ns_branch sx_p2\nso_p2:\n"
"s_waitcnt lgkmcnt(0)\ns_cmp_lt_i32 s47, 0\ns_cbranch_scc1 sf_p2\ns_cmp_gt_i32 s47, s32\n"
"s_cbranch_scc1 late_p2\ns_branch sgo_p2\nsx_p2:\ns_setpc_b64 s[60:61]\n"
"s_nop 0\ns_nop 0\ns_nop 0\ns_nop 0\n"
"s_nop 0\ns_nop 0\ns_nop 0\ns_nop 0\n"
"s_nop 0\ns_nop 0\ns_nop 0\ns_nop 0\n"
"s_nop 0\ns_nop 0\ns_nop 0\ns_nop 0\n"
"s_nop 0\ns_nop 0\ns_nop 0\ns_nop 0\n"
"s_nop 0\ns_nop 0\ns_nop 0\ns_nop 0\n"
"sgo_p2:\ns_nop 11\nbuffer_load_dwordx4 v[116:119], v20, s[8:11], s41 offen offset:0 sc1\nbuffer_load_dwordx4 v[120:123], v20, s[8:11], s41 offen offset:1024 sc1\n"
"v_smfmac_f32_16x16x64_bf16 v[68:71], v[64:67], a[48:55], v8\nv_mov_b32 v84, v100\nv_smfmac_f32_16x16x64_bf16 v[72:75], v[64:67], a[104:111], v8\nv_mov_b32 v85, v101\n"
"v_smfmac_f32_16x16x64_bf16 v[76:79], v[64:67], a[160:167], v8\nv_mov_b32 v86, v102\nv_smfmac_f32_16x16x64_bf16 v[80:83], v[64:67], a[216:223], v8\nv_mov_b32 v87, v103\n"
"v_smfmac_f32_16x16x64_bf16 v[68:71], v[40:43], a[0:7], v8\nv_mov_b32 v88, v104\nv_smfmac_f32_16x16x64_bf16 v[72:75], v[40:43], a[56:63], v8\nv_mov_b32 v89, v105\n"
"v_smfmac_f32_16x16x64_bf16 v[76:79], v[40:43], a[112:119], v8\nv_mov_b32 v90, v106\nv_smfmac_f32_16x16x64_bf16 v[80:83], v[40:43], a[168:175], v8\nv_mov_b32 v91, v107\n"
"v_smfmac_f32_16x16x64_bf16 v[68:71], v[44:47], a[8:15], v8\nv_mov_b32 v92, v108\nv_smfmac_f32_16x16x64_bf16 v[72:75], v[44:47], a[64:71], v8\nv_mov_b32 v93, v109\n"
"buffer_load_dwordx4 v[124:127], v21, s[8:11], s41 offen offset:0 sc1\nbuffer_load_dwordx4 v[128:131], v21, s[8:11], s41 offen offset:1024 sc1\nv_smfmac_f32_16x16x64_bf16 v[76:79], v[44:47], a[120:127], v8\nv_mov_b32 v94, v110\n"
"v_smfmac_f32_16x16x64_bf16 v[80:83], v[44:47], a[176:183], v8\nv_mov_b32 v95, v111\nv_smfmac_f32_16x16x64_bf16 v[68:71], v[48:51], a[16:23], v8\nv_mov_b32 v96, v112\n"
"v_smfmac_f32_16x16x64_bf16 v[72:75], v[48:51], a[72:79], v8\nv_mov_b32 v97, v113\nv_smfmac_f32_16x16x64_bf16 v[76:79], v[48:51], a[128:135], v8\nv_mov_b32 v98, v114\n"
"v_smfmac_f32_16x16x64_bf16 v[80:83], v[48:51], a[184:191], v8\nv_mov_b32 v99, v115\nv_smfmac_f32_16x16x64_bf16 v[68:71], v[52:55], a[24:31], v8\nv_smfmac_f32_16x16x64_bf16 v[72:75], v[52:55], a[80:87], v8\n"
"v_smfmac_f32_16x16x64_bf16 v[76:79], v[52:55], a[136:143], v8\nv_smfmac_f32_16x16x64_bf16 v[80:83], v[52:55], a[192:199], v8\nv_add_f32 v219, v68, v69\nv_add_f32 v220, v70, v71\n"
"v_cvt_pk_bf16_f32 v156, v219, v220\nv_add_f32 v219, v72, v73\nv_add_f32 v220, v74, v75\nv_cvt_pk_bf16_f32 v157, v219, v220\n"
"v_add_f32 v219, v76, v77\nv_add_f32 v220, v78, v79\nv_cvt_pk_bf16_f32 v158, v219, v220\nv_add_f32 v219, v80, v81\n"
"v_add_f32 v220, v82, v83\nv_cvt_pk_bf16_f32 v159, v219, v220\nbuffer_load_dwordx4 v[100:103], v9, s[16:19], s50 offen offset:0 sc1\nbuffer_load_dwordx4 v[104:107], v9, s[16:19], s50 offen offset:1024 sc1\n"
"buffer_load_dwordx4 v[108:111], v9, s[16:19], s50 offen offset:2048 sc1\nbuffer_load_dwordx4 v[112:115], v9, s[16:19], s50 offen offset:3072 sc1\ns_add_u32 s50, s50, 16384\nbuffer_store_dwordx4 v[156:159], v9, s[20:23], s51 offen sc1\n"
"s_waitcnt vmcnt(8)\nv_mfma_f32_16x16x32_bf16 v[132:135], v[84:87], v[116:119], 0\nv_mfma_f32_16x16x32_bf16 v[136:139], v[88:91], v[116:119], 0\nv_mfma_f32_16x16x32_bf16 v[148:151], v[176:179], v[116:119], 0\n"
"s_waitcnt vmcnt(7)\nv_mfma_f32_16x16x32_bf16 v[140:143], v[92:95], v[120:123], 0\nv_mfma_f32_16x16x32_bf16 v[144:147], v[96:99], v[120:123], 0\nv_mfma_f32_16x16x32_bf16 v[148:151], v[180:183], v[120:123], v[148:151]\n"
"v_cndmask_b32 v132, v136, v132, s[62:63]\nv_cndmask_b32 v133, v137, v133, s[62:63]\nv_cndmask_b32 v134, v138, v134, s[62:63]\nv_cndmask_b32 v135, v139, v135, s[62:63]\n"
"s_waitcnt vmcnt(6)\nv_mfma_f32_16x16x32_f16 v[152:155], v[168:171], v[124:127], 0\ns_waitcnt vmcnt(5)\nv_mfma_f32_16x16x32_f16 v[152:155], v[172:175], v[128:131], v[152:155]\n"
"v_cndmask_b32 v140, v144, v140, s[62:63]\nv_cndmask_b32 v141, v145, v141, s[62:63]\nv_cndmask_b32 v142, v146, v142, s[62:63]\nv_cndmask_b32 v143, v147, v143, s[62:63]\n"
"s_nop 2\nv_exp_f32 v152, v152\nv_exp_f32 v153, v153\nv_exp_f32 v154, v154\n"
"v_exp_f32 v155, v155\nv_cvt_pk_bf16_f32 v200, v152, v153\nv_cvt_pk_bf16_f32 v201, v154, v155\nv_and_b32 v160, v200, v223\n"
"v_and_b32 v161, v200, v224\nv_mfma_f32_16x16x16_bf16 v[148:151], v[214:215], v[200:201], v[148:151]\nv_and_b32 v162, v201, v223\nv_and_b32 v163, v201, v224\n"
"v_mfma_f32_16x16x16_bf16 v[132:135], v[156:157], v[160:161], v[132:135]\ns_nop 0\nv_mfma_f32_16x16x16_bf16 v[140:143], v[158:159], v[162:163], v[140:143]\nv_rcp_f32 v202, v148\n"
"v_rcp_f32 v203, v149\nv_mul_f32 v132, v132, v202\nv_mul_f32 v133, v133, v202\nv_mul_f32 v134, v134, v202\n"
"v_mul_f32 v135, v135, v202\nv_fmac_f32 v132, v140, v203\nv_fmac_f32 v133, v141, v203\nv_fmac_f32 v134, v142, v203\n"
"v_fmac_f32 v135, v143, v203\nv_add_f32_dpp v204, v132, v132 row_ror:8 row_mask:0xf bank_mask:0x3\nv_add_f32_dpp v205, v133, v133 row_ror:8 row_mask:0xf bank_mask:0x3\nv_add_f32_dpp v204, v134, v134 row_ror:8 row_mask:0xf bank_mask:0xc\n"
"v_add_f32_dpp v205, v135, v135 row_ror:8 row_mask:0xf bank_mask:0xc\nv_subrev_u32 v219, 0x2f800000, v148\nv_subrev_u32 v220, 0x2f800000, v149\nv_max_u32 v219, v219, v220\n"
"v_cmp_le_u32 vcc, 0x20000000, v219\nds_write_b64 v22, v[204:205]\ns_or_b64 s[56:57], vcc, s[94:95]\ns_cbranch_scc1 slow_p2\n"
"join_p2:\nv_mov_b32 v219, 0\ns_nop 1\nv_mov_b32 v219, 0\n"
"s_waitcnt lgkmcnt(0)\ns_barrier\nds_read_b64 v[206:207], v23 offset:0\nds_read_b64 v[208:209], v23 offset:1280\n"
"ds_read_b64 v[210:211], v23 offset:2560\nds_read_b64 v[212:213], v23 offset:3840\nds_read_b32 v221, v222\nv_mov_b32 v219, 0\n"
"s_nop 1\nv_mov_b32 v219, 0\ns_nop 1\nv_mov_b32 v219, 0\n"
"s_waitcnt lgkmcnt(3)\nv_pk_add_f32 v[206:207], v[206:207], v[208:209]\ns_waitcnt lgkmcnt(1)\nv_pk_add_f32 v[210:211], v[210:211], v[212:213]\n"
"v_pk_add_f32 v[206:207], v[206:207], v[210:211]\nv_cvt_pk_bf16_f32 v220, v206, v207\nbuffer_store_dword v220, v16, s[8:11], s41 offen sc1\ns_add_u32 s51, s51, 4096\n"
"s_add_u32 s40, s40, 1\ns_mov_b32 s43, s41\ns_add_u32 s41, s41, 131072\ns_and_b32 s41, s41, 524287\n"
"s_xor_b32 s42, s41, 262144\ns_and_b32 s47, s46, 15\ns_mul_i32 s48, s47, 8\ns_add_u32 s45, s45, s48\n"
"s_add_u32 s45, s45, s30\ns_lshr_b32 s48, s45, 4\ns_lshl_b32 s48, s48, 2\ns_add_u32 s44, s44, s48\n"
"s_and_b32 s45, s45, 15\ns_add_u32 s92, s92, 1\ns_waitcnt lgkmcnt(0)\ns_cmp_eq_u32 s40, 1\n"
"s_cbranch_scc1 one_p2\nv_readfirstlane_b32 s47, v221\ns_sub_u32 s47, s47, s40\ns_cmp_eq_u32 s47, 1\n"
"s_cbranch_scc1 rec_p2\ns_branch top_p2\none_p2:\nbuffer_load_dword v218, v11, s[8:11], 0 offen sc1\n"
"s_waitcnt vmcnt(0)\nf1_p2:\ns_add_u32 s47, s40, 2\ns_min_u32 s47, s47, 64\n"
"s_add_u32 s47, s47, s33\nv_subrev_u32 v218, s47, v218\nv_cmp_lt_u32 vcc, 64, v218\ns_cbranch_vccz f1ok_p2\n"
"s_add_u32 s53, s53, 0\ns_add_u32 s49, s49, 1\ns_cmp_gt_u32 s49, 65536\ns_cbranch_scc1 end_p2\n"
"s_sleep 2\nbuffer_load_dword v218, v11, s[8:11], 0 offen sc1\ns_waitcnt vmcnt(0)\ns_branch f1_p2\n"
"f1ok_p2:\ns_branch top_p2\nlast_p2:\nv_smfmac_f32_16x16x64_bf16 v[68:71], v[64:67], a[48:55], v8\n"
"v_smfmac_f32_16x16x64_bf16 v[72:75], v[64:67], a[104:111], v8\nv_smfmac_f32_16x16x64_bf16 v[76:79], v[64:67], a[160:167], v8\nv_smfmac_f32_16x16x64_bf16 v[80:83], v[64:67], a[216:223], v8\nv_smfmac_f32_16x16x64_bf16 v[68:71], v[40:43], a[0:7], v8\n"
"v_smfmac_f32_16x16x64_bf16 v[72:75], v[40:43], a[56:63], v8\nv_smfmac_f32_16x16x64_bf16 v[76:79], v[40:43], a[112:119], v8\nv_smfmac_f32_16x16x64_bf16 v[80:83], v[40:43], a[168:175], v8\nv_smfmac_f32_16x16x64_bf16 v[68:71], v[44:47], a[8:15], v8\n"
"v_smfmac_f32_16x16x64_bf16 v[72:75], v[44:47], a[64:71], v8\nv_smfmac_f32_16x16x64_bf16 v[76:79], v[44:47], a[120:127], v8\nv_smfmac_f32_16x16x64_bf16 v[80:83], v[44:47], a[176:183], v8\nv_smfmac_f32_16x16x64_bf16 v[68:71], v[48:51], a[16:23], v8\n"
"v_smfmac_f32_16x16x64_bf16 v[72:75], v[48:51], a[72:79], v8\nv_smfmac_f32_16x16x64_bf16 v[76:79], v[48:51], a[128:135], v8\nv_smfmac_f32_16x16x64_bf16 v[80:83], v[48:51], a[184:191], v8\nv_smfmac_f32_16x16x64_bf16 v[68:71], v[52:55], a[24:31], v8\n"
"v_smfmac_f32_16x16x64_bf16 v[72:75], v[52:55], a[80:87], v8\nv_smfmac_f32_16x16x64_bf16 v[76:79], v[52:55], a[136:143], v8\nv_smfmac_f32_16x16x64_bf16 v[80:83], v[52:55], a[192:199], v8\nv_add_f32 v219, v68, v69\n"
"v_add_f32 v220, v70, v71\nv_cvt_pk_bf16_f32 v156, v219, v220\nv_add_f32 v219, v72, v73\nv_add_f32 v220, v74, v75\n"
"v_cvt_pk_bf16_f32 v157, v219, v220\nv_add_f32 v219, v76, v77\nv_add_f32 v220, v78, v79\nv_cvt_pk_bf16_f32 v158, v219, v220\n"
"v_add_f32 v219, v80, v81\nv_add_f32 v220, v82, v83\nv_cvt_pk_bf16_f32 v159, v219, v220\nbuffer_store_dwordx4 v[156:159], v9, s[20:23], s51 offen sc1\n"
"s_add_u32 s47, s33, 64\nv_mov_b32 v219, s47\ns_waitcnt vmcnt(0)\nbuffer_store_dword v219, v18, s[8:11], 0 offen sc1\n"
"s_branch end_p2\nretry1_p2:\ns_add_u32 s53, s53, 256\ns_add_u32 s49, s49, 1\n"
"s_cmp_gt_u32 s49, 65536\ns_cbranch_scc1 end_p2\ns_add_u32 s47, s40, 1\nv_mov_b32 v221, s47\n"
"ds_write_b32 v222, v221\nrec_p2:\ns_sleep 4\nv_mov_b32 v68, 0\n"
"v_mov_b32 v69, 0\nv_mov_b32 v70, 0\nv_mov_b32 v71, 0\nv_mov_b32 v72, 0\n"
"v_mov_b32 v73, 0\nv_mov_b32 v74, 0\nv_mov_b32 v75, 0\nv_mov_b32 v76, 0\n"
"v_mov_b32 v77, 0\nv_mov_b32 v78, 0\nv_mov_b32 v79, 0\nv_mov_b32 v80, 0\n"
"v_mov_b32 v81, 0\nv_mov_b32 v82, 0\nv_mov_b32 v83, 0\nv_mov_b32 v218, 0\n"
"s_branch top2_p2\nadopt_p2:\nbuffer_load_dwordx4 v[164:167], v28, s[8:11], 0 offen sc1\ns_lshl_b32 s52, s40, 26\n"
"s_or_b32 s47, s52, 0x3ffffff\ns_waitcnt vmcnt(0)\nv_cmp_ge_u32 vcc, s47, v164\ns_nop 1\n"
"v_cndmask_b32 v164, 0, v164, vcc\nv_cmp_ge_u32 vcc, s47, v165\ns_nop 1\nv_cndmask_b32 v165, 0, v165, vcc\n"
"v_cmp_ge_u32 vcc, s47, v166\ns_nop 1\nv_cndmask_b32 v166, 0, v166, vcc\nv_cmp_ge_u32 vcc, s47, v167\n"
"s_nop 1\nv_cndmask_b32 v167, 0, v167, vcc\nv_max3_u32 v221, v164, v165, v166\nv_max_u32 v221, v221, v167\n"
"s_nop 1\nv_max_u32_dpp v221, v221, v221 row_ror:8 row_mask:0xf bank_mask:0xf\ns_nop 1\nv_max_u32_dpp v221, v221, v221 row_ror:4 row_mask:0xf bank_mask:0xf\n"
"s_nop 1\nv_max_u32_dpp v221, v221, v221 row_ror:2 row_mask:0xf bank_mask:0xf\ns_nop 1\nv_max_u32_dpp v221, v221, v221 row_ror:1 row_mask:0xf bank_mask:0xf\n"
"s_nop 0\nv_readlane_b32 s47, v221, 0\nv_readlane_b32 s48, v221, 16\ns_max_u32 s48, s48, s47\n"
"s_cmp_le_u32 s48, s52\ns_cbranch_scc1 back2_p2\ns_bfe_u32 s52, s48, 1179656\ns_add_u32 s44, s52, s28\n"
"s_mov_b32 s45, 0\ns_and_b32 s46, s48, 255\ns_mov_b32 s92, 0\ns_branch back2_p2\n"
"back2_p2:\ns_and_b32 s47, s46, 15\ns_mul_i32 s47, s47, 4\ns_add_u32 s47, s47, s29\n"
"s_add_u32 s47, s47, s45\ns_and_b32 s91, s47, 15\ns_lshr_b32 s47, s47, 4\ns_lshl_b32 s47, s47, 2\n"
"s_add_u32 s90, s44, s47\ns_mul_i32 s48, s91, 6\ns_and_b32 s48, s48, -4\ns_getpc_b64 s[60:61]\n"
"spc2_p2:\ns_sub_u32 s48, sgo_p2-spc2_p2, s48\ns_add_u32 s60, s60, s48\ns_addc_u32 s61, s61, -1\n"
"s_branch back_p2\nlate_p2:\ns_add_u32 s53, s53, 16777216\ns_mov_b64 s[94:95], 1\n"
"s_add_u32 s52, s90, 20\nlw_p2:\ns_memrealtime s[54:55]\ns_waitcnt lgkmcnt(0)\n"
"s_sub_u32 s47, s54, s52\ns_cmp_lt_i32 s47, 0\ns_cbranch_scc1 lw_p2\ns_branch sgo_p2\n"
"slow_p2:\ns_mov_b64 s[94:95], 0\ns_cbranch_vccnz slow2_p2\ns_memrealtime s[54:55]\n"
"s_add_u32 s52, s40, 1\ns_lshl_b32 s52, s52, 26\ns_waitcnt lgkmcnt(0)\ns_sub_u32 s47, s54, s28\n"
"s_add_u32 s47, s47, s31\ns_min_u32 s47, s47, 0x3ffff\ns_lshl_b32 s47, s47, 8\ns_or_b32 s47, s47, s52\n"
"s_sub_u32 s52, s92, 1\ns_cmp_lt_u32 s52, 6\ns_cselect_b32 s48, 1, 0\ns_cmp_ge_u32 s40, 5\n"
"s_cselect_b32 s48, s48, 0\ns_add_u32 s48, s48, s46\ns_min_u32 s48, s48, 15\ns_or_b32 s47, s47, s48\n"
"v_mov_b32 v225, s47\nbuffer_store_dword v225, v19, s[8:11], 0 offen sc1\ns_add_u32 s47, s40, 2\nv_mov_b32 v221, s47\n"
"ds_write_b32 v222, v221\ns_branch join_p2\nslow2_p2:\nv_max3_u32 v218, v124, v125, v126\n"
"v_max_u32 v218, v218, v127\nv_max3_u32 v218, v218, v128, v129\nv_max3_u32 v218, v218, v130, v131\nv_max3_u32 v218, v218, v116, v117\n"
"v_max3_u32 v218, v218, v118, v119\nv_max3_u32 v218, v218, v120, v121\nv_max3_u32 v218, v218, v122, v123\nv_cmp_eq_u32 vcc, -1, v218\n"
"s_cbranch_vccz exact_p2\ns_branch spin_p2\nspin2_p2:\ns_sleep 3\n"
"spin_p2:\ns_add_u32 s53, s53, 1\ns_add_u32 s49, s49, 1\ns_cmp_gt_u32 s49, 65536\n"
"s_cbranch_scc1 end_p2\nbuffer_load_dwordx4 v[116:119], v20, s[8:11], s41 offen offset:0 sc1\nbuffer_load_dwordx4 v[120:123], v20, s[8:11], s41 offen offset:1024 sc1\nbuffer_load_dwordx4 v[124:127], v21, s[8:11], s41 offen offset:0 sc1\n"
"buffer_load_dwordx4 v[128:131], v21, s[8:11], s41 offen offset:1024 sc1\ns_waitcnt vmcnt(0)\nv_max3_u32 v218, v124, v125, v126\nv_max_u32 v218, v218, v127\n"
"v_max3_u32 v218, v218, v128, v129\nv_max3_u32 v218, v218, v130, v131\nv_max3_u32 v218, v218, v116, v117\nv_max3_u32 v218, v218, v118, v119\n"
"v_max3_u32 v218, v218, v120, v121\nv_max3_u32 v218, v218, v122, v123\nv_cmp_eq_u32 vcc, -1, v218\ns_cbranch_vccnz spin2_p2\n"
"s_memrealtime s[54:55]\ns_add_u32 s52, s40, 1\ns_lshl_b32 s52, s52, 26\ns_waitcnt lgkmcnt(0)\n"
"s_sub_u32 s47, s54, s28\ns_add_u32 s47, s47, s38\ns_min_u32 s47, s47, 0x3ffff\ns_lshl_b32 s47, s47, 8\n"
"s_or_b32 s47, s47, s52\ns_sub_u32 s52, s92, 1\ns_cmp_lt_u32 s52, 6\ns_cselect_b32 s48, 1, 0\n"
"s_cmp_ge_u32 s40, 5\ns_cselect_b32 s48, s48, 0\ns_add_u32 s48, s48, s46\ns_min_u32 s48, s48, 15\n"
"s_or_b32 s47, s47, s48\nv_mov_b32 v225, s47\nbuffer_store_dword v225, v19, s[8:11], 0 offen sc1\ns_add_u32 s47, s40, 2\n"
"v_mov_b32 v221, s47\nds_write_b32 v222, v221\nv_mfma_f32_16x16x32_bf16 v[132:135], v[84:87], v[116:119], 0\nv_mfma_f32_16x16x32_bf16 v[136:139], v[88:91], v[116:119], 0\n"
"v_mfma_f32_16x16x32_bf16 v[148:151], v[176:179], v[116:119], 0\nv_mfma_f32_16x16x32_bf16 v[140:143], v[92:95], v[120:123], 0\nv_mfma_f32_16x16x32_bf16 v[144:147], v[96:99], v[120:123], 0\nv_mfma_f32_16x16x32_bf16 v[148:151], v[180:183], v[120:123], v[148:151]\n"
"v_cndmask_b32 v132, v136, v132, s[62:63]\nv_cndmask_b32 v133, v137, v133, s[62:63]\nv_cndmask_b32 v134, v138, v134, s[62:63]\nv_cndmask_b32 v135, v139, v135, s[62:63]\n"
"v_mfma_f32_16x16x32_f16 v[152:155], v[168:171], v[124:127], 0\nv_mfma_f32_16x16x32_f16 v[152:155], v[172:175], v[128:131], v[152:155]\nv_cndmask_b32 v140, v144, v140, s[62:63]\nv_cndmask_b32 v141, v145, v141, s[62:63]\n"
"v_cndmask_b32 v142, v146, v142, s[62:63]\nv_cndmask_b32 v143, v147, v143, s[62:63]\ns_nop 2\nv_exp_f32 v152, v152\n"
"v_exp_f32 v153, v153\nv_exp_f32 v154, v154\nv_exp_f32 v155, v155\nv_cvt_pk_bf16_f32 v200, v152, v153\n"
"v_cvt_pk_bf16_f32 v201, v154, v155\nv_and_b32 v160, v200, v223\nv_and_b32 v161, v200, v224\nv_mfma_f32_16x16x16_bf16 v[148:151], v[214:215], v[200:201], v[148:151]\n"
"v_and_b32 v162, v201, v223\nv_and_b32 v163, v201, v224\nv_mfma_f32_16x16x16_bf16 v[132:135], v[156:157], v[160:161], v[132:135]\ns_nop 0\n"
"v_mfma_f32_16x16x16_bf16 v[140:143], v[158:159], v[162:163], v[140:143]\nv_rcp_f32 v202, v148\nv_rcp_f32 v203, v149\nv_mul_f32 v132, v132, v202\n"
"v_mul_f32 v133, v133, v202\nv_mul_f32 v134, v134, v202\nv_mul_f32 v135, v135, v202\nv_fmac_f32 v132, v140, v203\n"
"v_fmac_f32 v133, v141, v203\nv_fmac_f32 v134, v142, v203\nv_fmac_f32 v135, v143, v203\nv_add_f32_dpp v204, v132, v132 row_ror:8 row_mask:0xf bank_mask:0x3\n"
"v_add_f32_dpp v205, v133, v133 row_ror:8 row_mask:0xf bank_mask:0x3\nv_add_f32_dpp v204, v134, v134 row_ror:8 row_mask:0xf bank_mask:0xc\nv_add_f32_dpp v205, v135, v135 row_ror:8 row_mask:0xf bank_mask:0xc\nv_subrev_u32 v219, 0x2f800000, v148\n"
"v_subrev_u32 v220, 0x2f800000, v149\nv_max_u32 v219, v219, v220\nv_cmp_le_u32 vcc, 0x20000000, v219\nds_write_b64 v22, v[204:205]\n"
"s_cbranch_vccz join_p2\nexact_p2:\ns_add_u32 s53, s53, 65536\nex0_p2:\n"
"s_add_u32 s47, s41, 0\nbuffer_load_dwordx2 v[184:185], v24, s[8:11], s47 offen offset:0 sc1\nbuffer_load_dwordx2 v[186:187], v24, s[8:11], s47 offen offset:512 sc1\nbuffer_load_dwordx2 v[188:189], v24, s[8:11], s47 offen offset:1024 sc1\n"
"buffer_load_dwordx2 v[190:191], v24, s[8:11], s47 offen offset:1536 sc1\nbuffer_load_dwordx4 v[192:195], v25, s[8:11], s47 offen sc1\nbuffer_load_dwordx4 v[196:199], v25, s[8:11], s47 offen offset:16 sc1\nv_mov_b32 v218, 0\n"
"s_waitcnt vmcnt(0)\nv_max3_u32 v218, v218, v184, v185\nv_max3_u32 v218, v218, v186, v187\nv_max3_u32 v218, v218, v188, v189\n"
"v_max3_u32 v218, v218, v190, v191\nv_max3_u32 v218, v218, v192, v193\nv_max3_u32 v218, v218, v194, v195\nv_max3_u32 v218, v218, v196, v197\n"
"v_max3_u32 v218, v218, v198, v199\nv_cmp_eq_u32 vcc, -1, v218\ns_cbranch_vccz exok0_p2\ns_add_u32 s53, s53, 0\n"
"s_add_u32 s49, s49, 1\ns_cmp_gt_u32 s49, 65536\ns_cbranch_scc1 end_p2\ns_branch ex0_p2\n"
"exok0_p2:\nv_add_f32 v184, v184, v186\nv_add_f32 v188, v188, v190\nv_add_f32 v184, v184, v188\n"
"v_add_f32 v185, v185, v187\nv_add_f32 v189, v189, v191\nv_add_f32 v185, v185, v189\nv_max_f32 v225, v184, v185\n"
"v_max3_f32 v225, v225, v192, v193\nv_max3_f32 v225, v225, v194, v195\nv_max3_f32 v225, v225, v196, v197\nv_max3_f32 v225, v225, v198, v199\n"
"v_mov_b32 v219, v225\ns_nop 1\nv_permlane16_swap_b32 v225, v219\nv_max_f32 v225, v225, v219\n"
"v_mov_b32 v219, v225\ns_nop 1\nv_permlane32_swap_b32 v225, v219\nv_max_f32 v225, v225, v219\n"
"v_sub_f32 v184, v184, v225\nv_exp_f32 v152, v184\nv_sub_f32 v185, v185, v225\nv_exp_f32 v153, v185\n"
"v_sub_f32 v192, v192, v225\nv_exp_f32 v192, v192\nv_sub_f32 v193, v193, v225\nv_exp_f32 v193, v193\n"
"v_sub_f32 v194, v194, v225\nv_exp_f32 v194, v194\nv_sub_f32 v195, v195, v225\nv_exp_f32 v195, v195\n"
"v_sub_f32 v196, v196, v225\nv_exp_f32 v196, v196\nv_sub_f32 v197, v197, v225\nv_exp_f32 v197, v197\n"
"v_sub_f32 v198, v198, v225\nv_exp_f32 v198, v198\nv_sub_f32 v199, v199, v225\nv_exp_f32 v199, v199\n"
"s_nop 0\nv_cvt_pk_bf16_f32 v116, v192, v193\nv_cvt_pk_bf16_f32 v117, v194, v195\nv_cvt_pk_bf16_f32 v118, v196, v197\n"
"v_cvt_pk_bf16_f32 v119, v198, v199\nex1_p2:\ns_add_u32 s47, s41, 4096\nbuffer_load_dwordx2 v[184:185], v24, s[8:11], s47 offen offset:0 sc1\n"
"buffer_load_dwordx2 v[186:187], v24, s[8:11], s47 offen offset:512 sc1\nbuffer_load_dwordx2 v[188:189], v24, s[8:11], s47 offen offset:1024 sc1\nbuffer_load_dwordx2 v[190:191], v24, s[8:11], s47 offen offset:1536 sc1\nbuffer_load_dwordx4 v[192:195], v25, s[8:11], s47 offen sc1\n"
"buffer_load_dwordx4 v[196:199], v25, s[8:11], s47 offen offset:16 sc1\nv_mov_b32 v218, 0\ns_waitcnt vmcnt(0)\nv_max3_u32 v218, v218, v184, v185\n"
"v_max3_u32 v218, v218, v186, v187\nv_max3_u32 v218, v218, v188, v189\nv_max3_u32 v218, v218, v190, v191\nv_max3_u32 v218, v218, v192, v193\n"
"v_max3_u32 v218, v218, v194, v195\nv_max3_u32 v218, v218, v196, v197\nv_max3_u32 v218, v218, v198, v199\nv_cmp_eq_u32 vcc, -1, v218\n"
"s_cbranch_vccz exok1_p2\ns_add_u32 s53, s53, 0\ns_add_u32 s49, s49, 1\ns_cmp_gt_u32 s49, 65536\n"
"s_cbranch_scc1 end_p2\ns_branch ex1_p2\nexok1_p2:\nv_add_f32 v184, v184, v186\n"
"v_add_f32 v188, v188, v190\nv_add_f32 v184, v184, v188\nv_add_f32 v185, v185, v187\nv_add_f32 v189, v189, v191\n"
"v_add_f32 v185, v185, v189\nv_max_f32 v225, v184, v185\nv_max3_f32 v225, v225, v192, v193\nv_max3_f32 v225, v225, v194, v195\n"
"v_max3_f32 v225, v225, v196, v197\nv_max3_f32 v225, v225, v198, v199\nv_mov_b32 v219, v225\ns_nop 1\n"
"v_permlane16_swap_b32 v225, v219\nv_max_f32 v225, v225, v219\nv_mov_b32 v219, v225\ns_nop 1\n"
"v_permlane32_swap_b32 v225, v219\nv_max_f32 v225, v225, v219\nv_sub_f32 v184, v184, v225\nv_exp_f32 v154, v184\n"
"v_sub_f32 v185, v185, v225\nv_exp_f32 v155, v185\nv_sub_f32 v192, v192, v225\nv_exp_f32 v192, v192\n"
"v_sub_f32 v193, v193, v225\nv_exp_f32 v193, v193\nv_sub_f32 v194, v194, v225\nv_exp_f32 v194, v194\n"
"v_sub_f32 v195, v195, v225\nv_exp_f32 v195, v195\nv_sub_f32 v196, v196, v225\nv_exp_f32 v196, v196\n"
"v_sub_f32 v197, v197, v225\nv_exp_f32 v197, v197\nv_sub_f32 v198, v198, v225\nv_exp_f32 v198, v198\n"
"v_sub_f32 v199, v199, v225\nv_exp_f32 v199, v199\ns_nop 0\nv_cvt_pk_bf16_f32 v120, v192, v193\n"
"v_cvt_pk_bf16_f32 v121, v194, v195\nv_cvt_pk_bf16_f32 v122, v196, v197\nv_cvt_pk_bf16_f32 v123, v198, v199\ns_memrealtime s[54:55]\n"
"s_add_u32 s52, s40, 1\ns_lshl_b32 s52, s52, 26\ns_waitcnt lgkmcnt(0)\ns_sub_u32 s47, s54, s28\n"
"s_add_u32 s47, s47, s38\ns_min_u32 s47, s47, 0x3ffff\ns_lshl_b32 s47, s47, 8\ns_or_b32 s47, s47, s52\n"
"s_sub_u32 s52, s92, 1\ns_cmp_lt_u32 s52, 6\ns_cselect_b32 s48, 1, 0\ns_cmp_ge_u32 s40, 5\n"
"s_cselect_b32 s48, s48, 0\ns_add_u32 s48, s48, s46\ns_min_u32 s48, s48, 15\ns_or_b32 s47, s47, s48\n"
"v_mov_b32 v225, s47\nbuffer_store_dword v225, v19, s[8:11], 0 offen sc1\ns_add_u32 s47, s40, 2\nv_mov_b32 v221, s47\n"
"ds_write_b32 v222, v221\nv_mfma_f32_16x16x32_bf16 v[132:135], v[84:87], v[116:119], 0\nv_mfma_f32_16x16x32_bf16 v[136:139], v[88:91], v[116:119], 0\nv_mfma_f32_16x16x32_bf16 v[148:151], v[176:179], v[116:119], 0\n"
"v_mfma_f32_16x16x32_bf16 v[140:143], v[92:95], v[120:123], 0\nv_mfma_f32_16x16x32_bf16 v[144:147], v[96:99], v[120:123], 0\nv_mfma_f32_16x16x32_bf16 v[148:151], v[180:183], v[120:123], v[148:151]\nv_cndmask_b32 v132, v136, v132, s[62:63]\n"
"v_cndmask_b32 v133, v137, v133, s[62:63]\nv_cndmask_b32 v134, v138, v134, s[62:63]\nv_cndmask_b32 v135, v139, v135, s[62:63]\ns_nop 0\n"
"v_cndmask_b32 v140, v144, v140, s[62:63]\nv_cndmask_b32 v141, v145, v141, s[62:63]\nv_cndmask_b32 v142, v146, v142, s[62:63]\nv_cndmask_b32 v143, v147, v143, s[62:63]\n"
"v_cvt_pk_bf16_f32 v200, v152, v153\nv_cvt_pk_bf16_f32 v201, v154, v155\nv_and_b32 v160, v200, v223\nv_and_b32 v161, v200, v224\n"
"v_mfma_f32_16x16x16_bf16 v[148:151], v[214:215], v[200:201], v[148:151]\nv_and_b32 v162, v201, v223\nv_and_b32 v163, v201, v224\nv_mfma_f32_16x16x16_bf16 v[132:135], v[156:157], v[160:161], v[132:135]\n"
"s_nop 0\nv_mfma_f32_16x16x16_bf16 v[140:143], v[158:159], v[162:163], v[140:143]\nv_rcp_f32 v202, v148\nv_rcp_f32 v203, v149\n"
"v_mul_f32 v132, v132, v202\nv_mul_f32 v133, v133, v202\nv_mul_f32 v134, v134, v202\nv_mul_f32 v135, v135, v202\n"
"v_fmac_f32 v132, v140, v203\nv_fmac_f32 v133, v141, v203\nv_fmac_f32 v134, v142, v203\nv_fmac_f32 v135, v143, v203\n"
"v_add_f32_dpp v204, v132, v132 row_ror:8 row_mask:0xf bank_mask:0x3\nv_add_f32_dpp v205, v133, v133 row_ror:8 row_mask:0xf bank_mask:0x3\nv_add_f32_dpp v204, v134, v134 row_ror:8 row_mask:0xf bank_mask:0xc\nv_add_f32_dpp v205, v135, v135 row_ror:8 row_mask:0xf bank_mask:0xc\n"
"ds_write_b64 v22, v[204:205]\ns_branch join_p2\nend_p2:\ns_branch fk_done\n"
"fk_p3:\ns_mov_b32 s40, 0\ns_mov_b32 s41, 0\ns_mov_b32 s42, 262144\n"
"s_mov_b32 s43, 393216\ns_mov_b32 s44, s28\ns_mov_b32 s45, 0\ns_mov_b32 s46, 0\n"
"s_mov_b32 s92, 0\ns_mov_b32 s49, 0\ns_mov_b32 s53, 0\ns_mov_b32 s50, s35\n"
"s_mov_b32 s51, s36\nv_mov_b32 v222, 24576\ns_mov_b32 s62, 0x00ff00ff\ns_mov_b32 s63, 0x00ff00ff\n"
"v_cndmask_b32 v223, 0, -1, s[62:63]\nv_not_b32 v224, v223\nv_bfe_u32 v219, v9, 4, 1\nv_lshlrev_b32 v220, 4, v219\n"
"v_mov_b32 v218, 0x3c00\nv_lshlrev_b32 v218, v220, v218\nv_bfe_u32 v220, v9, 5, 3\nv_cmp_eq_u32 vcc, 0, v220\n"
"s_nop 1\nv_cndmask_b32 v168, 0, v218, vcc\nv_cmp_eq_u32 vcc, 2, v220\ns_nop 1\n"
"v_cndmask_b32 v169, 0, v218, vcc\nv_cmp_eq_u32 vcc, 4, v220\ns_nop 1\nv_cndmask_b32 v170, 0, v218, vcc\n"
"v_cmp_eq_u32 vcc, 6, v220\ns_nop 1\nv_cndmask_b32 v171, 0, v218, vcc\nv_cmp_eq_u32 vcc, 1, v220\n"
"s_nop 1\nv_cndmask_b32 v172, 0, v218, vcc\nv_cmp_eq_u32 vcc, 3, v220\ns_nop 1\n"
"v_cndmask_b32 v173, 0, v218, vcc\nv_cmp_eq_u32 vcc, 5, v220\ns_nop 1\nv_cndmask_b32 v174, 0, v218, vcc\n"
"v_cmp_eq_u32 vcc, 7, v220\ns_nop 1\nv_cndmask_b32 v175, 0, v218, vcc\nv_mov_b32 v218, 0x3f803f80\n"
"v_cmp_eq_u32 vcc, 0, v219\ns_nop 1\nv_cndmask_b32 v176, 0, v218, vcc\nv_cndmask_b32 v177, 0, v218, vcc\n"
"v_cndmask_b32 v178, 0, v218, vcc\nv_cndmask_b32 v179, 0, v218, vcc\nv_cndmask_b32 v214, 0, v218, vcc\nv_cmp_eq_u32 vcc, 1, v219\n"
"s_nop 1\nv_cndmask_b32 v180, 0, v218, vcc\nv_cndmask_b32 v181, 0, v218, vcc\nv_cndmask_b32 v182, 0, v218, vcc\n"
"v_cndmask_b32 v183, 0, v218, vcc\nv_cndmask_b32 v215, 0, v218, vcc\ns_add_u32 s47, s34, 28672\nbuffer_load_dwordx4 a[0:3], v9, s[12:15], s47 offen sc1\n"
"buffer_load_dwordx4 a[4:7], v9, s[12:15], s47 offen offset:1024 sc1\ns_add_u32 s47, s34, 30720\nbuffer_load_dwordx4 a[8:11], v9, s[12:15], s47 offen sc1\nbuffer_load_dwordx4 a[12:15], v9, s[12:15], s47 offen offset:1024 sc1\n"
"s_add_u32 s47, s34, 32768\nbuffer_load_dwordx4 a[16:19], v9, s[12:15], s47 offen sc1\nbuffer_load_dwordx4 a[20:23], v9, s[12:15], s47 offen offset:1024 sc1\ns_add_u32 s47, s34, 34816\n"
"buffer_load_dwordx4 a[24:27], v9, s[12:15], s47 offen sc1\nbuffer_load_dwordx4 a[28:31], v9, s[12:15], s47 offen offset:1024 sc1\ns_add_u32 s47, s34, 36864\nbuffer_load_dwordx4 a[32:35], v9, s[12:15], s47 offen sc1\n"
"buffer_load_dwordx4 a[36:39], v9, s[12:15], s47 offen offset:1024 sc1\ns_add_u32 s47, s34, 38912\nbuffer_load_dwordx4 a[40:43], v9, s[12:15], s47 offen sc1\nbuffer_load_dwordx4 a[44:47], v9, s[12:15], s47 offen offset:1024 sc1\n"
"s_add_u32 s47, s34, 40960\nbuffer_load_dwordx4 a[48:51], v9, s[12:15], s47 offen sc1\nbuffer_load_dwordx4 a[52:55], v9, s[12:15], s47 offen offset:1024 sc1\ns_add_u32 s47, s34, 43008\n"
"buffer_load_dwordx4 a[56:59], v9, s[12:15], s47 offen sc1\nbuffer_load_dwordx4 a[60:63], v9, s[12:15], s47 offen offset:1024 sc1\ns_add_u32 s47, s34, 45056\nbuffer_load_dwordx4 a[64:67], v9, s[12:15], s47 offen sc1\n"
"buffer_load_dwordx4 a[68:71], v9, s[12:15], s47 offen offset:1024 sc1\ns_add_u32 s47, s34, 47104\nbuffer_load_dwordx4 a[72:75], v9, s[12:15], s47 offen sc1\nbuffer_load_dwordx4 a[76:79], v9, s[12:15], s47 offen offset:1024 sc1\n"
"s_add_u32 s47, s34, 49152\nbuffer_load_dwordx4 a[80:83], v9, s[12:15], s47 offen sc1\nbuffer_load_dwordx4 a[84:87], v9, s[12:15], s47 offen offset:1024 sc1\ns_add_u32 s47, s34, 51200\n"
"buffer_load_dwordx4 a[88:91], v9, s[12:15], s47 offen sc1\nbuffer_load_dwordx4 a[92:95], v9, s[12:15], s47 offen offset:1024 sc1\ns_add_u32 s47, s34, 53248\nbuffer_load_dwordx4 a[96:99], v9, s[12:15], s47 offen sc1\n"
"buffer_load_dwordx4 a[100:103], v9, s[12:15], s47 offen offset:1024 sc1\ns_add_u32 s47, s34, 55296\nbuffer_load_dwordx4 a[104:107], v9, s[12:15], s47 offen sc1\nbuffer_load_dwordx4 a[108:111], v9, s[12:15], s47 offen offset:1024 sc1\n"
"s_add_u32 s47, s34, 0\nbuffer_load_dwordx4 a[112:115], v9, s[12:15], s47 offen sc1\nbuffer_load_dwordx4 a[116:119], v9, s[12:15], s47 offen offset:1024 sc1\ns_add_u32 s47, s34, 2048\n"
"buffer_load_dwordx4 a[120:123], v9, s[12:15], s47 offen sc1\nbuffer_load_dwordx4 a[124:127], v9, s[12:15], s47 offen offset:1024 sc1\ns_add_u32 s47, s34, 4096\nbuffer_load_dwordx4 a[128:131], v9, s[12:15], s47 offen sc1\n"
"buffer_load_dwordx4 a[132:135], v9, s[12:15], s47 offen offset:1024 sc1\ns_add_u32 s47, s34, 6144\nbuffer_load_dwordx4 a[136:139], v9, s[12:15], s47 offen sc1\nbuffer_load_dwordx4 a[140:143], v9, s[12:15], s47 offen offset:1024 sc1\n"
"s_add_u32 s47, s34, 8192\nbuffer_load_dwordx4 a[144:147], v9, s[12:15], s47 offen sc1\nbuffer_load_dwordx4 a[148:151], v9, s[12:15], s47 offen offset:1024 sc1\ns_add_u32 s47, s34, 10240\n"
"buffer_load_dwordx4 a[152:155], v9, s[12:15], s47 offen sc1\nbuffer_load_dwordx4 a[156:159], v9, s[12:15], s47 offen offset:1024 sc1\ns_add_u32 s47, s34, 12288\nbuffer_load_dwordx4 a[160:163], v9, s[12:15], s47 offen sc1\n"
"buffer_load_dwordx4 a[164:167], v9, s[12:15], s47 offen offset:1024 sc1\ns_add_u32 s47, s34, 14336\nbuffer_load_dwordx4 a[168:171], v9, s[12:15], s47 offen sc1\nbuffer_load_dwordx4 a[172:175], v9, s[12:15], s47 offen offset:1024 sc1\n"
"s_add_u32 s47, s34, 16384\nbuffer_load_dwordx4 a[176:179], v9, s[12:15], s47 offen sc1\nbuffer_load_dwordx4 a[180:183], v9, s[12:15], s47 offen offset:1024 sc1\ns_add_u32 s47, s34, 18432\n"
"buffer_load_dwordx4 a[184:187], v9, s[12:15], s47 offen sc1\nbuffer_load_dwordx4 a[188:191], v9, s[12:15], s47 offen offset:1024 sc1\ns_add_u32 s47, s34, 20480\nbuffer_load_dwordx4 a[192:195], v9, s[12:15], s47 offen sc1\n"
"buffer_load_dwordx4 a[196:199], v9, s[12:15], s47 offen offset:1024 sc1\ns_add_u32 s47, s34, 22528\nbuffer_load_dwordx4 a[200:203], v9, s[12:15], s47 offen sc1\nbuffer_load_dwordx4 a[204:207], v9, s[12:15], s47 offen offset:1024 sc1\n"
"s_add_u32 s47, s34, 24576\nbuffer_load_dwordx4 a[208:211], v9, s[12:15], s47 offen sc1\nbuffer_load_dwordx4 a[212:215], v9, s[12:15], s47 offen offset:1024 sc1\ns_add_u32 s47, s34, 26624\n"
"buffer_load_dwordx4 a[216:219], v9, s[12:15], s47 offen sc1\nbuffer_load_dwordx4 a[220:223], v9, s[12:15], s47 offen offset:1024 sc1\nv_lshlrev_b32 v166, 2, v9\ns_mov_b32 s52, 1064960\n"
"buffer_load_dword v164, v166, s[8:11], s52 offen sc1\ns_add_u32 s52, s52, 4096\nbuffer_load_dword v165, v166, s[8:11], s52 offen sc1\nbuffer_load_dword v218, v11, s[8:11], 0 offen sc1\n"
"s_mov_b64 s[94:95], 1\ns_barrier\ns_waitcnt vmcnt(0)\nar_p3:\n"
"v_xor_b32 v164, s33, v164\nv_xor_b32 v165, s33, v165\nv_or_b32 v164, v164, v165\nv_cmp_ne_u32 vcc, 0, v164\n"
"s_cbranch_vccz arok_p3\ns_add_u32 s53, s53, 0\ns_add_u32 s49, s49, 1\ns_cmp_gt_u32 s49, 65536\n"
"s_cbranch_scc1 end_p3\ns_sleep 2\nv_lshlrev_b32 v166, 2, v9\ns_mov_b32 s52, 1064960\n"
"buffer_load_dword v164, v166, s[8:11], s52 offen sc1\ns_add_u32 s52, s52, 4096\nbuffer_load_dword v165, v166, s[8:11], s52 offen sc1\ns_waitcnt vmcnt(0)\n"
"s_branch ar_p3\narok_p3:\nfl_p3:\ns_add_u32 s47, s40, 2\n"
"s_min_u32 s47, s47, 64\ns_add_u32 s47, s47, s33\nv_subrev_u32 v218, s47, v218\nv_cmp_lt_u32 vcc, 64, v218\n"
"s_cbranch_vccz flok_p3\ns_add_u32 s53, s53, 0\ns_add_u32 s49, s49, 1\ns_cmp_gt_u32 s49, 65536\n"
"s_cbranch_scc1 end_p3\ns_sleep 2\nbuffer_load_dword v218, v11, s[8:11], 0 offen sc1\ns_waitcnt vmcnt(0)\n"
"s_branch fl_p3\nflok_p3:\nbuffer_load_dwordx4 v[100:103], v9, s[16:19], s50 offen offset:2048 sc1\nbuffer_load_dwordx4 v[104:107], v9, s[16:19], s50 offen offset:3072 sc1\n"
"buffer_load_dwordx4 v[108:111], v9, s[16:19], s50 offen offset:0 sc1\nbuffer_load_dwordx4 v[112:115], v9, s[16:19], s50 offen offset:1024 sc1\ns_add_u32 s50, s50, 16384\ns_memrealtime s[54:55]\n"
"s_waitcnt lgkmcnt(0)\ns_add_u32 s44, s54, 12\ns_mov_b32 s45, 8\n.p2align 6\n"
"top_p3:\nv_mov_b32 v68, 0\nv_mov_b32 v69, 0\nv_mov_b32 v70, 0\n"
"v_mov_b32 v71, 0\nv_mov_b32 v72, 0\nv_mov_b32 v73, 0\nv_mov_b32 v74, 0\n"
"v_mov_b32 v75, 0\nv_mov_b32 v76, 0\nv_mov_b32 v77, 0\nv_mov_b32 v78, 0\n"
"v_mov_b32 v79, 0\nv_mov_b32 v80, 0\nv_mov_b32 v81, 0\nv_mov_b32 v82, 0\n"
"v_mov_b32 v83, 0\nv_mov_b32 v218, 0\ns_add_u32 s52, s44, s37\nm_p3:\n"
"s_memrealtime s[54:55]\ns_waitcnt lgkmcnt(0)\ns_sub_u32 s47, s54, s52\ns_cmp_lt_i32 s47, 0\n"
"s_cbranch_scc1 m_p3\ntop2_p3:\nbuffer_load_dwordx4 v[64:67], v10, s[8:11], s43 offen offset:0 sc1\ns_and_b32 s47, s46, 15\n"
"s_mul_i32 s47, s47, 4\ns_add_u32 s47, s47, s29\ns_add_u32 s47, s47, s45\ns_and_b32 s91, s47, 15\n"
"s_lshr_b32 s47, s47, 4\ns_lshl_b32 s47, s47, 2\ns_add_u32 s90, s44, s47\ns_mul_i32 s48, s91, 6\n"
"s_and_b32 s48, s48, -4\ns_getpc_b64 s[60:61]\nspc_p3:\ns_sub_u32 s48, sgo_p3-spc_p3, s48\n"
"s_add_u32 s60, s60, s48\ns_addc_u32 s61, s61, 0\ns_waitcnt vmcnt(0)\nds_write_b128 v9, v[64:67] offset:6144\n"
"v_max3_u32 v218, v218, v64, v65\nv_max3_u32 v218, v218, v66, v67\nv_cmp_eq_u32 vcc, -1, v218\nv_smfmac_f32_16x16x64_bf16 v[68:71], v[64:67], a[48:55], v8\n"
"v_smfmac_f32_16x16x64_bf16 v[72:75], v[64:67], a[104:111], v8\ns_cbranch_vccnz retry1_p3\ns_waitcnt lgkmcnt(0)\ns_barrier\n"
"ds_read_b32 v221, v222\nds_read_b128 v[40:43], v9 offset:0\nds_read_b128 v[44:47], v9 offset:1024\nds_read_b128 v[48:51], v9 offset:2048\n"
"ds_read_b128 v[52:55], v9 offset:3072\nds_read_b128 v[56:59], v9 offset:4096\nds_read_b128 v[60:63], v9 offset:5120\nv_smfmac_f32_16x16x64_bf16 v[76:79], v[64:67], a[160:167], v8\n"
"v_smfmac_f32_16x16x64_bf16 v[80:83], v[64:67], a[216:223], v8\ns_waitcnt lgkmcnt(5)\nv_smfmac_f32_16x16x64_bf16 v[68:71], v[40:43], a[0:7], v8\nv_smfmac_f32_16x16x64_bf16 v[72:75], v[40:43], a[56:63], v8\n"
"v_smfmac_f32_16x16x64_bf16 v[76:79], v[40:43], a[112:119], v8\nv_smfmac_f32_16x16x64_bf16 v[80:83], v[40:43], a[168:175], v8\ns_waitcnt lgkmcnt(0)\nv_readfirstlane_b32 s47, v221\n"
"s_sub_u32 s47, s47, s40\ns_cmp_eq_u32 s47, 1\ns_cbranch_scc1 adopt_p3\nback_p3:\n"
"s_cmp_eq_u32 s40, 63\ns_cbranch_scc1 last_p3\nsf_p3:\ns_memrealtime s[54:55]\n"
"s_nop 2\ns_memrealtime s[56:57]\ns_nop 2\ns_memrealtime s[58:59]\n"
"s_waitcnt lgkmcnt(4)\ns_sub_u32 s47, s54, s90\ns_add_u32 s48, s47, 12\ns_cmp_ge_u32 s48, 12\n"
"s_cbranch_scc1 so_p3\ns_memrealtime s[54:55]\ns_waitcnt lgkmcnt(4)\ns_cmp_ge_u32 s56, s90\n"
"s_cbranch_scc1 sx_p3\ns_memrealtime s[56:57]\ns_waitcnt lgkmcnt(4)\ns_cmp_ge_u32 s58, s90\n"
"s_cbranch_scc1 sx_p3\ns_memrealtime s[58:59]\ns_waitcnt lgkmcnt(4)\ns_cmp_ge_u32 s54, s90\n"
"s_cbranch_scc1 sx_p3\ns_memrealtime s[54:55]\ns_waitcnt lgkmcnt(4)\ns_cmp_ge_u32 s56, s90\n"
"s_cbranch_scc1 sx_p3\ns_memrealtime s[56:57]\ns_waitcnt lgkmcnt(4)\ns_cmp_ge_u32 s58, s90\n"
"s_cbranch_scc1 sx_p3\ns_memrealtime s[58:59]\ns_waitcnt lgkmcnt(4)\ns_cmp_ge_u32 s54, s90\n"
"s_cbranch_scc1 sx_p3\ns_memrealtime s[54:55]\ns_waitcnt lgkmcnt(4)\ns_cmp_ge_u32 s56, s90\n"
"s_cbranch_scc1 sx_p3\ns_memrealtime s[56:57]\ns_waitcnt lgkmcnt(4)\ns_cmp_ge_u32 s58, s90\n"
"s_cbranch_scc1 sx_p3\ns_memrealtime s[58:59]\ns_waitcnt lgkmcnt(4)\ns_cmp_ge_u32 s54, s90\n"
"s_cbranch_scc1 sx_p3\ns_memrealtime s[54:55]\ns_waitcnt lgkmcnt(4)\ns_cmp_ge_u32 s56, s90\n"
"s_cbranch_scc1 sx_p3\ns_memrealtime s[56:57]\ns_waitcnt lgkmcnt(4)\ns_cmp_ge_u32 s58, s90\n"
"s_cbranch_scc1 sx_p3\ns_memrealtime s[58:59]\ns_waitcnt lgkmcnt(4)\ns_cmp_ge_u32 s54, s90\n"
"s_cbranch_scc1 sx_p3\ns_memrealtime s[54:55]\ns_waitcnt lgkmcnt(4)\ns_cmp_ge_u32 s56, s90\n"
"s_cbranch_scc1 sx_p3\ns_memrealtime s[56:57]\ns_waitcnt lgkmcnt(4)\ns_cmp_ge_u32 s58, s90\n"
"s_cbranch_scc1 sx_p3\ns_memrealtime s[58:59]\ns_waitcnt lgkmcnt(4)\ns_cmp_ge_u32 s54, s90\n"
"s_cbranch_scc1 sx_p3\ns_memrealtime s[54:55]\ns_waitcnt lgkmcnt(4)\ns_cmp_ge_u32 s56, s90\n"
"s_cbranch_scc1 sx_p3\ns_memrealtime s[56:57]\ns_branch sx_p3\nso_p3:\n"
"s_waitcnt lgkmcnt(0)\ns_cmp_lt_i32 s47, 0\ns_cbranch_scc1 sf_p3\ns_cmp_gt_i32 s47, s32\n"
"s_cbranch_scc1 late_p3\ns_branch sgo_p3\nsx_p3:\ns_setpc_b64 s[60:61]\n"
"s_nop 0\ns_nop 0\ns_nop 0\ns_nop 0\n"
"s_nop 0\ns_nop 0\ns_nop 0\ns_nop 0\n"
"s_nop 0\ns_nop 0\ns_nop 0\ns_nop 0\n"
"s_nop 0\ns_nop 0\ns_nop 0\ns_nop 0\n"
"s_nop 0\ns_nop 0\ns_nop 0\ns_nop 0\n"
"s_nop 0\ns_nop 0\ns_nop 0\ns_nop 0\n"
"sgo_p3:\ns_nop 15\ns_nop 1\nbuffer_load_dwordx4 v[116:119], v20, s[8:11], s41 offen offset:1024 sc1\n"
"buffer_load_dwordx4 v[120:123], v20, s[8:11], s41 offen offset:0 sc1\nv_smfmac_f32_16x16x64_bf16 v[68:71], v[44:47], a[8:15], v8\nv_mov_b32 v84, v100\nv_smfmac_f32_16x16x64_bf16 v[72:75], v[44:47], a[64:71], v8\n"
"v_mov_b32 v85, v101\nv_smfmac_f32_16x16x64_bf16 v[76:79], v[44:47], a[120:127], v8\nv_mov_b32 v86, v102\nv_smfmac_f32_16x16x64_bf16 v[80:83], v[44:47], a[176:183], v8\n"
"v_mov_b32 v87, v103\nv_smfmac_f32_16x16x64_bf16 v[68:71], v[48:51], a[16:23], v8\nv_mov_b32 v88, v104\nv_smfmac_f32_16x16x64_bf16 v[72:75], v[48:51], a[72:79], v8\n"
"v_mov_b32 v89, v105\nv_smfmac_f32_16x16x64_bf16 v[76:79], v[48:51], a[128:135], v8\nv_mov_b32 v90, v106\nv_smfmac_f32_16x16x64_bf16 v[80:83], v[48:51], a[184:191], v8\n"
"v_mov_b32 v91, v107\nv_smfmac_f32_16x16x64_bf16 v[68:71], v[52:55], a[24:31], v8\nv_mov_b32 v92, v108\nv_smfmac_f32_16x16x64_bf16 v[72:75], v[52:55], a[80:87], v8\n"
"v_mov_b32 v93, v109\nbuffer_load_dwordx4 v[124:127], v21, s[8:11], s41 offen offset:1024 sc1\nbuffer_load_dwordx4 v[128:131], v21, s[8:11], s41 offen offset:0 sc1\nv_smfmac_f32_16x16x64_bf16 v[76:79], v[52:55], a[136:143], v8\n"
"v_mov_b32 v94, v110\nv_smfmac_f32_16x16x64_bf16 v[80:83], v[52:55], a[192:199], v8\nv_mov_b32 v95, v111\nv_smfmac_f32_16x16x64_bf16 v[68:71], v[56:59], a[32:39], v8\n"
"v_mov_b32 v96, v112\nv_smfmac_f32_16x16x64_bf16 v[72:75], v[56:59], a[88:95], v8\nv_mov_b32 v97, v113\nv_smfmac_f32_16x16x64_bf16 v[76:79], v[56:59], a[144:151], v8\n"
"v_mov_b32 v98, v114\nv_smfmac_f32_16x16x64_bf16 v[80:83], v[56:59], a[200:207], v8\nv_mov_b32 v99, v115\nv_smfmac_f32_16x16x64_bf16 v[68:71], v[60:63], a[40:47], v8\n"
"v_smfmac_f32_16x16x64_bf16 v[72:75], v[60:63], a[96:103], v8\nv_smfmac_f32_16x16x64_bf16 v[76:79], v[60:63], a[152:159], v8\nv_smfmac_f32_16x16x64_bf16 v[80:83], v[60:63], a[208:215], v8\nv_add_f32 v219, v68, v69\n"
"v_add_f32 v220, v70, v71\nv_cvt_pk_bf16_f32 v156, v219, v220\nv_add_f32 v219, v72, v73\nv_add_f32 v220, v74, v75\n"
"v_cvt_pk_bf16_f32 v157, v219, v220\nv_add_f32 v219, v76, v77\nv_add_f32 v220, v78, v79\nv_cvt_pk_bf16_f32 v158, v219, v220\n"
"v_add_f32 v219, v80, v81\nv_add_f32 v220, v82, v83\nv_cvt_pk_bf16_f32 v159, v219, v220\nbuffer_load_dwordx4 v[100:103], v9, s[16:19], s50 offen offset:2048 sc1\n"
"buffer_load_dwordx4 v[104:107], v9, s[16:19], s50 offen offset:3072 sc1\nbuffer_load_dwordx4 v[108:111], v9, s[16:19], s50 offen offset:0 sc1\nbuffer_load_dwordx4 v[112:115], v9, s[16:19], s50 offen offset:1024 sc1\ns_add_u32 s50, s50, 16384\n"
"buffer_store_dwordx4 v[156:159], v9, s[20:23], s51 offen sc1\ns_waitcnt vmcnt(8)\nv_mfma_f32_16x16x32_bf16 v[132:135], v[84:87], v[116:119], 0\nv_mfma_f32_16x16x32_bf16 v[136:139], v[88:91], v[116:119], 0\n"
"v_mfma_f32_16x16x32_bf16 v[148:151], v[176:179], v[116:119], 0\ns_waitcnt vmcnt(7)\nv_mfma_f32_16x16x32_bf16 v[140:143], v[92:95], v[120:123], 0\nv_mfma_f32_16x16x32_bf16 v[144:147], v[96:99], v[120:123], 0\n"
"v_mfma_f32_16x16x32_bf16 v[148:151], v[180:183], v[120:123], v[148:151]\nv_cndmask_b32 v132, v136, v132, s[62:63]\nv_cndmask_b32 v133, v137, v133, s[62:63]\nv_cndmask_b32 v134, v138, v134, s[62:63]\n"
"v_cndmask_b32 v135, v139, v135, s[62:63]\ns_waitcnt vmcnt(6)\nv_mfma_f32_16x16x32_f16 v[152:155], v[168:171], v[124:127], 0\ns_waitcnt vmcnt(5)\n"
"v_mfma_f32_16x16x32_f16 v[152:155], v[172:175], v[128:131], v[152:155]\nv_cndmask_b32 v140, v144, v140, s[62:63]\nv_cndmask_b32 v141, v145, v141, s[62:63]\nv_cndmask_b32 v142, v146, v142, s[62:63]\n"
"v_cndmask_b32 v143, v147, v143, s[62:63]\ns_nop 2\nv_exp_f32 v152, v152\nv_exp_f32 v153, v153\n"
"v_exp_f32 v154, v154\nv_exp_f32 v155, v155\nv_cvt_pk_bf16_f32 v200, v152, v153\nv_cvt_pk_bf16_f32 v201, v154, v155\n"
"v_and_b32 v160, v200, v223\nv_and_b32 v161, v200, v224\nv_mfma_f32_16x16x16_bf16 v[148:151], v[214:215], v[200:201], v[148:151]\nv_and_b32 v162, v201, v223\n"
"v_and_b32 v163, v201, v224\nv_mfma_f32_16x16x16_bf16 v[132:135], v[156:157], v[160:161], v[132:135]\ns_nop 0\nv_mfma_f32_16x16x16_bf16 v[140:143], v[158:159], v[162:163], v[140:143]\n"
"v_rcp_f32 v202, v148\nv_rcp_f32 v203, v149\nv_mul_f32 v132, v132, v202\nv_mul_f32 v133, v133, v202\n"
"v_mul_f32 v134, v134, v202\nv_mul_f32 v135, v135, v202\nv_fmac_f32 v132, v140, v203\nv_fmac_f32 v133, v141, v203\n"
"v_fmac_f32 v134, v142, v203\nv_fmac_f32 v135, v143, v203\nv_add_f32_dpp v204, v132, v132 row_ror:8 row_mask:0xf bank_mask:0x3\nv_add_f32_dpp v205, v133, v133 row_ror:8 row_mask:0xf bank_mask:0x3\n"
"v_add_f32_dpp v204, v134, v134 row_ror:8 row_mask:0xf bank_mask:0xc\nv_add_f32_dpp v205, v135, v135 row_ror:8 row_mask:0xf bank_mask:0xc\nv_subrev_u32 v219, 0x2f800000, v148\nv_subrev_u32 v220, 0x2f800000, v149\n"
"v_max_u32 v219, v219, v220\nv_cmp_le_u32 vcc, 0x20000000, v219\nds_write_b64 v22, v[204:205]\ns_or_b64 s[56:57], vcc, s[94:95]\n"
"s_cbranch_scc1 slow_p3\njoin_p3:\nv_mov_b32 v219, 0\ns_nop 1\n"
"v_mov_b32 v219, 0\ns_waitcnt lgkmcnt(0)\ns_barrier\nds_read_b64 v[206:207], v23 offset:0\n"
"ds_read_b64 v[208:209], v23 offset:1280\nds_read_b64 v[210:211], v23 offset:2560\nds_read_b64 v[212:213], v23 offset:3840\nds_read_b32 v221, v222\n"
"v_mov_b32 v219, 0\ns_nop 1\nv_mov_b32 v219, 0\ns_nop 1\n"
"v_mov_b32 v219, 0\ns_waitcnt lgkmcnt(3)\nv_pk_add_f32 v[206:207], v[206:207], v[208:209]\ns_waitcnt lgkmcnt(1)\n"
"v_pk_add_f32 v[210:211], v[210:211], v[212:213]\nv_pk_add_f32 v[206:207], v[206:207], v[210:211]\nv_cvt_pk_bf16_f32 v220, v206, v207\nbuffer_store_dword v220, v16, s[8:11], s41 offen sc1\n"
"s_add_u32 s51, s51, 4096\ns_add_u32 s40, s40, 1\ns_mov_b32 s43, s41\ns_add_u32 s41, s41, 131072\n"
"s_and_b32 s41, s41, 524287\ns_xor_b32 s42, s41, 262144\ns_and_b32 s47, s46, 15\ns_mul_i32 s48, s47, 8\n"
"s_add_u32 s45, s45, s48\ns_add_u32 s45, s45, s30\ns_lshr_b32 s48, s45, 4\ns_lshl_b32 s48, s48, 2\n"
"s_add_u32 s44, s44, s48\ns_and_b32 s45, s45, 15\ns_add_u32 s92, s92, 1\ns_waitcnt lgkmcnt(0)\n"
"s_cmp_eq_u32 s40, 1\ns_cbranch_scc1 one_p3\nv_readfirstlane_b32 s47, v221\ns_sub_u32 s47, s47, s40\n"
"s_cmp_eq_u32 s47, 1\ns_cbranch_scc1 rec_p3\ns_branch top_p3\none_p3:\n"
"buffer_load_dword v218, v11, s[8:11], 0 offen sc1\ns_waitcnt vmcnt(0)\nf1_p3:\ns_add_u32 s47, s40, 2\n"
"s_min_u32 s47, s47, 64\ns_add_u32 s47, s47, s33\nv_subrev_u32 v218, s47, v218\nv_cmp_lt_u32 vcc, 64, v218\n"
"s_cbranch_vccz f1ok_p3\ns_add_u32 s53, s53, 0\ns_add_u32 s49, s49, 1\ns_cmp_gt_u32 s49, 65536\n"
"s_cbranch_scc1 end_p3\ns_sleep 2\nbuffer_load_dword v218, v11, s[8:11], 0 offen sc1\ns_waitcnt vmcnt(0)\n"
"s_branch f1_p3\nf1ok_p3:\ns_branch top_p3\nlast_p3:\n"
"v_smfmac_f32_16x16x64_bf16 v[68:71], v[44:47], a[8:15], v8\nv_smfmac_f32_16x16x64_bf16 v[72:75], v[44:47], a[64:71], v8\nv_smfmac_f32_16x16x64_bf16 v[76:79], v[44:47], a[120:127], v8\nv_smfmac_f32_16x16x64_bf16 v[80:83], v[44:47], a[176:183], v8\n"
"v_smfmac_f32_16x16x64_bf16 v[68:71], v[48:51], a[16:23], v8\nv_smfmac_f32_16x16x64_bf16 v[72:75], v[48:51], a[72:79], v8\nv_smfmac_f32_16x16x64_bf16 v[76:79], v[48:51], a[128:135], v8\nv_smfmac_f32_16x16x64_bf16 v[80:83], v[48:51], a[184:191], v8\n"
"v_smfmac_f32_16x16x64_bf16 v[68:71], v[52:55], a[24:31], v8\nv_smfmac_f32_16x16x64_bf16 v[72:75], v[52:55], a[80:87], v8\nv_smfmac_f32_16x16x64_bf16 v[76:79], v[52:55], a[136:143], v8\nv_smfmac_f32_16x16x64_bf16 v[80:83], v[52:55], a[192:199], v8\n"
"v_smfmac_f32_16x16x64_bf16 v[68:71], v[56:59], a[32:39], v8\nv_smfmac_f32_16x16x64_bf16 v[72:75], v[56:59], a[88:95], v8\nv_smfmac_f32_16x16x64_bf16 v[76:79], v[56:59], a[144:151], v8\nv_smfmac_f32_16x16x64_bf16 v[80:83], v[56:59], a[200:207], v8\n"
"v_smfmac_f32_16x16x64_bf16 v[68:71], v[60:63], a[40:47], v8\nv_smfmac_f32_16x16x64_bf16 v[72:75], v[60:63], a[96:103], v8\nv_smfmac_f32_16x16x64_bf16 v[76:79], v[60:63], a[152:159], v8\nv_smfmac_f32_16x16x64_bf16 v[80:83], v[60:63], a[208:215], v8\n"
"v_add_f32 v219, v68, v69\nv_add_f32 v220, v70, v71\nv_cvt_pk_bf16_f32 v156, v219, v220\nv_add_f32 v219, v72, v73\n"
"v_add_f32 v220, v74, v75\nv_cvt_pk_bf16_f32 v157, v219, v220\nv_add_f32 v219, v76, v77\nv_add_f32 v220, v78, v79\n"
"v_cvt_pk_bf16_f32 v158, v219, v220\nv_add_f32 v219, v80, v81\nv_add_f32 v220, v82, v83\nv_cvt_pk_bf16_f32 v159, v219, v220\n"
"buffer_store_dwordx4 v[156:159], v9, s[20:23], s51 offen sc1\ns_add_u32 s47, s33, 64\nv_mov_b32 v219, s47\ns_waitcnt vmcnt(0)\n"
"buffer_store_dword v219, v18, s[8:11], 0 offen sc1\ns_branch end_p3\nretry1_p3:\ns_add_u32 s53, s53, 256\n"
"s_add_u32 s49, s49, 1\ns_cmp_gt_u32 s49, 65536\ns_cbranch_scc1 end_p3\ns_add_u32 s47, s40, 1\n"
"v_mov_b32 v221, s47\nds_write_b32 v222, v221\nrec_p3:\ns_sleep 4\n"
"v_mov_b32 v68, 0\nv_mov_b32 v69, 0\nv_mov_b32 v70, 0\nv_mov_b32 v71, 0\n"
"v_mov_b32 v72, 0\nv_mov_b32 v73, 0\nv_mov_b32 v74, 0\nv_mov_b32 v75, 0\n"
"v_mov_b32 v76, 0\nv_mov_b32 v77, 0\nv_mov_b32 v78, 0\nv_mov_b32 v79, 0\n"
"v_mov_b32 v80, 0\nv_mov_b32 v81, 0\nv_mov_b32 v82, 0\nv_mov_b32 v83, 0\n"
"v_mov_b32 v218, 0\ns_branch top2_p3\nadopt_p3:\nbuffer_load_dwordx4 v[164:167], v28, s[8:11], 0 offen sc1\n"
"s_lshl_b32 s52, s40, 26\ns_or_b32 s47, s52, 0x3ffffff\ns_waitcnt vmcnt(0)\nv_cmp_ge_u32 vcc, s47, v164\n"
"s_nop 1\nv_cndmask_b32 v164, 0, v164, vcc\nv_cmp_ge_u32 vcc, s47, v165\ns_nop 1\n"
"v_cndmask_b32 v165, 0, v165, vcc\nv_cmp_ge_u32 vcc, s47, v166\ns_nop 1\nv_cndmask_b32 v166, 0, v166, vcc\n"
"v_cmp_ge_u32 vcc, s47, v167\ns_nop 1\nv_cndmask_b32 v167, 0, v167, vcc\nv_max3_u32 v221, v164, v165, v166\n"
"v_max_u32 v221, v221, v167\ns_nop 1\nv_max_u32_dpp v221, v221, v221 row_ror:8 row_mask:0xf bank_mask:0xf\ns_nop 1\n"
"v_max_u32_dpp v221, v221, v221 row_ror:4 row_mask:0xf bank_mask:0xf\ns_nop 1\nv_max_u32_dpp v221, v221, v221 row_ror:2 row_mask:0xf bank_mask:0xf\ns_nop 1\n"
"v_max_u32_dpp v221, v221, v221 row_ror:1 row_mask:0xf bank_mask:0xf\ns_nop 0\nv_readlane_b32 s47, v221, 0\nv_readlane_b32 s48, v221, 16\n"
"s_max_u32 s48, s48, s47\ns_cmp_le_u32 s48, s52\ns_cbranch_scc1 back2_p3\ns_bfe_u32 s52, s48, 1179656\n"
"s_add_u32 s44, s52, s28\ns_mov_b32 s45, 0\ns_and_b32 s46, s48, 255\ns_mov_b32 s92, 0\n"
"s_branch back2_p3\nback2_p3:\ns_and_b32 s47, s46, 15\ns_mul_i32 s47, s47, 4\n"
"s_add_u32 s47, s47, s29\ns_add_u32 s47, s47, s45\ns_and_b32 s91, s47, 15\ns_lshr_b32 s47, s47, 4\n"
"s_lshl_b32 s47, s47, 2\ns_add_u32 s90, s44, s47\ns_mul_i32 s48, s91, 6\ns_and_b32 s48, s48, -4\n"
"s_getpc_b64 s[60:61]\nspc2_p3:\ns_sub_u32 s48, sgo_p3-spc2_p3, s48\ns_add_u32 s60, s60, s48\n"
"s_addc_u32 s61, s61, -1\ns_branch back_p3\nlate_p3:\ns_add_u32 s53, s53, 16777216\n"
"s_mov_b64 s[94:95], 1\ns_add_u32 s52, s90, 20\nlw_p3:\ns_memrealtime s[54:55]\n"
"s_waitcnt lgkmcnt(0)\ns_sub_u32 s47, s54, s52\ns_cmp_lt_i32 s47, 0\ns_cbranch_scc1 lw_p3\n"
"s_branch sgo_p3\nslow_p3:\ns_mov_b64 s[94:95], 0\ns_cbranch_vccnz slow2_p3\n"
"s_memrealtime s[54:55]\ns_add_u32 s52, s40, 1\ns_lshl_b32 s52, s52, 26\ns_waitcnt lgkmcnt(0)\n"
"s_sub_u32 s47, s54, s28\ns_add_u32 s47, s47, s31\ns_min_u32 s47, s47, 0x3ffff\ns_lshl_b32 s47, s47, 8\n"
"s_or_b32 s47, s47, s52\ns_sub_u32 s52, s92, 1\ns_cmp_lt_u32 s52, 6\ns_cselect_b32 s48, 1, 0\n"
"s_cmp_ge_u32 s40, 5\ns_cselect_b32 s48, s48, 0\ns_add_u32 s48, s48, s46\ns_min_u32 s48, s48, 15\n"
"s_or_b32 s47, s47, s48\nv_mov_b32 v225, s47\nbuffer_store_dword v225, v19, s[8:11], 0 offen sc1\ns_add_u32 s47, s40, 2\n"
"v_mov_b32 v221, s47\nds_write_b32 v222, v221\ns_branch join_p3\nslow2_p3:\n"
"v_max3_u32 v218, v124, v125, v126\nv_max_u32 v218, v218, v127\nv_max3_u32 v218, v218, v128, v129\nv_max3_u32 v218, v218, v130, v131\n"
"v_max3_u32 v218, v218, v116, v117\nv_max3_u32 v218, v218, v118, v119\nv_max3_u32 v218, v218, v120, v121\nv_max3_u32 v218, v218, v122, v123\n"
"v_cmp_eq_u32 vcc, -1, v218\ns_cbranch_vccz exact_p3\ns_branch spin_p3\nspin2_p3:\n"
"s_sleep 3\nspin_p3:\ns_add_u32 s53, s53, 1\ns_add_u32 s49, s49, 1\n"
"s_cmp_gt_u32 s49, 65536\ns_cbranch_scc1 end_p3\nbuffer_load_dwordx4 v[116:119], v20, s[8:11], s41 offen offset:1024 sc1\nbuffer_load_dwordx4 v[120:123], v20, s[8:11], s41 offen offset:0 sc1\n"
"buffer_load_dwordx4 v[124:127], v21, s[8:11], s41 offen offset:1024 sc1\nbuffer_load_dwordx4 v[128:131], v21, s[8:11], s41 offen offset:0 sc1\ns_waitcnt vmcnt(0)\nv_max3_u32 v218, v124, v125, v126\n"
"v_max_u32 v218, v218, v127\nv_max3_u32 v218, v218, v128, v129\nv_max3_u32 v218, v218, v130, v131\nv_max3_u32 v218, v218, v116, v117\n"
"v_max3_u32 v218, v218, v118, v119\nv_max3_u32 v218, v218, v120, v121\nv_max3_u32 v218, v218, v122, v123\nv_cmp_eq_u32 vcc, -1, v218\n"
"s_cbranch_vccnz spin2_p3\ns_memrealtime s[54:55]\ns_add_u32 s52, s40, 1\ns_lshl_b32 s52, s52, 26\n"
"s_waitcnt lgkmcnt(0)\ns_sub_u32 s47, s54, s28\ns_add_u32 s47, s47, s38\ns_min_u32 s47, s47, 0x3ffff\n"
"s_lshl_b32 s47, s47, 8\ns_or_b32 s47, s47, s52\ns_sub_u32 s52, s92, 1\ns_cmp_lt_u32 s52, 6\n"
"s_cselect_b32 s48, 1, 0\ns_cmp_ge_u32 s40, 5\ns_cselect_b32 s48, s48, 0\ns_add_u32 s48, s48, s46\n"
"s_min_u32 s48, s48, 15\ns_or_b32 s47, s47, s48\nv_mov_b32 v225, s47\nbuffer_store_dword v225, v19, s[8:11], 0 offen sc1\n"
"s_add_u32 s47, s40, 2\nv_mov_b32 v221, s47\nds_write_b32 v222, v221\nv_mfma_f32_16x16x32_bf16 v[132:135], v[84:87], v[116:119], 0\n"
"v_mfma_f32_16x16x32_bf16 v[136:139], v[88:91], v[116:119], 0\nv_mfma_f32_16x16x32_bf16 v[148:151], v[176:179], v[116:119], 0\nv_mfma_f32_16x16x32_bf16 v[140:143], v[92:95], v[120:123], 0\nv_mfma_f32_16x16x32_bf16 v[144:147], v[96:99], v[120:123], 0\n"
"v_mfma_f32_16x16x32_bf16 v[148:151], v[180:183], v[120:123], v[148:151]\nv_cndmask_b32 v132, v136, v132, s[62:63]\nv_cndmask_b32 v133, v137, v133, s[62:63]\nv_cndmask_b32 v134, v138, v134, s[62:63]\n"
"v_cndmask_b32 v135, v139, v135, s[62:63]\nv_mfma_f32_16x16x32_f16 v[152:155], v[168:171], v[124:127], 0\nv_mfma_f32_16x16x32_f16 v[152:155], v[172:175], v[128:131], v[152:155]\nv_cndmask_b32 v140, v144, v140, s[62:63]\n"
"v_cndmask_b32 v141, v145, v141, s[62:63]\nv_cndmask_b32 v142, v146, v142, s[62:63]\nv_cndmask_b32 v143, v147, v143, s[62:63]\ns_nop 2\n"
"v_exp_f32 v152, v152\nv_exp_f32 v153, v153\nv_exp_f32 v154, v154\nv_exp_f32 v155, v155\n"
"v_cvt_pk_bf16_f32 v200, v152, v153\nv_cvt_pk_bf16_f32 v201, v154, v155\nv_and_b32 v160, v200, v223\nv_and_b32 v161, v200, v224\n"
"v_mfma_f32_16x16x16_bf16 v[148:151], v[214:215], v[200:201], v[148:151]\nv_and_b32 v162, v201, v223\nv_and_b32 v163, v201, v224\nv_mfma_f32_16x16x16_bf16 v[132:135], v[156:157], v[160:161], v[132:135]\n"
"s_nop 0\nv_mfma_f32_16x16x16_bf16 v[140:143], v[158:159], v[162:163], v[140:143]\nv_rcp_f32 v202, v148\nv_rcp_f32 v203, v149\n"
"v_mul_f32 v132, v132, v202\nv_mul_f32 v133, v133, v202\nv_mul_f32 v134, v134, v202\nv_mul_f32 v135, v135, v202\n"
"v_fmac_f32 v132, v140, v203\nv_fmac_f32 v133, v141, v203\nv_fmac_f32 v134, v142, v203\nv_fmac_f32 v135, v143, v203\n"
"v_add_f32_dpp v204, v132, v132 row_ror:8 row_mask:0xf bank_mask:0x3\nv_add_f32_dpp v205, v133, v133 row_ror:8 row_mask:0xf bank_mask:0x3\nv_add_f32_dpp v204, v134, v134 row_ror:8 row_mask:0xf bank_mask:0xc\nv_add_f32_dpp v205, v135, v135 row_ror:8 row_mask:0xf bank_mask:0xc\n"
"v_subrev_u32 v219, 0x2f800000, v148\nv_subrev_u32 v220, 0x2f800000, v149\nv_max_u32 v219, v219, v220\nv_cmp_le_u32 vcc, 0x20000000, v219\n"
"ds_write_b64 v22, v[204:205]\ns_cbranch_vccz join_p3\nexact_p3:\ns_add_u32 s53, s53, 65536\n"
"ex0_p3:\ns_add_u32 s47, s41, 4096\nbuffer_load_dwordx2 v[184:185], v24, s[8:11], s47 offen offset:0 sc1\nbuffer_load_dwordx2 v[186:187], v24, s[8:11], s47 offen offset:512 sc1\n"
"buffer_load_dwordx2 v[188:189], v24, s[8:11], s47 offen offset:1024 sc1\nbuffer_load_dwordx2 v[190:191], v24, s[8:11], s47 offen offset:1536 sc1\nbuffer_load_dwordx4 v[192:195], v25, s[8:11], s47 offen sc1\nbuffer_load_dwordx4 v[196:199], v25, s[8:11], s47 offen offset:16 sc1\n"
"v_mov_b32 v218, 0\ns_waitcnt vmcnt(0)\nv_max3_u32 v218, v218, v184, v185\nv_max3_u32 v218, v218, v186, v187\n"
"v_max3_u32 v218, v218, v188, v189\nv_max3_u32 v218, v218, v190, v191\nv_max3_u32 v218, v218, v192, v193\nv_max3_u32 v218, v218, v194, v195\n"
"v_max3_u32 v218, v218, v196, v197\nv_max3_u32 v218, v218, v198, v199\nv_cmp_eq_u32 vcc, -1, v218\ns_cbranch_vccz exok0_p3\n"
"s_add_u32 s53, s53, 0\ns_add_u32 s49, s49, 1\ns_cmp_gt_u32 s49, 65536\ns_cbranch_scc1 end_p3\n"
"s_branch ex0_p3\nexok0_p3:\nv_add_f32 v184, v184, v186\nv_add_f32 v188, v188, v190\n"
"v_add_f32 v184, v184, v188\nv_add_f32 v185, v185, v187\nv_add_f32 v189, v189, v191\nv_add_f32 v185, v185, v189\n"
"v_max_f32 v225, v184, v185\nv_max3_f32 v225, v225, v192, v193\nv_max3_f32 v225, v225, v194, v195\nv_max3_f32 v225, v225, v196, v197\n"
"v_max3_f32 v225, v225, v198, v199\nv_mov_b32 v219, v225\ns_nop 1\nv_permlane16_swap_b32 v225, v219\n"
"v_max_f32 v225, v225, v219\nv_mov_b32 v219, v225\ns_nop 1\nv_permlane32_swap_b32 v225, v219\n"
"v_max_f32 v225, v225, v219\nv_sub_f32 v184, v184, v225\nv_exp_f32 v152, v184\nv_sub_f32 v185, v185, v225\n"
"v_exp_f32 v153, v185\nv_sub_f32 v192, v192, v225\nv_exp_f32 v192, v192\nv_sub_f32 v193, v193, v225\n"
"v_exp_f32 v193, v193\nv_sub_f32 v194, v194, v225\nv_exp_f32 v194, v194\nv_sub_f32 v195, v195, v225\n"
"v_exp_f32 v195, v195\nv_sub_f32 v196, v196, v225\nv_exp_f32 v196, v196\nv_sub_f32 v197, v197, v225\n"
"v_exp_f32 v197, v197\nv_sub_f32 v198, v198, v225\nv_exp_f32 v198, v198\nv_sub_f32 v199, v199, v225\n"
"v_exp_f32 v199, v199\ns_nop 0\nv_cvt_pk_bf16_f32 v116, v192, v193\nv_cvt_pk_bf16_f32 v117, v194, v195\n"
"v_cvt_pk_bf16_f32 v118, v196, v197\nv_cvt_pk_bf16_f32 v119, v198, v199\nex1_p3:\ns_add_u32 s47, s41, 0\n"
"buffer_load_dwordx2 v[184:185], v24, s[8:11], s47 offen offset:0 sc1\nbuffer_load_dwordx2 v[186:187], v24, s[8:11], s47 offen offset:512 sc1\nbuffer_load_dwordx2 v[188:189], v24, s[8:11], s47 offen offset:1024 sc1\nbuffer_load_dwordx2 v[190:191], v24, s[8:11], s47 offen offset:1536 sc1\n"
"buffer_load_dwordx4 v[192:195], v25, s[8:11], s47 offen sc1\nbuffer_load_dwordx4 v[196:199], v25, s[8:11], s47 offen offset:16 sc1\nv_mov_b32 v218, 0\ns_waitcnt vmcnt(0)\n"
"v_max3_u32 v218, v218, v184, v185\nv_max3_u32 v218, v218, v186, v187\nv_max3_u32 v218, v218, v188, v189\nv_max3_u32 v218, v218, v190, v191\n"
"v_max3_u32 v218, v218, v192, v193\nv_max3_u32 v218, v218, v194, v195\nv_max3_u32 v218, v218, v196, v197\nv_max3_u32 v218, v218, v198, v199\n"
"v_cmp_eq_u32 vcc, -1, v218\ns_cbranch_vccz exok1_p3\ns_add_u32 s53, s53, 0\ns_add_u32 s49, s49, 1\n"
"s_cmp_gt_u32 s49, 65536\ns_cbranch_scc1 end_p3\ns_branch ex1_p3\nexok1_p3:\n"
"v_add_f32 v184, v184, v186\nv_add_f32 v188, v188, v190\nv_add_f32 v184, v184, v188\nv_add_f32 v185, v185, v187\n"
"v_add_f32 v189, v189, v191\nv_add_f32 v185, v185, v189\nv_max_f32 v225, v184, v185\nv_max3_f32 v225, v225, v192, v193\n"
"v_max3_f32 v225, v225, v194, v195\nv_max3_f32 v225, v225, v196, v197\nv_max3_f32 v225, v225, v198, v199\nv_mov_b32 v219, v225\n"
"s_nop 1\nv_permlane16_swap_b32 v225, v219\nv_max_f32 v225, v225, v219\nv_mov_b32 v219, v225\n"
"s_nop 1\nv_permlane32_swap_b32 v225, v219\nv_max_f32 v225, v225, v219\nv_sub_f32 v184, v184, v225\n"
"v_exp_f32 v154, v184\nv_sub_f32 v185, v185, v225\nv_exp_f32 v155, v185\nv_sub_f32 v192, v192, v225\n"
"v_exp_f32 v192, v192\nv_sub_f32 v193, v193, v225\nv_exp_f32 v193, v193\nv_sub_f32 v194, v194, v225\n"
"v_exp_f32 v194, v194\nv_sub_f32 v195, v195, v225\nv_exp_f32 v195, v195\nv_sub_f32 v196, v196, v225\n"
"v_exp_f32 v196, v196\nv_sub_f32 v197, v197, v225\nv_exp_f32 v197, v197\nv_sub_f32 v198, v198, v225\n"
"v_exp_f32 v198, v198\nv_sub_f32 v199, v199, v225\nv_exp_f32 v199, v199\ns_nop 0\n"
"v_cvt_pk_bf16_f32 v120, v192, v193\nv_cvt_pk_bf16_f32 v121, v194, v195\nv_cvt_pk_bf16_f32 v122, v196, v197\nv_cvt_pk_bf16_f32 v123, v198, v199\n"
"s_memrealtime s[54:55]\ns_add_u32 s52, s40, 1\ns_lshl_b32 s52, s52, 26\ns_waitcnt lgkmcnt(0)\n"
"s_sub_u32 s47, s54, s28\ns_add_u32 s47, s47, s38\ns_min_u32 s47, s47, 0x3ffff\ns_lshl_b32 s47, s47, 8\n"
"s_or_b32 s47, s47, s52\ns_sub_u32 s52, s92, 1\ns_cmp_lt_u32 s52, 6\ns_cselect_b32 s48, 1, 0\n"
"s_cmp_ge_u32 s40, 5\ns_cselect_b32 s48, s48, 0\ns_add_u32 s48, s48, s46\ns_min_u32 s48, s48, 15\n"
"s_or_b32 s47, s47, s48\nv_mov_b32 v225, s47\nbuffer_store_dword v225, v19, s[8:11], 0 offen sc1\ns_add_u32 s47, s40, 2\n"
"v_mov_b32 v221, s47\nds_write_b32 v222, v221\nv_mfma_f32_16x16x32_bf16 v[132:135], v[84:87], v[116:119], 0\nv_mfma_f32_16x16x32_bf16 v[136:139], v[88:91], v[116:119], 0\n"
"v_mfma_f32_16x16x32_bf16 v[148:151], v[176:179], v[116:119], 0\nv_mfma_f32_16x16x32_bf16 v[140:143], v[92:95], v[120:123], 0\nv_mfma_f32_16x16x32_bf16 v[144:147], v[96:99], v[120:123], 0\nv_mfma_f32_16x16x32_bf16 v[148:151], v[180:183], v[120:123], v[148:151]\n"
"v_cndmask_b32 v132, v136, v132, s[62:63]\nv_cndmask_b32 v133, v137, v133, s[62:63]\nv_cndmask_b32 v134, v138, v134, s[62:63]\nv_cndmask_b32 v135, v139, v135, s[62:63]\n"
"s_nop 0\nv_cndmask_b32 v140, v144, v140, s[62:63]\nv_cndmask_b32 v141, v145, v141, s[62:63]\nv_cndmask_b32 v142, v146, v142, s[62:63]\n"
"v_cndmask_b32 v143, v147, v143, s[62:63]\nv_cvt_pk_bf16_f32 v200, v152, v153\nv_cvt_pk_bf16_f32 v201, v154, v155\nv_and_b32 v160, v200, v223\n"
"v_and_b32 v161, v200, v224\nv_mfma_f32_16x16x16_bf16 v[148:151], v[214:215], v[200:201], v[148:151]\nv_and_b32 v162, v201, v223\nv_and_b32 v163, v201, v224\n"
"v_mfma_f32_16x16x16_bf16 v[132:135], v[156:157], v[160:161], v[132:135]\ns_nop 0\nv_mfma_f32_16x16x16_bf16 v[140:143], v[158:159], v[162:163], v[140:143]\nv_rcp_f32 v202, v148\n"
"v_rcp_f32 v203, v149\nv_mul_f32 v132, v132, v202\nv_mul_f32 v133, v133, v202\nv_mul_f32 v134, v134, v202\n"
"v_mul_f32 v135, v135, v202\nv_fmac_f32 v132, v140, v203\nv_fmac_f32 v133, v141, v203\nv_fmac_f32 v134, v142, v203\n"
"v_fmac_f32 v135, v143, v203\nv_add_f32_dpp v204, v132, v132 row_ror:8 row_mask:0xf bank_mask:0x3\nv_add_f32_dpp v205, v133, v133 row_ror:8 row_mask:0xf bank_mask:0x3\nv_add_f32_dpp v204, v134, v134 row_ror:8 row_mask:0xf bank_mask:0xc\n"
"v_add_f32_dpp v205, v135, v135 row_ror:8 row_mask:0xf bank_mask:0xc\nds_write_b64 v22, v[204:205]\ns_branch join_p3\nend_p3:\n"
"s_branch fk_done\nfk_done:\ns_endpgm\n"
    :: "{s[8:23]}"(s0), "{s[24:39]}"(s1), "{v[8:23]}"(v0), "{v[24:39]}"(v1) : "v40", "v41", "v42", "v43", "v44", "v45", "v46", "v47", "v48", "v49", "v50", "v51", "v52", "v53", "v54", "v55", "v56", "v57", "v58", "v59", "v60", "v61", "v62", "v63", "v64", "v65", "v66", "v67", "v68", "v69", "v70", "v71", "v72", "v73", "v74", "v75", "v76", "v77", "v78", "v79", "v80", "v81", "v82", "v83", "v84", "v85", "v86", "v87", "v88", "v89", "v90", "v91", "v92", "v93", "v94", "v95", "v96", "v97", "v98", "v99", "v100", "v101", "v102", "v103", "v104", "v105", "v106", "v107", "v108", "v109", "v110", "v111", "v112", "v113", "v114", "v115", "v116", "v117", "v118", "v119", "v120", "v121", "v122", "v123", "v124", "v125", "v126", "v127", "v128", "v129", "v130", "v131", "v132", "v133", "v134", "v135", "v136", "v137", "v138", "v139", "v140", "v141", "v142", "v143", "v144", "v145", "v146", "v147", "v148", "v149", "v150", "v151", "v152", "v153", "v154", "v155", "v156", "v157", "v158", "v159", "v160", "v161", "v162", "v163", "v164", "v165", "v166", "v167", "v168", "v169", "v170", "v171", "v172", "v173", "v174", "v175", "v176", "v177", "v178", "v179", "v180", "v181", "v182", "v183", "v184", "v185", "v186", "v187", "v188", "v189", "v190", "v191", "v192", "v193", "v194", "v195", "v196", "v197", "v198", "v199", "v200", "v201", "v202", "v203", "v204", "v205", "v206", "v207", "v208", "v209", "v210", "v211", "v212", "v213", "v214", "v215", "v216", "v217", "v218", "v219", "v220", "v221", "v222", "v223", "v224", "v225", "v226", "v227", "v228", "v229", "v230", "v231", "v232", "v233", "v234", "v235", "v236", "v237", "v238", "v239", "v240", "v241", "v242", "v243", "v244", "v245", "v246", "v247", "v248", "v249", "v250", "v251", "v252", "v253", "v254", "v255", "a0", "a1", "a2", "a3", "a4", "a5", "a6", "a7", "a8", "a9", "a10", "a11", "a12", "a13", "a14", "a15", "a16", "a17", "a18", "a19", "a20", "a21", "a22", "a23", "a24", "a25", "a26", "a27", "a28", "a29", "a30", "a31", "a32", "a33", "a34", "a35", "a36", "a37", "a38", "a39", "a40", "a41", "a42", "a43", "a44", "a45", "a46", "a47", "a48", "a49", "a50", "a51", "a52", "a53", "a54", "a55", "a56", "a57", "a58", "a59", "a60", "a61", "a62", "a63", "a64", "a65", "a66", "a67", "a68", "a69", "a70", "a71", "a72", "a73", "a74", "a75", "a76", "a77", "a78", "a79", "a80", "a81", "a82", "a83", "a84", "a85", "a86", "a87", "a88", "a89", "a90", "a91", "a92", "a93", "a94", "a95", "a96", "a97", "a98", "a99", "a100", "a101", "a102", "a103", "a104", "a105", "a106", "a107", "a108", "a109", "a110", "a111", "a112", "a113", "a114", "a115", "a116", "a117", "a118", "a119", "a120", "a121", "a122", "a123", "a124", "a125", "a126", "a127", "a128", "a129", "a130", "a131", "a132", "a133", "a134", "a135", "a136", "a137", "a138", "a139", "a140", "a141", "a142", "a143", "a144", "a145", "a146", "a147", "a148", "a149", "a150", "a151", "a152", "a153", "a154", "a155", "a156", "a157", "a158", "a159", "a160", "a161", "a162", "a163", "a164", "a165", "a166", "a167", "a168", "a169", "a170", "a171", "a172", "a173", "a174", "a175", "a176", "a177", "a178", "a179", "a180", "a181", "a182", "a183", "a184", "a185", "a186", "a187", "a188", "a189", "a190", "a191", "a192", "a193", "a194", "a195", "a196", "a197", "a198", "a199", "a200", "a201", "a202", "a203", "a204", "a205", "a206", "a207", "a208", "a209", "a210", "a211", "a212", "a213", "a214", "a215", "a216", "a217", "a218", "a219", "a220", "a221", "a222", "a223", "a224", "a225", "a226", "a227", "a228", "a229", "a230", "a231", "a232", "a233", "a234", "a235", "a236", "a237", "a238", "a239", "a240", "a241", "a242", "a243", "a244", "a245", "a246", "a247", "a248", "a249", "a250", "a251", "a252", "a253", "a254", "a255", "s40", "s41", "s42", "s43", "s44", "s45", "s46", "s47", "s48", "s49", "s50", "s51", "s52", "s53", "s54", "s55", "s56", "s57", "s58", "s59", "s60", "s61", "s62", "s63", "s64", "s65", "s66", "s67", "s68", "s69", "s70", "s71", "s72", "s73", "s74", "s75", "s76", "s77", "s78", "s79", "s80", "s81", "s82", "s83", "s84", "s85", "s86", "s87", "s88", "s89", "s90", "s91", "s92", "s93", "s94", "s95", "vcc", "scc", "memory");
    __builtin_unreachable();
}
struct GCtx { u4 rw1, rx, rexp, rout, rhb; unsigned tag, rot, sw1, mw, mx, sfg, stay, csc, ostr, kb, p1, p2; unsigned vr0, vr1, vx, wa, xa, vexp, vimp, vo0, vo1, vo2, vo3, vfl, vpf, vpo, vk, p5; };
DI unsigned gemm_asm0(const GCtx& c) {
    const u16v s0 = {RFL(c.rw1[0]), RFL(c.rw1[1]), RFL(c.rw1[2]), RFL(c.rw1[3]), RFL(c.rx[0]), RFL(c.rx[1]), RFL(c.rx[2]), RFL(c.rx[3]), RFL(c.rexp[0]), RFL(c.rexp[1]), RFL(c.rexp[2]), RFL(c.rexp[3]), RFL(c.rout[0]), RFL(c.rout[1]), RFL(c.rout[2]), RFL(c.rout[3])}, s1 = {RFL(c.rhb[0]), RFL(c.rhb[1]), RFL(c.rhb[2]), RFL(c.rhb[3]), RFL(c.tag), RFL(c.rot), RFL(c.sw1), RFL(c.mw), RFL(c.mx), RFL(c.sfg), RFL(c.stay), RFL(c.csc), RFL(c.ostr), RFL(c.kb), RFL(c.p1), RFL(c.p2)}, v0 = {c.vr0, c.vr1, c.vx, c.wa, c.xa, c.vexp, c.vimp, c.vo0, c.vo1, c.vo2, c.vo3, c.vfl, c.vpf, c.vpo, c.vk, c.p5};
    unsigned o = 0;
    asm volatile(
"s_nop 4\ns_add_u32 s40, s29, 0\ns_add_u32 s41, s29, 1\ns_add_u32 s42, s29, 2\n"
"s_add_u32 s43, s29, 3\nv_xor_b32 v24, 0, v11\nv_xor_b32 v25, 64, v11\nv_xor_b32 v26, 128, v11\n"
"v_xor_b32 v27, 192, v11\nv_xor_b32 v28, 2064, v11\nv_xor_b32 v29, 2128, v11\nv_xor_b32 v30, 2192, v11\n"
"v_xor_b32 v31, 2256, v11\nv_add_u32 v40, s37, v24\nv_add_u32 v41, s37, v25\nv_add_u32 v42, s37, v26\n"
"v_add_u32 v43, s37, v27\nv_add_u32 v44, s37, v28\nv_add_u32 v45, s37, v29\nv_add_u32 v46, s37, v30\n"
"v_add_u32 v47, s37, v31\nds_read_b128 a[128:131], v40 offset:0\nds_read_b128 a[132:135], v41 offset:0\nds_read_b128 a[136:139], v42 offset:0\n"
"ds_read_b128 a[140:143], v43 offset:0\ns_lshl_b32 s53, s41, 19\ns_add_u32 s53, s53, s30\ns_add_u32 m0, s31, 65536\n"
"s_add_u32 s53, s53, 0\nbuffer_load_dwordx4 v8, s[8:11], s53 offen lds\nbuffer_load_dwordx4 v9, s[8:11], s53 offen offset:1024 lds\nds_read_b128 a[144:147], v40 offset:256\n"
"ds_read_b128 a[148:151], v41 offset:256\nds_read_b128 a[152:155], v42 offset:256\nds_read_b128 a[156:159], v43 offset:256\ns_lshl_b32 s53, s41, 19\n"
"s_add_u32 s53, s53, s30\ns_add_u32 m0, s31, 73728\ns_add_u32 s53, s53, 131072\nbuffer_load_dwordx4 v8, s[8:11], s53 offen lds\n"
"buffer_load_dwordx4 v9, s[8:11], s53 offen offset:1024 lds\ns_waitcnt lgkmcnt(0)\nds_read_b128 a[160:163], v44 offset:0\nds_read_b128 a[164:167], v45 offset:0\n"
"ds_read_b128 a[168:171], v46 offset:0\nds_read_b128 a[172:175], v47 offset:0\ns_lshl_b32 s53, s41, 19\ns_add_u32 s53, s53, s30\n"
"s_add_u32 m0, s31, 81920\ns_add_u32 s53, s53, 262144\nbuffer_load_dwordx4 v8, s[8:11], s53 offen lds\nbuffer_load_dwordx4 v9, s[8:11], s53 offen offset:1024 lds\n"
"ds_read_b128 a[176:179], v44 offset:256\nds_read_b128 a[180:183], v45 offset:256\nds_read_b128 a[184:187], v46 offset:256\nds_read_b128 a[188:191], v47 offset:256\n"
"s_lshl_b32 s53, s41, 19\ns_add_u32 s53, s53, s30\ns_add_u32 m0, s31, 90112\ns_add_u32 s53, s53, 393216\n"
"buffer_load_dwordx4 v8, s[8:11], s53 offen lds\nbuffer_load_dwordx4 v9, s[8:11], s53 offen offset:1024 lds\ns_waitcnt lgkmcnt(0)\nv_add_u32 v40, 65536, v24\n"
"v_add_u32 v41, 65536, v25\nv_add_u32 v42, 65536, v26\nv_add_u32 v43, 65536, v27\nv_add_u32 v44, 65536, v28\n"
"v_add_u32 v45, 65536, v29\nv_add_u32 v46, 65536, v30\nv_add_u32 v47, 65536, v31\nv_xor_b32 v32, 0, v12\n"
"v_xor_b32 v33, 2048, v12\nv_xor_b32 v34, 64, v12\nv_xor_b32 v35, 2112, v12\ns_mov_b32 s55, 0\n"
"s_waitcnt vmcnt(8)\ns_barrier\nds_read_b128 v[96:99], v24 offset:0\nds_read_b128 v[100:103], v28 offset:0\n"
"ds_read_b128 v[104:107], v25 offset:0\nds_read_b128 v[108:111], v29 offset:0\nds_read_b128 v[112:115], v26 offset:0\nds_read_b128 v[116:119], v30 offset:0\n"
"ds_read_b128 v[120:123], v27 offset:0\nds_read_b128 v[124:127], v31 offset:0\ns_waitcnt lgkmcnt(6)\nv_mfma_f32_16x16x32_bf16 v[64:67], v[96:99], a[128:131], 0\n"
"s_lshl_b32 s53, s42, 19\ns_add_u32 s53, s53, s30\nv_mfma_f32_16x16x32_bf16 v[68:71], v[96:99], a[160:163], 0\nds_read_b128 v[96:99], v24 offset:256\n"
"s_add_u32 m0, s31, 98304\ns_add_u32 s53, s53, 0\nv_mfma_f32_16x16x32_bf16 v[72:75], v[100:103], a[128:131], 0\nbuffer_load_dwordx4 v8, s[8:11], s53 offen lds\n"
"v_mfma_f32_16x16x32_bf16 v[76:79], v[100:103], a[160:163], 0\nds_read_b128 v[100:103], v28 offset:256\nbuffer_load_dwordx4 v9, s[8:11], s53 offen offset:1024 lds\ns_waitcnt lgkmcnt(6)\n"
"v_mfma_f32_16x16x32_bf16 v[64:67], v[104:107], a[132:135], v[64:67]\ns_lshl_b32 s53, s42, 19\ns_add_u32 s53, s53, s30\nv_mfma_f32_16x16x32_bf16 v[68:71], v[104:107], a[164:167], v[68:71]\n"
"ds_read_b128 v[104:107], v25 offset:256\ns_add_u32 m0, s31, 106496\ns_add_u32 s53, s53, 131072\nv_mfma_f32_16x16x32_bf16 v[72:75], v[108:111], a[132:135], v[72:75]\n"
"buffer_load_dwordx4 v8, s[8:11], s53 offen lds\nv_mfma_f32_16x16x32_bf16 v[76:79], v[108:111], a[164:167], v[76:79]\nds_read_b128 v[108:111], v29 offset:256\nbuffer_load_dwordx4 v9, s[8:11], s53 offen offset:1024 lds\n"
"s_waitcnt lgkmcnt(6)\nv_mfma_f32_16x16x32_bf16 v[64:67], v[112:115], a[136:139], v[64:67]\nv_mfma_f32_16x16x32_bf16 v[68:71], v[112:115], a[168:171], v[68:71]\nds_read_b128 v[112:115], v26 offset:256\n"
"v_mfma_f32_16x16x32_bf16 v[72:75], v[116:119], a[136:139], v[72:75]\nv_mfma_f32_16x16x32_bf16 v[76:79], v[116:119], a[168:171], v[76:79]\nds_read_b128 v[116:119], v30 offset:256\ns_waitcnt lgkmcnt(6)\n"
"v_mfma_f32_16x16x32_bf16 v[64:67], v[120:123], a[140:143], v[64:67]\nv_mfma_f32_16x16x32_bf16 v[68:71], v[120:123], a[172:175], v[68:71]\nds_read_b128 v[120:123], v27 offset:256\nv_mfma_f32_16x16x32_bf16 v[72:75], v[124:127], a[140:143], v[72:75]\n"
"v_mfma_f32_16x16x32_bf16 v[76:79], v[124:127], a[172:175], v[76:79]\nds_read_b128 v[124:127], v31 offset:256\ns_waitcnt lgkmcnt(6)\nv_mfma_f32_16x16x32_bf16 v[64:67], v[96:99], a[144:147], v[64:67]\n"
"v_mfma_f32_16x16x32_bf16 v[68:71], v[96:99], a[176:179], v[68:71]\nds_read_b128 v[96:99], v24 offset:16384\nv_mfma_f32_16x16x32_bf16 v[72:75], v[100:103], a[144:147], v[72:75]\nv_mfma_f32_16x16x32_bf16 v[76:79], v[100:103], a[176:179], v[76:79]\n"
"ds_read_b128 v[100:103], v28 offset:16384\ns_waitcnt lgkmcnt(6)\nv_mfma_f32_16x16x32_bf16 v[64:67], v[104:107], a[148:151], v[64:67]\nv_mfma_f32_16x16x32_bf16 v[68:71], v[104:107], a[180:183], v[68:71]\n"
"ds_read_b128 v[104:107], v25 offset:16384\nv_mfma_f32_16x16x32_bf16 v[72:75], v[108:111], a[148:151], v[72:75]\nv_mfma_f32_16x16x32_bf16 v[76:79], v[108:111], a[180:183], v[76:79]\nds_read_b128 v[108:111], v29 offset:16384\n"
"s_waitcnt lgkmcnt(6)\nv_mfma_f32_16x16x32_bf16 v[64:67], v[112:115], a[152:155], v[64:67]\nv_mfma_f32_16x16x32_bf16 v[68:71], v[112:115], a[184:187], v[68:71]\nds_read_b128 v[112:115], v26 offset:16384\n"
"v_mfma_f32_16x16x32_bf16 v[72:75], v[116:119], a[152:155], v[72:75]\nv_mfma_f32_16x16x32_bf16 v[76:79], v[116:119], a[184:187], v[76:79]\nds_read_b128 v[116:119], v30 offset:16384\ns_waitcnt lgkmcnt(6)\n"
"v_mfma_f32_16x16x32_bf16 v[64:67], v[120:123], a[156:159], v[64:67]\nv_mfma_f32_16x16x32_bf16 v[68:71], v[120:123], a[188:191], v[68:71]\nds_read_b128 v[120:123], v27 offset:16384\nv_mfma_f32_16x16x32_bf16 v[72:75], v[124:127], a[156:159], v[72:75]\n"
"v_mfma_f32_16x16x32_bf16 v[76:79], v[124:127], a[188:191], v[76:79]\nds_read_b128 v[124:127], v31 offset:16384\ns_waitcnt lgkmcnt(6)\nv_mfma_f32_16x16x32_bf16 v[80:83], v[96:99], a[128:131], 0\n"
"s_lshl_b32 s53, s42, 19\ns_add_u32 s53, s53, s30\nv_mfma_f32_16x16x32_bf16 v[84:87], v[96:99], a[160:163], 0\nds_read_b128 v[96:99], v24 offset:16640\n"
"s_add_u32 m0, s31, 114688\ns_add_u32 s53, s53, 262144\nv_mfma_f32_16x16x32_bf16 v[88:91], v[100:103], a[128:131], 0\nbuffer_load_dwordx4 v8, s[8:11], s53 offen lds\n"
"v_mfma_f32_16x16x32_bf16 v[92:95], v[100:103], a[160:163], 0\nds_read_b128 v[100:103], v28 offset:16640\nbuffer_load_dwordx4 v9, s[8:11], s53 offen offset:1024 lds\ns_waitcnt lgkmcnt(6)\n"
"v_mfma_f32_16x16x32_bf16 v[80:83], v[104:107], a[132:135], v[80:83]\nv_mul_f32 v64, s35, v64\nv_mul_f32 v65, s35, v65\nv_mfma_f32_16x16x32_bf16 v[84:87], v[104:107], a[164:167], v[84:87]\n"
"ds_read_b128 v[104:107], v25 offset:16640\nv_mul_f32 v66, s35, v66\nv_mul_f32 v67, s35, v67\nv_mfma_f32_16x16x32_bf16 v[88:91], v[108:111], a[132:135], v[88:91]\n"
"v_cvt_pk_bf16_f32 v128, v64, v65\nv_cvt_pk_bf16_f32 v129, v66, v67\nv_mfma_f32_16x16x32_bf16 v[92:95], v[108:111], a[164:167], v[92:95]\nds_read_b128 v[108:111], v29 offset:16640\n"
"v_mul_f32 v72, s35, v72\nv_mul_f32 v73, s35, v73\ns_waitcnt lgkmcnt(6)\nv_mfma_f32_16x16x32_bf16 v[80:83], v[112:115], a[136:139], v[80:83]\n"
"v_mul_f32 v74, s35, v74\nv_mul_f32 v75, s35, v75\nv_mfma_f32_16x16x32_bf16 v[84:87], v[112:115], a[168:171], v[84:87]\nds_read_b128 v[112:115], v26 offset:16640\n"
"v_cvt_pk_bf16_f32 v130, v72, v73\nv_cvt_pk_bf16_f32 v131, v74, v75\nv_mfma_f32_16x16x32_bf16 v[88:91], v[116:119], a[136:139], v[88:91]\nv_mul_f32 v68, s35, v68\n"
"v_mul_f32 v69, s35, v69\nv_mfma_f32_16x16x32_bf16 v[92:95], v[116:119], a[168:171], v[92:95]\nds_read_b128 v[116:119], v30 offset:16640\nv_mul_f32 v70, s35, v70\n"
"v_mul_f32 v71, s35, v71\ns_waitcnt lgkmcnt(6)\nv_mfma_f32_16x16x32_bf16 v[80:83], v[120:123], a[140:143], v[80:83]\nv_cvt_pk_bf16_f32 v132, v68, v69\n"
"v_cvt_pk_bf16_f32 v133, v70, v71\nv_mfma_f32_16x16x32_bf16 v[84:87], v[120:123], a[172:175], v[84:87]\nds_read_b128 v[120:123], v27 offset:16640\nv_mul_f32 v76, s35, v76\n"
"v_mul_f32 v77, s35, v77\nv_mfma_f32_16x16x32_bf16 v[88:91], v[124:127], a[140:143], v[88:91]\nv_mul_f32 v78, s35, v78\nv_mul_f32 v79, s35, v79\n"
"v_mfma_f32_16x16x32_bf16 v[92:95], v[124:127], a[172:175], v[92:95]\nds_read_b128 v[124:127], v31 offset:16640\nv_cvt_pk_bf16_f32 v134, v76, v77\nv_cvt_pk_bf16_f32 v135, v78, v79\n"
"s_waitcnt vmcnt(6)\ns_barrier\ns_waitcnt lgkmcnt(6)\nv_mfma_f32_16x16x32_bf16 v[80:83], v[96:99], a[144:147], v[80:83]\n"
"s_lshl_b32 s54, s40, 11\ns_add_u32 s54, s54, 0\nv_mfma_f32_16x16x32_bf16 v[84:87], v[96:99], a[176:179], v[84:87]\nds_read_b128 v[96:99], v24 offset:32768\n"
"buffer_store_dwordx4 v[128:131], v13, s[16:19], s54 offen sc1\nv_mfma_f32_16x16x32_bf16 v[88:91], v[100:103], a[144:147], v[88:91]\ns_add_u32 s54, s54, 14336\nv_mfma_f32_16x16x32_bf16 v[92:95], v[100:103], a[176:179], v[92:95]\n"
"ds_read_b128 v[100:103], v28 offset:32768\nbuffer_store_dwordx4 v[132:135], v13, s[16:19], s54 offen sc1\ns_waitcnt lgkmcnt(6)\nv_mfma_f32_16x16x32_bf16 v[80:83], v[104:107], a[148:151], v[80:83]\n"
"s_lshl_b32 s53, s42, 19\ns_add_u32 s53, s53, s30\nv_mfma_f32_16x16x32_bf16 v[84:87], v[104:107], a[180:183], v[84:87]\nds_read_b128 v[104:107], v25 offset:32768\n"
"s_add_u32 m0, s31, 122880\ns_add_u32 s53, s53, 393216\nv_mfma_f32_16x16x32_bf16 v[88:91], v[108:111], a[148:151], v[88:91]\nbuffer_load_dwordx4 v8, s[8:11], s53 offen lds\n"
"v_mfma_f32_16x16x32_bf16 v[92:95], v[108:111], a[180:183], v[92:95]\nds_read_b128 v[108:111], v29 offset:32768\nbuffer_load_dwordx4 v9, s[8:11], s53 offen offset:1024 lds\ns_waitcnt lgkmcnt(6)\n"
"v_mfma_f32_16x16x32_bf16 v[80:83], v[112:115], a[152:155], v[80:83]\nv_mfma_f32_16x16x32_bf16 v[84:87], v[112:115], a[184:187], v[84:87]\nds_read_b128 v[112:115], v26 offset:32768\nv_mfma_f32_16x16x32_bf16 v[88:91], v[116:119], a[152:155], v[88:91]\n"
"v_mfma_f32_16x16x32_bf16 v[92:95], v[116:119], a[184:187], v[92:95]\nds_read_b128 v[116:119], v30 offset:32768\ns_waitcnt lgkmcnt(6)\nv_mfma_f32_16x16x32_bf16 v[80:83], v[120:123], a[156:159], v[80:83]\n"
"v_mfma_f32_16x16x32_bf16 v[84:87], v[120:123], a[188:191], v[84:87]\nds_read_b128 v[120:123], v27 offset:32768\nv_mfma_f32_16x16x32_bf16 v[88:91], v[124:127], a[156:159], v[88:91]\nv_mfma_f32_16x16x32_bf16 v[92:95], v[124:127], a[188:191], v[92:95]\n"
"ds_read_b128 v[124:127], v31 offset:32768\ns_waitcnt lgkmcnt(6)\nv_mfma_f32_16x16x32_bf16 v[64:67], v[96:99], a[128:131], 0\ns_lshl_b32 s53, s43, 19\n"
"s_add_u32 s53, s53, s30\nv_mfma_f32_16x16x32_bf16 v[68:71], v[96:99], a[160:163], 0\nds_read_b128 v[96:99], v24 offset:33024\ns_add_u32 m0, s31, 131072\n"
"s_add_u32 s53, s53, 0\nv_mfma_f32_16x16x32_bf16 v[72:75], v[100:103], a[128:131], 0\nbuffer_load_dwordx4 v8, s[8:11], s53 offen lds\nv_mfma_f32_16x16x32_bf16 v[76:79], v[100:103], a[160:163], 0\n"
"ds_read_b128 v[100:103], v28 offset:33024\nbuffer_load_dwordx4 v9, s[8:11], s53 offen offset:1024 lds\ns_waitcnt lgkmcnt(6)\nv_mfma_f32_16x16x32_bf16 v[64:67], v[104:107], a[132:135], v[64:67]\n"
"v_mul_f32 v80, s35, v80\nv_mul_f32 v81, s35, v81\nv_mfma_f32_16x16x32_bf16 v[68:71], v[104:107], a[164:167], v[68:71]\nds_read_b128 v[104:107], v25 offset:33024\n"
"v_mul_f32 v82, s35, v82\nv_mul_f32 v83, s35, v83\nv_mfma_f32_16x16x32_bf16 v[72:75], v[108:111], a[132:135], v[72:75]\nv_cvt_pk_bf16_f32 v136, v80, v81\n"
"v_cvt_pk_bf16_f32 v137, v82, v83\nv_mfma_f32_16x16x32_bf16 v[76:79], v[108:111], a[164:167], v[76:79]\nds_read_b128 v[108:111], v29 offset:33024\nv_mul_f32 v88, s35, v88\n"
"v_mul_f32 v89, s35, v89\ns_waitcnt lgkmcnt(6)\nv_mfma_f32_16x16x32_bf16 v[64:67], v[112:115], a[136:139], v[64:67]\nv_mul_f32 v90, s35, v90\n"
"v_mul_f32 v91, s35, v91\nv_mfma_f32_16x16x32_bf16 v[68:71], v[112:115], a[168:171], v[68:71]\nds_read_b128 v[112:115], v26 offset:33024\nv_cvt_pk_bf16_f32 v138, v88, v89\n"
"v_cvt_pk_bf16_f32 v139, v90, v91\nv_mfma_f32_16x16x32_bf16 v[72:75], v[116:119], a[136:139], v[72:75]\nv_mul_f32 v84, s35, v84\nv_mul_f32 v85, s35, v85\n"
"v_mfma_f32_16x16x32_bf16 v[76:79], v[116:119], a[168:171], v[76:79]\nds_read_b128 v[116:119], v30 offset:33024\nv_mul_f32 v86, s35, v86\nv_mul_f32 v87, s35, v87\n"
"s_waitcnt lgkmcnt(6)\nv_mfma_f32_16x16x32_bf16 v[64:67], v[120:123], a[140:143], v[64:67]\nv_cvt_pk_bf16_f32 v140, v84, v85\nv_cvt_pk_bf16_f32 v141, v86, v87\n"
"v_mfma_f32_16x16x32_bf16 v[68:71], v[120:123], a[172:175], v[68:71]\nds_read_b128 v[120:123], v27 offset:33024\nv_mul_f32 v92, s35, v92\nv_mul_f32 v93, s35, v93\n"
"v_mfma_f32_16x16x32_bf16 v[72:75], v[124:127], a[140:143], v[72:75]\nv_mul_f32 v94, s35, v94\nv_mul_f32 v95, s35, v95\nv_mfma_f32_16x16x32_bf16 v[76:79], v[124:127], a[172:175], v[76:79]\n"
"ds_read_b128 v[124:127], v31 offset:33024\nv_cvt_pk_bf16_f32 v142, v92, v93\nv_cvt_pk_bf16_f32 v143, v94, v95\ns_waitcnt lgkmcnt(6)\n"
"v_mfma_f32_16x16x32_bf16 v[64:67], v[96:99], a[144:147], v[64:67]\ns_lshl_b32 s54, s40, 11\ns_add_u32 s54, s54, 1024\nv_mfma_f32_16x16x32_bf16 v[68:71], v[96:99], a[176:179], v[68:71]\n"
"ds_read_b128 v[96:99], v24 offset:49152\nbuffer_store_dwordx4 v[136:139], v13, s[16:19], s54 offen sc1\nv_mfma_f32_16x16x32_bf16 v[72:75], v[100:103], a[144:147], v[72:75]\ns_add_u32 s54, s54, 14336\n"
"v_mfma_f32_16x16x32_bf16 v[76:79], v[100:103], a[176:179], v[76:79]\nds_read_b128 v[100:103], v28 offset:49152\nbuffer_store_dwordx4 v[140:143], v13, s[16:19], s54 offen sc1\ns_waitcnt lgkmcnt(6)\n"
"v_mfma_f32_16x16x32_bf16 v[64:67], v[104:107], a[148:151], v[64:67]\ns_lshl_b32 s53, s43, 19\ns_add_u32 s53, s53, s30\nv_mfma_f32_16x16x32_bf16 v[68:71], v[104:107], a[180:183], v[68:71]\n"
"ds_read_b128 v[104:107], v25 offset:49152\ns_add_u32 m0, s31, 139264\ns_add_u32 s53, s53, 131072\nv_mfma_f32_16x16x32_bf16 v[72:75], v[108:111], a[148:151], v[72:75]\n"
"buffer_load_dwordx4 v8, s[8:11], s53 offen lds\nv_mfma_f32_16x16x32_bf16 v[76:79], v[108:111], a[180:183], v[76:79]\nds_read_b128 v[108:111], v29 offset:49152\nbuffer_load_dwordx4 v9, s[8:11], s53 offen offset:1024 lds\n"
"s_waitcnt lgkmcnt(6)\nv_mfma_f32_16x16x32_bf16 v[64:67], v[112:115], a[152:155], v[64:67]\nv_mfma_f32_16x16x32_bf16 v[68:71], v[112:115], a[184:187], v[68:71]\nds_read_b128 v[112:115], v26 offset:49152\n"
"v_mfma_f32_16x16x32_bf16 v[72:75], v[116:119], a[152:155], v[72:75]\nv_mfma_f32_16x16x32_bf16 v[76:79], v[116:119], a[184:187], v[76:79]\nds_read_b128 v[116:119], v30 offset:49152\ns_waitcnt lgkmcnt(6)\n"
"v_mfma_f32_16x16x32_bf16 v[64:67], v[120:123], a[156:159], v[64:67]\nv_mfma_f32_16x16x32_bf16 v[68:71], v[120:123], a[188:191], v[68:71]\nds_read_b128 v[120:123], v27 offset:49152\nv_mfma_f32_16x16x32_bf16 v[72:75], v[124:127], a[156:159], v[72:75]\n"
"v_mfma_f32_16x16x32_bf16 v[76:79], v[124:127], a[188:191], v[76:79]\nds_read_b128 v[124:127], v31 offset:49152\ns_waitcnt lgkmcnt(6)\nv_mfma_f32_16x16x32_bf16 v[80:83], v[96:99], a[128:131], 0\n"
"s_lshl_b32 s53, s43, 19\ns_add_u32 s53, s53, s30\nv_mfma_f32_16x16x32_bf16 v[84:87], v[96:99], a[160:163], 0\nds_read_b128 v[96:99], v24 offset:49408\n"
"s_add_u32 m0, s31, 147456\ns_add_u32 s53, s53, 262144\nv_mfma_f32_16x16x32_bf16 v[88:91], v[100:103], a[128:131], 0\nbuffer_load_dwordx4 v8, s[8:11], s53 offen lds\n"
"v_mfma_f32_16x16x32_bf16 v[92:95], v[100:103], a[160:163], 0\nds_read_b128 v[100:103], v28 offset:49408\nbuffer_load_dwordx4 v9, s[8:11], s53 offen offset:1024 lds\ns_waitcnt lgkmcnt(6)\n"
"v_mfma_f32_16x16x32_bf16 v[80:83], v[104:107], a[132:135], v[80:83]\nv_mul_f32 v64, s35, v64\nv_mul_f32 v65, s35, v65\nv_mfma_f32_16x16x32_bf16 v[84:87], v[104:107], a[164:167], v[84:87]\n"
"ds_read_b128 v[104:107], v25 offset:49408\nv_mul_f32 v66, s35, v66\nv_mul_f32 v67, s35, v67\nv_mfma_f32_16x16x32_bf16 v[88:91], v[108:111], a[132:135], v[88:91]\n"
"v_cvt_pk_bf16_f32 v144, v64, v65\nv_cvt_pk_bf16_f32 v145, v66, v67\nv_mfma_f32_16x16x32_bf16 v[92:95], v[108:111], a[164:167], v[92:95]\nds_read_b128 v[108:111], v29 offset:49408\n"
"v_mul_f32 v72, s35, v72\nv_mul_f32 v73, s35, v73\ns_waitcnt lgkmcnt(6)\nv_mfma_f32_16x16x32_bf16 v[80:83], v[112:115], a[136:139], v[80:83]\n"
"v_mul_f32 v74, s35, v74\nv_mul_f32 v75, s35, v75\nv_mfma_f32_16x16x32_bf16 v[84:87], v[112:115], a[168:171], v[84:87]\nds_read_b128 v[112:115], v26 offset:49408\n"
"v_cvt_pk_bf16_f32 v146, v72, v73\nv_cvt_pk_bf16_f32 v147, v74, v75\nv_mfma_f32_16x16x32_bf16 v[88:91], v[116:119], a[136:139], v[88:91]\nv_mul_f32 v68, s35, v68\n"
"v_mul_f32 v69, s35, v69\nv_mfma_f32_16x16x32_bf16 v[92:95], v[116:119], a[168:171], v[92:95]\nds_read_b128 v[116:119], v30 offset:49408\nv_mul_f32 v70, s35, v70\n"
"v_mul_f32 v71, s35, v71\ns_waitcnt lgkmcnt(6)\nv_mfma_f32_16x16x32_bf16 v[80:83], v[120:123], a[140:143], v[80:83]\nv_cvt_pk_bf16_f32 v148, v68, v69\n"
"v_cvt_pk_bf16_f32 v149, v70, v71\nv_mfma_f32_16x16x32_bf16 v[84:87], v[120:123], a[172:175], v[84:87]\nds_read_b128 v[120:123], v27 offset:49408\nv_mul_f32 v76, s35, v76\n"
"v_mul_f32 v77, s35, v77\nv_mfma_f32_16x16x32_bf16 v[88:91], v[124:127], a[140:143], v[88:91]\nv_mul_f32 v78, s35, v78\nv_mul_f32 v79, s35, v79\n"
"v_mfma_f32_16x16x32_bf16 v[92:95], v[124:127], a[172:175], v[92:95]\nds_read_b128 v[124:127], v31 offset:49408\nv_cvt_pk_bf16_f32 v150, v76, v77\nv_cvt_pk_bf16_f32 v151, v78, v79\n"
"s_waitcnt vmcnt(8)\ns_barrier\ns_waitcnt lgkmcnt(6)\nv_mfma_f32_16x16x32_bf16 v[80:83], v[96:99], a[144:147], v[80:83]\n"
"s_lshl_b32 s54, s41, 11\ns_add_u32 s54, s54, 0\nv_mfma_f32_16x16x32_bf16 v[84:87], v[96:99], a[176:179], v[84:87]\nds_read_b128 v[96:99], v40 offset:0\n"
"buffer_store_dwordx4 v[144:147], v13, s[16:19], s54 offen sc1\nv_mfma_f32_16x16x32_bf16 v[88:91], v[100:103], a[144:147], v[88:91]\ns_add_u32 s54, s54, 14336\nv_mfma_f32_16x16x32_bf16 v[92:95], v[100:103], a[176:179], v[92:95]\n"
"ds_read_b128 v[100:103], v44 offset:0\nbuffer_store_dwordx4 v[148:151], v13, s[16:19], s54 offen sc1\ns_waitcnt lgkmcnt(6)\nv_mfma_f32_16x16x32_bf16 v[80:83], v[104:107], a[148:151], v[80:83]\n"
"s_lshl_b32 s53, s43, 19\ns_add_u32 s53, s53, s30\nv_mfma_f32_16x16x32_bf16 v[84:87], v[104:107], a[180:183], v[84:87]\nds_read_b128 v[104:107], v41 offset:0\n"
"s_add_u32 m0, s31, 155648\ns_add_u32 s53, s53, 393216\nv_mfma_f32_16x16x32_bf16 v[88:91], v[108:111], a[148:151], v[88:91]\nbuffer_load_dwordx4 v8, s[8:11], s53 offen lds\n"
"v_mfma_f32_16x16x32_bf16 v[92:95], v[108:111], a[180:183], v[92:95]\nds_read_b128 v[108:111], v45 offset:0\nbuffer_load_dwordx4 v9, s[8:11], s53 offen offset:1024 lds\ns_waitcnt lgkmcnt(6)\n"
"v_mfma_f32_16x16x32_bf16 v[80:83], v[112:115], a[152:155], v[80:83]\nv_mfma_f32_16x16x32_bf16 v[84:87], v[112:115], a[184:187], v[84:87]\nds_read_b128 v[112:115], v42 offset:0\nv_mfma_f32_16x16x32_bf16 v[88:91], v[116:119], a[152:155], v[88:91]\n"
"v_mfma_f32_16x16x32_bf16 v[92:95], v[116:119], a[184:187], v[92:95]\nds_read_b128 v[116:119], v46 offset:0\ns_waitcnt lgkmcnt(6)\nv_mfma_f32_16x16x32_bf16 v[80:83], v[120:123], a[156:159], v[80:83]\n"
"v_mfma_f32_16x16x32_bf16 v[84:87], v[120:123], a[188:191], v[84:87]\nds_read_b128 v[120:123], v43 offset:0\nv_mfma_f32_16x16x32_bf16 v[88:91], v[124:127], a[156:159], v[88:91]\nv_mfma_f32_16x16x32_bf16 v[92:95], v[124:127], a[188:191], v[92:95]\n"
"ds_read_b128 v[124:127], v47 offset:0\ns_waitcnt lgkmcnt(6)\nv_mfma_f32_16x16x32_bf16 v[64:67], v[96:99], a[128:131], 0\ns_add_u32 m0, s32, 0\n"
"v_mfma_f32_16x16x32_bf16 v[68:71], v[96:99], a[160:163], 0\nds_read_b128 v[96:99], v40 offset:256\ns_mov_b32 s53, 0\nv_mfma_f32_16x16x32_bf16 v[72:75], v[100:103], a[128:131], 0\n"
"buffer_load_dwordx4 v10, s[12:15], s53 offen lds\nv_mfma_f32_16x16x32_bf16 v[76:79], v[100:103], a[160:163], 0\nds_read_b128 v[100:103], v44 offset:256\nv_mul_f32 v80, s35, v80\n"
"v_mul_f32 v81, s35, v81\ns_waitcnt lgkmcnt(6)\nv_mfma_f32_16x16x32_bf16 v[64:67], v[104:107], a[132:135], v[64:67]\nv_mul_f32 v82, s35, v82\n"
"v_mul_f32 v83, s35, v83\nv_mfma_f32_16x16x32_bf16 v[68:71], v[104:107], a[164:167], v[68:71]\nds_read_b128 v[104:107], v41 offset:256\nv_cvt_pk_bf16_f32 v152, v80, v81\n"
"v_cvt_pk_bf16_f32 v153, v82, v83\nv_mfma_f32_16x16x32_bf16 v[72:75], v[108:111], a[132:135], v[72:75]\nv_mul_f32 v88, s35, v88\nv_mul_f32 v89, s35, v89\n"
"v_mfma_f32_16x16x32_bf16 v[76:79], v[108:111], a[164:167], v[76:79]\nds_read_b128 v[108:111], v45 offset:256\nv_mul_f32 v90, s35, v90\nv_mul_f32 v91, s35, v91\n"
"s_waitcnt lgkmcnt(6)\nv_mfma_f32_16x16x32_bf16 v[64:67], v[112:115], a[136:139], v[64:67]\nv_cvt_pk_bf16_f32 v154, v88, v89\nv_cvt_pk_bf16_f32 v155, v90, v91\n"
"v_mfma_f32_16x16x32_bf16 v[68:71], v[112:115], a[168:171], v[68:71]\nds_read_b128 v[112:115], v42 offset:256\nv_mul_f32 v84, s35, v84\nv_mul_f32 v85, s35, v85\n"
"v_mfma_f32_16x16x32_bf16 v[72:75], v[116:119], a[136:139], v[72:75]\nv_mul_f32 v86, s35, v86\nv_mul_f32 v87, s35, v87\nv_mfma_f32_16x16x32_bf16 v[76:79], v[116:119], a[168:171], v[76:79]\n"
"ds_read_b128 v[116:119], v46 offset:256\nv_cvt_pk_bf16_f32 v156, v84, v85\nv_cvt_pk_bf16_f32 v157, v86, v87\ns_waitcnt lgkmcnt(6)\n"
"v_mfma_f32_16x16x32_bf16 v[64:67], v[120:123], a[140:143], v[64:67]\nv_mul_f32 v92, s35, v92\nv_mul_f32 v93, s35, v93\nv_mfma_f32_16x16x32_bf16 v[68:71], v[120:123], a[172:175], v[68:71]\n"
"ds_read_b128 v[120:123], v43 offset:256\nv_mul_f32 v94, s35, v94\nv_mul_f32 v95, s35, v95\nv_mfma_f32_16x16x32_bf16 v[72:75], v[124:127], a[140:143], v[72:75]\n"
"v_cvt_pk_bf16_f32 v158, v92, v93\nv_cvt_pk_bf16_f32 v159, v94, v95\nv_mfma_f32_16x16x32_bf16 v[76:79], v[124:127], a[172:175], v[76:79]\nds_read_b128 v[124:127], v47 offset:256\n"
"s_lshl_b32 s54, s41, 11\ns_add_u32 s54, s54, 1024\ns_waitcnt lgkmcnt(6)\nv_mfma_f32_16x16x32_bf16 v[64:67], v[96:99], a[144:147], v[64:67]\n"
"buffer_store_dwordx4 v[152:155], v13, s[16:19], s54 offen sc1\nv_mfma_f32_16x16x32_bf16 v[68:71], v[96:99], a[176:179], v[68:71]\nds_read_b128 v[96:99], v40 offset:16384\ns_add_u32 s54, s54, 14336\n"
"v_mfma_f32_16x16x32_bf16 v[72:75], v[100:103], a[144:147], v[72:75]\nbuffer_store_dwordx4 v[156:159], v13, s[16:19], s54 offen sc1\nv_mfma_f32_16x16x32_bf16 v[76:79], v[100:103], a[176:179], v[76:79]\nds_read_b128 v[100:103], v44 offset:16384\n"
"s_add_u32 m0, s32, 4096\ns_waitcnt lgkmcnt(6)\nv_mfma_f32_16x16x32_bf16 v[64:67], v[104:107], a[148:151], v[64:67]\ns_mov_b32 s53, 128\n"
"v_mfma_f32_16x16x32_bf16 v[68:71], v[104:107], a[180:183], v[68:71]\nds_read_b128 v[104:107], v41 offset:16384\nbuffer_load_dwordx4 v10, s[12:15], s53 offen lds\nv_mfma_f32_16x16x32_bf16 v[72:75], v[108:111], a[148:151], v[72:75]\n"
"s_add_u32 m0, s32, 8192\nv_mfma_f32_16x16x32_bf16 v[76:79], v[108:111], a[180:183], v[76:79]\nds_read_b128 v[108:111], v45 offset:16384\ns_mov_b32 s53, 256\n"
"s_waitcnt lgkmcnt(6)\nv_mfma_f32_16x16x32_bf16 v[64:67], v[112:115], a[152:155], v[64:67]\nbuffer_load_dwordx4 v10, s[12:15], s53 offen lds\nv_mfma_f32_16x16x32_bf16 v[68:71], v[112:115], a[184:187], v[68:71]\n"
"ds_read_b128 v[112:115], v42 offset:16384\ns_add_u32 m0, s32, 12288\nv_mfma_f32_16x16x32_bf16 v[72:75], v[116:119], a[152:155], v[72:75]\ns_mov_b32 s53, 384\n"
"v_mfma_f32_16x16x32_bf16 v[76:79], v[116:119], a[184:187], v[76:79]\nds_read_b128 v[116:119], v46 offset:16384\nbuffer_load_dwordx4 v10, s[12:15], s53 offen lds\ns_waitcnt lgkmcnt(6)\n"
"v_mfma_f32_16x16x32_bf16 v[64:67], v[120:123], a[156:159], v[64:67]\nv_mfma_f32_16x16x32_bf16 v[68:71], v[120:123], a[188:191], v[68:71]\nds_read_b128 v[120:123], v43 offset:16384\nv_mfma_f32_16x16x32_bf16 v[72:75], v[124:127], a[156:159], v[72:75]\n"
"v_mfma_f32_16x16x32_bf16 v[76:79], v[124:127], a[188:191], v[76:79]\nds_read_b128 v[124:127], v47 offset:16384\ns_waitcnt lgkmcnt(6)\nv_mfma_f32_16x16x32_bf16 v[80:83], v[96:99], a[128:131], 0\n"
"s_add_u32 m0, s32, 16384\nv_mfma_f32_16x16x32_bf16 v[84:87], v[96:99], a[160:163], 0\nds_read_b128 v[96:99], v40 offset:16640\ns_mov_b32 s53, 512\n"
"v_mfma_f32_16x16x32_bf16 v[88:91], v[100:103], a[128:131], 0\nbuffer_load_dwordx4 v10, s[12:15], s53 offen lds\nv_mfma_f32_16x16x32_bf16 v[92:95], v[100:103], a[160:163], 0\nds_read_b128 v[100:103], v44 offset:16640\n"
"v_mul_f32 v64, s35, v64\nv_mul_f32 v65, s35, v65\ns_waitcnt lgkmcnt(6)\nv_mfma_f32_16x16x32_bf16 v[80:83], v[104:107], a[132:135], v[80:83]\n"
"v_mul_f32 v66, s35, v66\nv_mul_f32 v67, s35, v67\nv_mfma_f32_16x16x32_bf16 v[84:87], v[104:107], a[164:167], v[84:87]\nds_read_b128 v[104:107], v41 offset:16640\n"
"v_cvt_pk_bf16_f32 v160, v64, v65\nv_cvt_pk_bf16_f32 v161, v66, v67\nv_mfma_f32_16x16x32_bf16 v[88:91], v[108:111], a[132:135], v[88:91]\nv_mul_f32 v72, s35, v72\n"
"v_mul_f32 v73, s35, v73\nv_mfma_f32_16x16x32_bf16 v[92:95], v[108:111], a[164:167], v[92:95]\nds_read_b128 v[108:111], v45 offset:16640\nv_mul_f32 v74, s35, v74\n"
"v_mul_f32 v75, s35, v75\ns_waitcnt lgkmcnt(6)\nv_mfma_f32_16x16x32_bf16 v[80:83], v[112:115], a[136:139], v[80:83]\nv_cvt_pk_bf16_f32 v162, v72, v73\n"
"v_cvt_pk_bf16_f32 v163, v74, v75\nv_mfma_f32_16x16x32_bf16 v[84:87], v[112:115], a[168:171], v[84:87]\nds_read_b128 v[112:115], v42 offset:16640\nv_mul_f32 v68, s35, v68\n"
"v_mul_f32 v69, s35, v69\nv_mfma_f32_16x16x32_bf16 v[88:91], v[116:119], a[136:139], v[88:91]\nv_mul_f32 v70, s35, v70\nv_mul_f32 v71, s35, v71\n"
"v_mfma_f32_16x16x32_bf16 v[92:95], v[116:119], a[168:171], v[92:95]\nds_read_b128 v[116:119], v46 offset:16640\nv_cvt_pk_bf16_f32 v164, v68, v69\nv_cvt_pk_bf16_f32 v165, v70, v71\n"
"s_waitcnt lgkmcnt(6)\nv_mfma_f32_16x16x32_bf16 v[80:83], v[120:123], a[140:143], v[80:83]\nv_mul_f32 v76, s35, v76\nv_mul_f32 v77, s35, v77\n"
"v_mfma_f32_16x16x32_bf16 v[84:87], v[120:123], a[172:175], v[84:87]\nds_read_b128 v[120:123], v43 offset:16640\nv_mul_f32 v78, s35, v78\nv_mul_f32 v79, s35, v79\n"
"v_mfma_f32_16x16x32_bf16 v[88:91], v[124:127], a[140:143], v[88:91]\nv_cvt_pk_bf16_f32 v166, v76, v77\nv_cvt_pk_bf16_f32 v167, v78, v79\nv_mfma_f32_16x16x32_bf16 v[92:95], v[124:127], a[172:175], v[92:95]\n"
"ds_read_b128 v[124:127], v47 offset:16640\ns_lshl_b32 s54, s42, 11\ns_add_u32 s54, s54, 0\ns_waitcnt vmcnt(7)\n"
"s_barrier\ns_waitcnt lgkmcnt(6)\nv_mfma_f32_16x16x32_bf16 v[80:83], v[96:99], a[144:147], v[80:83]\nbuffer_store_dwordx4 v[160:163], v13, s[16:19], s54 offen sc1\n"
"v_mfma_f32_16x16x32_bf16 v[84:87], v[96:99], a[176:179], v[84:87]\nds_read_b128 v[96:99], v40 offset:32768\ns_add_u32 s54, s54, 14336\nv_mfma_f32_16x16x32_bf16 v[88:91], v[100:103], a[144:147], v[88:91]\n"
"buffer_store_dwordx4 v[164:167], v13, s[16:19], s54 offen sc1\nv_mfma_f32_16x16x32_bf16 v[92:95], v[100:103], a[176:179], v[92:95]\nds_read_b128 v[100:103], v44 offset:32768\ns_add_u32 m0, s32, 20480\n"
"s_waitcnt lgkmcnt(6)\nv_mfma_f32_16x16x32_bf16 v[80:83], v[104:107], a[148:151], v[80:83]\ns_mov_b32 s53, 640\nv_mfma_f32_16x16x32_bf16 v[84:87], v[104:107], a[180:183], v[84:87]\n"
"ds_read_b128 v[104:107], v41 offset:32768\nbuffer_load_dwordx4 v10, s[12:15], s53 offen lds\nv_mfma_f32_16x16x32_bf16 v[88:91], v[108:111], a[148:151], v[88:91]\ns_add_u32 m0, s32, 24576\n"
"v_mfma_f32_16x16x32_bf16 v[92:95], v[108:111], a[180:183], v[92:95]\nds_read_b128 v[108:111], v45 offset:32768\ns_mov_b32 s53, 768\ns_waitcnt lgkmcnt(6)\n"
"v_mfma_f32_16x16x32_bf16 v[80:83], v[112:115], a[152:155], v[80:83]\nbuffer_load_dwordx4 v10, s[12:15], s53 offen lds\nv_mfma_f32_16x16x32_bf16 v[84:87], v[112:115], a[184:187], v[84:87]\nds_read_b128 v[112:115], v42 offset:32768\n"
"v_mfma_f32_16x16x32_bf16 v[88:91], v[116:119], a[152:155], v[88:91]\nv_mfma_f32_16x16x32_bf16 v[92:95], v[116:119], a[184:187], v[92:95]\nds_read_b128 v[116:119], v46 offset:32768\ns_waitcnt lgkmcnt(6)\n"
"v_mfma_f32_16x16x32_bf16 v[80:83], v[120:123], a[156:159], v[80:83]\nv_mfma_f32_16x16x32_bf16 v[84:87], v[120:123], a[188:191], v[84:87]\nds_read_b128 v[120:123], v43 offset:32768\nv_mfma_f32_16x16x32_bf16 v[88:91], v[124:127], a[156:159], v[88:91]\n"
"v_mfma_f32_16x16x32_bf16 v[92:95], v[124:127], a[188:191], v[92:95]\nds_read_b128 v[124:127], v47 offset:32768\ns_cmp_lg_u32 s34, 2\ns_cbranch_scc1 fg0_main\n"
"s_nop 7\nv_mul_f32 v80, s35, v80\nv_mul_f32 v81, s35, v81\nv_mul_f32 v82, s35, v82\n"
"v_mul_f32 v83, s35, v83\nv_cvt_pk_bf16_f32 v168, v80, v81\nv_cvt_pk_bf16_f32 v169, v82, v83\nv_mul_f32 v88, s35, v88\n"
"v_mul_f32 v89, s35, v89\nv_mul_f32 v90, s35, v90\nv_mul_f32 v91, s35, v91\nv_cvt_pk_bf16_f32 v170, v88, v89\n"
"v_cvt_pk_bf16_f32 v171, v90, v91\nv_mul_f32 v84, s35, v84\nv_mul_f32 v85, s35, v85\nv_mul_f32 v86, s35, v86\n"
"v_mul_f32 v87, s35, v87\nv_cvt_pk_bf16_f32 v172, v84, v85\nv_cvt_pk_bf16_f32 v173, v86, v87\nv_mul_f32 v92, s35, v92\n"
"v_mul_f32 v93, s35, v93\nv_mul_f32 v94, s35, v94\nv_mul_f32 v95, s35, v95\nv_cvt_pk_bf16_f32 v174, v92, v93\n"
"v_cvt_pk_bf16_f32 v175, v94, v95\ns_lshl_b32 s54, s42, 11\ns_add_u32 s54, s54, 1024\nbuffer_store_dwordx4 v[168:171], v13, s[16:19], s54 offen sc1\n"
"s_add_u32 s54, s54, 14336\nbuffer_store_dwordx4 v[172:175], v13, s[16:19], s54 offen sc1\nv_mov_b32 v36, s28\ns_waitcnt vmcnt(0) lgkmcnt(0)\n"
"s_barrier\nbuffer_store_dword v36, v19, s[24:27], 0 offen sc1\ns_branch fg0_end\nfg0_main:\n"
"s_waitcnt lgkmcnt(6)\nv_mfma_f32_16x16x32_bf16 v[64:67], v[96:99], a[128:131], 0\ns_add_u32 m0, s32, 32768\nv_mfma_f32_16x16x32_bf16 v[68:71], v[96:99], a[160:163], 0\n"
"ds_read_b128 v[96:99], v40 offset:33024\ns_mov_b32 s53, 28672\nv_mfma_f32_16x16x32_bf16 v[72:75], v[100:103], a[128:131], 0\nbuffer_load_dwordx4 v10, s[12:15], s53 offen lds\n"
"v_mfma_f32_16x16x32_bf16 v[76:79], v[100:103], a[160:163], 0\nds_read_b128 v[100:103], v44 offset:33024\nv_mul_f32 v80, s35, v80\nv_mul_f32 v81, s35, v81\n"
"s_waitcnt lgkmcnt(6)\nv_mfma_f32_16x16x32_bf16 v[64:67], v[104:107], a[132:135], v[64:67]\nv_mul_f32 v82, s35, v82\nv_mul_f32 v83, s35, v83\n"
"v_mfma_f32_16x16x32_bf16 v[68:71], v[104:107], a[164:167], v[68:71]\nds_read_b128 v[104:107], v41 offset:33024\nv_cvt_pk_bf16_f32 v168, v80, v81\nv_cvt_pk_bf16_f32 v169, v82, v83\n"
"v_mfma_f32_16x16x32_bf16 v[72:75], v[108:111], a[132:135], v[72:75]\nv_mul_f32 v88, s35, v88\nv_mul_f32 v89, s35, v89\nv_mfma_f32_16x16x32_bf16 v[76:79], v[108:111], a[164:167], v[76:79]\n"
"ds_read_b128 v[108:111], v45 offset:33024\nv_mul_f32 v90, s35, v90\nv_mul_f32 v91, s35, v91\ns_waitcnt lgkmcnt(6)\n"
"v_mfma_f32_16x16x32_bf16 v[64:67], v[112:115], a[136:139], v[64:67]\nv_cvt_pk_bf16_f32 v170, v88, v89\nv_cvt_pk_bf16_f32 v171, v90, v91\nv_mfma_f32_16x16x32_bf16 v[68:71], v[112:115], a[168:171], v[68:71]\n"
"ds_read_b128 v[112:115], v42 offset:33024\nv_mul_f32 v84, s35, v84\nv_mul_f32 v85, s35, v85\nv_mfma_f32_16x16x32_bf16 v[72:75], v[116:119], a[136:139], v[72:75]\n"
"v_mul_f32 v86, s35, v86\nv_mul_f32 v87, s35, v87\nv_mfma_f32_16x16x32_bf16 v[76:79], v[116:119], a[168:171], v[76:79]\nds_read_b128 v[116:119], v46 offset:33024\n"
"v_cvt_pk_bf16_f32 v172, v84, v85\nv_cvt_pk_bf16_f32 v173, v86, v87\ns_waitcnt lgkmcnt(6)\nv_mfma_f32_16x16x32_bf16 v[64:67], v[120:123], a[140:143], v[64:67]\n"
"v_mul_f32 v92, s35, v92\nv_mul_f32 v93, s35, v93\nv_mfma_f32_16x16x32_bf16 v[68:71], v[120:123], a[172:175], v[68:71]\nds_read_b128 v[120:123], v43 offset:33024\n"
"v_mul_f32 v94, s35, v94\nv_mul_f32 v95, s35, v95\nv_mfma_f32_16x16x32_bf16 v[72:75], v[124:127], a[140:143], v[72:75]\nv_cvt_pk_bf16_f32 v174, v92, v93\n"
"v_cvt_pk_bf16_f32 v175, v94, v95\nv_mfma_f32_16x16x32_bf16 v[76:79], v[124:127], a[172:175], v[76:79]\nds_read_b128 v[124:127], v47 offset:33024\ns_lshl_b32 s54, s42, 11\n"
"s_add_u32 s54, s54, 1024\ns_waitcnt lgkmcnt(6)\nv_mfma_f32_16x16x32_bf16 v[64:67], v[96:99], a[144:147], v[64:67]\nbuffer_store_dwordx4 v[168:171], v13, s[16:19], s54 offen sc1\n"
"v_mfma_f32_16x16x32_bf16 v[68:71], v[96:99], a[176:179], v[68:71]\nds_read_b128 v[96:99], v40 offset:49152\ns_add_u32 s54, s54, 14336\nv_mfma_f32_16x16x32_bf16 v[72:75], v[100:103], a[144:147], v[72:75]\n"
"buffer_store_dwordx4 v[172:175], v13, s[16:19], s54 offen sc1\nv_mfma_f32_16x16x32_bf16 v[76:79], v[100:103], a[176:179], v[76:79]\nds_read_b128 v[100:103], v44 offset:49152\ns_add_u32 m0, s32, 36864\n"
"s_waitcnt lgkmcnt(6)\nv_mfma_f32_16x16x32_bf16 v[64:67], v[104:107], a[148:151], v[64:67]\ns_mov_b32 s53, 28800\nv_mfma_f32_16x16x32_bf16 v[68:71], v[104:107], a[180:183], v[68:71]\n"
"ds_read_b128 v[104:107], v41 offset:49152\nbuffer_load_dwordx4 v10, s[12:15], s53 offen lds\nv_mfma_f32_16x16x32_bf16 v[72:75], v[108:111], a[148:151], v[72:75]\ns_add_u32 m0, s32, 40960\n"
"v_mfma_f32_16x16x32_bf16 v[76:79], v[108:111], a[180:183], v[76:79]\nds_read_b128 v[108:111], v45 offset:49152\ns_mov_b32 s53, 28928\ns_waitcnt lgkmcnt(6)\n"
"v_mfma_f32_16x16x32_bf16 v[64:67], v[112:115], a[152:155], v[64:67]\nbuffer_load_dwordx4 v10, s[12:15], s53 offen lds\nv_mfma_f32_16x16x32_bf16 v[68:71], v[112:115], a[184:187], v[68:71]\nds_read_b128 v[112:115], v42 offset:49152\n"
"s_add_u32 m0, s32, 45056\nv_mfma_f32_16x16x32_bf16 v[72:75], v[116:119], a[152:155], v[72:75]\ns_mov_b32 s53, 29056\nv_mfma_f32_16x16x32_bf16 v[76:79], v[116:119], a[184:187], v[76:79]\n"
"ds_read_b128 v[116:119], v46 offset:49152\nbuffer_load_dwordx4 v10, s[12:15], s53 offen lds\ns_waitcnt lgkmcnt(6)\nv_mfma_f32_16x16x32_bf16 v[64:67], v[120:123], a[156:159], v[64:67]\n"
"v_mfma_f32_16x16x32_bf16 v[68:71], v[120:123], a[188:191], v[68:71]\nds_read_b128 v[120:123], v43 offset:49152\nv_mfma_f32_16x16x32_bf16 v[72:75], v[124:127], a[156:159], v[72:75]\nv_mfma_f32_16x16x32_bf16 v[76:79], v[124:127], a[188:191], v[76:79]\n"
"ds_read_b128 v[124:127], v47 offset:49152\ns_waitcnt lgkmcnt(6)\nv_mfma_f32_16x16x32_bf16 v[80:83], v[96:99], a[128:131], 0\ns_add_u32 m0, s32, 49152\n"
"v_mfma_f32_16x16x32_bf16 v[84:87], v[96:99], a[160:163], 0\nds_read_b128 v[96:99], v40 offset:49408\ns_mov_b32 s53, 29184\nv_mfma_f32_16x16x32_bf16 v[88:91], v[100:103], a[128:131], 0\n"
"buffer_load_dwordx4 v10, s[12:15], s53 offen lds\nv_mfma_f32_16x16x32_bf16 v[92:95], v[100:103], a[160:163], 0\nds_read_b128 v[100:103], v44 offset:49408\nv_mul_f32 v64, s35, v64\n"
"v_mul_f32 v65, s35, v65\ns_waitcnt lgkmcnt(6)\nv_mfma_f32_16x16x32_bf16 v[80:83], v[104:107], a[132:135], v[80:83]\nv_mul_f32 v66, s35, v66\n"
"v_mul_f32 v67, s35, v67\nv_mfma_f32_16x16x32_bf16 v[84:87], v[104:107], a[164:167], v[84:87]\nds_read_b128 v[104:107], v41 offset:49408\nv_cvt_pk_bf16_f32 v176, v64, v65\n"
"v_cvt_pk_bf16_f32 v177, v66, v67\nv_mfma_f32_16x16x32_bf16 v[88:91], v[108:111], a[132:135], v[88:91]\nv_mul_f32 v72, s35, v72\nv_mul_f32 v73, s35, v73\n"
"v_mfma_f32_16x16x32_bf16 v[92:95], v[108:111], a[164:167], v[92:95]\nds_read_b128 v[108:111], v45 offset:49408\nv_mul_f32 v74, s35, v74\nv_mul_f32 v75, s35, v75\n"
"s_waitcnt lgkmcnt(6)\nv_mfma_f32_16x16x32_bf16 v[80:83], v[112:115], a[136:139], v[80:83]\nv_cvt_pk_bf16_f32 v178, v72, v73\nv_cvt_pk_bf16_f32 v179, v74, v75\n"
"v_mfma_f32_16x16x32_bf16 v[84:87], v[112:115], a[168:171], v[84:87]\nds_read_b128 v[112:115], v42 offset:49408\nv_mul_f32 v68, s35, v68\nv_mul_f32 v69, s35, v69\n"
"v_mfma_f32_16x16x32_bf16 v[88:91], v[116:119], a[136:139], v[88:91]\nv_mul_f32 v70, s35, v70\nv_mul_f32 v71, s35, v71\nv_mfma_f32_16x16x32_bf16 v[92:95], v[116:119], a[168:171], v[92:95]\n"
"ds_read_b128 v[116:119], v46 offset:49408\nv_cvt_pk_bf16_f32 v180, v68, v69\nv_cvt_pk_bf16_f32 v181, v70, v71\ns_waitcnt lgkmcnt(6)\n"
"v_mfma_f32_16x16x32_bf16 v[80:83], v[120:123], a[140:143], v[80:83]\nv_mul_f32 v76, s35, v76\nv_mul_f32 v77, s35, v77\nv_mfma_f32_16x16x32_bf16 v[84:87], v[120:123], a[172:175], v[84:87]\n"
"ds_read_b128 v[120:123], v43 offset:49408\nv_mul_f32 v78, s35, v78\nv_mul_f32 v79, s35, v79\nv_mfma_f32_16x16x32_bf16 v[88:91], v[124:127], a[140:143], v[88:91]\n"
"v_cvt_pk_bf16_f32 v182, v76, v77\nv_cvt_pk_bf16_f32 v183, v78, v79\nv_mfma_f32_16x16x32_bf16 v[92:95], v[124:127], a[172:175], v[92:95]\nds_read_b128 v[124:127], v47 offset:49408\n"
"s_lshl_b32 s54, s43, 11\ns_add_u32 s54, s54, 0\ns_waitcnt lgkmcnt(6)\nv_mfma_f32_16x16x32_bf16 v[80:83], v[96:99], a[144:147], v[80:83]\n"
"buffer_store_dwordx4 v[176:179], v13, s[16:19], s54 offen sc1\nv_mfma_f32_16x16x32_bf16 v[84:87], v[96:99], a[176:179], v[84:87]\ns_add_u32 s54, s54, 14336\nv_mfma_f32_16x16x32_bf16 v[88:91], v[100:103], a[144:147], v[88:91]\n"
"buffer_store_dwordx4 v[180:183], v13, s[16:19], s54 offen sc1\nv_mfma_f32_16x16x32_bf16 v[92:95], v[100:103], a[176:179], v[92:95]\ns_add_u32 m0, s32, 53248\ns_waitcnt lgkmcnt(4)\n"
"v_mfma_f32_16x16x32_bf16 v[80:83], v[104:107], a[148:151], v[80:83]\ns_mov_b32 s53, 29312\nv_mfma_f32_16x16x32_bf16 v[84:87], v[104:107], a[180:183], v[84:87]\nbuffer_load_dwordx4 v10, s[12:15], s53 offen lds\n"
"v_mfma_f32_16x16x32_bf16 v[88:91], v[108:111], a[148:151], v[88:91]\ns_add_u32 m0, s32, 57344\nv_mfma_f32_16x16x32_bf16 v[92:95], v[108:111], a[180:183], v[92:95]\ns_mov_b32 s53, 29440\n"
"s_waitcnt lgkmcnt(2)\nv_mfma_f32_16x16x32_bf16 v[80:83], v[112:115], a[152:155], v[80:83]\nbuffer_load_dwordx4 v10, s[12:15], s53 offen lds\nv_mfma_f32_16x16x32_bf16 v[84:87], v[112:115], a[184:187], v[84:87]\n"
"buffer_load_dword v37, v21, s[24:27], 0 offen sc1\nv_mfma_f32_16x16x32_bf16 v[88:91], v[116:119], a[152:155], v[88:91]\nv_mfma_f32_16x16x32_bf16 v[92:95], v[116:119], a[184:187], v[92:95]\ns_waitcnt lgkmcnt(0)\n"
"v_mfma_f32_16x16x32_bf16 v[80:83], v[120:123], a[156:159], v[80:83]\nv_mfma_f32_16x16x32_bf16 v[84:87], v[120:123], a[188:191], v[84:87]\nv_mfma_f32_16x16x32_bf16 v[88:91], v[124:127], a[156:159], v[88:91]\nv_mfma_f32_16x16x32_bf16 v[92:95], v[124:127], a[188:191], v[92:95]\n"
"s_nop 7\nv_mul_f32 v80, s35, v80\nv_mul_f32 v81, s35, v81\nv_mul_f32 v82, s35, v82\n"
"v_mul_f32 v83, s35, v83\nv_cvt_pk_bf16_f32 v184, v80, v81\nv_cvt_pk_bf16_f32 v185, v82, v83\nv_mul_f32 v88, s35, v88\n"
"v_mul_f32 v89, s35, v89\nv_mul_f32 v90, s35, v90\nv_mul_f32 v91, s35, v91\nv_cvt_pk_bf16_f32 v186, v88, v89\n"
"v_cvt_pk_bf16_f32 v187, v90, v91\nv_mul_f32 v84, s35, v84\nv_mul_f32 v85, s35, v85\nv_mul_f32 v86, s35, v86\n"
"v_mul_f32 v87, s35, v87\nv_cvt_pk_bf16_f32 v188, v84, v85\nv_cvt_pk_bf16_f32 v189, v86, v87\nv_mul_f32 v92, s35, v92\n"
"v_mul_f32 v93, s35, v93\nv_mul_f32 v94, s35, v94\nv_mul_f32 v95, s35, v95\nv_cvt_pk_bf16_f32 v190, v92, v93\n"
"v_cvt_pk_bf16_f32 v191, v94, v95\ns_lshl_b32 s54, s43, 11\ns_add_u32 s54, s54, 1024\nbuffer_store_dwordx4 v[184:187], v13, s[16:19], s54 offen sc1\n"
"s_add_u32 s54, s54, 14336\nbuffer_store_dwordx4 v[188:191], v13, s[16:19], s54 offen sc1\ns_waitcnt vmcnt(2)\nv_mov_b32 v36, s28\n"
"v_cmp_ne_u32 vcc, s28, v37\ns_cbranch_vccz fg0_e1\ns_waitcnt vmcnt(0)\ns_barrier\n"
"buffer_store_dword v36, v19, s[24:27], 0 offen sc1\nfg0_po:\nv_cmp_ne_u32 vcc, s28, v37\ns_cbranch_vccz fg0_pook\n"
"s_add_u32 s55, s55, 1\ns_cmp_gt_u32 s55, 262144\ns_cbranch_scc0 fg0_po2\ns_endpgm\n"
"fg0_po2:\ns_sleep 1\nbuffer_load_dword v37, v21, s[24:27], 0 offen sc1\ns_waitcnt vmcnt(0)\n"
"s_branch fg0_po\nfg0_pook:\ns_mov_b32 s53, 8192\nbuffer_load_dwordx4 v[192:195], v13, s[16:19], s53 offen sc1\n"
"s_mov_b32 s53, 22528\nbuffer_load_dwordx4 v[196:199], v13, s[16:19], s53 offen sc1\ns_mov_b32 s53, 9216\nbuffer_load_dwordx4 v[200:203], v13, s[16:19], s53 offen sc1\n"
"s_mov_b32 s53, 23552\nbuffer_load_dwordx4 v[204:207], v13, s[16:19], s53 offen sc1\ns_mov_b32 s53, 10240\nbuffer_load_dwordx4 v[208:211], v13, s[16:19], s53 offen sc1\n"
"s_mov_b32 s53, 24576\nbuffer_load_dwordx4 v[212:215], v13, s[16:19], s53 offen sc1\ns_mov_b32 s53, 11264\nbuffer_load_dwordx4 v[216:219], v13, s[16:19], s53 offen sc1\n"
"s_mov_b32 s53, 25600\nbuffer_load_dwordx4 v[220:223], v13, s[16:19], s53 offen sc1\ns_mov_b32 s53, 12288\nbuffer_load_dwordx4 v[224:227], v13, s[16:19], s53 offen sc1\n"
"s_mov_b32 s53, 26624\nbuffer_load_dwordx4 v[228:231], v13, s[16:19], s53 offen sc1\ns_mov_b32 s53, 13312\nbuffer_load_dwordx4 v[232:235], v13, s[16:19], s53 offen sc1\n"
"s_mov_b32 s53, 27648\nbuffer_load_dwordx4 v[236:239], v13, s[16:19], s53 offen sc1\nbuffer_store_dword v36, v19, s[24:27], 0 offen sc1\ns_branch fg0_e2\n"
"fg0_e1:\ns_mov_b32 s53, 8192\nbuffer_load_dwordx4 v[192:195], v13, s[16:19], s53 offen sc1\ns_mov_b32 s53, 22528\n"
"buffer_load_dwordx4 v[196:199], v13, s[16:19], s53 offen sc1\ns_mov_b32 s53, 9216\nbuffer_load_dwordx4 v[200:203], v13, s[16:19], s53 offen sc1\ns_mov_b32 s53, 23552\n"
"buffer_load_dwordx4 v[204:207], v13, s[16:19], s53 offen sc1\ns_mov_b32 s53, 10240\nbuffer_load_dwordx4 v[208:211], v13, s[16:19], s53 offen sc1\ns_mov_b32 s53, 24576\n"
"buffer_load_dwordx4 v[212:215], v13, s[16:19], s53 offen sc1\ns_mov_b32 s53, 11264\nbuffer_load_dwordx4 v[216:219], v13, s[16:19], s53 offen sc1\ns_mov_b32 s53, 25600\n"
"buffer_load_dwordx4 v[220:223], v13, s[16:19], s53 offen sc1\ns_mov_b32 s53, 12288\nbuffer_load_dwordx4 v[224:227], v13, s[16:19], s53 offen sc1\ns_mov_b32 s53, 26624\n"
"buffer_load_dwordx4 v[228:231], v13, s[16:19], s53 offen sc1\ns_mov_b32 s53, 13312\nbuffer_load_dwordx4 v[232:235], v13, s[16:19], s53 offen sc1\ns_mov_b32 s53, 27648\n"
"buffer_load_dwordx4 v[236:239], v13, s[16:19], s53 offen sc1\ns_waitcnt vmcnt(12)\ns_barrier\nbuffer_store_dword v36, v19, s[24:27], 0 offen sc1\n"
"fg0_e2:\ns_add_u32 m0, s32, 65536\ns_mov_b32 s53, 57344\nbuffer_load_dwordx4 v10, s[12:15], s53 offen lds\n"
"s_add_u32 m0, s32, 69632\ns_mov_b32 s53, 57472\nbuffer_load_dwordx4 v10, s[12:15], s53 offen lds\ns_add_u32 m0, s32, 73728\n"
"s_mov_b32 s53, 57600\nbuffer_load_dwordx4 v10, s[12:15], s53 offen lds\ns_add_u32 m0, s32, 77824\ns_mov_b32 s53, 57728\n"
"buffer_load_dwordx4 v10, s[12:15], s53 offen lds\ns_add_u32 m0, s32, 81920\ns_mov_b32 s53, 57856\nbuffer_load_dwordx4 v10, s[12:15], s53 offen lds\n"
"s_add_u32 m0, s32, 86016\ns_mov_b32 s53, 57984\nbuffer_load_dwordx4 v10, s[12:15], s53 offen lds\ns_add_u32 m0, s32, 90112\n"
"s_mov_b32 s53, 58112\nbuffer_load_dwordx4 v10, s[12:15], s53 offen lds\ns_sub_u32 s48, s28, 1\ns_mov_b32 s49, 86016\n"
"s_mov_b32 s50, 0\ns_mov_b32 s51, 0\ns_add_u32 s52, s32, 98304\ns_add_u32 s47, s32, 131072\n"
"ds_read_b128 v[40:43], v32 offset:0\nds_read_b128 v[44:47], v33 offset:0\nds_read_b128 v[48:51], v34 offset:0\nds_read_b128 v[52:55], v35 offset:0\n"
"s_barrier\ns_waitcnt lgkmcnt(3)\nv_mfma_f32_16x16x32_bf16 v[64:67], v[128:131], v[40:43], 0\nv_mov_b32 v36, s48\n"
"s_add_u32 s48, s48, 1\ns_nop 0\nbuffer_store_dword v36, v19, s[24:27], s33 offen sc1\nv_mfma_f32_16x16x32_bf16 v[68:71], v[132:135], v[40:43], 0\n"
"ds_read_b128 v[40:43], v32 offset:4096\ns_waitcnt lgkmcnt(3)\nv_mfma_f32_16x16x32_bf16 v[72:75], v[128:131], v[44:47], 0\nv_mfma_f32_16x16x32_bf16 v[76:79], v[132:135], v[44:47], 0\n"
"ds_read_b128 v[44:47], v33 offset:4096\ns_waitcnt lgkmcnt(3)\nv_mfma_f32_16x16x32_bf16 v[64:67], v[136:139], v[48:51], v[64:67]\ns_add_u32 m0, s52, 0\n"
"v_mfma_f32_16x16x32_bf16 v[68:71], v[140:143], v[48:51], v[68:71]\nds_read_b128 v[48:51], v34 offset:4096\ns_add_u32 s53, s49, 0\ns_waitcnt lgkmcnt(3)\n"
"v_mfma_f32_16x16x32_bf16 v[72:75], v[136:139], v[52:55], v[72:75]\nbuffer_load_dwordx4 v10, s[12:15], s53 offen lds\nv_mfma_f32_16x16x32_bf16 v[76:79], v[140:143], v[52:55], v[76:79]\nds_read_b128 v[52:55], v35 offset:4096\n"
"s_add_u32 m0, s52, 4096\ns_waitcnt lgkmcnt(3)\nv_mfma_f32_16x16x32_bf16 v[64:67], v[144:147], v[40:43], v[64:67]\ns_add_u32 s53, s49, 128\n"
"v_mfma_f32_16x16x32_bf16 v[68:71], v[148:151], v[40:43], v[68:71]\nds_read_b128 v[40:43], v32 offset:8192\nbuffer_load_dwordx4 v10, s[12:15], s53 offen lds\ns_waitcnt lgkmcnt(3)\n"
"v_mfma_f32_16x16x32_bf16 v[72:75], v[144:147], v[44:47], v[72:75]\ns_add_u32 m0, s52, 8192\nv_mfma_f32_16x16x32_bf16 v[76:79], v[148:151], v[44:47], v[76:79]\nds_read_b128 v[44:47], v33 offset:8192\n"
"s_add_u32 s53, s49, 256\ns_waitcnt lgkmcnt(3)\nv_mfma_f32_16x16x32_bf16 v[64:67], v[152:155], v[48:51], v[64:67]\nbuffer_load_dwordx4 v10, s[12:15], s53 offen lds\n"
"v_mfma_f32_16x16x32_bf16 v[68:71], v[156:159], v[48:51], v[68:71]\nds_read_b128 v[48:51], v34 offset:8192\ns_add_u32 m0, s52, 12288\ns_waitcnt lgkmcnt(3)\n"
"v_mfma_f32_16x16x32_bf16 v[72:75], v[152:155], v[52:55], v[72:75]\ns_add_u32 s53, s49, 384\nv_mfma_f32_16x16x32_bf16 v[76:79], v[156:159], v[52:55], v[76:79]\nds_read_b128 v[52:55], v35 offset:8192\n"
"buffer_load_dwordx4 v10, s[12:15], s53 offen lds\ns_waitcnt lgkmcnt(3)\nv_mfma_f32_16x16x32_bf16 v[64:67], v[160:163], v[40:43], v[64:67]\ns_add_u32 m0, s52, 16384\n"
"v_mfma_f32_16x16x32_bf16 v[68:71], v[164:167], v[40:43], v[68:71]\nds_read_b128 v[40:43], v32 offset:12288\ns_add_u32 s53, s49, 512\ns_waitcnt lgkmcnt(3)\n"
"v_mfma_f32_16x16x32_bf16 v[72:75], v[160:163], v[44:47], v[72:75]\nbuffer_load_dwordx4 v10, s[12:15], s53 offen lds\nv_mfma_f32_16x16x32_bf16 v[76:79], v[164:167], v[44:47], v[76:79]\nds_read_b128 v[44:47], v33 offset:12288\n"
"s_add_u32 m0, s52, 20480\ns_waitcnt lgkmcnt(3)\nv_mfma_f32_16x16x32_bf16 v[64:67], v[168:171], v[48:51], v[64:67]\ns_add_u32 s53, s49, 640\n"
"v_mfma_f32_16x16x32_bf16 v[68:71], v[172:175], v[48:51], v[68:71]\nds_read_b128 v[48:51], v34 offset:12288\nbuffer_load_dwordx4 v10, s[12:15], s53 offen lds\ns_waitcnt lgkmcnt(3)\n"
"v_mfma_f32_16x16x32_bf16 v[72:75], v[168:171], v[52:55], v[72:75]\ns_add_u32 m0, s52, 24576\nv_mfma_f32_16x16x32_bf16 v[76:79], v[172:175], v[52:55], v[76:79]\nds_read_b128 v[52:55], v35 offset:12288\n"
"s_add_u32 s53, s49, 768\ns_waitcnt lgkmcnt(3)\nv_mfma_f32_16x16x32_bf16 v[64:67], v[176:179], v[40:43], v[64:67]\nbuffer_load_dwordx4 v10, s[12:15], s53 offen lds\n"
"v_mfma_f32_16x16x32_bf16 v[68:71], v[180:183], v[40:43], v[68:71]\nds_read_b128 v[40:43], v32 offset:16384\ns_waitcnt lgkmcnt(3)\nv_mfma_f32_16x16x32_bf16 v[72:75], v[176:179], v[44:47], v[72:75]\n"
"v_mfma_f32_16x16x32_bf16 v[76:79], v[180:183], v[44:47], v[76:79]\nds_read_b128 v[44:47], v33 offset:16384\ns_waitcnt lgkmcnt(3)\nv_mfma_f32_16x16x32_bf16 v[64:67], v[184:187], v[48:51], v[64:67]\n"
"v_mfma_f32_16x16x32_bf16 v[68:71], v[188:191], v[48:51], v[68:71]\nds_read_b128 v[48:51], v34 offset:16384\ns_waitcnt lgkmcnt(3)\nv_mfma_f32_16x16x32_bf16 v[72:75], v[184:187], v[52:55], v[72:75]\n"
"v_mfma_f32_16x16x32_bf16 v[76:79], v[188:191], v[52:55], v[76:79]\nds_read_b128 v[52:55], v35 offset:16384\ns_waitcnt vmcnt(16)\ns_waitcnt lgkmcnt(3)\n"
"v_mfma_f32_16x16x32_bf16 v[64:67], v[192:195], v[40:43], v[64:67]\nv_mfma_f32_16x16x32_bf16 v[68:71], v[196:199], v[40:43], v[68:71]\nds_read_b128 v[40:43], v32 offset:20480\ns_waitcnt lgkmcnt(3)\n"
"v_mfma_f32_16x16x32_bf16 v[72:75], v[192:195], v[44:47], v[72:75]\nv_mfma_f32_16x16x32_bf16 v[76:79], v[196:199], v[44:47], v[76:79]\nds_read_b128 v[44:47], v33 offset:20480\ns_waitcnt lgkmcnt(3)\n"
"v_mfma_f32_16x16x32_bf16 v[64:67], v[200:203], v[48:51], v[64:67]\nv_mfma_f32_16x16x32_bf16 v[68:71], v[204:207], v[48:51], v[68:71]\nds_read_b128 v[48:51], v34 offset:20480\ns_waitcnt lgkmcnt(3)\n"
"v_mfma_f32_16x16x32_bf16 v[72:75], v[200:203], v[52:55], v[72:75]\nv_mfma_f32_16x16x32_bf16 v[76:79], v[204:207], v[52:55], v[76:79]\nds_read_b128 v[52:55], v35 offset:20480\ns_waitcnt lgkmcnt(3)\n"
"v_mfma_f32_16x16x32_bf16 v[64:67], v[208:211], v[40:43], v[64:67]\nv_mfma_f32_16x16x32_bf16 v[68:71], v[212:215], v[40:43], v[68:71]\nds_read_b128 v[40:43], v32 offset:24576\ns_waitcnt lgkmcnt(3)\n"
"v_mfma_f32_16x16x32_bf16 v[72:75], v[208:211], v[44:47], v[72:75]\nv_mfma_f32_16x16x32_bf16 v[76:79], v[212:215], v[44:47], v[76:79]\nds_read_b128 v[44:47], v33 offset:24576\ns_waitcnt lgkmcnt(3)\n"
"v_mfma_f32_16x16x32_bf16 v[64:67], v[216:219], v[48:51], v[64:67]\ns_add_u32 s54, s51, 32768\ns_cmp_eq_u32 s54, 163840\nv_mfma_f32_16x16x32_bf16 v[68:71], v[220:223], v[48:51], v[68:71]\n"
"s_cselect_b32 s54, 0, s54\ns_sub_u32 s54, s54, s51\nds_read_b128 v[48:51], v34 offset:24576\ns_waitcnt lgkmcnt(3)\n"
"v_mfma_f32_16x16x32_bf16 v[72:75], v[216:219], v[52:55], v[72:75]\nv_mfma_f32_16x16x32_bf16 v[76:79], v[220:223], v[52:55], v[76:79]\nds_read_b128 v[52:55], v35 offset:24576\ns_waitcnt lgkmcnt(3)\n"
"v_mfma_f32_16x16x32_bf16 v[64:67], v[224:227], v[40:43], v[64:67]\nv_add_u32 v32, s54, v32\nv_mfma_f32_16x16x32_bf16 v[68:71], v[228:231], v[40:43], v[68:71]\nds_read_b128 v[40:43], v32 offset:0\n"
"s_waitcnt lgkmcnt(3)\nv_mfma_f32_16x16x32_bf16 v[72:75], v[224:227], v[44:47], v[72:75]\nv_add_u32 v33, s54, v33\nv_mfma_f32_16x16x32_bf16 v[76:79], v[228:231], v[44:47], v[76:79]\n"
"ds_read_b128 v[44:47], v33 offset:0\ns_waitcnt lgkmcnt(3)\nv_mfma_f32_16x16x32_bf16 v[64:67], v[232:235], v[48:51], v[64:67]\nv_add_u32 v34, s54, v34\n"
"v_mfma_f32_16x16x32_bf16 v[68:71], v[236:239], v[48:51], v[68:71]\nds_read_b128 v[48:51], v34 offset:0\ns_waitcnt lgkmcnt(3)\nv_mfma_f32_16x16x32_bf16 v[72:75], v[232:235], v[52:55], v[72:75]\n"
"v_add_u32 v35, s54, v35\nv_mfma_f32_16x16x32_bf16 v[76:79], v[236:239], v[52:55], v[76:79]\nds_read_b128 v[52:55], v35 offset:0\ns_mov_b32 s52, s47\n"
"s_add_u32 s47, s51, s32\ns_add_u32 s51, s51, s54\ns_add_u32 s49, s49, 28672\ns_waitcnt vmcnt(8)\n"
"s_barrier\ns_waitcnt lgkmcnt(3)\nv_mfma_f32_16x16x32_bf16 v[96:99], v[128:131], v[40:43], 0\nv_mov_b32 v36, s48\n"
"s_add_u32 s48, s48, 1\ns_nop 0\nbuffer_store_dword v36, v19, s[24:27], s33 offen sc1\nv_mfma_f32_16x16x32_bf16 v[100:103], v[132:135], v[40:43], 0\n"
"ds_read_b128 v[40:43], v32 offset:4096\ns_waitcnt lgkmcnt(3)\nv_mfma_f32_16x16x32_bf16 v[104:107], v[128:131], v[44:47], 0\nv_mfma_f32_16x16x32_bf16 v[108:111], v[132:135], v[44:47], 0\n"
"ds_read_b128 v[44:47], v33 offset:4096\nv_cvt_pk_bf16_f32 v56, v64, v65\ns_waitcnt lgkmcnt(3)\nv_mfma_f32_16x16x32_bf16 v[96:99], v[136:139], v[48:51], v[96:99]\n"
"v_cvt_pk_bf16_f32 v57, v66, v67\nv_mfma_f32_16x16x32_bf16 v[100:103], v[140:143], v[48:51], v[100:103]\nds_read_b128 v[48:51], v34 offset:4096\nv_cvt_pk_bf16_f32 v58, v68, v69\n"
"s_waitcnt lgkmcnt(3)\nv_mfma_f32_16x16x32_bf16 v[104:107], v[136:139], v[52:55], v[104:107]\nv_cvt_pk_bf16_f32 v59, v70, v71\nv_mfma_f32_16x16x32_bf16 v[108:111], v[140:143], v[52:55], v[108:111]\n"
"ds_read_b128 v[52:55], v35 offset:4096\ns_waitcnt lgkmcnt(3)\nv_mfma_f32_16x16x32_bf16 v[96:99], v[144:147], v[40:43], v[96:99]\nv_mfma_f32_16x16x32_bf16 v[100:103], v[148:151], v[40:43], v[100:103]\n"
"ds_read_b128 v[40:43], v32 offset:8192\nbuffer_store_dwordx4 v[56:59], v15, s[20:23], s50 offen sc1\ns_waitcnt lgkmcnt(3)\nv_mfma_f32_16x16x32_bf16 v[104:107], v[144:147], v[44:47], v[104:107]\n"
"v_cvt_pk_bf16_f32 v60, v72, v73\nv_mfma_f32_16x16x32_bf16 v[108:111], v[148:151], v[44:47], v[108:111]\nds_read_b128 v[44:47], v33 offset:8192\nv_cvt_pk_bf16_f32 v61, v74, v75\n"
"s_waitcnt lgkmcnt(3)\nv_mfma_f32_16x16x32_bf16 v[96:99], v[152:155], v[48:51], v[96:99]\nv_cvt_pk_bf16_f32 v62, v76, v77\nv_mfma_f32_16x16x32_bf16 v[100:103], v[156:159], v[48:51], v[100:103]\n"
"ds_read_b128 v[48:51], v34 offset:8192\nv_cvt_pk_bf16_f32 v63, v78, v79\ns_waitcnt lgkmcnt(3)\nv_mfma_f32_16x16x32_bf16 v[104:107], v[152:155], v[52:55], v[104:107]\n"
"v_mfma_f32_16x16x32_bf16 v[108:111], v[156:159], v[52:55], v[108:111]\nds_read_b128 v[52:55], v35 offset:8192\ns_waitcnt lgkmcnt(3)\nv_mfma_f32_16x16x32_bf16 v[96:99], v[160:163], v[40:43], v[96:99]\n"
"buffer_store_dwordx4 v[60:63], v16, s[20:23], s50 offen sc1\nv_mfma_f32_16x16x32_bf16 v[100:103], v[164:167], v[40:43], v[100:103]\nds_read_b128 v[40:43], v32 offset:12288\ns_add_u32 s50, s50, s36\n"
"s_waitcnt lgkmcnt(3)\nv_mfma_f32_16x16x32_bf16 v[104:107], v[160:163], v[44:47], v[104:107]\ns_add_u32 m0, s52, 0\nv_mfma_f32_16x16x32_bf16 v[108:111], v[164:167], v[44:47], v[108:111]\n"
"ds_read_b128 v[44:47], v33 offset:12288\ns_add_u32 s53, s49, 0\ns_waitcnt lgkmcnt(3)\nv_mfma_f32_16x16x32_bf16 v[96:99], v[168:171], v[48:51], v[96:99]\n"
"buffer_load_dwordx4 v10, s[12:15], s53 offen lds\nv_mfma_f32_16x16x32_bf16 v[100:103], v[172:175], v[48:51], v[100:103]\nds_read_b128 v[48:51], v34 offset:12288\ns_add_u32 m0, s52, 4096\n"
"s_waitcnt lgkmcnt(3)\nv_mfma_f32_16x16x32_bf16 v[104:107], v[168:171], v[52:55], v[104:107]\ns_add_u32 s53, s49, 128\nv_mfma_f32_16x16x32_bf16 v[108:111], v[172:175], v[52:55], v[108:111]\n"
"ds_read_b128 v[52:55], v35 offset:12288\nbuffer_load_dwordx4 v10, s[12:15], s53 offen lds\ns_waitcnt lgkmcnt(3)\nv_mfma_f32_16x16x32_bf16 v[96:99], v[176:179], v[40:43], v[96:99]\n"
"s_add_u32 m0, s52, 8192\nv_mfma_f32_16x16x32_bf16 v[100:103], v[180:183], v[40:43], v[100:103]\nds_read_b128 v[40:43], v32 offset:16384\ns_add_u32 s53, s49, 256\n"
"s_waitcnt lgkmcnt(3)\nv_mfma_f32_16x16x32_bf16 v[104:107], v[176:179], v[44:47], v[104:107]\nbuffer_load_dwordx4 v10, s[12:15], s53 offen lds\nv_mfma_f32_16x16x32_bf16 v[108:111], v[180:183], v[44:47], v[108:111]\n"
"ds_read_b128 v[44:47], v33 offset:16384\ns_add_u32 m0, s52, 12288\ns_waitcnt lgkmcnt(3)\nv_mfma_f32_16x16x32_bf16 v[96:99], v[184:187], v[48:51], v[96:99]\n"
"s_add_u32 s53, s49, 384\nv_mfma_f32_16x16x32_bf16 v[100:103], v[188:191], v[48:51], v[100:103]\nds_read_b128 v[48:51], v34 offset:16384\nbuffer_load_dwordx4 v10, s[12:15], s53 offen lds\n"
"s_waitcnt lgkmcnt(3)\nv_mfma_f32_16x16x32_bf16 v[104:107], v[184:187], v[52:55], v[104:107]\ns_add_u32 m0, s52, 16384\nv_mfma_f32_16x16x32_bf16 v[108:111], v[188:191], v[52:55], v[108:111]\n"
"ds_read_b128 v[52:55], v35 offset:16384\ns_add_u32 s53, s49, 512\ns_waitcnt lgkmcnt(3)\nv_mfma_f32_16x16x32_bf16 v[96:99], v[192:195], v[40:43], v[96:99]\n"
"buffer_load_dwordx4 v10, s[12:15], s53 offen lds\nv_mfma_f32_16x16x32_bf16 v[100:103], v[196:199], v[40:43], v[100:103]\nds_read_b128 v[40:43], v32 offset:20480\ns_add_u32 m0, s52, 20480\n"
"s_waitcnt lgkmcnt(3)\nv_mfma_f32_16x16x32_bf16 v[104:107], v[192:195], v[44:47], v[104:107]\ns_add_u32 s53, s49, 640\nv_mfma_f32_16x16x32_bf16 v[108:111], v[196:199], v[44:47], v[108:111]\n"
"ds_read_b128 v[44:47], v33 offset:20480\nbuffer_load_dwordx4 v10, s[12:15], s53 offen lds\ns_waitcnt lgkmcnt(3)\nv_mfma_f32_16x16x32_bf16 v[96:99], v[200:203], v[48:51], v[96:99]\n"
"s_add_u32 m0, s52, 24576\nv_mfma_f32_16x16x32_bf16 v[100:103], v[204:207], v[48:51], v[100:103]\nds_read_b128 v[48:51], v34 offset:20480\ns_add_u32 s53, s49, 768\n"
"s_waitcnt lgkmcnt(3)\nv_mfma_f32_16x16x32_bf16 v[104:107], v[200:203], v[52:55], v[104:107]\nbuffer_load_dwordx4 v10, s[12:15], s53 offen lds\nv_mfma_f32_16x16x32_bf16 v[108:111], v[204:207], v[52:55], v[108:111]\n"
"ds_read_b128 v[52:55], v35 offset:20480\ns_waitcnt lgkmcnt(3)\nv_mfma_f32_16x16x32_bf16 v[96:99], v[208:211], v[40:43], v[96:99]\nv_mfma_f32_16x16x32_bf16 v[100:103], v[212:215], v[40:43], v[100:103]\n"
"ds_read_b128 v[40:43], v32 offset:24576\ns_waitcnt lgkmcnt(3)\nv_mfma_f32_16x16x32_bf16 v[104:107], v[208:211], v[44:47], v[104:107]\nv_mfma_f32_16x16x32_bf16 v[108:111], v[212:215], v[44:47], v[108:111]\n"
"ds_read_b128 v[44:47], v33 offset:24576\ns_waitcnt lgkmcnt(3)\nv_mfma_f32_16x16x32_bf16 v[96:99], v[216:219], v[48:51], v[96:99]\ns_add_u32 s54, s51, 32768\n"
"s_cmp_eq_u32 s54, 163840\nv_mfma_f32_16x16x32_bf16 v[100:103], v[220:223], v[48:51], v[100:103]\ns_cselect_b32 s54, 0, s54\ns_sub_u32 s54, s54, s51\n"
"ds_read_b128 v[48:51], v34 offset:24576\ns_waitcnt lgkmcnt(3)\nv_mfma_f32_16x16x32_bf16 v[104:107], v[216:219], v[52:55], v[104:107]\nv_mfma_f32_16x16x32_bf16 v[108:111], v[220:223], v[52:55], v[108:111]\n"
"ds_read_b128 v[52:55], v35 offset:24576\ns_waitcnt lgkmcnt(3)\nv_mfma_f32_16x16x32_bf16 v[96:99], v[224:227], v[40:43], v[96:99]\nv_add_u32 v32, s54, v32\n"
"v_mfma_f32_16x16x32_bf16 v[100:103], v[228:231], v[40:43], v[100:103]\nds_read_b128 v[40:43], v32 offset:0\ns_waitcnt lgkmcnt(3)\nv_mfma_f32_16x16x32_bf16 v[104:107], v[224:227], v[44:47], v[104:107]\n"
"v_add_u32 v33, s54, v33\nv_mfma_f32_16x16x32_bf16 v[108:111], v[228:231], v[44:47], v[108:111]\nds_read_b128 v[44:47], v33 offset:0\ns_waitcnt lgkmcnt(3)\n"
"v_mfma_f32_16x16x32_bf16 v[96:99], v[232:235], v[48:51], v[96:99]\nv_add_u32 v34, s54, v34\nv_mfma_f32_16x16x32_bf16 v[100:103], v[236:239], v[48:51], v[100:103]\nds_read_b128 v[48:51], v34 offset:0\n"
"s_waitcnt lgkmcnt(3)\nv_mfma_f32_16x16x32_bf16 v[104:107], v[232:235], v[52:55], v[104:107]\nv_add_u32 v35, s54, v35\nv_mfma_f32_16x16x32_bf16 v[108:111], v[236:239], v[52:55], v[108:111]\n"
"ds_read_b128 v[52:55], v35 offset:0\ns_mov_b32 s52, s47\ns_add_u32 s47, s51, s32\ns_add_u32 s51, s51, s54\n"
"s_add_u32 s49, s49, 28672\ns_waitcnt vmcnt(7)\ns_barrier\ns_waitcnt lgkmcnt(3)\n"
"v_mfma_f32_16x16x32_bf16 v[64:67], v[128:131], v[40:43], 0\nv_mov_b32 v36, s48\ns_add_u32 s48, s48, 1\ns_nop 0\n"
"buffer_store_dword v36, v19, s[24:27], s33 offen sc1\nv_mfma_f32_16x16x32_bf16 v[68:71], v[132:135], v[40:43], 0\nds_read_b128 v[40:43], v32 offset:4096\ns_waitcnt lgkmcnt(3)\n"
"v_mfma_f32_16x16x32_bf16 v[72:75], v[128:131], v[44:47], 0\nv_mfma_f32_16x16x32_bf16 v[76:79], v[132:135], v[44:47], 0\nds_read_b128 v[44:47], v33 offset:4096\nv_cvt_pk_bf16_f32 v56, v96, v97\n"
"s_waitcnt lgkmcnt(3)\nv_mfma_f32_16x16x32_bf16 v[64:67], v[136:139], v[48:51], v[64:67]\nv_cvt_pk_bf16_f32 v57, v98, v99\nv_mfma_f32_16x16x32_bf16 v[68:71], v[140:143], v[48:51], v[68:71]\n"
"ds_read_b128 v[48:51], v34 offset:4096\nv_cvt_pk_bf16_f32 v58, v100, v101\ns_waitcnt lgkmcnt(3)\nv_mfma_f32_16x16x32_bf16 v[72:75], v[136:139], v[52:55], v[72:75]\n"
"v_cvt_pk_bf16_f32 v59, v102, v103\nv_mfma_f32_16x16x32_bf16 v[76:79], v[140:143], v[52:55], v[76:79]\nds_read_b128 v[52:55], v35 offset:4096\ns_waitcnt lgkmcnt(3)\n"
"v_mfma_f32_16x16x32_bf16 v[64:67], v[144:147], v[40:43], v[64:67]\nv_mfma_f32_16x16x32_bf16 v[68:71], v[148:151], v[40:43], v[68:71]\nds_read_b128 v[40:43], v32 offset:8192\nbuffer_store_dwordx4 v[56:59], v15, s[20:23], s50 offen sc1\n"
"s_waitcnt lgkmcnt(3)\nv_mfma_f32_16x16x32_bf16 v[72:75], v[144:147], v[44:47], v[72:75]\nv_cvt_pk_bf16_f32 v60, v104, v105\nv_mfma_f32_16x16x32_bf16 v[76:79], v[148:151], v[44:47], v[76:79]\n"
"ds_read_b128 v[44:47], v33 offset:8192\nv_cvt_pk_bf16_f32 v61, v106, v107\ns_waitcnt lgkmcnt(3)\nv_mfma_f32_16x16x32_bf16 v[64:67], v[152:155], v[48:51], v[64:67]\n"
"v_cvt_pk_bf16_f32 v62, v108, v109\nv_mfma_f32_16x16x32_bf16 v[68:71], v[156:159], v[48:51], v[68:71]\nds_read_b128 v[48:51], v34 offset:8192\nv_cvt_pk_bf16_f32 v63, v110, v111\n"
"s_waitcnt lgkmcnt(3)\nv_mfma_f32_16x16x32_bf16 v[72:75], v[152:155], v[52:55], v[72:75]\nv_mfma_f32_16x16x32_bf16 v[76:79], v[156:159], v[52:55], v[76:79]\nds_read_b128 v[52:55], v35 offset:8192\n"
"s_waitcnt lgkmcnt(3)\nv_mfma_f32_16x16x32_bf16 v[64:67], v[160:163], v[40:43], v[64:67]\nbuffer_store_dwordx4 v[60:63], v16, s[20:23], s50 offen sc1\nv_mfma_f32_16x16x32_bf16 v[68:71], v[164:167], v[40:43], v[68:71]\n"
"ds_read_b128 v[40:43], v32 offset:12288\ns_add_u32 s50, s50, s36\ns_waitcnt lgkmcnt(3)\nv_mfma_f32_16x16x32_bf16 v[72:75], v[160:163], v[44:47], v[72:75]\n"
"s_add_u32 m0, s52, 0\nv_mfma_f32_16x16x32_bf16 v[76:79], v[164:167], v[44:47], v[76:79]\nds_read_b128 v[44:47], v33 offset:12288\ns_add_u32 s53, s49, 0\n"
"s_waitcnt lgkmcnt(3)\nv_mfma_f32_16x16x32_bf16 v[64:67], v[168:171], v[48:51], v[64:67]\nbuffer_load_dwordx4 v10, s[12:15], s53 offen lds\nv_mfma_f32_16x16x32_bf16 v[68:71], v[172:175], v[48:51], v[68:71]\n"
"ds_read_b128 v[48:51], v34 offset:12288\ns_add_u32 m0, s52, 4096\ns_waitcnt lgkmcnt(3)\nv_mfma_f32_16x16x32_bf16 v[72:75], v[168:171], v[52:55], v[72:75]\n"
"s_add_u32 s53, s49, 128\nv_mfma_f32_16x16x32_bf16 v[76:79], v[172:175], v[52:55], v[76:79]\nds_read_b128 v[52:55], v35 offset:12288\nbuffer_load_dwordx4 v10, s[12:15], s53 offen lds\n"
"s_waitcnt lgkmcnt(3)\nv_mfma_f32_16x16x32_bf16 v[64:67], v[176:179], v[40:43], v[64:67]\ns_add_u32 m0, s52, 8192\nv_mfma_f32_16x16x32_bf16 v[68:71], v[180:183], v[40:43], v[68:71]\n"
"ds_read_b128 v[40:43], v32 offset:16384\ns_add_u32 s53, s49, 256\ns_waitcnt lgkmcnt(3)\nv_mfma_f32_16x16x32_bf16 v[72:75], v[176:179], v[44:47], v[72:75]\n"
"buffer_load_dwordx4 v10, s[12:15], s53 offen lds\nv_mfma_f32_16x16x32_bf16 v[76:79], v[180:183], v[44:47], v[76:79]\nds_read_b128 v[44:47], v33 offset:16384\ns_add_u32 m0, s52, 12288\n"
"s_waitcnt lgkmcnt(3)\nv_mfma_f32_16x16x32_bf16 v[64:67], v[184:187], v[48:51], v[64:67]\ns_add_u32 s53, s49, 384\nv_mfma_f32_16x16x32_bf16 v[68:71], v[188:191], v[48:51], v[68:71]\n"
"ds_read_b128 v[48:51], v34 offset:16384\nbuffer_load_dwordx4 v10, s[12:15], s53 offen lds\ns_waitcnt lgkmcnt(3)\nv_mfma_f32_16x16x32_bf16 v[72:75], v[184:187], v[52:55], v[72:75]\n"
"s_add_u32 m0, s52, 16384\nv_mfma_f32_16x16x32_bf16 v[76:79], v[188:191], v[52:55], v[76:79]\nds_read_b128 v[52:55], v35 offset:16384\ns_add_u32 s53, s49, 512\n"
"s_waitcnt lgkmcnt(3)\nv_mfma_f32_16x16x32_bf16 v[64:67], v[192:195], v[40:43], v[64:67]\nbuffer_load_dwordx4 v10, s[12:15], s53 offen lds\nv_mfma_f32_16x16x32_bf16 v[68:71], v[196:199], v[40:43], v[68:71]\n"
"ds_read_b128 v[40:43], v32 offset:20480\ns_add_u32 m0, s52, 20480\ns_waitcnt lgkmcnt(3)\nv_mfma_f32_16x16x32_bf16 v[72:75], v[192:195], v[44:47], v[72:75]\n"
"s_add_u32 s53, s49, 640\nv_mfma_f32_16x16x32_bf16 v[76:79], v[196:199], v[44:47], v[76:79]\nds_read_b128 v[44:47], v33 offset:20480\nbuffer_load_dwordx4 v10, s[12:15], s53 offen lds\n"
"s_waitcnt lgkmcnt(3)\nv_mfma_f32_16x16x32_bf16 v[64:67], v[200:203], v[48:51], v[64:67]\ns_add_u32 m0, s52, 24576\nv_mfma_f32_16x16x32_bf16 v[68:71], v[204:207], v[48:51], v[68:71]\n"
"ds_read_b128 v[48:51], v34 offset:20480\ns_add_u32 s53, s49, 768\ns_waitcnt lgkmcnt(3)\nv_mfma_f32_16x16x32_bf16 v[72:75], v[200:203], v[52:55], v[72:75]\n"
"buffer_load_dwordx4 v10, s[12:15], s53 offen lds\nv_mfma_f32_16x16x32_bf16 v[76:79], v[204:207], v[52:55], v[76:79]\nds_read_b128 v[52:55], v35 offset:20480\ns_waitcnt lgkmcnt(3)\n"
"v_mfma_f32_16x16x32_bf16 v[64:67], v[208:211], v[40:43], v[64:67]\nv_mfma_f32_16x16x32_bf16 v[68:71], v[212:215], v[40:43], v[68:71]\nds_read_b128 v[40:43], v32 offset:24576\ns_waitcnt lgkmcnt(3)\n"
"v_mfma_f32_16x16x32_bf16 v[72:75], v[208:211], v[44:47], v[72:75]\nv_mfma_f32_16x16x32_bf16 v[76:79], v[212:215], v[44:47], v[76:79]\nds_read_b128 v[44:47], v33 offset:24576\ns_waitcnt lgkmcnt(3)\n"
"v_mfma_f32_16x16x32_bf16 v[64:67], v[216:219], v[48:51], v[64:67]\ns_add_u32 s54, s51, 32768\ns_cmp_eq_u32 s54, 163840\nv_mfma_f32_16x16x32_bf16 v[68:71], v[220:223], v[48:51], v[68:71]\n"
"s_cselect_b32 s54, 0, s54\ns_sub_u32 s54, s54, s51\nds_read_b128 v[48:51], v34 offset:24576\ns_waitcnt lgkmcnt(3)\n"
"v_mfma_f32_16x16x32_bf16 v[72:75], v[216:219], v[52:55], v[72:75]\nv_mfma_f32_16x16x32_bf16 v[76:79], v[220:223], v[52:55], v[76:79]\nds_read_b128 v[52:55], v35 offset:24576\ns_waitcnt lgkmcnt(3)\n"
"v_mfma_f32_16x16x32_bf16 v[64:67], v[224:227], v[40:43], v[64:67]\nv_add_u32 v32, s54, v32\nv_mfma_f32_16x16x32_bf16 v[68:71], v[228:231], v[40:43], v[68:71]\nds_read_b128 v[40:43], v32 offset:0\n"
"s_waitcnt lgkmcnt(3)\nv_mfma_f32_16x16x32_bf16 v[72:75], v[224:227], v[44:47], v[72:75]\nv_add_u32 v33, s54, v33\nv_mfma_f32_16x16x32_bf16 v[76:79], v[228:231], v[44:47], v[76:79]\n"
"ds_read_b128 v[44:47], v33 offset:0\ns_waitcnt lgkmcnt(3)\nv_mfma_f32_16x16x32_bf16 v[64:67], v[232:235], v[48:51], v[64:67]\nv_add_u32 v34, s54, v34\n"
"v_mfma_f32_16x16x32_bf16 v[68:71], v[236:239], v[48:51], v[68:71]\nds_read_b128 v[48:51], v34 offset:0\ns_waitcnt lgkmcnt(3)\nv_mfma_f32_16x16x32_bf16 v[72:75], v[232:235], v[52:55], v[72:75]\n"
"v_add_u32 v35, s54, v35\nv_mfma_f32_16x16x32_bf16 v[76:79], v[236:239], v[52:55], v[76:79]\nds_read_b128 v[52:55], v35 offset:0\ns_mov_b32 s52, s47\n"
"s_add_u32 s47, s51, s32\ns_add_u32 s51, s51, s54\ns_add_u32 s49, s49, 28672\ns_waitcnt vmcnt(7)\n"
"s_barrier\ns_waitcnt lgkmcnt(3)\nv_mfma_f32_16x16x32_bf16 v[96:99], v[128:131], v[40:43], 0\nv_mov_b32 v36, s48\n"
"s_add_u32 s48, s48, 1\ns_nop 0\nbuffer_store_dword v36, v19, s[24:27], s33 offen sc1\nv_mfma_f32_16x16x32_bf16 v[100:103], v[132:135], v[40:43], 0\n"
"ds_read_b128 v[40:43], v32 offset:4096\ns_waitcnt lgkmcnt(3)\nv_mfma_f32_16x16x32_bf16 v[104:107], v[128:131], v[44:47], 0\nv_mfma_f32_16x16x32_bf16 v[108:111], v[132:135], v[44:47], 0\n"
"ds_read_b128 v[44:47], v33 offset:4096\nv_cvt_pk_bf16_f32 v56, v64, v65\ns_waitcnt lgkmcnt(3)\nv_mfma_f32_16x16x32_bf16 v[96:99], v[136:139], v[48:51], v[96:99]\n"
"v_cvt_pk_bf16_f32 v57, v66, v67\nv_mfma_f32_16x16x32_bf16 v[100:103], v[140:143], v[48:51], v[100:103]\nds_read_b128 v[48:51], v34 offset:4096\nv_cvt_pk_bf16_f32 v58, v68, v69\n"
"s_waitcnt lgkmcnt(3)\nv_mfma_f32_16x16x32_bf16 v[104:107], v[136:139], v[52:55], v[104:107]\nv_cvt_pk_bf16_f32 v59, v70, v71\nv_mfma_f32_16x16x32_bf16 v[108:111], v[140:143], v[52:55], v[108:111]\n"
"ds_read_b128 v[52:55], v35 offset:4096\ns_waitcnt lgkmcnt(3)\nv_mfma_f32_16x16x32_bf16 v[96:99], v[144:147], v[40:43], v[96:99]\nv_mfma_f32_16x16x32_bf16 v[100:103], v[148:151], v[40:43], v[100:103]\n"
"ds_read_b128 v[40:43], v32 offset:8192\nbuffer_store_dwordx4 v[56:59], v15, s[20:23], s50 offen sc1\ns_waitcnt lgkmcnt(3)\nv_mfma_f32_16x16x32_bf16 v[104:107], v[144:147], v[44:47], v[104:107]\n"
"v_cvt_pk_bf16_f32 v60, v72, v73\nv_mfma_f32_16x16x32_bf16 v[108:111], v[148:151], v[44:47], v[108:111]\nds_read_b128 v[44:47], v33 offset:8192\nv_cvt_pk_bf16_f32 v61, v74, v75\n"
"s_waitcnt lgkmcnt(3)\nv_mfma_f32_16x16x32_bf16 v[96:99], v[152:155], v[48:51], v[96:99]\nv_cvt_pk_bf16_f32 v62, v76, v77\nv_mfma_f32_16x16x32_bf16 v[100:103], v[156:159], v[48:51], v[100:103]\n"
"ds_read_b128 v[48:51], v34 offset:8192\nv_cvt_pk_bf16_f32 v63, v78, v79\ns_waitcnt lgkmcnt(3)\nv_mfma_f32_16x16x32_bf16 v[104:107], v[152:155], v[52:55], v[104:107]\n"
"v_mfma_f32_16x16x32_bf16 v[108:111], v[156:159], v[52:55], v[108:111]\nds_read_b128 v[52:55], v35 offset:8192\ns_waitcnt lgkmcnt(3)\nv_mfma_f32_16x16x32_bf16 v[96:99], v[160:163], v[40:43], v[96:99]\n"
"buffer_store_dwordx4 v[60:63], v16, s[20:23], s50 offen sc1\nv_mfma_f32_16x16x32_bf16 v[100:103], v[164:167], v[40:43], v[100:103]\nds_read_b128 v[40:43], v32 offset:12288\ns_add_u32 s50, s50, s36\n"
"s_waitcnt lgkmcnt(3)\nv_mfma_f32_16x16x32_bf16 v[104:107], v[160:163], v[44:47], v[104:107]\ns_add_u32 m0, s52, 0\nv_mfma_f32_16x16x32_bf16 v[108:111], v[164:167], v[44:47], v[108:111]\n"
"ds_read_b128 v[44:47], v33 offset:12288\ns_add_u32 s53, s49, 0\ns_waitcnt lgkmcnt(3)\nv_mfma_f32_16x16x32_bf16 v[96:99], v[168:171], v[48:51], v[96:99]\n"
"buffer_load_dwordx4 v10, s[12:15], s53 offen lds\nv_mfma_f32_16x16x32_bf16 v[100:103], v[172:175], v[48:51], v[100:103]\nds_read_b128 v[48:51], v34 offset:12288\ns_add_u32 m0, s52, 4096\n"
"s_waitcnt lgkmcnt(3)\nv_mfma_f32_16x16x32_bf16 v[104:107], v[168:171], v[52:55], v[104:107]\ns_add_u32 s53, s49, 128\nv_mfma_f32_16x16x32_bf16 v[108:111], v[172:175], v[52:55], v[108:111]\n"
"ds_read_b128 v[52:55], v35 offset:12288\nbuffer_load_dwordx4 v10, s[12:15], s53 offen lds\ns_waitcnt lgkmcnt(3)\nv_mfma_f32_16x16x32_bf16 v[96:99], v[176:179], v[40:43], v[96:99]\n"
"s_add_u32 m0, s52, 8192\nv_mfma_f32_16x16x32_bf16 v[100:103], v[180:183], v[40:43], v[100:103]\nds_read_b128 v[40:43], v32 offset:16384\ns_add_u32 s53, s49, 256\n"
"s_waitcnt lgkmcnt(3)\nv_mfma_f32_16x16x32_bf16 v[104:107], v[176:179], v[44:47], v[104:107]\nbuffer_load_dwordx4 v10, s[12:15], s53 offen lds\nv_mfma_f32_16x16x32_bf16 v[108:111], v[180:183], v[44:47], v[108:111]\n"
"ds_read_b128 v[44:47], v33 offset:16384\ns_add_u32 m0, s52, 12288\ns_waitcnt lgkmcnt(3)\nv_mfma_f32_16x16x32_bf16 v[96:99], v[184:187], v[48:51], v[96:99]\n"
"s_add_u32 s53, s49, 384\nv_mfma_f32_16x16x32_bf16 v[100:103], v[188:191], v[48:51], v[100:103]\nds_read_b128 v[48:51], v34 offset:16384\nbuffer_load_dwordx4 v10, s[12:15], s53 offen lds\n"
"s_waitcnt lgkmcnt(3)\nv_mfma_f32_16x16x32_bf16 v[104:107], v[184:187], v[52:55], v[104:107]\ns_add_u32 m0, s52, 16384\nv_mfma_f32_16x16x32_bf16 v[108:111], v[188:191], v[52:55], v[108:111]\n"
"ds_read_b128 v[52:55], v35 offset:16384\ns_add_u32 s53, s49, 512\ns_waitcnt lgkmcnt(3)\nv_mfma_f32_16x16x32_bf16 v[96:99], v[192:195], v[40:43], v[96:99]\n"
"buffer_load_dwordx4 v10, s[12:15], s53 offen lds\nv_mfma_f32_16x16x32_bf16 v[100:103], v[196:199], v[40:43], v[100:103]\nds_read_b128 v[40:43], v32 offset:20480\ns_add_u32 m0, s52, 20480\n"
"s_waitcnt lgkmcnt(3)\nv_mfma_f32_16x16x32_bf16 v[104:107], v[192:195], v[44:47], v[104:107]\ns_add_u32 s53, s49, 640\nv_mfma_f32_16x16x32_bf16 v[108:111], v[196:199], v[44:47], v[108:111]\n"
"ds_read_b128 v[44:47], v33 offset:20480\nbuffer_load_dwordx4 v10, s[12:15], s53 offen lds\ns_waitcnt lgkmcnt(3)\nv_mfma_f32_16x16x32_bf16 v[96:99], v[200:203], v[48:51], v[96:99]\n"
"s_add_u32 m0, s52, 24576\nv_mfma_f32_16x16x32_bf16 v[100:103], v[204:207], v[48:51], v[100:103]\nds_read_b128 v[48:51], v34 offset:20480\ns_add_u32 s53, s49, 768\n"
"s_waitcnt lgkmcnt(3)\nv_mfma_f32_16x16x32_bf16 v[104:107], v[200:203], v[52:55], v[104:107]\nbuffer_load_dwordx4 v10, s[12:15], s53 offen lds\nv_mfma_f32_16x16x32_bf16 v[108:111], v[204:207], v[52:55], v[108:111]\n"
"ds_read_b128 v[52:55], v35 offset:20480\ns_waitcnt lgkmcnt(3)\nv_mfma_f32_16x16x32_bf16 v[96:99], v[208:211], v[40:43], v[96:99]\nv_mfma_f32_16x16x32_bf16 v[100:103], v[212:215], v[40:43], v[100:103]\n"
"ds_read_b128 v[40:43], v32 offset:24576\ns_waitcnt lgkmcnt(3)\nv_mfma_f32_16x16x32_bf16 v[104:107], v[208:211], v[44:47], v[104:107]\nv_mfma_f32_16x16x32_bf16 v[108:111], v[212:215], v[44:47], v[108:111]\n"
"ds_read_b128 v[44:47], v33 offset:24576\ns_waitcnt lgkmcnt(3)\nv_mfma_f32_16x16x32_bf16 v[96:99], v[216:219], v[48:51], v[96:99]\ns_add_u32 s54, s51, 32768\n"
"s_cmp_eq_u32 s54, 163840\nv_mfma_f32_16x16x32_bf16 v[100:103], v[220:223], v[48:51], v[100:103]\ns_cselect_b32 s54, 0, s54\ns_sub_u32 s54, s54, s51\n"
"ds_read_b128 v[48:51], v34 offset:24576\ns_waitcnt lgkmcnt(3)\nv_mfma_f32_16x16x32_bf16 v[104:107], v[216:219], v[52:55], v[104:107]\nv_mfma_f32_16x16x32_bf16 v[108:111], v[220:223], v[52:55], v[108:111]\n"
"ds_read_b128 v[52:55], v35 offset:24576\ns_waitcnt lgkmcnt(3)\nv_mfma_f32_16x16x32_bf16 v[96:99], v[224:227], v[40:43], v[96:99]\nv_add_u32 v32, s54, v32\n"
"v_mfma_f32_16x16x32_bf16 v[100:103], v[228:231], v[40:43], v[100:103]\nds_read_b128 v[40:43], v32 offset:0\ns_waitcnt lgkmcnt(3)\nv_mfma_f32_16x16x32_bf16 v[104:107], v[224:227], v[44:47], v[104:107]\n"
"v_add_u32 v33, s54, v33\nv_mfma_f32_16x16x32_bf16 v[108:111], v[228:231], v[44:47], v[108:111]\nds_read_b128 v[44:47], v33 offset:0\ns_waitcnt lgkmcnt(3)\n"
"v_mfma_f32_16x16x32_bf16 v[96:99], v[232:235], v[48:51], v[96:99]\nv_add_u32 v34, s54, v34\nv_mfma_f32_16x16x32_bf16 v[100:103], v[236:239], v[48:51], v[100:103]\nds_read_b128 v[48:51], v34 offset:0\n"
"s_waitcnt lgkmcnt(3)\nv_mfma_f32_16x16x32_bf16 v[104:107], v[232:235], v[52:55], v[104:107]\nv_add_u32 v35, s54, v35\nv_mfma_f32_16x16x32_bf16 v[108:111], v[236:239], v[52:55], v[108:111]\n"
"ds_read_b128 v[52:55], v35 offset:0\ns_mov_b32 s52, s47\ns_add_u32 s47, s51, s32\ns_add_u32 s51, s51, s54\n"
"s_add_u32 s49, s49, 28672\ns_waitcnt vmcnt(7)\ns_barrier\ns_waitcnt lgkmcnt(3)\n"
"v_mfma_f32_16x16x32_bf16 v[64:67], v[128:131], v[40:43], 0\nv_mov_b32 v36, s48\ns_add_u32 s48, s48, 1\ns_nop 0\n"
"buffer_store_dword v36, v19, s[24:27], s33 offen sc1\nv_mfma_f32_16x16x32_bf16 v[68:71], v[132:135], v[40:43], 0\nds_read_b128 v[40:43], v32 offset:4096\ns_waitcnt lgkmcnt(3)\n"
"v_mfma_f32_16x16x32_bf16 v[72:75], v[128:131], v[44:47], 0\nv_mfma_f32_16x16x32_bf16 v[76:79], v[132:135], v[44:47], 0\nds_read_b128 v[44:47], v33 offset:4096\nv_cvt_pk_bf16_f32 v56, v96, v97\n"
"s_waitcnt lgkmcnt(3)\nv_mfma_f32_16x16x32_bf16 v[64:67], v[136:139], v[48:51], v[64:67]\nv_cvt_pk_bf16_f32 v57, v98, v99\nv_mfma_f32_16x16x32_bf16 v[68:71], v[140:143], v[48:51], v[68:71]\n"
"ds_read_b128 v[48:51], v34 offset:4096\nv_cvt_pk_bf16_f32 v58, v100, v101\ns_waitcnt lgkmcnt(3)\nv_mfma_f32_16x16x32_bf16 v[72:75], v[136:139], v[52:55], v[72:75]\n"
"v_cvt_pk_bf16_f32 v59, v102, v103\nv_mfma_f32_16x16x32_bf16 v[76:79], v[140:143], v[52:55], v[76:79]\nds_read_b128 v[52:55], v35 offset:4096\ns_waitcnt lgkmcnt(3)\n"
"v_mfma_f32_16x16x32_bf16 v[64:67], v[144:147], v[40:43], v[64:67]\nv_mfma_f32_16x16x32_bf16 v[68:71], v[148:151], v[40:43], v[68:71]\nds_read_b128 v[40:43], v32 offset:8192\nbuffer_store_dwordx4 v[56:59], v15, s[20:23], s50 offen sc1\n"
"s_waitcnt lgkmcnt(3)\nv_mfma_f32_16x16x32_bf16 v[72:75], v[144:147], v[44:47], v[72:75]\nv_cvt_pk_bf16_f32 v60, v104, v105\nv_mfma_f32_16x16x32_bf16 v[76:79], v[148:151], v[44:47], v[76:79]\n"
"ds_read_b128 v[44:47], v33 offset:8192\nv_cvt_pk_bf16_f32 v61, v106, v107\ns_waitcnt lgkmcnt(3)\nv_mfma_f32_16x16x32_bf16 v[64:67], v[152:155], v[48:51], v[64:67]\n"
"v_cvt_pk_bf16_f32 v62, v108, v109\nv_mfma_f32_16x16x32_bf16 v[68:71], v[156:159], v[48:51], v[68:71]\nds_read_b128 v[48:51], v34 offset:8192\nv_cvt_pk_bf16_f32 v63, v110, v111\n"
"s_waitcnt lgkmcnt(3)\nv_mfma_f32_16x16x32_bf16 v[72:75], v[152:155], v[52:55], v[72:75]\nv_mfma_f32_16x16x32_bf16 v[76:79], v[156:159], v[52:55], v[76:79]\nds_read_b128 v[52:55], v35 offset:8192\n"
"s_waitcnt lgkmcnt(3)\nv_mfma_f32_16x16x32_bf16 v[64:67], v[160:163], v[40:43], v[64:67]\nbuffer_store_dwordx4 v[60:63], v16, s[20:23], s50 offen sc1\nv_mfma_f32_16x16x32_bf16 v[68:71], v[164:167], v[40:43], v[68:71]\n"
"ds_read_b128 v[40:43], v32 offset:12288\ns_add_u32 s50, s50, s36\ns_waitcnt lgkmcnt(3)\nv_mfma_f32_16x16x32_bf16 v[72:75], v[160:163], v[44:47], v[72:75]\n"
"s_add_u32 m0, s52, 0\nv_mfma_f32_16x16x32_bf16 v[76:79], v[164:167], v[44:47], v[76:79]\nds_read_b128 v[44:47], v33 offset:12288\ns_add_u32 s53, s49, 0\n"
"s_waitcnt lgkmcnt(3)\nv_mfma_f32_16x16x32_bf16 v[64:67], v[168:171], v[48:51], v[64:67]\nbuffer_load_dwordx4 v10, s[12:15], s53 offen lds\nv_mfma_f32_16x16x32_bf16 v[68:71], v[172:175], v[48:51], v[68:71]\n"
"ds_read_b128 v[48:51], v34 offset:12288\ns_add_u32 m0, s52, 4096\ns_waitcnt lgkmcnt(3)\nv_mfma_f32_16x16x32_bf16 v[72:75], v[168:171], v[52:55], v[72:75]\n"
"s_add_u32 s53, s49, 128\nv_mfma_f32_16x16x32_bf16 v[76:79], v[172:175], v[52:55], v[76:79]\nds_read_b128 v[52:55], v35 offset:12288\nbuffer_load_dwordx4 v10, s[12:15], s53 offen lds\n"
"s_waitcnt lgkmcnt(3)\nv_mfma_f32_16x16x32_bf16 v[64:67], v[176:179], v[40:43], v[64:67]\ns_add_u32 m0, s52, 8192\nv_mfma_f32_16x16x32_bf16 v[68:71], v[180:183], v[40:43], v[68:71]\n"
"ds_read_b128 v[40:43], v32 offset:16384\ns_add_u32 s53, s49, 256\ns_waitcnt lgkmcnt(3)\nv_mfma_f32_16x16x32_bf16 v[72:75], v[176:179], v[44:47], v[72:75]\n"
"buffer_load_dwordx4 v10, s[12:15], s53 offen lds\nv_mfma_f32_16x16x32_bf16 v[76:79], v[180:183], v[44:47], v[76:79]\nds_read_b128 v[44:47], v33 offset:16384\ns_add_u32 m0, s52, 12288\n"
"s_waitcnt lgkmcnt(3)\nv_mfma_f32_16x16x32_bf16 v[64:67], v[184:187], v[48:51], v[64:67]\ns_add_u32 s53, s49, 384\nv_mfma_f32_16x16x32_bf16 v[68:71], v[188:191], v[48:51], v[68:71]\n"
"ds_read_b128 v[48:51], v34 offset:16384\nbuffer_load_dwordx4 v10, s[12:15], s53 offen lds\ns_waitcnt lgkmcnt(3)\nv_mfma_f32_16x16x32_bf16 v[72:75], v[184:187], v[52:55], v[72:75]\n"
"s_add_u32 m0, s52, 16384\nv_mfma_f32_16x16x32_bf16 v[76:79], v[188:191], v[52:55], v[76:79]\nds_read_b128 v[52:55], v35 offset:16384\ns_add_u32 s53, s49, 512\n"
"s_waitcnt lgkmcnt(3)\nv_mfma_f32_16x16x32_bf16 v[64:67], v[192:195], v[40:43], v[64:67]\nbuffer_load_dwordx4 v10, s[12:15], s53 offen lds\nv_mfma_f32_16x16x32_bf16 v[68:71], v[196:199], v[40:43], v[68:71]\n"
"ds_read_b128 v[40:43], v32 offset:20480\ns_add_u32 m0, s52, 20480\ns_waitcnt lgkmcnt(3)\nv_mfma_f32_16x16x32_bf16 v[72:75], v[192:195], v[44:47], v[72:75]\n"
"s_add_u32 s53, s49, 640\nv_mfma_f32_16x16x32_bf16 v[76:79], v[196:199], v[44:47], v[76:79]\nds_read_b128 v[44:47], v33 offset:20480\nbuffer_load_dwordx4 v10, s[12:15], s53 offen lds\n"
"s_waitcnt lgkmcnt(3)\nv_mfma_f32_16x16x32_bf16 v[64:67], v[200:203], v[48:51], v[64:67]\ns_add_u32 m0, s52, 24576\nv_mfma_f32_16x16x32_bf16 v[68:71], v[204:207], v[48:51], v[68:71]\n"
"ds_read_b128 v[48:51], v34 offset:20480\ns_add_u32 s53, s49, 768\ns_waitcnt lgkmcnt(3)\nv_mfma_f32_16x16x32_bf16 v[72:75], v[200:203], v[52:55], v[72:75]\n"
"buffer_load_dwordx4 v10, s[12:15], s53 offen lds\nv_mfma_f32_16x16x32_bf16 v[76:79], v[204:207], v[52:55], v[76:79]\nds_read_b128 v[52:55], v35 offset:20480\ns_waitcnt lgkmcnt(3)\n"
"v_mfma_f32_16x16x32_bf16 v[64:67], v[208:211], v[40:43], v[64:67]\nv_mfma_f32_16x16x32_bf16 v[68:71], v[212:215], v[40:43], v[68:71]\nds_read_b128 v[40:43], v32 offset:24576\ns_waitcnt lgkmcnt(3)\n"
"v_mfma_f32_16x16x32_bf16 v[72:75], v[208:211], v[44:47], v[72:75]\nv_mfma_f32_16x16x32_bf16 v[76:79], v[212:215], v[44:47], v[76:79]\nds_read_b128 v[44:47], v33 offset:24576\ns_waitcnt lgkmcnt(3)\n"
"v_mfma_f32_16x16x32_bf16 v[64:67], v[216:219], v[48:51], v[64:67]\ns_add_u32 s54, s51, 32768\ns_cmp_eq_u32 s54, 163840\nv_mfma_f32_16x16x32_bf16 v[68:71], v[220:223], v[48:51], v[68:71]\n"
"s_cselect_b32 s54, 0, s54\ns_sub_u32 s54, s54, s51\nds_read_b128 v[48:51], v34 offset:24576\ns_waitcnt lgkmcnt(3)\n"
"v_mfma_f32_16x16x32_bf16 v[72:75], v[216:219], v[52:55], v[72:75]\nv_mfma_f32_16x16x32_bf16 v[76:79], v[220:223], v[52:55], v[76:79]\nds_read_b128 v[52:55], v35 offset:24576\ns_waitcnt lgkmcnt(3)\n"
"v_mfma_f32_16x16x32_bf16 v[64:67], v[224:227], v[40:43], v[64:67]\nv_add_u32 v32, s54, v32\nv_mfma_f32_16x16x32_bf16 v[68:71], v[228:231], v[40:43], v[68:71]\nds_read_b128 v[40:43], v32 offset:0\n"
"s_waitcnt lgkmcnt(3)\nv_mfma_f32_16x16x32_bf16 v[72:75], v[224:227], v[44:47], v[72:75]\nv_add_u32 v33, s54, v33\nv_mfma_f32_16x16x32_bf16 v[76:79], v[228:231], v[44:47], v[76:79]\n"
"ds_read_b128 v[44:47], v33 offset:0\ns_waitcnt lgkmcnt(3)\nv_mfma_f32_16x16x32_bf16 v[64:67], v[232:235], v[48:51], v[64:67]\nv_add_u32 v34, s54, v34\n"
"v_mfma_f32_16x16x32_bf16 v[68:71], v[236:239], v[48:51], v[68:71]\nds_read_b128 v[48:51], v34 offset:0\ns_waitcnt lgkmcnt(3)\nv_mfma_f32_16x16x32_bf16 v[72:75], v[232:235], v[52:55], v[72:75]\n"
"v_add_u32 v35, s54, v35\nv_mfma_f32_16x16x32_bf16 v[76:79], v[236:239], v[52:55], v[76:79]\nds_read_b128 v[52:55], v35 offset:0\ns_mov_b32 s52, s47\n"
"s_add_u32 s47, s51, s32\ns_add_u32 s51, s51, s54\ns_add_u32 s49, s49, 28672\ns_waitcnt vmcnt(7)\n"
"s_barrier\ns_waitcnt lgkmcnt(3)\nv_mfma_f32_16x16x32_bf16 v[96:99], v[128:131], v[40:43], 0\nv_mov_b32 v36, s48\n"
"s_add_u32 s48, s48, 1\ns_nop 0\nbuffer_store_dword v36, v19, s[24:27], s33 offen sc1\nv_mfma_f32_16x16x32_bf16 v[100:103], v[132:135], v[40:43], 0\n"
"ds_read_b128 v[40:43], v32 offset:4096\ns_waitcnt lgkmcnt(3)\nv_mfma_f32_16x16x32_bf16 v[104:107], v[128:131], v[44:47], 0\nv_mfma_f32_16x16x32_bf16 v[108:111], v[132:135], v[44:47], 0\n"
"ds_read_b128 v[44:47], v33 offset:4096\nv_cvt_pk_bf16_f32 v56, v64, v65\ns_waitcnt lgkmcnt(3)\nv_mfma_f32_16x16x32_bf16 v[96:99], v[136:139], v[48:51], v[96:99]\n"
"v_cvt_pk_bf16_f32 v57, v66, v67\nv_mfma_f32_16x16x32_bf16 v[100:103], v[140:143], v[48:51], v[100:103]\nds_read_b128 v[48:51], v34 offset:4096\nv_cvt_pk_bf16_f32 v58, v68, v69\n"
"s_waitcnt lgkmcnt(3)\nv_mfma_f32_16x16x32_bf16 v[104:107], v[136:139], v[52:55], v[104:107]\nv_cvt_pk_bf16_f32 v59, v70, v71\nv_mfma_f32_16x16x32_bf16 v[108:111], v[140:143], v[52:55], v[108:111]\n"
"ds_read_b128 v[52:55], v35 offset:4096\ns_waitcnt lgkmcnt(3)\nv_mfma_f32_16x16x32_bf16 v[96:99], v[144:147], v[40:43], v[96:99]\nv_mfma_f32_16x16x32_bf16 v[100:103], v[148:151], v[40:43], v[100:103]\n"
"ds_read_b128 v[40:43], v32 offset:8192\nbuffer_store_dwordx4 v[56:59], v15, s[20:23], s50 offen sc1\ns_waitcnt lgkmcnt(3)\nv_mfma_f32_16x16x32_bf16 v[104:107], v[144:147], v[44:47], v[104:107]\n"
"v_cvt_pk_bf16_f32 v60, v72, v73\nv_mfma_f32_16x16x32_bf16 v[108:111], v[148:151], v[44:47], v[108:111]\nds_read_b128 v[44:47], v33 offset:8192\nv_cvt_pk_bf16_f32 v61, v74, v75\n"
"s_waitcnt lgkmcnt(3)\nv_mfma_f32_16x16x32_bf16 v[96:99], v[152:155], v[48:51], v[96:99]\nv_cvt_pk_bf16_f32 v62, v76, v77\nv_mfma_f32_16x16x32_bf16 v[100:103], v[156:159], v[48:51], v[100:103]\n"
"ds_read_b128 v[48:51], v34 offset:8192\nv_cvt_pk_bf16_f32 v63, v78, v79\ns_waitcnt lgkmcnt(3)\nv_mfma_f32_16x16x32_bf16 v[104:107], v[152:155], v[52:55], v[104:107]\n"
"v_mfma_f32_16x16x32_bf16 v[108:111], v[156:159], v[52:55], v[108:111]\nds_read_b128 v[52:55], v35 offset:8192\ns_waitcnt lgkmcnt(3)\nv_mfma_f32_16x16x32_bf16 v[96:99], v[160:163], v[40:43], v[96:99]\n"
"buffer_store_dwordx4 v[60:63], v16, s[20:23], s50 offen sc1\nv_mfma_f32_16x16x32_bf16 v[100:103], v[164:167], v[40:43], v[100:103]\nds_read_b128 v[40:43], v32 offset:12288\ns_add_u32 s50, s50, s36\n"
"s_waitcnt lgkmcnt(3)\nv_mfma_f32_16x16x32_bf16 v[104:107], v[160:163], v[44:47], v[104:107]\ns_add_u32 m0, s52, 0\nv_mfma_f32_16x16x32_bf16 v[108:111], v[164:167], v[44:47], v[108:111]\n"
"ds_read_b128 v[44:47], v33 offset:12288\ns_add_u32 s53, s49, 0\ns_waitcnt lgkmcnt(3)\nv_mfma_f32_16x16x32_bf16 v[96:99], v[168:171], v[48:51], v[96:99]\n"
"buffer_load_dwordx4 v10, s[12:15], s53 offen lds\nv_mfma_f32_16x16x32_bf16 v[100:103], v[172:175], v[48:51], v[100:103]\nds_read_b128 v[48:51], v34 offset:12288\ns_add_u32 m0, s52, 4096\n"
"s_waitcnt lgkmcnt(3)\nv_mfma_f32_16x16x32_bf16 v[104:107], v[168:171], v[52:55], v[104:107]\ns_add_u32 s53, s49, 128\nv_mfma_f32_16x16x32_bf16 v[108:111], v[172:175], v[52:55], v[108:111]\n"
"ds_read_b128 v[52:55], v35 offset:12288\nbuffer_load_dwordx4 v10, s[12:15], s53 offen lds\ns_waitcnt lgkmcnt(3)\nv_mfma_f32_16x16x32_bf16 v[96:99], v[176:179], v[40:43], v[96:99]\n"
"s_add_u32 m0, s52, 8192\nv_mfma_f32_16x16x32_bf16 v[100:103], v[180:183], v[40:43], v[100:103]\nds_read_b128 v[40:43], v32 offset:16384\ns_add_u32 s53, s49, 256\n"
"s_waitcnt lgkmcnt(3)\nv_mfma_f32_16x16x32_bf16 v[104:107], v[176:179], v[44:47], v[104:107]\nbuffer_load_dwordx4 v10, s[12:15], s53 offen lds\nv_mfma_f32_16x16x32_bf16 v[108:111], v[180:183], v[44:47], v[108:111]\n"
"ds_read_b128 v[44:47], v33 offset:16384\ns_add_u32 m0, s52, 12288\ns_waitcnt lgkmcnt(3)\nv_mfma_f32_16x16x32_bf16 v[96:99], v[184:187], v[48:51], v[96:99]\n"
"s_add_u32 s53, s49, 384\nv_mfma_f32_16x16x32_bf16 v[100:103], v[188:191], v[48:51], v[100:103]\nds_read_b128 v[48:51], v34 offset:16384\nbuffer_load_dwordx4 v10, s[12:15], s53 offen lds\n"
"s_waitcnt lgkmcnt(3)\nv_mfma_f32_16x16x32_bf16 v[104:107], v[184:187], v[52:55], v[104:107]\ns_add_u32 m0, s52, 16384\nv_mfma_f32_16x16x32_bf16 v[108:111], v[188:191], v[52:55], v[108:111]\n"
"ds_read_b128 v[52:55], v35 offset:16384\ns_add_u32 s53, s49, 512\ns_waitcnt lgkmcnt(3)\nv_mfma_f32_16x16x32_bf16 v[96:99], v[192:195], v[40:43], v[96:99]\n"
"buffer_load_dwordx4 v10, s[12:15], s53 offen lds\nv_mfma_f32_16x16x32_bf16 v[100:103], v[196:199], v[40:43], v[100:103]\nds_read_b128 v[40:43], v32 offset:20480\ns_add_u32 m0, s52, 20480\n"
"s_waitcnt lgkmcnt(3)\nv_mfma_f32_16x16x32_bf16 v[104:107], v[192:195], v[44:47], v[104:107]\ns_add_u32 s53, s49, 640\nv_mfma_f32_16x16x32_bf16 v[108:111], v[196:199], v[44:47], v[108:111]\n"
"ds_read_b128 v[44:47], v33 offset:20480\nbuffer_load_dwordx4 v10, s[12:15], s53 offen lds\ns_waitcnt lgkmcnt(3)\nv_mfma_f32_16x16x32_bf16 v[96:99], v[200:203], v[48:51], v[96:99]\n"
"s_add_u32 m0, s52, 24576\nv_mfma_f32_16x16x32_bf16 v[100:103], v[204:207], v[48:51], v[100:103]\nds_read_b128 v[48:51], v34 offset:20480\ns_add_u32 s53, s49, 768\n"
"s_waitcnt lgkmcnt(3)\nv_mfma_f32_16x16x32_bf16 v[104:107], v[200:203], v[52:55], v[104:107]\nbuffer_load_dwordx4 v10, s[12:15], s53 offen lds\nv_mfma_f32_16x16x32_bf16 v[108:111], v[204:207], v[52:55], v[108:111]\n"
"ds_read_b128 v[52:55], v35 offset:20480\ns_waitcnt lgkmcnt(3)\nv_mfma_f32_16x16x32_bf16 v[96:99], v[208:211], v[40:43], v[96:99]\nv_mfma_f32_16x16x32_bf16 v[100:103], v[212:215], v[40:43], v[100:103]\n"
"ds_read_b128 v[40:43], v32 offset:24576\ns_waitcnt lgkmcnt(3)\nv_mfma_f32_16x16x32_bf16 v[104:107], v[208:211], v[44:47], v[104:107]\nv_mfma_f32_16x16x32_bf16 v[108:111], v[212:215], v[44:47], v[108:111]\n"
"ds_read_b128 v[44:47], v33 offset:24576\ns_waitcnt lgkmcnt(3)\nv_mfma_f32_16x16x32_bf16 v[96:99], v[216:219], v[48:51], v[96:99]\ns_add_u32 s54, s51, 32768\n"
"s_cmp_eq_u32 s54, 163840\nv_mfma_f32_16x16x32_bf16 v[100:103], v[220:223], v[48:51], v[100:103]\ns_cselect_b32 s54, 0, s54\ns_sub_u32 s54, s54, s51\n"
"ds_read_b128 v[48:51], v34 offset:24576\ns_waitcnt lgkmcnt(3)\nv_mfma_f32_16x16x32_bf16 v[104:107], v[216:219], v[52:55], v[104:107]\nv_mfma_f32_16x16x32_bf16 v[108:111], v[220:223], v[52:55], v[108:111]\n"
"ds_read_b128 v[52:55], v35 offset:24576\ns_waitcnt lgkmcnt(3)\nv_mfma_f32_16x16x32_bf16 v[96:99], v[224:227], v[40:43], v[96:99]\nv_add_u32 v32, s54, v32\n"
"v_mfma_f32_16x16x32_bf16 v[100:103], v[228:231], v[40:43], v[100:103]\nds_read_b128 v[40:43], v32 offset:0\ns_waitcnt lgkmcnt(3)\nv_mfma_f32_16x16x32_bf16 v[104:107], v[224:227], v[44:47], v[104:107]\n"
"v_add_u32 v33, s54, v33\nv_mfma_f32_16x16x32_bf16 v[108:111], v[228:231], v[44:47], v[108:111]\nds_read_b128 v[44:47], v33 offset:0\ns_waitcnt lgkmcnt(3)\n"
"v_mfma_f32_16x16x32_bf16 v[96:99], v[232:235], v[48:51], v[96:99]\nv_add_u32 v34, s54, v34\nv_mfma_f32_16x16x32_bf16 v[100:103], v[236:239], v[48:51], v[100:103]\nds_read_b128 v[48:51], v34 offset:0\n"
"s_waitcnt lgkmcnt(3)\nv_mfma_f32_16x16x32_bf16 v[104:107], v[232:235], v[52:55], v[104:107]\nv_add_u32 v35, s54, v35\nv_mfma_f32_16x16x32_bf16 v[108:111], v[236:239], v[52:55], v[108:111]\n"
"ds_read_b128 v[52:55], v35 offset:0\ns_mov_b32 s52, s47\ns_add_u32 s47, s51, s32\ns_add_u32 s51, s51, s54\n"
"s_add_u32 s49, s49, 28672\ns_waitcnt vmcnt(7)\ns_barrier\ns_waitcnt lgkmcnt(3)\n"
"v_mfma_f32_16x16x32_bf16 v[64:67], v[128:131], v[40:43], 0\nv_mov_b32 v36, s48\ns_add_u32 s48, s48, 1\ns_nop 0\n"
"buffer_store_dword v36, v19, s[24:27], s33 offen sc1\nv_mfma_f32_16x16x32_bf16 v[68:71], v[132:135], v[40:43], 0\nds_read_b128 v[40:43], v32 offset:4096\ns_waitcnt lgkmcnt(3)\n"
"v_mfma_f32_16x16x32_bf16 v[72:75], v[128:131], v[44:47], 0\nv_mfma_f32_16x16x32_bf16 v[76:79], v[132:135], v[44:47], 0\nds_read_b128 v[44:47], v33 offset:4096\nv_cvt_pk_bf16_f32 v56, v96, v97\n"
"s_waitcnt lgkmcnt(3)\nv_mfma_f32_16x16x32_bf16 v[64:67], v[136:139], v[48:51], v[64:67]\nv_cvt_pk_bf16_f32 v57, v98, v99\nv_mfma_f32_16x16x32_bf16 v[68:71], v[140:143], v[48:51], v[68:71]\n"
"ds_read_b128 v[48:51], v34 offset:4096\nv_cvt_pk_bf16_f32 v58, v100, v101\ns_waitcnt lgkmcnt(3)\nv_mfma_f32_16x16x32_bf16 v[72:75], v[136:139], v[52:55], v[72:75]\n"
"v_cvt_pk_bf16_f32 v59, v102, v103\nv_mfma_f32_16x16x32_bf16 v[76:79], v[140:143], v[52:55], v[76:79]\nds_read_b128 v[52:55], v35 offset:4096\ns_waitcnt lgkmcnt(3)\n"
"v_mfma_f32_16x16x32_bf16 v[64:67], v[144:147], v[40:43], v[64:67]\nv_mfma_f32_16x16x32_bf16 v[68:71], v[148:151], v[40:43], v[68:71]\nds_read_b128 v[40:43], v32 offset:8192\nbuffer_store_dwordx4 v[56:59], v15, s[20:23], s50 offen sc1\n"
"s_waitcnt lgkmcnt(3)\nv_mfma_f32_16x16x32_bf16 v[72:75], v[144:147], v[44:47], v[72:75]\nv_cvt_pk_bf16_f32 v60, v104, v105\nv_mfma_f32_16x16x32_bf16 v[76:79], v[148:151], v[44:47], v[76:79]\n"
"ds_read_b128 v[44:47], v33 offset:8192\nv_cvt_pk_bf16_f32 v61, v106, v107\ns_waitcnt lgkmcnt(3)\nv_mfma_f32_16x16x32_bf16 v[64:67], v[152:155], v[48:51], v[64:67]\n"
"v_cvt_pk_bf16_f32 v62, v108, v109\nv_mfma_f32_16x16x32_bf16 v[68:71], v[156:159], v[48:51], v[68:71]\nds_read_b128 v[48:51], v34 offset:8192\nv_cvt_pk_bf16_f32 v63, v110, v111\n"
"s_waitcnt lgkmcnt(3)\nv_mfma_f32_16x16x32_bf16 v[72:75], v[152:155], v[52:55], v[72:75]\nv_mfma_f32_16x16x32_bf16 v[76:79], v[156:159], v[52:55], v[76:79]\nds_read_b128 v[52:55], v35 offset:8192\n"
"s_waitcnt lgkmcnt(3)\nv_mfma_f32_16x16x32_bf16 v[64:67], v[160:163], v[40:43], v[64:67]\nbuffer_store_dwordx4 v[60:63], v16, s[20:23], s50 offen sc1\nv_mfma_f32_16x16x32_bf16 v[68:71], v[164:167], v[40:43], v[68:71]\n"
"ds_read_b128 v[40:43], v32 offset:12288\ns_add_u32 s50, s50, s36\ns_waitcnt lgkmcnt(3)\nv_mfma_f32_16x16x32_bf16 v[72:75], v[160:163], v[44:47], v[72:75]\n"
"s_add_u32 m0, s52, 0\nv_mfma_f32_16x16x32_bf16 v[76:79], v[164:167], v[44:47], v[76:79]\nds_read_b128 v[44:47], v33 offset:12288\ns_add_u32 s53, s49, 0\n"
"s_waitcnt lgkmcnt(3)\nv_mfma_f32_16x16x32_bf16 v[64:67], v[168:171], v[48:51], v[64:67]\nbuffer_load_dwordx4 v10, s[12:15], s53 offen lds\nv_mfma_f32_16x16x32_bf16 v[68:71], v[172:175], v[48:51], v[68:71]\n"
"ds_read_b128 v[48:51], v34 offset:12288\ns_add_u32 m0, s52, 4096\ns_waitcnt lgkmcnt(3)\nv_mfma_f32_16x16x32_bf16 v[72:75], v[168:171], v[52:55], v[72:75]\n"
"s_add_u32 s53, s49, 128\nv_mfma_f32_16x16x32_bf16 v[76:79], v[172:175], v[52:55], v[76:79]\nds_read_b128 v[52:55], v35 offset:12288\nbuffer_load_dwordx4 v10, s[12:15], s53 offen lds\n"
"s_waitcnt lgkmcnt(3)\nv_mfma_f32_16x16x32_bf16 v[64:67], v[176:179], v[40:43], v[64:67]\ns_add_u32 m0, s52, 8192\nv_mfma_f32_16x16x32_bf16 v[68:71], v[180:183], v[40:43], v[68:71]\n"
"ds_read_b128 v[40:43], v32 offset:16384\ns_add_u32 s53, s49, 256\ns_waitcnt lgkmcnt(3)\nv_mfma_f32_16x16x32_bf16 v[72:75], v[176:179], v[44:47], v[72:75]\n"
"buffer_load_dwordx4 v10, s[12:15], s53 offen lds\nv_mfma_f32_16x16x32_bf16 v[76:79], v[180:183], v[44:47], v[76:79]\nds_read_b128 v[44:47], v33 offset:16384\ns_add_u32 m0, s52, 12288\n"
"s_waitcnt lgkmcnt(3)\nv_mfma_f32_16x16x32_bf16 v[64:67], v[184:187], v[48:51], v[64:67]\ns_add_u32 s53, s49, 384\nv_mfma_f32_16x16x32_bf16 v[68:71], v[188:191], v[48:51], v[68:71]\n"
"ds_read_b128 v[48:51], v34 offset:16384\nbuffer_load_dwordx4 v10, s[12:15], s53 offen lds\ns_waitcnt lgkmcnt(3)\nv_mfma_f32_16x16x32_bf16 v[72:75], v[184:187], v[52:55], v[72:75]\n"
"s_add_u32 m0, s52, 16384\nv_mfma_f32_16x16x32_bf16 v[76:79], v[188:191], v[52:55], v[76:79]\nds_read_b128 v[52:55], v35 offset:16384\ns_add_u32 s53, s49, 512\n"
"s_waitcnt lgkmcnt(3)\nv_mfma_f32_16x16x32_bf16 v[64:67], v[192:195], v[40:43], v[64:67]\nbuffer_load_dwordx4 v10, s[12:15], s53 offen lds\nv_mfma_f32_16x16x32_bf16 v[68:71], v[196:199], v[40:43], v[68:71]\n"
"ds_read_b128 v[40:43], v32 offset:20480\ns_add_u32 m0, s52, 20480\ns_waitcnt lgkmcnt(3)\nv_mfma_f32_16x16x32_bf16 v[72:75], v[192:195], v[44:47], v[72:75]\n"
"s_add_u32 s53, s49, 640\nv_mfma_f32_16x16x32_bf16 v[76:79], v[196:199], v[44:47], v[76:79]\nds_read_b128 v[44:47], v33 offset:20480\nbuffer_load_dwordx4 v10, s[12:15], s53 offen lds\n"
"s_waitcnt lgkmcnt(3)\nv_mfma_f32_16x16x32_bf16 v[64:67], v[200:203], v[48:51], v[64:67]\ns_add_u32 m0, s52, 24576\nv_mfma_f32_16x16x32_bf16 v[68:71], v[204:207], v[48:51], v[68:71]\n"
"ds_read_b128 v[48:51], v34 offset:20480\ns_add_u32 s53, s49, 768\ns_waitcnt lgkmcnt(3)\nv_mfma_f32_16x16x32_bf16 v[72:75], v[200:203], v[52:55], v[72:75]\n"
"buffer_load_dwordx4 v10, s[12:15], s53 offen lds\nv_mfma_f32_16x16x32_bf16 v[76:79], v[204:207], v[52:55], v[76:79]\nds_read_b128 v[52:55], v35 offset:20480\nglobal_load_dwordx4 v[240:243], v23, s[38:39] offset:0\n"
"s_waitcnt lgkmcnt(3)\nv_mfma_f32_16x16x32_bf16 v[64:67], v[208:211], v[40:43], v[64:67]\nv_mfma_f32_16x16x32_bf16 v[68:71], v[212:215], v[40:43], v[68:71]\nds_read_b128 v[40:43], v32 offset:24576\n"
"s_waitcnt lgkmcnt(3)\nv_mfma_f32_16x16x32_bf16 v[72:75], v[208:211], v[44:47], v[72:75]\nv_mfma_f32_16x16x32_bf16 v[76:79], v[212:215], v[44:47], v[76:79]\nds_read_b128 v[44:47], v33 offset:24576\n"
"s_waitcnt lgkmcnt(3)\nv_mfma_f32_16x16x32_bf16 v[64:67], v[216:219], v[48:51], v[64:67]\ns_add_u32 s54, s51, 32768\ns_cmp_eq_u32 s54, 163840\n"
"v_mfma_f32_16x16x32_bf16 v[68:71], v[220:223], v[48:51], v[68:71]\ns_cselect_b32 s54, 0, s54\ns_sub_u32 s54, s54, s51\nds_read_b128 v[48:51], v34 offset:24576\n"
"s_waitcnt lgkmcnt(3)\nv_mfma_f32_16x16x32_bf16 v[72:75], v[216:219], v[52:55], v[72:75]\nv_mfma_f32_16x16x32_bf16 v[76:79], v[220:223], v[52:55], v[76:79]\nds_read_b128 v[52:55], v35 offset:24576\n"
"s_waitcnt lgkmcnt(3)\nv_mfma_f32_16x16x32_bf16 v[64:67], v[224:227], v[40:43], v[64:67]\nv_add_u32 v32, s54, v32\nv_mfma_f32_16x16x32_bf16 v[68:71], v[228:231], v[40:43], v[68:71]\n"
"ds_read_b128 v[40:43], v32 offset:0\ns_waitcnt lgkmcnt(3)\nv_mfma_f32_16x16x32_bf16 v[72:75], v[224:227], v[44:47], v[72:75]\nv_add_u32 v33, s54, v33\n"
"v_mfma_f32_16x16x32_bf16 v[76:79], v[228:231], v[44:47], v[76:79]\nds_read_b128 v[44:47], v33 offset:0\ns_waitcnt lgkmcnt(3)\nv_mfma_f32_16x16x32_bf16 v[64:67], v[232:235], v[48:51], v[64:67]\n"
"v_add_u32 v34, s54, v34\nv_mfma_f32_16x16x32_bf16 v[68:71], v[236:239], v[48:51], v[68:71]\nds_read_b128 v[48:51], v34 offset:0\ns_waitcnt lgkmcnt(3)\n"
"v_mfma_f32_16x16x32_bf16 v[72:75], v[232:235], v[52:55], v[72:75]\nv_add_u32 v35, s54, v35\nv_mfma_f32_16x16x32_bf16 v[76:79], v[236:239], v[52:55], v[76:79]\nds_read_b128 v[52:55], v35 offset:0\n"
"s_mov_b32 s52, s47\ns_add_u32 s47, s51, s32\ns_add_u32 s51, s51, s54\ns_add_u32 s49, s49, 28672\n"
"s_waitcnt vmcnt(8)\ns_barrier\ns_waitcnt lgkmcnt(3)\nv_mfma_f32_16x16x32_bf16 v[96:99], v[128:131], v[40:43], 0\n"
"v_mov_b32 v36, s48\ns_add_u32 s48, s48, 1\ns_nop 0\nbuffer_store_dword v36, v19, s[24:27], s33 offen sc1\n"
"buffer_load_dword v37, v20, s[24:27], 0 offen sc1\nv_mfma_f32_16x16x32_bf16 v[100:103], v[132:135], v[40:43], 0\nds_read_b128 v[40:43], v32 offset:4096\ns_waitcnt lgkmcnt(3)\n"
"v_mfma_f32_16x16x32_bf16 v[104:107], v[128:131], v[44:47], 0\nv_mfma_f32_16x16x32_bf16 v[108:111], v[132:135], v[44:47], 0\nds_read_b128 v[44:47], v33 offset:4096\nv_cvt_pk_bf16_f32 v56, v64, v65\n"
"s_waitcnt lgkmcnt(3)\nv_mfma_f32_16x16x32_bf16 v[96:99], v[136:139], v[48:51], v[96:99]\nv_cvt_pk_bf16_f32 v57, v66, v67\nv_mfma_f32_16x16x32_bf16 v[100:103], v[140:143], v[48:51], v[100:103]\n"
"ds_read_b128 v[48:51], v34 offset:4096\nv_cvt_pk_bf16_f32 v58, v68, v69\ns_waitcnt lgkmcnt(3)\nv_mfma_f32_16x16x32_bf16 v[104:107], v[136:139], v[52:55], v[104:107]\n"
"v_cvt_pk_bf16_f32 v59, v70, v71\nv_mfma_f32_16x16x32_bf16 v[108:111], v[140:143], v[52:55], v[108:111]\nds_read_b128 v[52:55], v35 offset:4096\ns_waitcnt lgkmcnt(3)\n"
"v_mfma_f32_16x16x32_bf16 v[96:99], v[144:147], v[40:43], v[96:99]\nv_mfma_f32_16x16x32_bf16 v[100:103], v[148:151], v[40:43], v[100:103]\nds_read_b128 v[40:43], v32 offset:8192\nbuffer_store_dwordx4 v[56:59], v15, s[20:23], s50 offen sc1\n"
"s_waitcnt lgkmcnt(3)\nv_mfma_f32_16x16x32_bf16 v[104:107], v[144:147], v[44:47], v[104:107]\nv_cvt_pk_bf16_f32 v60, v72, v73\nv_mfma_f32_16x16x32_bf16 v[108:111], v[148:151], v[44:47], v[108:111]\n"
"ds_read_b128 v[44:47], v33 offset:8192\nv_cvt_pk_bf16_f32 v61, v74, v75\ns_waitcnt lgkmcnt(3)\nv_mfma_f32_16x16x32_bf16 v[96:99], v[152:155], v[48:51], v[96:99]\n"
"v_cvt_pk_bf16_f32 v62, v76, v77\nv_mfma_f32_16x16x32_bf16 v[100:103], v[156:159], v[48:51], v[100:103]\nds_read_b128 v[48:51], v34 offset:8192\nv_cvt_pk_bf16_f32 v63, v78, v79\n"
"s_waitcnt lgkmcnt(3)\nv_mfma_f32_16x16x32_bf16 v[104:107], v[152:155], v[52:55], v[104:107]\nv_mfma_f32_16x16x32_bf16 v[108:111], v[156:159], v[52:55], v[108:111]\nds_read_b128 v[52:55], v35 offset:8192\n"
"s_waitcnt lgkmcnt(3)\nv_mfma_f32_16x16x32_bf16 v[96:99], v[160:163], v[40:43], v[96:99]\nbuffer_store_dwordx4 v[60:63], v16, s[20:23], s50 offen sc1\nv_mfma_f32_16x16x32_bf16 v[100:103], v[164:167], v[40:43], v[100:103]\n"
"ds_read_b128 v[40:43], v32 offset:12288\ns_add_u32 s50, s50, s36\ns_waitcnt lgkmcnt(3)\nv_mfma_f32_16x16x32_bf16 v[104:107], v[160:163], v[44:47], v[104:107]\n"
"s_add_u32 m0, s52, 0\nv_mfma_f32_16x16x32_bf16 v[108:111], v[164:167], v[44:47], v[108:111]\nds_read_b128 v[44:47], v33 offset:12288\ns_add_u32 s53, s49, 0\n"
"s_waitcnt lgkmcnt(3)\nv_mfma_f32_16x16x32_bf16 v[96:99], v[168:171], v[48:51], v[96:99]\nbuffer_load_dwordx4 v10, s[12:15], s53 offen lds\nv_mfma_f32_16x16x32_bf16 v[100:103], v[172:175], v[48:51], v[100:103]\n"
"ds_read_b128 v[48:51], v34 offset:12288\ns_add_u32 m0, s52, 4096\ns_waitcnt lgkmcnt(3)\nv_mfma_f32_16x16x32_bf16 v[104:107], v[168:171], v[52:55], v[104:107]\n"
"s_add_u32 s53, s49, 128\nv_mfma_f32_16x16x32_bf16 v[108:111], v[172:175], v[52:55], v[108:111]\nds_read_b128 v[52:55], v35 offset:12288\nbuffer_load_dwordx4 v10, s[12:15], s53 offen lds\n"
"s_waitcnt lgkmcnt(3)\nv_mfma_f32_16x16x32_bf16 v[96:99], v[176:179], v[40:43], v[96:99]\ns_add_u32 m0, s52, 8192\nv_mfma_f32_16x16x32_bf16 v[100:103], v[180:183], v[40:43], v[100:103]\n"
"ds_read_b128 v[40:43], v32 offset:16384\ns_add_u32 s53, s49, 256\ns_waitcnt lgkmcnt(3)\nv_mfma_f32_16x16x32_bf16 v[104:107], v[176:179], v[44:47], v[104:107]\n"
"buffer_load_dwordx4 v10, s[12:15], s53 offen lds\nv_mfma_f32_16x16x32_bf16 v[108:111], v[180:183], v[44:47], v[108:111]\nds_read_b128 v[44:47], v33 offset:16384\ns_add_u32 m0, s52, 12288\n"
"s_waitcnt lgkmcnt(3)\nv_mfma_f32_16x16x32_bf16 v[96:99], v[184:187], v[48:51], v[96:99]\ns_add_u32 s53, s49, 384\nv_mfma_f32_16x16x32_bf16 v[100:103], v[188:191], v[48:51], v[100:103]\n"
"ds_read_b128 v[48:51], v34 offset:16384\nbuffer_load_dwordx4 v10, s[12:15], s53 offen lds\ns_waitcnt lgkmcnt(3)\nv_mfma_f32_16x16x32_bf16 v[104:107], v[184:187], v[52:55], v[104:107]\n"
"s_add_u32 m0, s52, 16384\nv_mfma_f32_16x16x32_bf16 v[108:111], v[188:191], v[52:55], v[108:111]\nds_read_b128 v[52:55], v35 offset:16384\ns_add_u32 s53, s49, 512\n"
"s_waitcnt lgkmcnt(3)\nv_mfma_f32_16x16x32_bf16 v[96:99], v[192:195], v[40:43], v[96:99]\nbuffer_load_dwordx4 v10, s[12:15], s53 offen lds\nv_mfma_f32_16x16x32_bf16 v[100:103], v[196:199], v[40:43], v[100:103]\n"
"ds_read_b128 v[40:43], v32 offset:20480\ns_add_u32 m0, s52, 20480\ns_waitcnt lgkmcnt(3)\nv_mfma_f32_16x16x32_bf16 v[104:107], v[192:195], v[44:47], v[104:107]\n"
"s_add_u32 s53, s49, 640\nv_mfma_f32_16x16x32_bf16 v[108:111], v[196:199], v[44:47], v[108:111]\nds_read_b128 v[44:47], v33 offset:20480\nbuffer_load_dwordx4 v10, s[12:15], s53 offen lds\n"
"s_waitcnt lgkmcnt(3)\nv_mfma_f32_16x16x32_bf16 v[96:99], v[200:203], v[48:51], v[96:99]\ns_add_u32 m0, s52, 24576\nv_mfma_f32_16x16x32_bf16 v[100:103], v[204:207], v[48:51], v[100:103]\n"
"ds_read_b128 v[48:51], v34 offset:20480\ns_add_u32 s53, s49, 768\ns_waitcnt lgkmcnt(3)\nv_mfma_f32_16x16x32_bf16 v[104:107], v[200:203], v[52:55], v[104:107]\n"
"buffer_load_dwordx4 v10, s[12:15], s53 offen lds\nv_mfma_f32_16x16x32_bf16 v[108:111], v[204:207], v[52:55], v[108:111]\nds_read_b128 v[52:55], v35 offset:20480\nglobal_load_dwordx4 v[240:243], v23, s[38:39] offset:1024\n"
"s_waitcnt lgkmcnt(3)\nv_mfma_f32_16x16x32_bf16 v[96:99], v[208:211], v[40:43], v[96:99]\nv_mfma_f32_16x16x32_bf16 v[100:103], v[212:215], v[40:43], v[100:103]\nds_read_b128 v[40:43], v32 offset:24576\n"
"s_waitcnt lgkmcnt(3)\nv_mfma_f32_16x16x32_bf16 v[104:107], v[208:211], v[44:47], v[104:107]\nv_mfma_f32_16x16x32_bf16 v[108:111], v[212:215], v[44:47], v[108:111]\nds_read_b128 v[44:47], v33 offset:24576\n"
"s_waitcnt lgkmcnt(3)\nv_mfma_f32_16x16x32_bf16 v[96:99], v[216:219], v[48:51], v[96:99]\ns_add_u32 s54, s51, 32768\ns_cmp_eq_u32 s54, 163840\n"
"v_mfma_f32_16x16x32_bf16 v[100:103], v[220:223], v[48:51], v[100:103]\ns_cselect_b32 s54, 0, s54\ns_sub_u32 s54, s54, s51\nds_read_b128 v[48:51], v34 offset:24576\n"
"s_waitcnt lgkmcnt(3)\nv_mfma_f32_16x16x32_bf16 v[104:107], v[216:219], v[52:55], v[104:107]\nv_mfma_f32_16x16x32_bf16 v[108:111], v[220:223], v[52:55], v[108:111]\nds_read_b128 v[52:55], v35 offset:24576\n"
"s_waitcnt lgkmcnt(3)\nv_mfma_f32_16x16x32_bf16 v[96:99], v[224:227], v[40:43], v[96:99]\nv_add_u32 v32, s54, v32\nv_mfma_f32_16x16x32_bf16 v[100:103], v[228:231], v[40:43], v[100:103]\n"
"ds_read_b128 v[40:43], v32 offset:0\ns_waitcnt lgkmcnt(3)\nv_mfma_f32_16x16x32_bf16 v[104:107], v[224:227], v[44:47], v[104:107]\nv_add_u32 v33, s54, v33\n"
"v_mfma_f32_16x16x32_bf16 v[108:111], v[228:231], v[44:47], v[108:111]\nds_read_b128 v[44:47], v33 offset:0\ns_waitcnt lgkmcnt(3)\nv_mfma_f32_16x16x32_bf16 v[96:99], v[232:235], v[48:51], v[96:99]\n"
"v_add_u32 v34, s54, v34\nv_mfma_f32_16x16x32_bf16 v[100:103], v[236:239], v[48:51], v[100:103]\nds_read_b128 v[48:51], v34 offset:0\ns_waitcnt lgkmcnt(3)\n"
"v_mfma_f32_16x16x32_bf16 v[104:107], v[232:235], v[52:55], v[104:107]\nv_add_u32 v35, s54, v35\nv_mfma_f32_16x16x32_bf16 v[108:111], v[236:239], v[52:55], v[108:111]\nds_read_b128 v[52:55], v35 offset:0\n"
"s_mov_b32 s52, s47\ns_add_u32 s47, s51, s32\ns_add_u32 s51, s51, s54\ns_add_u32 s49, s49, 28672\n"
"s_waitcnt vmcnt(8)\nfg0_pf:\nv_cmp_ne_u32 vcc, s28, v37\ns_cbranch_vccz fg0_pfok\n"
"s_add_u32 s55, s55, 1\ns_cmp_gt_u32 s55, 262144\ns_cbranch_scc0 fg0_pf2\ns_endpgm\n"
"fg0_pf2:\ns_sleep 1\nbuffer_load_dword v37, v20, s[24:27], 0 offen sc1\ns_waitcnt vmcnt(0)\n"
"s_branch fg0_pf\nfg0_pfok:\ns_barrier\ns_waitcnt lgkmcnt(3)\n"
"v_mfma_f32_16x16x32_bf16 v[64:67], v[128:131], v[40:43], 0\nv_mov_b32 v36, s48\ns_add_u32 s48, s48, 1\ns_nop 0\n"
"buffer_store_dword v36, v19, s[24:27], s33 offen sc1\nv_mfma_f32_16x16x32_bf16 v[68:71], v[132:135], v[40:43], 0\nds_read_b128 v[40:43], v32 offset:4096\ns_waitcnt lgkmcnt(3)\n"
"v_mfma_f32_16x16x32_bf16 v[72:75], v[128:131], v[44:47], 0\nv_mfma_f32_16x16x32_bf16 v[76:79], v[132:135], v[44:47], 0\nds_read_b128 v[44:47], v33 offset:4096\nv_cvt_pk_bf16_f32 v56, v96, v97\n"
"s_waitcnt lgkmcnt(3)\nv_mfma_f32_16x16x32_bf16 v[64:67], v[136:139], v[48:51], v[64:67]\nv_cvt_pk_bf16_f32 v57, v98, v99\nv_mfma_f32_16x16x32_bf16 v[68:71], v[140:143], v[48:51], v[68:71]\n"
"ds_read_b128 v[48:51], v34 offset:4096\nv_cvt_pk_bf16_f32 v58, v100, v101\ns_waitcnt lgkmcnt(3)\nv_mfma_f32_16x16x32_bf16 v[72:75], v[136:139], v[52:55], v[72:75]\n"
"v_cvt_pk_bf16_f32 v59, v102, v103\nv_mfma_f32_16x16x32_bf16 v[76:79], v[140:143], v[52:55], v[76:79]\nds_read_b128 v[52:55], v35 offset:4096\ns_waitcnt lgkmcnt(3)\n"
"v_mfma_f32_16x16x32_bf16 v[64:67], v[144:147], v[40:43], v[64:67]\nv_mfma_f32_16x16x32_bf16 v[68:71], v[148:151], v[40:43], v[68:71]\nds_read_b128 v[40:43], v32 offset:8192\nbuffer_store_dwordx4 v[56:59], v15, s[20:23], s50 offen sc1\n"
"s_waitcnt lgkmcnt(3)\nv_mfma_f32_16x16x32_bf16 v[72:75], v[144:147], v[44:47], v[72:75]\nv_cvt_pk_bf16_f32 v60, v104, v105\nv_mfma_f32_16x16x32_bf16 v[76:79], v[148:151], v[44:47], v[76:79]\n"
"ds_read_b128 v[44:47], v33 offset:8192\nv_cvt_pk_bf16_f32 v61, v106, v107\ns_waitcnt lgkmcnt(3)\nv_mfma_f32_16x16x32_bf16 v[64:67], v[152:155], v[48:51], v[64:67]\n"
"v_cvt_pk_bf16_f32 v62, v108, v109\nv_mfma_f32_16x16x32_bf16 v[68:71], v[156:159], v[48:51], v[68:71]\nds_read_b128 v[48:51], v34 offset:8192\nv_cvt_pk_bf16_f32 v63, v110, v111\n"
"s_waitcnt lgkmcnt(3)\nv_mfma_f32_16x16x32_bf16 v[72:75], v[152:155], v[52:55], v[72:75]\nv_mfma_f32_16x16x32_bf16 v[76:79], v[156:159], v[52:55], v[76:79]\nds_read_b128 v[52:55], v35 offset:8192\n"
"s_waitcnt lgkmcnt(3)\nv_mfma_f32_16x16x32_bf16 v[64:67], v[160:163], v[40:43], v[64:67]\nbuffer_store_dwordx4 v[60:63], v16, s[20:23], s50 offen sc1\nv_mfma_f32_16x16x32_bf16 v[68:71], v[164:167], v[40:43], v[68:71]\n"
"ds_read_b128 v[40:43], v32 offset:12288\ns_add_u32 s50, s50, s36\ns_waitcnt lgkmcnt(3)\nv_mfma_f32_16x16x32_bf16 v[72:75], v[160:163], v[44:47], v[72:75]\n"
"s_add_u32 m0, s52, 0\nv_mfma_f32_16x16x32_bf16 v[76:79], v[164:167], v[44:47], v[76:79]\nds_read_b128 v[44:47], v33 offset:12288\ns_add_u32 s53, s49, 0\n"
"s_waitcnt lgkmcnt(3)\nv_mfma_f32_16x16x32_bf16 v[64:67], v[168:171], v[48:51], v[64:67]\nbuffer_load_dwordx4 v10, s[12:15], s53 offen lds\nv_mfma_f32_16x16x32_bf16 v[68:71], v[172:175], v[48:51], v[68:71]\n"
"ds_read_b128 v[48:51], v34 offset:12288\ns_add_u32 m0, s52, 4096\ns_waitcnt lgkmcnt(3)\nv_mfma_f32_16x16x32_bf16 v[72:75], v[168:171], v[52:55], v[72:75]\n"
"s_add_u32 s53, s49, 128\nv_mfma_f32_16x16x32_bf16 v[76:79], v[172:175], v[52:55], v[76:79]\nds_read_b128 v[52:55], v35 offset:12288\nbuffer_load_dwordx4 v10, s[12:15], s53 offen lds\n"
"s_waitcnt lgkmcnt(3)\nv_mfma_f32_16x16x32_bf16 v[64:67], v[176:179], v[40:43], v[64:67]\ns_add_u32 m0, s52, 8192\nv_mfma_f32_16x16x32_bf16 v[68:71], v[180:183], v[40:43], v[68:71]\n"
"ds_read_b128 v[40:43], v32 offset:16384\ns_add_u32 s53, s49, 256\ns_waitcnt lgkmcnt(3)\nv_mfma_f32_16x16x32_bf16 v[72:75], v[176:179], v[44:47], v[72:75]\n"
"buffer_load_dwordx4 v10, s[12:15], s53 offen lds\nv_mfma_f32_16x16x32_bf16 v[76:79], v[180:183], v[44:47], v[76:79]\nds_read_b128 v[44:47], v33 offset:16384\ns_add_u32 m0, s52, 12288\n"
"s_waitcnt lgkmcnt(3)\nv_mfma_f32_16x16x32_bf16 v[64:67], v[184:187], v[48:51], v[64:67]\ns_add_u32 s53, s49, 384\nv_mfma_f32_16x16x32_bf16 v[68:71], v[188:191], v[48:51], v[68:71]\n"
"ds_read_b128 v[48:51], v34 offset:16384\nbuffer_load_dwordx4 v10, s[12:15], s53 offen lds\ns_waitcnt lgkmcnt(3)\nv_mfma_f32_16x16x32_bf16 v[72:75], v[184:187], v[52:55], v[72:75]\n"
"s_add_u32 m0, s52, 16384\nv_mfma_f32_16x16x32_bf16 v[76:79], v[188:191], v[52:55], v[76:79]\nds_read_b128 v[52:55], v35 offset:16384\ns_add_u32 s53, s49, 512\n"
"s_waitcnt lgkmcnt(3)\nv_mfma_f32_16x16x32_bf16 v[64:67], v[192:195], v[40:43], v[64:67]\nbuffer_load_dwordx4 v10, s[12:15], s53 offen lds\nv_mfma_f32_16x16x32_bf16 v[68:71], v[196:199], v[40:43], v[68:71]\n"
"ds_read_b128 v[40:43], v32 offset:20480\ns_add_u32 m0, s52, 20480\ns_waitcnt lgkmcnt(3)\nv_mfma_f32_16x16x32_bf16 v[72:75], v[192:195], v[44:47], v[72:75]\n"
"s_add_u32 s53, s49, 640\nv_mfma_f32_16x16x32_bf16 v[76:79], v[196:199], v[44:47], v[76:79]\nds_read_b128 v[44:47], v33 offset:20480\nbuffer_load_dwordx4 v10, s[12:15], s53 offen lds\n"
"s_waitcnt lgkmcnt(3)\nv_mfma_f32_16x16x32_bf16 v[64:67], v[200:203], v[48:51], v[64:67]\ns_add_u32 m0, s52, 24576\nv_mfma_f32_16x16x32_bf16 v[68:71], v[204:207], v[48:51], v[68:71]\n"
"ds_read_b128 v[48:51], v34 offset:20480\ns_add_u32 s53, s49, 768\ns_waitcnt lgkmcnt(3)\nv_mfma_f32_16x16x32_bf16 v[72:75], v[200:203], v[52:55], v[72:75]\n"
"buffer_load_dwordx4 v10, s[12:15], s53 offen lds\nv_mfma_f32_16x16x32_bf16 v[76:79], v[204:207], v[52:55], v[76:79]\nds_read_b128 v[52:55], v35 offset:20480\nglobal_load_dwordx4 v[240:243], v23, s[38:39] offset:2048\n"
"s_waitcnt lgkmcnt(3)\nv_mfma_f32_16x16x32_bf16 v[64:67], v[208:211], v[40:43], v[64:67]\ns_mov_b32 s53, 0\nbuffer_load_dwordx4 a[0:3], v14, s[16:19], s53 offen sc1\n"
"v_mfma_f32_16x16x32_bf16 v[68:71], v[212:215], v[40:43], v[68:71]\nds_read_b128 v[40:43], v32 offset:24576\ns_mov_b32 s53, 14336\nbuffer_load_dwordx4 a[4:7], v14, s[16:19], s53 offen sc1\n"
"s_waitcnt lgkmcnt(3)\nv_mfma_f32_16x16x32_bf16 v[72:75], v[208:211], v[44:47], v[72:75]\ns_mov_b32 s53, 1024\nbuffer_load_dwordx4 a[8:11], v14, s[16:19], s53 offen sc1\n"
"v_mfma_f32_16x16x32_bf16 v[76:79], v[212:215], v[44:47], v[76:79]\nds_read_b128 v[44:47], v33 offset:24576\ns_mov_b32 s53, 15360\nbuffer_load_dwordx4 a[12:15], v14, s[16:19], s53 offen sc1\n"
"s_waitcnt lgkmcnt(3)\nv_mfma_f32_16x16x32_bf16 v[64:67], v[216:219], v[48:51], v[64:67]\ns_add_u32 s54, s51, 32768\ns_cmp_eq_u32 s54, 163840\n"
"v_mfma_f32_16x16x32_bf16 v[68:71], v[220:223], v[48:51], v[68:71]\ns_cselect_b32 s54, 0, s54\ns_sub_u32 s54, s54, s51\nds_read_b128 v[48:51], v34 offset:24576\n"
"s_mov_b32 s53, 2048\nbuffer_load_dwordx4 a[16:19], v14, s[16:19], s53 offen sc1\ns_waitcnt lgkmcnt(3)\nv_mfma_f32_16x16x32_bf16 v[72:75], v[216:219], v[52:55], v[72:75]\n"
"v_mfma_f32_16x16x32_bf16 v[76:79], v[220:223], v[52:55], v[76:79]\nds_read_b128 v[52:55], v35 offset:24576\ns_waitcnt lgkmcnt(3)\nv_mfma_f32_16x16x32_bf16 v[64:67], v[224:227], v[40:43], v[64:67]\n"
"v_add_u32 v32, s54, v32\nv_mfma_f32_16x16x32_bf16 v[68:71], v[228:231], v[40:43], v[68:71]\nds_read_b128 v[40:43], v32 offset:0\ns_waitcnt lgkmcnt(3)\n"
"v_mfma_f32_16x16x32_bf16 v[72:75], v[224:227], v[44:47], v[72:75]\nv_add_u32 v33, s54, v33\nv_mfma_f32_16x16x32_bf16 v[76:79], v[228:231], v[44:47], v[76:79]\nds_read_b128 v[44:47], v33 offset:0\n"
"s_waitcnt lgkmcnt(3)\nv_mfma_f32_16x16x32_bf16 v[64:67], v[232:235], v[48:51], v[64:67]\nv_add_u32 v34, s54, v34\nv_mfma_f32_16x16x32_bf16 v[68:71], v[236:239], v[48:51], v[68:71]\n"
"ds_read_b128 v[48:51], v34 offset:0\ns_waitcnt lgkmcnt(3)\nv_mfma_f32_16x16x32_bf16 v[72:75], v[232:235], v[52:55], v[72:75]\nv_add_u32 v35, s54, v35\n"
"v_mfma_f32_16x16x32_bf16 v[76:79], v[236:239], v[52:55], v[76:79]\nds_read_b128 v[52:55], v35 offset:0\ns_mov_b32 s52, s47\ns_add_u32 s47, s51, s32\n"
"s_add_u32 s51, s51, s54\ns_add_u32 s49, s49, 28672\ns_waitcnt vmcnt(13)\ns_barrier\n"
"s_waitcnt lgkmcnt(3)\nv_mfma_f32_16x16x32_bf16 v[96:99], v[128:131], v[40:43], 0\nv_mov_b32 v36, s48\ns_add_u32 s48, s48, 1\n"
"s_nop 0\nbuffer_store_dword v36, v19, s[24:27], s33 offen sc1\nv_mfma_f32_16x16x32_bf16 v[100:103], v[132:135], v[40:43], 0\nds_read_b128 v[40:43], v32 offset:4096\n"
"s_waitcnt lgkmcnt(3)\nv_mfma_f32_16x16x32_bf16 v[104:107], v[128:131], v[44:47], 0\nv_mfma_f32_16x16x32_bf16 v[108:111], v[132:135], v[44:47], 0\nds_read_b128 v[44:47], v33 offset:4096\n"
"v_cvt_pk_bf16_f32 v56, v64, v65\ns_waitcnt lgkmcnt(3)\nv_mfma_f32_16x16x32_bf16 v[96:99], v[136:139], v[48:51], v[96:99]\nv_cvt_pk_bf16_f32 v57, v66, v67\n"
"v_mfma_f32_16x16x32_bf16 v[100:103], v[140:143], v[48:51], v[100:103]\nds_read_b128 v[48:51], v34 offset:4096\nv_cvt_pk_bf16_f32 v58, v68, v69\ns_waitcnt lgkmcnt(3)\n"
"v_mfma_f32_16x16x32_bf16 v[104:107], v[136:139], v[52:55], v[104:107]\nv_cvt_pk_bf16_f32 v59, v70, v71\nv_mfma_f32_16x16x32_bf16 v[108:111], v[140:143], v[52:55], v[108:111]\nds_read_b128 v[52:55], v35 offset:4096\n"
"s_waitcnt lgkmcnt(3)\nv_mfma_f32_16x16x32_bf16 v[96:99], v[144:147], v[40:43], v[96:99]\nv_mfma_f32_16x16x32_bf16 v[100:103], v[148:151], v[40:43], v[100:103]\nds_read_b128 v[40:43], v32 offset:8192\n"
"buffer_store_dwordx4 v[56:59], v15, s[20:23], s50 offen sc1\ns_waitcnt lgkmcnt(3)\nv_mfma_f32_16x16x32_bf16 v[104:107], v[144:147], v[44:47], v[104:107]\nv_cvt_pk_bf16_f32 v60, v72, v73\n"
"v_mfma_f32_16x16x32_bf16 v[108:111], v[148:151], v[44:47], v[108:111]\nds_read_b128 v[44:47], v33 offset:8192\nv_cvt_pk_bf16_f32 v61, v74, v75\ns_waitcnt lgkmcnt(3)\n"
"v_mfma_f32_16x16x32_bf16 v[96:99], v[152:155], v[48:51], v[96:99]\nv_cvt_pk_bf16_f32 v62, v76, v77\nv_mfma_f32_16x16x32_bf16 v[100:103], v[156:159], v[48:51], v[100:103]\nds_read_b128 v[48:51], v34 offset:8192\n"
"v_cvt_pk_bf16_f32 v63, v78, v79\ns_waitcnt lgkmcnt(3)\nv_mfma_f32_16x16x32_bf16 v[104:107], v[152:155], v[52:55], v[104:107]\nv_mfma_f32_16x16x32_bf16 v[108:111], v[156:159], v[52:55], v[108:111]\n"
"ds_read_b128 v[52:55], v35 offset:8192\ns_waitcnt lgkmcnt(3)\nv_mfma_f32_16x16x32_bf16 v[96:99], v[160:163], v[40:43], v[96:99]\nbuffer_store_dwordx4 v[60:63], v16, s[20:23], s50 offen sc1\n"
"v_mfma_f32_16x16x32_bf16 v[100:103], v[164:167], v[40:43], v[100:103]\nds_read_b128 v[40:43], v32 offset:12288\ns_add_u32 s50, s50, s36\ns_waitcnt lgkmcnt(3)\n"
"v_mfma_f32_16x16x32_bf16 v[104:107], v[160:163], v[44:47], v[104:107]\ns_add_u32 m0, s52, 0\nv_mfma_f32_16x16x32_bf16 v[108:111], v[164:167], v[44:47], v[108:111]\nds_read_b128 v[44:47], v33 offset:12288\n"
"s_add_u32 s53, s49, 0\ns_waitcnt lgkmcnt(3)\nv_mfma_f32_16x16x32_bf16 v[96:99], v[168:171], v[48:51], v[96:99]\nbuffer_load_dwordx4 v10, s[12:15], s53 offen lds\n"
"v_mfma_f32_16x16x32_bf16 v[100:103], v[172:175], v[48:51], v[100:103]\nds_read_b128 v[48:51], v34 offset:12288\ns_add_u32 m0, s52, 4096\ns_waitcnt lgkmcnt(3)\n"
"v_mfma_f32_16x16x32_bf16 v[104:107], v[168:171], v[52:55], v[104:107]\ns_add_u32 s53, s49, 128\nv_mfma_f32_16x16x32_bf16 v[108:111], v[172:175], v[52:55], v[108:111]\nds_read_b128 v[52:55], v35 offset:12288\n"
"buffer_load_dwordx4 v10, s[12:15], s53 offen lds\ns_waitcnt lgkmcnt(3)\nv_mfma_f32_16x16x32_bf16 v[96:99], v[176:179], v[40:43], v[96:99]\ns_add_u32 m0, s52, 8192\n"
"v_mfma_f32_16x16x32_bf16 v[100:103], v[180:183], v[40:43], v[100:103]\nds_read_b128 v[40:43], v32 offset:16384\ns_add_u32 s53, s49, 256\ns_waitcnt lgkmcnt(3)\n"
"v_mfma_f32_16x16x32_bf16 v[104:107], v[176:179], v[44:47], v[104:107]\nbuffer_load_dwordx4 v10, s[12:15], s53 offen lds\nv_mfma_f32_16x16x32_bf16 v[108:111], v[180:183], v[44:47], v[108:111]\nds_read_b128 v[44:47], v33 offset:16384\n"
"s_add_u32 m0, s52, 12288\ns_waitcnt lgkmcnt(3)\nv_mfma_f32_16x16x32_bf16 v[96:99], v[184:187], v[48:51], v[96:99]\ns_add_u32 s53, s49, 384\n"
"v_mfma_f32_16x16x32_bf16 v[100:103], v[188:191], v[48:51], v[100:103]\nds_read_b128 v[48:51], v34 offset:16384\nbuffer_load_dwordx4 v10, s[12:15], s53 offen lds\ns_waitcnt lgkmcnt(3)\n"
"v_mfma_f32_16x16x32_bf16 v[104:107], v[184:187], v[52:55], v[104:107]\ns_add_u32 m0, s52, 16384\nv_mfma_f32_16x16x32_bf16 v[108:111], v[188:191], v[52:55], v[108:111]\nds_read_b128 v[52:55], v35 offset:16384\n"
"s_add_u32 s53, s49, 512\ns_waitcnt lgkmcnt(3)\nv_mfma_f32_16x16x32_bf16 v[96:99], v[192:195], v[40:43], v[96:99]\nbuffer_load_dwordx4 v10, s[12:15], s53 offen lds\n"
"v_mfma_f32_16x16x32_bf16 v[100:103], v[196:199], v[40:43], v[100:103]\nds_read_b128 v[40:43], v32 offset:20480\ns_add_u32 m0, s52, 20480\ns_waitcnt lgkmcnt(3)\n"
"v_mfma_f32_16x16x32_bf16 v[104:107], v[192:195], v[44:47], v[104:107]\ns_add_u32 s53, s49, 640\nv_mfma_f32_16x16x32_bf16 v[108:111], v[196:199], v[44:47], v[108:111]\nds_read_b128 v[44:47], v33 offset:20480\n"
"buffer_load_dwordx4 v10, s[12:15], s53 offen lds\ns_waitcnt lgkmcnt(3)\nv_mfma_f32_16x16x32_bf16 v[96:99], v[200:203], v[48:51], v[96:99]\ns_add_u32 m0, s52, 24576\n"
"v_mfma_f32_16x16x32_bf16 v[100:103], v[204:207], v[48:51], v[100:103]\nds_read_b128 v[48:51], v34 offset:20480\ns_add_u32 s53, s49, 768\ns_waitcnt lgkmcnt(3)\n"
"v_mfma_f32_16x16x32_bf16 v[104:107], v[200:203], v[52:55], v[104:107]\nbuffer_load_dwordx4 v10, s[12:15], s53 offen lds\nv_mfma_f32_16x16x32_bf16 v[108:111], v[204:207], v[52:55], v[108:111]\nds_read_b128 v[52:55], v35 offset:20480\n"
"global_load_dwordx4 v[240:243], v23, s[38:39] offset:3072\ns_waitcnt lgkmcnt(3)\nv_mfma_f32_16x16x32_bf16 v[96:99], v[208:211], v[40:43], v[96:99]\ns_mov_b32 s53, 16384\n"
"buffer_load_dwordx4 a[20:23], v14, s[16:19], s53 offen sc1\nv_mfma_f32_16x16x32_bf16 v[100:103], v[212:215], v[40:43], v[100:103]\nds_read_b128 v[40:43], v32 offset:24576\ns_mov_b32 s53, 3072\n"
"buffer_load_dwordx4 a[24:27], v14, s[16:19], s53 offen sc1\ns_waitcnt lgkmcnt(3)\nv_mfma_f32_16x16x32_bf16 v[104:107], v[208:211], v[44:47], v[104:107]\ns_mov_b32 s53, 17408\n"
"buffer_load_dwordx4 a[28:31], v14, s[16:19], s53 offen sc1\nv_mfma_f32_16x16x32_bf16 v[108:111], v[212:215], v[44:47], v[108:111]\nds_read_b128 v[44:47], v33 offset:24576\ns_mov_b32 s53, 4096\n"
"buffer_load_dwordx4 a[32:35], v14, s[16:19], s53 offen sc1\ns_waitcnt lgkmcnt(3)\nv_mfma_f32_16x16x32_bf16 v[96:99], v[216:219], v[48:51], v[96:99]\ns_add_u32 s54, s51, 32768\n"
"s_cmp_eq_u32 s54, 163840\nv_mfma_f32_16x16x32_bf16 v[100:103], v[220:223], v[48:51], v[100:103]\ns_cselect_b32 s54, 0, s54\ns_sub_u32 s54, s54, s51\n"
"ds_read_b128 v[48:51], v34 offset:24576\ns_mov_b32 s53, 18432\nbuffer_load_dwordx4 a[36:39], v14, s[16:19], s53 offen sc1\ns_waitcnt lgkmcnt(3)\n"
"v_mfma_f32_16x16x32_bf16 v[104:107], v[216:219], v[52:55], v[104:107]\nv_mfma_f32_16x16x32_bf16 v[108:111], v[220:223], v[52:55], v[108:111]\nds_read_b128 v[52:55], v35 offset:24576\ns_waitcnt lgkmcnt(3)\n"
"v_mfma_f32_16x16x32_bf16 v[96:99], v[224:227], v[40:43], v[96:99]\nv_add_u32 v32, s54, v32\nv_mfma_f32_16x16x32_bf16 v[100:103], v[228:231], v[40:43], v[100:103]\nds_read_b128 v[40:43], v32 offset:0\n"
"s_waitcnt lgkmcnt(3)\nv_mfma_f32_16x16x32_bf16 v[104:107], v[224:227], v[44:47], v[104:107]\nv_add_u32 v33, s54, v33\nv_mfma_f32_16x16x32_bf16 v[108:111], v[228:231], v[44:47], v[108:111]\n"
"ds_read_b128 v[44:47], v33 offset:0\ns_waitcnt lgkmcnt(3)\nv_mfma_f32_16x16x32_bf16 v[96:99], v[232:235], v[48:51], v[96:99]\nv_add_u32 v34, s54, v34\n"
"v_mfma_f32_16x16x32_bf16 v[100:103], v[236:239], v[48:51], v[100:103]\nds_read_b128 v[48:51], v34 offset:0\ns_waitcnt lgkmcnt(3)\nv_mfma_f32_16x16x32_bf16 v[104:107], v[232:235], v[52:55], v[104:107]\n"
"v_add_u32 v35, s54, v35\nv_mfma_f32_16x16x32_bf16 v[108:111], v[236:239], v[52:55], v[108:111]\nds_read_b128 v[52:55], v35 offset:0\ns_mov_b32 s52, s47\n"
"s_add_u32 s47, s51, s32\ns_add_u32 s51, s51, s54\ns_add_u32 s49, s49, 28672\ns_waitcnt vmcnt(13)\n"
"s_barrier\ns_waitcnt lgkmcnt(3)\nv_mfma_f32_16x16x32_bf16 v[64:67], v[128:131], v[40:43], 0\nv_mov_b32 v36, s48\n"
"s_add_u32 s48, s48, 1\ns_nop 0\nbuffer_store_dword v36, v19, s[24:27], s33 offen sc1\nv_mfma_f32_16x16x32_bf16 v[68:71], v[132:135], v[40:43], 0\n"
"ds_read_b128 v[40:43], v32 offset:4096\ns_waitcnt lgkmcnt(3)\nv_mfma_f32_16x16x32_bf16 v[72:75], v[128:131], v[44:47], 0\nv_mfma_f32_16x16x32_bf16 v[76:79], v[132:135], v[44:47], 0\n"
"ds_read_b128 v[44:47], v33 offset:4096\nv_cvt_pk_bf16_f32 v56, v96, v97\ns_waitcnt lgkmcnt(3)\nv_mfma_f32_16x16x32_bf16 v[64:67], v[136:139], v[48:51], v[64:67]\n"
"v_cvt_pk_bf16_f32 v57, v98, v99\nv_mfma_f32_16x16x32_bf16 v[68:71], v[140:143], v[48:51], v[68:71]\nds_read_b128 v[48:51], v34 offset:4096\nv_cvt_pk_bf16_f32 v58, v100, v101\n"
"s_waitcnt lgkmcnt(3)\nv_mfma_f32_16x16x32_bf16 v[72:75], v[136:139], v[52:55], v[72:75]\nv_cvt_pk_bf16_f32 v59, v102, v103\nv_mfma_f32_16x16x32_bf16 v[76:79], v[140:143], v[52:55], v[76:79]\n"
"ds_read_b128 v[52:55], v35 offset:4096\ns_waitcnt lgkmcnt(3)\nv_mfma_f32_16x16x32_bf16 v[64:67], v[144:147], v[40:43], v[64:67]\nv_mfma_f32_16x16x32_bf16 v[68:71], v[148:151], v[40:43], v[68:71]\n"
"ds_read_b128 v[40:43], v32 offset:8192\nbuffer_store_dwordx4 v[56:59], v15, s[20:23], s50 offen sc1\ns_waitcnt lgkmcnt(3)\nv_mfma_f32_16x16x32_bf16 v[72:75], v[144:147], v[44:47], v[72:75]\n"
"v_cvt_pk_bf16_f32 v60, v104, v105\nv_mfma_f32_16x16x32_bf16 v[76:79], v[148:151], v[44:47], v[76:79]\nds_read_b128 v[44:47], v33 offset:8192\nv_cvt_pk_bf16_f32 v61, v106, v107\n"
"s_waitcnt lgkmcnt(3)\nv_mfma_f32_16x16x32_bf16 v[64:67], v[152:155], v[48:51], v[64:67]\nv_cvt_pk_bf16_f32 v62, v108, v109\nv_mfma_f32_16x16x32_bf16 v[68:71], v[156:159], v[48:51], v[68:71]\n"
"ds_read_b128 v[48:51], v34 offset:8192\nv_cvt_pk_bf16_f32 v63, v110, v111\ns_waitcnt lgkmcnt(3)\nv_mfma_f32_16x16x32_bf16 v[72:75], v[152:155], v[52:55], v[72:75]\n"
"v_mfma_f32_16x16x32_bf16 v[76:79], v[156:159], v[52:55], v[76:79]\nds_read_b128 v[52:55], v35 offset:8192\ns_waitcnt lgkmcnt(3)\nv_mfma_f32_16x16x32_bf16 v[64:67], v[160:163], v[40:43], v[64:67]\n"
"buffer_store_dwordx4 v[60:63], v16, s[20:23], s50 offen sc1\nv_mfma_f32_16x16x32_bf16 v[68:71], v[164:167], v[40:43], v[68:71]\nds_read_b128 v[40:43], v32 offset:12288\ns_add_u32 s50, s50, s36\n"
"s_waitcnt lgkmcnt(3)\nv_mfma_f32_16x16x32_bf16 v[72:75], v[160:163], v[44:47], v[72:75]\ns_add_u32 m0, s52, 0\nv_mfma_f32_16x16x32_bf16 v[76:79], v[164:167], v[44:47], v[76:79]\n"
"ds_read_b128 v[44:47], v33 offset:12288\ns_add_u32 s53, s49, 0\ns_waitcnt lgkmcnt(3)\nv_mfma_f32_16x16x32_bf16 v[64:67], v[168:171], v[48:51], v[64:67]\n"
"buffer_load_dwordx4 v10, s[12:15], s53 offen lds\nv_mfma_f32_16x16x32_bf16 v[68:71], v[172:175], v[48:51], v[68:71]\nds_read_b128 v[48:51], v34 offset:12288\ns_add_u32 m0, s52, 4096\n"
"s_waitcnt lgkmcnt(3)\nv_mfma_f32_16x16x32_bf16 v[72:75], v[168:171], v[52:55], v[72:75]\ns_add_u32 s53, s49, 128\nv_mfma_f32_16x16x32_bf16 v[76:79], v[172:175], v[52:55], v[76:79]\n"
"ds_read_b128 v[52:55], v35 offset:12288\nbuffer_load_dwordx4 v10, s[12:15], s53 offen lds\ns_waitcnt lgkmcnt(3)\nv_mfma_f32_16x16x32_bf16 v[64:67], v[176:179], v[40:43], v[64:67]\n"
"s_add_u32 m0, s52, 8192\nv_mfma_f32_16x16x32_bf16 v[68:71], v[180:183], v[40:43], v[68:71]\nds_read_b128 v[40:43], v32 offset:16384\ns_add_u32 s53, s49, 256\n"
"s_waitcnt lgkmcnt(3)\nv_mfma_f32_16x16x32_bf16 v[72:75], v[176:179], v[44:47], v[72:75]\nbuffer_load_dwordx4 v10, s[12:15], s53 offen lds\nv_mfma_f32_16x16x32_bf16 v[76:79], v[180:183], v[44:47], v[76:79]\n"
"ds_read_b128 v[44:47], v33 offset:16384\ns_add_u32 m0, s52, 12288\ns_waitcnt lgkmcnt(3)\nv_mfma_f32_16x16x32_bf16 v[64:67], v[184:187], v[48:51], v[64:67]\n"
"s_add_u32 s53, s49, 384\nv_mfma_f32_16x16x32_bf16 v[68:71], v[188:191], v[48:51], v[68:71]\nds_read_b128 v[48:51], v34 offset:16384\nbuffer_load_dwordx4 v10, s[12:15], s53 offen lds\n"
"s_waitcnt lgkmcnt(3)\nv_mfma_f32_16x16x32_bf16 v[72:75], v[184:187], v[52:55], v[72:75]\ns_add_u32 m0, s52, 16384\nv_mfma_f32_16x16x32_bf16 v[76:79], v[188:191], v[52:55], v[76:79]\n"
"ds_read_b128 v[52:55], v35 offset:16384\ns_add_u32 s53, s49, 512\ns_waitcnt lgkmcnt(3)\nv_mfma_f32_16x16x32_bf16 v[64:67], v[192:195], v[40:43], v[64:67]\n"
"buffer_load_dwordx4 v10, s[12:15], s53 offen lds\nv_mfma_f32_16x16x32_bf16 v[68:71], v[196:199], v[40:43], v[68:71]\nds_read_b128 v[40:43], v32 offset:20480\ns_add_u32 m0, s52, 20480\n"
"s_waitcnt lgkmcnt(3)\nv_mfma_f32_16x16x32_bf16 v[72:75], v[192:195], v[44:47], v[72:75]\ns_add_u32 s53, s49, 640\nv_mfma_f32_16x16x32_bf16 v[76:79], v[196:199], v[44:47], v[76:79]\n"
"ds_read_b128 v[44:47], v33 offset:20480\nbuffer_load_dwordx4 v10, s[12:15], s53 offen lds\ns_waitcnt lgkmcnt(3)\nv_mfma_f32_16x16x32_bf16 v[64:67], v[200:203], v[48:51], v[64:67]\n"
"s_add_u32 m0, s52, 24576\nv_mfma_f32_16x16x32_bf16 v[68:71], v[204:207], v[48:51], v[68:71]\nds_read_b128 v[48:51], v34 offset:20480\ns_add_u32 s53, s49, 768\n"
"s_waitcnt lgkmcnt(3)\nv_mfma_f32_16x16x32_bf16 v[72:75], v[200:203], v[52:55], v[72:75]\nbuffer_load_dwordx4 v10, s[12:15], s53 offen lds\nv_mfma_f32_16x16x32_bf16 v[76:79], v[204:207], v[52:55], v[76:79]\n"
"ds_read_b128 v[52:55], v35 offset:20480\ns_mov_b32 s53, 5120\nbuffer_load_dwordx4 a[40:43], v14, s[16:19], s53 offen sc1\ns_waitcnt lgkmcnt(3)\n"
"v_mfma_f32_16x16x32_bf16 v[64:67], v[208:211], v[40:43], v[64:67]\ns_mov_b32 s53, 19456\nbuffer_load_dwordx4 a[44:47], v14, s[16:19], s53 offen sc1\nv_mfma_f32_16x16x32_bf16 v[68:71], v[212:215], v[40:43], v[68:71]\n"
"ds_read_b128 v[40:43], v32 offset:24576\ns_mov_b32 s53, 6144\nbuffer_load_dwordx4 a[48:51], v14, s[16:19], s53 offen sc1\ns_waitcnt lgkmcnt(3)\n"
"v_mfma_f32_16x16x32_bf16 v[72:75], v[208:211], v[44:47], v[72:75]\ns_mov_b32 s53, 20480\nbuffer_load_dwordx4 a[52:55], v14, s[16:19], s53 offen sc1\nv_mfma_f32_16x16x32_bf16 v[76:79], v[212:215], v[44:47], v[76:79]\n"
"ds_read_b128 v[44:47], v33 offset:24576\ns_mov_b32 s53, 7168\nbuffer_load_dwordx4 a[56:59], v14, s[16:19], s53 offen sc1\ns_waitcnt lgkmcnt(3)\n"
"v_mfma_f32_16x16x32_bf16 v[64:67], v[216:219], v[48:51], v[64:67]\ns_add_u32 s54, s51, 32768\ns_cmp_eq_u32 s54, 163840\nv_mfma_f32_16x16x32_bf16 v[68:71], v[220:223], v[48:51], v[68:71]\n"
"s_cselect_b32 s54, 0, s54\ns_sub_u32 s54, s54, s51\nds_read_b128 v[48:51], v34 offset:24576\ns_waitcnt lgkmcnt(3)\n"
"v_mfma_f32_16x16x32_bf16 v[72:75], v[216:219], v[52:55], v[72:75]\nv_mfma_f32_16x16x32_bf16 v[76:79], v[220:223], v[52:55], v[76:79]\nds_read_b128 v[52:55], v35 offset:24576\ns_waitcnt lgkmcnt(3)\n"
"v_mfma_f32_16x16x32_bf16 v[64:67], v[224:227], v[40:43], v[64:67]\nv_add_u32 v32, s54, v32\nv_mfma_f32_16x16x32_bf16 v[68:71], v[228:231], v[40:43], v[68:71]\nds_read_b128 v[40:43], v32 offset:0\n"
"s_waitcnt lgkmcnt(3)\nv_mfma_f32_16x16x32_bf16 v[72:75], v[224:227], v[44:47], v[72:75]\nv_add_u32 v33, s54, v33\nv_mfma_f32_16x16x32_bf16 v[76:79], v[228:231], v[44:47], v[76:79]\n"
"ds_read_b128 v[44:47], v33 offset:0\ns_waitcnt lgkmcnt(3)\nv_mfma_f32_16x16x32_bf16 v[64:67], v[232:235], v[48:51], v[64:67]\nv_add_u32 v34, s54, v34\n"
"v_mfma_f32_16x16x32_bf16 v[68:71], v[236:239], v[48:51], v[68:71]\nds_read_b128 v[48:51], v34 offset:0\ns_waitcnt lgkmcnt(3)\nv_mfma_f32_16x16x32_bf16 v[72:75], v[232:235], v[52:55], v[72:75]\n"
"v_add_u32 v35, s54, v35\nv_mfma_f32_16x16x32_bf16 v[76:79], v[236:239], v[52:55], v[76:79]\nds_read_b128 v[52:55], v35 offset:0\ns_mov_b32 s52, s47\n"
"s_add_u32 s47, s51, s32\ns_add_u32 s51, s51, s54\ns_add_u32 s49, s49, 28672\ns_waitcnt vmcnt(12)\n"
"s_barrier\ns_waitcnt lgkmcnt(3)\nv_mfma_f32_16x16x32_bf16 v[96:99], v[128:131], v[40:43], 0\nv_mov_b32 v36, s48\n"
"s_add_u32 s48, s48, 1\ns_nop 0\nbuffer_store_dword v36, v19, s[24:27], s33 offen sc1\nv_mfma_f32_16x16x32_bf16 v[100:103], v[132:135], v[40:43], 0\n"
"ds_read_b128 v[40:43], v32 offset:4096\ns_waitcnt lgkmcnt(3)\nv_mfma_f32_16x16x32_bf16 v[104:107], v[128:131], v[44:47], 0\nv_mfma_f32_16x16x32_bf16 v[108:111], v[132:135], v[44:47], 0\n"
"ds_read_b128 v[44:47], v33 offset:4096\nv_cvt_pk_bf16_f32 v56, v64, v65\ns_waitcnt lgkmcnt(3)\nv_mfma_f32_16x16x32_bf16 v[96:99], v[136:139], v[48:51], v[96:99]\n"
"v_cvt_pk_bf16_f32 v57, v66, v67\nv_mfma_f32_16x16x32_bf16 v[100:103], v[140:143], v[48:51], v[100:103]\nds_read_b128 v[48:51], v34 offset:4096\nv_cvt_pk_bf16_f32 v58, v68, v69\n"
"s_waitcnt lgkmcnt(3)\nv_mfma_f32_16x16x32_bf16 v[104:107], v[136:139], v[52:55], v[104:107]\nv_cvt_pk_bf16_f32 v59, v70, v71\nv_mfma_f32_16x16x32_bf16 v[108:111], v[140:143], v[52:55], v[108:111]\n"
"ds_read_b128 v[52:55], v35 offset:4096\ns_waitcnt lgkmcnt(3)\nv_mfma_f32_16x16x32_bf16 v[96:99], v[144:147], v[40:43], v[96:99]\nv_mfma_f32_16x16x32_bf16 v[100:103], v[148:151], v[40:43], v[100:103]\n"
"ds_read_b128 v[40:43], v32 offset:8192\nbuffer_store_dwordx4 v[56:59], v15, s[20:23], s50 offen sc1\ns_waitcnt lgkmcnt(3)\nv_mfma_f32_16x16x32_bf16 v[104:107], v[144:147], v[44:47], v[104:107]\n"
"v_cvt_pk_bf16_f32 v60, v72, v73\nv_mfma_f32_16x16x32_bf16 v[108:111], v[148:151], v[44:47], v[108:111]\nds_read_b128 v[44:47], v33 offset:8192\nv_cvt_pk_bf16_f32 v61, v74, v75\n"
"s_waitcnt lgkmcnt(3)\nv_mfma_f32_16x16x32_bf16 v[96:99], v[152:155], v[48:51], v[96:99]\nv_cvt_pk_bf16_f32 v62, v76, v77\nv_mfma_f32_16x16x32_bf16 v[100:103], v[156:159], v[48:51], v[100:103]\n"
"ds_read_b128 v[48:51], v34 offset:8192\nv_cvt_pk_bf16_f32 v63, v78, v79\ns_waitcnt lgkmcnt(3)\nv_mfma_f32_16x16x32_bf16 v[104:107], v[152:155], v[52:55], v[104:107]\n"
"v_mfma_f32_16x16x32_bf16 v[108:111], v[156:159], v[52:55], v[108:111]\nds_read_b128 v[52:55], v35 offset:8192\ns_waitcnt lgkmcnt(3)\nv_mfma_f32_16x16x32_bf16 v[96:99], v[160:163], v[40:43], v[96:99]\n"
"buffer_store_dwordx4 v[60:63], v16, s[20:23], s50 offen sc1\nv_mfma_f32_16x16x32_bf16 v[100:103], v[164:167], v[40:43], v[100:103]\nds_read_b128 v[40:43], v32 offset:12288\ns_add_u32 s50, s50, s36\n"
"s_waitcnt lgkmcnt(3)\nv_mfma_f32_16x16x32_bf16 v[104:107], v[160:163], v[44:47], v[104:107]\ns_add_u32 m0, s52, 0\nv_mfma_f32_16x16x32_bf16 v[108:111], v[164:167], v[44:47], v[108:111]\n"
"ds_read_b128 v[44:47], v33 offset:12288\ns_add_u32 s53, s49, 0\ns_waitcnt lgkmcnt(3)\nv_mfma_f32_16x16x32_bf16 v[96:99], v[168:171], v[48:51], v[96:99]\n"
"buffer_load_dwordx4 v10, s[12:15], s53 offen lds\nv_mfma_f32_16x16x32_bf16 v[100:103], v[172:175], v[48:51], v[100:103]\nds_read_b128 v[48:51], v34 offset:12288\ns_add_u32 m0, s52, 4096\n"
"s_waitcnt lgkmcnt(3)\nv_mfma_f32_16x16x32_bf16 v[104:107], v[168:171], v[52:55], v[104:107]\ns_add_u32 s53, s49, 128\nv_mfma_f32_16x16x32_bf16 v[108:111], v[172:175], v[52:55], v[108:111]\n"
"ds_read_b128 v[52:55], v35 offset:12288\nbuffer_load_dwordx4 v10, s[12:15], s53 offen lds\ns_waitcnt lgkmcnt(3)\nv_mfma_f32_16x16x32_bf16 v[96:99], v[176:179], v[40:43], v[96:99]\n"
"s_add_u32 m0, s52, 8192\nv_mfma_f32_16x16x32_bf16 v[100:103], v[180:183], v[40:43], v[100:103]\nds_read_b128 v[40:43], v32 offset:16384\ns_add_u32 s53, s49, 256\n"
"s_waitcnt lgkmcnt(3)\nv_mfma_f32_16x16x32_bf16 v[104:107], v[176:179], v[44:47], v[104:107]\nbuffer_load_dwordx4 v10, s[12:15], s53 offen lds\nv_mfma_f32_16x16x32_bf16 v[108:111], v[180:183], v[44:47], v[108:111]\n"
"ds_read_b128 v[44:47], v33 offset:16384\ns_add_u32 m0, s52, 12288\ns_waitcnt lgkmcnt(3)\nv_mfma_f32_16x16x32_bf16 v[96:99], v[184:187], v[48:51], v[96:99]\n"
"s_add_u32 s53, s49, 384\nv_mfma_f32_16x16x32_bf16 v[100:103], v[188:191], v[48:51], v[100:103]\nds_read_b128 v[48:51], v34 offset:16384\nbuffer_load_dwordx4 v10, s[12:15], s53 offen lds\n"
"s_waitcnt lgkmcnt(3)\nv_mfma_f32_16x16x32_bf16 v[104:107], v[184:187], v[52:55], v[104:107]\ns_add_u32 m0, s52, 16384\nv_mfma_f32_16x16x32_bf16 v[108:111], v[188:191], v[52:55], v[108:111]\n"
"ds_read_b128 v[52:55], v35 offset:16384\ns_add_u32 s53, s49, 512\ns_waitcnt lgkmcnt(3)\nv_mfma_f32_16x16x32_bf16 v[96:99], v[192:195], v[40:43], v[96:99]\n"
"buffer_load_dwordx4 v10, s[12:15], s53 offen lds\nv_mfma_f32_16x16x32_bf16 v[100:103], v[196:199], v[40:43], v[100:103]\nds_read_b128 v[40:43], v32 offset:20480\ns_add_u32 m0, s52, 20480\n"
"s_waitcnt lgkmcnt(3)\nv_mfma_f32_16x16x32_bf16 v[104:107], v[192:195], v[44:47], v[104:107]\ns_add_u32 s53, s49, 640\nv_mfma_f32_16x16x32_bf16 v[108:111], v[196:199], v[44:47], v[108:111]\n"
"ds_read_b128 v[44:47], v33 offset:20480\nbuffer_load_dwordx4 v10, s[12:15], s53 offen lds\ns_waitcnt lgkmcnt(3)\nv_mfma_f32_16x16x32_bf16 v[96:99], v[200:203], v[48:51], v[96:99]\n"
"s_add_u32 m0, s52, 24576\nv_mfma_f32_16x16x32_bf16 v[100:103], v[204:207], v[48:51], v[100:103]\nds_read_b128 v[48:51], v34 offset:20480\ns_add_u32 s53, s49, 768\n"
"s_waitcnt lgkmcnt(3)\nv_mfma_f32_16x16x32_bf16 v[104:107], v[200:203], v[52:55], v[104:107]\nbuffer_load_dwordx4 v10, s[12:15], s53 offen lds\nv_mfma_f32_16x16x32_bf16 v[108:111], v[204:207], v[52:55], v[108:111]\n"
"ds_read_b128 v[52:55], v35 offset:20480\ns_mov_b32 s53, 21504\nbuffer_load_dwordx4 a[60:63], v14, s[16:19], s53 offen sc1\ns_waitcnt lgkmcnt(3)\n"
"v_mfma_f32_16x16x32_bf16 v[96:99], v[208:211], v[40:43], v[96:99]\ns_mov_b32 s53, 8192\nbuffer_load_dwordx4 a[64:67], v14, s[16:19], s53 offen sc1\nv_mfma_f32_16x16x32_bf16 v[100:103], v[212:215], v[40:43], v[100:103]\n"
"ds_read_b128 v[40:43], v32 offset:24576\ns_mov_b32 s53, 22528\nbuffer_load_dwordx4 a[68:71], v14, s[16:19], s53 offen sc1\ns_waitcnt lgkmcnt(3)\n"
"v_mfma_f32_16x16x32_bf16 v[104:107], v[208:211], v[44:47], v[104:107]\ns_mov_b32 s53, 9216\nbuffer_load_dwordx4 a[72:75], v14, s[16:19], s53 offen sc1\nv_mfma_f32_16x16x32_bf16 v[108:111], v[212:215], v[44:47], v[108:111]\n"
"ds_read_b128 v[44:47], v33 offset:24576\ns_mov_b32 s53, 23552\nbuffer_load_dwordx4 a[76:79], v14, s[16:19], s53 offen sc1\ns_waitcnt lgkmcnt(3)\n"
"v_mfma_f32_16x16x32_bf16 v[96:99], v[216:219], v[48:51], v[96:99]\ns_add_u32 s54, s51, 32768\ns_cmp_eq_u32 s54, 163840\nv_mfma_f32_16x16x32_bf16 v[100:103], v[220:223], v[48:51], v[100:103]\n"
"s_cselect_b32 s54, 0, s54\ns_sub_u32 s54, s54, s51\nds_read_b128 v[48:51], v34 offset:24576\ns_waitcnt lgkmcnt(3)\n"
"v_mfma_f32_16x16x32_bf16 v[104:107], v[216:219], v[52:55], v[104:107]\nv_mfma_f32_16x16x32_bf16 v[108:111], v[220:223], v[52:55], v[108:111]\nds_read_b128 v[52:55], v35 offset:24576\ns_waitcnt lgkmcnt(3)\n"
"v_mfma_f32_16x16x32_bf16 v[96:99], v[224:227], v[40:43], v[96:99]\nv_add_u32 v32, s54, v32\nv_mfma_f32_16x16x32_bf16 v[100:103], v[228:231], v[40:43], v[100:103]\nds_read_b128 v[40:43], v32 offset:0\n"
"s_waitcnt lgkmcnt(3)\nv_mfma_f32_16x16x32_bf16 v[104:107], v[224:227], v[44:47], v[104:107]\nv_add_u32 v33, s54, v33\nv_mfma_f32_16x16x32_bf16 v[108:111], v[228:231], v[44:47], v[108:111]\n"
"ds_read_b128 v[44:47], v33 offset:0\ns_waitcnt lgkmcnt(3)\nv_mfma_f32_16x16x32_bf16 v[96:99], v[232:235], v[48:51], v[96:99]\nv_add_u32 v34, s54, v34\n"
"v_mfma_f32_16x16x32_bf16 v[100:103], v[236:239], v[48:51], v[100:103]\nds_read_b128 v[48:51], v34 offset:0\ns_waitcnt lgkmcnt(3)\nv_mfma_f32_16x16x32_bf16 v[104:107], v[232:235], v[52:55], v[104:107]\n"
"v_add_u32 v35, s54, v35\nv_mfma_f32_16x16x32_bf16 v[108:111], v[236:239], v[52:55], v[108:111]\nds_read_b128 v[52:55], v35 offset:0\ns_mov_b32 s52, s47\n"
"s_add_u32 s47, s51, s32\ns_add_u32 s51, s51, s54\ns_add_u32 s49, s49, 28672\ns_waitcnt vmcnt(12)\n"
"s_barrier\ns_waitcnt lgkmcnt(3)\nv_mfma_f32_16x16x32_bf16 v[64:67], v[128:131], v[40:43], 0\nv_mov_b32 v36, s48\n"
"s_add_u32 s48, s48, 1\ns_nop 0\nbuffer_store_dword v36, v19, s[24:27], s33 offen sc1\nv_mfma_f32_16x16x32_bf16 v[68:71], v[132:135], v[40:43], 0\n"
"ds_read_b128 v[40:43], v32 offset:4096\ns_waitcnt lgkmcnt(3)\nv_mfma_f32_16x16x32_bf16 v[72:75], v[128:131], v[44:47], 0\nv_mfma_f32_16x16x32_bf16 v[76:79], v[132:135], v[44:47], 0\n"
"ds_read_b128 v[44:47], v33 offset:4096\nv_cvt_pk_bf16_f32 v56, v96, v97\ns_waitcnt lgkmcnt(3)\nv_mfma_f32_16x16x32_bf16 v[64:67], v[136:139], v[48:51], v[64:67]\n"
"v_cvt_pk_bf16_f32 v57, v98, v99\nv_mfma_f32_16x16x32_bf16 v[68:71], v[140:143], v[48:51], v[68:71]\nds_read_b128 v[48:51], v34 offset:4096\nv_cvt_pk_bf16_f32 v58, v100, v101\n"
"s_waitcnt lgkmcnt(3)\nv_mfma_f32_16x16x32_bf16 v[72:75], v[136:139], v[52:55], v[72:75]\nv_cvt_pk_bf16_f32 v59, v102, v103\nv_mfma_f32_16x16x32_bf16 v[76:79], v[140:143], v[52:55], v[76:79]\n"
"ds_read_b128 v[52:55], v35 offset:4096\ns_waitcnt lgkmcnt(3)\nv_mfma_f32_16x16x32_bf16 v[64:67], v[144:147], v[40:43], v[64:67]\nv_mfma_f32_16x16x32_bf16 v[68:71], v[148:151], v[40:43], v[68:71]\n"
"ds_read_b128 v[40:43], v32 offset:8192\nbuffer_store_dwordx4 v[56:59], v15, s[20:23], s50 offen sc1\ns_waitcnt lgkmcnt(3)\nv_mfma_f32_16x16x32_bf16 v[72:75], v[144:147], v[44:47], v[72:75]\n"
"v_cvt_pk_bf16_f32 v60, v104, v105\nv_mfma_f32_16x16x32_bf16 v[76:79], v[148:151], v[44:47], v[76:79]\nds_read_b128 v[44:47], v33 offset:8192\nv_cvt_pk_bf16_f32 v61, v106, v107\n"
"s_waitcnt lgkmcnt(3)\nv_mfma_f32_16x16x32_bf16 v[64:67], v[152:155], v[48:51], v[64:67]\nv_cvt_pk_bf16_f32 v62, v108, v109\nv_mfma_f32_16x16x32_bf16 v[68:71], v[156:159], v[48:51], v[68:71]\n"
"ds_read_b128 v[48:51], v34 offset:8192\nv_cvt_pk_bf16_f32 v63, v110, v111\ns_waitcnt lgkmcnt(3)\nv_mfma_f32_16x16x32_bf16 v[72:75], v[152:155], v[52:55], v[72:75]\n"
"v_mfma_f32_16x16x32_bf16 v[76:79], v[156:159], v[52:55], v[76:79]\nds_read_b128 v[52:55], v35 offset:8192\ns_waitcnt lgkmcnt(3)\nv_mfma_f32_16x16x32_bf16 v[64:67], v[160:163], v[40:43], v[64:67]\n"
"buffer_store_dwordx4 v[60:63], v16, s[20:23], s50 offen sc1\nv_mfma_f32_16x16x32_bf16 v[68:71], v[164:167], v[40:43], v[68:71]\nds_read_b128 v[40:43], v32 offset:12288\ns_add_u32 s50, s50, s36\n"
"s_waitcnt lgkmcnt(3)\nv_mfma_f32_16x16x32_bf16 v[72:75], v[160:163], v[44:47], v[72:75]\ns_add_u32 m0, s52, 0\nv_mfma_f32_16x16x32_bf16 v[76:79], v[164:167], v[44:47], v[76:79]\n"
"ds_read_b128 v[44:47], v33 offset:12288\ns_add_u32 s53, s49, 0\ns_waitcnt lgkmcnt(3)\nv_mfma_f32_16x16x32_bf16 v[64:67], v[168:171], v[48:51], v[64:67]\n"
"buffer_load_dwordx4 v10, s[12:15], s53 offen lds\nv_mfma_f32_16x16x32_bf16 v[68:71], v[172:175], v[48:51], v[68:71]\nds_read_b128 v[48:51], v34 offset:12288\ns_add_u32 m0, s52, 4096\n"
"s_waitcnt lgkmcnt(3)\nv_mfma_f32_16x16x32_bf16 v[72:75], v[168:171], v[52:55], v[72:75]\ns_add_u32 s53, s49, 128\nv_mfma_f32_16x16x32_bf16 v[76:79], v[172:175], v[52:55], v[76:79]\n"
"ds_read_b128 v[52:55], v35 offset:12288\nbuffer_load_dwordx4 v10, s[12:15], s53 offen lds\ns_waitcnt lgkmcnt(3)\nv_mfma_f32_16x16x32_bf16 v[64:67], v[176:179], v[40:43], v[64:67]\n"
"s_add_u32 m0, s52, 8192\nv_mfma_f32_16x16x32_bf16 v[68:71], v[180:183], v[40:43], v[68:71]\nds_read_b128 v[40:43], v32 offset:16384\ns_add_u32 s53, s49, 256\n"
"s_waitcnt lgkmcnt(3)\nv_mfma_f32_16x16x32_bf16 v[72:75], v[176:179], v[44:47], v[72:75]\nbuffer_load_dwordx4 v10, s[12:15], s53 offen lds\nv_mfma_f32_16x16x32_bf16 v[76:79], v[180:183], v[44:47], v[76:79]\n"
"ds_read_b128 v[44:47], v33 offset:16384\ns_add_u32 m0, s52, 12288\ns_waitcnt lgkmcnt(3)\nv_mfma_f32_16x16x32_bf16 v[64:67], v[184:187], v[48:51], v[64:67]\n"
"s_add_u32 s53, s49, 384\nv_mfma_f32_16x16x32_bf16 v[68:71], v[188:191], v[48:51], v[68:71]\nds_read_b128 v[48:51], v34 offset:16384\nbuffer_load_dwordx4 v10, s[12:15], s53 offen lds\n"
"s_waitcnt lgkmcnt(3)\nv_mfma_f32_16x16x32_bf16 v[72:75], v[184:187], v[52:55], v[72:75]\ns_add_u32 m0, s52, 16384\nv_mfma_f32_16x16x32_bf16 v[76:79], v[188:191], v[52:55], v[76:79]\n"
"ds_read_b128 v[52:55], v35 offset:16384\ns_add_u32 s53, s49, 512\ns_waitcnt lgkmcnt(3)\nv_mfma_f32_16x16x32_bf16 v[64:67], v[192:195], v[40:43], v[64:67]\n"
"buffer_load_dwordx4 v10, s[12:15], s53 offen lds\nv_mfma_f32_16x16x32_bf16 v[68:71], v[196:199], v[40:43], v[68:71]\nds_read_b128 v[40:43], v32 offset:20480\ns_add_u32 m0, s52, 20480\n"
"s_waitcnt lgkmcnt(3)\nv_mfma_f32_16x16x32_bf16 v[72:75], v[192:195], v[44:47], v[72:75]\ns_add_u32 s53, s49, 640\nv_mfma_f32_16x16x32_bf16 v[76:79], v[196:199], v[44:47], v[76:79]\n"
"ds_read_b128 v[44:47], v33 offset:20480\nbuffer_load_dwordx4 v10, s[12:15], s53 offen lds\ns_waitcnt lgkmcnt(3)\nv_mfma_f32_16x16x32_bf16 v[64:67], v[200:203], v[48:51], v[64:67]\n"
"s_add_u32 m0, s52, 24576\nv_mfma_f32_16x16x32_bf16 v[68:71], v[204:207], v[48:51], v[68:71]\nds_read_b128 v[48:51], v34 offset:20480\ns_add_u32 s53, s49, 768\n"
"s_waitcnt lgkmcnt(3)\nv_mfma_f32_16x16x32_bf16 v[72:75], v[200:203], v[52:55], v[72:75]\nbuffer_load_dwordx4 v10, s[12:15], s53 offen lds\nv_mfma_f32_16x16x32_bf16 v[76:79], v[204:207], v[52:55], v[76:79]\n"
"ds_read_b128 v[52:55], v35 offset:20480\ns_mov_b32 s53, 10240\nbuffer_load_dwordx4 a[80:83], v14, s[16:19], s53 offen sc1\ns_waitcnt lgkmcnt(3)\n"
"v_mfma_f32_16x16x32_bf16 v[64:67], v[208:211], v[40:43], v[64:67]\ns_mov_b32 s53, 24576\nbuffer_load_dwordx4 a[84:87], v14, s[16:19], s53 offen sc1\nv_mfma_f32_16x16x32_bf16 v[68:71], v[212:215], v[40:43], v[68:71]\n"
"ds_read_b128 v[40:43], v32 offset:24576\ns_mov_b32 s53, 11264\nbuffer_load_dwordx4 a[88:91], v14, s[16:19], s53 offen sc1\ns_waitcnt lgkmcnt(3)\n"
"v_mfma_f32_16x16x32_bf16 v[72:75], v[208:211], v[44:47], v[72:75]\ns_mov_b32 s53, 25600\nbuffer_load_dwordx4 a[92:95], v14, s[16:19], s53 offen sc1\nv_mfma_f32_16x16x32_bf16 v[76:79], v[212:215], v[44:47], v[76:79]\n"
"ds_read_b128 v[44:47], v33 offset:24576\ns_mov_b32 s53, 12288\nbuffer_load_dwordx4 a[96:99], v14, s[16:19], s53 offen sc1\ns_waitcnt lgkmcnt(3)\n"
"v_mfma_f32_16x16x32_bf16 v[64:67], v[216:219], v[48:51], v[64:67]\ns_add_u32 s54, s51, 32768\ns_cmp_eq_u32 s54, 163840\nv_mfma_f32_16x16x32_bf16 v[68:71], v[220:223], v[48:51], v[68:71]\n"
"s_cselect_b32 s54, 0, s54\ns_sub_u32 s54, s54, s51\nds_read_b128 v[48:51], v34 offset:24576\ns_waitcnt lgkmcnt(3)\n"
"v_mfma_f32_16x16x32_bf16 v[72:75], v[216:219], v[52:55], v[72:75]\nv_mfma_f32_16x16x32_bf16 v[76:79], v[220:223], v[52:55], v[76:79]\nds_read_b128 v[52:55], v35 offset:24576\ns_waitcnt lgkmcnt(3)\n"
"v_mfma_f32_16x16x32_bf16 v[64:67], v[224:227], v[40:43], v[64:67]\nv_add_u32 v32, s54, v32\nv_mfma_f32_16x16x32_bf16 v[68:71], v[228:231], v[40:43], v[68:71]\nds_read_b128 v[40:43], v32 offset:0\n"
"s_waitcnt lgkmcnt(3)\nv_mfma_f32_16x16x32_bf16 v[72:75], v[224:227], v[44:47], v[72:75]\nv_add_u32 v33, s54, v33\nv_mfma_f32_16x16x32_bf16 v[76:79], v[228:231], v[44:47], v[76:79]\n"
"ds_read_b128 v[44:47], v33 offset:0\ns_waitcnt lgkmcnt(3)\nv_mfma_f32_16x16x32_bf16 v[64:67], v[232:235], v[48:51], v[64:67]\nv_add_u32 v34, s54, v34\n"
"v_mfma_f32_16x16x32_bf16 v[68:71], v[236:239], v[48:51], v[68:71]\nds_read_b128 v[48:51], v34 offset:0\ns_waitcnt lgkmcnt(3)\nv_mfma_f32_16x16x32_bf16 v[72:75], v[232:235], v[52:55], v[72:75]\n"
"v_add_u32 v35, s54, v35\nv_mfma_f32_16x16x32_bf16 v[76:79], v[236:239], v[52:55], v[76:79]\nds_read_b128 v[52:55], v35 offset:0\ns_mov_b32 s52, s47\n"
"s_add_u32 s47, s51, s32\ns_add_u32 s51, s51, s54\ns_add_u32 s49, s49, 28672\ns_waitcnt vmcnt(12)\n"
"s_barrier\ns_waitcnt lgkmcnt(3)\nv_mfma_f32_16x16x32_bf16 v[96:99], v[128:131], v[40:43], 0\nv_mov_b32 v36, s48\n"
"s_add_u32 s48, s48, 1\ns_nop 0\nbuffer_store_dword v36, v19, s[24:27], s33 offen sc1\nv_mfma_f32_16x16x32_bf16 v[100:103], v[132:135], v[40:43], 0\n"
"ds_read_b128 v[40:43], v32 offset:4096\ns_waitcnt lgkmcnt(3)\nv_mfma_f32_16x16x32_bf16 v[104:107], v[128:131], v[44:47], 0\nv_mfma_f32_16x16x32_bf16 v[108:111], v[132:135], v[44:47], 0\n"
"ds_read_b128 v[44:47], v33 offset:4096\nv_cvt_pk_bf16_f32 v56, v64, v65\ns_waitcnt lgkmcnt(3)\nv_mfma_f32_16x16x32_bf16 v[96:99], v[136:139], v[48:51], v[96:99]\n"
"v_cvt_pk_bf16_f32 v57, v66, v67\nv_mfma_f32_16x16x32_bf16 v[100:103], v[140:143], v[48:51], v[100:103]\nds_read_b128 v[48:51], v34 offset:4096\nv_cvt_pk_bf16_f32 v58, v68, v69\n"
"s_waitcnt lgkmcnt(3)\nv_mfma_f32_16x16x32_bf16 v[104:107], v[136:139], v[52:55], v[104:107]\nv_cvt_pk_bf16_f32 v59, v70, v71\nv_mfma_f32_16x16x32_bf16 v[108:111], v[140:143], v[52:55], v[108:111]\n"
"ds_read_b128 v[52:55], v35 offset:4096\ns_waitcnt lgkmcnt(3)\nv_mfma_f32_16x16x32_bf16 v[96:99], v[144:147], v[40:43], v[96:99]\nv_mfma_f32_16x16x32_bf16 v[100:103], v[148:151], v[40:43], v[100:103]\n"
"ds_read_b128 v[40:43], v32 offset:8192\nbuffer_store_dwordx4 v[56:59], v15, s[20:23], s50 offen sc1\ns_waitcnt lgkmcnt(3)\nv_mfma_f32_16x16x32_bf16 v[104:107], v[144:147], v[44:47], v[104:107]\n"
"v_cvt_pk_bf16_f32 v60, v72, v73\nv_mfma_f32_16x16x32_bf16 v[108:111], v[148:151], v[44:47], v[108:111]\nds_read_b128 v[44:47], v33 offset:8192\nv_cvt_pk_bf16_f32 v61, v74, v75\n"
"s_waitcnt lgkmcnt(3)\nv_mfma_f32_16x16x32_bf16 v[96:99], v[152:155], v[48:51], v[96:99]\nv_cvt_pk_bf16_f32 v62, v76, v77\nv_mfma_f32_16x16x32_bf16 v[100:103], v[156:159], v[48:51], v[100:103]\n"
"ds_read_b128 v[48:51], v34 offset:8192\nv_cvt_pk_bf16_f32 v63, v78, v79\ns_waitcnt lgkmcnt(3)\nv_mfma_f32_16x16x32_bf16 v[104:107], v[152:155], v[52:55], v[104:107]\n"
"v_mfma_f32_16x16x32_bf16 v[108:111], v[156:159], v[52:55], v[108:111]\nds_read_b128 v[52:55], v35 offset:8192\ns_waitcnt lgkmcnt(3)\nv_mfma_f32_16x16x32_bf16 v[96:99], v[160:163], v[40:43], v[96:99]\n"
"buffer_store_dwordx4 v[60:63], v16, s[20:23], s50 offen sc1\nv_mfma_f32_16x16x32_bf16 v[100:103], v[164:167], v[40:43], v[100:103]\nds_read_b128 v[40:43], v32 offset:12288\ns_add_u32 s50, s50, s36\n"
"s_waitcnt lgkmcnt(3)\nv_mfma_f32_16x16x32_bf16 v[104:107], v[160:163], v[44:47], v[104:107]\ns_add_u32 m0, s52, 0\nv_mfma_f32_16x16x32_bf16 v[108:111], v[164:167], v[44:47], v[108:111]\n"
"ds_read_b128 v[44:47], v33 offset:12288\ns_add_u32 s53, s49, 0\ns_waitcnt lgkmcnt(3)\nv_mfma_f32_16x16x32_bf16 v[96:99], v[168:171], v[48:51], v[96:99]\n"
"buffer_load_dwordx4 v10, s[12:15], s53 offen lds\nv_mfma_f32_16x16x32_bf16 v[100:103], v[172:175], v[48:51], v[100:103]\nds_read_b128 v[48:51], v34 offset:12288\ns_add_u32 m0, s52, 4096\n"
"s_waitcnt lgkmcnt(3)\nv_mfma_f32_16x16x32_bf16 v[104:107], v[168:171], v[52:55], v[104:107]\ns_add_u32 s53, s49, 128\nv_mfma_f32_16x16x32_bf16 v[108:111], v[172:175], v[52:55], v[108:111]\n"
"ds_read_b128 v[52:55], v35 offset:12288\nbuffer_load_dwordx4 v10, s[12:15], s53 offen lds\ns_waitcnt lgkmcnt(3)\nv_mfma_f32_16x16x32_bf16 v[96:99], v[176:179], v[40:43], v[96:99]\n"
"s_add_u32 m0, s52, 8192\nv_mfma_f32_16x16x32_bf16 v[100:103], v[180:183], v[40:43], v[100:103]\nds_read_b128 v[40:43], v32 offset:16384\ns_add_u32 s53, s49, 256\n"
"s_waitcnt lgkmcnt(3)\nv_mfma_f32_16x16x32_bf16 v[104:107], v[176:179], v[44:47], v[104:107]\nbuffer_load_dwordx4 v10, s[12:15], s53 offen lds\nv_mfma_f32_16x16x32_bf16 v[108:111], v[180:183], v[44:47], v[108:111]\n"
"ds_read_b128 v[44:47], v33 offset:16384\ns_add_u32 m0, s52, 12288\ns_waitcnt lgkmcnt(3)\nv_mfma_f32_16x16x32_bf16 v[96:99], v[184:187], v[48:51], v[96:99]\n"
"s_add_u32 s53, s49, 384\nv_mfma_f32_16x16x32_bf16 v[100:103], v[188:191], v[48:51], v[100:103]\nds_read_b128 v[48:51], v34 offset:16384\nbuffer_load_dwordx4 v10, s[12:15], s53 offen lds\n"
"s_waitcnt lgkmcnt(3)\nv_mfma_f32_16x16x32_bf16 v[104:107], v[184:187], v[52:55], v[104:107]\ns_add_u32 m0, s52, 16384\nv_mfma_f32_16x16x32_bf16 v[108:111], v[188:191], v[52:55], v[108:111]\n"
"ds_read_b128 v[52:55], v35 offset:16384\ns_add_u32 s53, s49, 512\ns_waitcnt lgkmcnt(3)\nv_mfma_f32_16x16x32_bf16 v[96:99], v[192:195], v[40:43], v[96:99]\n"
"buffer_load_dwordx4 v10, s[12:15], s53 offen lds\nv_mfma_f32_16x16x32_bf16 v[100:103], v[196:199], v[40:43], v[100:103]\nds_read_b128 v[40:43], v32 offset:20480\ns_add_u32 m0, s52, 20480\n"
"s_waitcnt lgkmcnt(3)\nv_mfma_f32_16x16x32_bf16 v[104:107], v[192:195], v[44:47], v[104:107]\ns_add_u32 s53, s49, 640\nv_mfma_f32_16x16x32_bf16 v[108:111], v[196:199], v[44:47], v[108:111]\n"
"ds_read_b128 v[44:47], v33 offset:20480\nbuffer_load_dwordx4 v10, s[12:15], s53 offen lds\ns_waitcnt lgkmcnt(3)\nv_mfma_f32_16x16x32_bf16 v[96:99], v[200:203], v[48:51], v[96:99]\n"
"s_add_u32 m0, s52, 24576\nv_mfma_f32_16x16x32_bf16 v[100:103], v[204:207], v[48:51], v[100:103]\nds_read_b128 v[48:51], v34 offset:20480\ns_add_u32 s53, s49, 768\n"
"s_waitcnt lgkmcnt(3)\nv_mfma_f32_16x16x32_bf16 v[104:107], v[200:203], v[52:55], v[104:107]\nbuffer_load_dwordx4 v10, s[12:15], s53 offen lds\nv_mfma_f32_16x16x32_bf16 v[108:111], v[204:207], v[52:55], v[108:111]\n"
"ds_read_b128 v[52:55], v35 offset:20480\ns_mov_b32 s53, 26624\nbuffer_load_dwordx4 a[100:103], v14, s[16:19], s53 offen sc1\ns_waitcnt lgkmcnt(3)\n"
"v_mfma_f32_16x16x32_bf16 v[96:99], v[208:211], v[40:43], v[96:99]\ns_mov_b32 s53, 13312\nbuffer_load_dwordx4 a[104:107], v14, s[16:19], s53 offen sc1\nv_mfma_f32_16x16x32_bf16 v[100:103], v[212:215], v[40:43], v[100:103]\n"
"ds_read_b128 v[40:43], v32 offset:24576\ns_mov_b32 s53, 27648\nbuffer_load_dwordx4 a[108:111], v14, s[16:19], s53 offen sc1\ns_waitcnt lgkmcnt(3)\n"
"v_mfma_f32_16x16x32_bf16 v[104:107], v[208:211], v[44:47], v[104:107]\nv_mfma_f32_16x16x32_bf16 v[108:111], v[212:215], v[44:47], v[108:111]\nds_read_b128 v[44:47], v33 offset:24576\ns_waitcnt lgkmcnt(3)\n"
"v_mfma_f32_16x16x32_bf16 v[96:99], v[216:219], v[48:51], v[96:99]\ns_add_u32 s54, s51, 32768\ns_cmp_eq_u32 s54, 163840\nv_mfma_f32_16x16x32_bf16 v[100:103], v[220:223], v[48:51], v[100:103]\n"
"s_cselect_b32 s54, 0, s54\ns_sub_u32 s54, s54, s51\nds_read_b128 v[48:51], v34 offset:24576\ns_waitcnt lgkmcnt(3)\n"
"v_mfma_f32_16x16x32_bf16 v[104:107], v[216:219], v[52:55], v[104:107]\nv_mfma_f32_16x16x32_bf16 v[108:111], v[220:223], v[52:55], v[108:111]\nds_read_b128 v[52:55], v35 offset:24576\ns_waitcnt lgkmcnt(3)\n"
"v_mfma_f32_16x16x32_bf16 v[96:99], v[224:227], v[40:43], v[96:99]\nv_add_u32 v32, s54, v32\nv_mfma_f32_16x16x32_bf16 v[100:103], v[228:231], v[40:43], v[100:103]\nds_read_b128 v[40:43], v32 offset:0\n"
"s_waitcnt lgkmcnt(3)\nv_mfma_f32_16x16x32_bf16 v[104:107], v[224:227], v[44:47], v[104:107]\nv_add_u32 v33, s54, v33\nv_mfma_f32_16x16x32_bf16 v[108:111], v[228:231], v[44:47], v[108:111]\n"
"ds_read_b128 v[44:47], v33 offset:0\ns_waitcnt lgkmcnt(3)\nv_mfma_f32_16x16x32_bf16 v[96:99], v[232:235], v[48:51], v[96:99]\nv_add_u32 v34, s54, v34\n"
"v_mfma_f32_16x16x32_bf16 v[100:103], v[236:239], v[48:51], v[100:103]\nds_read_b128 v[48:51], v34 offset:0\ns_waitcnt lgkmcnt(3)\nv_mfma_f32_16x16x32_bf16 v[104:107], v[232:235], v[52:55], v[104:107]\n"
"v_add_u32 v35, s54, v35\nv_mfma_f32_16x16x32_bf16 v[108:111], v[236:239], v[52:55], v[108:111]\nds_read_b128 v[52:55], v35 offset:0\ns_mov_b32 s52, s47\n"
"s_add_u32 s47, s51, s32\ns_add_u32 s51, s51, s54\ns_add_u32 s49, s49, 28672\ns_cmp_lg_u32 s34, 0\n"
"s_cbranch_scc1 fg0_stay\ns_nop 7\nv_cvt_pk_bf16_f32 v56, v96, v97\nv_cvt_pk_bf16_f32 v57, v98, v99\n"
"v_cvt_pk_bf16_f32 v58, v100, v101\nv_cvt_pk_bf16_f32 v59, v102, v103\ns_nop 0\ns_nop 0\n"
"buffer_store_dwordx4 v[56:59], v15, s[20:23], s50 offen sc1\nv_cvt_pk_bf16_f32 v60, v104, v105\nv_cvt_pk_bf16_f32 v61, v106, v107\nv_cvt_pk_bf16_f32 v62, v108, v109\n"
"v_cvt_pk_bf16_f32 v63, v110, v111\ns_nop 0\ns_nop 0\nbuffer_store_dwordx4 v[60:63], v16, s[20:23], s50 offen sc1\n"
"s_add_u32 s48, s28, 64\nv_mov_b32 v36, s48\ns_waitcnt vmcnt(0) lgkmcnt(0)\ns_barrier\n"
"buffer_store_dword v36, v19, s[24:27], s33 offen sc1\ns_branch fg0_end\nfg0_stay:\ns_waitcnt vmcnt(0)\n"
"s_barrier\ns_waitcnt lgkmcnt(3)\nv_mfma_f32_16x16x32_bf16 v[64:67], v[128:131], v[40:43], 0\nv_mov_b32 v36, s48\n"
"s_add_u32 s48, s48, 1\ns_nop 0\nbuffer_store_dword v36, v19, s[24:27], s33 offen sc1\nv_mfma_f32_16x16x32_bf16 v[68:71], v[132:135], v[40:43], 0\n"
"v_mfma_f32_16x16x32_bf16 v[72:75], a[0:3], v[40:43], 0\nv_mfma_f32_16x16x32_bf16 v[76:79], a[4:7], v[40:43], 0\nds_read_b128 v[40:43], v32 offset:4096\nv_cvt_pk_bf16_f32 v56, v96, v97\n"
"s_waitcnt lgkmcnt(3)\nv_mfma_f32_16x16x32_bf16 v[80:83], v[128:131], v[44:47], 0\nv_cvt_pk_bf16_f32 v57, v98, v99\nv_mfma_f32_16x16x32_bf16 v[84:87], v[132:135], v[44:47], 0\n"
"v_cvt_pk_bf16_f32 v58, v100, v101\nv_mfma_f32_16x16x32_bf16 v[88:91], a[0:3], v[44:47], 0\nv_cvt_pk_bf16_f32 v59, v102, v103\nv_mfma_f32_16x16x32_bf16 v[92:95], a[4:7], v[44:47], 0\n"
"ds_read_b128 v[44:47], v33 offset:4096\ns_waitcnt lgkmcnt(3)\nv_mfma_f32_16x16x32_bf16 v[64:67], v[136:139], v[48:51], v[64:67]\nv_mfma_f32_16x16x32_bf16 v[68:71], v[140:143], v[48:51], v[68:71]\n"
"buffer_store_dwordx4 v[56:59], v15, s[20:23], s50 offen sc1\nv_mfma_f32_16x16x32_bf16 v[72:75], a[8:11], v[48:51], v[72:75]\nv_cvt_pk_bf16_f32 v60, v104, v105\nv_mfma_f32_16x16x32_bf16 v[76:79], a[12:15], v[48:51], v[76:79]\n"
"ds_read_b128 v[48:51], v34 offset:4096\nv_cvt_pk_bf16_f32 v61, v106, v107\ns_waitcnt lgkmcnt(3)\nv_mfma_f32_16x16x32_bf16 v[80:83], v[136:139], v[52:55], v[80:83]\n"
"v_cvt_pk_bf16_f32 v62, v108, v109\nv_mfma_f32_16x16x32_bf16 v[84:87], v[140:143], v[52:55], v[84:87]\nv_cvt_pk_bf16_f32 v63, v110, v111\nv_mfma_f32_16x16x32_bf16 v[88:91], a[8:11], v[52:55], v[88:91]\n"
"v_mfma_f32_16x16x32_bf16 v[92:95], a[12:15], v[52:55], v[92:95]\nds_read_b128 v[52:55], v35 offset:4096\ns_waitcnt lgkmcnt(3)\nv_mfma_f32_16x16x32_bf16 v[64:67], v[144:147], v[40:43], v[64:67]\n"
"buffer_store_dwordx4 v[60:63], v16, s[20:23], s50 offen sc1\nv_mfma_f32_16x16x32_bf16 v[68:71], v[148:151], v[40:43], v[68:71]\ns_add_u32 s50, s50, s36\nv_mfma_f32_16x16x32_bf16 v[72:75], a[16:19], v[40:43], v[72:75]\n"
"s_add_u32 m0, s52, 0\nv_mfma_f32_16x16x32_bf16 v[76:79], a[20:23], v[40:43], v[76:79]\nds_read_b128 v[40:43], v32 offset:8192\ns_add_u32 s53, s49, 0\n"
"s_waitcnt lgkmcnt(3)\nv_mfma_f32_16x16x32_bf16 v[80:83], v[144:147], v[44:47], v[80:83]\nbuffer_load_dwordx4 v10, s[12:15], s53 offen lds\nv_mfma_f32_16x16x32_bf16 v[84:87], v[148:151], v[44:47], v[84:87]\n"
"v_mfma_f32_16x16x32_bf16 v[88:91], a[16:19], v[44:47], v[88:91]\nv_mfma_f32_16x16x32_bf16 v[92:95], a[20:23], v[44:47], v[92:95]\nds_read_b128 v[44:47], v33 offset:8192\ns_waitcnt lgkmcnt(3)\n"
"v_mfma_f32_16x16x32_bf16 v[64:67], v[152:155], v[48:51], v[64:67]\nv_mfma_f32_16x16x32_bf16 v[68:71], v[156:159], v[48:51], v[68:71]\nv_mfma_f32_16x16x32_bf16 v[72:75], a[24:27], v[48:51], v[72:75]\nv_mfma_f32_16x16x32_bf16 v[76:79], a[28:31], v[48:51], v[76:79]\n"
"ds_read_b128 v[48:51], v34 offset:8192\ns_waitcnt lgkmcnt(3)\nv_mfma_f32_16x16x32_bf16 v[80:83], v[152:155], v[52:55], v[80:83]\nv_mfma_f32_16x16x32_bf16 v[84:87], v[156:159], v[52:55], v[84:87]\n"
"s_add_u32 m0, s52, 4096\nv_mfma_f32_16x16x32_bf16 v[88:91], a[24:27], v[52:55], v[88:91]\ns_add_u32 s53, s49, 128\nv_mfma_f32_16x16x32_bf16 v[92:95], a[28:31], v[52:55], v[92:95]\n"
"ds_read_b128 v[52:55], v35 offset:8192\nbuffer_load_dwordx4 v10, s[12:15], s53 offen lds\ns_waitcnt lgkmcnt(3)\nv_mfma_f32_16x16x32_bf16 v[64:67], v[160:163], v[40:43], v[64:67]\n"
"v_mfma_f32_16x16x32_bf16 v[68:71], v[164:167], v[40:43], v[68:71]\nv_mfma_f32_16x16x32_bf16 v[72:75], a[32:35], v[40:43], v[72:75]\nv_mfma_f32_16x16x32_bf16 v[76:79], a[36:39], v[40:43], v[76:79]\nds_read_b128 v[40:43], v32 offset:12288\n"
"s_waitcnt lgkmcnt(3)\nv_mfma_f32_16x16x32_bf16 v[80:83], v[160:163], v[44:47], v[80:83]\nv_mfma_f32_16x16x32_bf16 v[84:87], v[164:167], v[44:47], v[84:87]\nv_mfma_f32_16x16x32_bf16 v[88:91], a[32:35], v[44:47], v[88:91]\n"
"v_mfma_f32_16x16x32_bf16 v[92:95], a[36:39], v[44:47], v[92:95]\nds_read_b128 v[44:47], v33 offset:12288\ns_waitcnt lgkmcnt(3)\nv_mfma_f32_16x16x32_bf16 v[64:67], v[168:171], v[48:51], v[64:67]\n"
"s_add_u32 m0, s52, 8192\nv_mfma_f32_16x16x32_bf16 v[68:71], v[172:175], v[48:51], v[68:71]\ns_add_u32 s53, s49, 256\nv_mfma_f32_16x16x32_bf16 v[72:75], a[40:43], v[48:51], v[72:75]\n"
"buffer_load_dwordx4 v10, s[12:15], s53 offen lds\nv_mfma_f32_16x16x32_bf16 v[76:79], a[44:47], v[48:51], v[76:79]\nds_read_b128 v[48:51], v34 offset:12288\ns_waitcnt lgkmcnt(3)\n"
"v_mfma_f32_16x16x32_bf16 v[80:83], v[168:171], v[52:55], v[80:83]\nv_mfma_f32_16x16x32_bf16 v[84:87], v[172:175], v[52:55], v[84:87]\nv_mfma_f32_16x16x32_bf16 v[88:91], a[40:43], v[52:55], v[88:91]\nv_mfma_f32_16x16x32_bf16 v[92:95], a[44:47], v[52:55], v[92:95]\n"
"ds_read_b128 v[52:55], v35 offset:12288\ns_waitcnt lgkmcnt(3)\nv_mfma_f32_16x16x32_bf16 v[64:67], v[176:179], v[40:43], v[64:67]\nv_mfma_f32_16x16x32_bf16 v[68:71], v[180:183], v[40:43], v[68:71]\n"
"v_mfma_f32_16x16x32_bf16 v[72:75], a[48:51], v[40:43], v[72:75]\nv_mfma_f32_16x16x32_bf16 v[76:79], a[52:55], v[40:43], v[76:79]\nds_read_b128 v[40:43], v32 offset:16384\ns_add_u32 m0, s52, 12288\n"
"s_waitcnt lgkmcnt(3)\nv_mfma_f32_16x16x32_bf16 v[80:83], v[176:179], v[44:47], v[80:83]\ns_add_u32 s53, s49, 384\nv_mfma_f32_16x16x32_bf16 v[84:87], v[180:183], v[44:47], v[84:87]\n"
"buffer_load_dwordx4 v10, s[12:15], s53 offen lds\nv_mfma_f32_16x16x32_bf16 v[88:91], a[48:51], v[44:47], v[88:91]\nv_mfma_f32_16x16x32_bf16 v[92:95], a[52:55], v[44:47], v[92:95]\nds_read_b128 v[44:47], v33 offset:16384\n"
"s_waitcnt lgkmcnt(3)\nv_mfma_f32_16x16x32_bf16 v[64:67], v[184:187], v[48:51], v[64:67]\nv_mfma_f32_16x16x32_bf16 v[68:71], v[188:191], v[48:51], v[68:71]\nv_mfma_f32_16x16x32_bf16 v[72:75], a[56:59], v[48:51], v[72:75]\n"
"v_mfma_f32_16x16x32_bf16 v[76:79], a[60:63], v[48:51], v[76:79]\nds_read_b128 v[48:51], v34 offset:16384\ns_waitcnt lgkmcnt(3)\nv_mfma_f32_16x16x32_bf16 v[80:83], v[184:187], v[52:55], v[80:83]\n"
"v_mfma_f32_16x16x32_bf16 v[84:87], v[188:191], v[52:55], v[84:87]\nv_mfma_f32_16x16x32_bf16 v[88:91], a[56:59], v[52:55], v[88:91]\ns_add_u32 m0, s52, 16384\nv_mfma_f32_16x16x32_bf16 v[92:95], a[60:63], v[52:55], v[92:95]\n"
"ds_read_b128 v[52:55], v35 offset:16384\ns_add_u32 s53, s49, 512\ns_waitcnt lgkmcnt(3)\nv_mfma_f32_16x16x32_bf16 v[64:67], v[192:195], v[40:43], v[64:67]\n"
"buffer_load_dwordx4 v10, s[12:15], s53 offen lds\nv_mfma_f32_16x16x32_bf16 v[68:71], v[196:199], v[40:43], v[68:71]\nv_mfma_f32_16x16x32_bf16 v[72:75], a[64:67], v[40:43], v[72:75]\nv_mfma_f32_16x16x32_bf16 v[76:79], a[68:71], v[40:43], v[76:79]\n"
"ds_read_b128 v[40:43], v32 offset:20480\ns_waitcnt lgkmcnt(3)\nv_mfma_f32_16x16x32_bf16 v[80:83], v[192:195], v[44:47], v[80:83]\nv_mfma_f32_16x16x32_bf16 v[84:87], v[196:199], v[44:47], v[84:87]\n"
"v_mfma_f32_16x16x32_bf16 v[88:91], a[64:67], v[44:47], v[88:91]\nv_mfma_f32_16x16x32_bf16 v[92:95], a[68:71], v[44:47], v[92:95]\nds_read_b128 v[44:47], v33 offset:20480\ns_waitcnt lgkmcnt(3)\n"
"v_mfma_f32_16x16x32_bf16 v[64:67], v[200:203], v[48:51], v[64:67]\nv_mfma_f32_16x16x32_bf16 v[68:71], v[204:207], v[48:51], v[68:71]\ns_add_u32 m0, s52, 20480\nv_mfma_f32_16x16x32_bf16 v[72:75], a[72:75], v[48:51], v[72:75]\n"
"s_add_u32 s53, s49, 640\nv_mfma_f32_16x16x32_bf16 v[76:79], a[76:79], v[48:51], v[76:79]\nds_read_b128 v[48:51], v34 offset:20480\nbuffer_load_dwordx4 v10, s[12:15], s53 offen lds\n"
"s_waitcnt lgkmcnt(3)\nv_mfma_f32_16x16x32_bf16 v[80:83], v[200:203], v[52:55], v[80:83]\nv_mfma_f32_16x16x32_bf16 v[84:87], v[204:207], v[52:55], v[84:87]\nv_mfma_f32_16x16x32_bf16 v[88:91], a[72:75], v[52:55], v[88:91]\n"
"v_mfma_f32_16x16x32_bf16 v[92:95], a[76:79], v[52:55], v[92:95]\nds_read_b128 v[52:55], v35 offset:20480\ns_waitcnt lgkmcnt(3)\nv_mfma_f32_16x16x32_bf16 v[64:67], v[208:211], v[40:43], v[64:67]\n"
"v_mfma_f32_16x16x32_bf16 v[68:71], v[212:215], v[40:43], v[68:71]\nv_mfma_f32_16x16x32_bf16 v[72:75], a[80:83], v[40:43], v[72:75]\nv_mfma_f32_16x16x32_bf16 v[76:79], a[84:87], v[40:43], v[76:79]\nds_read_b128 v[40:43], v32 offset:24576\n"
"s_waitcnt lgkmcnt(3)\nv_mfma_f32_16x16x32_bf16 v[80:83], v[208:211], v[44:47], v[80:83]\ns_add_u32 m0, s52, 24576\nv_mfma_f32_16x16x32_bf16 v[84:87], v[212:215], v[44:47], v[84:87]\n"
"s_add_u32 s53, s49, 768\nv_mfma_f32_16x16x32_bf16 v[88:91], a[80:83], v[44:47], v[88:91]\nbuffer_load_dwordx4 v10, s[12:15], s53 offen lds\nv_mfma_f32_16x16x32_bf16 v[92:95], a[84:87], v[44:47], v[92:95]\n"
"ds_read_b128 v[44:47], v33 offset:24576\ns_waitcnt lgkmcnt(3)\nv_mfma_f32_16x16x32_bf16 v[64:67], v[216:219], v[48:51], v[64:67]\ns_add_u32 s54, s51, 32768\n"
"s_cmp_eq_u32 s54, 163840\nv_mfma_f32_16x16x32_bf16 v[68:71], v[220:223], v[48:51], v[68:71]\nv_mfma_f32_16x16x32_bf16 v[72:75], a[88:91], v[48:51], v[72:75]\nv_mfma_f32_16x16x32_bf16 v[76:79], a[92:95], v[48:51], v[76:79]\n"
"s_cselect_b32 s54, 0, s54\ns_sub_u32 s54, s54, s51\nds_read_b128 v[48:51], v34 offset:24576\ns_waitcnt lgkmcnt(3)\n"
"v_mfma_f32_16x16x32_bf16 v[80:83], v[216:219], v[52:55], v[80:83]\nv_mfma_f32_16x16x32_bf16 v[84:87], v[220:223], v[52:55], v[84:87]\nv_mfma_f32_16x16x32_bf16 v[88:91], a[88:91], v[52:55], v[88:91]\nv_mfma_f32_16x16x32_bf16 v[92:95], a[92:95], v[52:55], v[92:95]\n"
"ds_read_b128 v[52:55], v35 offset:24576\ns_waitcnt lgkmcnt(3)\nv_mfma_f32_16x16x32_bf16 v[64:67], v[224:227], v[40:43], v[64:67]\nv_add_u32 v32, s54, v32\n"
"v_mfma_f32_16x16x32_bf16 v[68:71], v[228:231], v[40:43], v[68:71]\nv_mfma_f32_16x16x32_bf16 v[72:75], a[96:99], v[40:43], v[72:75]\nv_mfma_f32_16x16x32_bf16 v[76:79], a[100:103], v[40:43], v[76:79]\nds_read_b128 v[40:43], v32 offset:0\n"
"s_waitcnt lgkmcnt(3)\nv_mfma_f32_16x16x32_bf16 v[80:83], v[224:227], v[44:47], v[80:83]\nv_add_u32 v33, s54, v33\nv_mfma_f32_16x16x32_bf16 v[84:87], v[228:231], v[44:47], v[84:87]\n"
"v_mfma_f32_16x16x32_bf16 v[88:91], a[96:99], v[44:47], v[88:91]\nv_mfma_f32_16x16x32_bf16 v[92:95], a[100:103], v[44:47], v[92:95]\nds_read_b128 v[44:47], v33 offset:0\ns_waitcnt lgkmcnt(3)\n"
"v_mfma_f32_16x16x32_bf16 v[64:67], v[232:235], v[48:51], v[64:67]\nv_add_u32 v34, s54, v34\nv_mfma_f32_16x16x32_bf16 v[68:71], v[236:239], v[48:51], v[68:71]\nv_mfma_f32_16x16x32_bf16 v[72:75], a[104:107], v[48:51], v[72:75]\n"
"v_mfma_f32_16x16x32_bf16 v[76:79], a[108:111], v[48:51], v[76:79]\nds_read_b128 v[48:51], v34 offset:0\ns_waitcnt lgkmcnt(3)\nv_mfma_f32_16x16x32_bf16 v[80:83], v[232:235], v[52:55], v[80:83]\n"
"v_add_u32 v35, s54, v35\nv_mfma_f32_16x16x32_bf16 v[84:87], v[236:239], v[52:55], v[84:87]\nv_mfma_f32_16x16x32_bf16 v[88:91], a[104:107], v[52:55], v[88:91]\nv_mfma_f32_16x16x32_bf16 v[92:95], a[108:111], v[52:55], v[92:95]\n"
"ds_read_b128 v[52:55], v35 offset:0\ns_mov_b32 s52, s47\ns_add_u32 s47, s51, s32\ns_add_u32 s51, s51, s54\n"
"s_add_u32 s49, s49, 28672\ns_waitcnt vmcnt(7)\ns_barrier\ns_waitcnt lgkmcnt(3)\n"
"v_mfma_f32_16x16x32_bf16 v[96:99], v[128:131], v[40:43], 0\nv_mov_b32 v36, s48\ns_add_u32 s48, s48, 1\ns_nop 0\n"
"buffer_store_dword v36, v19, s[24:27], s33 offen sc1\nv_mfma_f32_16x16x32_bf16 v[100:103], v[132:135], v[40:43], 0\nv_mfma_f32_16x16x32_bf16 v[104:107], a[0:3], v[40:43], 0\nv_mfma_f32_16x16x32_bf16 v[108:111], a[4:7], v[40:43], 0\n"
"ds_read_b128 v[40:43], v32 offset:4096\nv_cvt_pk_bf16_f32 v56, v64, v65\ns_waitcnt lgkmcnt(3)\nv_mfma_f32_16x16x32_bf16 v[112:115], v[128:131], v[44:47], 0\n"
"v_cvt_pk_bf16_f32 v57, v66, v67\nv_mfma_f32_16x16x32_bf16 v[116:119], v[132:135], v[44:47], 0\nv_cvt_pk_bf16_f32 v58, v68, v69\nv_mfma_f32_16x16x32_bf16 v[120:123], a[0:3], v[44:47], 0\n"
"v_cvt_pk_bf16_f32 v59, v70, v71\nv_mfma_f32_16x16x32_bf16 v[124:127], a[4:7], v[44:47], 0\nds_read_b128 v[44:47], v33 offset:4096\ns_waitcnt lgkmcnt(3)\n"
"v_mfma_f32_16x16x32_bf16 v[96:99], v[136:139], v[48:51], v[96:99]\nv_mfma_f32_16x16x32_bf16 v[100:103], v[140:143], v[48:51], v[100:103]\nbuffer_store_dwordx4 v[56:59], v15, s[20:23], s50 offen sc1\nv_mfma_f32_16x16x32_bf16 v[104:107], a[8:11], v[48:51], v[104:107]\n"
"v_cvt_pk_bf16_f32 v60, v80, v81\nv_mfma_f32_16x16x32_bf16 v[108:111], a[12:15], v[48:51], v[108:111]\nds_read_b128 v[48:51], v34 offset:4096\nv_cvt_pk_bf16_f32 v61, v82, v83\n"
"s_waitcnt lgkmcnt(3)\nv_mfma_f32_16x16x32_bf16 v[112:115], v[136:139], v[52:55], v[112:115]\nv_cvt_pk_bf16_f32 v62, v84, v85\nv_mfma_f32_16x16x32_bf16 v[116:119], v[140:143], v[52:55], v[116:119]\n"
"v_cvt_pk_bf16_f32 v63, v86, v87\nv_mfma_f32_16x16x32_bf16 v[120:123], a[8:11], v[52:55], v[120:123]\nv_mfma_f32_16x16x32_bf16 v[124:127], a[12:15], v[52:55], v[124:127]\nds_read_b128 v[52:55], v35 offset:4096\n"
"s_waitcnt lgkmcnt(3)\nv_mfma_f32_16x16x32_bf16 v[96:99], v[144:147], v[40:43], v[96:99]\nbuffer_store_dwordx4 v[60:63], v16, s[20:23], s50 offen sc1\nv_mfma_f32_16x16x32_bf16 v[100:103], v[148:151], v[40:43], v[100:103]\n"
"v_cvt_pk_bf16_f32 v56, v72, v73\nv_mfma_f32_16x16x32_bf16 v[104:107], a[16:19], v[40:43], v[104:107]\nv_cvt_pk_bf16_f32 v57, v74, v75\nv_mfma_f32_16x16x32_bf16 v[108:111], a[20:23], v[40:43], v[108:111]\n"
"ds_read_b128 v[40:43], v32 offset:8192\nv_cvt_pk_bf16_f32 v58, v76, v77\ns_waitcnt lgkmcnt(3)\nv_mfma_f32_16x16x32_bf16 v[112:115], v[144:147], v[44:47], v[112:115]\n"
"v_cvt_pk_bf16_f32 v59, v78, v79\nv_mfma_f32_16x16x32_bf16 v[116:119], v[148:151], v[44:47], v[116:119]\nv_mfma_f32_16x16x32_bf16 v[120:123], a[16:19], v[44:47], v[120:123]\nv_mfma_f32_16x16x32_bf16 v[124:127], a[20:23], v[44:47], v[124:127]\n"
"ds_read_b128 v[44:47], v33 offset:8192\nbuffer_store_dwordx4 v[56:59], v17, s[20:23], s50 offen sc1\ns_waitcnt lgkmcnt(3)\nv_mfma_f32_16x16x32_bf16 v[96:99], v[152:155], v[48:51], v[96:99]\n"
"v_cvt_pk_bf16_f32 v60, v88, v89\nv_mfma_f32_16x16x32_bf16 v[100:103], v[156:159], v[48:51], v[100:103]\nv_cvt_pk_bf16_f32 v61, v90, v91\nv_mfma_f32_16x16x32_bf16 v[104:107], a[24:27], v[48:51], v[104:107]\n"
"v_cvt_pk_bf16_f32 v62, v92, v93\nv_mfma_f32_16x16x32_bf16 v[108:111], a[28:31], v[48:51], v[108:111]\nds_read_b128 v[48:51], v34 offset:8192\nv_cvt_pk_bf16_f32 v63, v94, v95\n"
"s_waitcnt lgkmcnt(3)\nv_mfma_f32_16x16x32_bf16 v[112:115], v[152:155], v[52:55], v[112:115]\nv_mfma_f32_16x16x32_bf16 v[116:119], v[156:159], v[52:55], v[116:119]\nv_mfma_f32_16x16x32_bf16 v[120:123], a[24:27], v[52:55], v[120:123]\n"
"buffer_store_dwordx4 v[60:63], v18, s[20:23], s50 offen sc1\nv_mfma_f32_16x16x32_bf16 v[124:127], a[28:31], v[52:55], v[124:127]\nds_read_b128 v[52:55], v35 offset:8192\ns_add_u32 s50, s50, s36\n"
"s_waitcnt lgkmcnt(3)\nv_mfma_f32_16x16x32_bf16 v[96:99], v[160:163], v[40:43], v[96:99]\ns_add_u32 m0, s52, 0\nv_mfma_f32_16x16x32_bf16 v[100:103], v[164:167], v[40:43], v[100:103]\n"
"s_add_u32 s53, s49, 0\nv_mfma_f32_16x16x32_bf16 v[104:107], a[32:35], v[40:43], v[104:107]\nbuffer_load_dwordx4 v10, s[12:15], s53 offen lds\nv_mfma_f32_16x16x32_bf16 v[108:111], a[36:39], v[40:43], v[108:111]\n"
"ds_read_b128 v[40:43], v32 offset:12288\ns_waitcnt lgkmcnt(3)\nv_mfma_f32_16x16x32_bf16 v[112:115], v[160:163], v[44:47], v[112:115]\nv_mfma_f32_16x16x32_bf16 v[116:119], v[164:167], v[44:47], v[116:119]\n"
"v_mfma_f32_16x16x32_bf16 v[120:123], a[32:35], v[44:47], v[120:123]\nv_mfma_f32_16x16x32_bf16 v[124:127], a[36:39], v[44:47], v[124:127]\nds_read_b128 v[44:47], v33 offset:12288\ns_waitcnt lgkmcnt(3)\n"
"v_mfma_f32_16x16x32_bf16 v[96:99], v[168:171], v[48:51], v[96:99]\nv_mfma_f32_16x16x32_bf16 v[100:103], v[172:175], v[48:51], v[100:103]\nv_mfma_f32_16x16x32_bf16 v[104:107], a[40:43], v[48:51], v[104:107]\nv_mfma_f32_16x16x32_bf16 v[108:111], a[44:47], v[48:51], v[108:111]\n"
"ds_read_b128 v[48:51], v34 offset:12288\ns_add_u32 m0, s52, 4096\ns_waitcnt lgkmcnt(3)\nv_mfma_f32_16x16x32_bf16 v[112:115], v[168:171], v[52:55], v[112:115]\n"
"s_add_u32 s53, s49, 128\nv_mfma_f32_16x16x32_bf16 v[116:119], v[172:175], v[52:55], v[116:119]\nbuffer_load_dwordx4 v10, s[12:15], s53 offen lds\nv_mfma_f32_16x16x32_bf16 v[120:123], a[40:43], v[52:55], v[120:123]\n"
"v_mfma_f32_16x16x32_bf16 v[124:127], a[44:47], v[52:55], v[124:127]\nds_read_b128 v[52:55], v35 offset:12288\ns_waitcnt lgkmcnt(3)\nv_mfma_f32_16x16x32_bf16 v[96:99], v[176:179], v[40:43], v[96:99]\n"
"v_mfma_f32_16x16x32_bf16 v[100:103], v[180:183], v[40:43], v[100:103]\nv_mfma_f32_16x16x32_bf16 v[104:107], a[48:51], v[40:43], v[104:107]\nv_mfma_f32_16x16x32_bf16 v[108:111], a[52:55], v[40:43], v[108:111]\nds_read_b128 v[40:43], v32 offset:16384\n"
"s_waitcnt lgkmcnt(3)\nv_mfma_f32_16x16x32_bf16 v[112:115], v[176:179], v[44:47], v[112:115]\nv_mfma_f32_16x16x32_bf16 v[116:119], v[180:183], v[44:47], v[116:119]\nv_mfma_f32_16x16x32_bf16 v[120:123], a[48:51], v[44:47], v[120:123]\n"
"s_add_u32 m0, s52, 8192\nv_mfma_f32_16x16x32_bf16 v[124:127], a[52:55], v[44:47], v[124:127]\nds_read_b128 v[44:47], v33 offset:16384\ns_add_u32 s53, s49, 256\n"
"s_waitcnt lgkmcnt(3)\nv_mfma_f32_16x16x32_bf16 v[96:99], v[184:187], v[48:51], v[96:99]\nbuffer_load_dwordx4 v10, s[12:15], s53 offen lds\nv_mfma_f32_16x16x32_bf16 v[100:103], v[188:191], v[48:51], v[100:103]\n"
"v_mfma_f32_16x16x32_bf16 v[104:107], a[56:59], v[48:51], v[104:107]\nv_mfma_f32_16x16x32_bf16 v[108:111], a[60:63], v[48:51], v[108:111]\nds_read_b128 v[48:51], v34 offset:16384\ns_waitcnt lgkmcnt(3)\n"
"v_mfma_f32_16x16x32_bf16 v[112:115], v[184:187], v[52:55], v[112:115]\nv_mfma_f32_16x16x32_bf16 v[116:119], v[188:191], v[52:55], v[116:119]\nv_mfma_f32_16x16x32_bf16 v[120:123], a[56:59], v[52:55], v[120:123]\nv_mfma_f32_16x16x32_bf16 v[124:127], a[60:63], v[52:55], v[124:127]\n"
"ds_read_b128 v[52:55], v35 offset:16384\ns_waitcnt lgkmcnt(3)\nv_mfma_f32_16x16x32_bf16 v[96:99], v[192:195], v[40:43], v[96:99]\nv_mfma_f32_16x16x32_bf16 v[100:103], v[196:199], v[40:43], v[100:103]\n"
"s_add_u32 m0, s52, 12288\nv_mfma_f32_16x16x32_bf16 v[104:107], a[64:67], v[40:43], v[104:107]\ns_add_u32 s53, s49, 384\nv_mfma_f32_16x16x32_bf16 v[108:111], a[68:71], v[40:43], v[108:111]\n"
"ds_read_b128 v[40:43], v32 offset:20480\nbuffer_load_dwordx4 v10, s[12:15], s53 offen lds\ns_waitcnt lgkmcnt(3)\nv_mfma_f32_16x16x32_bf16 v[112:115], v[192:195], v[44:47], v[112:115]\n"
"v_mfma_f32_16x16x32_bf16 v[116:119], v[196:199], v[44:47], v[116:119]\nv_mfma_f32_16x16x32_bf16 v[120:123], a[64:67], v[44:47], v[120:123]\nv_mfma_f32_16x16x32_bf16 v[124:127], a[68:71], v[44:47], v[124:127]\nds_read_b128 v[44:47], v33 offset:20480\n"
"s_waitcnt lgkmcnt(3)\nv_mfma_f32_16x16x32_bf16 v[96:99], v[200:203], v[48:51], v[96:99]\nv_mfma_f32_16x16x32_bf16 v[100:103], v[204:207], v[48:51], v[100:103]\nv_mfma_f32_16x16x32_bf16 v[104:107], a[72:75], v[48:51], v[104:107]\n"
"v_mfma_f32_16x16x32_bf16 v[108:111], a[76:79], v[48:51], v[108:111]\nds_read_b128 v[48:51], v34 offset:20480\ns_waitcnt lgkmcnt(3)\nv_mfma_f32_16x16x32_bf16 v[112:115], v[200:203], v[52:55], v[112:115]\n"
"s_add_u32 m0, s52, 16384\nv_mfma_f32_16x16x32_bf16 v[116:119], v[204:207], v[52:55], v[116:119]\ns_add_u32 s53, s49, 512\nv_mfma_f32_16x16x32_bf16 v[120:123], a[72:75], v[52:55], v[120:123]\n"
"buffer_load_dwordx4 v10, s[12:15], s53 offen lds\nv_mfma_f32_16x16x32_bf16 v[124:127], a[76:79], v[52:55], v[124:127]\nds_read_b128 v[52:55], v35 offset:20480\ns_waitcnt lgkmcnt(3)\n"
"v_mfma_f32_16x16x32_bf16 v[96:99], v[208:211], v[40:43], v[96:99]\nv_mfma_f32_16x16x32_bf16 v[100:103], v[212:215], v[40:43], v[100:103]\nv_mfma_f32_16x16x32_bf16 v[104:107], a[80:83], v[40:43], v[104:107]\nv_mfma_f32_16x16x32_bf16 v[108:111], a[84:87], v[40:43], v[108:111]\n"
"ds_read_b128 v[40:43], v32 offset:24576\ns_waitcnt lgkmcnt(3)\nv_mfma_f32_16x16x32_bf16 v[112:115], v[208:211], v[44:47], v[112:115]\nv_mfma_f32_16x16x32_bf16 v[116:119], v[212:215], v[44:47], v[116:119]\n"
"v_mfma_f32_16x16x32_bf16 v[120:123], a[80:83], v[44:47], v[120:123]\nv_mfma_f32_16x16x32_bf16 v[124:127], a[84:87], v[44:47], v[124:127]\nds_read_b128 v[44:47], v33 offset:24576\ns_add_u32 m0, s52, 20480\n"
"s_waitcnt lgkmcnt(3)\nv_mfma_f32_16x16x32_bf16 v[96:99], v[216:219], v[48:51], v[96:99]\ns_add_u32 s54, s51, 32768\ns_cmp_eq_u32 s54, 163840\n"
"v_mfma_f32_16x16x32_bf16 v[100:103], v[220:223], v[48:51], v[100:103]\nv_mfma_f32_16x16x32_bf16 v[104:107], a[88:91], v[48:51], v[104:107]\nv_mfma_f32_16x16x32_bf16 v[108:111], a[92:95], v[48:51], v[108:111]\ns_cselect_b32 s54, 0, s54\n"
"s_sub_u32 s54, s54, s51\nds_read_b128 v[48:51], v34 offset:24576\ns_add_u32 s53, s49, 640\ns_waitcnt lgkmcnt(3)\n"
"v_mfma_f32_16x16x32_bf16 v[112:115], v[216:219], v[52:55], v[112:115]\nbuffer_load_dwordx4 v10, s[12:15], s53 offen lds\nv_mfma_f32_16x16x32_bf16 v[116:119], v[220:223], v[52:55], v[116:119]\nv_mfma_f32_16x16x32_bf16 v[120:123], a[88:91], v[52:55], v[120:123]\n"
"v_mfma_f32_16x16x32_bf16 v[124:127], a[92:95], v[52:55], v[124:127]\nds_read_b128 v[52:55], v35 offset:24576\ns_waitcnt lgkmcnt(3)\nv_mfma_f32_16x16x32_bf16 v[96:99], v[224:227], v[40:43], v[96:99]\n"
"v_add_u32 v32, s54, v32\nv_mfma_f32_16x16x32_bf16 v[100:103], v[228:231], v[40:43], v[100:103]\nv_mfma_f32_16x16x32_bf16 v[104:107], a[96:99], v[40:43], v[104:107]\nv_mfma_f32_16x16x32_bf16 v[108:111], a[100:103], v[40:43], v[108:111]\n"
"ds_read_b128 v[40:43], v32 offset:0\ns_waitcnt lgkmcnt(3)\nv_mfma_f32_16x16x32_bf16 v[112:115], v[224:227], v[44:47], v[112:115]\nv_add_u32 v33, s54, v33\n"
"v_mfma_f32_16x16x32_bf16 v[116:119], v[228:231], v[44:47], v[116:119]\ns_add_u32 m0, s52, 24576\nv_mfma_f32_16x16x32_bf16 v[120:123], a[96:99], v[44:47], v[120:123]\ns_add_u32 s53, s49, 768\n"
"v_mfma_f32_16x16x32_bf16 v[124:127], a[100:103], v[44:47], v[124:127]\nds_read_b128 v[44:47], v33 offset:0\nbuffer_load_dwordx4 v10, s[12:15], s53 offen lds\ns_waitcnt lgkmcnt(3)\n"
"v_mfma_f32_16x16x32_bf16 v[96:99], v[232:235], v[48:51], v[96:99]\nv_add_u32 v34, s54, v34\nv_mfma_f32_16x16x32_bf16 v[100:103], v[236:239], v[48:51], v[100:103]\nv_mfma_f32_16x16x32_bf16 v[104:107], a[104:107], v[48:51], v[104:107]\n"
"v_mfma_f32_16x16x32_bf16 v[108:111], a[108:111], v[48:51], v[108:111]\nds_read_b128 v[48:51], v34 offset:0\ns_waitcnt lgkmcnt(3)\nv_mfma_f32_16x16x32_bf16 v[112:115], v[232:235], v[52:55], v[112:115]\n"
"v_add_u32 v35, s54, v35\nv_mfma_f32_16x16x32_bf16 v[116:119], v[236:239], v[52:55], v[116:119]\nv_mfma_f32_16x16x32_bf16 v[120:123], a[104:107], v[52:55], v[120:123]\nv_mfma_f32_16x16x32_bf16 v[124:127], a[108:111], v[52:55], v[124:127]\n"
"ds_read_b128 v[52:55], v35 offset:0\ns_mov_b32 s52, s47\ns_add_u32 s47, s51, s32\ns_add_u32 s51, s51, s54\n"
"s_add_u32 s49, s49, 28672\n.p2align 6\nfg0_loop:\ns_waitcnt vmcnt(7)\n"
"s_barrier\ns_waitcnt lgkmcnt(3)\nv_mfma_f32_16x16x32_bf16 v[64:67], v[128:131], v[40:43], 0\nv_mov_b32 v36, s48\n"
"s_add_u32 s48, s48, 1\ns_nop 0\nbuffer_store_dword v36, v19, s[24:27], s33 offen sc1\nv_mfma_f32_16x16x32_bf16 v[68:71], v[132:135], v[40:43], 0\n"
"v_mfma_f32_16x16x32_bf16 v[72:75], a[0:3], v[40:43], 0\nv_mfma_f32_16x16x32_bf16 v[76:79], a[4:7], v[40:43], 0\nds_read_b128 v[40:43], v32 offset:4096\nv_cvt_pk_bf16_f32 v56, v96, v97\n"
"s_waitcnt lgkmcnt(3)\nv_mfma_f32_16x16x32_bf16 v[80:83], v[128:131], v[44:47], 0\nv_cvt_pk_bf16_f32 v57, v98, v99\nv_mfma_f32_16x16x32_bf16 v[84:87], v[132:135], v[44:47], 0\n"
"v_cvt_pk_bf16_f32 v58, v100, v101\nv_mfma_f32_16x16x32_bf16 v[88:91], a[0:3], v[44:47], 0\nv_cvt_pk_bf16_f32 v59, v102, v103\nv_mfma_f32_16x16x32_bf16 v[92:95], a[4:7], v[44:47], 0\n"
"ds_read_b128 v[44:47], v33 offset:4096\ns_waitcnt lgkmcnt(3)\nv_mfma_f32_16x16x32_bf16 v[64:67], v[136:139], v[48:51], v[64:67]\nv_mfma_f32_16x16x32_bf16 v[68:71], v[140:143], v[48:51], v[68:71]\n"
"buffer_store_dwordx4 v[56:59], v15, s[20:23], s50 offen sc1\nv_mfma_f32_16x16x32_bf16 v[72:75], a[8:11], v[48:51], v[72:75]\nv_cvt_pk_bf16_f32 v60, v112, v113\nv_mfma_f32_16x16x32_bf16 v[76:79], a[12:15], v[48:51], v[76:79]\n"
"ds_read_b128 v[48:51], v34 offset:4096\nv_cvt_pk_bf16_f32 v61, v114, v115\ns_waitcnt lgkmcnt(3)\nv_mfma_f32_16x16x32_bf16 v[80:83], v[136:139], v[52:55], v[80:83]\n"
"v_cvt_pk_bf16_f32 v62, v116, v117\nv_mfma_f32_16x16x32_bf16 v[84:87], v[140:143], v[52:55], v[84:87]\nv_cvt_pk_bf16_f32 v63, v118, v119\nv_mfma_f32_16x16x32_bf16 v[88:91], a[8:11], v[52:55], v[88:91]\n"
"v_mfma_f32_16x16x32_bf16 v[92:95], a[12:15], v[52:55], v[92:95]\nds_read_b128 v[52:55], v35 offset:4096\ns_waitcnt lgkmcnt(3)\nv_mfma_f32_16x16x32_bf16 v[64:67], v[144:147], v[40:43], v[64:67]\n"
"buffer_store_dwordx4 v[60:63], v16, s[20:23], s50 offen sc1\nv_mfma_f32_16x16x32_bf16 v[68:71], v[148:151], v[40:43], v[68:71]\nv_cvt_pk_bf16_f32 v56, v104, v105\nv_mfma_f32_16x16x32_bf16 v[72:75], a[16:19], v[40:43], v[72:75]\n"
"v_cvt_pk_bf16_f32 v57, v106, v107\nv_mfma_f32_16x16x32_bf16 v[76:79], a[20:23], v[40:43], v[76:79]\nds_read_b128 v[40:43], v32 offset:8192\nv_cvt_pk_bf16_f32 v58, v108, v109\n"
"s_waitcnt lgkmcnt(3)\nv_mfma_f32_16x16x32_bf16 v[80:83], v[144:147], v[44:47], v[80:83]\nv_cvt_pk_bf16_f32 v59, v110, v111\nv_mfma_f32_16x16x32_bf16 v[84:87], v[148:151], v[44:47], v[84:87]\n"
"v_mfma_f32_16x16x32_bf16 v[88:91], a[16:19], v[44:47], v[88:91]\nv_mfma_f32_16x16x32_bf16 v[92:95], a[20:23], v[44:47], v[92:95]\nds_read_b128 v[44:47], v33 offset:8192\nbuffer_store_dwordx4 v[56:59], v17, s[20:23], s50 offen sc1\n"
"s_waitcnt lgkmcnt(3)\nv_mfma_f32_16x16x32_bf16 v[64:67], v[152:155], v[48:51], v[64:67]\nv_cvt_pk_bf16_f32 v60, v120, v121\nv_mfma_f32_16x16x32_bf16 v[68:71], v[156:159], v[48:51], v[68:71]\n"
"v_cvt_pk_bf16_f32 v61, v122, v123\nv_mfma_f32_16x16x32_bf16 v[72:75], a[24:27], v[48:51], v[72:75]\nv_cvt_pk_bf16_f32 v62, v124, v125\nv_mfma_f32_16x16x32_bf16 v[76:79], a[28:31], v[48:51], v[76:79]\n"
"ds_read_b128 v[48:51], v34 offset:8192\nv_cvt_pk_bf16_f32 v63, v126, v127\ns_waitcnt lgkmcnt(3)\nv_mfma_f32_16x16x32_bf16 v[80:83], v[152:155], v[52:55], v[80:83]\n"
"v_mfma_f32_16x16x32_bf16 v[84:87], v[156:159], v[52:55], v[84:87]\nv_mfma_f32_16x16x32_bf16 v[88:91], a[24:27], v[52:55], v[88:91]\nbuffer_store_dwordx4 v[60:63], v18, s[20:23], s50 offen sc1\nv_mfma_f32_16x16x32_bf16 v[92:95], a[28:31], v[52:55], v[92:95]\n"
"ds_read_b128 v[52:55], v35 offset:8192\ns_add_u32 s50, s50, s36\ns_waitcnt lgkmcnt(3)\nv_mfma_f32_16x16x32_bf16 v[64:67], v[160:163], v[40:43], v[64:67]\n"
"s_add_u32 m0, s52, 0\nv_mfma_f32_16x16x32_bf16 v[68:71], v[164:167], v[40:43], v[68:71]\ns_add_u32 s53, s49, 0\nv_mfma_f32_16x16x32_bf16 v[72:75], a[32:35], v[40:43], v[72:75]\n"
"buffer_load_dwordx4 v10, s[12:15], s53 offen lds\nv_mfma_f32_16x16x32_bf16 v[76:79], a[36:39], v[40:43], v[76:79]\nds_read_b128 v[40:43], v32 offset:12288\ns_waitcnt lgkmcnt(3)\n"
"v_mfma_f32_16x16x32_bf16 v[80:83], v[160:163], v[44:47], v[80:83]\nv_mfma_f32_16x16x32_bf16 v[84:87], v[164:167], v[44:47], v[84:87]\nv_mfma_f32_16x16x32_bf16 v[88:91], a[32:35], v[44:47], v[88:91]\nv_mfma_f32_16x16x32_bf16 v[92:95], a[36:39], v[44:47], v[92:95]\n"
"ds_read_b128 v[44:47], v33 offset:12288\ns_waitcnt lgkmcnt(3)\nv_mfma_f32_16x16x32_bf16 v[64:67], v[168:171], v[48:51], v[64:67]\nv_mfma_f32_16x16x32_bf16 v[68:71], v[172:175], v[48:51], v[68:71]\n"
"v_mfma_f32_16x16x32_bf16 v[72:75], a[40:43], v[48:51], v[72:75]\nv_mfma_f32_16x16x32_bf16 v[76:79], a[44:47], v[48:51], v[76:79]\nds_read_b128 v[48:51], v34 offset:12288\ns_add_u32 m0, s52, 4096\n"
"s_waitcnt lgkmcnt(3)\nv_mfma_f32_16x16x32_bf16 v[80:83], v[168:171], v[52:55], v[80:83]\ns_add_u32 s53, s49, 128\nv_mfma_f32_16x16x32_bf16 v[84:87], v[172:175], v[52:55], v[84:87]\n"
"buffer_load_dwordx4 v10, s[12:15], s53 offen lds\nv_mfma_f32_16x16x32_bf16 v[88:91], a[40:43], v[52:55], v[88:91]\nv_mfma_f32_16x16x32_bf16 v[92:95], a[44:47], v[52:55], v[92:95]\nds_read_b128 v[52:55], v35 offset:12288\n"
"s_waitcnt lgkmcnt(3)\nv_mfma_f32_16x16x32_bf16 v[64:67], v[176:179], v[40:43], v[64:67]\nv_mfma_f32_16x16x32_bf16 v[68:71], v[180:183], v[40:43], v[68:71]\nv_mfma_f32_16x16x32_bf16 v[72:75], a[48:51], v[40:43], v[72:75]\n"
"v_mfma_f32_16x16x32_bf16 v[76:79], a[52:55], v[40:43], v[76:79]\nds_read_b128 v[40:43], v32 offset:16384\ns_waitcnt lgkmcnt(3)\nv_mfma_f32_16x16x32_bf16 v[80:83], v[176:179], v[44:47], v[80:83]\n"
"v_mfma_f32_16x16x32_bf16 v[84:87], v[180:183], v[44:47], v[84:87]\nv_mfma_f32_16x16x32_bf16 v[88:91], a[48:51], v[44:47], v[88:91]\ns_add_u32 m0, s52, 8192\nv_mfma_f32_16x16x32_bf16 v[92:95], a[52:55], v[44:47], v[92:95]\n"
"ds_read_b128 v[44:47], v33 offset:16384\ns_add_u32 s53, s49, 256\ns_waitcnt lgkmcnt(3)\nv_mfma_f32_16x16x32_bf16 v[64:67], v[184:187], v[48:51], v[64:67]\n"
"buffer_load_dwordx4 v10, s[12:15], s53 offen lds\nv_mfma_f32_16x16x32_bf16 v[68:71], v[188:191], v[48:51], v[68:71]\nv_mfma_f32_16x16x32_bf16 v[72:75], a[56:59], v[48:51], v[72:75]\nv_mfma_f32_16x16x32_bf16 v[76:79], a[60:63], v[48:51], v[76:79]\n"
"ds_read_b128 v[48:51], v34 offset:16384\ns_waitcnt lgkmcnt(3)\nv_mfma_f32_16x16x32_bf16 v[80:83], v[184:187], v[52:55], v[80:83]\nv_mfma_f32_16x16x32_bf16 v[84:87], v[188:191], v[52:55], v[84:87]\n"
"v_mfma_f32_16x16x32_bf16 v[88:91], a[56:59], v[52:55], v[88:91]\nv_mfma_f32_16x16x32_bf16 v[92:95], a[60:63], v[52:55], v[92:95]\nds_read_b128 v[52:55], v35 offset:16384\ns_waitcnt lgkmcnt(3)\n"
"v_mfma_f32_16x16x32_bf16 v[64:67], v[192:195], v[40:43], v[64:67]\nv_mfma_f32_16x16x32_bf16 v[68:71], v[196:199], v[40:43], v[68:71]\ns_add_u32 m0, s52, 12288\nv_mfma_f32_16x16x32_bf16 v[72:75], a[64:67], v[40:43], v[72:75]\n"
"s_add_u32 s53, s49, 384\nv_mfma_f32_16x16x32_bf16 v[76:79], a[68:71], v[40:43], v[76:79]\nds_read_b128 v[40:43], v32 offset:20480\nbuffer_load_dwordx4 v10, s[12:15], s53 offen lds\n"
"s_waitcnt lgkmcnt(3)\nv_mfma_f32_16x16x32_bf16 v[80:83], v[192:195], v[44:47], v[80:83]\nv_mfma_f32_16x16x32_bf16 v[84:87], v[196:199], v[44:47], v[84:87]\nv_mfma_f32_16x16x32_bf16 v[88:91], a[64:67], v[44:47], v[88:91]\n"
"v_mfma_f32_16x16x32_bf16 v[92:95], a[68:71], v[44:47], v[92:95]\nds_read_b128 v[44:47], v33 offset:20480\ns_waitcnt lgkmcnt(3)\nv_mfma_f32_16x16x32_bf16 v[64:67], v[200:203], v[48:51], v[64:67]\n"
"v_mfma_f32_16x16x32_bf16 v[68:71], v[204:207], v[48:51], v[68:71]\nv_mfma_f32_16x16x32_bf16 v[72:75], a[72:75], v[48:51], v[72:75]\nv_mfma_f32_16x16x32_bf16 v[76:79], a[76:79], v[48:51], v[76:79]\nds_read_b128 v[48:51], v34 offset:20480\n"
"s_waitcnt lgkmcnt(3)\nv_mfma_f32_16x16x32_bf16 v[80:83], v[200:203], v[52:55], v[80:83]\ns_add_u32 m0, s52, 16384\nv_mfma_f32_16x16x32_bf16 v[84:87], v[204:207], v[52:55], v[84:87]\n"
"s_add_u32 s53, s49, 512\nv_mfma_f32_16x16x32_bf16 v[88:91], a[72:75], v[52:55], v[88:91]\nbuffer_load_dwordx4 v10, s[12:15], s53 offen lds\nv_mfma_f32_16x16x32_bf16 v[92:95], a[76:79], v[52:55], v[92:95]\n"
"ds_read_b128 v[52:55], v35 offset:20480\ns_waitcnt lgkmcnt(3)\nv_mfma_f32_16x16x32_bf16 v[64:67], v[208:211], v[40:43], v[64:67]\nv_mfma_f32_16x16x32_bf16 v[68:71], v[212:215], v[40:43], v[68:71]\n"
"v_mfma_f32_16x16x32_bf16 v[72:75], a[80:83], v[40:43], v[72:75]\nv_mfma_f32_16x16x32_bf16 v[76:79], a[84:87], v[40:43], v[76:79]\nds_read_b128 v[40:43], v32 offset:24576\ns_waitcnt lgkmcnt(3)\n"
"v_mfma_f32_16x16x32_bf16 v[80:83], v[208:211], v[44:47], v[80:83]\nv_mfma_f32_16x16x32_bf16 v[84:87], v[212:215], v[44:47], v[84:87]\nv_mfma_f32_16x16x32_bf16 v[88:91], a[80:83], v[44:47], v[88:91]\nv_mfma_f32_16x16x32_bf16 v[92:95], a[84:87], v[44:47], v[92:95]\n"
"ds_read_b128 v[44:47], v33 offset:24576\ns_add_u32 m0, s52, 20480\ns_waitcnt lgkmcnt(3)\nv_mfma_f32_16x16x32_bf16 v[64:67], v[216:219], v[48:51], v[64:67]\n"
"s_add_u32 s54, s51, 32768\ns_cmp_eq_u32 s54, 163840\nv_mfma_f32_16x16x32_bf16 v[68:71], v[220:223], v[48:51], v[68:71]\nv_mfma_f32_16x16x32_bf16 v[72:75], a[88:91], v[48:51], v[72:75]\n"
"v_mfma_f32_16x16x32_bf16 v[76:79], a[92:95], v[48:51], v[76:79]\ns_cselect_b32 s54, 0, s54\ns_sub_u32 s54, s54, s51\nds_read_b128 v[48:51], v34 offset:24576\n"
"s_add_u32 s53, s49, 640\ns_waitcnt lgkmcnt(3)\nv_mfma_f32_16x16x32_bf16 v[80:83], v[216:219], v[52:55], v[80:83]\nbuffer_load_dwordx4 v10, s[12:15], s53 offen lds\n"
"v_mfma_f32_16x16x32_bf16 v[84:87], v[220:223], v[52:55], v[84:87]\nv_mfma_f32_16x16x32_bf16 v[88:91], a[88:91], v[52:55], v[88:91]\nv_mfma_f32_16x16x32_bf16 v[92:95], a[92:95], v[52:55], v[92:95]\nds_read_b128 v[52:55], v35 offset:24576\n"
"s_waitcnt lgkmcnt(3)\nv_mfma_f32_16x16x32_bf16 v[64:67], v[224:227], v[40:43], v[64:67]\nv_add_u32 v32, s54, v32\nv_mfma_f32_16x16x32_bf16 v[68:71], v[228:231], v[40:43], v[68:71]\n"
"v_mfma_f32_16x16x32_bf16 v[72:75], a[96:99], v[40:43], v[72:75]\nv_mfma_f32_16x16x32_bf16 v[76:79], a[100:103], v[40:43], v[76:79]\nds_read_b128 v[40:43], v32 offset:0\ns_waitcnt lgkmcnt(3)\n"
"v_mfma_f32_16x16x32_bf16 v[80:83], v[224:227], v[44:47], v[80:83]\nv_add_u32 v33, s54, v33\nv_mfma_f32_16x16x32_bf16 v[84:87], v[228:231], v[44:47], v[84:87]\ns_add_u32 m0, s52, 24576\n"
"v_mfma_f32_16x16x32_bf16 v[88:91], a[96:99], v[44:47], v[88:91]\ns_add_u32 s53, s49, 768\nv_mfma_f32_16x16x32_bf16 v[92:95], a[100:103], v[44:47], v[92:95]\nds_read_b128 v[44:47], v33 offset:0\n"
"buffer_load_dwordx4 v10, s[12:15], s53 offen lds\ns_waitcnt lgkmcnt(3)\nv_mfma_f32_16x16x32_bf16 v[64:67], v[232:235], v[48:51], v[64:67]\nv_add_u32 v34, s54, v34\n"
"v_mfma_f32_16x16x32_bf16 v[68:71], v[236:239], v[48:51], v[68:71]\nv_mfma_f32_16x16x32_bf16 v[72:75], a[104:107], v[48:51], v[72:75]\nv_mfma_f32_16x16x32_bf16 v[76:79], a[108:111], v[48:51], v[76:79]\nds_read_b128 v[48:51], v34 offset:0\n"
"s_waitcnt lgkmcnt(3)\nv_mfma_f32_16x16x32_bf16 v[80:83], v[232:235], v[52:55], v[80:83]\nv_add_u32 v35, s54, v35\nv_mfma_f32_16x16x32_bf16 v[84:87], v[236:239], v[52:55], v[84:87]\n"
"v_mfma_f32_16x16x32_bf16 v[88:91], a[104:107], v[52:55], v[88:91]\nv_mfma_f32_16x16x32_bf16 v[92:95], a[108:111], v[52:55], v[92:95]\nds_read_b128 v[52:55], v35 offset:0\ns_mov_b32 s52, s47\n"
"s_add_u32 s47, s51, s32\ns_add_u32 s51, s51, s54\ns_add_u32 s49, s49, 28672\ns_waitcnt vmcnt(7)\n"
"s_barrier\ns_waitcnt lgkmcnt(3)\nv_mfma_f32_16x16x32_bf16 v[96:99], v[128:131], v[40:43], 0\ns_add_u32 s48, s48, 1\n"
"v_mfma_f32_16x16x32_bf16 v[100:103], v[132:135], v[40:43], 0\nv_mfma_f32_16x16x32_bf16 v[104:107], a[0:3], v[40:43], 0\nv_mfma_f32_16x16x32_bf16 v[108:111], a[4:7], v[40:43], 0\nds_read_b128 v[40:43], v32 offset:4096\n"
"v_cvt_pk_bf16_f32 v56, v64, v65\ns_waitcnt lgkmcnt(3)\nv_mfma_f32_16x16x32_bf16 v[112:115], v[128:131], v[44:47], 0\nv_cvt_pk_bf16_f32 v57, v66, v67\n"
"v_mfma_f32_16x16x32_bf16 v[116:119], v[132:135], v[44:47], 0\nv_cvt_pk_bf16_f32 v58, v68, v69\nv_mfma_f32_16x16x32_bf16 v[120:123], a[0:3], v[44:47], 0\nv_cvt_pk_bf16_f32 v59, v70, v71\n"
"v_mfma_f32_16x16x32_bf16 v[124:127], a[4:7], v[44:47], 0\nds_read_b128 v[44:47], v33 offset:4096\ns_waitcnt lgkmcnt(3)\nv_mfma_f32_16x16x32_bf16 v[96:99], v[136:139], v[48:51], v[96:99]\n"
"v_mfma_f32_16x16x32_bf16 v[100:103], v[140:143], v[48:51], v[100:103]\nbuffer_store_dwordx4 v[56:59], v15, s[20:23], s50 offen sc1\nv_mfma_f32_16x16x32_bf16 v[104:107], a[8:11], v[48:51], v[104:107]\nv_cvt_pk_bf16_f32 v60, v80, v81\n"
"v_mfma_f32_16x16x32_bf16 v[108:111], a[12:15], v[48:51], v[108:111]\nds_read_b128 v[48:51], v34 offset:4096\nv_cvt_pk_bf16_f32 v61, v82, v83\ns_waitcnt lgkmcnt(3)\n"
"v_mfma_f32_16x16x32_bf16 v[112:115], v[136:139], v[52:55], v[112:115]\nv_cvt_pk_bf16_f32 v62, v84, v85\nv_mfma_f32_16x16x32_bf16 v[116:119], v[140:143], v[52:55], v[116:119]\nv_cvt_pk_bf16_f32 v63, v86, v87\n"
"v_mfma_f32_16x16x32_bf16 v[120:123], a[8:11], v[52:55], v[120:123]\nv_mfma_f32_16x16x32_bf16 v[124:127], a[12:15], v[52:55], v[124:127]\nds_read_b128 v[52:55], v35 offset:4096\ns_waitcnt lgkmcnt(3)\n"
"v_mfma_f32_16x16x32_bf16 v[96:99], v[144:147], v[40:43], v[96:99]\nbuffer_store_dwordx4 v[60:63], v16, s[20:23], s50 offen sc1\nv_mfma_f32_16x16x32_bf16 v[100:103], v[148:151], v[40:43], v[100:103]\nv_cvt_pk_bf16_f32 v56, v72, v73\n"
"v_mfma_f32_16x16x32_bf16 v[104:107], a[16:19], v[40:43], v[104:107]\nv_cvt_pk_bf16_f32 v57, v74, v75\nv_mfma_f32_16x16x32_bf16 v[108:111], a[20:23], v[40:43], v[108:111]\nds_read_b128 v[40:43], v32 offset:8192\n"
"v_cvt_pk_bf16_f32 v58, v76, v77\ns_waitcnt lgkmcnt(3)\nv_mfma_f32_16x16x32_bf16 v[112:115], v[144:147], v[44:47], v[112:115]\nv_cvt_pk_bf16_f32 v59, v78, v79\n"
"v_mfma_f32_16x16x32_bf16 v[116:119], v[148:151], v[44:47], v[116:119]\nv_mfma_f32_16x16x32_bf16 v[120:123], a[16:19], v[44:47], v[120:123]\nv_mfma_f32_16x16x32_bf16 v[124:127], a[20:23], v[44:47], v[124:127]\nds_read_b128 v[44:47], v33 offset:8192\n"
"buffer_store_dwordx4 v[56:59], v17, s[20:23], s50 offen sc1\ns_waitcnt lgkmcnt(3)\nv_mfma_f32_16x16x32_bf16 v[96:99], v[152:155], v[48:51], v[96:99]\nv_cvt_pk_bf16_f32 v60, v88, v89\n"
"v_mfma_f32_16x16x32_bf16 v[100:103], v[156:159], v[48:51], v[100:103]\nv_cvt_pk_bf16_f32 v61, v90, v91\nv_mfma_f32_16x16x32_bf16 v[104:107], a[24:27], v[48:51], v[104:107]\nv_cvt_pk_bf16_f32 v62, v92, v93\n"
"v_mfma_f32_16x16x32_bf16 v[108:111], a[28:31], v[48:51], v[108:111]\nds_read_b128 v[48:51], v34 offset:8192\nv_cvt_pk_bf16_f32 v63, v94, v95\ns_waitcnt lgkmcnt(3)\n"
"v_mfma_f32_16x16x32_bf16 v[112:115], v[152:155], v[52:55], v[112:115]\nv_mfma_f32_16x16x32_bf16 v[116:119], v[156:159], v[52:55], v[116:119]\nv_mfma_f32_16x16x32_bf16 v[120:123], a[24:27], v[52:55], v[120:123]\nbuffer_store_dwordx4 v[60:63], v18, s[20:23], s50 offen sc1\n"
"v_mfma_f32_16x16x32_bf16 v[124:127], a[28:31], v[52:55], v[124:127]\nds_read_b128 v[52:55], v35 offset:8192\ns_add_u32 s50, s50, s36\ns_waitcnt lgkmcnt(3)\n"
"v_mfma_f32_16x16x32_bf16 v[96:99], v[160:163], v[40:43], v[96:99]\ns_add_u32 m0, s52, 0\nv_mfma_f32_16x16x32_bf16 v[100:103], v[164:167], v[40:43], v[100:103]\ns_add_u32 s53, s49, 0\n"
"v_mfma_f32_16x16x32_bf16 v[104:107], a[32:35], v[40:43], v[104:107]\nbuffer_load_dwordx4 v10, s[12:15], s53 offen lds\nv_mfma_f32_16x16x32_bf16 v[108:111], a[36:39], v[40:43], v[108:111]\nds_read_b128 v[40:43], v32 offset:12288\n"
"s_waitcnt lgkmcnt(3)\nv_mfma_f32_16x16x32_bf16 v[112:115], v[160:163], v[44:47], v[112:115]\nv_mfma_f32_16x16x32_bf16 v[116:119], v[164:167], v[44:47], v[116:119]\nv_mfma_f32_16x16x32_bf16 v[120:123], a[32:35], v[44:47], v[120:123]\n"
"v_mfma_f32_16x16x32_bf16 v[124:127], a[36:39], v[44:47], v[124:127]\nds_read_b128 v[44:47], v33 offset:12288\ns_waitcnt lgkmcnt(3)\nv_mfma_f32_16x16x32_bf16 v[96:99], v[168:171], v[48:51], v[96:99]\n"
"v_mfma_f32_16x16x32_bf16 v[100:103], v[172:175], v[48:51], v[100:103]\nv_mfma_f32_16x16x32_bf16 v[104:107], a[40:43], v[48:51], v[104:107]\nv_mfma_f32_16x16x32_bf16 v[108:111], a[44:47], v[48:51], v[108:111]\nds_read_b128 v[48:51], v34 offset:12288\n"
"s_add_u32 m0, s52, 4096\ns_waitcnt lgkmcnt(3)\nv_mfma_f32_16x16x32_bf16 v[112:115], v[168:171], v[52:55], v[112:115]\ns_add_u32 s53, s49, 128\n"
"v_mfma_f32_16x16x32_bf16 v[116:119], v[172:175], v[52:55], v[116:119]\nbuffer_load_dwordx4 v10, s[12:15], s53 offen lds\nv_mfma_f32_16x16x32_bf16 v[120:123], a[40:43], v[52:55], v[120:123]\nv_mfma_f32_16x16x32_bf16 v[124:127], a[44:47], v[52:55], v[124:127]\n"
"ds_read_b128 v[52:55], v35 offset:12288\ns_waitcnt lgkmcnt(3)\nv_mfma_f32_16x16x32_bf16 v[96:99], v[176:179], v[40:43], v[96:99]\nv_mfma_f32_16x16x32_bf16 v[100:103], v[180:183], v[40:43], v[100:103]\n"
"v_mfma_f32_16x16x32_bf16 v[104:107], a[48:51], v[40:43], v[104:107]\nv_mfma_f32_16x16x32_bf16 v[108:111], a[52:55], v[40:43], v[108:111]\nds_read_b128 v[40:43], v32 offset:16384\ns_waitcnt lgkmcnt(3)\n"
"v_mfma_f32_16x16x32_bf16 v[112:115], v[176:179], v[44:47], v[112:115]\nv_mfma_f32_16x16x32_bf16 v[116:119], v[180:183], v[44:47], v[116:119]\nv_mfma_f32_16x16x32_bf16 v[120:123], a[48:51], v[44:47], v[120:123]\ns_add_u32 m0, s52, 8192\n"
"v_mfma_f32_16x16x32_bf16 v[124:127], a[52:55], v[44:47], v[124:127]\nds_read_b128 v[44:47], v33 offset:16384\ns_add_u32 s53, s49, 256\ns_waitcnt lgkmcnt(3)\n"
"v_mfma_f32_16x16x32_bf16 v[96:99], v[184:187], v[48:51], v[96:99]\nbuffer_load_dwordx4 v10, s[12:15], s53 offen lds\nv_mfma_f32_16x16x32_bf16 v[100:103], v[188:191], v[48:51], v[100:103]\nv_mfma_f32_16x16x32_bf16 v[104:107], a[56:59], v[48:51], v[104:107]\n"
"v_mfma_f32_16x16x32_bf16 v[108:111], a[60:63], v[48:51], v[108:111]\nds_read_b128 v[48:51], v34 offset:16384\ns_waitcnt lgkmcnt(3)\nv_mfma_f32_16x16x32_bf16 v[112:115], v[184:187], v[52:55], v[112:115]\n"
"v_mfma_f32_16x16x32_bf16 v[116:119], v[188:191], v[52:55], v[116:119]\nv_mfma_f32_16x16x32_bf16 v[120:123], a[56:59], v[52:55], v[120:123]\nv_mfma_f32_16x16x32_bf16 v[124:127], a[60:63], v[52:55], v[124:127]\nds_read_b128 v[52:55], v35 offset:16384\n"
"s_waitcnt lgkmcnt(3)\nv_mfma_f32_16x16x32_bf16 v[96:99], v[192:195], v[40:43], v[96:99]\nv_mfma_f32_16x16x32_bf16 v[100:103], v[196:199], v[40:43], v[100:103]\ns_add_u32 m0, s52, 12288\n"
"v_mfma_f32_16x16x32_bf16 v[104:107], a[64:67], v[40:43], v[104:107]\ns_add_u32 s53, s49, 384\nv_mfma_f32_16x16x32_bf16 v[108:111], a[68:71], v[40:43], v[108:111]\nds_read_b128 v[40:43], v32 offset:20480\n"
"buffer_load_dwordx4 v10, s[12:15], s53 offen lds\ns_waitcnt lgkmcnt(3)\nv_mfma_f32_16x16x32_bf16 v[112:115], v[192:195], v[44:47], v[112:115]\nv_mfma_f32_16x16x32_bf16 v[116:119], v[196:199], v[44:47], v[116:119]\n"
"v_mfma_f32_16x16x32_bf16 v[120:123], a[64:67], v[44:47], v[120:123]\nv_mfma_f32_16x16x32_bf16 v[124:127], a[68:71], v[44:47], v[124:127]\nds_read_b128 v[44:47], v33 offset:20480\ns_waitcnt lgkmcnt(3)\n"
"v_mfma_f32_16x16x32_bf16 v[96:99], v[200:203], v[48:51], v[96:99]\nv_mfma_f32_16x16x32_bf16 v[100:103], v[204:207], v[48:51], v[100:103]\nv_mfma_f32_16x16x32_bf16 v[104:107], a[72:75], v[48:51], v[104:107]\nv_mfma_f32_16x16x32_bf16 v[108:111], a[76:79], v[48:51], v[108:111]\n"
"ds_read_b128 v[48:51], v34 offset:20480\ns_waitcnt lgkmcnt(3)\nv_mfma_f32_16x16x32_bf16 v[112:115], v[200:203], v[52:55], v[112:115]\ns_add_u32 m0, s52, 16384\n"
"v_mfma_f32_16x16x32_bf16 v[116:119], v[204:207], v[52:55], v[116:119]\ns_add_u32 s53, s49, 512\nv_mfma_f32_16x16x32_bf16 v[120:123], a[72:75], v[52:55], v[120:123]\nbuffer_load_dwordx4 v10, s[12:15], s53 offen lds\n"
"v_mfma_f32_16x16x32_bf16 v[124:127], a[76:79], v[52:55], v[124:127]\nds_read_b128 v[52:55], v35 offset:20480\ns_waitcnt lgkmcnt(3)\nv_mfma_f32_16x16x32_bf16 v[96:99], v[208:211], v[40:43], v[96:99]\n"
"v_mfma_f32_16x16x32_bf16 v[100:103], v[212:215], v[40:43], v[100:103]\nv_mfma_f32_16x16x32_bf16 v[104:107], a[80:83], v[40:43], v[104:107]\nv_mfma_f32_16x16x32_bf16 v[108:111], a[84:87], v[40:43], v[108:111]\nds_read_b128 v[40:43], v32 offset:24576\n"
"s_waitcnt lgkmcnt(3)\nv_mfma_f32_16x16x32_bf16 v[112:115], v[208:211], v[44:47], v[112:115]\nv_mfma_f32_16x16x32_bf16 v[116:119], v[212:215], v[44:47], v[116:119]\nv_mfma_f32_16x16x32_bf16 v[120:123], a[80:83], v[44:47], v[120:123]\n"
"v_mfma_f32_16x16x32_bf16 v[124:127], a[84:87], v[44:47], v[124:127]\nds_read_b128 v[44:47], v33 offset:24576\ns_add_u32 m0, s52, 20480\ns_waitcnt lgkmcnt(3)\n"
"v_mfma_f32_16x16x32_bf16 v[96:99], v[216:219], v[48:51], v[96:99]\ns_add_u32 s54, s51, 32768\ns_cmp_eq_u32 s54, 163840\nv_mfma_f32_16x16x32_bf16 v[100:103], v[220:223], v[48:51], v[100:103]\n"
"v_mfma_f32_16x16x32_bf16 v[104:107], a[88:91], v[48:51], v[104:107]\nv_mfma_f32_16x16x32_bf16 v[108:111], a[92:95], v[48:51], v[108:111]\ns_cselect_b32 s54, 0, s54\ns_sub_u32 s54, s54, s51\n"
"ds_read_b128 v[48:51], v34 offset:24576\ns_add_u32 s53, s49, 640\ns_waitcnt lgkmcnt(3)\nv_mfma_f32_16x16x32_bf16 v[112:115], v[216:219], v[52:55], v[112:115]\n"
"buffer_load_dwordx4 v10, s[12:15], s53 offen lds\nv_mfma_f32_16x16x32_bf16 v[116:119], v[220:223], v[52:55], v[116:119]\nv_mfma_f32_16x16x32_bf16 v[120:123], a[88:91], v[52:55], v[120:123]\nv_mfma_f32_16x16x32_bf16 v[124:127], a[92:95], v[52:55], v[124:127]\n"
"ds_read_b128 v[52:55], v35 offset:24576\ns_waitcnt lgkmcnt(3)\nv_mfma_f32_16x16x32_bf16 v[96:99], v[224:227], v[40:43], v[96:99]\nv_add_u32 v32, s54, v32\n"
"v_mfma_f32_16x16x32_bf16 v[100:103], v[228:231], v[40:43], v[100:103]\nv_mfma_f32_16x16x32_bf16 v[104:107], a[96:99], v[40:43], v[104:107]\nv_mfma_f32_16x16x32_bf16 v[108:111], a[100:103], v[40:43], v[108:111]\nds_read_b128 v[40:43], v32 offset:0\n"
"s_waitcnt lgkmcnt(3)\nv_mfma_f32_16x16x32_bf16 v[112:115], v[224:227], v[44:47], v[112:115]\nv_add_u32 v33, s54, v33\nv_mfma_f32_16x16x32_bf16 v[116:119], v[228:231], v[44:47], v[116:119]\n"
"s_add_u32 m0, s52, 24576\nv_mfma_f32_16x16x32_bf16 v[120:123], a[96:99], v[44:47], v[120:123]\ns_add_u32 s53, s49, 768\nv_mfma_f32_16x16x32_bf16 v[124:127], a[100:103], v[44:47], v[124:127]\n"
"ds_read_b128 v[44:47], v33 offset:0\nbuffer_load_dwordx4 v10, s[12:15], s53 offen lds\ns_waitcnt lgkmcnt(3)\nv_mfma_f32_16x16x32_bf16 v[96:99], v[232:235], v[48:51], v[96:99]\n"
"v_add_u32 v34, s54, v34\nv_mfma_f32_16x16x32_bf16 v[100:103], v[236:239], v[48:51], v[100:103]\nv_mfma_f32_16x16x32_bf16 v[104:107], a[104:107], v[48:51], v[104:107]\nv_mfma_f32_16x16x32_bf16 v[108:111], a[108:111], v[48:51], v[108:111]\n"
"ds_read_b128 v[48:51], v34 offset:0\ns_waitcnt lgkmcnt(3)\nv_mfma_f32_16x16x32_bf16 v[112:115], v[232:235], v[52:55], v[112:115]\nv_add_u32 v35, s54, v35\n"
"v_mfma_f32_16x16x32_bf16 v[116:119], v[236:239], v[52:55], v[116:119]\nv_mfma_f32_16x16x32_bf16 v[120:123], a[104:107], v[52:55], v[120:123]\nv_mfma_f32_16x16x32_bf16 v[124:127], a[108:111], v[52:55], v[124:127]\nds_read_b128 v[52:55], v35 offset:0\n"
"s_mov_b32 s52, s47\ns_add_u32 s47, s51, s32\ns_add_u32 s51, s51, s54\ns_add_u32 s49, s49, 28672\n"
"s_cmp_lt_u32 s49, 1921024\ns_cbranch_scc1 fg0_loop\ns_nop 7\nv_cvt_pk_bf16_f32 v56, v96, v97\n"
"v_cvt_pk_bf16_f32 v57, v98, v99\nv_cvt_pk_bf16_f32 v58, v100, v101\nv_cvt_pk_bf16_f32 v59, v102, v103\ns_nop 0\n"
"s_nop 0\nbuffer_store_dwordx4 v[56:59], v15, s[20:23], s50 offen sc1\nv_cvt_pk_bf16_f32 v60, v112, v113\nv_cvt_pk_bf16_f32 v61, v114, v115\n"
"v_cvt_pk_bf16_f32 v62, v116, v117\nv_cvt_pk_bf16_f32 v63, v118, v119\ns_nop 0\ns_nop 0\n"
"buffer_store_dwordx4 v[60:63], v16, s[20:23], s50 offen sc1\nv_cvt_pk_bf16_f32 v56, v104, v105\nv_cvt_pk_bf16_f32 v57, v106, v107\nv_cvt_pk_bf16_f32 v58, v108, v109\n"
"v_cvt_pk_bf16_f32 v59, v110, v111\ns_nop 0\ns_nop 0\nbuffer_store_dwordx4 v[56:59], v17, s[20:23], s50 offen sc1\n"
"v_cvt_pk_bf16_f32 v60, v120, v121\nv_cvt_pk_bf16_f32 v61, v122, v123\nv_cvt_pk_bf16_f32 v62, v124, v125\nv_cvt_pk_bf16_f32 v63, v126, v127\n"
"s_nop 0\ns_nop 0\nbuffer_store_dwordx4 v[60:63], v18, s[20:23], s50 offen sc1\ns_add_u32 s48, s28, 64\n"
"v_mov_b32 v36, s48\ns_waitcnt vmcnt(0) lgkmcnt(0)\ns_barrier\nbuffer_store_dword v36, v19, s[24:27], s33 offen sc1\n"
"fg0_end:\n"
    :  : "{s[8:23]}"(s0), "{s[24:39]}"(s1), "{v[8:23]}"(v0) : "v24", "v25", "v26", "v27", "v28", "v29", "v30", "v31", "v32", "v33", "v34", "v35", "v36", "v37", "v38", "v39", "v40", "v41", "v42", "v43", "v44", "v45", "v46", "v47", "v48", "v49", "v50", "v51", "v52", "v53", "v54", "v55", "v56", "v57", "v58", "v59", "v60", "v61", "v62", "v63", "v64", "v65", "v66", "v67", "v68", "v69", "v70", "v71", "v72", "v73", "v74", "v75", "v76", "v77", "v78", "v79", "v80", "v81", "v82", "v83", "v84", "v85", "v86", "v87", "v88", "v89", "v90", "v91", "v92", "v93", "v94", "v95", "v96", "v97", "v98", "v99", "v100", "v101", "v102", "v103", "v104", "v105", "v106", "v107", "v108", "v109", "v110", "v111", "v112", "v113", "v114", "v115", "v116", "v117", "v118", "v119", "v120", "v121", "v122", "v123", "v124", "v125", "v126", "v127", "v128", "v129", "v130", "v131", "v132", "v133", "v134", "v135", "v136", "v137", "v138", "v139", "v140", "v141", "v142", "v143", "v144", "v145", "v146", "v147", "v148", "v149", "v150", "v151", "v152", "v153", "v154", "v155", "v156", "v157", "v158", "v159", "v160", "v161", "v162", "v163", "v164", "v165", "v166", "v167", "v168", "v169", "v170", "v171", "v172", "v173", "v174", "v175", "v176", "v177", "v178", "v179", "v180", "v181", "v182", "v183", "v184", "v185", "v186", "v187", "v188", "v189", "v190", "v191", "v192", "v193", "v194", "v195", "v196", "v197", "v198", "v199", "v200", "v201", "v202", "v203", "v204", "v205", "v206", "v207", "v208", "v209", "v210", "v211", "v212", "v213", "v214", "v215", "v216", "v217", "v218", "v219", "v220", "v221", "v222", "v223", "v224", "v225", "v226", "v227", "v228", "v229", "v230", "v231", "v232", "v233", "v234", "v235", "v236", "v237", "v238", "v239", "v240", "v241", "v242", "v243", "a0", "a1", "a2", "a3", "a4", "a5", "a6", "a7", "a8", "a9", "a10", "a11", "a12", "a13", "a14", "a15", "a16", "a17", "a18", "a19", "a20", "a21", "a22", "a23", "a24", "a25", "a26", "a27", "a28", "a29", "a30", "a31", "a32", "a33", "a34", "a35", "a36", "a37", "a38", "a39", "a40", "a41", "a42", "a43", "a44", "a45", "a46", "a47", "a48", "a49", "a50", "a51", "a52", "a53", "a54", "a55", "a56", "a57", "a58", "a59", "a60", "a61", "a62", "a63", "a64", "a65", "a66", "a67", "a68", "a69", "a70", "a71", "a72", "a73", "a74", "a75", "a76", "a77", "a78", "a79", "a80", "a81", "a82", "a83", "a84", "a85", "a86", "a87", "a88", "a89", "a90", "a91", "a92", "a93", "a94", "a95", "a96", "a97", "a98", "a99", "a100", "a101", "a102", "a103", "a104", "a105", "a106", "a107", "a108", "a109", "a110", "a111", "a112", "a113", "a114", "a115", "a116", "a117", "a118", "a119", "a120", "a121", "a122", "a123", "a124", "a125", "a126", "a127", "a128", "a129", "a130", "a131", "a132", "a133", "a134", "a135", "a136", "a137", "a138", "a139", "a140", "a141", "a142", "a143", "a144", "a145", "a146", "a147", "a148", "a149", "a150", "a151", "a152", "a153", "a154", "a155", "a156", "a157", "a158", "a159", "a160", "a161", "a162", "a163", "a164", "a165", "a166", "a167", "a168", "a169", "a170", "a171", "a172", "a173", "a174", "a175", "a176", "a177", "a178", "a179", "a180", "a181", "a182", "a183", "a184", "a185", "a186", "a187", "a188", "a189", "a190", "a191", "s40", "s41", "s42", "s43", "s44", "s45", "s46", "s47", "s48", "s49", "s50", "s51", "s52", "s53", "s54", "s55", "s56", "s57", "s58", "s59", "s60", "s61", "vcc", "scc", "memory");
    return o;
}
DI unsigned gemm_asm1(const GCtx& c) {
    const u16v s0 = {RFL(c.rw1[0]), RFL(c.rw1[1]), RFL(c.rw1[2]), RFL(c.rw1[3]), RFL(c.rx[0]), RFL(c.rx[1]), RFL(c.rx[2]), RFL(c.rx[3]), RFL(c.rexp[0]), RFL(c.rexp[1]), RFL(c.rexp[2]), RFL(c.rexp[3]), RFL(c.rout[0]), RFL(c.rout[1]), RFL(c.rout[2]), RFL(c.rout[3])}, s1 = {RFL(c.rhb[0]), RFL(c.rhb[1]), RFL(c.rhb[2]), RFL(c.rhb[3]), RFL(c.tag), RFL(c.rot), RFL(c.sw1), RFL(c.mw), RFL(c.mx), RFL(c.sfg), RFL(c.stay), RFL(c.csc), RFL(c.ostr), RFL(c.kb), RFL(c.p1), RFL(c.p2)}, v0 = {c.vr0, c.vr1, c.vx, c.wa, c.xa, c.vexp, c.vimp, c.vo0, c.vo1, c.vo2, c.vo3, c.vfl, c.vpf, c.vpo, c.vk, c.p5};
    unsigned o = 0;
    asm volatile(
"s_nop 4\ns_add_u32 s40, s29, 0\ns_add_u32 s41, s29, 1\ns_add_u32 s42, s29, 2\n"
"s_add_u32 s43, s29, 3\nv_xor_b32 v24, 0, v11\nv_xor_b32 v25, 64, v11\nv_xor_b32 v26, 128, v11\n"
"v_xor_b32 v27, 192, v11\nv_xor_b32 v28, 2064, v11\nv_xor_b32 v29, 2128, v11\nv_xor_b32 v30, 2192, v11\n"
"v_xor_b32 v31, 2256, v11\nv_xor_b32 v40, 32, v22\nds_read_b64_tr_b16 a[128:129], v22 offset:0\nds_read_b64_tr_b16 a[130:131], v22 offset:512\n"
"ds_read_b64_tr_b16 a[132:133], v22 offset:4096\nds_read_b64_tr_b16 a[134:135], v22 offset:4608\nds_read_b64_tr_b16 a[136:137], v22 offset:8192\nds_read_b64_tr_b16 a[138:139], v22 offset:8704\n"
"ds_read_b64_tr_b16 a[140:141], v22 offset:12288\nds_read_b64_tr_b16 a[142:143], v22 offset:12800\ns_lshl_b32 s53, s41, 19\ns_add_u32 s53, s53, s30\n"
"s_add_u32 m0, s31, 65536\ns_add_u32 s53, s53, 0\nbuffer_load_dwordx4 v8, s[8:11], s53 offen lds\nbuffer_load_dwordx4 v9, s[8:11], s53 offen offset:1024 lds\n"
"s_waitcnt lgkmcnt(0)\nds_read_b64_tr_b16 a[144:145], v22 offset:16384\nds_read_b64_tr_b16 a[146:147], v22 offset:16896\nds_read_b64_tr_b16 a[148:149], v22 offset:20480\n"
"ds_read_b64_tr_b16 a[150:151], v22 offset:20992\nds_read_b64_tr_b16 a[152:153], v22 offset:24576\nds_read_b64_tr_b16 a[154:155], v22 offset:25088\nds_read_b64_tr_b16 a[156:157], v22 offset:28672\n"
"ds_read_b64_tr_b16 a[158:159], v22 offset:29184\ns_lshl_b32 s53, s41, 19\ns_add_u32 s53, s53, s30\ns_add_u32 m0, s31, 73728\n"
"s_add_u32 s53, s53, 131072\nbuffer_load_dwordx4 v8, s[8:11], s53 offen lds\nbuffer_load_dwordx4 v9, s[8:11], s53 offen offset:1024 lds\ns_waitcnt lgkmcnt(0)\n"
"ds_read_b64_tr_b16 a[160:161], v40 offset:0\nds_read_b64_tr_b16 a[162:163], v40 offset:512\nds_read_b64_tr_b16 a[164:165], v40 offset:4096\nds_read_b64_tr_b16 a[166:167], v40 offset:4608\n"
"ds_read_b64_tr_b16 a[168:169], v40 offset:8192\nds_read_b64_tr_b16 a[170:171], v40 offset:8704\nds_read_b64_tr_b16 a[172:173], v40 offset:12288\nds_read_b64_tr_b16 a[174:175], v40 offset:12800\n"
"s_lshl_b32 s53, s41, 19\ns_add_u32 s53, s53, s30\ns_add_u32 m0, s31, 81920\ns_add_u32 s53, s53, 262144\n"
"buffer_load_dwordx4 v8, s[8:11], s53 offen lds\nbuffer_load_dwordx4 v9, s[8:11], s53 offen offset:1024 lds\ns_waitcnt lgkmcnt(0)\nds_read_b64_tr_b16 a[176:177], v40 offset:16384\n"
"ds_read_b64_tr_b16 a[178:179], v40 offset:16896\nds_read_b64_tr_b16 a[180:181], v40 offset:20480\nds_read_b64_tr_b16 a[182:183], v40 offset:20992\nds_read_b64_tr_b16 a[184:185], v40 offset:24576\n"
"ds_read_b64_tr_b16 a[186:187], v40 offset:25088\nds_read_b64_tr_b16 a[188:189], v40 offset:28672\nds_read_b64_tr_b16 a[190:191], v40 offset:29184\ns_lshl_b32 s53, s41, 19\n"
"s_add_u32 s53, s53, s30\ns_add_u32 m0, s31, 90112\ns_add_u32 s53, s53, 393216\nbuffer_load_dwordx4 v8, s[8:11], s53 offen lds\n"
"buffer_load_dwordx4 v9, s[8:11], s53 offen offset:1024 lds\ns_waitcnt lgkmcnt(0)\ns_waitcnt lgkmcnt(0)\nv_add_u32 v40, 65536, v24\n"
"v_add_u32 v41, 65536, v25\nv_add_u32 v42, 65536, v26\nv_add_u32 v43, 65536, v27\nv_add_u32 v44, 65536, v28\n"
"v_add_u32 v45, 65536, v29\nv_add_u32 v46, 65536, v30\nv_add_u32 v47, 65536, v31\nv_xor_b32 v32, 0, v12\n"
"v_xor_b32 v33, 512, v12\nv_xor_b32 v34, 64, v12\nv_xor_b32 v35, 576, v12\ns_mov_b32 s55, 0\n"
"s_waitcnt vmcnt(8)\ns_barrier\nds_read_b128 v[96:99], v24 offset:0\nds_read_b128 v[100:103], v28 offset:0\n"
"ds_read_b128 v[104:107], v25 offset:0\nds_read_b128 v[108:111], v29 offset:0\nds_read_b128 v[112:115], v26 offset:0\nds_read_b128 v[116:119], v30 offset:0\n"
"ds_read_b128 v[120:123], v27 offset:0\nds_read_b128 v[124:127], v31 offset:0\ns_waitcnt lgkmcnt(6)\nv_mfma_f32_16x16x32_bf16 v[64:67], v[96:99], a[128:131], 0\n"
"s_lshl_b32 s53, s42, 19\ns_add_u32 s53, s53, s30\nv_mfma_f32_16x16x32_bf16 v[68:71], v[96:99], a[160:163], 0\nds_read_b128 v[96:99], v24 offset:256\n"
"s_add_u32 m0, s31, 98304\ns_add_u32 s53, s53, 0\nv_mfma_f32_16x16x32_bf16 v[72:75], v[100:103], a[128:131], 0\nbuffer_load_dwordx4 v8, s[8:11], s53 offen lds\n"
"v_mfma_f32_16x16x32_bf16 v[76:79], v[100:103], a[160:163], 0\nds_read_b128 v[100:103], v28 offset:256\nbuffer_load_dwordx4 v9, s[8:11], s53 offen offset:1024 lds\ns_waitcnt lgkmcnt(6)\n"
"v_mfma_f32_16x16x32_bf16 v[64:67], v[104:107], a[132:135], v[64:67]\ns_lshl_b32 s53, s42, 19\ns_add_u32 s53, s53, s30\nv_mfma_f32_16x16x32_bf16 v[68:71], v[104:107], a[164:167], v[68:71]\n"
"ds_read_b128 v[104:107], v25 offset:256\ns_add_u32 m0, s31, 106496\ns_add_u32 s53, s53, 131072\nv_mfma_f32_16x16x32_bf16 v[72:75], v[108:111], a[132:135], v[72:75]\n"
"buffer_load_dwordx4 v8, s[8:11], s53 offen lds\nv_mfma_f32_16x16x32_bf16 v[76:79], v[108:111], a[164:167], v[76:79]\nds_read_b128 v[108:111], v29 offset:256\nbuffer_load_dwordx4 v9, s[8:11], s53 offen offset:1024 lds\n"
"s_waitcnt lgkmcnt(6)\nv_mfma_f32_16x16x32_bf16 v[64:67], v[112:115], a[136:139], v[64:67]\nv_mfma_f32_16x16x32_bf16 v[68:71], v[112:115], a[168:171], v[68:71]\nds_read_b128 v[112:115], v26 offset:256\n"
"v_mfma_f32_16x16x32_bf16 v[72:75], v[116:119], a[136:139], v[72:75]\nv_mfma_f32_16x16x32_bf16 v[76:79], v[116:119], a[168:171], v[76:79]\nds_read_b128 v[116:119], v30 offset:256\ns_waitcnt lgkmcnt(6)\n"
"v_mfma_f32_16x16x32_bf16 v[64:67], v[120:123], a[140:143], v[64:67]\nv_mfma_f32_16x16x32_bf16 v[68:71], v[120:123], a[172:175], v[68:71]\nds_read_b128 v[120:123], v27 offset:256\nv_mfma_f32_16x16x32_bf16 v[72:75], v[124:127], a[140:143], v[72:75]\n"
"v_mfma_f32_16x16x32_bf16 v[76:79], v[124:127], a[172:175], v[76:79]\nds_read_b128 v[124:127], v31 offset:256\ns_waitcnt lgkmcnt(6)\nv_mfma_f32_16x16x32_bf16 v[64:67], v[96:99], a[144:147], v[64:67]\n"
"v_mfma_f32_16x16x32_bf16 v[68:71], v[96:99], a[176:179], v[68:71]\nds_read_b128 v[96:99], v24 offset:16384\nv_mfma_f32_16x16x32_bf16 v[72:75], v[100:103], a[144:147], v[72:75]\nv_mfma_f32_16x16x32_bf16 v[76:79], v[100:103], a[176:179], v[76:79]\n"
"ds_read_b128 v[100:103], v28 offset:16384\ns_waitcnt lgkmcnt(6)\nv_mfma_f32_16x16x32_bf16 v[64:67], v[104:107], a[148:151], v[64:67]\nv_mfma_f32_16x16x32_bf16 v[68:71], v[104:107], a[180:183], v[68:71]\n"
"ds_read_b128 v[104:107], v25 offset:16384\nv_mfma_f32_16x16x32_bf16 v[72:75], v[108:111], a[148:151], v[72:75]\nv_mfma_f32_16x16x32_bf16 v[76:79], v[108:111], a[180:183], v[76:79]\nds_read_b128 v[108:111], v29 offset:16384\n"
"s_waitcnt lgkmcnt(6)\nv_mfma_f32_16x16x32_bf16 v[64:67], v[112:115], a[152:155], v[64:67]\nv_mfma_f32_16x16x32_bf16 v[68:71], v[112:115], a[184:187], v[68:71]\nds_read_b128 v[112:115], v26 offset:16384\n"
"v_mfma_f32_16x16x32_bf16 v[72:75], v[116:119], a[152:155], v[72:75]\nv_mfma_f32_16x16x32_bf16 v[76:79], v[116:119], a[184:187], v[76:79]\nds_read_b128 v[116:119], v30 offset:16384\ns_waitcnt lgkmcnt(6)\n"
"v_mfma_f32_16x16x32_bf16 v[64:67], v[120:123], a[156:159], v[64:67]\nv_mfma_f32_16x16x32_bf16 v[68:71], v[120:123], a[188:191], v[68:71]\nds_read_b128 v[120:123], v27 offset:16384\nv_mfma_f32_16x16x32_bf16 v[72:75], v[124:127], a[156:159], v[72:75]\n"
"v_mfma_f32_16x16x32_bf16 v[76:79], v[124:127], a[188:191], v[76:79]\nds_read_b128 v[124:127], v31 offset:16384\ns_waitcnt lgkmcnt(6)\nv_mfma_f32_16x16x32_bf16 v[80:83], v[96:99], a[128:131], 0\n"
"s_lshl_b32 s53, s42, 19\ns_add_u32 s53, s53, s30\nv_mfma_f32_16x16x32_bf16 v[84:87], v[96:99], a[160:163], 0\nds_read_b128 v[96:99], v24 offset:16640\n"
"s_add_u32 m0, s31, 114688\ns_add_u32 s53, s53, 262144\nv_mfma_f32_16x16x32_bf16 v[88:91], v[100:103], a[128:131], 0\nbuffer_load_dwordx4 v8, s[8:11], s53 offen lds\n"
"v_mfma_f32_16x16x32_bf16 v[92:95], v[100:103], a[160:163], 0\nds_read_b128 v[100:103], v28 offset:16640\nbuffer_load_dwordx4 v9, s[8:11], s53 offen offset:1024 lds\ns_waitcnt lgkmcnt(6)\n"
"v_mfma_f32_16x16x32_bf16 v[80:83], v[104:107], a[132:135], v[80:83]\nv_cvt_pk_bf16_f32 v128, v64, v65\nv_cvt_pk_bf16_f32 v129, v66, v67\nv_mfma_f32_16x16x32_bf16 v[84:87], v[104:107], a[164:167], v[84:87]\n"
"ds_read_b128 v[104:107], v25 offset:16640\nv_cvt_pk_bf16_f32 v130, v72, v73\nv_cvt_pk_bf16_f32 v131, v74, v75\nv_mfma_f32_16x16x32_bf16 v[88:91], v[108:111], a[132:135], v[88:91]\n"
"v_cvt_pk_bf16_f32 v132, v68, v69\nv_cvt_pk_bf16_f32 v133, v70, v71\nv_mfma_f32_16x16x32_bf16 v[92:95], v[108:111], a[164:167], v[92:95]\nds_read_b128 v[108:111], v29 offset:16640\n"
"v_cvt_pk_bf16_f32 v134, v76, v77\nv_cvt_pk_bf16_f32 v135, v78, v79\ns_waitcnt lgkmcnt(6)\nv_mfma_f32_16x16x32_bf16 v[80:83], v[112:115], a[136:139], v[80:83]\n"
"s_lshl_b32 s54, s40, 11\ns_add_u32 s54, s54, 0\nv_mfma_f32_16x16x32_bf16 v[84:87], v[112:115], a[168:171], v[84:87]\nds_read_b128 v[112:115], v26 offset:16640\n"
"buffer_store_dwordx4 v[128:131], v13, s[16:19], s54 offen sc1\nv_mfma_f32_16x16x32_bf16 v[88:91], v[116:119], a[136:139], v[88:91]\ns_add_u32 s54, s54, 229376\nv_mfma_f32_16x16x32_bf16 v[92:95], v[116:119], a[168:171], v[92:95]\n"
"ds_read_b128 v[116:119], v30 offset:16640\nbuffer_store_dwordx4 v[132:135], v13, s[16:19], s54 offen sc1\ns_waitcnt lgkmcnt(6)\nv_mfma_f32_16x16x32_bf16 v[80:83], v[120:123], a[140:143], v[80:83]\n"
"s_lshl_b32 s53, s42, 19\ns_add_u32 s53, s53, s30\nv_mfma_f32_16x16x32_bf16 v[84:87], v[120:123], a[172:175], v[84:87]\nds_read_b128 v[120:123], v27 offset:16640\n"
"s_add_u32 m0, s31, 122880\ns_add_u32 s53, s53, 393216\nv_mfma_f32_16x16x32_bf16 v[88:91], v[124:127], a[140:143], v[88:91]\nbuffer_load_dwordx4 v8, s[8:11], s53 offen lds\n"
"v_mfma_f32_16x16x32_bf16 v[92:95], v[124:127], a[172:175], v[92:95]\nds_read_b128 v[124:127], v31 offset:16640\nbuffer_load_dwordx4 v9, s[8:11], s53 offen offset:1024 lds\ns_waitcnt vmcnt(10)\n"
"s_barrier\ns_waitcnt lgkmcnt(6)\nv_mfma_f32_16x16x32_bf16 v[80:83], v[96:99], a[144:147], v[80:83]\nv_mfma_f32_16x16x32_bf16 v[84:87], v[96:99], a[176:179], v[84:87]\n"
"ds_read_b128 v[96:99], v24 offset:32768\nv_mfma_f32_16x16x32_bf16 v[88:91], v[100:103], a[144:147], v[88:91]\nv_mfma_f32_16x16x32_bf16 v[92:95], v[100:103], a[176:179], v[92:95]\nds_read_b128 v[100:103], v28 offset:32768\n"
"s_waitcnt lgkmcnt(6)\nv_mfma_f32_16x16x32_bf16 v[80:83], v[104:107], a[148:151], v[80:83]\nv_mfma_f32_16x16x32_bf16 v[84:87], v[104:107], a[180:183], v[84:87]\nds_read_b128 v[104:107], v25 offset:32768\n"
"v_mfma_f32_16x16x32_bf16 v[88:91], v[108:111], a[148:151], v[88:91]\nv_mfma_f32_16x16x32_bf16 v[92:95], v[108:111], a[180:183], v[92:95]\nds_read_b128 v[108:111], v29 offset:32768\ns_waitcnt lgkmcnt(6)\n"
"v_mfma_f32_16x16x32_bf16 v[80:83], v[112:115], a[152:155], v[80:83]\nv_mfma_f32_16x16x32_bf16 v[84:87], v[112:115], a[184:187], v[84:87]\nds_read_b128 v[112:115], v26 offset:32768\nv_mfma_f32_16x16x32_bf16 v[88:91], v[116:119], a[152:155], v[88:91]\n"
"v_mfma_f32_16x16x32_bf16 v[92:95], v[116:119], a[184:187], v[92:95]\nds_read_b128 v[116:119], v30 offset:32768\ns_waitcnt lgkmcnt(6)\nv_mfma_f32_16x16x32_bf16 v[80:83], v[120:123], a[156:159], v[80:83]\n"
"v_mfma_f32_16x16x32_bf16 v[84:87], v[120:123], a[188:191], v[84:87]\nds_read_b128 v[120:123], v27 offset:32768\nv_mfma_f32_16x16x32_bf16 v[88:91], v[124:127], a[156:159], v[88:91]\nv_mfma_f32_16x16x32_bf16 v[92:95], v[124:127], a[188:191], v[92:95]\n"
"ds_read_b128 v[124:127], v31 offset:32768\ns_waitcnt lgkmcnt(6)\nv_mfma_f32_16x16x32_bf16 v[64:67], v[96:99], a[128:131], 0\ns_lshl_b32 s53, s43, 19\n"
"s_add_u32 s53, s53, s30\nv_mfma_f32_16x16x32_bf16 v[68:71], v[96:99], a[160:163], 0\nds_read_b128 v[96:99], v24 offset:33024\ns_add_u32 m0, s31, 131072\n"
"s_add_u32 s53, s53, 0\nv_mfma_f32_16x16x32_bf16 v[72:75], v[100:103], a[128:131], 0\nbuffer_load_dwordx4 v8, s[8:11], s53 offen lds\nv_mfma_f32_16x16x32_bf16 v[76:79], v[100:103], a[160:163], 0\n"
"ds_read_b128 v[100:103], v28 offset:33024\nbuffer_load_dwordx4 v9, s[8:11], s53 offen offset:1024 lds\ns_waitcnt lgkmcnt(6)\nv_mfma_f32_16x16x32_bf16 v[64:67], v[104:107], a[132:135], v[64:67]\n"
"v_cvt_pk_bf16_f32 v136, v80, v81\nv_cvt_pk_bf16_f32 v137, v82, v83\nv_mfma_f32_16x16x32_bf16 v[68:71], v[104:107], a[164:167], v[68:71]\nds_read_b128 v[104:107], v25 offset:33024\n"
"v_cvt_pk_bf16_f32 v138, v88, v89\nv_cvt_pk_bf16_f32 v139, v90, v91\nv_mfma_f32_16x16x32_bf16 v[72:75], v[108:111], a[132:135], v[72:75]\nv_cvt_pk_bf16_f32 v140, v84, v85\n"
"v_cvt_pk_bf16_f32 v141, v86, v87\nv_mfma_f32_16x16x32_bf16 v[76:79], v[108:111], a[164:167], v[76:79]\nds_read_b128 v[108:111], v29 offset:33024\nv_cvt_pk_bf16_f32 v142, v92, v93\n"
"v_cvt_pk_bf16_f32 v143, v94, v95\ns_waitcnt lgkmcnt(6)\nv_mfma_f32_16x16x32_bf16 v[64:67], v[112:115], a[136:139], v[64:67]\ns_lshl_b32 s54, s40, 11\n"
"s_add_u32 s54, s54, 1024\nv_mfma_f32_16x16x32_bf16 v[68:71], v[112:115], a[168:171], v[68:71]\nds_read_b128 v[112:115], v26 offset:33024\nbuffer_store_dwordx4 v[136:139], v13, s[16:19], s54 offen sc1\n"
"v_mfma_f32_16x16x32_bf16 v[72:75], v[116:119], a[136:139], v[72:75]\ns_add_u32 s54, s54, 229376\nv_mfma_f32_16x16x32_bf16 v[76:79], v[116:119], a[168:171], v[76:79]\nds_read_b128 v[116:119], v30 offset:33024\n"
"buffer_store_dwordx4 v[140:143], v13, s[16:19], s54 offen sc1\ns_waitcnt lgkmcnt(6)\nv_mfma_f32_16x16x32_bf16 v[64:67], v[120:123], a[140:143], v[64:67]\ns_lshl_b32 s53, s43, 19\n"
"s_add_u32 s53, s53, s30\nv_mfma_f32_16x16x32_bf16 v[68:71], v[120:123], a[172:175], v[68:71]\nds_read_b128 v[120:123], v27 offset:33024\ns_add_u32 m0, s31, 139264\n"
"s_add_u32 s53, s53, 131072\nv_mfma_f32_16x16x32_bf16 v[72:75], v[124:127], a[140:143], v[72:75]\nbuffer_load_dwordx4 v8, s[8:11], s53 offen lds\nv_mfma_f32_16x16x32_bf16 v[76:79], v[124:127], a[172:175], v[76:79]\n"
"ds_read_b128 v[124:127], v31 offset:33024\nbuffer_load_dwordx4 v9, s[8:11], s53 offen offset:1024 lds\ns_waitcnt lgkmcnt(6)\nv_mfma_f32_16x16x32_bf16 v[64:67], v[96:99], a[144:147], v[64:67]\n"
"v_mfma_f32_16x16x32_bf16 v[68:71], v[96:99], a[176:179], v[68:71]\nds_read_b128 v[96:99], v24 offset:49152\nv_mfma_f32_16x16x32_bf16 v[72:75], v[100:103], a[144:147], v[72:75]\nv_mfma_f32_16x16x32_bf16 v[76:79], v[100:103], a[176:179], v[76:79]\n"
"ds_read_b128 v[100:103], v28 offset:49152\ns_waitcnt lgkmcnt(6)\nv_mfma_f32_16x16x32_bf16 v[64:67], v[104:107], a[148:151], v[64:67]\nv_mfma_f32_16x16x32_bf16 v[68:71], v[104:107], a[180:183], v[68:71]\n"
"ds_read_b128 v[104:107], v25 offset:49152\nv_mfma_f32_16x16x32_bf16 v[72:75], v[108:111], a[148:151], v[72:75]\nv_mfma_f32_16x16x32_bf16 v[76:79], v[108:111], a[180:183], v[76:79]\nds_read_b128 v[108:111], v29 offset:49152\n"
"s_waitcnt lgkmcnt(6)\nv_mfma_f32_16x16x32_bf16 v[64:67], v[112:115], a[152:155], v[64:67]\nv_mfma_f32_16x16x32_bf16 v[68:71], v[112:115], a[184:187], v[68:71]\nds_read_b128 v[112:115], v26 offset:49152\n"
"v_mfma_f32_16x16x32_bf16 v[72:75], v[116:119], a[152:155], v[72:75]\nv_mfma_f32_16x16x32_bf16 v[76:79], v[116:119], a[184:187], v[76:79]\nds_read_b128 v[116:119], v30 offset:49152\ns_waitcnt lgkmcnt(6)\n"
"v_mfma_f32_16x16x32_bf16 v[64:67], v[120:123], a[156:159], v[64:67]\nv_mfma_f32_16x16x32_bf16 v[68:71], v[120:123], a[188:191], v[68:71]\nds_read_b128 v[120:123], v27 offset:49152\nv_mfma_f32_16x16x32_bf16 v[72:75], v[124:127], a[156:159], v[72:75]\n"
"v_mfma_f32_16x16x32_bf16 v[76:79], v[124:127], a[188:191], v[76:79]\nds_read_b128 v[124:127], v31 offset:49152\ns_waitcnt lgkmcnt(6)\nv_mfma_f32_16x16x32_bf16 v[80:83], v[96:99], a[128:131], 0\n"
"s_lshl_b32 s53, s43, 19\ns_add_u32 s53, s53, s30\nv_mfma_f32_16x16x32_bf16 v[84:87], v[96:99], a[160:163], 0\nds_read_b128 v[96:99], v24 offset:49408\n"
"s_add_u32 m0, s31, 147456\ns_add_u32 s53, s53, 262144\nv_mfma_f32_16x16x32_bf16 v[88:91], v[100:103], a[128:131], 0\nbuffer_load_dwordx4 v8, s[8:11], s53 offen lds\n"
"v_mfma_f32_16x16x32_bf16 v[92:95], v[100:103], a[160:163], 0\nds_read_b128 v[100:103], v28 offset:49408\nbuffer_load_dwordx4 v9, s[8:11], s53 offen offset:1024 lds\ns_waitcnt lgkmcnt(6)\n"
"v_mfma_f32_16x16x32_bf16 v[80:83], v[104:107], a[132:135], v[80:83]\nv_cvt_pk_bf16_f32 v144, v64, v65\nv_cvt_pk_bf16_f32 v145, v66, v67\nv_mfma_f32_16x16x32_bf16 v[84:87], v[104:107], a[164:167], v[84:87]\n"
"ds_read_b128 v[104:107], v25 offset:49408\nv_cvt_pk_bf16_f32 v146, v72, v73\nv_cvt_pk_bf16_f32 v147, v74, v75\nv_mfma_f32_16x16x32_bf16 v[88:91], v[108:111], a[132:135], v[88:91]\n"
"v_cvt_pk_bf16_f32 v148, v68, v69\nv_cvt_pk_bf16_f32 v149, v70, v71\nv_mfma_f32_16x16x32_bf16 v[92:95], v[108:111], a[164:167], v[92:95]\nds_read_b128 v[108:111], v29 offset:49408\n"
"v_cvt_pk_bf16_f32 v150, v76, v77\nv_cvt_pk_bf16_f32 v151, v78, v79\ns_waitcnt lgkmcnt(6)\nv_mfma_f32_16x16x32_bf16 v[80:83], v[112:115], a[136:139], v[80:83]\n"
"s_lshl_b32 s54, s41, 11\ns_add_u32 s54, s54, 0\nv_mfma_f32_16x16x32_bf16 v[84:87], v[112:115], a[168:171], v[84:87]\nds_read_b128 v[112:115], v26 offset:49408\n"
"buffer_store_dwordx4 v[144:147], v13, s[16:19], s54 offen sc1\nv_mfma_f32_16x16x32_bf16 v[88:91], v[116:119], a[136:139], v[88:91]\ns_add_u32 s54, s54, 229376\nv_mfma_f32_16x16x32_bf16 v[92:95], v[116:119], a[168:171], v[92:95]\n"
"ds_read_b128 v[116:119], v30 offset:49408\nbuffer_store_dwordx4 v[148:151], v13, s[16:19], s54 offen sc1\ns_waitcnt lgkmcnt(6)\nv_mfma_f32_16x16x32_bf16 v[80:83], v[120:123], a[140:143], v[80:83]\n"
"s_lshl_b32 s53, s43, 19\ns_add_u32 s53, s53, s30\nv_mfma_f32_16x16x32_bf16 v[84:87], v[120:123], a[172:175], v[84:87]\nds_read_b128 v[120:123], v27 offset:49408\n"
"s_add_u32 m0, s31, 155648\ns_add_u32 s53, s53, 393216\nv_mfma_f32_16x16x32_bf16 v[88:91], v[124:127], a[140:143], v[88:91]\nbuffer_load_dwordx4 v8, s[8:11], s53 offen lds\n"
"v_mfma_f32_16x16x32_bf16 v[92:95], v[124:127], a[172:175], v[92:95]\nds_read_b128 v[124:127], v31 offset:49408\nbuffer_load_dwordx4 v9, s[8:11], s53 offen offset:1024 lds\ns_waitcnt vmcnt(12)\n"
"s_barrier\ns_waitcnt lgkmcnt(6)\nv_mfma_f32_16x16x32_bf16 v[80:83], v[96:99], a[144:147], v[80:83]\nv_mfma_f32_16x16x32_bf16 v[84:87], v[96:99], a[176:179], v[84:87]\n"
"ds_read_b128 v[96:99], v40 offset:0\nv_mfma_f32_16x16x32_bf16 v[88:91], v[100:103], a[144:147], v[88:91]\nv_mfma_f32_16x16x32_bf16 v[92:95], v[100:103], a[176:179], v[92:95]\nds_read_b128 v[100:103], v44 offset:0\n"
"s_waitcnt lgkmcnt(6)\nv_mfma_f32_16x16x32_bf16 v[80:83], v[104:107], a[148:151], v[80:83]\nv_mfma_f32_16x16x32_bf16 v[84:87], v[104:107], a[180:183], v[84:87]\nds_read_b128 v[104:107], v41 offset:0\n"
"v_mfma_f32_16x16x32_bf16 v[88:91], v[108:111], a[148:151], v[88:91]\nv_mfma_f32_16x16x32_bf16 v[92:95], v[108:111], a[180:183], v[92:95]\nds_read_b128 v[108:111], v45 offset:0\ns_waitcnt lgkmcnt(6)\n"
"v_mfma_f32_16x16x32_bf16 v[80:83], v[112:115], a[152:155], v[80:83]\nv_mfma_f32_16x16x32_bf16 v[84:87], v[112:115], a[184:187], v[84:87]\nds_read_b128 v[112:115], v42 offset:0\nv_mfma_f32_16x16x32_bf16 v[88:91], v[116:119], a[152:155], v[88:91]\n"
"v_mfma_f32_16x16x32_bf16 v[92:95], v[116:119], a[184:187], v[92:95]\nds_read_b128 v[116:119], v46 offset:0\ns_waitcnt lgkmcnt(6)\nv_mfma_f32_16x16x32_bf16 v[80:83], v[120:123], a[156:159], v[80:83]\n"
"v_mfma_f32_16x16x32_bf16 v[84:87], v[120:123], a[188:191], v[84:87]\nds_read_b128 v[120:123], v43 offset:0\nv_mfma_f32_16x16x32_bf16 v[88:91], v[124:127], a[156:159], v[88:91]\nv_mfma_f32_16x16x32_bf16 v[92:95], v[124:127], a[188:191], v[92:95]\n"
"ds_read_b128 v[124:127], v47 offset:0\ns_waitcnt lgkmcnt(6)\nv_mfma_f32_16x16x32_bf16 v[64:67], v[96:99], a[128:131], 0\ns_add_u32 m0, s32, 0\n"
"v_mfma_f32_16x16x32_bf16 v[68:71], v[96:99], a[160:163], 0\nds_read_b128 v[96:99], v40 offset:256\ns_mov_b32 s53, 0\nv_mfma_f32_16x16x32_bf16 v[72:75], v[100:103], a[128:131], 0\n"
"buffer_load_dwordx4 v10, s[12:15], s53 offen lds\nv_mfma_f32_16x16x32_bf16 v[76:79], v[100:103], a[160:163], 0\nds_read_b128 v[100:103], v44 offset:256\nv_cvt_pk_bf16_f32 v152, v80, v81\n"
"v_cvt_pk_bf16_f32 v153, v82, v83\ns_waitcnt lgkmcnt(6)\nv_mfma_f32_16x16x32_bf16 v[64:67], v[104:107], a[132:135], v[64:67]\nv_cvt_pk_bf16_f32 v154, v88, v89\n"
"v_cvt_pk_bf16_f32 v155, v90, v91\nv_mfma_f32_16x16x32_bf16 v[68:71], v[104:107], a[164:167], v[68:71]\nds_read_b128 v[104:107], v41 offset:256\nv_cvt_pk_bf16_f32 v156, v84, v85\n"
"v_cvt_pk_bf16_f32 v157, v86, v87\nv_mfma_f32_16x16x32_bf16 v[72:75], v[108:111], a[132:135], v[72:75]\nv_cvt_pk_bf16_f32 v158, v92, v93\nv_cvt_pk_bf16_f32 v159, v94, v95\n"
"v_mfma_f32_16x16x32_bf16 v[76:79], v[108:111], a[164:167], v[76:79]\nds_read_b128 v[108:111], v45 offset:256\ns_lshl_b32 s54, s41, 11\ns_add_u32 s54, s54, 1024\n"
"s_waitcnt lgkmcnt(6)\nv_mfma_f32_16x16x32_bf16 v[64:67], v[112:115], a[136:139], v[64:67]\nbuffer_store_dwordx4 v[152:155], v13, s[16:19], s54 offen sc1\nv_mfma_f32_16x16x32_bf16 v[68:71], v[112:115], a[168:171], v[68:71]\n"
"ds_read_b128 v[112:115], v42 offset:256\ns_add_u32 s54, s54, 229376\nv_mfma_f32_16x16x32_bf16 v[72:75], v[116:119], a[136:139], v[72:75]\nbuffer_store_dwordx4 v[156:159], v13, s[16:19], s54 offen sc1\n"
"v_mfma_f32_16x16x32_bf16 v[76:79], v[116:119], a[168:171], v[76:79]\nds_read_b128 v[116:119], v46 offset:256\ns_add_u32 m0, s32, 4096\ns_waitcnt lgkmcnt(6)\n"
"v_mfma_f32_16x16x32_bf16 v[64:67], v[120:123], a[140:143], v[64:67]\ns_mov_b32 s53, 128\nv_mfma_f32_16x16x32_bf16 v[68:71], v[120:123], a[172:175], v[68:71]\nds_read_b128 v[120:123], v43 offset:256\n"
"buffer_load_dwordx4 v10, s[12:15], s53 offen lds\nv_mfma_f32_16x16x32_bf16 v[72:75], v[124:127], a[140:143], v[72:75]\ns_add_u32 m0, s32, 8192\nv_mfma_f32_16x16x32_bf16 v[76:79], v[124:127], a[172:175], v[76:79]\n"
"ds_read_b128 v[124:127], v47 offset:256\ns_mov_b32 s53, 256\ns_waitcnt lgkmcnt(6)\nv_mfma_f32_16x16x32_bf16 v[64:67], v[96:99], a[144:147], v[64:67]\n"
"buffer_load_dwordx4 v10, s[12:15], s53 offen lds\nv_mfma_f32_16x16x32_bf16 v[68:71], v[96:99], a[176:179], v[68:71]\nds_read_b128 v[96:99], v40 offset:16384\ns_add_u32 m0, s32, 12288\n"
"v_mfma_f32_16x16x32_bf16 v[72:75], v[100:103], a[144:147], v[72:75]\ns_mov_b32 s53, 384\nv_mfma_f32_16x16x32_bf16 v[76:79], v[100:103], a[176:179], v[76:79]\nds_read_b128 v[100:103], v44 offset:16384\n"
"buffer_load_dwordx4 v10, s[12:15], s53 offen lds\ns_waitcnt lgkmcnt(6)\nv_mfma_f32_16x16x32_bf16 v[64:67], v[104:107], a[148:151], v[64:67]\nv_mfma_f32_16x16x32_bf16 v[68:71], v[104:107], a[180:183], v[68:71]\n"
"ds_read_b128 v[104:107], v41 offset:16384\nv_mfma_f32_16x16x32_bf16 v[72:75], v[108:111], a[148:151], v[72:75]\nv_mfma_f32_16x16x32_bf16 v[76:79], v[108:111], a[180:183], v[76:79]\nds_read_b128 v[108:111], v45 offset:16384\n"
"s_waitcnt lgkmcnt(6)\nv_mfma_f32_16x16x32_bf16 v[64:67], v[112:115], a[152:155], v[64:67]\nv_mfma_f32_16x16x32_bf16 v[68:71], v[112:115], a[184:187], v[68:71]\nds_read_b128 v[112:115], v42 offset:16384\n"
"v_mfma_f32_16x16x32_bf16 v[72:75], v[116:119], a[152:155], v[72:75]\nv_mfma_f32_16x16x32_bf16 v[76:79], v[116:119], a[184:187], v[76:79]\nds_read_b128 v[116:119], v46 offset:16384\ns_waitcnt lgkmcnt(6)\n"
"v_mfma_f32_16x16x32_bf16 v[64:67], v[120:123], a[156:159], v[64:67]\nv_mfma_f32_16x16x32_bf16 v[68:71], v[120:123], a[188:191], v[68:71]\nds_read_b128 v[120:123], v43 offset:16384\nv_mfma_f32_16x16x32_bf16 v[72:75], v[124:127], a[156:159], v[72:75]\n"
"v_mfma_f32_16x16x32_bf16 v[76:79], v[124:127], a[188:191], v[76:79]\nds_read_b128 v[124:127], v47 offset:16384\ns_waitcnt lgkmcnt(6)\nv_mfma_f32_16x16x32_bf16 v[80:83], v[96:99], a[128:131], 0\n"
"s_add_u32 m0, s32, 16384\nv_mfma_f32_16x16x32_bf16 v[84:87], v[96:99], a[160:163], 0\nds_read_b128 v[96:99], v40 offset:16640\ns_mov_b32 s53, 512\n"
"v_mfma_f32_16x16x32_bf16 v[88:91], v[100:103], a[128:131], 0\nbuffer_load_dwordx4 v10, s[12:15], s53 offen lds\nv_mfma_f32_16x16x32_bf16 v[92:95], v[100:103], a[160:163], 0\nds_read_b128 v[100:103], v44 offset:16640\n"
"v_cvt_pk_bf16_f32 v160, v64, v65\nv_cvt_pk_bf16_f32 v161, v66, v67\ns_waitcnt lgkmcnt(6)\nv_mfma_f32_16x16x32_bf16 v[80:83], v[104:107], a[132:135], v[80:83]\n"
"v_cvt_pk_bf16_f32 v162, v72, v73\nv_cvt_pk_bf16_f32 v163, v74, v75\nv_mfma_f32_16x16x32_bf16 v[84:87], v[104:107], a[164:167], v[84:87]\nds_read_b128 v[104:107], v41 offset:16640\n"
"v_cvt_pk_bf16_f32 v164, v68, v69\nv_cvt_pk_bf16_f32 v165, v70, v71\nv_mfma_f32_16x16x32_bf16 v[88:91], v[108:111], a[132:135], v[88:91]\nv_cvt_pk_bf16_f32 v166, v76, v77\n"
"v_cvt_pk_bf16_f32 v167, v78, v79\nv_mfma_f32_16x16x32_bf16 v[92:95], v[108:111], a[164:167], v[92:95]\nds_read_b128 v[108:111], v45 offset:16640\ns_lshl_b32 s54, s42, 11\n"
"s_add_u32 s54, s54, 0\ns_waitcnt lgkmcnt(6)\nv_mfma_f32_16x16x32_bf16 v[80:83], v[112:115], a[136:139], v[80:83]\nbuffer_store_dwordx4 v[160:163], v13, s[16:19], s54 offen sc1\n"
"v_mfma_f32_16x16x32_bf16 v[84:87], v[112:115], a[168:171], v[84:87]\nds_read_b128 v[112:115], v42 offset:16640\ns_add_u32 s54, s54, 229376\nv_mfma_f32_16x16x32_bf16 v[88:91], v[116:119], a[136:139], v[88:91]\n"
"buffer_store_dwordx4 v[164:167], v13, s[16:19], s54 offen sc1\nv_mfma_f32_16x16x32_bf16 v[92:95], v[116:119], a[168:171], v[92:95]\nds_read_b128 v[116:119], v46 offset:16640\ns_add_u32 m0, s32, 20480\n"
"s_waitcnt lgkmcnt(6)\nv_mfma_f32_16x16x32_bf16 v[80:83], v[120:123], a[140:143], v[80:83]\ns_mov_b32 s53, 640\nv_mfma_f32_16x16x32_bf16 v[84:87], v[120:123], a[172:175], v[84:87]\n"
"ds_read_b128 v[120:123], v43 offset:16640\nbuffer_load_dwordx4 v10, s[12:15], s53 offen lds\nv_mfma_f32_16x16x32_bf16 v[88:91], v[124:127], a[140:143], v[88:91]\ns_add_u32 m0, s32, 24576\n"
"v_mfma_f32_16x16x32_bf16 v[92:95], v[124:127], a[172:175], v[92:95]\nds_read_b128 v[124:127], v47 offset:16640\ns_mov_b32 s53, 768\ns_waitcnt vmcnt(10)\n"
"s_barrier\ns_waitcnt lgkmcnt(6)\nv_mfma_f32_16x16x32_bf16 v[80:83], v[96:99], a[144:147], v[80:83]\nbuffer_load_dwordx4 v10, s[12:15], s53 offen lds\n"
"v_mfma_f32_16x16x32_bf16 v[84:87], v[96:99], a[176:179], v[84:87]\nds_read_b128 v[96:99], v40 offset:32768\nv_mfma_f32_16x16x32_bf16 v[88:91], v[100:103], a[144:147], v[88:91]\nv_mfma_f32_16x16x32_bf16 v[92:95], v[100:103], a[176:179], v[92:95]\n"
"ds_read_b128 v[100:103], v44 offset:32768\ns_waitcnt lgkmcnt(6)\nv_mfma_f32_16x16x32_bf16 v[80:83], v[104:107], a[148:151], v[80:83]\nv_mfma_f32_16x16x32_bf16 v[84:87], v[104:107], a[180:183], v[84:87]\n"
"ds_read_b128 v[104:107], v41 offset:32768\nv_mfma_f32_16x16x32_bf16 v[88:91], v[108:111], a[148:151], v[88:91]\nv_mfma_f32_16x16x32_bf16 v[92:95], v[108:111], a[180:183], v[92:95]\nds_read_b128 v[108:111], v45 offset:32768\n"
"s_waitcnt lgkmcnt(6)\nv_mfma_f32_16x16x32_bf16 v[80:83], v[112:115], a[152:155], v[80:83]\nv_mfma_f32_16x16x32_bf16 v[84:87], v[112:115], a[184:187], v[84:87]\nds_read_b128 v[112:115], v42 offset:32768\n"
"v_mfma_f32_16x16x32_bf16 v[88:91], v[116:119], a[152:155], v[88:91]\nv_mfma_f32_16x16x32_bf16 v[92:95], v[116:119], a[184:187], v[92:95]\nds_read_b128 v[116:119], v46 offset:32768\ns_waitcnt lgkmcnt(6)\n"
"v_mfma_f32_16x16x32_bf16 v[80:83], v[120:123], a[156:159], v[80:83]\nv_mfma_f32_16x16x32_bf16 v[84:87], v[120:123], a[188:191], v[84:87]\nds_read_b128 v[120:123], v43 offset:32768\nv_mfma_f32_16x16x32_bf16 v[88:91], v[124:127], a[156:159], v[88:91]\n"
"v_mfma_f32_16x16x32_bf16 v[92:95], v[124:127], a[188:191], v[92:95]\nds_read_b128 v[124:127], v47 offset:32768\ns_cmp_lg_u32 s34, 2\ns_cbranch_scc1 fg1_main\n"
"s_nop 7\nv_cvt_pk_bf16_f32 v168, v80, v81\nv_cvt_pk_bf16_f32 v169, v82, v83\nv_cvt_pk_bf16_f32 v170, v88, v89\n"
"v_cvt_pk_bf16_f32 v171, v90, v91\nv_cvt_pk_bf16_f32 v172, v84, v85\nv_cvt_pk_bf16_f32 v173, v86, v87\nv_cvt_pk_bf16_f32 v174, v92, v93\n"
"v_cvt_pk_bf16_f32 v175, v94, v95\ns_lshl_b32 s54, s42, 11\ns_add_u32 s54, s54, 1024\nbuffer_store_dwordx4 v[168:171], v13, s[16:19], s54 offen sc1\n"
"s_add_u32 s54, s54, 229376\nbuffer_store_dwordx4 v[172:175], v13, s[16:19], s54 offen sc1\nv_mov_b32 v36, s28\ns_waitcnt vmcnt(0) lgkmcnt(0)\n"
"s_barrier\nbuffer_store_dword v36, v19, s[24:27], 0 offen sc1\ns_branch fg1_end\nfg1_main:\n"
"s_waitcnt lgkmcnt(6)\nv_mfma_f32_16x16x32_bf16 v[64:67], v[96:99], a[128:131], 0\ns_add_u32 m0, s32, 32768\nv_mfma_f32_16x16x32_bf16 v[68:71], v[96:99], a[160:163], 0\n"
"ds_read_b128 v[96:99], v40 offset:33024\ns_mov_b32 s53, 28672\nv_mfma_f32_16x16x32_bf16 v[72:75], v[100:103], a[128:131], 0\nbuffer_load_dwordx4 v10, s[12:15], s53 offen lds\n"
"v_mfma_f32_16x16x32_bf16 v[76:79], v[100:103], a[160:163], 0\nds_read_b128 v[100:103], v44 offset:33024\nv_cvt_pk_bf16_f32 v168, v80, v81\nv_cvt_pk_bf16_f32 v169, v82, v83\n"
"s_waitcnt lgkmcnt(6)\nv_mfma_f32_16x16x32_bf16 v[64:67], v[104:107], a[132:135], v[64:67]\nv_cvt_pk_bf16_f32 v170, v88, v89\nv_cvt_pk_bf16_f32 v171, v90, v91\n"
"v_mfma_f32_16x16x32_bf16 v[68:71], v[104:107], a[164:167], v[68:71]\nds_read_b128 v[104:107], v41 offset:33024\nv_cvt_pk_bf16_f32 v172, v84, v85\nv_cvt_pk_bf16_f32 v173, v86, v87\n"
"v_mfma_f32_16x16x32_bf16 v[72:75], v[108:111], a[132:135], v[72:75]\nv_cvt_pk_bf16_f32 v174, v92, v93\nv_cvt_pk_bf16_f32 v175, v94, v95\nv_mfma_f32_16x16x32_bf16 v[76:79], v[108:111], a[164:167], v[76:79]\n"
"ds_read_b128 v[108:111], v45 offset:33024\ns_lshl_b32 s54, s42, 11\ns_add_u32 s54, s54, 1024\ns_waitcnt lgkmcnt(6)\n"
"v_mfma_f32_16x16x32_bf16 v[64:67], v[112:115], a[136:139], v[64:67]\nbuffer_store_dwordx4 v[168:171], v13, s[16:19], s54 offen sc1\nv_mfma_f32_16x16x32_bf16 v[68:71], v[112:115], a[168:171], v[68:71]\nds_read_b128 v[112:115], v42 offset:33024\n"
"s_add_u32 s54, s54, 229376\nv_mfma_f32_16x16x32_bf16 v[72:75], v[116:119], a[136:139], v[72:75]\nbuffer_store_dwordx4 v[172:175], v13, s[16:19], s54 offen sc1\nv_mfma_f32_16x16x32_bf16 v[76:79], v[116:119], a[168:171], v[76:79]\n"
"ds_read_b128 v[116:119], v46 offset:33024\ns_add_u32 m0, s32, 36864\ns_waitcnt lgkmcnt(6)\nv_mfma_f32_16x16x32_bf16 v[64:67], v[120:123], a[140:143], v[64:67]\n"
"s_mov_b32 s53, 28800\nv_mfma_f32_16x16x32_bf16 v[68:71], v[120:123], a[172:175], v[68:71]\nds_read_b128 v[120:123], v43 offset:33024\nbuffer_load_dwordx4 v10, s[12:15], s53 offen lds\n"
"v_mfma_f32_16x16x32_bf16 v[72:75], v[124:127], a[140:143], v[72:75]\ns_add_u32 m0, s32, 40960\nv_mfma_f32_16x16x32_bf16 v[76:79], v[124:127], a[172:175], v[76:79]\nds_read_b128 v[124:127], v47 offset:33024\n"
"s_mov_b32 s53, 28928\ns_waitcnt lgkmcnt(6)\nv_mfma_f32_16x16x32_bf16 v[64:67], v[96:99], a[144:147], v[64:67]\nbuffer_load_dwordx4 v10, s[12:15], s53 offen lds\n"
"v_mfma_f32_16x16x32_bf16 v[68:71], v[96:99], a[176:179], v[68:71]\nds_read_b128 v[96:99], v40 offset:49152\ns_add_u32 m0, s32, 45056\nv_mfma_f32_16x16x32_bf16 v[72:75], v[100:103], a[144:147], v[72:75]\n"
"s_mov_b32 s53, 29056\nv_mfma_f32_16x16x32_bf16 v[76:79], v[100:103], a[176:179], v[76:79]\nds_read_b128 v[100:103], v44 offset:49152\nbuffer_load_dwordx4 v10, s[12:15], s53 offen lds\n"
"s_waitcnt lgkmcnt(6)\nv_mfma_f32_16x16x32_bf16 v[64:67], v[104:107], a[148:151], v[64:67]\nv_mfma_f32_16x16x32_bf16 v[68:71], v[104:107], a[180:183], v[68:71]\nds_read_b128 v[104:107], v41 offset:49152\n"
"v_mfma_f32_16x16x32_bf16 v[72:75], v[108:111], a[148:151], v[72:75]\nv_mfma_f32_16x16x32_bf16 v[76:79], v[108:111], a[180:183], v[76:79]\nds_read_b128 v[108:111], v45 offset:49152\ns_waitcnt lgkmcnt(6)\n"
"v_mfma_f32_16x16x32_bf16 v[64:67], v[112:115], a[152:155], v[64:67]\nv_mfma_f32_16x16x32_bf16 v[68:71], v[112:115], a[184:187], v[68:71]\nds_read_b128 v[112:115], v42 offset:49152\nv_mfma_f32_16x16x32_bf16 v[72:75], v[116:119], a[152:155], v[72:75]\n"
"v_mfma_f32_16x16x32_bf16 v[76:79], v[116:119], a[184:187], v[76:79]\nds_read_b128 v[116:119], v46 offset:49152\ns_waitcnt lgkmcnt(6)\nv_mfma_f32_16x16x32_bf16 v[64:67], v[120:123], a[156:159], v[64:67]\n"
"v_mfma_f32_16x16x32_bf16 v[68:71], v[120:123], a[188:191], v[68:71]\nds_read_b128 v[120:123], v43 offset:49152\nv_mfma_f32_16x16x32_bf16 v[72:75], v[124:127], a[156:159], v[72:75]\nv_mfma_f32_16x16x32_bf16 v[76:79], v[124:127], a[188:191], v[76:79]\n"
"ds_read_b128 v[124:127], v47 offset:49152\ns_waitcnt lgkmcnt(6)\nv_mfma_f32_16x16x32_bf16 v[80:83], v[96:99], a[128:131], 0\ns_add_u32 m0, s32, 49152\n"
"v_mfma_f32_16x16x32_bf16 v[84:87], v[96:99], a[160:163], 0\nds_read_b128 v[96:99], v40 offset:49408\ns_mov_b32 s53, 29184\nv_mfma_f32_16x16x32_bf16 v[88:91], v[100:103], a[128:131], 0\n"
"buffer_load_dwordx4 v10, s[12:15], s53 offen lds\nv_mfma_f32_16x16x32_bf16 v[92:95], v[100:103], a[160:163], 0\nds_read_b128 v[100:103], v44 offset:49408\nv_cvt_pk_bf16_f32 v176, v64, v65\n"
"v_cvt_pk_bf16_f32 v177, v66, v67\ns_waitcnt lgkmcnt(6)\nv_mfma_f32_16x16x32_bf16 v[80:83], v[104:107], a[132:135], v[80:83]\nv_cvt_pk_bf16_f32 v178, v72, v73\n"
"v_cvt_pk_bf16_f32 v179, v74, v75\nv_mfma_f32_16x16x32_bf16 v[84:87], v[104:107], a[164:167], v[84:87]\nds_read_b128 v[104:107], v41 offset:49408\nv_cvt_pk_bf16_f32 v180, v68, v69\n"
"v_cvt_pk_bf16_f32 v181, v70, v71\nv_mfma_f32_16x16x32_bf16 v[88:91], v[108:111], a[132:135], v[88:91]\nv_cvt_pk_bf16_f32 v182, v76, v77\nv_cvt_pk_bf16_f32 v183, v78, v79\n"
"v_mfma_f32_16x16x32_bf16 v[92:95], v[108:111], a[164:167], v[92:95]\nds_read_b128 v[108:111], v45 offset:49408\ns_lshl_b32 s54, s43, 11\ns_add_u32 s54, s54, 0\n"
"s_waitcnt lgkmcnt(6)\nv_mfma_f32_16x16x32_bf16 v[80:83], v[112:115], a[136:139], v[80:83]\nbuffer_store_dwordx4 v[176:179], v13, s[16:19], s54 offen sc1\nv_mfma_f32_16x16x32_bf16 v[84:87], v[112:115], a[168:171], v[84:87]\n"
"ds_read_b128 v[112:115], v42 offset:49408\ns_add_u32 s54, s54, 229376\nv_mfma_f32_16x16x32_bf16 v[88:91], v[116:119], a[136:139], v[88:91]\nbuffer_store_dwordx4 v[180:183], v13, s[16:19], s54 offen sc1\n"
"v_mfma_f32_16x16x32_bf16 v[92:95], v[116:119], a[168:171], v[92:95]\nds_read_b128 v[116:119], v46 offset:49408\ns_add_u32 m0, s32, 53248\ns_waitcnt lgkmcnt(6)\n"
"v_mfma_f32_16x16x32_bf16 v[80:83], v[120:123], a[140:143], v[80:83]\ns_mov_b32 s53, 29312\nv_mfma_f32_16x16x32_bf16 v[84:87], v[120:123], a[172:175], v[84:87]\nds_read_b128 v[120:123], v43 offset:49408\n"
"buffer_load_dwordx4 v10, s[12:15], s53 offen lds\nv_mfma_f32_16x16x32_bf16 v[88:91], v[124:127], a[140:143], v[88:91]\ns_add_u32 m0, s32, 57344\nv_mfma_f32_16x16x32_bf16 v[92:95], v[124:127], a[172:175], v[92:95]\n"
"ds_read_b128 v[124:127], v47 offset:49408\ns_mov_b32 s53, 29440\ns_waitcnt lgkmcnt(6)\nv_mfma_f32_16x16x32_bf16 v[80:83], v[96:99], a[144:147], v[80:83]\n"
"buffer_load_dwordx4 v10, s[12:15], s53 offen lds\nv_mfma_f32_16x16x32_bf16 v[84:87], v[96:99], a[176:179], v[84:87]\nbuffer_load_dword v37, v21, s[24:27], 0 offen sc1\nv_mfma_f32_16x16x32_bf16 v[88:91], v[100:103], a[144:147], v[88:91]\n"
"v_mfma_f32_16x16x32_bf16 v[92:95], v[100:103], a[176:179], v[92:95]\ns_waitcnt lgkmcnt(4)\nv_mfma_f32_16x16x32_bf16 v[80:83], v[104:107], a[148:151], v[80:83]\nv_mfma_f32_16x16x32_bf16 v[84:87], v[104:107], a[180:183], v[84:87]\n"
"v_mfma_f32_16x16x32_bf16 v[88:91], v[108:111], a[148:151], v[88:91]\nv_mfma_f32_16x16x32_bf16 v[92:95], v[108:111], a[180:183], v[92:95]\ns_waitcnt lgkmcnt(2)\nv_mfma_f32_16x16x32_bf16 v[80:83], v[112:115], a[152:155], v[80:83]\n"
"v_mfma_f32_16x16x32_bf16 v[84:87], v[112:115], a[184:187], v[84:87]\nv_mfma_f32_16x16x32_bf16 v[88:91], v[116:119], a[152:155], v[88:91]\nv_mfma_f32_16x16x32_bf16 v[92:95], v[116:119], a[184:187], v[92:95]\ns_waitcnt lgkmcnt(0)\n"
"v_mfma_f32_16x16x32_bf16 v[80:83], v[120:123], a[156:159], v[80:83]\nv_mfma_f32_16x16x32_bf16 v[84:87], v[120:123], a[188:191], v[84:87]\nv_mfma_f32_16x16x32_bf16 v[88:91], v[124:127], a[156:159], v[88:91]\nv_mfma_f32_16x16x32_bf16 v[92:95], v[124:127], a[188:191], v[92:95]\n"
"s_nop 7\nv_cvt_pk_bf16_f32 v184, v80, v81\nv_cvt_pk_bf16_f32 v185, v82, v83\nv_cvt_pk_bf16_f32 v186, v88, v89\n"
"v_cvt_pk_bf16_f32 v187, v90, v91\nv_cvt_pk_bf16_f32 v188, v84, v85\nv_cvt_pk_bf16_f32 v189, v86, v87\nv_cvt_pk_bf16_f32 v190, v92, v93\n"
"v_cvt_pk_bf16_f32 v191, v94, v95\ns_lshl_b32 s54, s43, 11\ns_add_u32 s54, s54, 1024\nbuffer_store_dwordx4 v[184:187], v13, s[16:19], s54 offen sc1\n"
"s_add_u32 s54, s54, 229376\nbuffer_store_dwordx4 v[188:191], v13, s[16:19], s54 offen sc1\ns_waitcnt vmcnt(2)\nv_mov_b32 v36, s28\n"
"v_cmp_ne_u32 vcc, s28, v37\ns_cbranch_vccz fg1_e1\ns_waitcnt vmcnt(0)\ns_barrier\n"
"buffer_store_dword v36, v19, s[24:27], 0 offen sc1\nfg1_po:\nv_cmp_ne_u32 vcc, s28, v37\ns_cbranch_vccz fg1_pook\n"
"s_add_u32 s55, s55, 1\ns_cmp_gt_u32 s55, 262144\ns_cbranch_scc0 fg1_po2\ns_endpgm\n"
"fg1_po2:\ns_sleep 1\nbuffer_load_dword v37, v21, s[24:27], 0 offen sc1\ns_waitcnt vmcnt(0)\n"
"s_branch fg1_po\nfg1_pook:\ns_mov_b32 s53, 8192\nbuffer_load_dwordx4 v[192:195], v13, s[16:19], s53 offen sc1\n"
"s_mov_b32 s53, 237568\nbuffer_load_dwordx4 v[196:199], v13, s[16:19], s53 offen sc1\ns_mov_b32 s53, 9216\nbuffer_load_dwordx4 v[200:203], v13, s[16:19], s53 offen sc1\n"
"s_mov_b32 s53, 238592\nbuffer_load_dwordx4 v[204:207], v13, s[16:19], s53 offen sc1\ns_mov_b32 s53, 10240\nbuffer_load_dwordx4 v[208:211], v13, s[16:19], s53 offen sc1\n"
"s_mov_b32 s53, 239616\nbuffer_load_dwordx4 v[212:215], v13, s[16:19], s53 offen sc1\ns_mov_b32 s53, 11264\nbuffer_load_dwordx4 v[216:219], v13, s[16:19], s53 offen sc1\n"
"s_mov_b32 s53, 240640\nbuffer_load_dwordx4 v[220:223], v13, s[16:19], s53 offen sc1\ns_mov_b32 s53, 12288\nbuffer_load_dwordx4 v[224:227], v13, s[16:19], s53 offen sc1\n"
"s_mov_b32 s53, 241664\nbuffer_load_dwordx4 v[228:231], v13, s[16:19], s53 offen sc1\ns_mov_b32 s53, 13312\nbuffer_load_dwordx4 v[232:235], v13, s[16:19], s53 offen sc1\n"
"s_mov_b32 s53, 242688\nbuffer_load_dwordx4 v[236:239], v13, s[16:19], s53 offen sc1\nbuffer_store_dword v36, v19, s[24:27], 0 offen sc1\ns_branch fg1_e2\n"
"fg1_e1:\ns_mov_b32 s53, 8192\nbuffer_load_dwordx4 v[192:195], v13, s[16:19], s53 offen sc1\ns_mov_b32 s53, 237568\n"
"buffer_load_dwordx4 v[196:199], v13, s[16:19], s53 offen sc1\ns_mov_b32 s53, 9216\nbuffer_load_dwordx4 v[200:203], v13, s[16:19], s53 offen sc1\ns_mov_b32 s53, 238592\n"
"buffer_load_dwordx4 v[204:207], v13, s[16:19], s53 offen sc1\ns_mov_b32 s53, 10240\nbuffer_load_dwordx4 v[208:211], v13, s[16:19], s53 offen sc1\ns_mov_b32 s53, 239616\n"
"buffer_load_dwordx4 v[212:215], v13, s[16:19], s53 offen sc1\ns_mov_b32 s53, 11264\nbuffer_load_dwordx4 v[216:219], v13, s[16:19], s53 offen sc1\ns_mov_b32 s53, 240640\n"
"buffer_load_dwordx4 v[220:223], v13, s[16:19], s53 offen sc1\ns_mov_b32 s53, 12288\nbuffer_load_dwordx4 v[224:227], v13, s[16:19], s53 offen sc1\ns_mov_b32 s53, 241664\n"
"buffer_load_dwordx4 v[228:231], v13, s[16:19], s53 offen sc1\ns_mov_b32 s53, 13312\nbuffer_load_dwordx4 v[232:235], v13, s[16:19], s53 offen sc1\ns_mov_b32 s53, 242688\n"
"buffer_load_dwordx4 v[236:239], v13, s[16:19], s53 offen sc1\ns_waitcnt vmcnt(12)\ns_barrier\nbuffer_store_dword v36, v19, s[24:27], 0 offen sc1\n"
"fg1_e2:\ns_add_u32 m0, s32, 65536\ns_mov_b32 s53, 57344\nbuffer_load_dwordx4 v10, s[12:15], s53 offen lds\n"
"s_add_u32 m0, s32, 69632\ns_mov_b32 s53, 57472\nbuffer_load_dwordx4 v10, s[12:15], s53 offen lds\ns_add_u32 m0, s32, 73728\n"
"s_mov_b32 s53, 57600\nbuffer_load_dwordx4 v10, s[12:15], s53 offen lds\ns_add_u32 m0, s32, 77824\ns_mov_b32 s53, 57728\n"
"buffer_load_dwordx4 v10, s[12:15], s53 offen lds\ns_add_u32 m0, s32, 81920\ns_mov_b32 s53, 57856\nbuffer_load_dwordx4 v10, s[12:15], s53 offen lds\n"
"s_add_u32 m0, s32, 86016\ns_mov_b32 s53, 57984\nbuffer_load_dwordx4 v10, s[12:15], s53 offen lds\ns_add_u32 m0, s32, 90112\n"
"s_mov_b32 s53, 58112\nbuffer_load_dwordx4 v10, s[12:15], s53 offen lds\ns_sub_u32 s48, s28, 1\ns_mov_b32 s49, 86016\n"
"s_mov_b32 s50, 0\ns_mov_b32 s51, 0\ns_add_u32 s52, s32, 98304\ns_add_u32 s47, s32, 131072\n"
"ds_read_b128 v[40:43], v32 offset:0\nds_read_b128 v[44:47], v33 offset:0\nds_read_b128 v[48:51], v34 offset:0\nds_read_b128 v[52:55], v35 offset:0\n"
"s_barrier\ns_waitcnt lgkmcnt(3)\nv_mfma_f32_16x16x32_bf16 v[64:67], v[40:43], v[128:131], 0\nv_mov_b32 v36, s48\n"
"s_add_u32 s48, s48, 1\ns_nop 0\nbuffer_store_dword v36, v19, s[24:27], s33 offen sc1\nv_mfma_f32_16x16x32_bf16 v[68:71], v[40:43], v[132:135], 0\n"
"ds_read_b128 v[40:43], v32 offset:4096\ns_waitcnt lgkmcnt(3)\nv_mfma_f32_16x16x32_bf16 v[72:75], v[44:47], v[128:131], 0\nv_mfma_f32_16x16x32_bf16 v[76:79], v[44:47], v[132:135], 0\n"
"ds_read_b128 v[44:47], v33 offset:4096\ns_waitcnt lgkmcnt(3)\nv_mfma_f32_16x16x32_bf16 v[64:67], v[48:51], v[136:139], v[64:67]\ns_add_u32 m0, s52, 0\n"
"v_mfma_f32_16x16x32_bf16 v[68:71], v[48:51], v[140:143], v[68:71]\nds_read_b128 v[48:51], v34 offset:4096\ns_add_u32 s53, s49, 0\ns_waitcnt lgkmcnt(3)\n"
"v_mfma_f32_16x16x32_bf16 v[72:75], v[52:55], v[136:139], v[72:75]\nbuffer_load_dwordx4 v10, s[12:15], s53 offen lds\nv_mfma_f32_16x16x32_bf16 v[76:79], v[52:55], v[140:143], v[76:79]\nds_read_b128 v[52:55], v35 offset:4096\n"
"s_add_u32 m0, s52, 4096\ns_waitcnt lgkmcnt(3)\nv_mfma_f32_16x16x32_bf16 v[64:67], v[40:43], v[144:147], v[64:67]\ns_add_u32 s53, s49, 128\n"
"v_mfma_f32_16x16x32_bf16 v[68:71], v[40:43], v[148:151], v[68:71]\nds_read_b128 v[40:43], v32 offset:8192\nbuffer_load_dwordx4 v10, s[12:15], s53 offen lds\ns_waitcnt lgkmcnt(3)\n"
"v_mfma_f32_16x16x32_bf16 v[72:75], v[44:47], v[144:147], v[72:75]\ns_add_u32 m0, s52, 8192\nv_mfma_f32_16x16x32_bf16 v[76:79], v[44:47], v[148:151], v[76:79]\nds_read_b128 v[44:47], v33 offset:8192\n"
"s_add_u32 s53, s49, 256\ns_waitcnt lgkmcnt(3)\nv_mfma_f32_16x16x32_bf16 v[64:67], v[48:51], v[152:155], v[64:67]\nbuffer_load_dwordx4 v10, s[12:15], s53 offen lds\n"
"v_mfma_f32_16x16x32_bf16 v[68:71], v[48:51], v[156:159], v[68:71]\nds_read_b128 v[48:51], v34 offset:8192\ns_add_u32 m0, s52, 12288\ns_waitcnt lgkmcnt(3)\n"
"v_mfma_f32_16x16x32_bf16 v[72:75], v[52:55], v[152:155], v[72:75]\ns_add_u32 s53, s49, 384\nv_mfma_f32_16x16x32_bf16 v[76:79], v[52:55], v[156:159], v[76:79]\nds_read_b128 v[52:55], v35 offset:8192\n"
"buffer_load_dwordx4 v10, s[12:15], s53 offen lds\ns_waitcnt lgkmcnt(3)\nv_mfma_f32_16x16x32_bf16 v[64:67], v[40:43], v[160:163], v[64:67]\ns_add_u32 m0, s52, 16384\n"
"v_mfma_f32_16x16x32_bf16 v[68:71], v[40:43], v[164:167], v[68:71]\nds_read_b128 v[40:43], v32 offset:12288\ns_add_u32 s53, s49, 512\ns_waitcnt lgkmcnt(3)\n"
"v_mfma_f32_16x16x32_bf16 v[72:75], v[44:47], v[160:163], v[72:75]\nbuffer_load_dwordx4 v10, s[12:15], s53 offen lds\nv_mfma_f32_16x16x32_bf16 v[76:79], v[44:47], v[164:167], v[76:79]\nds_read_b128 v[44:47], v33 offset:12288\n"
"s_add_u32 m0, s52, 20480\ns_waitcnt lgkmcnt(3)\nv_mfma_f32_16x16x32_bf16 v[64:67], v[48:51], v[168:171], v[64:67]\ns_add_u32 s53, s49, 640\n"
"v_mfma_f32_16x16x32_bf16 v[68:71], v[48:51], v[172:175], v[68:71]\nds_read_b128 v[48:51], v34 offset:12288\nbuffer_load_dwordx4 v10, s[12:15], s53 offen lds\ns_waitcnt lgkmcnt(3)\n"
"v_mfma_f32_16x16x32_bf16 v[72:75], v[52:55], v[168:171], v[72:75]\ns_add_u32 m0, s52, 24576\nv_mfma_f32_16x16x32_bf16 v[76:79], v[52:55], v[172:175], v[76:79]\nds_read_b128 v[52:55], v35 offset:12288\n"
"s_add_u32 s53, s49, 768\ns_waitcnt lgkmcnt(3)\nv_mfma_f32_16x16x32_bf16 v[64:67], v[40:43], v[176:179], v[64:67]\nbuffer_load_dwordx4 v10, s[12:15], s53 offen lds\n"
"v_mfma_f32_16x16x32_bf16 v[68:71], v[40:43], v[180:183], v[68:71]\nds_read_b128 v[40:43], v32 offset:16384\ns_waitcnt lgkmcnt(3)\nv_mfma_f32_16x16x32_bf16 v[72:75], v[44:47], v[176:179], v[72:75]\n"
"v_mfma_f32_16x16x32_bf16 v[76:79], v[44:47], v[180:183], v[76:79]\nds_read_b128 v[44:47], v33 offset:16384\ns_waitcnt lgkmcnt(3)\nv_mfma_f32_16x16x32_bf16 v[64:67], v[48:51], v[184:187], v[64:67]\n"
"v_mfma_f32_16x16x32_bf16 v[68:71], v[48:51], v[188:191], v[68:71]\nds_read_b128 v[48:51], v34 offset:16384\ns_waitcnt lgkmcnt(3)\nv_mfma_f32_16x16x32_bf16 v[72:75], v[52:55], v[184:187], v[72:75]\n"
"v_mfma_f32_16x16x32_bf16 v[76:79], v[52:55], v[188:191], v[76:79]\nds_read_b128 v[52:55], v35 offset:16384\ns_waitcnt vmcnt(16)\ns_waitcnt lgkmcnt(3)\n"
"v_mfma_f32_16x16x32_bf16 v[64:67], v[40:43], v[192:195], v[64:67]\nv_mfma_f32_16x16x32_bf16 v[68:71], v[40:43], v[196:199], v[68:71]\nds_read_b128 v[40:43], v32 offset:20480\ns_waitcnt lgkmcnt(3)\n"
"v_mfma_f32_16x16x32_bf16 v[72:75], v[44:47], v[192:195], v[72:75]\nv_mfma_f32_16x16x32_bf16 v[76:79], v[44:47], v[196:199], v[76:79]\nds_read_b128 v[44:47], v33 offset:20480\ns_waitcnt lgkmcnt(3)\n"
"v_mfma_f32_16x16x32_bf16 v[64:67], v[48:51], v[200:203], v[64:67]\nv_mfma_f32_16x16x32_bf16 v[68:71], v[48:51], v[204:207], v[68:71]\nds_read_b128 v[48:51], v34 offset:20480\ns_waitcnt lgkmcnt(3)\n"
"v_mfma_f32_16x16x32_bf16 v[72:75], v[52:55], v[200:203], v[72:75]\nv_mfma_f32_16x16x32_bf16 v[76:79], v[52:55], v[204:207], v[76:79]\nds_read_b128 v[52:55], v35 offset:20480\ns_waitcnt lgkmcnt(3)\n"
"v_mfma_f32_16x16x32_bf16 v[64:67], v[40:43], v[208:211], v[64:67]\nv_mfma_f32_16x16x32_bf16 v[68:71], v[40:43], v[212:215], v[68:71]\nds_read_b128 v[40:43], v32 offset:24576\ns_waitcnt lgkmcnt(3)\n"
"v_mfma_f32_16x16x32_bf16 v[72:75], v[44:47], v[208:211], v[72:75]\nv_mfma_f32_16x16x32_bf16 v[76:79], v[44:47], v[212:215], v[76:79]\nds_read_b128 v[44:47], v33 offset:24576\ns_waitcnt lgkmcnt(3)\n"
"v_mfma_f32_16x16x32_bf16 v[64:67], v[48:51], v[216:219], v[64:67]\ns_add_u32 s54, s51, 32768\ns_cmp_eq_u32 s54, 163840\nv_mfma_f32_16x16x32_bf16 v[68:71], v[48:51], v[220:223], v[68:71]\n"
"s_cselect_b32 s54, 0, s54\ns_sub_u32 s54, s54, s51\nds_read_b128 v[48:51], v34 offset:24576\ns_waitcnt lgkmcnt(3)\n"
"v_mfma_f32_16x16x32_bf16 v[72:75], v[52:55], v[216:219], v[72:75]\nv_mfma_f32_16x16x32_bf16 v[76:79], v[52:55], v[220:223], v[76:79]\nds_read_b128 v[52:55], v35 offset:24576\ns_waitcnt lgkmcnt(3)\n"
"v_mfma_f32_16x16x32_bf16 v[64:67], v[40:43], v[224:227], v[64:67]\nv_add_u32 v32, s54, v32\nv_mfma_f32_16x16x32_bf16 v[68:71], v[40:43], v[228:231], v[68:71]\nds_read_b128 v[40:43], v32 offset:0\n"
"s_waitcnt lgkmcnt(3)\nv_mfma_f32_16x16x32_bf16 v[72:75], v[44:47], v[224:227], v[72:75]\nv_add_u32 v33, s54, v33\nv_mfma_f32_16x16x32_bf16 v[76:79], v[44:47], v[228:231], v[76:79]\n"
"ds_read_b128 v[44:47], v33 offset:0\ns_waitcnt lgkmcnt(3)\nv_mfma_f32_16x16x32_bf16 v[64:67], v[48:51], v[232:235], v[64:67]\nv_add_u32 v34, s54, v34\n"
"v_mfma_f32_16x16x32_bf16 v[68:71], v[48:51], v[236:239], v[68:71]\nds_read_b128 v[48:51], v34 offset:0\ns_waitcnt lgkmcnt(3)\nv_mfma_f32_16x16x32_bf16 v[72:75], v[52:55], v[232:235], v[72:75]\n"
"v_add_u32 v35, s54, v35\nv_mfma_f32_16x16x32_bf16 v[76:79], v[52:55], v[236:239], v[76:79]\nds_read_b128 v[52:55], v35 offset:0\ns_mov_b32 s52, s47\n"
"s_add_u32 s47, s51, s32\ns_add_u32 s51, s51, s54\ns_add_u32 s49, s49, 28672\ns_waitcnt vmcnt(8)\n"
"s_barrier\ns_waitcnt lgkmcnt(3)\nv_mfma_f32_16x16x32_bf16 v[96:99], v[40:43], v[128:131], 0\nv_mov_b32 v36, s48\n"
"s_add_u32 s48, s48, 1\ns_nop 0\nbuffer_store_dword v36, v19, s[24:27], s33 offen sc1\nv_mfma_f32_16x16x32_bf16 v[100:103], v[40:43], v[132:135], 0\n"
"ds_read_b128 v[40:43], v32 offset:4096\ns_waitcnt lgkmcnt(3)\nv_mfma_f32_16x16x32_bf16 v[104:107], v[44:47], v[128:131], 0\nv_mfma_f32_16x16x32_bf16 v[108:111], v[44:47], v[132:135], 0\n"
"ds_read_b128 v[44:47], v33 offset:4096\nv_cvt_pk_bf16_f32 v56, v64, v65\ns_waitcnt lgkmcnt(3)\nv_mfma_f32_16x16x32_bf16 v[96:99], v[48:51], v[136:139], v[96:99]\n"
"v_cvt_pk_bf16_f32 v57, v66, v67\nv_mfma_f32_16x16x32_bf16 v[100:103], v[48:51], v[140:143], v[100:103]\nds_read_b128 v[48:51], v34 offset:4096\nv_cvt_pk_bf16_f32 v58, v72, v73\n"
"s_waitcnt lgkmcnt(3)\nv_mfma_f32_16x16x32_bf16 v[104:107], v[52:55], v[136:139], v[104:107]\nv_cvt_pk_bf16_f32 v59, v74, v75\nv_mfma_f32_16x16x32_bf16 v[108:111], v[52:55], v[140:143], v[108:111]\n"
"ds_read_b128 v[52:55], v35 offset:4096\ns_waitcnt lgkmcnt(3)\nv_mfma_f32_16x16x32_bf16 v[96:99], v[40:43], v[144:147], v[96:99]\nv_mfma_f32_16x16x32_bf16 v[100:103], v[40:43], v[148:151], v[100:103]\n"
"ds_read_b128 v[40:43], v32 offset:8192\nbuffer_store_dwordx4 v[56:59], v15, s[20:23], s50 offen sc1\ns_waitcnt lgkmcnt(3)\nv_mfma_f32_16x16x32_bf16 v[104:107], v[44:47], v[144:147], v[104:107]\n"
"v_cvt_pk_bf16_f32 v60, v68, v69\nv_mfma_f32_16x16x32_bf16 v[108:111], v[44:47], v[148:151], v[108:111]\nds_read_b128 v[44:47], v33 offset:8192\nv_cvt_pk_bf16_f32 v61, v70, v71\n"
"s_waitcnt lgkmcnt(3)\nv_mfma_f32_16x16x32_bf16 v[96:99], v[48:51], v[152:155], v[96:99]\nv_cvt_pk_bf16_f32 v62, v76, v77\nv_mfma_f32_16x16x32_bf16 v[100:103], v[48:51], v[156:159], v[100:103]\n"
"ds_read_b128 v[48:51], v34 offset:8192\nv_cvt_pk_bf16_f32 v63, v78, v79\ns_waitcnt lgkmcnt(3)\nv_mfma_f32_16x16x32_bf16 v[104:107], v[52:55], v[152:155], v[104:107]\n"
"v_mfma_f32_16x16x32_bf16 v[108:111], v[52:55], v[156:159], v[108:111]\nds_read_b128 v[52:55], v35 offset:8192\ns_waitcnt lgkmcnt(3)\nv_mfma_f32_16x16x32_bf16 v[96:99], v[40:43], v[160:163], v[96:99]\n"
"buffer_store_dwordx4 v[60:63], v16, s[20:23], s50 offen sc1\nv_mfma_f32_16x16x32_bf16 v[100:103], v[40:43], v[164:167], v[100:103]\nds_read_b128 v[40:43], v32 offset:12288\ns_add_u32 s50, s50, s36\n"
"s_waitcnt lgkmcnt(3)\nv_mfma_f32_16x16x32_bf16 v[104:107], v[44:47], v[160:163], v[104:107]\ns_add_u32 m0, s52, 0\nv_mfma_f32_16x16x32_bf16 v[108:111], v[44:47], v[164:167], v[108:111]\n"
"ds_read_b128 v[44:47], v33 offset:12288\ns_add_u32 s53, s49, 0\ns_waitcnt lgkmcnt(3)\nv_mfma_f32_16x16x32_bf16 v[96:99], v[48:51], v[168:171], v[96:99]\n"
"buffer_load_dwordx4 v10, s[12:15], s53 offen lds\nv_mfma_f32_16x16x32_bf16 v[100:103], v[48:51], v[172:175], v[100:103]\nds_read_b128 v[48:51], v34 offset:12288\ns_add_u32 m0, s52, 4096\n"
"s_waitcnt lgkmcnt(3)\nv_mfma_f32_16x16x32_bf16 v[104:107], v[52:55], v[168:171], v[104:107]\ns_add_u32 s53, s49, 128\nv_mfma_f32_16x16x32_bf16 v[108:111], v[52:55], v[172:175], v[108:111]\n"
"ds_read_b128 v[52:55], v35 offset:12288\nbuffer_load_dwordx4 v10, s[12:15], s53 offen lds\ns_waitcnt lgkmcnt(3)\nv_mfma_f32_16x16x32_bf16 v[96:99], v[40:43], v[176:179], v[96:99]\n"
"s_add_u32 m0, s52, 8192\nv_mfma_f32_16x16x32_bf16 v[100:103], v[40:43], v[180:183], v[100:103]\nds_read_b128 v[40:43], v32 offset:16384\ns_add_u32 s53, s49, 256\n"
"s_waitcnt lgkmcnt(3)\nv_mfma_f32_16x16x32_bf16 v[104:107], v[44:47], v[176:179], v[104:107]\nbuffer_load_dwordx4 v10, s[12:15], s53 offen lds\nv_mfma_f32_16x16x32_bf16 v[108:111], v[44:47], v[180:183], v[108:111]\n"
"ds_read_b128 v[44:47], v33 offset:16384\ns_add_u32 m0, s52, 12288\ns_waitcnt lgkmcnt(3)\nv_mfma_f32_16x16x32_bf16 v[96:99], v[48:51], v[184:187], v[96:99]\n"
"s_add_u32 s53, s49, 384\nv_mfma_f32_16x16x32_bf16 v[100:103], v[48:51], v[188:191], v[100:103]\nds_read_b128 v[48:51], v34 offset:16384\nbuffer_load_dwordx4 v10, s[12:15], s53 offen lds\n"
"s_waitcnt lgkmcnt(3)\nv_mfma_f32_16x16x32_bf16 v[104:107], v[52:55], v[184:187], v[104:107]\ns_add_u32 m0, s52, 16384\nv_mfma_f32_16x16x32_bf16 v[108:111], v[52:55], v[188:191], v[108:111]\n"
"ds_read_b128 v[52:55], v35 offset:16384\ns_add_u32 s53, s49, 512\ns_waitcnt lgkmcnt(3)\nv_mfma_f32_16x16x32_bf16 v[96:99], v[40:43], v[192:195], v[96:99]\n"
"buffer_load_dwordx4 v10, s[12:15], s53 offen lds\nv_mfma_f32_16x16x32_bf16 v[100:103], v[40:43], v[196:199], v[100:103]\nds_read_b128 v[40:43], v32 offset:20480\ns_add_u32 m0, s52, 20480\n"
"s_waitcnt lgkmcnt(3)\nv_mfma_f32_16x16x32_bf16 v[104:107], v[44:47], v[192:195], v[104:107]\ns_add_u32 s53, s49, 640\nv_mfma_f32_16x16x32_bf16 v[108:111], v[44:47], v[196:199], v[108:111]\n"
"ds_read_b128 v[44:47], v33 offset:20480\nbuffer_load_dwordx4 v10, s[12:15], s53 offen lds\ns_waitcnt lgkmcnt(3)\nv_mfma_f32_16x16x32_bf16 v[96:99], v[48:51], v[200:203], v[96:99]\n"
"s_add_u32 m0, s52, 24576\nv_mfma_f32_16x16x32_bf16 v[100:103], v[48:51], v[204:207], v[100:103]\nds_read_b128 v[48:51], v34 offset:20480\ns_add_u32 s53, s49, 768\n"
"s_waitcnt lgkmcnt(3)\nv_mfma_f32_16x16x32_bf16 v[104:107], v[52:55], v[200:203], v[104:107]\nbuffer_load_dwordx4 v10, s[12:15], s53 offen lds\nv_mfma_f32_16x16x32_bf16 v[108:111], v[52:55], v[204:207], v[108:111]\n"
"ds_read_b128 v[52:55], v35 offset:20480\ns_waitcnt lgkmcnt(3)\nv_mfma_f32_16x16x32_bf16 v[96:99], v[40:43], v[208:211], v[96:99]\nv_mfma_f32_16x16x32_bf16 v[100:103], v[40:43], v[212:215], v[100:103]\n"
"ds_read_b128 v[40:43], v32 offset:24576\ns_waitcnt lgkmcnt(3)\nv_mfma_f32_16x16x32_bf16 v[104:107], v[44:47], v[208:211], v[104:107]\nv_mfma_f32_16x16x32_bf16 v[108:111], v[44:47], v[212:215], v[108:111]\n"
"ds_read_b128 v[44:47], v33 offset:24576\ns_waitcnt lgkmcnt(3)\nv_mfma_f32_16x16x32_bf16 v[96:99], v[48:51], v[216:219], v[96:99]\ns_add_u32 s54, s51, 32768\n"
"s_cmp_eq_u32 s54, 163840\nv_mfma_f32_16x16x32_bf16 v[100:103], v[48:51], v[220:223], v[100:103]\ns_cselect_b32 s54, 0, s54\ns_sub_u32 s54, s54, s51\n"
"ds_read_b128 v[48:51], v34 offset:24576\ns_waitcnt lgkmcnt(3)\nv_mfma_f32_16x16x32_bf16 v[104:107], v[52:55], v[216:219], v[104:107]\nv_mfma_f32_16x16x32_bf16 v[108:111], v[52:55], v[220:223], v[108:111]\n"
"ds_read_b128 v[52:55], v35 offset:24576\ns_waitcnt lgkmcnt(3)\nv_mfma_f32_16x16x32_bf16 v[96:99], v[40:43], v[224:227], v[96:99]\nv_add_u32 v32, s54, v32\n"
"v_mfma_f32_16x16x32_bf16 v[100:103], v[40:43], v[228:231], v[100:103]\nds_read_b128 v[40:43], v32 offset:0\ns_waitcnt lgkmcnt(3)\nv_mfma_f32_16x16x32_bf16 v[104:107], v[44:47], v[224:227], v[104:107]\n"
"v_add_u32 v33, s54, v33\nv_mfma_f32_16x16x32_bf16 v[108:111], v[44:47], v[228:231], v[108:111]\nds_read_b128 v[44:47], v33 offset:0\ns_waitcnt lgkmcnt(3)\n"
"v_mfma_f32_16x16x32_bf16 v[96:99], v[48:51], v[232:235], v[96:99]\nv_add_u32 v34, s54, v34\nv_mfma_f32_16x16x32_bf16 v[100:103], v[48:51], v[236:239], v[100:103]\nds_read_b128 v[48:51], v34 offset:0\n"
"s_waitcnt lgkmcnt(3)\nv_mfma_f32_16x16x32_bf16 v[104:107], v[52:55], v[232:235], v[104:107]\nv_add_u32 v35, s54, v35\nv_mfma_f32_16x16x32_bf16 v[108:111], v[52:55], v[236:239], v[108:111]\n"
"ds_read_b128 v[52:55], v35 offset:0\ns_mov_b32 s52, s47\ns_add_u32 s47, s51, s32\ns_add_u32 s51, s51, s54\n"
"s_add_u32 s49, s49, 28672\ns_waitcnt vmcnt(7)\ns_barrier\ns_waitcnt lgkmcnt(3)\n"
"v_mfma_f32_16x16x32_bf16 v[64:67], v[40:43], v[128:131], 0\nv_mov_b32 v36, s48\ns_add_u32 s48, s48, 1\ns_nop 0\n"
"buffer_store_dword v36, v19, s[24:27], s33 offen sc1\nv_mfma_f32_16x16x32_bf16 v[68:71], v[40:43], v[132:135], 0\nds_read_b128 v[40:43], v32 offset:4096\ns_waitcnt lgkmcnt(3)\n"
"v_mfma_f32_16x16x32_bf16 v[72:75], v[44:47], v[128:131], 0\nv_mfma_f32_16x16x32_bf16 v[76:79], v[44:47], v[132:135], 0\nds_read_b128 v[44:47], v33 offset:4096\nv_cvt_pk_bf16_f32 v56, v96, v97\n"
"s_waitcnt lgkmcnt(3)\nv_mfma_f32_16x16x32_bf16 v[64:67], v[48:51], v[136:139], v[64:67]\nv_cvt_pk_bf16_f32 v57, v98, v99\nv_mfma_f32_16x16x32_bf16 v[68:71], v[48:51], v[140:143], v[68:71]\n"
"ds_read_b128 v[48:51], v34 offset:4096\nv_cvt_pk_bf16_f32 v58, v104, v105\ns_waitcnt lgkmcnt(3)\nv_mfma_f32_16x16x32_bf16 v[72:75], v[52:55], v[136:139], v[72:75]\n"
"v_cvt_pk_bf16_f32 v59, v106, v107\nv_mfma_f32_16x16x32_bf16 v[76:79], v[52:55], v[140:143], v[76:79]\nds_read_b128 v[52:55], v35 offset:4096\ns_waitcnt lgkmcnt(3)\n"
"v_mfma_f32_16x16x32_bf16 v[64:67], v[40:43], v[144:147], v[64:67]\nv_mfma_f32_16x16x32_bf16 v[68:71], v[40:43], v[148:151], v[68:71]\nds_read_b128 v[40:43], v32 offset:8192\nbuffer_store_dwordx4 v[56:59], v15, s[20:23], s50 offen sc1\n"
"s_waitcnt lgkmcnt(3)\nv_mfma_f32_16x16x32_bf16 v[72:75], v[44:47], v[144:147], v[72:75]\nv_cvt_pk_bf16_f32 v60, v100, v101\nv_mfma_f32_16x16x32_bf16 v[76:79], v[44:47], v[148:151], v[76:79]\n"
"ds_read_b128 v[44:47], v33 offset:8192\nv_cvt_pk_bf16_f32 v61, v102, v103\ns_waitcnt lgkmcnt(3)\nv_mfma_f32_16x16x32_bf16 v[64:67], v[48:51], v[152:155], v[64:67]\n"
"v_cvt_pk_bf16_f32 v62, v108, v109\nv_mfma_f32_16x16x32_bf16 v[68:71], v[48:51], v[156:159], v[68:71]\nds_read_b128 v[48:51], v34 offset:8192\nv_cvt_pk_bf16_f32 v63, v110, v111\n"
"s_waitcnt lgkmcnt(3)\nv_mfma_f32_16x16x32_bf16 v[72:75], v[52:55], v[152:155], v[72:75]\nv_mfma_f32_16x16x32_bf16 v[76:79], v[52:55], v[156:159], v[76:79]\nds_read_b128 v[52:55], v35 offset:8192\n"
"s_waitcnt lgkmcnt(3)\nv_mfma_f32_16x16x32_bf16 v[64:67], v[40:43], v[160:163], v[64:67]\nbuffer_store_dwordx4 v[60:63], v16, s[20:23], s50 offen sc1\nv_mfma_f32_16x16x32_bf16 v[68:71], v[40:43], v[164:167], v[68:71]\n"
"ds_read_b128 v[40:43], v32 offset:12288\ns_add_u32 s50, s50, s36\ns_waitcnt lgkmcnt(3)\nv_mfma_f32_16x16x32_bf16 v[72:75], v[44:47], v[160:163], v[72:75]\n"
"s_add_u32 m0, s52, 0\nv_mfma_f32_16x16x32_bf16 v[76:79], v[44:47], v[164:167], v[76:79]\nds_read_b128 v[44:47], v33 offset:12288\ns_add_u32 s53, s49, 0\n"
"s_waitcnt lgkmcnt(3)\nv_mfma_f32_16x16x32_bf16 v[64:67], v[48:51], v[168:171], v[64:67]\nbuffer_load_dwordx4 v10, s[12:15], s53 offen lds\nv_mfma_f32_16x16x32_bf16 v[68:71], v[48:51], v[172:175], v[68:71]\n"
"ds_read_b128 v[48:51], v34 offset:12288\ns_add_u32 m0, s52, 4096\ns_waitcnt lgkmcnt(3)\nv_mfma_f32_16x16x32_bf16 v[72:75], v[52:55], v[168:171], v[72:75]\n"
"s_add_u32 s53, s49, 128\nv_mfma_f32_16x16x32_bf16 v[76:79], v[52:55], v[172:175], v[76:79]\nds_read_b128 v[52:55], v35 offset:12288\nbuffer_load_dwordx4 v10, s[12:15], s53 offen lds\n"
"s_waitcnt lgkmcnt(3)\nv_mfma_f32_16x16x32_bf16 v[64:67], v[40:43], v[176:179], v[64:67]\ns_add_u32 m0, s52, 8192\nv_mfma_f32_16x16x32_bf16 v[68:71], v[40:43], v[180:183], v[68:71]\n"
"ds_read_b128 v[40:43], v32 offset:16384\ns_add_u32 s53, s49, 256\ns_waitcnt lgkmcnt(3)\nv_mfma_f32_16x16x32_bf16 v[72:75], v[44:47], v[176:179], v[72:75]\n"
"buffer_load_dwordx4 v10, s[12:15], s53 offen lds\nv_mfma_f32_16x16x32_bf16 v[76:79], v[44:47], v[180:183], v[76:79]\nds_read_b128 v[44:47], v33 offset:16384\ns_add_u32 m0, s52, 12288\n"
"s_waitcnt lgkmcnt(3)\nv_mfma_f32_16x16x32_bf16 v[64:67], v[48:51], v[184:187], v[64:67]\ns_add_u32 s53, s49, 384\nv_mfma_f32_16x16x32_bf16 v[68:71], v[48:51], v[188:191], v[68:71]\n"
"ds_read_b128 v[48:51], v34 offset:16384\nbuffer_load_dwordx4 v10, s[12:15], s53 offen lds\ns_waitcnt lgkmcnt(3)\nv_mfma_f32_16x16x32_bf16 v[72:75], v[52:55], v[184:187], v[72:75]\n"
"s_add_u32 m0, s52, 16384\nv_mfma_f32_16x16x32_bf16 v[76:79], v[52:55], v[188:191], v[76:79]\nds_read_b128 v[52:55], v35 offset:16384\ns_add_u32 s53, s49, 512\n"
"s_waitcnt lgkmcnt(3)\nv_mfma_f32_16x16x32_bf16 v[64:67], v[40:43], v[192:195], v[64:67]\nbuffer_load_dwordx4 v10, s[12:15], s53 offen lds\nv_mfma_f32_16x16x32_bf16 v[68:71], v[40:43], v[196:199], v[68:71]\n"
"ds_read_b128 v[40:43], v32 offset:20480\ns_add_u32 m0, s52, 20480\ns_waitcnt lgkmcnt(3)\nv_mfma_f32_16x16x32_bf16 v[72:75], v[44:47], v[192:195], v[72:75]\n"
"s_add_u32 s53, s49, 640\nv_mfma_f32_16x16x32_bf16 v[76:79], v[44:47], v[196:199], v[76:79]\nds_read_b128 v[44:47], v33 offset:20480\nbuffer_load_dwordx4 v10, s[12:15], s53 offen lds\n"
"s_waitcnt lgkmcnt(3)\nv_mfma_f32_16x16x32_bf16 v[64:67], v[48:51], v[200:203], v[64:67]\ns_add_u32 m0, s52, 24576\nv_mfma_f32_16x16x32_bf16 v[68:71], v[48:51], v[204:207], v[68:71]\n"
"ds_read_b128 v[48:51], v34 offset:20480\ns_add_u32 s53, s49, 768\ns_waitcnt lgkmcnt(3)\nv_mfma_f32_16x16x32_bf16 v[72:75], v[52:55], v[200:203], v[72:75]\n"
"buffer_load_dwordx4 v10, s[12:15], s53 offen lds\nv_mfma_f32_16x16x32_bf16 v[76:79], v[52:55], v[204:207], v[76:79]\nds_read_b128 v[52:55], v35 offset:20480\ns_waitcnt lgkmcnt(3)\n"
"v_mfma_f32_16x16x32_bf16 v[64:67], v[40:43], v[208:211], v[64:67]\nv_mfma_f32_16x16x32_bf16 v[68:71], v[40:43], v[212:215], v[68:71]\nds_read_b128 v[40:43], v32 offset:24576\ns_waitcnt lgkmcnt(3)\n"
"v_mfma_f32_16x16x32_bf16 v[72:75], v[44:47], v[208:211], v[72:75]\nv_mfma_f32_16x16x32_bf16 v[76:79], v[44:47], v[212:215], v[76:79]\nds_read_b128 v[44:47], v33 offset:24576\ns_waitcnt lgkmcnt(3)\n"
"v_mfma_f32_16x16x32_bf16 v[64:67], v[48:51], v[216:219], v[64:67]\ns_add_u32 s54, s51, 32768\ns_cmp_eq_u32 s54, 163840\nv_mfma_f32_16x16x32_bf16 v[68:71], v[48:51], v[220:223], v[68:71]\n"
"s_cselect_b32 s54, 0, s54\ns_sub_u32 s54, s54, s51\nds_read_b128 v[48:51], v34 offset:24576\ns_waitcnt lgkmcnt(3)\n"
"v_mfma_f32_16x16x32_bf16 v[72:75], v[52:55], v[216:219], v[72:75]\nv_mfma_f32_16x16x32_bf16 v[76:79], v[52:55], v[220:223], v[76:79]\nds_read_b128 v[52:55], v35 offset:24576\ns_waitcnt lgkmcnt(3)\n"
"v_mfma_f32_16x16x32_bf16 v[64:67], v[40:43], v[224:227], v[64:67]\nv_add_u32 v32, s54, v32\nv_mfma_f32_16x16x32_bf16 v[68:71], v[40:43], v[228:231], v[68:71]\nds_read_b128 v[40:43], v32 offset:0\n"
"s_waitcnt lgkmcnt(3)\nv_mfma_f32_16x16x32_bf16 v[72:75], v[44:47], v[224:227], v[72:75]\nv_add_u32 v33, s54, v33\nv_mfma_f32_16x16x32_bf16 v[76:79], v[44:47], v[228:231], v[76:79]\n"
"ds_read_b128 v[44:47], v33 offset:0\ns_waitcnt lgkmcnt(3)\nv_mfma_f32_16x16x32_bf16 v[64:67], v[48:51], v[232:235], v[64:67]\nv_add_u32 v34, s54, v34\n"
"v_mfma_f32_16x16x32_bf16 v[68:71], v[48:51], v[236:239], v[68:71]\nds_read_b128 v[48:51], v34 offset:0\ns_waitcnt lgkmcnt(3)\nv_mfma_f32_16x16x32_bf16 v[72:75], v[52:55], v[232:235], v[72:75]\n"
"v_add_u32 v35, s54, v35\nv_mfma_f32_16x16x32_bf16 v[76:79], v[52:55], v[236:239], v[76:79]\nds_read_b128 v[52:55], v35 offset:0\ns_mov_b32 s52, s47\n"
"s_add_u32 s47, s51, s32\ns_add_u32 s51, s51, s54\ns_add_u32 s49, s49, 28672\ns_waitcnt vmcnt(7)\n"
"s_barrier\ns_waitcnt lgkmcnt(3)\nv_mfma_f32_16x16x32_bf16 v[96:99], v[40:43], v[128:131], 0\nv_mov_b32 v36, s48\n"
"s_add_u32 s48, s48, 1\ns_nop 0\nbuffer_store_dword v36, v19, s[24:27], s33 offen sc1\nv_mfma_f32_16x16x32_bf16 v[100:103], v[40:43], v[132:135], 0\n"
"ds_read_b128 v[40:43], v32 offset:4096\ns_waitcnt lgkmcnt(3)\nv_mfma_f32_16x16x32_bf16 v[104:107], v[44:47], v[128:131], 0\nv_mfma_f32_16x16x32_bf16 v[108:111], v[44:47], v[132:135], 0\n"
"ds_read_b128 v[44:47], v33 offset:4096\nv_cvt_pk_bf16_f32 v56, v64, v65\ns_waitcnt lgkmcnt(3)\nv_mfma_f32_16x16x32_bf16 v[96:99], v[48:51], v[136:139], v[96:99]\n"
"v_cvt_pk_bf16_f32 v57, v66, v67\nv_mfma_f32_16x16x32_bf16 v[100:103], v[48:51], v[140:143], v[100:103]\nds_read_b128 v[48:51], v34 offset:4096\nv_cvt_pk_bf16_f32 v58, v72, v73\n"
"s_waitcnt lgkmcnt(3)\nv_mfma_f32_16x16x32_bf16 v[104:107], v[52:55], v[136:139], v[104:107]\nv_cvt_pk_bf16_f32 v59, v74, v75\nv_mfma_f32_16x16x32_bf16 v[108:111], v[52:55], v[140:143], v[108:111]\n"
"ds_read_b128 v[52:55], v35 offset:4096\ns_waitcnt lgkmcnt(3)\nv_mfma_f32_16x16x32_bf16 v[96:99], v[40:43], v[144:147], v[96:99]\nv_mfma_f32_16x16x32_bf16 v[100:103], v[40:43], v[148:151], v[100:103]\n"
"ds_read_b128 v[40:43], v32 offset:8192\nbuffer_store_dwordx4 v[56:59], v15, s[20:23], s50 offen sc1\ns_waitcnt lgkmcnt(3)\nv_mfma_f32_16x16x32_bf16 v[104:107], v[44:47], v[144:147], v[104:107]\n"
"v_cvt_pk_bf16_f32 v60, v68, v69\nv_mfma_f32_16x16x32_bf16 v[108:111], v[44:47], v[148:151], v[108:111]\nds_read_b128 v[44:47], v33 offset:8192\nv_cvt_pk_bf16_f32 v61, v70, v71\n"
"s_waitcnt lgkmcnt(3)\nv_mfma_f32_16x16x32_bf16 v[96:99], v[48:51], v[152:155], v[96:99]\nv_cvt_pk_bf16_f32 v62, v76, v77\nv_mfma_f32_16x16x32_bf16 v[100:103], v[48:51], v[156:159], v[100:103]\n"
"ds_read_b128 v[48:51], v34 offset:8192\nv_cvt_pk_bf16_f32 v63, v78, v79\ns_waitcnt lgkmcnt(3)\nv_mfma_f32_16x16x32_bf16 v[104:107], v[52:55], v[152:155], v[104:107]\n"
"v_mfma_f32_16x16x32_bf16 v[108:111], v[52:55], v[156:159], v[108:111]\nds_read_b128 v[52:55], v35 offset:8192\ns_waitcnt lgkmcnt(3)\nv_mfma_f32_16x16x32_bf16 v[96:99], v[40:43], v[160:163], v[96:99]\n"
"buffer_store_dwordx4 v[60:63], v16, s[20:23], s50 offen sc1\nv_mfma_f32_16x16x32_bf16 v[100:103], v[40:43], v[164:167], v[100:103]\nds_read_b128 v[40:43], v32 offset:12288\ns_add_u32 s50, s50, s36\n"
"s_waitcnt lgkmcnt(3)\nv_mfma_f32_16x16x32_bf16 v[104:107], v[44:47], v[160:163], v[104:107]\ns_add_u32 m0, s52, 0\nv_mfma_f32_16x16x32_bf16 v[108:111], v[44:47], v[164:167], v[108:111]\n"
"ds_read_b128 v[44:47], v33 offset:12288\ns_add_u32 s53, s49, 0\ns_waitcnt lgkmcnt(3)\nv_mfma_f32_16x16x32_bf16 v[96:99], v[48:51], v[168:171], v[96:99]\n"
"buffer_load_dwordx4 v10, s[12:15], s53 offen lds\nv_mfma_f32_16x16x32_bf16 v[100:103], v[48:51], v[172:175], v[100:103]\nds_read_b128 v[48:51], v34 offset:12288\ns_add_u32 m0, s52, 4096\n"
"s_waitcnt lgkmcnt(3)\nv_mfma_f32_16x16x32_bf16 v[104:107], v[52:55], v[168:171], v[104:107]\ns_add_u32 s53, s49, 128\nv_mfma_f32_16x16x32_bf16 v[108:111], v[52:55], v[172:175], v[108:111]\n"
"ds_read_b128 v[52:55], v35 offset:12288\nbuffer_load_dwordx4 v10, s[12:15], s53 offen lds\ns_waitcnt lgkmcnt(3)\nv_mfma_f32_16x16x32_bf16 v[96:99], v[40:43], v[176:179], v[96:99]\n"
"s_add_u32 m0, s52, 8192\nv_mfma_f32_16x16x32_bf16 v[100:103], v[40:43], v[180:183], v[100:103]\nds_read_b128 v[40:43], v32 offset:16384\ns_add_u32 s53, s49, 256\n"
"s_waitcnt lgkmcnt(3)\nv_mfma_f32_16x16x32_bf16 v[104:107], v[44:47], v[176:179], v[104:107]\nbuffer_load_dwordx4 v10, s[12:15], s53 offen lds\nv_mfma_f32_16x16x32_bf16 v[108:111], v[44:47], v[180:183], v[108:111]\n"
"ds_read_b128 v[44:47], v33 offset:16384\ns_add_u32 m0, s52, 12288\ns_waitcnt lgkmcnt(3)\nv_mfma_f32_16x16x32_bf16 v[96:99], v[48:51], v[184:187], v[96:99]\n"
"s_add_u32 s53, s49, 384\nv_mfma_f32_16x16x32_bf16 v[100:103], v[48:51], v[188:191], v[100:103]\nds_read_b128 v[48:51], v34 offset:16384\nbuffer_load_dwordx4 v10, s[12:15], s53 offen lds\n"
"s_waitcnt lgkmcnt(3)\nv_mfma_f32_16x16x32_bf16 v[104:107], v[52:55], v[184:187], v[104:107]\ns_add_u32 m0, s52, 16384\nv_mfma_f32_16x16x32_bf16 v[108:111], v[52:55], v[188:191], v[108:111]\n"
"ds_read_b128 v[52:55], v35 offset:16384\ns_add_u32 s53, s49, 512\ns_waitcnt lgkmcnt(3)\nv_mfma_f32_16x16x32_bf16 v[96:99], v[40:43], v[192:195], v[96:99]\n"
"buffer_load_dwordx4 v10, s[12:15], s53 offen lds\nv_mfma_f32_16x16x32_bf16 v[100:103], v[40:43], v[196:199], v[100:103]\nds_read_b128 v[40:43], v32 offset:20480\ns_add_u32 m0, s52, 20480\n"
"s_waitcnt lgkmcnt(3)\nv_mfma_f32_16x16x32_bf16 v[104:107], v[44:47], v[192:195], v[104:107]\ns_add_u32 s53, s49, 640\nv_mfma_f32_16x16x32_bf16 v[108:111], v[44:47], v[196:199], v[108:111]\n"
"ds_read_b128 v[44:47], v33 offset:20480\nbuffer_load_dwordx4 v10, s[12:15], s53 offen lds\ns_waitcnt lgkmcnt(3)\nv_mfma_f32_16x16x32_bf16 v[96:99], v[48:51], v[200:203], v[96:99]\n"
"s_add_u32 m0, s52, 24576\nv_mfma_f32_16x16x32_bf16 v[100:103], v[48:51], v[204:207], v[100:103]\nds_read_b128 v[48:51], v34 offset:20480\ns_add_u32 s53, s49, 768\n"
"s_waitcnt lgkmcnt(3)\nv_mfma_f32_16x16x32_bf16 v[104:107], v[52:55], v[200:203], v[104:107]\nbuffer_load_dwordx4 v10, s[12:15], s53 offen lds\nv_mfma_f32_16x16x32_bf16 v[108:111], v[52:55], v[204:207], v[108:111]\n"
"ds_read_b128 v[52:55], v35 offset:20480\ns_waitcnt lgkmcnt(3)\nv_mfma_f32_16x16x32_bf16 v[96:99], v[40:43], v[208:211], v[96:99]\nv_mfma_f32_16x16x32_bf16 v[100:103], v[40:43], v[212:215], v[100:103]\n"
"ds_read_b128 v[40:43], v32 offset:24576\ns_waitcnt lgkmcnt(3)\nv_mfma_f32_16x16x32_bf16 v[104:107], v[44:47], v[208:211], v[104:107]\nv_mfma_f32_16x16x32_bf16 v[108:111], v[44:47], v[212:215], v[108:111]\n"
"ds_read_b128 v[44:47], v33 offset:24576\ns_waitcnt lgkmcnt(3)\nv_mfma_f32_16x16x32_bf16 v[96:99], v[48:51], v[216:219], v[96:99]\ns_add_u32 s54, s51, 32768\n"
"s_cmp_eq_u32 s54, 163840\nv_mfma_f32_16x16x32_bf16 v[100:103], v[48:51], v[220:223], v[100:103]\ns_cselect_b32 s54, 0, s54\ns_sub_u32 s54, s54, s51\n"
"ds_read_b128 v[48:51], v34 offset:24576\ns_waitcnt lgkmcnt(3)\nv_mfma_f32_16x16x32_bf16 v[104:107], v[52:55], v[216:219], v[104:107]\nv_mfma_f32_16x16x32_bf16 v[108:111], v[52:55], v[220:223], v[108:111]\n"
"ds_read_b128 v[52:55], v35 offset:24576\ns_waitcnt lgkmcnt(3)\nv_mfma_f32_16x16x32_bf16 v[96:99], v[40:43], v[224:227], v[96:99]\nv_add_u32 v32, s54, v32\n"
"v_mfma_f32_16x16x32_bf16 v[100:103], v[40:43], v[228:231], v[100:103]\nds_read_b128 v[40:43], v32 offset:0\ns_waitcnt lgkmcnt(3)\nv_mfma_f32_16x16x32_bf16 v[104:107], v[44:47], v[224:227], v[104:107]\n"
"v_add_u32 v33, s54, v33\nv_mfma_f32_16x16x32_bf16 v[108:111], v[44:47], v[228:231], v[108:111]\nds_read_b128 v[44:47], v33 offset:0\ns_waitcnt lgkmcnt(3)\n"
"v_mfma_f32_16x16x32_bf16 v[96:99], v[48:51], v[232:235], v[96:99]\nv_add_u32 v34, s54, v34\nv_mfma_f32_16x16x32_bf16 v[100:103], v[48:51], v[236:239], v[100:103]\nds_read_b128 v[48:51], v34 offset:0\n"
"s_waitcnt lgkmcnt(3)\nv_mfma_f32_16x16x32_bf16 v[104:107], v[52:55], v[232:235], v[104:107]\nv_add_u32 v35, s54, v35\nv_mfma_f32_16x16x32_bf16 v[108:111], v[52:55], v[236:239], v[108:111]\n"
"ds_read_b128 v[52:55], v35 offset:0\ns_mov_b32 s52, s47\ns_add_u32 s47, s51, s32\ns_add_u32 s51, s51, s54\n"
"s_add_u32 s49, s49, 28672\ns_waitcnt vmcnt(7)\ns_barrier\ns_waitcnt lgkmcnt(3)\n"
"v_mfma_f32_16x16x32_bf16 v[64:67], v[40:43], v[128:131], 0\nv_mov_b32 v36, s48\ns_add_u32 s48, s48, 1\ns_nop 0\n"
"buffer_store_dword v36, v19, s[24:27], s33 offen sc1\nv_mfma_f32_16x16x32_bf16 v[68:71], v[40:43], v[132:135], 0\nds_read_b128 v[40:43], v32 offset:4096\ns_waitcnt lgkmcnt(3)\n"
"v_mfma_f32_16x16x32_bf16 v[72:75], v[44:47], v[128:131], 0\nv_mfma_f32_16x16x32_bf16 v[76:79], v[44:47], v[132:135], 0\nds_read_b128 v[44:47], v33 offset:4096\nv_cvt_pk_bf16_f32 v56, v96, v97\n"
"s_waitcnt lgkmcnt(3)\nv_mfma_f32_16x16x32_bf16 v[64:67], v[48:51], v[136:139], v[64:67]\nv_cvt_pk_bf16_f32 v57, v98, v99\nv_mfma_f32_16x16x32_bf16 v[68:71], v[48:51], v[140:143], v[68:71]\n"
"ds_read_b128 v[48:51], v34 offset:4096\nv_cvt_pk_bf16_f32 v58, v104, v105\ns_waitcnt lgkmcnt(3)\nv_mfma_f32_16x16x32_bf16 v[72:75], v[52:55], v[136:139], v[72:75]\n"
"v_cvt_pk_bf16_f32 v59, v106, v107\nv_mfma_f32_16x16x32_bf16 v[76:79], v[52:55], v[140:143], v[76:79]\nds_read_b128 v[52:55], v35 offset:4096\ns_waitcnt lgkmcnt(3)\n"
"v_mfma_f32_16x16x32_bf16 v[64:67], v[40:43], v[144:147], v[64:67]\nv_mfma_f32_16x16x32_bf16 v[68:71], v[40:43], v[148:151], v[68:71]\nds_read_b128 v[40:43], v32 offset:8192\nbuffer_store_dwordx4 v[56:59], v15, s[20:23], s50 offen sc1\n"
"s_waitcnt lgkmcnt(3)\nv_mfma_f32_16x16x32_bf16 v[72:75], v[44:47], v[144:147], v[72:75]\nv_cvt_pk_bf16_f32 v60, v100, v101\nv_mfma_f32_16x16x32_bf16 v[76:79], v[44:47], v[148:151], v[76:79]\n"
"ds_read_b128 v[44:47], v33 offset:8192\nv_cvt_pk_bf16_f32 v61, v102, v103\ns_waitcnt lgkmcnt(3)\nv_mfma_f32_16x16x32_bf16 v[64:67], v[48:51], v[152:155], v[64:67]\n"
"v_cvt_pk_bf16_f32 v62, v108, v109\nv_mfma_f32_16x16x32_bf16 v[68:71], v[48:51], v[156:159], v[68:71]\nds_read_b128 v[48:51], v34 offset:8192\nv_cvt_pk_bf16_f32 v63, v110, v111\n"
"s_waitcnt lgkmcnt(3)\nv_mfma_f32_16x16x32_bf16 v[72:75], v[52:55], v[152:155], v[72:75]\nv_mfma_f32_16x16x32_bf16 v[76:79], v[52:55], v[156:159], v[76:79]\nds_read_b128 v[52:55], v35 offset:8192\n"
"s_waitcnt lgkmcnt(3)\nv_mfma_f32_16x16x32_bf16 v[64:67], v[40:43], v[160:163], v[64:67]\nbuffer_store_dwordx4 v[60:63], v16, s[20:23], s50 offen sc1\nv_mfma_f32_16x16x32_bf16 v[68:71], v[40:43], v[164:167], v[68:71]\n"
"ds_read_b128 v[40:43], v32 offset:12288\ns_add_u32 s50, s50, s36\ns_waitcnt lgkmcnt(3)\nv_mfma_f32_16x16x32_bf16 v[72:75], v[44:47], v[160:163], v[72:75]\n"
"s_add_u32 m0, s52, 0\nv_mfma_f32_16x16x32_bf16 v[76:79], v[44:47], v[164:167], v[76:79]\nds_read_b128 v[44:47], v33 offset:12288\ns_add_u32 s53, s49, 0\n"
"s_waitcnt lgkmcnt(3)\nv_mfma_f32_16x16x32_bf16 v[64:67], v[48:51], v[168:171], v[64:67]\nbuffer_load_dwordx4 v10, s[12:15], s53 offen lds\nv_mfma_f32_16x16x32_bf16 v[68:71], v[48:51], v[172:175], v[68:71]\n"
"ds_read_b128 v[48:51], v34 offset:12288\ns_add_u32 m0, s52, 4096\ns_waitcnt lgkmcnt(3)\nv_mfma_f32_16x16x32_bf16 v[72:75], v[52:55], v[168:171], v[72:75]\n"
"s_add_u32 s53, s49, 128\nv_mfma_f32_16x16x32_bf16 v[76:79], v[52:55], v[172:175], v[76:79]\nds_read_b128 v[52:55], v35 offset:12288\nbuffer_load_dwordx4 v10, s[12:15], s53 offen lds\n"
"s_waitcnt lgkmcnt(3)\nv_mfma_f32_16x16x32_bf16 v[64:67], v[40:43], v[176:179], v[64:67]\ns_add_u32 m0, s52, 8192\nv_mfma_f32_16x16x32_bf16 v[68:71], v[40:43], v[180:183], v[68:71]\n"
"ds_read_b128 v[40:43], v32 offset:16384\ns_add_u32 s53, s49, 256\ns_waitcnt lgkmcnt(3)\nv_mfma_f32_16x16x32_bf16 v[72:75], v[44:47], v[176:179], v[72:75]\n"
"buffer_load_dwordx4 v10, s[12:15], s53 offen lds\nv_mfma_f32_16x16x32_bf16 v[76:79], v[44:47], v[180:183], v[76:79]\nds_read_b128 v[44:47], v33 offset:16384\ns_add_u32 m0, s52, 12288\n"
"s_waitcnt lgkmcnt(3)\nv_mfma_f32_16x16x32_bf16 v[64:67], v[48:51], v[184:187], v[64:67]\ns_add_u32 s53, s49, 384\nv_mfma_f32_16x16x32_bf16 v[68:71], v[48:51], v[188:191], v[68:71]\n"
"ds_read_b128 v[48:51], v34 offset:16384\nbuffer_load_dwordx4 v10, s[12:15], s53 offen lds\ns_waitcnt lgkmcnt(3)\nv_mfma_f32_16x16x32_bf16 v[72:75], v[52:55], v[184:187], v[72:75]\n"
"s_add_u32 m0, s52, 16384\nv_mfma_f32_16x16x32_bf16 v[76:79], v[52:55], v[188:191], v[76:79]\nds_read_b128 v[52:55], v35 offset:16384\ns_add_u32 s53, s49, 512\n"
"s_waitcnt lgkmcnt(3)\nv_mfma_f32_16x16x32_bf16 v[64:67], v[40:43], v[192:195], v[64:67]\nbuffer_load_dwordx4 v10, s[12:15], s53 offen lds\nv_mfma_f32_16x16x32_bf16 v[68:71], v[40:43], v[196:199], v[68:71]\n"
"ds_read_b128 v[40:43], v32 offset:20480\ns_add_u32 m0, s52, 20480\ns_waitcnt lgkmcnt(3)\nv_mfma_f32_16x16x32_bf16 v[72:75], v[44:47], v[192:195], v[72:75]\n"
"s_add_u32 s53, s49, 640\nv_mfma_f32_16x16x32_bf16 v[76:79], v[44:47], v[196:199], v[76:79]\nds_read_b128 v[44:47], v33 offset:20480\nbuffer_load_dwordx4 v10, s[12:15], s53 offen lds\n"
"s_waitcnt lgkmcnt(3)\nv_mfma_f32_16x16x32_bf16 v[64:67], v[48:51], v[200:203], v[64:67]\ns_add_u32 m0, s52, 24576\nv_mfma_f32_16x16x32_bf16 v[68:71], v[48:51], v[204:207], v[68:71]\n"
"ds_read_b128 v[48:51], v34 offset:20480\ns_add_u32 s53, s49, 768\ns_waitcnt lgkmcnt(3)\nv_mfma_f32_16x16x32_bf16 v[72:75], v[52:55], v[200:203], v[72:75]\n"
"buffer_load_dwordx4 v10, s[12:15], s53 offen lds\nv_mfma_f32_16x16x32_bf16 v[76:79], v[52:55], v[204:207], v[76:79]\nds_read_b128 v[52:55], v35 offset:20480\ns_waitcnt lgkmcnt(3)\n"
"v_mfma_f32_16x16x32_bf16 v[64:67], v[40:43], v[208:211], v[64:67]\nv_mfma_f32_16x16x32_bf16 v[68:71], v[40:43], v[212:215], v[68:71]\nds_read_b128 v[40:43], v32 offset:24576\ns_waitcnt lgkmcnt(3)\n"
"v_mfma_f32_16x16x32_bf16 v[72:75], v[44:47], v[208:211], v[72:75]\nv_mfma_f32_16x16x32_bf16 v[76:79], v[44:47], v[212:215], v[76:79]\nds_read_b128 v[44:47], v33 offset:24576\ns_waitcnt lgkmcnt(3)\n"
"v_mfma_f32_16x16x32_bf16 v[64:67], v[48:51], v[216:219], v[64:67]\ns_add_u32 s54, s51, 32768\ns_cmp_eq_u32 s54, 163840\nv_mfma_f32_16x16x32_bf16 v[68:71], v[48:51], v[220:223], v[68:71]\n"
"s_cselect_b32 s54, 0, s54\ns_sub_u32 s54, s54, s51\nds_read_b128 v[48:51], v34 offset:24576\ns_waitcnt lgkmcnt(3)\n"
"v_mfma_f32_16x16x32_bf16 v[72:75], v[52:55], v[216:219], v[72:75]\nv_mfma_f32_16x16x32_bf16 v[76:79], v[52:55], v[220:223], v[76:79]\nds_read_b128 v[52:55], v35 offset:24576\ns_waitcnt lgkmcnt(3)\n"
"v_mfma_f32_16x16x32_bf16 v[64:67], v[40:43], v[224:227], v[64:67]\nv_add_u32 v32, s54, v32\nv_mfma_f32_16x16x32_bf16 v[68:71], v[40:43], v[228:231], v[68:71]\nds_read_b128 v[40:43], v32 offset:0\n"
"s_waitcnt lgkmcnt(3)\nv_mfma_f32_16x16x32_bf16 v[72:75], v[44:47], v[224:227], v[72:75]\nv_add_u32 v33, s54, v33\nv_mfma_f32_16x16x32_bf16 v[76:79], v[44:47], v[228:231], v[76:79]\n"
"ds_read_b128 v[44:47], v33 offset:0\ns_waitcnt lgkmcnt(3)\nv_mfma_f32_16x16x32_bf16 v[64:67], v[48:51], v[232:235], v[64:67]\nv_add_u32 v34, s54, v34\n"
"v_mfma_f32_16x16x32_bf16 v[68:71], v[48:51], v[236:239], v[68:71]\nds_read_b128 v[48:51], v34 offset:0\ns_waitcnt lgkmcnt(3)\nv_mfma_f32_16x16x32_bf16 v[72:75], v[52:55], v[232:235], v[72:75]\n"
"v_add_u32 v35, s54, v35\nv_mfma_f32_16x16x32_bf16 v[76:79], v[52:55], v[236:239], v[76:79]\nds_read_b128 v[52:55], v35 offset:0\ns_mov_b32 s52, s47\n"
"s_add_u32 s47, s51, s32\ns_add_u32 s51, s51, s54\ns_add_u32 s49, s49, 28672\ns_waitcnt vmcnt(7)\n"
"s_barrier\ns_waitcnt lgkmcnt(3)\nv_mfma_f32_16x16x32_bf16 v[96:99], v[40:43], v[128:131], 0\nv_mov_b32 v36, s48\n"
"s_add_u32 s48, s48, 1\ns_nop 0\nbuffer_store_dword v36, v19, s[24:27], s33 offen sc1\nv_mfma_f32_16x16x32_bf16 v[100:103], v[40:43], v[132:135], 0\n"
"ds_read_b128 v[40:43], v32 offset:4096\ns_waitcnt lgkmcnt(3)\nv_mfma_f32_16x16x32_bf16 v[104:107], v[44:47], v[128:131], 0\nv_mfma_f32_16x16x32_bf16 v[108:111], v[44:47], v[132:135], 0\n"
"ds_read_b128 v[44:47], v33 offset:4096\nv_cvt_pk_bf16_f32 v56, v64, v65\ns_waitcnt lgkmcnt(3)\nv_mfma_f32_16x16x32_bf16 v[96:99], v[48:51], v[136:139], v[96:99]\n"
"v_cvt_pk_bf16_f32 v57, v66, v67\nv_mfma_f32_16x16x32_bf16 v[100:103], v[48:51], v[140:143], v[100:103]\nds_read_b128 v[48:51], v34 offset:4096\nv_cvt_pk_bf16_f32 v58, v72, v73\n"
"s_waitcnt lgkmcnt(3)\nv_mfma_f32_16x16x32_bf16 v[104:107], v[52:55], v[136:139], v[104:107]\nv_cvt_pk_bf16_f32 v59, v74, v75\nv_mfma_f32_16x16x32_bf16 v[108:111], v[52:55], v[140:143], v[108:111]\n"
"ds_read_b128 v[52:55], v35 offset:4096\ns_waitcnt lgkmcnt(3)\nv_mfma_f32_16x16x32_bf16 v[96:99], v[40:43], v[144:147], v[96:99]\nv_mfma_f32_16x16x32_bf16 v[100:103], v[40:43], v[148:151], v[100:103]\n"
"ds_read_b128 v[40:43], v32 offset:8192\nbuffer_store_dwordx4 v[56:59], v15, s[20:23], s50 offen sc1\ns_waitcnt lgkmcnt(3)\nv_mfma_f32_16x16x32_bf16 v[104:107], v[44:47], v[144:147], v[104:107]\n"
"v_cvt_pk_bf16_f32 v60, v68, v69\nv_mfma_f32_16x16x32_bf16 v[108:111], v[44:47], v[148:151], v[108:111]\nds_read_b128 v[44:47], v33 offset:8192\nv_cvt_pk_bf16_f32 v61, v70, v71\n"
"s_waitcnt lgkmcnt(3)\nv_mfma_f32_16x16x32_bf16 v[96:99], v[48:51], v[152:155], v[96:99]\nv_cvt_pk_bf16_f32 v62, v76, v77\nv_mfma_f32_16x16x32_bf16 v[100:103], v[48:51], v[156:159], v[100:103]\n"
"ds_read_b128 v[48:51], v34 offset:8192\nv_cvt_pk_bf16_f32 v63, v78, v79\ns_waitcnt lgkmcnt(3)\nv_mfma_f32_16x16x32_bf16 v[104:107], v[52:55], v[152:155], v[104:107]\n"
"v_mfma_f32_16x16x32_bf16 v[108:111], v[52:55], v[156:159], v[108:111]\nds_read_b128 v[52:55], v35 offset:8192\ns_waitcnt lgkmcnt(3)\nv_mfma_f32_16x16x32_bf16 v[96:99], v[40:43], v[160:163], v[96:99]\n"
"buffer_store_dwordx4 v[60:63], v16, s[20:23], s50 offen sc1\nv_mfma_f32_16x16x32_bf16 v[100:103], v[40:43], v[164:167], v[100:103]\nds_read_b128 v[40:43], v32 offset:12288\ns_add_u32 s50, s50, s36\n"
"s_waitcnt lgkmcnt(3)\nv_mfma_f32_16x16x32_bf16 v[104:107], v[44:47], v[160:163], v[104:107]\ns_add_u32 m0, s52, 0\nv_mfma_f32_16x16x32_bf16 v[108:111], v[44:47], v[164:167], v[108:111]\n"
"ds_read_b128 v[44:47], v33 offset:12288\ns_add_u32 s53, s49, 0\ns_waitcnt lgkmcnt(3)\nv_mfma_f32_16x16x32_bf16 v[96:99], v[48:51], v[168:171], v[96:99]\n"
"buffer_load_dwordx4 v10, s[12:15], s53 offen lds\nv_mfma_f32_16x16x32_bf16 v[100:103], v[48:51], v[172:175], v[100:103]\nds_read_b128 v[48:51], v34 offset:12288\ns_add_u32 m0, s52, 4096\n"
"s_waitcnt lgkmcnt(3)\nv_mfma_f32_16x16x32_bf16 v[104:107], v[52:55], v[168:171], v[104:107]\ns_add_u32 s53, s49, 128\nv_mfma_f32_16x16x32_bf16 v[108:111], v[52:55], v[172:175], v[108:111]\n"
"ds_read_b128 v[52:55], v35 offset:12288\nbuffer_load_dwordx4 v10, s[12:15], s53 offen lds\ns_waitcnt lgkmcnt(3)\nv_mfma_f32_16x16x32_bf16 v[96:99], v[40:43], v[176:179], v[96:99]\n"
"s_add_u32 m0, s52, 8192\nv_mfma_f32_16x16x32_bf16 v[100:103], v[40:43], v[180:183], v[100:103]\nds_read_b128 v[40:43], v32 offset:16384\ns_add_u32 s53, s49, 256\n"
"s_waitcnt lgkmcnt(3)\nv_mfma_f32_16x16x32_bf16 v[104:107], v[44:47], v[176:179], v[104:107]\nbuffer_load_dwordx4 v10, s[12:15], s53 offen lds\nv_mfma_f32_16x16x32_bf16 v[108:111], v[44:47], v[180:183], v[108:111]\n"
"ds_read_b128 v[44:47], v33 offset:16384\ns_add_u32 m0, s52, 12288\ns_waitcnt lgkmcnt(3)\nv_mfma_f32_16x16x32_bf16 v[96:99], v[48:51], v[184:187], v[96:99]\n"
"s_add_u32 s53, s49, 384\nv_mfma_f32_16x16x32_bf16 v[100:103], v[48:51], v[188:191], v[100:103]\nds_read_b128 v[48:51], v34 offset:16384\nbuffer_load_dwordx4 v10, s[12:15], s53 offen lds\n"
"s_waitcnt lgkmcnt(3)\nv_mfma_f32_16x16x32_bf16 v[104:107], v[52:55], v[184:187], v[104:107]\ns_add_u32 m0, s52, 16384\nv_mfma_f32_16x16x32_bf16 v[108:111], v[52:55], v[188:191], v[108:111]\n"
"ds_read_b128 v[52:55], v35 offset:16384\ns_add_u32 s53, s49, 512\ns_waitcnt lgkmcnt(3)\nv_mfma_f32_16x16x32_bf16 v[96:99], v[40:43], v[192:195], v[96:99]\n"
"buffer_load_dwordx4 v10, s[12:15], s53 offen lds\nv_mfma_f32_16x16x32_bf16 v[100:103], v[40:43], v[196:199], v[100:103]\nds_read_b128 v[40:43], v32 offset:20480\ns_add_u32 m0, s52, 20480\n"
"s_waitcnt lgkmcnt(3)\nv_mfma_f32_16x16x32_bf16 v[104:107], v[44:47], v[192:195], v[104:107]\ns_add_u32 s53, s49, 640\nv_mfma_f32_16x16x32_bf16 v[108:111], v[44:47], v[196:199], v[108:111]\n"
"ds_read_b128 v[44:47], v33 offset:20480\nbuffer_load_dwordx4 v10, s[12:15], s53 offen lds\ns_waitcnt lgkmcnt(3)\nv_mfma_f32_16x16x32_bf16 v[96:99], v[48:51], v[200:203], v[96:99]\n"
"s_add_u32 m0, s52, 24576\nv_mfma_f32_16x16x32_bf16 v[100:103], v[48:51], v[204:207], v[100:103]\nds_read_b128 v[48:51], v34 offset:20480\ns_add_u32 s53, s49, 768\n"
"s_waitcnt lgkmcnt(3)\nv_mfma_f32_16x16x32_bf16 v[104:107], v[52:55], v[200:203], v[104:107]\nbuffer_load_dwordx4 v10, s[12:15], s53 offen lds\nv_mfma_f32_16x16x32_bf16 v[108:111], v[52:55], v[204:207], v[108:111]\n"
"ds_read_b128 v[52:55], v35 offset:20480\ns_waitcnt lgkmcnt(3)\nv_mfma_f32_16x16x32_bf16 v[96:99], v[40:43], v[208:211], v[96:99]\nv_mfma_f32_16x16x32_bf16 v[100:103], v[40:43], v[212:215], v[100:103]\n"
"ds_read_b128 v[40:43], v32 offset:24576\ns_waitcnt lgkmcnt(3)\nv_mfma_f32_16x16x32_bf16 v[104:107], v[44:47], v[208:211], v[104:107]\nv_mfma_f32_16x16x32_bf16 v[108:111], v[44:47], v[212:215], v[108:111]\n"
"ds_read_b128 v[44:47], v33 offset:24576\ns_waitcnt lgkmcnt(3)\nv_mfma_f32_16x16x32_bf16 v[96:99], v[48:51], v[216:219], v[96:99]\ns_add_u32 s54, s51, 32768\n"
"s_cmp_eq_u32 s54, 163840\nv_mfma_f32_16x16x32_bf16 v[100:103], v[48:51], v[220:223], v[100:103]\ns_cselect_b32 s54, 0, s54\ns_sub_u32 s54, s54, s51\n"
"ds_read_b128 v[48:51], v34 offset:24576\ns_waitcnt lgkmcnt(3)\nv_mfma_f32_16x16x32_bf16 v[104:107], v[52:55], v[216:219], v[104:107]\nv_mfma_f32_16x16x32_bf16 v[108:111], v[52:55], v[220:223], v[108:111]\n"
"ds_read_b128 v[52:55], v35 offset:24576\ns_waitcnt lgkmcnt(3)\nv_mfma_f32_16x16x32_bf16 v[96:99], v[40:43], v[224:227], v[96:99]\nv_add_u32 v32, s54, v32\n"
"v_mfma_f32_16x16x32_bf16 v[100:103], v[40:43], v[228:231], v[100:103]\nds_read_b128 v[40:43], v32 offset:0\ns_waitcnt lgkmcnt(3)\nv_mfma_f32_16x16x32_bf16 v[104:107], v[44:47], v[224:227], v[104:107]\n"
"v_add_u32 v33, s54, v33\nv_mfma_f32_16x16x32_bf16 v[108:111], v[44:47], v[228:231], v[108:111]\nds_read_b128 v[44:47], v33 offset:0\ns_waitcnt lgkmcnt(3)\n"
"v_mfma_f32_16x16x32_bf16 v[96:99], v[48:51], v[232:235], v[96:99]\nv_add_u32 v34, s54, v34\nv_mfma_f32_16x16x32_bf16 v[100:103], v[48:51], v[236:239], v[100:103]\nds_read_b128 v[48:51], v34 offset:0\n"
"s_waitcnt lgkmcnt(3)\nv_mfma_f32_16x16x32_bf16 v[104:107], v[52:55], v[232:235], v[104:107]\nv_add_u32 v35, s54, v35\nv_mfma_f32_16x16x32_bf16 v[108:111], v[52:55], v[236:239], v[108:111]\n"
"ds_read_b128 v[52:55], v35 offset:0\ns_mov_b32 s52, s47\ns_add_u32 s47, s51, s32\ns_add_u32 s51, s51, s54\n"
"s_add_u32 s49, s49, 28672\ns_waitcnt vmcnt(7)\ns_barrier\ns_waitcnt lgkmcnt(3)\n"
"v_mfma_f32_16x16x32_bf16 v[64:67], v[40:43], v[128:131], 0\nv_mov_b32 v36, s48\ns_add_u32 s48, s48, 1\ns_nop 0\n"
"buffer_store_dword v36, v19, s[24:27], s33 offen sc1\nv_mfma_f32_16x16x32_bf16 v[68:71], v[40:43], v[132:135], 0\nds_read_b128 v[40:43], v32 offset:4096\ns_waitcnt lgkmcnt(3)\n"
"v_mfma_f32_16x16x32_bf16 v[72:75], v[44:47], v[128:131], 0\nv_mfma_f32_16x16x32_bf16 v[76:79], v[44:47], v[132:135], 0\nds_read_b128 v[44:47], v33 offset:4096\nv_cvt_pk_bf16_f32 v56, v96, v97\n"
"s_waitcnt lgkmcnt(3)\nv_mfma_f32_16x16x32_bf16 v[64:67], v[48:51], v[136:139], v[64:67]\nv_cvt_pk_bf16_f32 v57, v98, v99\nv_mfma_f32_16x16x32_bf16 v[68:71], v[48:51], v[140:143], v[68:71]\n"
"ds_read_b128 v[48:51], v34 offset:4096\nv_cvt_pk_bf16_f32 v58, v104, v105\ns_waitcnt lgkmcnt(3)\nv_mfma_f32_16x16x32_bf16 v[72:75], v[52:55], v[136:139], v[72:75]\n"
"v_cvt_pk_bf16_f32 v59, v106, v107\nv_mfma_f32_16x16x32_bf16 v[76:79], v[52:55], v[140:143], v[76:79]\nds_read_b128 v[52:55], v35 offset:4096\ns_waitcnt lgkmcnt(3)\n"
"v_mfma_f32_16x16x32_bf16 v[64:67], v[40:43], v[144:147], v[64:67]\nv_mfma_f32_16x16x32_bf16 v[68:71], v[40:43], v[148:151], v[68:71]\nds_read_b128 v[40:43], v32 offset:8192\nbuffer_store_dwordx4 v[56:59], v15, s[20:23], s50 offen sc1\n"
"s_waitcnt lgkmcnt(3)\nv_mfma_f32_16x16x32_bf16 v[72:75], v[44:47], v[144:147], v[72:75]\nv_cvt_pk_bf16_f32 v60, v100, v101\nv_mfma_f32_16x16x32_bf16 v[76:79], v[44:47], v[148:151], v[76:79]\n"
"ds_read_b128 v[44:47], v33 offset:8192\nv_cvt_pk_bf16_f32 v61, v102, v103\ns_waitcnt lgkmcnt(3)\nv_mfma_f32_16x16x32_bf16 v[64:67], v[48:51], v[152:155], v[64:67]\n"
"v_cvt_pk_bf16_f32 v62, v108, v109\nv_mfma_f32_16x16x32_bf16 v[68:71], v[48:51], v[156:159], v[68:71]\nds_read_b128 v[48:51], v34 offset:8192\nv_cvt_pk_bf16_f32 v63, v110, v111\n"
"s_waitcnt lgkmcnt(3)\nv_mfma_f32_16x16x32_bf16 v[72:75], v[52:55], v[152:155], v[72:75]\nv_mfma_f32_16x16x32_bf16 v[76:79], v[52:55], v[156:159], v[76:79]\nds_read_b128 v[52:55], v35 offset:8192\n"
"s_waitcnt lgkmcnt(3)\nv_mfma_f32_16x16x32_bf16 v[64:67], v[40:43], v[160:163], v[64:67]\nbuffer_store_dwordx4 v[60:63], v16, s[20:23], s50 offen sc1\nv_mfma_f32_16x16x32_bf16 v[68:71], v[40:43], v[164:167], v[68:71]\n"
"ds_read_b128 v[40:43], v32 offset:12288\ns_add_u32 s50, s50, s36\ns_waitcnt lgkmcnt(3)\nv_mfma_f32_16x16x32_bf16 v[72:75], v[44:47], v[160:163], v[72:75]\n"
"s_add_u32 m0, s52, 0\nv_mfma_f32_16x16x32_bf16 v[76:79], v[44:47], v[164:167], v[76:79]\nds_read_b128 v[44:47], v33 offset:12288\ns_add_u32 s53, s49, 0\n"
"s_waitcnt lgkmcnt(3)\nv_mfma_f32_16x16x32_bf16 v[64:67], v[48:51], v[168:171], v[64:67]\nbuffer_load_dwordx4 v10, s[12:15], s53 offen lds\nv_mfma_f32_16x16x32_bf16 v[68:71], v[48:51], v[172:175], v[68:71]\n"
"ds_read_b128 v[48:51], v34 offset:12288\ns_add_u32 m0, s52, 4096\ns_waitcnt lgkmcnt(3)\nv_mfma_f32_16x16x32_bf16 v[72:75], v[52:55], v[168:171], v[72:75]\n"
"s_add_u32 s53, s49, 128\nv_mfma_f32_16x16x32_bf16 v[76:79], v[52:55], v[172:175], v[76:79]\nds_read_b128 v[52:55], v35 offset:12288\nbuffer_load_dwordx4 v10, s[12:15], s53 offen lds\n"
"s_waitcnt lgkmcnt(3)\nv_mfma_f32_16x16x32_bf16 v[64:67], v[40:43], v[176:179], v[64:67]\ns_add_u32 m0, s52, 8192\nv_mfma_f32_16x16x32_bf16 v[68:71], v[40:43], v[180:183], v[68:71]\n"
"ds_read_b128 v[40:43], v32 offset:16384\ns_add_u32 s53, s49, 256\ns_waitcnt lgkmcnt(3)\nv_mfma_f32_16x16x32_bf16 v[72:75], v[44:47], v[176:179], v[72:75]\n"
"buffer_load_dwordx4 v10, s[12:15], s53 offen lds\nv_mfma_f32_16x16x32_bf16 v[76:79], v[44:47], v[180:183], v[76:79]\nds_read_b128 v[44:47], v33 offset:16384\ns_add_u32 m0, s52, 12288\n"
"s_waitcnt lgkmcnt(3)\nv_mfma_f32_16x16x32_bf16 v[64:67], v[48:51], v[184:187], v[64:67]\ns_add_u32 s53, s49, 384\nv_mfma_f32_16x16x32_bf16 v[68:71], v[48:51], v[188:191], v[68:71]\n"
"ds_read_b128 v[48:51], v34 offset:16384\nbuffer_load_dwordx4 v10, s[12:15], s53 offen lds\ns_waitcnt lgkmcnt(3)\nv_mfma_f32_16x16x32_bf16 v[72:75], v[52:55], v[184:187], v[72:75]\n"
"s_add_u32 m0, s52, 16384\nv_mfma_f32_16x16x32_bf16 v[76:79], v[52:55], v[188:191], v[76:79]\nds_read_b128 v[52:55], v35 offset:16384\ns_add_u32 s53, s49, 512\n"
"s_waitcnt lgkmcnt(3)\nv_mfma_f32_16x16x32_bf16 v[64:67], v[40:43], v[192:195], v[64:67]\nbuffer_load_dwordx4 v10, s[12:15], s53 offen lds\nv_mfma_f32_16x16x32_bf16 v[68:71], v[40:43], v[196:199], v[68:71]\n"
"ds_read_b128 v[40:43], v32 offset:20480\ns_add_u32 m0, s52, 20480\ns_waitcnt lgkmcnt(3)\nv_mfma_f32_16x16x32_bf16 v[72:75], v[44:47], v[192:195], v[72:75]\n"
"s_add_u32 s53, s49, 640\nv_mfma_f32_16x16x32_bf16 v[76:79], v[44:47], v[196:199], v[76:79]\nds_read_b128 v[44:47], v33 offset:20480\nbuffer_load_dwordx4 v10, s[12:15], s53 offen lds\n"
"s_waitcnt lgkmcnt(3)\nv_mfma_f32_16x16x32_bf16 v[64:67], v[48:51], v[200:203], v[64:67]\ns_add_u32 m0, s52, 24576\nv_mfma_f32_16x16x32_bf16 v[68:71], v[48:51], v[204:207], v[68:71]\n"
"ds_read_b128 v[48:51], v34 offset:20480\ns_add_u32 s53, s49, 768\ns_waitcnt lgkmcnt(3)\nv_mfma_f32_16x16x32_bf16 v[72:75], v[52:55], v[200:203], v[72:75]\n"
"buffer_load_dwordx4 v10, s[12:15], s53 offen lds\nv_mfma_f32_16x16x32_bf16 v[76:79], v[52:55], v[204:207], v[76:79]\nds_read_b128 v[52:55], v35 offset:20480\nglobal_load_dwordx4 v[240:243], v23, s[38:39] offset:0\n"
"s_waitcnt lgkmcnt(3)\nv_mfma_f32_16x16x32_bf16 v[64:67], v[40:43], v[208:211], v[64:67]\nv_mfma_f32_16x16x32_bf16 v[68:71], v[40:43], v[212:215], v[68:71]\nds_read_b128 v[40:43], v32 offset:24576\n"
"s_waitcnt lgkmcnt(3)\nv_mfma_f32_16x16x32_bf16 v[72:75], v[44:47], v[208:211], v[72:75]\nv_mfma_f32_16x16x32_bf16 v[76:79], v[44:47], v[212:215], v[76:79]\nds_read_b128 v[44:47], v33 offset:24576\n"
"s_waitcnt lgkmcnt(3)\nv_mfma_f32_16x16x32_bf16 v[64:67], v[48:51], v[216:219], v[64:67]\ns_add_u32 s54, s51, 32768\ns_cmp_eq_u32 s54, 163840\n"
"v_mfma_f32_16x16x32_bf16 v[68:71], v[48:51], v[220:223], v[68:71]\ns_cselect_b32 s54, 0, s54\ns_sub_u32 s54, s54, s51\nds_read_b128 v[48:51], v34 offset:24576\n"
"s_waitcnt lgkmcnt(3)\nv_mfma_f32_16x16x32_bf16 v[72:75], v[52:55], v[216:219], v[72:75]\nv_mfma_f32_16x16x32_bf16 v[76:79], v[52:55], v[220:223], v[76:79]\nds_read_b128 v[52:55], v35 offset:24576\n"
"s_waitcnt lgkmcnt(3)\nv_mfma_f32_16x16x32_bf16 v[64:67], v[40:43], v[224:227], v[64:67]\nv_add_u32 v32, s54, v32\nv_mfma_f32_16x16x32_bf16 v[68:71], v[40:43], v[228:231], v[68:71]\n"
"ds_read_b128 v[40:43], v32 offset:0\ns_waitcnt lgkmcnt(3)\nv_mfma_f32_16x16x32_bf16 v[72:75], v[44:47], v[224:227], v[72:75]\nv_add_u32 v33, s54, v33\n"
"v_mfma_f32_16x16x32_bf16 v[76:79], v[44:47], v[228:231], v[76:79]\nds_read_b128 v[44:47], v33 offset:0\ns_waitcnt lgkmcnt(3)\nv_mfma_f32_16x16x32_bf16 v[64:67], v[48:51], v[232:235], v[64:67]\n"
"v_add_u32 v34, s54, v34\nv_mfma_f32_16x16x32_bf16 v[68:71], v[48:51], v[236:239], v[68:71]\nds_read_b128 v[48:51], v34 offset:0\ns_waitcnt lgkmcnt(3)\n"
"v_mfma_f32_16x16x32_bf16 v[72:75], v[52:55], v[232:235], v[72:75]\nv_add_u32 v35, s54, v35\nv_mfma_f32_16x16x32_bf16 v[76:79], v[52:55], v[236:239], v[76:79]\nds_read_b128 v[52:55], v35 offset:0\n"
"s_mov_b32 s52, s47\ns_add_u32 s47, s51, s32\ns_add_u32 s51, s51, s54\ns_add_u32 s49, s49, 28672\n"
"s_waitcnt vmcnt(8)\ns_barrier\ns_waitcnt lgkmcnt(3)\nv_mfma_f32_16x16x32_bf16 v[96:99], v[40:43], v[128:131], 0\n"
"v_mov_b32 v36, s48\ns_add_u32 s48, s48, 1\ns_nop 0\nbuffer_store_dword v36, v19, s[24:27], s33 offen sc1\n"
"buffer_load_dword v37, v20, s[24:27], 0 offen sc1\nv_mfma_f32_16x16x32_bf16 v[100:103], v[40:43], v[132:135], 0\nds_read_b128 v[40:43], v32 offset:4096\ns_waitcnt lgkmcnt(3)\n"
"v_mfma_f32_16x16x32_bf16 v[104:107], v[44:47], v[128:131], 0\nv_mfma_f32_16x16x32_bf16 v[108:111], v[44:47], v[132:135], 0\nds_read_b128 v[44:47], v33 offset:4096\nv_cvt_pk_bf16_f32 v56, v64, v65\n"
"s_waitcnt lgkmcnt(3)\nv_mfma_f32_16x16x32_bf16 v[96:99], v[48:51], v[136:139], v[96:99]\nv_cvt_pk_bf16_f32 v57, v66, v67\nv_mfma_f32_16x16x32_bf16 v[100:103], v[48:51], v[140:143], v[100:103]\n"
"ds_read_b128 v[48:51], v34 offset:4096\nv_cvt_pk_bf16_f32 v58, v72, v73\ns_waitcnt lgkmcnt(3)\nv_mfma_f32_16x16x32_bf16 v[104:107], v[52:55], v[136:139], v[104:107]\n"
"v_cvt_pk_bf16_f32 v59, v74, v75\nv_mfma_f32_16x16x32_bf16 v[108:111], v[52:55], v[140:143], v[108:111]\nds_read_b128 v[52:55], v35 offset:4096\ns_waitcnt lgkmcnt(3)\n"
"v_mfma_f32_16x16x32_bf16 v[96:99], v[40:43], v[144:147], v[96:99]\nv_mfma_f32_16x16x32_bf16 v[100:103], v[40:43], v[148:151], v[100:103]\nds_read_b128 v[40:43], v32 offset:8192\nbuffer_store_dwordx4 v[56:59], v15, s[20:23], s50 offen sc1\n"
"s_waitcnt lgkmcnt(3)\nv_mfma_f32_16x16x32_bf16 v[104:107], v[44:47], v[144:147], v[104:107]\nv_cvt_pk_bf16_f32 v60, v68, v69\nv_mfma_f32_16x16x32_bf16 v[108:111], v[44:47], v[148:151], v[108:111]\n"
"ds_read_b128 v[44:47], v33 offset:8192\nv_cvt_pk_bf16_f32 v61, v70, v71\ns_waitcnt lgkmcnt(3)\nv_mfma_f32_16x16x32_bf16 v[96:99], v[48:51], v[152:155], v[96:99]\n"
"v_cvt_pk_bf16_f32 v62, v76, v77\nv_mfma_f32_16x16x32_bf16 v[100:103], v[48:51], v[156:159], v[100:103]\nds_read_b128 v[48:51], v34 offset:8192\nv_cvt_pk_bf16_f32 v63, v78, v79\n"
"s_waitcnt lgkmcnt(3)\nv_mfma_f32_16x16x32_bf16 v[104:107], v[52:55], v[152:155], v[104:107]\nv_mfma_f32_16x16x32_bf16 v[108:111], v[52:55], v[156:159], v[108:111]\nds_read_b128 v[52:55], v35 offset:8192\n"
"s_waitcnt lgkmcnt(3)\nv_mfma_f32_16x16x32_bf16 v[96:99], v[40:43], v[160:163], v[96:99]\nbuffer_store_dwordx4 v[60:63], v16, s[20:23], s50 offen sc1\nv_mfma_f32_16x16x32_bf16 v[100:103], v[40:43], v[164:167], v[100:103]\n"
"ds_read_b128 v[40:43], v32 offset:12288\ns_add_u32 s50, s50, s36\ns_waitcnt lgkmcnt(3)\nv_mfma_f32_16x16x32_bf16 v[104:107], v[44:47], v[160:163], v[104:107]\n"
"s_add_u32 m0, s52, 0\nv_mfma_f32_16x16x32_bf16 v[108:111], v[44:47], v[164:167], v[108:111]\nds_read_b128 v[44:47], v33 offset:12288\ns_add_u32 s53, s49, 0\n"
"s_waitcnt lgkmcnt(3)\nv_mfma_f32_16x16x32_bf16 v[96:99], v[48:51], v[168:171], v[96:99]\nbuffer_load_dwordx4 v10, s[12:15], s53 offen lds\nv_mfma_f32_16x16x32_bf16 v[100:103], v[48:51], v[172:175], v[100:103]\n"
"ds_read_b128 v[48:51], v34 offset:12288\ns_add_u32 m0, s52, 4096\ns_waitcnt lgkmcnt(3)\nv_mfma_f32_16x16x32_bf16 v[104:107], v[52:55], v[168:171], v[104:107]\n"
"s_add_u32 s53, s49, 128\nv_mfma_f32_16x16x32_bf16 v[108:111], v[52:55], v[172:175], v[108:111]\nds_read_b128 v[52:55], v35 offset:12288\nbuffer_load_dwordx4 v10, s[12:15], s53 offen lds\n"
"s_waitcnt lgkmcnt(3)\nv_mfma_f32_16x16x32_bf16 v[96:99], v[40:43], v[176:179], v[96:99]\ns_add_u32 m0, s52, 8192\nv_mfma_f32_16x16x32_bf16 v[100:103], v[40:43], v[180:183], v[100:103]\n"
"ds_read_b128 v[40:43], v32 offset:16384\ns_add_u32 s53, s49, 256\ns_waitcnt lgkmcnt(3)\nv_mfma_f32_16x16x32_bf16 v[104:107], v[44:47], v[176:179], v[104:107]\n"
"buffer_load_dwordx4 v10, s[12:15], s53 offen lds\nv_mfma_f32_16x16x32_bf16 v[108:111], v[44:47], v[180:183], v[108:111]\nds_read_b128 v[44:47], v33 offset:16384\ns_add_u32 m0, s52, 12288\n"
"s_waitcnt lgkmcnt(3)\nv_mfma_f32_16x16x32_bf16 v[96:99], v[48:51], v[184:187], v[96:99]\ns_add_u32 s53, s49, 384\nv_mfma_f32_16x16x32_bf16 v[100:103], v[48:51], v[188:191], v[100:103]\n"
"ds_read_b128 v[48:51], v34 offset:16384\nbuffer_load_dwordx4 v10, s[12:15], s53 offen lds\ns_waitcnt lgkmcnt(3)\nv_mfma_f32_16x16x32_bf16 v[104:107], v[52:55], v[184:187], v[104:107]\n"
"s_add_u32 m0, s52, 16384\nv_mfma_f32_16x16x32_bf16 v[108:111], v[52:55], v[188:191], v[108:111]\nds_read_b128 v[52:55], v35 offset:16384\ns_add_u32 s53, s49, 512\n"
"s_waitcnt lgkmcnt(3)\nv_mfma_f32_16x16x32_bf16 v[96:99], v[40:43], v[192:195], v[96:99]\nbuffer_load_dwordx4 v10, s[12:15], s53 offen lds\nv_mfma_f32_16x16x32_bf16 v[100:103], v[40:43], v[196:199], v[100:103]\n"
"ds_read_b128 v[40:43], v32 offset:20480\ns_add_u32 m0, s52, 20480\ns_waitcnt lgkmcnt(3)\nv_mfma_f32_16x16x32_bf16 v[104:107], v[44:47], v[192:195], v[104:107]\n"
"s_add_u32 s53, s49, 640\nv_mfma_f32_16x16x32_bf16 v[108:111], v[44:47], v[196:199], v[108:111]\nds_read_b128 v[44:47], v33 offset:20480\nbuffer_load_dwordx4 v10, s[12:15], s53 offen lds\n"
"s_waitcnt lgkmcnt(3)\nv_mfma_f32_16x16x32_bf16 v[96:99], v[48:51], v[200:203], v[96:99]\ns_add_u32 m0, s52, 24576\nv_mfma_f32_16x16x32_bf16 v[100:103], v[48:51], v[204:207], v[100:103]\n"
"ds_read_b128 v[48:51], v34 offset:20480\ns_add_u32 s53, s49, 768\ns_waitcnt lgkmcnt(3)\nv_mfma_f32_16x16x32_bf16 v[104:107], v[52:55], v[200:203], v[104:107]\n"
"buffer_load_dwordx4 v10, s[12:15], s53 offen lds\nv_mfma_f32_16x16x32_bf16 v[108:111], v[52:55], v[204:207], v[108:111]\nds_read_b128 v[52:55], v35 offset:20480\nglobal_load_dwordx4 v[240:243], v23, s[38:39] offset:1024\n"
"s_waitcnt lgkmcnt(3)\nv_mfma_f32_16x16x32_bf16 v[96:99], v[40:43], v[208:211], v[96:99]\nv_mfma_f32_16x16x32_bf16 v[100:103], v[40:43], v[212:215], v[100:103]\nds_read_b128 v[40:43], v32 offset:24576\n"
"s_waitcnt lgkmcnt(3)\nv_mfma_f32_16x16x32_bf16 v[104:107], v[44:47], v[208:211], v[104:107]\nv_mfma_f32_16x16x32_bf16 v[108:111], v[44:47], v[212:215], v[108:111]\nds_read_b128 v[44:47], v33 offset:24576\n"
"s_waitcnt lgkmcnt(3)\nv_mfma_f32_16x16x32_bf16 v[96:99], v[48:51], v[216:219], v[96:99]\ns_add_u32 s54, s51, 32768\ns_cmp_eq_u32 s54, 163840\n"
"v_mfma_f32_16x16x32_bf16 v[100:103], v[48:51], v[220:223], v[100:103]\ns_cselect_b32 s54, 0, s54\ns_sub_u32 s54, s54, s51\nds_read_b128 v[48:51], v34 offset:24576\n"
"s_waitcnt lgkmcnt(3)\nv_mfma_f32_16x16x32_bf16 v[104:107], v[52:55], v[216:219], v[104:107]\nv_mfma_f32_16x16x32_bf16 v[108:111], v[52:55], v[220:223], v[108:111]\nds_read_b128 v[52:55], v35 offset:24576\n"
"s_waitcnt lgkmcnt(3)\nv_mfma_f32_16x16x32_bf16 v[96:99], v[40:43], v[224:227], v[96:99]\nv_add_u32 v32, s54, v32\nv_mfma_f32_16x16x32_bf16 v[100:103], v[40:43], v[228:231], v[100:103]\n"
"ds_read_b128 v[40:43], v32 offset:0\ns_waitcnt lgkmcnt(3)\nv_mfma_f32_16x16x32_bf16 v[104:107], v[44:47], v[224:227], v[104:107]\nv_add_u32 v33, s54, v33\n"
"v_mfma_f32_16x16x32_bf16 v[108:111], v[44:47], v[228:231], v[108:111]\nds_read_b128 v[44:47], v33 offset:0\ns_waitcnt lgkmcnt(3)\nv_mfma_f32_16x16x32_bf16 v[96:99], v[48:51], v[232:235], v[96:99]\n"
"v_add_u32 v34, s54, v34\nv_mfma_f32_16x16x32_bf16 v[100:103], v[48:51], v[236:239], v[100:103]\nds_read_b128 v[48:51], v34 offset:0\ns_waitcnt lgkmcnt(3)\n"
"v_mfma_f32_16x16x32_bf16 v[104:107], v[52:55], v[232:235], v[104:107]\nv_add_u32 v35, s54, v35\nv_mfma_f32_16x16x32_bf16 v[108:111], v[52:55], v[236:239], v[108:111]\nds_read_b128 v[52:55], v35 offset:0\n"
"s_mov_b32 s52, s47\ns_add_u32 s47, s51, s32\ns_add_u32 s51, s51, s54\ns_add_u32 s49, s49, 28672\n"
"s_waitcnt vmcnt(8)\nfg1_pf:\nv_cmp_ne_u32 vcc, s28, v37\ns_cbranch_vccz fg1_pfok\n"
"s_add_u32 s55, s55, 1\ns_cmp_gt_u32 s55, 262144\ns_cbranch_scc0 fg1_pf2\ns_endpgm\n"
"fg1_pf2:\ns_sleep 1\nbuffer_load_dword v37, v20, s[24:27], 0 offen sc1\ns_waitcnt vmcnt(0)\n"
"s_branch fg1_pf\nfg1_pfok:\ns_barrier\ns_waitcnt lgkmcnt(3)\n"
"v_mfma_f32_16x16x32_bf16 v[64:67], v[40:43], v[128:131], 0\nv_mov_b32 v36, s48\ns_add_u32 s48, s48, 1\ns_nop 0\n"
"buffer_store_dword v36, v19, s[24:27], s33 offen sc1\nv_mfma_f32_16x16x32_bf16 v[68:71], v[40:43], v[132:135], 0\nds_read_b128 v[40:43], v32 offset:4096\ns_waitcnt lgkmcnt(3)\n"
"v_mfma_f32_16x16x32_bf16 v[72:75], v[44:47], v[128:131], 0\nv_mfma_f32_16x16x32_bf16 v[76:79], v[44:47], v[132:135], 0\nds_read_b128 v[44:47], v33 offset:4096\nv_cvt_pk_bf16_f32 v56, v96, v97\n"
"s_waitcnt lgkmcnt(3)\nv_mfma_f32_16x16x32_bf16 v[64:67], v[48:51], v[136:139], v[64:67]\nv_cvt_pk_bf16_f32 v57, v98, v99\nv_mfma_f32_16x16x32_bf16 v[68:71], v[48:51], v[140:143], v[68:71]\n"
"ds_read_b128 v[48:51], v34 offset:4096\nv_cvt_pk_bf16_f32 v58, v104, v105\ns_waitcnt lgkmcnt(3)\nv_mfma_f32_16x16x32_bf16 v[72:75], v[52:55], v[136:139], v[72:75]\n"
"v_cvt_pk_bf16_f32 v59, v106, v107\nv_mfma_f32_16x16x32_bf16 v[76:79], v[52:55], v[140:143], v[76:79]\nds_read_b128 v[52:55], v35 offset:4096\ns_waitcnt lgkmcnt(3)\n"
"v_mfma_f32_16x16x32_bf16 v[64:67], v[40:43], v[144:147], v[64:67]\nv_mfma_f32_16x16x32_bf16 v[68:71], v[40:43], v[148:151], v[68:71]\nds_read_b128 v[40:43], v32 offset:8192\nbuffer_store_dwordx4 v[56:59], v15, s[20:23], s50 offen sc1\n"
"s_waitcnt lgkmcnt(3)\nv_mfma_f32_16x16x32_bf16 v[72:75], v[44:47], v[144:147], v[72:75]\nv_cvt_pk_bf16_f32 v60, v100, v101\nv_mfma_f32_16x16x32_bf16 v[76:79], v[44:47], v[148:151], v[76:79]\n"
"ds_read_b128 v[44:47], v33 offset:8192\nv_cvt_pk_bf16_f32 v61, v102, v103\ns_waitcnt lgkmcnt(3)\nv_mfma_f32_16x16x32_bf16 v[64:67], v[48:51], v[152:155], v[64:67]\n"
"v_cvt_pk_bf16_f32 v62, v108, v109\nv_mfma_f32_16x16x32_bf16 v[68:71], v[48:51], v[156:159], v[68:71]\nds_read_b128 v[48:51], v34 offset:8192\nv_cvt_pk_bf16_f32 v63, v110, v111\n"
"s_waitcnt lgkmcnt(3)\nv_mfma_f32_16x16x32_bf16 v[72:75], v[52:55], v[152:155], v[72:75]\nv_mfma_f32_16x16x32_bf16 v[76:79], v[52:55], v[156:159], v[76:79]\nds_read_b128 v[52:55], v35 offset:8192\n"
"s_waitcnt lgkmcnt(3)\nv_mfma_f32_16x16x32_bf16 v[64:67], v[40:43], v[160:163], v[64:67]\nbuffer_store_dwordx4 v[60:63], v16, s[20:23], s50 offen sc1\nv_mfma_f32_16x16x32_bf16 v[68:71], v[40:43], v[164:167], v[68:71]\n"
"ds_read_b128 v[40:43], v32 offset:12288\ns_add_u32 s50, s50, s36\ns_waitcnt lgkmcnt(3)\nv_mfma_f32_16x16x32_bf16 v[72:75], v[44:47], v[160:163], v[72:75]\n"
"s_add_u32 m0, s52, 0\nv_mfma_f32_16x16x32_bf16 v[76:79], v[44:47], v[164:167], v[76:79]\nds_read_b128 v[44:47], v33 offset:12288\ns_add_u32 s53, s49, 0\n"
"s_waitcnt lgkmcnt(3)\nv_mfma_f32_16x16x32_bf16 v[64:67], v[48:51], v[168:171], v[64:67]\nbuffer_load_dwordx4 v10, s[12:15], s53 offen lds\nv_mfma_f32_16x16x32_bf16 v[68:71], v[48:51], v[172:175], v[68:71]\n"
"ds_read_b128 v[48:51], v34 offset:12288\ns_add_u32 m0, s52, 4096\ns_waitcnt lgkmcnt(3)\nv_mfma_f32_16x16x32_bf16 v[72:75], v[52:55], v[168:171], v[72:75]\n"
"s_add_u32 s53, s49, 128\nv_mfma_f32_16x16x32_bf16 v[76:79], v[52:55], v[172:175], v[76:79]\nds_read_b128 v[52:55], v35 offset:12288\nbuffer_load_dwordx4 v10, s[12:15], s53 offen lds\n"
"s_waitcnt lgkmcnt(3)\nv_mfma_f32_16x16x32_bf16 v[64:67], v[40:43], v[176:179], v[64:67]\ns_add_u32 m0, s52, 8192\nv_mfma_f32_16x16x32_bf16 v[68:71], v[40:43], v[180:183], v[68:71]\n"
"ds_read_b128 v[40:43], v32 offset:16384\ns_add_u32 s53, s49, 256\ns_waitcnt lgkmcnt(3)\nv_mfma_f32_16x16x32_bf16 v[72:75], v[44:47], v[176:179], v[72:75]\n"
"buffer_load_dwordx4 v10, s[12:15], s53 offen lds\nv_mfma_f32_16x16x32_bf16 v[76:79], v[44:47], v[180:183], v[76:79]\nds_read_b128 v[44:47], v33 offset:16384\ns_add_u32 m0, s52, 12288\n"
"s_waitcnt lgkmcnt(3)\nv_mfma_f32_16x16x32_bf16 v[64:67], v[48:51], v[184:187], v[64:67]\ns_add_u32 s53, s49, 384\nv_mfma_f32_16x16x32_bf16 v[68:71], v[48:51], v[188:191], v[68:71]\n"
"ds_read_b128 v[48:51], v34 offset:16384\nbuffer_load_dwordx4 v10, s[12:15], s53 offen lds\ns_waitcnt lgkmcnt(3)\nv_mfma_f32_16x16x32_bf16 v[72:75], v[52:55], v[184:187], v[72:75]\n"
"s_add_u32 m0, s52, 16384\nv_mfma_f32_16x16x32_bf16 v[76:79], v[52:55], v[188:191], v[76:79]\nds_read_b128 v[52:55], v35 offset:16384\ns_add_u32 s53, s49, 512\n"
"s_waitcnt lgkmcnt(3)\nv_mfma_f32_16x16x32_bf16 v[64:67], v[40:43], v[192:195], v[64:67]\nbuffer_load_dwordx4 v10, s[12:15], s53 offen lds\nv_mfma_f32_16x16x32_bf16 v[68:71], v[40:43], v[196:199], v[68:71]\n"
"ds_read_b128 v[40:43], v32 offset:20480\ns_add_u32 m0, s52, 20480\ns_waitcnt lgkmcnt(3)\nv_mfma_f32_16x16x32_bf16 v[72:75], v[44:47], v[192:195], v[72:75]\n"
"s_add_u32 s53, s49, 640\nv_mfma_f32_16x16x32_bf16 v[76:79], v[44:47], v[196:199], v[76:79]\nds_read_b128 v[44:47], v33 offset:20480\nbuffer_load_dwordx4 v10, s[12:15], s53 offen lds\n"
"s_waitcnt lgkmcnt(3)\nv_mfma_f32_16x16x32_bf16 v[64:67], v[48:51], v[200:203], v[64:67]\ns_add_u32 m0, s52, 24576\nv_mfma_f32_16x16x32_bf16 v[68:71], v[48:51], v[204:207], v[68:71]\n"
"ds_read_b128 v[48:51], v34 offset:20480\ns_add_u32 s53, s49, 768\ns_waitcnt lgkmcnt(3)\nv_mfma_f32_16x16x32_bf16 v[72:75], v[52:55], v[200:203], v[72:75]\n"
"buffer_load_dwordx4 v10, s[12:15], s53 offen lds\nv_mfma_f32_16x16x32_bf16 v[76:79], v[52:55], v[204:207], v[76:79]\nds_read_b128 v[52:55], v35 offset:20480\nglobal_load_dwordx4 v[240:243], v23, s[38:39] offset:2048\n"
"s_waitcnt lgkmcnt(3)\nv_mfma_f32_16x16x32_bf16 v[64:67], v[40:43], v[208:211], v[64:67]\ns_mov_b32 s53, 0\nbuffer_load_dwordx4 a[0:3], v14, s[16:19], s53 offen sc1\n"
"v_mfma_f32_16x16x32_bf16 v[68:71], v[40:43], v[212:215], v[68:71]\nds_read_b128 v[40:43], v32 offset:24576\ns_mov_b32 s53, 229376\nbuffer_load_dwordx4 a[4:7], v14, s[16:19], s53 offen sc1\n"
"s_waitcnt lgkmcnt(3)\nv_mfma_f32_16x16x32_bf16 v[72:75], v[44:47], v[208:211], v[72:75]\ns_mov_b32 s53, 1024\nbuffer_load_dwordx4 a[8:11], v14, s[16:19], s53 offen sc1\n"
"v_mfma_f32_16x16x32_bf16 v[76:79], v[44:47], v[212:215], v[76:79]\nds_read_b128 v[44:47], v33 offset:24576\ns_mov_b32 s53, 230400\nbuffer_load_dwordx4 a[12:15], v14, s[16:19], s53 offen sc1\n"
"s_waitcnt lgkmcnt(3)\nv_mfma_f32_16x16x32_bf16 v[64:67], v[48:51], v[216:219], v[64:67]\ns_add_u32 s54, s51, 32768\ns_cmp_eq_u32 s54, 163840\n"
"v_mfma_f32_16x16x32_bf16 v[68:71], v[48:51], v[220:223], v[68:71]\ns_cselect_b32 s54, 0, s54\ns_sub_u32 s54, s54, s51\nds_read_b128 v[48:51], v34 offset:24576\n"
"s_mov_b32 s53, 2048\nbuffer_load_dwordx4 a[16:19], v14, s[16:19], s53 offen sc1\ns_waitcnt lgkmcnt(3)\nv_mfma_f32_16x16x32_bf16 v[72:75], v[52:55], v[216:219], v[72:75]\n"
"v_mfma_f32_16x16x32_bf16 v[76:79], v[52:55], v[220:223], v[76:79]\nds_read_b128 v[52:55], v35 offset:24576\ns_waitcnt lgkmcnt(3)\nv_mfma_f32_16x16x32_bf16 v[64:67], v[40:43], v[224:227], v[64:67]\n"
"v_add_u32 v32, s54, v32\nv_mfma_f32_16x16x32_bf16 v[68:71], v[40:43], v[228:231], v[68:71]\nds_read_b128 v[40:43], v32 offset:0\ns_waitcnt lgkmcnt(3)\n"
"v_mfma_f32_16x16x32_bf16 v[72:75], v[44:47], v[224:227], v[72:75]\nv_add_u32 v33, s54, v33\nv_mfma_f32_16x16x32_bf16 v[76:79], v[44:47], v[228:231], v[76:79]\nds_read_b128 v[44:47], v33 offset:0\n"
"s_waitcnt lgkmcnt(3)\nv_mfma_f32_16x16x32_bf16 v[64:67], v[48:51], v[232:235], v[64:67]\nv_add_u32 v34, s54, v34\nv_mfma_f32_16x16x32_bf16 v[68:71], v[48:51], v[236:239], v[68:71]\n"
"ds_read_b128 v[48:51], v34 offset:0\ns_waitcnt lgkmcnt(3)\nv_mfma_f32_16x16x32_bf16 v[72:75], v[52:55], v[232:235], v[72:75]\nv_add_u32 v35, s54, v35\n"
"v_mfma_f32_16x16x32_bf16 v[76:79], v[52:55], v[236:239], v[76:79]\nds_read_b128 v[52:55], v35 offset:0\ns_mov_b32 s52, s47\ns_add_u32 s47, s51, s32\n"
"s_add_u32 s51, s51, s54\ns_add_u32 s49, s49, 28672\ns_waitcnt vmcnt(13)\ns_barrier\n"
"s_waitcnt lgkmcnt(3)\nv_mfma_f32_16x16x32_bf16 v[96:99], v[40:43], v[128:131], 0\nv_mov_b32 v36, s48\ns_add_u32 s48, s48, 1\n"
"s_nop 0\nbuffer_store_dword v36, v19, s[24:27], s33 offen sc1\nv_mfma_f32_16x16x32_bf16 v[100:103], v[40:43], v[132:135], 0\nds_read_b128 v[40:43], v32 offset:4096\n"
"s_waitcnt lgkmcnt(3)\nv_mfma_f32_16x16x32_bf16 v[104:107], v[44:47], v[128:131], 0\nv_mfma_f32_16x16x32_bf16 v[108:111], v[44:47], v[132:135], 0\nds_read_b128 v[44:47], v33 offset:4096\n"
"v_cvt_pk_bf16_f32 v56, v64, v65\ns_waitcnt lgkmcnt(3)\nv_mfma_f32_16x16x32_bf16 v[96:99], v[48:51], v[136:139], v[96:99]\nv_cvt_pk_bf16_f32 v57, v66, v67\n"
"v_mfma_f32_16x16x32_bf16 v[100:103], v[48:51], v[140:143], v[100:103]\nds_read_b128 v[48:51], v34 offset:4096\nv_cvt_pk_bf16_f32 v58, v72, v73\ns_waitcnt lgkmcnt(3)\n"
"v_mfma_f32_16x16x32_bf16 v[104:107], v[52:55], v[136:139], v[104:107]\nv_cvt_pk_bf16_f32 v59, v74, v75\nv_mfma_f32_16x16x32_bf16 v[108:111], v[52:55], v[140:143], v[108:111]\nds_read_b128 v[52:55], v35 offset:4096\n"
"s_waitcnt lgkmcnt(3)\nv_mfma_f32_16x16x32_bf16 v[96:99], v[40:43], v[144:147], v[96:99]\nv_mfma_f32_16x16x32_bf16 v[100:103], v[40:43], v[148:151], v[100:103]\nds_read_b128 v[40:43], v32 offset:8192\n"
"buffer_store_dwordx4 v[56:59], v15, s[20:23], s50 offen sc1\ns_waitcnt lgkmcnt(3)\nv_mfma_f32_16x16x32_bf16 v[104:107], v[44:47], v[144:147], v[104:107]\nv_cvt_pk_bf16_f32 v60, v68, v69\n"
"v_mfma_f32_16x16x32_bf16 v[108:111], v[44:47], v[148:151], v[108:111]\nds_read_b128 v[44:47], v33 offset:8192\nv_cvt_pk_bf16_f32 v61, v70, v71\ns_waitcnt lgkmcnt(3)\n"
"v_mfma_f32_16x16x32_bf16 v[96:99], v[48:51], v[152:155], v[96:99]\nv_cvt_pk_bf16_f32 v62, v76, v77\nv_mfma_f32_16x16x32_bf16 v[100:103], v[48:51], v[156:159], v[100:103]\nds_read_b128 v[48:51], v34 offset:8192\n"
"v_cvt_pk_bf16_f32 v63, v78, v79\ns_waitcnt lgkmcnt(3)\nv_mfma_f32_16x16x32_bf16 v[104:107], v[52:55], v[152:155], v[104:107]\nv_mfma_f32_16x16x32_bf16 v[108:111], v[52:55], v[156:159], v[108:111]\n"
"ds_read_b128 v[52:55], v35 offset:8192\ns_waitcnt lgkmcnt(3)\nv_mfma_f32_16x16x32_bf16 v[96:99], v[40:43], v[160:163], v[96:99]\nbuffer_store_dwordx4 v[60:63], v16, s[20:23], s50 offen sc1\n"
"v_mfma_f32_16x16x32_bf16 v[100:103], v[40:43], v[164:167], v[100:103]\nds_read_b128 v[40:43], v32 offset:12288\ns_add_u32 s50, s50, s36\ns_waitcnt lgkmcnt(3)\n"
"v_mfma_f32_16x16x32_bf16 v[104:107], v[44:47], v[160:163], v[104:107]\ns_add_u32 m0, s52, 0\nv_mfma_f32_16x16x32_bf16 v[108:111], v[44:47], v[164:167], v[108:111]\nds_read_b128 v[44:47], v33 offset:12288\n"
"s_add_u32 s53, s49, 0\ns_waitcnt lgkmcnt(3)\nv_mfma_f32_16x16x32_bf16 v[96:99], v[48:51], v[168:171], v[96:99]\nbuffer_load_dwordx4 v10, s[12:15], s53 offen lds\n"
"v_mfma_f32_16x16x32_bf16 v[100:103], v[48:51], v[172:175], v[100:103]\nds_read_b128 v[48:51], v34 offset:12288\ns_add_u32 m0, s52, 4096\ns_waitcnt lgkmcnt(3)\n"
"v_mfma_f32_16x16x32_bf16 v[104:107], v[52:55], v[168:171], v[104:107]\ns_add_u32 s53, s49, 128\nv_mfma_f32_16x16x32_bf16 v[108:111], v[52:55], v[172:175], v[108:111]\nds_read_b128 v[52:55], v35 offset:12288\n"
"buffer_load_dwordx4 v10, s[12:15], s53 offen lds\ns_waitcnt lgkmcnt(3)\nv_mfma_f32_16x16x32_bf16 v[96:99], v[40:43], v[176:179], v[96:99]\ns_add_u32 m0, s52, 8192\n"
"v_mfma_f32_16x16x32_bf16 v[100:103], v[40:43], v[180:183], v[100:103]\nds_read_b128 v[40:43], v32 offset:16384\ns_add_u32 s53, s49, 256\ns_waitcnt lgkmcnt(3)\n"
"v_mfma_f32_16x16x32_bf16 v[104:107], v[44:47], v[176:179], v[104:107]\nbuffer_load_dwordx4 v10, s[12:15], s53 offen lds\nv_mfma_f32_16x16x32_bf16 v[108:111], v[44:47], v[180:183], v[108:111]\nds_read_b128 v[44:47], v33 offset:16384\n"
"s_add_u32 m0, s52, 12288\ns_waitcnt lgkmcnt(3)\nv_mfma_f32_16x16x32_bf16 v[96:99], v[48:51], v[184:187], v[96:99]\ns_add_u32 s53, s49, 384\n"
"v_mfma_f32_16x16x32_bf16 v[100:103], v[48:51], v[188:191], v[100:103]\nds_read_b128 v[48:51], v34 offset:16384\nbuffer_load_dwordx4 v10, s[12:15], s53 offen lds\ns_waitcnt lgkmcnt(3)\n"
"v_mfma_f32_16x16x32_bf16 v[104:107], v[52:55], v[184:187], v[104:107]\ns_add_u32 m0, s52, 16384\nv_mfma_f32_16x16x32_bf16 v[108:111], v[52:55], v[188:191], v[108:111]\nds_read_b128 v[52:55], v35 offset:16384\n"
"s_add_u32 s53, s49, 512\ns_waitcnt lgkmcnt(3)\nv_mfma_f32_16x16x32_bf16 v[96:99], v[40:43], v[192:195], v[96:99]\nbuffer_load_dwordx4 v10, s[12:15], s53 offen lds\n"
"v_mfma_f32_16x16x32_bf16 v[100:103], v[40:43], v[196:199], v[100:103]\nds_read_b128 v[40:43], v32 offset:20480\ns_add_u32 m0, s52, 20480\ns_waitcnt lgkmcnt(3)\n"
"v_mfma_f32_16x16x32_bf16 v[104:107], v[44:47], v[192:195], v[104:107]\ns_add_u32 s53, s49, 640\nv_mfma_f32_16x16x32_bf16 v[108:111], v[44:47], v[196:199], v[108:111]\nds_read_b128 v[44:47], v33 offset:20480\n"
"buffer_load_dwordx4 v10, s[12:15], s53 offen lds\ns_waitcnt lgkmcnt(3)\nv_mfma_f32_16x16x32_bf16 v[96:99], v[48:51], v[200:203], v[96:99]\ns_add_u32 m0, s52, 24576\n"
"v_mfma_f32_16x16x32_bf16 v[100:103], v[48:51], v[204:207], v[100:103]\nds_read_b128 v[48:51], v34 offset:20480\ns_add_u32 s53, s49, 768\ns_waitcnt lgkmcnt(3)\n"
"v_mfma_f32_16x16x32_bf16 v[104:107], v[52:55], v[200:203], v[104:107]\nbuffer_load_dwordx4 v10, s[12:15], s53 offen lds\nv_mfma_f32_16x16x32_bf16 v[108:111], v[52:55], v[204:207], v[108:111]\nds_read_b128 v[52:55], v35 offset:20480\n"
"global_load_dwordx4 v[240:243], v23, s[38:39] offset:3072\ns_waitcnt lgkmcnt(3)\nv_mfma_f32_16x16x32_bf16 v[96:99], v[40:43], v[208:211], v[96:99]\ns_mov_b32 s53, 231424\n"
"buffer_load_dwordx4 a[20:23], v14, s[16:19], s53 offen sc1\nv_mfma_f32_16x16x32_bf16 v[100:103], v[40:43], v[212:215], v[100:103]\nds_read_b128 v[40:43], v32 offset:24576\ns_mov_b32 s53, 3072\n"
"buffer_load_dwordx4 a[24:27], v14, s[16:19], s53 offen sc1\ns_waitcnt lgkmcnt(3)\nv_mfma_f32_16x16x32_bf16 v[104:107], v[44:47], v[208:211], v[104:107]\ns_mov_b32 s53, 232448\n"
"buffer_load_dwordx4 a[28:31], v14, s[16:19], s53 offen sc1\nv_mfma_f32_16x16x32_bf16 v[108:111], v[44:47], v[212:215], v[108:111]\nds_read_b128 v[44:47], v33 offset:24576\ns_mov_b32 s53, 4096\n"
"buffer_load_dwordx4 a[32:35], v14, s[16:19], s53 offen sc1\ns_waitcnt lgkmcnt(3)\nv_mfma_f32_16x16x32_bf16 v[96:99], v[48:51], v[216:219], v[96:99]\ns_add_u32 s54, s51, 32768\n"
"s_cmp_eq_u32 s54, 163840\nv_mfma_f32_16x16x32_bf16 v[100:103], v[48:51], v[220:223], v[100:103]\ns_cselect_b32 s54, 0, s54\ns_sub_u32 s54, s54, s51\n"
"ds_read_b128 v[48:51], v34 offset:24576\ns_mov_b32 s53, 233472\nbuffer_load_dwordx4 a[36:39], v14, s[16:19], s53 offen sc1\ns_waitcnt lgkmcnt(3)\n"
"v_mfma_f32_16x16x32_bf16 v[104:107], v[52:55], v[216:219], v[104:107]\nv_mfma_f32_16x16x32_bf16 v[108:111], v[52:55], v[220:223], v[108:111]\nds_read_b128 v[52:55], v35 offset:24576\ns_waitcnt lgkmcnt(3)\n"
"v_mfma_f32_16x16x32_bf16 v[96:99], v[40:43], v[224:227], v[96:99]\nv_add_u32 v32, s54, v32\nv_mfma_f32_16x16x32_bf16 v[100:103], v[40:43], v[228:231], v[100:103]\nds_read_b128 v[40:43], v32 offset:0\n"
"s_waitcnt lgkmcnt(3)\nv_mfma_f32_16x16x32_bf16 v[104:107], v[44:47], v[224:227], v[104:107]\nv_add_u32 v33, s54, v33\nv_mfma_f32_16x16x32_bf16 v[108:111], v[44:47], v[228:231], v[108:111]\n"
"ds_read_b128 v[44:47], v33 offset:0\ns_waitcnt lgkmcnt(3)\nv_mfma_f32_16x16x32_bf16 v[96:99], v[48:51], v[232:235], v[96:99]\nv_add_u32 v34, s54, v34\n"
"v_mfma_f32_16x16x32_bf16 v[100:103], v[48:51], v[236:239], v[100:103]\nds_read_b128 v[48:51], v34 offset:0\ns_waitcnt lgkmcnt(3)\nv_mfma_f32_16x16x32_bf16 v[104:107], v[52:55], v[232:235], v[104:107]\n"
"v_add_u32 v35, s54, v35\nv_mfma_f32_16x16x32_bf16 v[108:111], v[52:55], v[236:239], v[108:111]\nds_read_b128 v[52:55], v35 offset:0\ns_mov_b32 s52, s47\n"
"s_add_u32 s47, s51, s32\ns_add_u32 s51, s51, s54\ns_add_u32 s49, s49, 28672\ns_waitcnt vmcnt(13)\n"
"s_barrier\ns_waitcnt lgkmcnt(3)\nv_mfma_f32_16x16x32_bf16 v[64:67], v[40:43], v[128:131], 0\nv_mov_b32 v36, s48\n"
"s_add_u32 s48, s48, 1\ns_nop 0\nbuffer_store_dword v36, v19, s[24:27], s33 offen sc1\nv_mfma_f32_16x16x32_bf16 v[68:71], v[40:43], v[132:135], 0\n"
"ds_read_b128 v[40:43], v32 offset:4096\ns_waitcnt lgkmcnt(3)\nv_mfma_f32_16x16x32_bf16 v[72:75], v[44:47], v[128:131], 0\nv_mfma_f32_16x16x32_bf16 v[76:79], v[44:47], v[132:135], 0\n"
"ds_read_b128 v[44:47], v33 offset:4096\nv_cvt_pk_bf16_f32 v56, v96, v97\ns_waitcnt lgkmcnt(3)\nv_mfma_f32_16x16x32_bf16 v[64:67], v[48:51], v[136:139], v[64:67]\n"
"v_cvt_pk_bf16_f32 v57, v98, v99\nv_mfma_f32_16x16x32_bf16 v[68:71], v[48:51], v[140:143], v[68:71]\nds_read_b128 v[48:51], v34 offset:4096\nv_cvt_pk_bf16_f32 v58, v104, v105\n"
"s_waitcnt lgkmcnt(3)\nv_mfma_f32_16x16x32_bf16 v[72:75], v[52:55], v[136:139], v[72:75]\nv_cvt_pk_bf16_f32 v59, v106, v107\nv_mfma_f32_16x16x32_bf16 v[76:79], v[52:55], v[140:143], v[76:79]\n"
"ds_read_b128 v[52:55], v35 offset:4096\ns_waitcnt lgkmcnt(3)\nv_mfma_f32_16x16x32_bf16 v[64:67], v[40:43], v[144:147], v[64:67]\nv_mfma_f32_16x16x32_bf16 v[68:71], v[40:43], v[148:151], v[68:71]\n"
"ds_read_b128 v[40:43], v32 offset:8192\nbuffer_store_dwordx4 v[56:59], v15, s[20:23], s50 offen sc1\ns_waitcnt lgkmcnt(3)\nv_mfma_f32_16x16x32_bf16 v[72:75], v[44:47], v[144:147], v[72:75]\n"
"v_cvt_pk_bf16_f32 v60, v100, v101\nv_mfma_f32_16x16x32_bf16 v[76:79], v[44:47], v[148:151], v[76:79]\nds_read_b128 v[44:47], v33 offset:8192\nv_cvt_pk_bf16_f32 v61, v102, v103\n"
"s_waitcnt lgkmcnt(3)\nv_mfma_f32_16x16x32_bf16 v[64:67], v[48:51], v[152:155], v[64:67]\nv_cvt_pk_bf16_f32 v62, v108, v109\nv_mfma_f32_16x16x32_bf16 v[68:71], v[48:51], v[156:159], v[68:71]\n"
"ds_read_b128 v[48:51], v34 offset:8192\nv_cvt_pk_bf16_f32 v63, v110, v111\ns_waitcnt lgkmcnt(3)\nv_mfma_f32_16x16x32_bf16 v[72:75], v[52:55], v[152:155], v[72:75]\n"
"v_mfma_f32_16x16x32_bf16 v[76:79], v[52:55], v[156:159], v[76:79]\nds_read_b128 v[52:55], v35 offset:8192\ns_waitcnt lgkmcnt(3)\nv_mfma_f32_16x16x32_bf16 v[64:67], v[40:43], v[160:163], v[64:67]\n"
"buffer_store_dwordx4 v[60:63], v16, s[20:23], s50 offen sc1\nv_mfma_f32_16x16x32_bf16 v[68:71], v[40:43], v[164:167], v[68:71]\nds_read_b128 v[40:43], v32 offset:12288\ns_add_u32 s50, s50, s36\n"
"s_waitcnt lgkmcnt(3)\nv_mfma_f32_16x16x32_bf16 v[72:75], v[44:47], v[160:163], v[72:75]\ns_add_u32 m0, s52, 0\nv_mfma_f32_16x16x32_bf16 v[76:79], v[44:47], v[164:167], v[76:79]\n"
"ds_read_b128 v[44:47], v33 offset:12288\ns_add_u32 s53, s49, 0\ns_waitcnt lgkmcnt(3)\nv_mfma_f32_16x16x32_bf16 v[64:67], v[48:51], v[168:171], v[64:67]\n"
"buffer_load_dwordx4 v10, s[12:15], s53 offen lds\nv_mfma_f32_16x16x32_bf16 v[68:71], v[48:51], v[172:175], v[68:71]\nds_read_b128 v[48:51], v34 offset:12288\ns_add_u32 m0, s52, 4096\n"
"s_waitcnt lgkmcnt(3)\nv_mfma_f32_16x16x32_bf16 v[72:75], v[52:55], v[168:171], v[72:75]\ns_add_u32 s53, s49, 128\nv_mfma_f32_16x16x32_bf16 v[76:79], v[52:55], v[172:175], v[76:79]\n"
"ds_read_b128 v[52:55], v35 offset:12288\nbuffer_load_dwordx4 v10, s[12:15], s53 offen lds\ns_waitcnt lgkmcnt(3)\nv_mfma_f32_16x16x32_bf16 v[64:67], v[40:43], v[176:179], v[64:67]\n"
"s_add_u32 m0, s52, 8192\nv_mfma_f32_16x16x32_bf16 v[68:71], v[40:43], v[180:183], v[68:71]\nds_read_b128 v[40:43], v32 offset:16384\ns_add_u32 s53, s49, 256\n"
"s_waitcnt lgkmcnt(3)\nv_mfma_f32_16x16x32_bf16 v[72:75], v[44:47], v[176:179], v[72:75]\nbuffer_load_dwordx4 v10, s[12:15], s53 offen lds\nv_mfma_f32_16x16x32_bf16 v[76:79], v[44:47], v[180:183], v[76:79]\n"
"ds_read_b128 v[44:47], v33 offset:16384\ns_add_u32 m0, s52, 12288\ns_waitcnt lgkmcnt(3)\nv_mfma_f32_16x16x32_bf16 v[64:67], v[48:51], v[184:187], v[64:67]\n"
"s_add_u32 s53, s49, 384\nv_mfma_f32_16x16x32_bf16 v[68:71], v[48:51], v[188:191], v[68:71]\nds_read_b128 v[48:51], v34 offset:16384\nbuffer_load_dwordx4 v10, s[12:15], s53 offen lds\n"
"s_waitcnt lgkmcnt(3)\nv_mfma_f32_16x16x32_bf16 v[72:75], v[52:55], v[184:187], v[72:75]\ns_add_u32 m0, s52, 16384\nv_mfma_f32_16x16x32_bf16 v[76:79], v[52:55], v[188:191], v[76:79]\n"
"ds_read_b128 v[52:55], v35 offset:16384\ns_add_u32 s53, s49, 512\ns_waitcnt lgkmcnt(3)\nv_mfma_f32_16x16x32_bf16 v[64:67], v[40:43], v[192:195], v[64:67]\n"
"buffer_load_dwordx4 v10, s[12:15], s53 offen lds\nv_mfma_f32_16x16x32_bf16 v[68:71], v[40:43], v[196:199], v[68:71]\nds_read_b128 v[40:43], v32 offset:20480\ns_add_u32 m0, s52, 20480\n"
"s_waitcnt lgkmcnt(3)\nv_mfma_f32_16x16x32_bf16 v[72:75], v[44:47], v[192:195], v[72:75]\ns_add_u32 s53, s49, 640\nv_mfma_f32_16x16x32_bf16 v[76:79], v[44:47], v[196:199], v[76:79]\n"
"ds_read_b128 v[44:47], v33 offset:20480\nbuffer_load_dwordx4 v10, s[12:15], s53 offen lds\ns_waitcnt lgkmcnt(3)\nv_mfma_f32_16x16x32_bf16 v[64:67], v[48:51], v[200:203], v[64:67]\n"
"s_add_u32 m0, s52, 24576\nv_mfma_f32_16x16x32_bf16 v[68:71], v[48:51], v[204:207], v[68:71]\nds_read_b128 v[48:51], v34 offset:20480\ns_add_u32 s53, s49, 768\n"
"s_waitcnt lgkmcnt(3)\nv_mfma_f32_16x16x32_bf16 v[72:75], v[52:55], v[200:203], v[72:75]\nbuffer_load_dwordx4 v10, s[12:15], s53 offen lds\nv_mfma_f32_16x16x32_bf16 v[76:79], v[52:55], v[204:207], v[76:79]\n"
"ds_read_b128 v[52:55], v35 offset:20480\ns_mov_b32 s53, 5120\nbuffer_load_dwordx4 a[40:43], v14, s[16:19], s53 offen sc1\ns_waitcnt lgkmcnt(3)\n"
"v_mfma_f32_16x16x32_bf16 v[64:67], v[40:43], v[208:211], v[64:67]\ns_mov_b32 s53, 234496\nbuffer_load_dwordx4 a[44:47], v14, s[16:19], s53 offen sc1\nv_mfma_f32_16x16x32_bf16 v[68:71], v[40:43], v[212:215], v[68:71]\n"
"ds_read_b128 v[40:43], v32 offset:24576\ns_mov_b32 s53, 6144\nbuffer_load_dwordx4 a[48:51], v14, s[16:19], s53 offen sc1\ns_waitcnt lgkmcnt(3)\n"
"v_mfma_f32_16x16x32_bf16 v[72:75], v[44:47], v[208:211], v[72:75]\ns_mov_b32 s53, 235520\nbuffer_load_dwordx4 a[52:55], v14, s[16:19], s53 offen sc1\nv_mfma_f32_16x16x32_bf16 v[76:79], v[44:47], v[212:215], v[76:79]\n"
"ds_read_b128 v[44:47], v33 offset:24576\ns_mov_b32 s53, 7168\nbuffer_load_dwordx4 a[56:59], v14, s[16:19], s53 offen sc1\ns_waitcnt lgkmcnt(3)\n"
"v_mfma_f32_16x16x32_bf16 v[64:67], v[48:51], v[216:219], v[64:67]\ns_add_u32 s54, s51, 32768\ns_cmp_eq_u32 s54, 163840\nv_mfma_f32_16x16x32_bf16 v[68:71], v[48:51], v[220:223], v[68:71]\n"
"s_cselect_b32 s54, 0, s54\ns_sub_u32 s54, s54, s51\nds_read_b128 v[48:51], v34 offset:24576\ns_waitcnt lgkmcnt(3)\n"
"v_mfma_f32_16x16x32_bf16 v[72:75], v[52:55], v[216:219], v[72:75]\nv_mfma_f32_16x16x32_bf16 v[76:79], v[52:55], v[220:223], v[76:79]\nds_read_b128 v[52:55], v35 offset:24576\ns_waitcnt lgkmcnt(3)\n"
"v_mfma_f32_16x16x32_bf16 v[64:67], v[40:43], v[224:227], v[64:67]\nv_add_u32 v32, s54, v32\nv_mfma_f32_16x16x32_bf16 v[68:71], v[40:43], v[228:231], v[68:71]\nds_read_b128 v[40:43], v32 offset:0\n"
"s_waitcnt lgkmcnt(3)\nv_mfma_f32_16x16x32_bf16 v[72:75], v[44:47], v[224:227], v[72:75]\nv_add_u32 v33, s54, v33\nv_mfma_f32_16x16x32_bf16 v[76:79], v[44:47], v[228:231], v[76:79]\n"
"ds_read_b128 v[44:47], v33 offset:0\ns_waitcnt lgkmcnt(3)\nv_mfma_f32_16x16x32_bf16 v[64:67], v[48:51], v[232:235], v[64:67]\nv_add_u32 v34, s54, v34\n"
"v_mfma_f32_16x16x32_bf16 v[68:71], v[48:51], v[236:239], v[68:71]\nds_read_b128 v[48:51], v34 offset:0\ns_waitcnt lgkmcnt(3)\nv_mfma_f32_16x16x32_bf16 v[72:75], v[52:55], v[232:235], v[72:75]\n"
"v_add_u32 v35, s54, v35\nv_mfma_f32_16x16x32_bf16 v[76:79], v[52:55], v[236:239], v[76:79]\nds_read_b128 v[52:55], v35 offset:0\ns_mov_b32 s52, s47\n"
"s_add_u32 s47, s51, s32\ns_add_u32 s51, s51, s54\ns_add_u32 s49, s49, 28672\ns_waitcnt vmcnt(12)\n"
"s_barrier\ns_waitcnt lgkmcnt(3)\nv_mfma_f32_16x16x32_bf16 v[96:99], v[40:43], v[128:131], 0\nv_mov_b32 v36, s48\n"
"s_add_u32 s48, s48, 1\ns_nop 0\nbuffer_store_dword v36, v19, s[24:27], s33 offen sc1\nv_mfma_f32_16x16x32_bf16 v[100:103], v[40:43], v[132:135], 0\n"
"ds_read_b128 v[40:43], v32 offset:4096\ns_waitcnt lgkmcnt(3)\nv_mfma_f32_16x16x32_bf16 v[104:107], v[44:47], v[128:131], 0\nv_mfma_f32_16x16x32_bf16 v[108:111], v[44:47], v[132:135], 0\n"
"ds_read_b128 v[44:47], v33 offset:4096\nv_cvt_pk_bf16_f32 v56, v64, v65\ns_waitcnt lgkmcnt(3)\nv_mfma_f32_16x16x32_bf16 v[96:99], v[48:51], v[136:139], v[96:99]\n"
"v_cvt_pk_bf16_f32 v57, v66, v67\nv_mfma_f32_16x16x32_bf16 v[100:103], v[48:51], v[140:143], v[100:103]\nds_read_b128 v[48:51], v34 offset:4096\nv_cvt_pk_bf16_f32 v58, v72, v73\n"
"s_waitcnt lgkmcnt(3)\nv_mfma_f32_16x16x32_bf16 v[104:107], v[52:55], v[136:139], v[104:107]\nv_cvt_pk_bf16_f32 v59, v74, v75\nv_mfma_f32_16x16x32_bf16 v[108:111], v[52:55], v[140:143], v[108:111]\n"
"ds_read_b128 v[52:55], v35 offset:4096\ns_waitcnt lgkmcnt(3)\nv_mfma_f32_16x16x32_bf16 v[96:99], v[40:43], v[144:147], v[96:99]\nv_mfma_f32_16x16x32_bf16 v[100:103], v[40:43], v[148:151], v[100:103]\n"
"ds_read_b128 v[40:43], v32 offset:8192\nbuffer_store_dwordx4 v[56:59], v15, s[20:23], s50 offen sc1\ns_waitcnt lgkmcnt(3)\nv_mfma_f32_16x16x32_bf16 v[104:107], v[44:47], v[144:147], v[104:107]\n"
"v_cvt_pk_bf16_f32 v60, v68, v69\nv_mfma_f32_16x16x32_bf16 v[108:111], v[44:47], v[148:151], v[108:111]\nds_read_b128 v[44:47], v33 offset:8192\nv_cvt_pk_bf16_f32 v61, v70, v71\n"
"s_waitcnt lgkmcnt(3)\nv_mfma_f32_16x16x32_bf16 v[96:99], v[48:51], v[152:155], v[96:99]\nv_cvt_pk_bf16_f32 v62, v76, v77\nv_mfma_f32_16x16x32_bf16 v[100:103], v[48:51], v[156:159], v[100:103]\n"
"ds_read_b128 v[48:51], v34 offset:8192\nv_cvt_pk_bf16_f32 v63, v78, v79\ns_waitcnt lgkmcnt(3)\nv_mfma_f32_16x16x32_bf16 v[104:107], v[52:55], v[152:155], v[104:107]\n"
"v_mfma_f32_16x16x32_bf16 v[108:111], v[52:55], v[156:159], v[108:111]\nds_read_b128 v[52:55], v35 offset:8192\ns_waitcnt lgkmcnt(3)\nv_mfma_f32_16x16x32_bf16 v[96:99], v[40:43], v[160:163], v[96:99]\n"
"buffer_store_dwordx4 v[60:63], v16, s[20:23], s50 offen sc1\nv_mfma_f32_16x16x32_bf16 v[100:103], v[40:43], v[164:167], v[100:103]\nds_read_b128 v[40:43], v32 offset:12288\ns_add_u32 s50, s50, s36\n"
"s_waitcnt lgkmcnt(3)\nv_mfma_f32_16x16x32_bf16 v[104:107], v[44:47], v[160:163], v[104:107]\ns_add_u32 m0, s52, 0\nv_mfma_f32_16x16x32_bf16 v[108:111], v[44:47], v[164:167], v[108:111]\n"
"ds_read_b128 v[44:47], v33 offset:12288\ns_add_u32 s53, s49, 0\ns_waitcnt lgkmcnt(3)\nv_mfma_f32_16x16x32_bf16 v[96:99], v[48:51], v[168:171], v[96:99]\n"
"buffer_load_dwordx4 v10, s[12:15], s53 offen lds\nv_mfma_f32_16x16x32_bf16 v[100:103], v[48:51], v[172:175], v[100:103]\nds_read_b128 v[48:51], v34 offset:12288\ns_add_u32 m0, s52, 4096\n"
"s_waitcnt lgkmcnt(3)\nv_mfma_f32_16x16x32_bf16 v[104:107], v[52:55], v[168:171], v[104:107]\ns_add_u32 s53, s49, 128\nv_mfma_f32_16x16x32_bf16 v[108:111], v[52:55], v[172:175], v[108:111]\n"
"ds_read_b128 v[52:55], v35 offset:12288\nbuffer_load_dwordx4 v10, s[12:15], s53 offen lds\ns_waitcnt lgkmcnt(3)\nv_mfma_f32_16x16x32_bf16 v[96:99], v[40:43], v[176:179], v[96:99]\n"
"s_add_u32 m0, s52, 8192\nv_mfma_f32_16x16x32_bf16 v[100:103], v[40:43], v[180:183], v[100:103]\nds_read_b128 v[40:43], v32 offset:16384\ns_add_u32 s53, s49, 256\n"
"s_waitcnt lgkmcnt(3)\nv_mfma_f32_16x16x32_bf16 v[104:107], v[44:47], v[176:179], v[104:107]\nbuffer_load_dwordx4 v10, s[12:15], s53 offen lds\nv_mfma_f32_16x16x32_bf16 v[108:111], v[44:47], v[180:183], v[108:111]\n"
"ds_read_b128 v[44:47], v33 offset:16384\ns_add_u32 m0, s52, 12288\ns_waitcnt lgkmcnt(3)\nv_mfma_f32_16x16x32_bf16 v[96:99], v[48:51], v[184:187], v[96:99]\n"
"s_add_u32 s53, s49, 384\nv_mfma_f32_16x16x32_bf16 v[100:103], v[48:51], v[188:191], v[100:103]\nds_read_b128 v[48:51], v34 offset:16384\nbuffer_load_dwordx4 v10, s[12:15], s53 offen lds\n"
"s_waitcnt lgkmcnt(3)\nv_mfma_f32_16x16x32_bf16 v[104:107], v[52:55], v[184:187], v[104:107]\ns_add_u32 m0, s52, 16384\nv_mfma_f32_16x16x32_bf16 v[108:111], v[52:55], v[188:191], v[108:111]\n"
"ds_read_b128 v[52:55], v35 offset:16384\ns_add_u32 s53, s49, 512\ns_waitcnt lgkmcnt(3)\nv_mfma_f32_16x16x32_bf16 v[96:99], v[40:43], v[192:195], v[96:99]\n"
"buffer_load_dwordx4 v10, s[12:15], s53 offen lds\nv_mfma_f32_16x16x32_bf16 v[100:103], v[40:43], v[196:199], v[100:103]\nds_read_b128 v[40:43], v32 offset:20480\ns_add_u32 m0, s52, 20480\n"
"s_waitcnt lgkmcnt(3)\nv_mfma_f32_16x16x32_bf16 v[104:107], v[44:47], v[192:195], v[104:107]\ns_add_u32 s53, s49, 640\nv_mfma_f32_16x16x32_bf16 v[108:111], v[44:47], v[196:199], v[108:111]\n"
"ds_read_b128 v[44:47], v33 offset:20480\nbuffer_load_dwordx4 v10, s[12:15], s53 offen lds\ns_waitcnt lgkmcnt(3)\nv_mfma_f32_16x16x32_bf16 v[96:99], v[48:51], v[200:203], v[96:99]\n"
"s_add_u32 m0, s52, 24576\nv_mfma_f32_16x16x32_bf16 v[100:103], v[48:51], v[204:207], v[100:103]\nds_read_b128 v[48:51], v34 offset:20480\ns_add_u32 s53, s49, 768\n"
"s_waitcnt lgkmcnt(3)\nv_mfma_f32_16x16x32_bf16 v[104:107], v[52:55], v[200:203], v[104:107]\nbuffer_load_dwordx4 v10, s[12:15], s53 offen lds\nv_mfma_f32_16x16x32_bf16 v[108:111], v[52:55], v[204:207], v[108:111]\n"
"ds_read_b128 v[52:55], v35 offset:20480\ns_mov_b32 s53, 236544\nbuffer_load_dwordx4 a[60:63], v14, s[16:19], s53 offen sc1\ns_waitcnt lgkmcnt(3)\n"
"v_mfma_f32_16x16x32_bf16 v[96:99], v[40:43], v[208:211], v[96:99]\ns_mov_b32 s53, 8192\nbuffer_load_dwordx4 a[64:67], v14, s[16:19], s53 offen sc1\nv_mfma_f32_16x16x32_bf16 v[100:103], v[40:43], v[212:215], v[100:103]\n"
"ds_read_b128 v[40:43], v32 offset:24576\ns_mov_b32 s53, 237568\nbuffer_load_dwordx4 a[68:71], v14, s[16:19], s53 offen sc1\ns_waitcnt lgkmcnt(3)\n"
"v_mfma_f32_16x16x32_bf16 v[104:107], v[44:47], v[208:211], v[104:107]\ns_mov_b32 s53, 9216\nbuffer_load_dwordx4 a[72:75], v14, s[16:19], s53 offen sc1\nv_mfma_f32_16x16x32_bf16 v[108:111], v[44:47], v[212:215], v[108:111]\n"
"ds_read_b128 v[44:47], v33 offset:24576\ns_mov_b32 s53, 238592\nbuffer_load_dwordx4 a[76:79], v14, s[16:19], s53 offen sc1\ns_waitcnt lgkmcnt(3)\n"
"v_mfma_f32_16x16x32_bf16 v[96:99], v[48:51], v[216:219], v[96:99]\ns_add_u32 s54, s51, 32768\ns_cmp_eq_u32 s54, 163840\nv_mfma_f32_16x16x32_bf16 v[100:103], v[48:51], v[220:223], v[100:103]\n"
"s_cselect_b32 s54, 0, s54\ns_sub_u32 s54, s54, s51\nds_read_b128 v[48:51], v34 offset:24576\ns_waitcnt lgkmcnt(3)\n"
"v_mfma_f32_16x16x32_bf16 v[104:107], v[52:55], v[216:219], v[104:107]\nv_mfma_f32_16x16x32_bf16 v[108:111], v[52:55], v[220:223], v[108:111]\nds_read_b128 v[52:55], v35 offset:24576\ns_waitcnt lgkmcnt(3)\n"
"v_mfma_f32_16x16x32_bf16 v[96:99], v[40:43], v[224:227], v[96:99]\nv_add_u32 v32, s54, v32\nv_mfma_f32_16x16x32_bf16 v[100:103], v[40:43], v[228:231], v[100:103]\nds_read_b128 v[40:43], v32 offset:0\n"
"s_waitcnt lgkmcnt(3)\nv_mfma_f32_16x16x32_bf16 v[104:107], v[44:47], v[224:227], v[104:107]\nv_add_u32 v33, s54, v33\nv_mfma_f32_16x16x32_bf16 v[108:111], v[44:47], v[228:231], v[108:111]\n"
"ds_read_b128 v[44:47], v33 offset:0\ns_waitcnt lgkmcnt(3)\nv_mfma_f32_16x16x32_bf16 v[96:99], v[48:51], v[232:235], v[96:99]\nv_add_u32 v34, s54, v34\n"
"v_mfma_f32_16x16x32_bf16 v[100:103], v[48:51], v[236:239], v[100:103]\nds_read_b128 v[48:51], v34 offset:0\ns_waitcnt lgkmcnt(3)\nv_mfma_f32_16x16x32_bf16 v[104:107], v[52:55], v[232:235], v[104:107]\n"
"v_add_u32 v35, s54, v35\nv_mfma_f32_16x16x32_bf16 v[108:111], v[52:55], v[236:239], v[108:111]\nds_read_b128 v[52:55], v35 offset:0\ns_mov_b32 s52, s47\n"
"s_add_u32 s47, s51, s32\ns_add_u32 s51, s51, s54\ns_add_u32 s49, s49, 28672\ns_waitcnt vmcnt(12)\n"
"s_barrier\ns_waitcnt lgkmcnt(3)\nv_mfma_f32_16x16x32_bf16 v[64:67], v[40:43], v[128:131], 0\nv_mov_b32 v36, s48\n"
"s_add_u32 s48, s48, 1\ns_nop 0\nbuffer_store_dword v36, v19, s[24:27], s33 offen sc1\nv_mfma_f32_16x16x32_bf16 v[68:71], v[40:43], v[132:135], 0\n"
"ds_read_b128 v[40:43], v32 offset:4096\ns_waitcnt lgkmcnt(3)\nv_mfma_f32_16x16x32_bf16 v[72:75], v[44:47], v[128:131], 0\nv_mfma_f32_16x16x32_bf16 v[76:79], v[44:47], v[132:135], 0\n"
"ds_read_b128 v[44:47], v33 offset:4096\nv_cvt_pk_bf16_f32 v56, v96, v97\ns_waitcnt lgkmcnt(3)\nv_mfma_f32_16x16x32_bf16 v[64:67], v[48:51], v[136:139], v[64:67]\n"
"v_cvt_pk_bf16_f32 v57, v98, v99\nv_mfma_f32_16x16x32_bf16 v[68:71], v[48:51], v[140:143], v[68:71]\nds_read_b128 v[48:51], v34 offset:4096\nv_cvt_pk_bf16_f32 v58, v104, v105\n"
"s_waitcnt lgkmcnt(3)\nv_mfma_f32_16x16x32_bf16 v[72:75], v[52:55], v[136:139], v[72:75]\nv_cvt_pk_bf16_f32 v59, v106, v107\nv_mfma_f32_16x16x32_bf16 v[76:79], v[52:55], v[140:143], v[76:79]\n"
"ds_read_b128 v[52:55], v35 offset:4096\ns_waitcnt lgkmcnt(3)\nv_mfma_f32_16x16x32_bf16 v[64:67], v[40:43], v[144:147], v[64:67]\nv_mfma_f32_16x16x32_bf16 v[68:71], v[40:43], v[148:151], v[68:71]\n"
"ds_read_b128 v[40:43], v32 offset:8192\nbuffer_store_dwordx4 v[56:59], v15, s[20:23], s50 offen sc1\ns_waitcnt lgkmcnt(3)\nv_mfma_f32_16x16x32_bf16 v[72:75], v[44:47], v[144:147], v[72:75]\n"
"v_cvt_pk_bf16_f32 v60, v100, v101\nv_mfma_f32_16x16x32_bf16 v[76:79], v[44:47], v[148:151], v[76:79]\nds_read_b128 v[44:47], v33 offset:8192\nv_cvt_pk_bf16_f32 v61, v102, v103\n"
"s_waitcnt lgkmcnt(3)\nv_mfma_f32_16x16x32_bf16 v[64:67], v[48:51], v[152:155], v[64:67]\nv_cvt_pk_bf16_f32 v62, v108, v109\nv_mfma_f32_16x16x32_bf16 v[68:71], v[48:51], v[156:159], v[68:71]\n"
"ds_read_b128 v[48:51], v34 offset:8192\nv_cvt_pk_bf16_f32 v63, v110, v111\ns_waitcnt lgkmcnt(3)\nv_mfma_f32_16x16x32_bf16 v[72:75], v[52:55], v[152:155], v[72:75]\n"
"v_mfma_f32_16x16x32_bf16 v[76:79], v[52:55], v[156:159], v[76:79]\nds_read_b128 v[52:55], v35 offset:8192\ns_waitcnt lgkmcnt(3)\nv_mfma_f32_16x16x32_bf16 v[64:67], v[40:43], v[160:163], v[64:67]\n"
"buffer_store_dwordx4 v[60:63], v16, s[20:23], s50 offen sc1\nv_mfma_f32_16x16x32_bf16 v[68:71], v[40:43], v[164:167], v[68:71]\nds_read_b128 v[40:43], v32 offset:12288\ns_add_u32 s50, s50, s36\n"
"s_waitcnt lgkmcnt(3)\nv_mfma_f32_16x16x32_bf16 v[72:75], v[44:47], v[160:163], v[72:75]\ns_add_u32 m0, s52, 0\nv_mfma_f32_16x16x32_bf16 v[76:79], v[44:47], v[164:167], v[76:79]\n"
"ds_read_b128 v[44:47], v33 offset:12288\ns_add_u32 s53, s49, 0\ns_waitcnt lgkmcnt(3)\nv_mfma_f32_16x16x32_bf16 v[64:67], v[48:51], v[168:171], v[64:67]\n"
"buffer_load_dwordx4 v10, s[12:15], s53 offen lds\nv_mfma_f32_16x16x32_bf16 v[68:71], v[48:51], v[172:175], v[68:71]\nds_read_b128 v[48:51], v34 offset:12288\ns_add_u32 m0, s52, 4096\n"
"s_waitcnt lgkmcnt(3)\nv_mfma_f32_16x16x32_bf16 v[72:75], v[52:55], v[168:171], v[72:75]\ns_add_u32 s53, s49, 128\nv_mfma_f32_16x16x32_bf16 v[76:79], v[52:55], v[172:175], v[76:79]\n"
"ds_read_b128 v[52:55], v35 offset:12288\nbuffer_load_dwordx4 v10, s[12:15], s53 offen lds\ns_waitcnt lgkmcnt(3)\nv_mfma_f32_16x16x32_bf16 v[64:67], v[40:43], v[176:179], v[64:67]\n"
"s_add_u32 m0, s52, 8192\nv_mfma_f32_16x16x32_bf16 v[68:71], v[40:43], v[180:183], v[68:71]\nds_read_b128 v[40:43], v32 offset:16384\ns_add_u32 s53, s49, 256\n"
"s_waitcnt lgkmcnt(3)\nv_mfma_f32_16x16x32_bf16 v[72:75], v[44:47], v[176:179], v[72:75]\nbuffer_load_dwordx4 v10, s[12:15], s53 offen lds\nv_mfma_f32_16x16x32_bf16 v[76:79], v[44:47], v[180:183], v[76:79]\n"
"ds_read_b128 v[44:47], v33 offset:16384\ns_add_u32 m0, s52, 12288\ns_waitcnt lgkmcnt(3)\nv_mfma_f32_16x16x32_bf16 v[64:67], v[48:51], v[184:187], v[64:67]\n"
"s_add_u32 s53, s49, 384\nv_mfma_f32_16x16x32_bf16 v[68:71], v[48:51], v[188:191], v[68:71]\nds_read_b128 v[48:51], v34 offset:16384\nbuffer_load_dwordx4 v10, s[12:15], s53 offen lds\n"
"s_waitcnt lgkmcnt(3)\nv_mfma_f32_16x16x32_bf16 v[72:75], v[52:55], v[184:187], v[72:75]\ns_add_u32 m0, s52, 16384\nv_mfma_f32_16x16x32_bf16 v[76:79], v[52:55], v[188:191], v[76:79]\n"
"ds_read_b128 v[52:55], v35 offset:16384\ns_add_u32 s53, s49, 512\ns_waitcnt lgkmcnt(3)\nv_mfma_f32_16x16x32_bf16 v[64:67], v[40:43], v[192:195], v[64:67]\n"
"buffer_load_dwordx4 v10, s[12:15], s53 offen lds\nv_mfma_f32_16x16x32_bf16 v[68:71], v[40:43], v[196:199], v[68:71]\nds_read_b128 v[40:43], v32 offset:20480\ns_add_u32 m0, s52, 20480\n"
"s_waitcnt lgkmcnt(3)\nv_mfma_f32_16x16x32_bf16 v[72:75], v[44:47], v[192:195], v[72:75]\ns_add_u32 s53, s49, 640\nv_mfma_f32_16x16x32_bf16 v[76:79], v[44:47], v[196:199], v[76:79]\n"
"ds_read_b128 v[44:47], v33 offset:20480\nbuffer_load_dwordx4 v10, s[12:15], s53 offen lds\ns_waitcnt lgkmcnt(3)\nv_mfma_f32_16x16x32_bf16 v[64:67], v[48:51], v[200:203], v[64:67]\n"
"s_add_u32 m0, s52, 24576\nv_mfma_f32_16x16x32_bf16 v[68:71], v[48:51], v[204:207], v[68:71]\nds_read_b128 v[48:51], v34 offset:20480\ns_add_u32 s53, s49, 768\n"
"s_waitcnt lgkmcnt(3)\nv_mfma_f32_16x16x32_bf16 v[72:75], v[52:55], v[200:203], v[72:75]\nbuffer_load_dwordx4 v10, s[12:15], s53 offen lds\nv_mfma_f32_16x16x32_bf16 v[76:79], v[52:55], v[204:207], v[76:79]\n"
"ds_read_b128 v[52:55], v35 offset:20480\ns_mov_b32 s53, 10240\nbuffer_load_dwordx4 a[80:83], v14, s[16:19], s53 offen sc1\ns_waitcnt lgkmcnt(3)\n"
"v_mfma_f32_16x16x32_bf16 v[64:67], v[40:43], v[208:211], v[64:67]\ns_mov_b32 s53, 239616\nbuffer_load_dwordx4 a[84:87], v14, s[16:19], s53 offen sc1\nv_mfma_f32_16x16x32_bf16 v[68:71], v[40:43], v[212:215], v[68:71]\n"
"ds_read_b128 v[40:43], v32 offset:24576\ns_mov_b32 s53, 11264\nbuffer_load_dwordx4 a[88:91], v14, s[16:19], s53 offen sc1\ns_waitcnt lgkmcnt(3)\n"
"v_mfma_f32_16x16x32_bf16 v[72:75], v[44:47], v[208:211], v[72:75]\ns_mov_b32 s53, 240640\nbuffer_load_dwordx4 a[92:95], v14, s[16:19], s53 offen sc1\nv_mfma_f32_16x16x32_bf16 v[76:79], v[44:47], v[212:215], v[76:79]\n"
"ds_read_b128 v[44:47], v33 offset:24576\ns_mov_b32 s53, 12288\nbuffer_load_dwordx4 a[96:99], v14, s[16:19], s53 offen sc1\ns_waitcnt lgkmcnt(3)\n"
"v_mfma_f32_16x16x32_bf16 v[64:67], v[48:51], v[216:219], v[64:67]\ns_add_u32 s54, s51, 32768\ns_cmp_eq_u32 s54, 163840\nv_mfma_f32_16x16x32_bf16 v[68:71], v[48:51], v[220:223], v[68:71]\n"
"s_cselect_b32 s54, 0, s54\ns_sub_u32 s54, s54, s51\nds_read_b128 v[48:51], v34 offset:24576\ns_waitcnt lgkmcnt(3)\n"
"v_mfma_f32_16x16x32_bf16 v[72:75], v[52:55], v[216:219], v[72:75]\nv_mfma_f32_16x16x32_bf16 v[76:79], v[52:55], v[220:223], v[76:79]\nds_read_b128 v[52:55], v35 offset:24576\ns_waitcnt lgkmcnt(3)\n"
"v_mfma_f32_16x16x32_bf16 v[64:67], v[40:43], v[224:227], v[64:67]\nv_add_u32 v32, s54, v32\nv_mfma_f32_16x16x32_bf16 v[68:71], v[40:43], v[228:231], v[68:71]\nds_read_b128 v[40:43], v32 offset:0\n"
"s_waitcnt lgkmcnt(3)\nv_mfma_f32_16x16x32_bf16 v[72:75], v[44:47], v[224:227], v[72:75]\nv_add_u32 v33, s54, v33\nv_mfma_f32_16x16x32_bf16 v[76:79], v[44:47], v[228:231], v[76:79]\n"
"ds_read_b128 v[44:47], v33 offset:0\ns_waitcnt lgkmcnt(3)\nv_mfma_f32_16x16x32_bf16 v[64:67], v[48:51], v[232:235], v[64:67]\nv_add_u32 v34, s54, v34\n"
"v_mfma_f32_16x16x32_bf16 v[68:71], v[48:51], v[236:239], v[68:71]\nds_read_b128 v[48:51], v34 offset:0\ns_waitcnt lgkmcnt(3)\nv_mfma_f32_16x16x32_bf16 v[72:75], v[52:55], v[232:235], v[72:75]\n"
"v_add_u32 v35, s54, v35\nv_mfma_f32_16x16x32_bf16 v[76:79], v[52:55], v[236:239], v[76:79]\nds_read_b128 v[52:55], v35 offset:0\ns_mov_b32 s52, s47\n"
"s_add_u32 s47, s51, s32\ns_add_u32 s51, s51, s54\ns_add_u32 s49, s49, 28672\ns_waitcnt vmcnt(12)\n"
"s_barrier\ns_waitcnt lgkmcnt(3)\nv_mfma_f32_16x16x32_bf16 v[96:99], v[40:43], v[128:131], 0\nv_mov_b32 v36, s48\n"
"s_add_u32 s48, s48, 1\ns_nop 0\nbuffer_store_dword v36, v19, s[24:27], s33 offen sc1\nv_mfma_f32_16x16x32_bf16 v[100:103], v[40:43], v[132:135], 0\n"
"ds_read_b128 v[40:43], v32 offset:4096\ns_waitcnt lgkmcnt(3)\nv_mfma_f32_16x16x32_bf16 v[104:107], v[44:47], v[128:131], 0\nv_mfma_f32_16x16x32_bf16 v[108:111], v[44:47], v[132:135], 0\n"
"ds_read_b128 v[44:47], v33 offset:4096\nv_cvt_pk_bf16_f32 v56, v64, v65\ns_waitcnt lgkmcnt(3)\nv_mfma_f32_16x16x32_bf16 v[96:99], v[48:51], v[136:139], v[96:99]\n"
"v_cvt_pk_bf16_f32 v57, v66, v67\nv_mfma_f32_16x16x32_bf16 v[100:103], v[48:51], v[140:143], v[100:103]\nds_read_b128 v[48:51], v34 offset:4096\nv_cvt_pk_bf16_f32 v58, v72, v73\n"
"s_waitcnt lgkmcnt(3)\nv_mfma_f32_16x16x32_bf16 v[104:107], v[52:55], v[136:139], v[104:107]\nv_cvt_pk_bf16_f32 v59, v74, v75\nv_mfma_f32_16x16x32_bf16 v[108:111], v[52:55], v[140:143], v[108:111]\n"
"ds_read_b128 v[52:55], v35 offset:4096\ns_waitcnt lgkmcnt(3)\nv_mfma_f32_16x16x32_bf16 v[96:99], v[40:43], v[144:147], v[96:99]\nv_mfma_f32_16x16x32_bf16 v[100:103], v[40:43], v[148:151], v[100:103]\n"
"ds_read_b128 v[40:43], v32 offset:8192\nbuffer_store_dwordx4 v[56:59], v15, s[20:23], s50 offen sc1\ns_waitcnt lgkmcnt(3)\nv_mfma_f32_16x16x32_bf16 v[104:107], v[44:47], v[144:147], v[104:107]\n"
"v_cvt_pk_bf16_f32 v60, v68, v69\nv_mfma_f32_16x16x32_bf16 v[108:111], v[44:47], v[148:151], v[108:111]\nds_read_b128 v[44:47], v33 offset:8192\nv_cvt_pk_bf16_f32 v61, v70, v71\n"
"s_waitcnt lgkmcnt(3)\nv_mfma_f32_16x16x32_bf16 v[96:99], v[48:51], v[152:155], v[96:99]\nv_cvt_pk_bf16_f32 v62, v76, v77\nv_mfma_f32_16x16x32_bf16 v[100:103], v[48:51], v[156:159], v[100:103]\n"
"ds_read_b128 v[48:51], v34 offset:8192\nv_cvt_pk_bf16_f32 v63, v78, v79\ns_waitcnt lgkmcnt(3)\nv_mfma_f32_16x16x32_bf16 v[104:107], v[52:55], v[152:155], v[104:107]\n"
"v_mfma_f32_16x16x32_bf16 v[108:111], v[52:55], v[156:159], v[108:111]\nds_read_b128 v[52:55], v35 offset:8192\ns_waitcnt lgkmcnt(3)\nv_mfma_f32_16x16x32_bf16 v[96:99], v[40:43], v[160:163], v[96:99]\n"
"buffer_store_dwordx4 v[60:63], v16, s[20:23], s50 offen sc1\nv_mfma_f32_16x16x32_bf16 v[100:103], v[40:43], v[164:167], v[100:103]\nds_read_b128 v[40:43], v32 offset:12288\ns_add_u32 s50, s50, s36\n"
"s_waitcnt lgkmcnt(3)\nv_mfma_f32_16x16x32_bf16 v[104:107], v[44:47], v[160:163], v[104:107]\ns_add_u32 m0, s52, 0\nv_mfma_f32_16x16x32_bf16 v[108:111], v[44:47], v[164:167], v[108:111]\n"
"ds_read_b128 v[44:47], v33 offset:12288\ns_add_u32 s53, s49, 0\ns_waitcnt lgkmcnt(3)\nv_mfma_f32_16x16x32_bf16 v[96:99], v[48:51], v[168:171], v[96:99]\n"
"buffer_load_dwordx4 v10, s[12:15], s53 offen lds\nv_mfma_f32_16x16x32_bf16 v[100:103], v[48:51], v[172:175], v[100:103]\nds_read_b128 v[48:51], v34 offset:12288\ns_add_u32 m0, s52, 4096\n"
"s_waitcnt lgkmcnt(3)\nv_mfma_f32_16x16x32_bf16 v[104:107], v[52:55], v[168:171], v[104:107]\ns_add_u32 s53, s49, 128\nv_mfma_f32_16x16x32_bf16 v[108:111], v[52:55], v[172:175], v[108:111]\n"
"ds_read_b128 v[52:55], v35 offset:12288\nbuffer_load_dwordx4 v10, s[12:15], s53 offen lds\ns_waitcnt lgkmcnt(3)\nv_mfma_f32_16x16x32_bf16 v[96:99], v[40:43], v[176:179], v[96:99]\n"
"s_add_u32 m0, s52, 8192\nv_mfma_f32_16x16x32_bf16 v[100:103], v[40:43], v[180:183], v[100:103]\nds_read_b128 v[40:43], v32 offset:16384\ns_add_u32 s53, s49, 256\n"
"s_waitcnt lgkmcnt(3)\nv_mfma_f32_16x16x32_bf16 v[104:107], v[44:47], v[176:179], v[104:107]\nbuffer_load_dwordx4 v10, s[12:15], s53 offen lds\nv_mfma_f32_16x16x32_bf16 v[108:111], v[44:47], v[180:183], v[108:111]\n"
"ds_read_b128 v[44:47], v33 offset:16384\ns_add_u32 m0, s52, 12288\ns_waitcnt lgkmcnt(3)\nv_mfma_f32_16x16x32_bf16 v[96:99], v[48:51], v[184:187], v[96:99]\n"
"s_add_u32 s53, s49, 384\nv_mfma_f32_16x16x32_bf16 v[100:103], v[48:51], v[188:191], v[100:103]\nds_read_b128 v[48:51], v34 offset:16384\nbuffer_load_dwordx4 v10, s[12:15], s53 offen lds\n"
"s_waitcnt lgkmcnt(3)\nv_mfma_f32_16x16x32_bf16 v[104:107], v[52:55], v[184:187], v[104:107]\ns_add_u32 m0, s52, 16384\nv_mfma_f32_16x16x32_bf16 v[108:111], v[52:55], v[188:191], v[108:111]\n"
"ds_read_b128 v[52:55], v35 offset:16384\ns_add_u32 s53, s49, 512\ns_waitcnt lgkmcnt(3)\nv_mfma_f32_16x16x32_bf16 v[96:99], v[40:43], v[192:195], v[96:99]\n"
"buffer_load_dwordx4 v10, s[12:15], s53 offen lds\nv_mfma_f32_16x16x32_bf16 v[100:103], v[40:43], v[196:199], v[100:103]\nds_read_b128 v[40:43], v32 offset:20480\ns_add_u32 m0, s52, 20480\n"
"s_waitcnt lgkmcnt(3)\nv_mfma_f32_16x16x32_bf16 v[104:107], v[44:47], v[192:195], v[104:107]\ns_add_u32 s53, s49, 640\nv_mfma_f32_16x16x32_bf16 v[108:111], v[44:47], v[196:199], v[108:111]\n"
"ds_read_b128 v[44:47], v33 offset:20480\nbuffer_load_dwordx4 v10, s[12:15], s53 offen lds\ns_waitcnt lgkmcnt(3)\nv_mfma_f32_16x16x32_bf16 v[96:99], v[48:51], v[200:203], v[96:99]\n"
"s_add_u32 m0, s52, 24576\nv_mfma_f32_16x16x32_bf16 v[100:103], v[48:51], v[204:207], v[100:103]\nds_read_b128 v[48:51], v34 offset:20480\ns_add_u32 s53, s49, 768\n"
"s_waitcnt lgkmcnt(3)\nv_mfma_f32_16x16x32_bf16 v[104:107], v[52:55], v[200:203], v[104:107]\nbuffer_load_dwordx4 v10, s[12:15], s53 offen lds\nv_mfma_f32_16x16x32_bf16 v[108:111], v[52:55], v[204:207], v[108:111]\n"
"ds_read_b128 v[52:55], v35 offset:20480\ns_mov_b32 s53, 241664\nbuffer_load_dwordx4 a[100:103], v14, s[16:19], s53 offen sc1\ns_waitcnt lgkmcnt(3)\n"
"v_mfma_f32_16x16x32_bf16 v[96:99], v[40:43], v[208:211], v[96:99]\ns_mov_b32 s53, 13312\nbuffer_load_dwordx4 a[104:107], v14, s[16:19], s53 offen sc1\nv_mfma_f32_16x16x32_bf16 v[100:103], v[40:43], v[212:215], v[100:103]\n"
"ds_read_b128 v[40:43], v32 offset:24576\ns_mov_b32 s53, 242688\nbuffer_load_dwordx4 a[108:111], v14, s[16:19], s53 offen sc1\ns_waitcnt lgkmcnt(3)\n"
"v_mfma_f32_16x16x32_bf16 v[104:107], v[44:47], v[208:211], v[104:107]\nv_mfma_f32_16x16x32_bf16 v[108:111], v[44:47], v[212:215], v[108:111]\nds_read_b128 v[44:47], v33 offset:24576\ns_waitcnt lgkmcnt(3)\n"
"v_mfma_f32_16x16x32_bf16 v[96:99], v[48:51], v[216:219], v[96:99]\ns_add_u32 s54, s51, 32768\ns_cmp_eq_u32 s54, 163840\nv_mfma_f32_16x16x32_bf16 v[100:103], v[48:51], v[220:223], v[100:103]\n"
"s_cselect_b32 s54, 0, s54\ns_sub_u32 s54, s54, s51\nds_read_b128 v[48:51], v34 offset:24576\ns_waitcnt lgkmcnt(3)\n"
"v_mfma_f32_16x16x32_bf16 v[104:107], v[52:55], v[216:219], v[104:107]\nv_mfma_f32_16x16x32_bf16 v[108:111], v[52:55], v[220:223], v[108:111]\nds_read_b128 v[52:55], v35 offset:24576\ns_waitcnt lgkmcnt(3)\n"
"v_mfma_f32_16x16x32_bf16 v[96:99], v[40:43], v[224:227], v[96:99]\nv_add_u32 v32, s54, v32\nv_mfma_f32_16x16x32_bf16 v[100:103], v[40:43], v[228:231], v[100:103]\nds_read_b128 v[40:43], v32 offset:0\n"
"s_waitcnt lgkmcnt(3)\nv_mfma_f32_16x16x32_bf16 v[104:107], v[44:47], v[224:227], v[104:107]\nv_add_u32 v33, s54, v33\nv_mfma_f32_16x16x32_bf16 v[108:111], v[44:47], v[228:231], v[108:111]\n"
"ds_read_b128 v[44:47], v33 offset:0\ns_waitcnt lgkmcnt(3)\nv_mfma_f32_16x16x32_bf16 v[96:99], v[48:51], v[232:235], v[96:99]\nv_add_u32 v34, s54, v34\n"
"v_mfma_f32_16x16x32_bf16 v[100:103], v[48:51], v[236:239], v[100:103]\nds_read_b128 v[48:51], v34 offset:0\ns_waitcnt lgkmcnt(3)\nv_mfma_f32_16x16x32_bf16 v[104:107], v[52:55], v[232:235], v[104:107]\n"
"v_add_u32 v35, s54, v35\nv_mfma_f32_16x16x32_bf16 v[108:111], v[52:55], v[236:239], v[108:111]\nds_read_b128 v[52:55], v35 offset:0\ns_mov_b32 s52, s47\n"
"s_add_u32 s47, s51, s32\ns_add_u32 s51, s51, s54\ns_add_u32 s49, s49, 28672\ns_cmp_lg_u32 s34, 0\n"
"s_cbranch_scc1 fg1_stay\ns_nop 7\nv_cvt_pk_bf16_f32 v56, v96, v97\nv_cvt_pk_bf16_f32 v57, v98, v99\n"
"v_cvt_pk_bf16_f32 v58, v104, v105\nv_cvt_pk_bf16_f32 v59, v106, v107\ns_nop 0\ns_nop 0\n"
"buffer_store_dwordx4 v[56:59], v15, s[20:23], s50 offen sc1\nv_cvt_pk_bf16_f32 v60, v100, v101\nv_cvt_pk_bf16_f32 v61, v102, v103\nv_cvt_pk_bf16_f32 v62, v108, v109\n"
"v_cvt_pk_bf16_f32 v63, v110, v111\ns_nop 0\ns_nop 0\nbuffer_store_dwordx4 v[60:63], v16, s[20:23], s50 offen sc1\n"
"s_add_u32 s48, s28, 64\nv_mov_b32 v36, s48\ns_waitcnt vmcnt(0) lgkmcnt(0)\ns_barrier\n"
"buffer_store_dword v36, v19, s[24:27], s33 offen sc1\ns_branch fg1_end\nfg1_stay:\ns_waitcnt vmcnt(0)\n"
"s_barrier\ns_waitcnt lgkmcnt(3)\nv_mfma_f32_16x16x32_bf16 v[64:67], v[40:43], v[128:131], 0\nv_mov_b32 v36, s48\n"
"s_add_u32 s48, s48, 1\ns_nop 0\nbuffer_store_dword v36, v19, s[24:27], s33 offen sc1\nv_mfma_f32_16x16x32_bf16 v[68:71], v[40:43], v[132:135], 0\n"
"v_mfma_f32_16x16x32_bf16 v[72:75], v[40:43], a[0:3], 0\nv_mfma_f32_16x16x32_bf16 v[76:79], v[40:43], a[4:7], 0\nds_read_b128 v[40:43], v32 offset:4096\nv_cvt_pk_bf16_f32 v56, v96, v97\n"
"s_waitcnt lgkmcnt(3)\nv_mfma_f32_16x16x32_bf16 v[80:83], v[44:47], v[128:131], 0\nv_cvt_pk_bf16_f32 v57, v98, v99\nv_mfma_f32_16x16x32_bf16 v[84:87], v[44:47], v[132:135], 0\n"
"v_cvt_pk_bf16_f32 v58, v104, v105\nv_mfma_f32_16x16x32_bf16 v[88:91], v[44:47], a[0:3], 0\nv_cvt_pk_bf16_f32 v59, v106, v107\nv_mfma_f32_16x16x32_bf16 v[92:95], v[44:47], a[4:7], 0\n"
"ds_read_b128 v[44:47], v33 offset:4096\ns_waitcnt lgkmcnt(3)\nv_mfma_f32_16x16x32_bf16 v[64:67], v[48:51], v[136:139], v[64:67]\nv_mfma_f32_16x16x32_bf16 v[68:71], v[48:51], v[140:143], v[68:71]\n"
"buffer_store_dwordx4 v[56:59], v15, s[20:23], s50 offen sc1\nv_mfma_f32_16x16x32_bf16 v[72:75], v[48:51], a[8:11], v[72:75]\nv_cvt_pk_bf16_f32 v60, v100, v101\nv_mfma_f32_16x16x32_bf16 v[76:79], v[48:51], a[12:15], v[76:79]\n"
"ds_read_b128 v[48:51], v34 offset:4096\nv_cvt_pk_bf16_f32 v61, v102, v103\ns_waitcnt lgkmcnt(3)\nv_mfma_f32_16x16x32_bf16 v[80:83], v[52:55], v[136:139], v[80:83]\n"
"v_cvt_pk_bf16_f32 v62, v108, v109\nv_mfma_f32_16x16x32_bf16 v[84:87], v[52:55], v[140:143], v[84:87]\nv_cvt_pk_bf16_f32 v63, v110, v111\nv_mfma_f32_16x16x32_bf16 v[88:91], v[52:55], a[8:11], v[88:91]\n"
"v_mfma_f32_16x16x32_bf16 v[92:95], v[52:55], a[12:15], v[92:95]\nds_read_b128 v[52:55], v35 offset:4096\ns_waitcnt lgkmcnt(3)\nv_mfma_f32_16x16x32_bf16 v[64:67], v[40:43], v[144:147], v[64:67]\n"
"buffer_store_dwordx4 v[60:63], v16, s[20:23], s50 offen sc1\nv_mfma_f32_16x16x32_bf16 v[68:71], v[40:43], v[148:151], v[68:71]\ns_add_u32 s50, s50, s36\nv_mfma_f32_16x16x32_bf16 v[72:75], v[40:43], a[16:19], v[72:75]\n"
"s_add_u32 m0, s52, 0\nv_mfma_f32_16x16x32_bf16 v[76:79], v[40:43], a[20:23], v[76:79]\nds_read_b128 v[40:43], v32 offset:8192\ns_add_u32 s53, s49, 0\n"
"s_waitcnt lgkmcnt(3)\nv_mfma_f32_16x16x32_bf16 v[80:83], v[44:47], v[144:147], v[80:83]\nbuffer_load_dwordx4 v10, s[12:15], s53 offen lds\nv_mfma_f32_16x16x32_bf16 v[84:87], v[44:47], v[148:151], v[84:87]\n"
"v_mfma_f32_16x16x32_bf16 v[88:91], v[44:47], a[16:19], v[88:91]\nv_mfma_f32_16x16x32_bf16 v[92:95], v[44:47], a[20:23], v[92:95]\nds_read_b128 v[44:47], v33 offset:8192\ns_waitcnt lgkmcnt(3)\n"
"v_mfma_f32_16x16x32_bf16 v[64:67], v[48:51], v[152:155], v[64:67]\nv_mfma_f32_16x16x32_bf16 v[68:71], v[48:51], v[156:159], v[68:71]\nv_mfma_f32_16x16x32_bf16 v[72:75], v[48:51], a[24:27], v[72:75]\nv_mfma_f32_16x16x32_bf16 v[76:79], v[48:51], a[28:31], v[76:79]\n"
"ds_read_b128 v[48:51], v34 offset:8192\ns_waitcnt lgkmcnt(3)\nv_mfma_f32_16x16x32_bf16 v[80:83], v[52:55], v[152:155], v[80:83]\nv_mfma_f32_16x16x32_bf16 v[84:87], v[52:55], v[156:159], v[84:87]\n"
"s_add_u32 m0, s52, 4096\nv_mfma_f32_16x16x32_bf16 v[88:91], v[52:55], a[24:27], v[88:91]\ns_add_u32 s53, s49, 128\nv_mfma_f32_16x16x32_bf16 v[92:95], v[52:55], a[28:31], v[92:95]\n"
"ds_read_b128 v[52:55], v35 offset:8192\nbuffer_load_dwordx4 v10, s[12:15], s53 offen lds\ns_waitcnt lgkmcnt(3)\nv_mfma_f32_16x16x32_bf16 v[64:67], v[40:43], v[160:163], v[64:67]\n"
"v_mfma_f32_16x16x32_bf16 v[68:71], v[40:43], v[164:167], v[68:71]\nv_mfma_f32_16x16x32_bf16 v[72:75], v[40:43], a[32:35], v[72:75]\nv_mfma_f32_16x16x32_bf16 v[76:79], v[40:43], a[36:39], v[76:79]\nds_read_b128 v[40:43], v32 offset:12288\n"
"s_waitcnt lgkmcnt(3)\nv_mfma_f32_16x16x32_bf16 v[80:83], v[44:47], v[160:163], v[80:83]\nv_mfma_f32_16x16x32_bf16 v[84:87], v[44:47], v[164:167], v[84:87]\nv_mfma_f32_16x16x32_bf16 v[88:91], v[44:47], a[32:35], v[88:91]\n"
"v_mfma_f32_16x16x32_bf16 v[92:95], v[44:47], a[36:39], v[92:95]\nds_read_b128 v[44:47], v33 offset:12288\ns_waitcnt lgkmcnt(3)\nv_mfma_f32_16x16x32_bf16 v[64:67], v[48:51], v[168:171], v[64:67]\n"
"s_add_u32 m0, s52, 8192\nv_mfma_f32_16x16x32_bf16 v[68:71], v[48:51], v[172:175], v[68:71]\ns_add_u32 s53, s49, 256\nv_mfma_f32_16x16x32_bf16 v[72:75], v[48:51], a[40:43], v[72:75]\n"
"buffer_load_dwordx4 v10, s[12:15], s53 offen lds\nv_mfma_f32_16x16x32_bf16 v[76:79], v[48:51], a[44:47], v[76:79]\nds_read_b128 v[48:51], v34 offset:12288\ns_waitcnt lgkmcnt(3)\n"
"v_mfma_f32_16x16x32_bf16 v[80:83], v[52:55], v[168:171], v[80:83]\nv_mfma_f32_16x16x32_bf16 v[84:87], v[52:55], v[172:175], v[84:87]\nv_mfma_f32_16x16x32_bf16 v[88:91], v[52:55], a[40:43], v[88:91]\nv_mfma_f32_16x16x32_bf16 v[92:95], v[52:55], a[44:47], v[92:95]\n"
"ds_read_b128 v[52:55], v35 offset:12288\ns_waitcnt lgkmcnt(3)\nv_mfma_f32_16x16x32_bf16 v[64:67], v[40:43], v[176:179], v[64:67]\nv_mfma_f32_16x16x32_bf16 v[68:71], v[40:43], v[180:183], v[68:71]\n"
"v_mfma_f32_16x16x32_bf16 v[72:75], v[40:43], a[48:51], v[72:75]\nv_mfma_f32_16x16x32_bf16 v[76:79], v[40:43], a[52:55], v[76:79]\nds_read_b128 v[40:43], v32 offset:16384\ns_add_u32 m0, s52, 12288\n"
"s_waitcnt lgkmcnt(3)\nv_mfma_f32_16x16x32_bf16 v[80:83], v[44:47], v[176:179], v[80:83]\ns_add_u32 s53, s49, 384\nv_mfma_f32_16x16x32_bf16 v[84:87], v[44:47], v[180:183], v[84:87]\n"
"buffer_load_dwordx4 v10, s[12:15], s53 offen lds\nv_mfma_f32_16x16x32_bf16 v[88:91], v[44:47], a[48:51], v[88:91]\nv_mfma_f32_16x16x32_bf16 v[92:95], v[44:47], a[52:55], v[92:95]\nds_read_b128 v[44:47], v33 offset:16384\n"
"s_waitcnt lgkmcnt(3)\nv_mfma_f32_16x16x32_bf16 v[64:67], v[48:51], v[184:187], v[64:67]\nv_mfma_f32_16x16x32_bf16 v[68:71], v[48:51], v[188:191], v[68:71]\nv_mfma_f32_16x16x32_bf16 v[72:75], v[48:51], a[56:59], v[72:75]\n"
"v_mfma_f32_16x16x32_bf16 v[76:79], v[48:51], a[60:63], v[76:79]\nds_read_b128 v[48:51], v34 offset:16384\ns_waitcnt lgkmcnt(3)\nv_mfma_f32_16x16x32_bf16 v[80:83], v[52:55], v[184:187], v[80:83]\n"
"v_mfma_f32_16x16x32_bf16 v[84:87], v[52:55], v[188:191], v[84:87]\nv_mfma_f32_16x16x32_bf16 v[88:91], v[52:55], a[56:59], v[88:91]\ns_add_u32 m0, s52, 16384\nv_mfma_f32_16x16x32_bf16 v[92:95], v[52:55], a[60:63], v[92:95]\n"
"ds_read_b128 v[52:55], v35 offset:16384\ns_add_u32 s53, s49, 512\ns_waitcnt lgkmcnt(3)\nv_mfma_f32_16x16x32_bf16 v[64:67], v[40:43], v[192:195], v[64:67]\n"
"buffer_load_dwordx4 v10, s[12:15], s53 offen lds\nv_mfma_f32_16x16x32_bf16 v[68:71], v[40:43], v[196:199], v[68:71]\nv_mfma_f32_16x16x32_bf16 v[72:75], v[40:43], a[64:67], v[72:75]\nv_mfma_f32_16x16x32_bf16 v[76:79], v[40:43], a[68:71], v[76:79]\n"
"ds_read_b128 v[40:43], v32 offset:20480\ns_waitcnt lgkmcnt(3)\nv_mfma_f32_16x16x32_bf16 v[80:83], v[44:47], v[192:195], v[80:83]\nv_mfma_f32_16x16x32_bf16 v[84:87], v[44:47], v[196:199], v[84:87]\n"
"v_mfma_f32_16x16x32_bf16 v[88:91], v[44:47], a[64:67], v[88:91]\nv_mfma_f32_16x16x32_bf16 v[92:95], v[44:47], a[68:71], v[92:95]\nds_read_b128 v[44:47], v33 offset:20480\ns_waitcnt lgkmcnt(3)\n"
"v_mfma_f32_16x16x32_bf16 v[64:67], v[48:51], v[200:203], v[64:67]\nv_mfma_f32_16x16x32_bf16 v[68:71], v[48:51], v[204:207], v[68:71]\ns_add_u32 m0, s52, 20480\nv_mfma_f32_16x16x32_bf16 v[72:75], v[48:51], a[72:75], v[72:75]\n"
"s_add_u32 s53, s49, 640\nv_mfma_f32_16x16x32_bf16 v[76:79], v[48:51], a[76:79], v[76:79]\nds_read_b128 v[48:51], v34 offset:20480\nbuffer_load_dwordx4 v10, s[12:15], s53 offen lds\n"
"s_waitcnt lgkmcnt(3)\nv_mfma_f32_16x16x32_bf16 v[80:83], v[52:55], v[200:203], v[80:83]\nv_mfma_f32_16x16x32_bf16 v[84:87], v[52:55], v[204:207], v[84:87]\nv_mfma_f32_16x16x32_bf16 v[88:91], v[52:55], a[72:75], v[88:91]\n"
"v_mfma_f32_16x16x32_bf16 v[92:95], v[52:55], a[76:79], v[92:95]\nds_read_b128 v[52:55], v35 offset:20480\ns_waitcnt lgkmcnt(3)\nv_mfma_f32_16x16x32_bf16 v[64:67], v[40:43], v[208:211], v[64:67]\n"
"v_mfma_f32_16x16x32_bf16 v[68:71], v[40:43], v[212:215], v[68:71]\nv_mfma_f32_16x16x32_bf16 v[72:75], v[40:43], a[80:83], v[72:75]\nv_mfma_f32_16x16x32_bf16 v[76:79], v[40:43], a[84:87], v[76:79]\nds_read_b128 v[40:43], v32 offset:24576\n"
"s_waitcnt lgkmcnt(3)\nv_mfma_f32_16x16x32_bf16 v[80:83], v[44:47], v[208:211], v[80:83]\ns_add_u32 m0, s52, 24576\nv_mfma_f32_16x16x32_bf16 v[84:87], v[44:47], v[212:215], v[84:87]\n"
"s_add_u32 s53, s49, 768\nv_mfma_f32_16x16x32_bf16 v[88:91], v[44:47], a[80:83], v[88:91]\nbuffer_load_dwordx4 v10, s[12:15], s53 offen lds\nv_mfma_f32_16x16x32_bf16 v[92:95], v[44:47], a[84:87], v[92:95]\n"
"ds_read_b128 v[44:47], v33 offset:24576\ns_waitcnt lgkmcnt(3)\nv_mfma_f32_16x16x32_bf16 v[64:67], v[48:51], v[216:219], v[64:67]\ns_add_u32 s54, s51, 32768\n"
"s_cmp_eq_u32 s54, 163840\nv_mfma_f32_16x16x32_bf16 v[68:71], v[48:51], v[220:223], v[68:71]\nv_mfma_f32_16x16x32_bf16 v[72:75], v[48:51], a[88:91], v[72:75]\nv_mfma_f32_16x16x32_bf16 v[76:79], v[48:51], a[92:95], v[76:79]\n"
"s_cselect_b32 s54, 0, s54\ns_sub_u32 s54, s54, s51\nds_read_b128 v[48:51], v34 offset:24576\ns_waitcnt lgkmcnt(3)\n"
"v_mfma_f32_16x16x32_bf16 v[80:83], v[52:55], v[216:219], v[80:83]\nv_mfma_f32_16x16x32_bf16 v[84:87], v[52:55], v[220:223], v[84:87]\nv_mfma_f32_16x16x32_bf16 v[88:91], v[52:55], a[88:91], v[88:91]\nv_mfma_f32_16x16x32_bf16 v[92:95], v[52:55], a[92:95], v[92:95]\n"
"ds_read_b128 v[52:55], v35 offset:24576\ns_waitcnt lgkmcnt(3)\nv_mfma_f32_16x16x32_bf16 v[64:67], v[40:43], v[224:227], v[64:67]\nv_add_u32 v32, s54, v32\n"
"v_mfma_f32_16x16x32_bf16 v[68:71], v[40:43], v[228:231], v[68:71]\nv_mfma_f32_16x16x32_bf16 v[72:75], v[40:43], a[96:99], v[72:75]\nv_mfma_f32_16x16x32_bf16 v[76:79], v[40:43], a[100:103], v[76:79]\nds_read_b128 v[40:43], v32 offset:0\n"
"s_waitcnt lgkmcnt(3)\nv_mfma_f32_16x16x32_bf16 v[80:83], v[44:47], v[224:227], v[80:83]\nv_add_u32 v33, s54, v33\nv_mfma_f32_16x16x32_bf16 v[84:87], v[44:47], v[228:231], v[84:87]\n"
"v_mfma_f32_16x16x32_bf16 v[88:91], v[44:47], a[96:99], v[88:91]\nv_mfma_f32_16x16x32_bf16 v[92:95], v[44:47], a[100:103], v[92:95]\nds_read_b128 v[44:47], v33 offset:0\ns_waitcnt lgkmcnt(3)\n"
"v_mfma_f32_16x16x32_bf16 v[64:67], v[48:51], v[232:235], v[64:67]\nv_add_u32 v34, s54, v34\nv_mfma_f32_16x16x32_bf16 v[68:71], v[48:51], v[236:239], v[68:71]\nv_mfma_f32_16x16x32_bf16 v[72:75], v[48:51], a[104:107], v[72:75]\n"
"v_mfma_f32_16x16x32_bf16 v[76:79], v[48:51], a[108:111], v[76:79]\nds_read_b128 v[48:51], v34 offset:0\ns_waitcnt lgkmcnt(3)\nv_mfma_f32_16x16x32_bf16 v[80:83], v[52:55], v[232:235], v[80:83]\n"
"v_add_u32 v35, s54, v35\nv_mfma_f32_16x16x32_bf16 v[84:87], v[52:55], v[236:239], v[84:87]\nv_mfma_f32_16x16x32_bf16 v[88:91], v[52:55], a[104:107], v[88:91]\nv_mfma_f32_16x16x32_bf16 v[92:95], v[52:55], a[108:111], v[92:95]\n"
"ds_read_b128 v[52:55], v35 offset:0\ns_mov_b32 s52, s47\ns_add_u32 s47, s51, s32\ns_add_u32 s51, s51, s54\n"
"s_add_u32 s49, s49, 28672\ns_waitcnt vmcnt(7)\ns_barrier\ns_waitcnt lgkmcnt(3)\n"
"v_mfma_f32_16x16x32_bf16 v[96:99], v[40:43], v[128:131], 0\nv_mov_b32 v36, s48\ns_add_u32 s48, s48, 1\ns_nop 0\n"
"buffer_store_dword v36, v19, s[24:27], s33 offen sc1\nv_mfma_f32_16x16x32_bf16 v[100:103], v[40:43], v[132:135], 0\nv_mfma_f32_16x16x32_bf16 v[104:107], v[40:43], a[0:3], 0\nv_mfma_f32_16x16x32_bf16 v[108:111], v[40:43], a[4:7], 0\n"
"ds_read_b128 v[40:43], v32 offset:4096\nv_cvt_pk_bf16_f32 v56, v64, v65\ns_waitcnt lgkmcnt(3)\nv_mfma_f32_16x16x32_bf16 v[112:115], v[44:47], v[128:131], 0\n"
"v_cvt_pk_bf16_f32 v57, v66, v67\nv_mfma_f32_16x16x32_bf16 v[116:119], v[44:47], v[132:135], 0\nv_cvt_pk_bf16_f32 v58, v80, v81\nv_mfma_f32_16x16x32_bf16 v[120:123], v[44:47], a[0:3], 0\n"
"v_cvt_pk_bf16_f32 v59, v82, v83\nv_mfma_f32_16x16x32_bf16 v[124:127], v[44:47], a[4:7], 0\nds_read_b128 v[44:47], v33 offset:4096\ns_waitcnt lgkmcnt(3)\n"
"v_mfma_f32_16x16x32_bf16 v[96:99], v[48:51], v[136:139], v[96:99]\nv_mfma_f32_16x16x32_bf16 v[100:103], v[48:51], v[140:143], v[100:103]\nbuffer_store_dwordx4 v[56:59], v15, s[20:23], s50 offen sc1\nv_mfma_f32_16x16x32_bf16 v[104:107], v[48:51], a[8:11], v[104:107]\n"
"v_cvt_pk_bf16_f32 v60, v68, v69\nv_mfma_f32_16x16x32_bf16 v[108:111], v[48:51], a[12:15], v[108:111]\nds_read_b128 v[48:51], v34 offset:4096\nv_cvt_pk_bf16_f32 v61, v70, v71\n"
"s_waitcnt lgkmcnt(3)\nv_mfma_f32_16x16x32_bf16 v[112:115], v[52:55], v[136:139], v[112:115]\nv_cvt_pk_bf16_f32 v62, v84, v85\nv_mfma_f32_16x16x32_bf16 v[116:119], v[52:55], v[140:143], v[116:119]\n"
"v_cvt_pk_bf16_f32 v63, v86, v87\nv_mfma_f32_16x16x32_bf16 v[120:123], v[52:55], a[8:11], v[120:123]\nv_mfma_f32_16x16x32_bf16 v[124:127], v[52:55], a[12:15], v[124:127]\nds_read_b128 v[52:55], v35 offset:4096\n"
"s_waitcnt lgkmcnt(3)\nv_mfma_f32_16x16x32_bf16 v[96:99], v[40:43], v[144:147], v[96:99]\nbuffer_store_dwordx4 v[60:63], v16, s[20:23], s50 offen sc1\nv_mfma_f32_16x16x32_bf16 v[100:103], v[40:43], v[148:151], v[100:103]\n"
"v_cvt_pk_bf16_f32 v56, v72, v73\nv_mfma_f32_16x16x32_bf16 v[104:107], v[40:43], a[16:19], v[104:107]\nv_cvt_pk_bf16_f32 v57, v74, v75\nv_mfma_f32_16x16x32_bf16 v[108:111], v[40:43], a[20:23], v[108:111]\n"
"ds_read_b128 v[40:43], v32 offset:8192\nv_cvt_pk_bf16_f32 v58, v88, v89\ns_waitcnt lgkmcnt(3)\nv_mfma_f32_16x16x32_bf16 v[112:115], v[44:47], v[144:147], v[112:115]\n"
"v_cvt_pk_bf16_f32 v59, v90, v91\nv_mfma_f32_16x16x32_bf16 v[116:119], v[44:47], v[148:151], v[116:119]\nv_mfma_f32_16x16x32_bf16 v[120:123], v[44:47], a[16:19], v[120:123]\nv_mfma_f32_16x16x32_bf16 v[124:127], v[44:47], a[20:23], v[124:127]\n"
"ds_read_b128 v[44:47], v33 offset:8192\nbuffer_store_dwordx4 v[56:59], v17, s[20:23], s50 offen sc1\ns_waitcnt lgkmcnt(3)\nv_mfma_f32_16x16x32_bf16 v[96:99], v[48:51], v[152:155], v[96:99]\n"
"v_cvt_pk_bf16_f32 v60, v76, v77\nv_mfma_f32_16x16x32_bf16 v[100:103], v[48:51], v[156:159], v[100:103]\nv_cvt_pk_bf16_f32 v61, v78, v79\nv_mfma_f32_16x16x32_bf16 v[104:107], v[48:51], a[24:27], v[104:107]\n"
"v_cvt_pk_bf16_f32 v62, v92, v93\nv_mfma_f32_16x16x32_bf16 v[108:111], v[48:51], a[28:31], v[108:111]\nds_read_b128 v[48:51], v34 offset:8192\nv_cvt_pk_bf16_f32 v63, v94, v95\n"
"s_waitcnt lgkmcnt(3)\nv_mfma_f32_16x16x32_bf16 v[112:115], v[52:55], v[152:155], v[112:115]\nv_mfma_f32_16x16x32_bf16 v[116:119], v[52:55], v[156:159], v[116:119]\nv_mfma_f32_16x16x32_bf16 v[120:123], v[52:55], a[24:27], v[120:123]\n"
"buffer_store_dwordx4 v[60:63], v18, s[20:23], s50 offen sc1\nv_mfma_f32_16x16x32_bf16 v[124:127], v[52:55], a[28:31], v[124:127]\nds_read_b128 v[52:55], v35 offset:8192\ns_add_u32 s50, s50, s36\n"
"s_waitcnt lgkmcnt(3)\nv_mfma_f32_16x16x32_bf16 v[96:99], v[40:43], v[160:163], v[96:99]\ns_add_u32 m0, s52, 0\nv_mfma_f32_16x16x32_bf16 v[100:103], v[40:43], v[164:167], v[100:103]\n"
"s_add_u32 s53, s49, 0\nv_mfma_f32_16x16x32_bf16 v[104:107], v[40:43], a[32:35], v[104:107]\nbuffer_load_dwordx4 v10, s[12:15], s53 offen lds\nv_mfma_f32_16x16x32_bf16 v[108:111], v[40:43], a[36:39], v[108:111]\n"
"ds_read_b128 v[40:43], v32 offset:12288\ns_waitcnt lgkmcnt(3)\nv_mfma_f32_16x16x32_bf16 v[112:115], v[44:47], v[160:163], v[112:115]\nv_mfma_f32_16x16x32_bf16 v[116:119], v[44:47], v[164:167], v[116:119]\n"
"v_mfma_f32_16x16x32_bf16 v[120:123], v[44:47], a[32:35], v[120:123]\nv_mfma_f32_16x16x32_bf16 v[124:127], v[44:47], a[36:39], v[124:127]\nds_read_b128 v[44:47], v33 offset:12288\ns_waitcnt lgkmcnt(3)\n"
"v_mfma_f32_16x16x32_bf16 v[96:99], v[48:51], v[168:171], v[96:99]\nv_mfma_f32_16x16x32_bf16 v[100:103], v[48:51], v[172:175], v[100:103]\nv_mfma_f32_16x16x32_bf16 v[104:107], v[48:51], a[40:43], v[104:107]\nv_mfma_f32_16x16x32_bf16 v[108:111], v[48:51], a[44:47], v[108:111]\n"
"ds_read_b128 v[48:51], v34 offset:12288\ns_add_u32 m0, s52, 4096\ns_waitcnt lgkmcnt(3)\nv_mfma_f32_16x16x32_bf16 v[112:115], v[52:55], v[168:171], v[112:115]\n"
"s_add_u32 s53, s49, 128\nv_mfma_f32_16x16x32_bf16 v[116:119], v[52:55], v[172:175], v[116:119]\nbuffer_load_dwordx4 v10, s[12:15], s53 offen lds\nv_mfma_f32_16x16x32_bf16 v[120:123], v[52:55], a[40:43], v[120:123]\n"
"v_mfma_f32_16x16x32_bf16 v[124:127], v[52:55], a[44:47], v[124:127]\nds_read_b128 v[52:55], v35 offset:12288\ns_waitcnt lgkmcnt(3)\nv_mfma_f32_16x16x32_bf16 v[96:99], v[40:43], v[176:179], v[96:99]\n"
"v_mfma_f32_16x16x32_bf16 v[100:103], v[40:43], v[180:183], v[100:103]\nv_mfma_f32_16x16x32_bf16 v[104:107], v[40:43], a[48:51], v[104:107]\nv_mfma_f32_16x16x32_bf16 v[108:111], v[40:43], a[52:55], v[108:111]\nds_read_b128 v[40:43], v32 offset:16384\n"
"s_waitcnt lgkmcnt(3)\nv_mfma_f32_16x16x32_bf16 v[112:115], v[44:47], v[176:179], v[112:115]\nv_mfma_f32_16x16x32_bf16 v[116:119], v[44:47], v[180:183], v[116:119]\nv_mfma_f32_16x16x32_bf16 v[120:123], v[44:47], a[48:51], v[120:123]\n"
"s_add_u32 m0, s52, 8192\nv_mfma_f32_16x16x32_bf16 v[124:127], v[44:47], a[52:55], v[124:127]\nds_read_b128 v[44:47], v33 offset:16384\ns_add_u32 s53, s49, 256\n"
"s_waitcnt lgkmcnt(3)\nv_mfma_f32_16x16x32_bf16 v[96:99], v[48:51], v[184:187], v[96:99]\nbuffer_load_dwordx4 v10, s[12:15], s53 offen lds\nv_mfma_f32_16x16x32_bf16 v[100:103], v[48:51], v[188:191], v[100:103]\n"
"v_mfma_f32_16x16x32_bf16 v[104:107], v[48:51], a[56:59], v[104:107]\nv_mfma_f32_16x16x32_bf16 v[108:111], v[48:51], a[60:63], v[108:111]\nds_read_b128 v[48:51], v34 offset:16384\ns_waitcnt lgkmcnt(3)\n"
"v_mfma_f32_16x16x32_bf16 v[112:115], v[52:55], v[184:187], v[112:115]\nv_mfma_f32_16x16x32_bf16 v[116:119], v[52:55], v[188:191], v[116:119]\nv_mfma_f32_16x16x32_bf16 v[120:123], v[52:55], a[56:59], v[120:123]\nv_mfma_f32_16x16x32_bf16 v[124:127], v[52:55], a[60:63], v[124:127]\n"
"ds_read_b128 v[52:55], v35 offset:16384\ns_waitcnt lgkmcnt(3)\nv_mfma_f32_16x16x32_bf16 v[96:99], v[40:43], v[192:195], v[96:99]\nv_mfma_f32_16x16x32_bf16 v[100:103], v[40:43], v[196:199], v[100:103]\n"
"s_add_u32 m0, s52, 12288\nv_mfma_f32_16x16x32_bf16 v[104:107], v[40:43], a[64:67], v[104:107]\ns_add_u32 s53, s49, 384\nv_mfma_f32_16x16x32_bf16 v[108:111], v[40:43], a[68:71], v[108:111]\n"
"ds_read_b128 v[40:43], v32 offset:20480\nbuffer_load_dwordx4 v10, s[12:15], s53 offen lds\ns_waitcnt lgkmcnt(3)\nv_mfma_f32_16x16x32_bf16 v[112:115], v[44:47], v[192:195], v[112:115]\n"
"v_mfma_f32_16x16x32_bf16 v[116:119], v[44:47], v[196:199], v[116:119]\nv_mfma_f32_16x16x32_bf16 v[120:123], v[44:47], a[64:67], v[120:123]\nv_mfma_f32_16x16x32_bf16 v[124:127], v[44:47], a[68:71], v[124:127]\nds_read_b128 v[44:47], v33 offset:20480\n"
"s_waitcnt lgkmcnt(3)\nv_mfma_f32_16x16x32_bf16 v[96:99], v[48:51], v[200:203], v[96:99]\nv_mfma_f32_16x16x32_bf16 v[100:103], v[48:51], v[204:207], v[100:103]\nv_mfma_f32_16x16x32_bf16 v[104:107], v[48:51], a[72:75], v[104:107]\n"
"v_mfma_f32_16x16x32_bf16 v[108:111], v[48:51], a[76:79], v[108:111]\nds_read_b128 v[48:51], v34 offset:20480\ns_waitcnt lgkmcnt(3)\nv_mfma_f32_16x16x32_bf16 v[112:115], v[52:55], v[200:203], v[112:115]\n"
"s_add_u32 m0, s52, 16384\nv_mfma_f32_16x16x32_bf16 v[116:119], v[52:55], v[204:207], v[116:119]\ns_add_u32 s53, s49, 512\nv_mfma_f32_16x16x32_bf16 v[120:123], v[52:55], a[72:75], v[120:123]\n"
"buffer_load_dwordx4 v10, s[12:15], s53 offen lds\nv_mfma_f32_16x16x32_bf16 v[124:127], v[52:55], a[76:79], v[124:127]\nds_read_b128 v[52:55], v35 offset:20480\ns_waitcnt lgkmcnt(3)\n"
"v_mfma_f32_16x16x32_bf16 v[96:99], v[40:43], v[208:211], v[96:99]\nv_mfma_f32_16x16x32_bf16 v[100:103], v[40:43], v[212:215], v[100:103]\nv_mfma_f32_16x16x32_bf16 v[104:107], v[40:43], a[80:83], v[104:107]\nv_mfma_f32_16x16x32_bf16 v[108:111], v[40:43], a[84:87], v[108:111]\n"
"ds_read_b128 v[40:43], v32 offset:24576\ns_waitcnt lgkmcnt(3)\nv_mfma_f32_16x16x32_bf16 v[112:115], v[44:47], v[208:211], v[112:115]\nv_mfma_f32_16x16x32_bf16 v[116:119], v[44:47], v[212:215], v[116:119]\n"
"v_mfma_f32_16x16x32_bf16 v[120:123], v[44:47], a[80:83], v[120:123]\nv_mfma_f32_16x16x32_bf16 v[124:127], v[44:47], a[84:87], v[124:127]\nds_read_b128 v[44:47], v33 offset:24576\ns_add_u32 m0, s52, 20480\n"
"s_waitcnt lgkmcnt(3)\nv_mfma_f32_16x16x32_bf16 v[96:99], v[48:51], v[216:219], v[96:99]\ns_add_u32 s54, s51, 32768\ns_cmp_eq_u32 s54, 163840\n"
"v_mfma_f32_16x16x32_bf16 v[100:103], v[48:51], v[220:223], v[100:103]\nv_mfma_f32_16x16x32_bf16 v[104:107], v[48:51], a[88:91], v[104:107]\nv_mfma_f32_16x16x32_bf16 v[108:111], v[48:51], a[92:95], v[108:111]\ns_cselect_b32 s54, 0, s54\n"
"s_sub_u32 s54, s54, s51\nds_read_b128 v[48:51], v34 offset:24576\ns_add_u32 s53, s49, 640\ns_waitcnt lgkmcnt(3)\n"
"v_mfma_f32_16x16x32_bf16 v[112:115], v[52:55], v[216:219], v[112:115]\nbuffer_load_dwordx4 v10, s[12:15], s53 offen lds\nv_mfma_f32_16x16x32_bf16 v[116:119], v[52:55], v[220:223], v[116:119]\nv_mfma_f32_16x16x32_bf16 v[120:123], v[52:55], a[88:91], v[120:123]\n"
"v_mfma_f32_16x16x32_bf16 v[124:127], v[52:55], a[92:95], v[124:127]\nds_read_b128 v[52:55], v35 offset:24576\ns_waitcnt lgkmcnt(3)\nv_mfma_f32_16x16x32_bf16 v[96:99], v[40:43], v[224:227], v[96:99]\n"
"v_add_u32 v32, s54, v32\nv_mfma_f32_16x16x32_bf16 v[100:103], v[40:43], v[228:231], v[100:103]\nv_mfma_f32_16x16x32_bf16 v[104:107], v[40:43], a[96:99], v[104:107]\nv_mfma_f32_16x16x32_bf16 v[108:111], v[40:43], a[100:103], v[108:111]\n"
"ds_read_b128 v[40:43], v32 offset:0\ns_waitcnt lgkmcnt(3)\nv_mfma_f32_16x16x32_bf16 v[112:115], v[44:47], v[224:227], v[112:115]\nv_add_u32 v33, s54, v33\n"
"v_mfma_f32_16x16x32_bf16 v[116:119], v[44:47], v[228:231], v[116:119]\ns_add_u32 m0, s52, 24576\nv_mfma_f32_16x16x32_bf16 v[120:123], v[44:47], a[96:99], v[120:123]\ns_add_u32 s53, s49, 768\n"
"v_mfma_f32_16x16x32_bf16 v[124:127], v[44:47], a[100:103], v[124:127]\nds_read_b128 v[44:47], v33 offset:0\nbuffer_load_dwordx4 v10, s[12:15], s53 offen lds\ns_waitcnt lgkmcnt(3)\n"
"v_mfma_f32_16x16x32_bf16 v[96:99], v[48:51], v[232:235], v[96:99]\nv_add_u32 v34, s54, v34\nv_mfma_f32_16x16x32_bf16 v[100:103], v[48:51], v[236:239], v[100:103]\nv_mfma_f32_16x16x32_bf16 v[104:107], v[48:51], a[104:107], v[104:107]\n"
"v_mfma_f32_16x16x32_bf16 v[108:111], v[48:51], a[108:111], v[108:111]\nds_read_b128 v[48:51], v34 offset:0\ns_waitcnt lgkmcnt(3)\nv_mfma_f32_16x16x32_bf16 v[112:115], v[52:55], v[232:235], v[112:115]\n"
"v_add_u32 v35, s54, v35\nv_mfma_f32_16x16x32_bf16 v[116:119], v[52:55], v[236:239], v[116:119]\nv_mfma_f32_16x16x32_bf16 v[120:123], v[52:55], a[104:107], v[120:123]\nv_mfma_f32_16x16x32_bf16 v[124:127], v[52:55], a[108:111], v[124:127]\n"
"ds_read_b128 v[52:55], v35 offset:0\ns_mov_b32 s52, s47\ns_add_u32 s47, s51, s32\ns_add_u32 s51, s51, s54\n"
"s_add_u32 s49, s49, 28672\n.p2align 6\nfg1_loop:\ns_waitcnt vmcnt(7)\n"
"s_barrier\ns_waitcnt lgkmcnt(3)\nv_mfma_f32_16x16x32_bf16 v[64:67], v[40:43], v[128:131], 0\nv_mov_b32 v36, s48\n"
"s_add_u32 s48, s48, 1\ns_nop 0\nbuffer_store_dword v36, v19, s[24:27], s33 offen sc1\nv_mfma_f32_16x16x32_bf16 v[68:71], v[40:43], v[132:135], 0\n"
"v_mfma_f32_16x16x32_bf16 v[72:75], v[40:43], a[0:3], 0\nv_mfma_f32_16x16x32_bf16 v[76:79], v[40:43], a[4:7], 0\nds_read_b128 v[40:43], v32 offset:4096\nv_cvt_pk_bf16_f32 v56, v96, v97\n"
"s_waitcnt lgkmcnt(3)\nv_mfma_f32_16x16x32_bf16 v[80:83], v[44:47], v[128:131], 0\nv_cvt_pk_bf16_f32 v57, v98, v99\nv_mfma_f32_16x16x32_bf16 v[84:87], v[44:47], v[132:135], 0\n"
"v_cvt_pk_bf16_f32 v58, v112, v113\nv_mfma_f32_16x16x32_bf16 v[88:91], v[44:47], a[0:3], 0\nv_cvt_pk_bf16_f32 v59, v114, v115\nv_mfma_f32_16x16x32_bf16 v[92:95], v[44:47], a[4:7], 0\n"
"ds_read_b128 v[44:47], v33 offset:4096\ns_waitcnt lgkmcnt(3)\nv_mfma_f32_16x16x32_bf16 v[64:67], v[48:51], v[136:139], v[64:67]\nv_mfma_f32_16x16x32_bf16 v[68:71], v[48:51], v[140:143], v[68:71]\n"
"buffer_store_dwordx4 v[56:59], v15, s[20:23], s50 offen sc1\nv_mfma_f32_16x16x32_bf16 v[72:75], v[48:51], a[8:11], v[72:75]\nv_cvt_pk_bf16_f32 v60, v100, v101\nv_mfma_f32_16x16x32_bf16 v[76:79], v[48:51], a[12:15], v[76:79]\n"
"ds_read_b128 v[48:51], v34 offset:4096\nv_cvt_pk_bf16_f32 v61, v102, v103\ns_waitcnt lgkmcnt(3)\nv_mfma_f32_16x16x32_bf16 v[80:83], v[52:55], v[136:139], v[80:83]\n"
"v_cvt_pk_bf16_f32 v62, v116, v117\nv_mfma_f32_16x16x32_bf16 v[84:87], v[52:55], v[140:143], v[84:87]\nv_cvt_pk_bf16_f32 v63, v118, v119\nv_mfma_f32_16x16x32_bf16 v[88:91], v[52:55], a[8:11], v[88:91]\n"
"v_mfma_f32_16x16x32_bf16 v[92:95], v[52:55], a[12:15], v[92:95]\nds_read_b128 v[52:55], v35 offset:4096\ns_waitcnt lgkmcnt(3)\nv_mfma_f32_16x16x32_bf16 v[64:67], v[40:43], v[144:147], v[64:67]\n"
"buffer_store_dwordx4 v[60:63], v16, s[20:23], s50 offen sc1\nv_mfma_f32_16x16x32_bf16 v[68:71], v[40:43], v[148:151], v[68:71]\nv_cvt_pk_bf16_f32 v56, v104, v105\nv_mfma_f32_16x16x32_bf16 v[72:75], v[40:43], a[16:19], v[72:75]\n"
"v_cvt_pk_bf16_f32 v57, v106, v107\nv_mfma_f32_16x16x32_bf16 v[76:79], v[40:43], a[20:23], v[76:79]\nds_read_b128 v[40:43], v32 offset:8192\nv_cvt_pk_bf16_f32 v58, v120, v121\n"
"s_waitcnt lgkmcnt(3)\nv_mfma_f32_16x16x32_bf16 v[80:83], v[44:47], v[144:147], v[80:83]\nv_cvt_pk_bf16_f32 v59, v122, v123\nv_mfma_f32_16x16x32_bf16 v[84:87], v[44:47], v[148:151], v[84:87]\n"
"v_mfma_f32_16x16x32_bf16 v[88:91], v[44:47], a[16:19], v[88:91]\nv_mfma_f32_16x16x32_bf16 v[92:95], v[44:47], a[20:23], v[92:95]\nds_read_b128 v[44:47], v33 offset:8192\nbuffer_store_dwordx4 v[56:59], v17, s[20:23], s50 offen sc1\n"
"s_waitcnt lgkmcnt(3)\nv_mfma_f32_16x16x32_bf16 v[64:67], v[48:51], v[152:155], v[64:67]\nv_cvt_pk_bf16_f32 v60, v108, v109\nv_mfma_f32_16x16x32_bf16 v[68:71], v[48:51], v[156:159], v[68:71]\n"
"v_cvt_pk_bf16_f32 v61, v110, v111\nv_mfma_f32_16x16x32_bf16 v[72:75], v[48:51], a[24:27], v[72:75]\nv_cvt_pk_bf16_f32 v62, v124, v125\nv_mfma_f32_16x16x32_bf16 v[76:79], v[48:51], a[28:31], v[76:79]\n"
"ds_read_b128 v[48:51], v34 offset:8192\nv_cvt_pk_bf16_f32 v63, v126, v127\ns_waitcnt lgkmcnt(3)\nv_mfma_f32_16x16x32_bf16 v[80:83], v[52:55], v[152:155], v[80:83]\n"
"v_mfma_f32_16x16x32_bf16 v[84:87], v[52:55], v[156:159], v[84:87]\nv_mfma_f32_16x16x32_bf16 v[88:91], v[52:55], a[24:27], v[88:91]\nbuffer_store_dwordx4 v[60:63], v18, s[20:23], s50 offen sc1\nv_mfma_f32_16x16x32_bf16 v[92:95], v[52:55], a[28:31], v[92:95]\n"
"ds_read_b128 v[52:55], v35 offset:8192\ns_add_u32 s50, s50, s36\ns_waitcnt lgkmcnt(3)\nv_mfma_f32_16x16x32_bf16 v[64:67], v[40:43], v[160:163], v[64:67]\n"
"s_add_u32 m0, s52, 0\nv_mfma_f32_16x16x32_bf16 v[68:71], v[40:43], v[164:167], v[68:71]\ns_add_u32 s53, s49, 0\nv_mfma_f32_16x16x32_bf16 v[72:75], v[40:43], a[32:35], v[72:75]\n"
"buffer_load_dwordx4 v10, s[12:15], s53 offen lds\nv_mfma_f32_16x16x32_bf16 v[76:79], v[40:43], a[36:39], v[76:79]\nds_read_b128 v[40:43], v32 offset:12288\ns_waitcnt lgkmcnt(3)\n"
"v_mfma_f32_16x16x32_bf16 v[80:83], v[44:47], v[160:163], v[80:83]\nv_mfma_f32_16x16x32_bf16 v[84:87], v[44:47], v[164:167], v[84:87]\nv_mfma_f32_16x16x32_bf16 v[88:91], v[44:47], a[32:35], v[88:91]\nv_mfma_f32_16x16x32_bf16 v[92:95], v[44:47], a[36:39], v[92:95]\n"
"ds_read_b128 v[44:47], v33 offset:12288\ns_waitcnt lgkmcnt(3)\nv_mfma_f32_16x16x32_bf16 v[64:67], v[48:51], v[168:171], v[64:67]\nv_mfma_f32_16x16x32_bf16 v[68:71], v[48:51], v[172:175], v[68:71]\n"
"v_mfma_f32_16x16x32_bf16 v[72:75], v[48:51], a[40:43], v[72:75]\nv_mfma_f32_16x16x32_bf16 v[76:79], v[48:51], a[44:47], v[76:79]\nds_read_b128 v[48:51], v34 offset:12288\ns_add_u32 m0, s52, 4096\n"
"s_waitcnt lgkmcnt(3)\nv_mfma_f32_16x16x32_bf16 v[80:83], v[52:55], v[168:171], v[80:83]\ns_add_u32 s53, s49, 128\nv_mfma_f32_16x16x32_bf16 v[84:87], v[52:55], v[172:175], v[84:87]\n"
"buffer_load_dwordx4 v10, s[12:15], s53 offen lds\nv_mfma_f32_16x16x32_bf16 v[88:91], v[52:55], a[40:43], v[88:91]\nv_mfma_f32_16x16x32_bf16 v[92:95], v[52:55], a[44:47], v[92:95]\nds_read_b128 v[52:55], v35 offset:12288\n"
"s_waitcnt lgkmcnt(3)\nv_mfma_f32_16x16x32_bf16 v[64:67], v[40:43], v[176:179], v[64:67]\nv_mfma_f32_16x16x32_bf16 v[68:71], v[40:43], v[180:183], v[68:71]\nv_mfma_f32_16x16x32_bf16 v[72:75], v[40:43], a[48:51], v[72:75]\n"
"v_mfma_f32_16x16x32_bf16 v[76:79], v[40:43], a[52:55], v[76:79]\nds_read_b128 v[40:43], v32 offset:16384\ns_waitcnt lgkmcnt(3)\nv_mfma_f32_16x16x32_bf16 v[80:83], v[44:47], v[176:179], v[80:83]\n"
"v_mfma_f32_16x16x32_bf16 v[84:87], v[44:47], v[180:183], v[84:87]\nv_mfma_f32_16x16x32_bf16 v[88:91], v[44:47], a[48:51], v[88:91]\ns_add_u32 m0, s52, 8192\nv_mfma_f32_16x16x32_bf16 v[92:95], v[44:47], a[52:55], v[92:95]\n"
"ds_read_b128 v[44:47], v33 offset:16384\ns_add_u32 s53, s49, 256\ns_waitcnt lgkmcnt(3)\nv_mfma_f32_16x16x32_bf16 v[64:67], v[48:51], v[184:187], v[64:67]\n"
"buffer_load_dwordx4 v10, s[12:15], s53 offen lds\nv_mfma_f32_16x16x32_bf16 v[68:71], v[48:51], v[188:191], v[68:71]\nv_mfma_f32_16x16x32_bf16 v[72:75], v[48:51], a[56:59], v[72:75]\nv_mfma_f32_16x16x32_bf16 v[76:79], v[48:51], a[60:63], v[76:79]\n"
"ds_read_b128 v[48:51], v34 offset:16384\ns_waitcnt lgkmcnt(3)\nv_mfma_f32_16x16x32_bf16 v[80:83], v[52:55], v[184:187], v[80:83]\nv_mfma_f32_16x16x32_bf16 v[84:87], v[52:55], v[188:191], v[84:87]\n"
"v_mfma_f32_16x16x32_bf16 v[88:91], v[52:55], a[56:59], v[88:91]\nv_mfma_f32_16x16x32_bf16 v[92:95], v[52:55], a[60:63], v[92:95]\nds_read_b128 v[52:55], v35 offset:16384\ns_waitcnt lgkmcnt(3)\n"
"v_mfma_f32_16x16x32_bf16 v[64:67], v[40:43], v[192:195], v[64:67]\nv_mfma_f32_16x16x32_bf16 v[68:71], v[40:43], v[196:199], v[68:71]\ns_add_u32 m0, s52, 12288\nv_mfma_f32_16x16x32_bf16 v[72:75], v[40:43], a[64:67], v[72:75]\n"
"s_add_u32 s53, s49, 384\nv_mfma_f32_16x16x32_bf16 v[76:79], v[40:43], a[68:71], v[76:79]\nds_read_b128 v[40:43], v32 offset:20480\nbuffer_load_dwordx4 v10, s[12:15], s53 offen lds\n"
"s_waitcnt lgkmcnt(3)\nv_mfma_f32_16x16x32_bf16 v[80:83], v[44:47], v[192:195], v[80:83]\nv_mfma_f32_16x16x32_bf16 v[84:87], v[44:47], v[196:199], v[84:87]\nv_mfma_f32_16x16x32_bf16 v[88:91], v[44:47], a[64:67], v[88:91]\n"
"v_mfma_f32_16x16x32_bf16 v[92:95], v[44:47], a[68:71], v[92:95]\nds_read_b128 v[44:47], v33 offset:20480\ns_waitcnt lgkmcnt(3)\nv_mfma_f32_16x16x32_bf16 v[64:67], v[48:51], v[200:203], v[64:67]\n"
"v_mfma_f32_16x16x32_bf16 v[68:71], v[48:51], v[204:207], v[68:71]\nv_mfma_f32_16x16x32_bf16 v[72:75], v[48:51], a[72:75], v[72:75]\nv_mfma_f32_16x16x32_bf16 v[76:79], v[48:51], a[76:79], v[76:79]\nds_read_b128 v[48:51], v34 offset:20480\n"
"s_waitcnt lgkmcnt(3)\nv_mfma_f32_16x16x32_bf16 v[80:83], v[52:55], v[200:203], v[80:83]\ns_add_u32 m0, s52, 16384\nv_mfma_f32_16x16x32_bf16 v[84:87], v[52:55], v[204:207], v[84:87]\n"
"s_add_u32 s53, s49, 512\nv_mfma_f32_16x16x32_bf16 v[88:91], v[52:55], a[72:75], v[88:91]\nbuffer_load_dwordx4 v10, s[12:15], s53 offen lds\nv_mfma_f32_16x16x32_bf16 v[92:95], v[52:55], a[76:79], v[92:95]\n"
"ds_read_b128 v[52:55], v35 offset:20480\ns_waitcnt lgkmcnt(3)\nv_mfma_f32_16x16x32_bf16 v[64:67], v[40:43], v[208:211], v[64:67]\nv_mfma_f32_16x16x32_bf16 v[68:71], v[40:43], v[212:215], v[68:71]\n"
"v_mfma_f32_16x16x32_bf16 v[72:75], v[40:43], a[80:83], v[72:75]\nv_mfma_f32_16x16x32_bf16 v[76:79], v[40:43], a[84:87], v[76:79]\nds_read_b128 v[40:43], v32 offset:24576\ns_waitcnt lgkmcnt(3)\n"
"v_mfma_f32_16x16x32_bf16 v[80:83], v[44:47], v[208:211], v[80:83]\nv_mfma_f32_16x16x32_bf16 v[84:87], v[44:47], v[212:215], v[84:87]\nv_mfma_f32_16x16x32_bf16 v[88:91], v[44:47], a[80:83], v[88:91]\nv_mfma_f32_16x16x32_bf16 v[92:95], v[44:47], a[84:87], v[92:95]\n"
"ds_read_b128 v[44:47], v33 offset:24576\ns_add_u32 m0, s52, 20480\ns_waitcnt lgkmcnt(3)\nv_mfma_f32_16x16x32_bf16 v[64:67], v[48:51], v[216:219], v[64:67]\n"
"s_add_u32 s54, s51, 32768\ns_cmp_eq_u32 s54, 163840\nv_mfma_f32_16x16x32_bf16 v[68:71], v[48:51], v[220:223], v[68:71]\nv_mfma_f32_16x16x32_bf16 v[72:75], v[48:51], a[88:91], v[72:75]\n"
"v_mfma_f32_16x16x32_bf16 v[76:79], v[48:51], a[92:95], v[76:79]\ns_cselect_b32 s54, 0, s54\ns_sub_u32 s54, s54, s51\nds_read_b128 v[48:51], v34 offset:24576\n"
"s_add_u32 s53, s49, 640\ns_waitcnt lgkmcnt(3)\nv_mfma_f32_16x16x32_bf16 v[80:83], v[52:55], v[216:219], v[80:83]\nbuffer_load_dwordx4 v10, s[12:15], s53 offen lds\n"
"v_mfma_f32_16x16x32_bf16 v[84:87], v[52:55], v[220:223], v[84:87]\nv_mfma_f32_16x16x32_bf16 v[88:91], v[52:55], a[88:91], v[88:91]\nv_mfma_f32_16x16x32_bf16 v[92:95], v[52:55], a[92:95], v[92:95]\nds_read_b128 v[52:55], v35 offset:24576\n"
"s_waitcnt lgkmcnt(3)\nv_mfma_f32_16x16x32_bf16 v[64:67], v[40:43], v[224:227], v[64:67]\nv_add_u32 v32, s54, v32\nv_mfma_f32_16x16x32_bf16 v[68:71], v[40:43], v[228:231], v[68:71]\n"
"v_mfma_f32_16x16x32_bf16 v[72:75], v[40:43], a[96:99], v[72:75]\nv_mfma_f32_16x16x32_bf16 v[76:79], v[40:43], a[100:103], v[76:79]\nds_read_b128 v[40:43], v32 offset:0\ns_waitcnt lgkmcnt(3)\n"
"v_mfma_f32_16x16x32_bf16 v[80:83], v[44:47], v[224:227], v[80:83]\nv_add_u32 v33, s54, v33\nv_mfma_f32_16x16x32_bf16 v[84:87], v[44:47], v[228:231], v[84:87]\ns_add_u32 m0, s52, 24576\n"
"v_mfma_f32_16x16x32_bf16 v[88:91], v[44:47], a[96:99], v[88:91]\ns_add_u32 s53, s49, 768\nv_mfma_f32_16x16x32_bf16 v[92:95], v[44:47], a[100:103], v[92:95]\nds_read_b128 v[44:47], v33 offset:0\n"
"buffer_load_dwordx4 v10, s[12:15], s53 offen lds\ns_waitcnt lgkmcnt(3)\nv_mfma_f32_16x16x32_bf16 v[64:67], v[48:51], v[232:235], v[64:67]\nv_add_u32 v34, s54, v34\n"
"v_mfma_f32_16x16x32_bf16 v[68:71], v[48:51], v[236:239], v[68:71]\nv_mfma_f32_16x16x32_bf16 v[72:75], v[48:51], a[104:107], v[72:75]\nv_mfma_f32_16x16x32_bf16 v[76:79], v[48:51], a[108:111], v[76:79]\nds_read_b128 v[48:51], v34 offset:0\n"
"s_waitcnt lgkmcnt(3)\nv_mfma_f32_16x16x32_bf16 v[80:83], v[52:55], v[232:235], v[80:83]\nv_add_u32 v35, s54, v35\nv_mfma_f32_16x16x32_bf16 v[84:87], v[52:55], v[236:239], v[84:87]\n"
"v_mfma_f32_16x16x32_bf16 v[88:91], v[52:55], a[104:107], v[88:91]\nv_mfma_f32_16x16x32_bf16 v[92:95], v[52:55], a[108:111], v[92:95]\nds_read_b128 v[52:55], v35 offset:0\ns_mov_b32 s52, s47\n"
"s_add_u32 s47, s51, s32\ns_add_u32 s51, s51, s54\ns_add_u32 s49, s49, 28672\ns_waitcnt vmcnt(7)\n"
"s_barrier\ns_waitcnt lgkmcnt(3)\nv_mfma_f32_16x16x32_bf16 v[96:99], v[40:43], v[128:131], 0\ns_add_u32 s48, s48, 1\n"
"v_mfma_f32_16x16x32_bf16 v[100:103], v[40:43], v[132:135], 0\nv_mfma_f32_16x16x32_bf16 v[104:107], v[40:43], a[0:3], 0\nv_mfma_f32_16x16x32_bf16 v[108:111], v[40:43], a[4:7], 0\nds_read_b128 v[40:43], v32 offset:4096\n"
"v_cvt_pk_bf16_f32 v56, v64, v65\ns_waitcnt lgkmcnt(3)\nv_mfma_f32_16x16x32_bf16 v[112:115], v[44:47], v[128:131], 0\nv_cvt_pk_bf16_f32 v57, v66, v67\n"
"v_mfma_f32_16x16x32_bf16 v[116:119], v[44:47], v[132:135], 0\nv_cvt_pk_bf16_f32 v58, v80, v81\nv_mfma_f32_16x16x32_bf16 v[120:123], v[44:47], a[0:3], 0\nv_cvt_pk_bf16_f32 v59, v82, v83\n"
"v_mfma_f32_16x16x32_bf16 v[124:127], v[44:47], a[4:7], 0\nds_read_b128 v[44:47], v33 offset:4096\ns_waitcnt lgkmcnt(3)\nv_mfma_f32_16x16x32_bf16 v[96:99], v[48:51], v[136:139], v[96:99]\n"
"v_mfma_f32_16x16x32_bf16 v[100:103], v[48:51], v[140:143], v[100:103]\nbuffer_store_dwordx4 v[56:59], v15, s[20:23], s50 offen sc1\nv_mfma_f32_16x16x32_bf16 v[104:107], v[48:51], a[8:11], v[104:107]\nv_cvt_pk_bf16_f32 v60, v68, v69\n"
"v_mfma_f32_16x16x32_bf16 v[108:111], v[48:51], a[12:15], v[108:111]\nds_read_b128 v[48:51], v34 offset:4096\nv_cvt_pk_bf16_f32 v61, v70, v71\ns_waitcnt lgkmcnt(3)\n"
"v_mfma_f32_16x16x32_bf16 v[112:115], v[52:55], v[136:139], v[112:115]\nv_cvt_pk_bf16_f32 v62, v84, v85\nv_mfma_f32_16x16x32_bf16 v[116:119], v[52:55], v[140:143], v[116:119]\nv_cvt_pk_bf16_f32 v63, v86, v87\n"
"v_mfma_f32_16x16x32_bf16 v[120:123], v[52:55], a[8:11], v[120:123]\nv_mfma_f32_16x16x32_bf16 v[124:127], v[52:55], a[12:15], v[124:127]\nds_read_b128 v[52:55], v35 offset:4096\ns_waitcnt lgkmcnt(3)\n"
"v_mfma_f32_16x16x32_bf16 v[96:99], v[40:43], v[144:147], v[96:99]\nbuffer_store_dwordx4 v[60:63], v16, s[20:23], s50 offen sc1\nv_mfma_f32_16x16x32_bf16 v[100:103], v[40:43], v[148:151], v[100:103]\nv_cvt_pk_bf16_f32 v56, v72, v73\n"
"v_mfma_f32_16x16x32_bf16 v[104:107], v[40:43], a[16:19], v[104:107]\nv_cvt_pk_bf16_f32 v57, v74, v75\nv_mfma_f32_16x16x32_bf16 v[108:111], v[40:43], a[20:23], v[108:111]\nds_read_b128 v[40:43], v32 offset:8192\n"
"v_cvt_pk_bf16_f32 v58, v88, v89\ns_waitcnt lgkmcnt(3)\nv_mfma_f32_16x16x32_bf16 v[112:115], v[44:47], v[144:147], v[112:115]\nv_cvt_pk_bf16_f32 v59, v90, v91\n"
"v_mfma_f32_16x16x32_bf16 v[116:119], v[44:47], v[148:151], v[116:119]\nv_mfma_f32_16x16x32_bf16 v[120:123], v[44:47], a[16:19], v[120:123]\nv_mfma_f32_16x16x32_bf16 v[124:127], v[44:47], a[20:23], v[124:127]\nds_read_b128 v[44:47], v33 offset:8192\n"
"buffer_store_dwordx4 v[56:59], v17, s[20:23], s50 offen sc1\ns_waitcnt lgkmcnt(3)\nv_mfma_f32_16x16x32_bf16 v[96:99], v[48:51], v[152:155], v[96:99]\nv_cvt_pk_bf16_f32 v60, v76, v77\n"
"v_mfma_f32_16x16x32_bf16 v[100:103], v[48:51], v[156:159], v[100:103]\nv_cvt_pk_bf16_f32 v61, v78, v79\nv_mfma_f32_16x16x32_bf16 v[104:107], v[48:51], a[24:27], v[104:107]\nv_cvt_pk_bf16_f32 v62, v92, v93\n"
"v_mfma_f32_16x16x32_bf16 v[108:111], v[48:51], a[28:31], v[108:111]\nds_read_b128 v[48:51], v34 offset:8192\nv_cvt_pk_bf16_f32 v63, v94, v95\ns_waitcnt lgkmcnt(3)\n"
"v_mfma_f32_16x16x32_bf16 v[112:115], v[52:55], v[152:155], v[112:115]\nv_mfma_f32_16x16x32_bf16 v[116:119], v[52:55], v[156:159], v[116:119]\nv_mfma_f32_16x16x32_bf16 v[120:123], v[52:55], a[24:27], v[120:123]\nbuffer_store_dwordx4 v[60:63], v18, s[20:23], s50 offen sc1\n"
"v_mfma_f32_16x16x32_bf16 v[124:127], v[52:55], a[28:31], v[124:127]\nds_read_b128 v[52:55], v35 offset:8192\ns_add_u32 s50, s50, s36\ns_waitcnt lgkmcnt(3)\n"
"v_mfma_f32_16x16x32_bf16 v[96:99], v[40:43], v[160:163], v[96:99]\ns_add_u32 m0, s52, 0\nv_mfma_f32_16x16x32_bf16 v[100:103], v[40:43], v[164:167], v[100:103]\ns_add_u32 s53, s49, 0\n"
"v_mfma_f32_16x16x32_bf16 v[104:107], v[40:43], a[32:35], v[104:107]\nbuffer_load_dwordx4 v10, s[12:15], s53 offen lds\nv_mfma_f32_16x16x32_bf16 v[108:111], v[40:43], a[36:39], v[108:111]\nds_read_b128 v[40:43], v32 offset:12288\n"
"s_waitcnt lgkmcnt(3)\nv_mfma_f32_16x16x32_bf16 v[112:115], v[44:47], v[160:163], v[112:115]\nv_mfma_f32_16x16x32_bf16 v[116:119], v[44:47], v[164:167], v[116:119]\nv_mfma_f32_16x16x32_bf16 v[120:123], v[44:47], a[32:35], v[120:123]\n"
"v_mfma_f32_16x16x32_bf16 v[124:127], v[44:47], a[36:39], v[124:127]\nds_read_b128 v[44:47], v33 offset:12288\ns_waitcnt lgkmcnt(3)\nv_mfma_f32_16x16x32_bf16 v[96:99], v[48:51], v[168:171], v[96:99]\n"
"v_mfma_f32_16x16x32_bf16 v[100:103], v[48:51], v[172:175], v[100:103]\nv_mfma_f32_16x16x32_bf16 v[104:107], v[48:51], a[40:43], v[104:107]\nv_mfma_f32_16x16x32_bf16 v[108:111], v[48:51], a[44:47], v[108:111]\nds_read_b128 v[48:51], v34 offset:12288\n"
"s_add_u32 m0, s52, 4096\ns_waitcnt lgkmcnt(3)\nv_mfma_f32_16x16x32_bf16 v[112:115], v[52:55], v[168:171], v[112:115]\ns_add_u32 s53, s49, 128\n"
"v_mfma_f32_16x16x32_bf16 v[116:119], v[52:55], v[172:175], v[116:119]\nbuffer_load_dwordx4 v10, s[12:15], s53 offen lds\nv_mfma_f32_16x16x32_bf16 v[120:123], v[52:55], a[40:43], v[120:123]\nv_mfma_f32_16x16x32_bf16 v[124:127], v[52:55], a[44:47], v[124:127]\n"
"ds_read_b128 v[52:55], v35 offset:12288\ns_waitcnt lgkmcnt(3)\nv_mfma_f32_16x16x32_bf16 v[96:99], v[40:43], v[176:179], v[96:99]\nv_mfma_f32_16x16x32_bf16 v[100:103], v[40:43], v[180:183], v[100:103]\n"
"v_mfma_f32_16x16x32_bf16 v[104:107], v[40:43], a[48:51], v[104:107]\nv_mfma_f32_16x16x32_bf16 v[108:111], v[40:43], a[52:55], v[108:111]\nds_read_b128 v[40:43], v32 offset:16384\ns_waitcnt lgkmcnt(3)\n"
"v_mfma_f32_16x16x32_bf16 v[112:115], v[44:47], v[176:179], v[112:115]\nv_mfma_f32_16x16x32_bf16 v[116:119], v[44:47], v[180:183], v[116:119]\nv_mfma_f32_16x16x32_bf16 v[120:123], v[44:47], a[48:51], v[120:123]\ns_add_u32 m0, s52, 8192\n"
"v_mfma_f32_16x16x32_bf16 v[124:127], v[44:47], a[52:55], v[124:127]\nds_read_b128 v[44:47], v33 offset:16384\ns_add_u32 s53, s49, 256\ns_waitcnt lgkmcnt(3)\n"
"v_mfma_f32_16x16x32_bf16 v[96:99], v[48:51], v[184:187], v[96:99]\nbuffer_load_dwordx4 v10, s[12:15], s53 offen lds\nv_mfma_f32_16x16x32_bf16 v[100:103], v[48:51], v[188:191], v[100:103]\nv_mfma_f32_16x16x32_bf16 v[104:107], v[48:51], a[56:59], v[104:107]\n"
"v_mfma_f32_16x16x32_bf16 v[108:111], v[48:51], a[60:63], v[108:111]\nds_read_b128 v[48:51], v34 offset:16384\ns_waitcnt lgkmcnt(3)\nv_mfma_f32_16x16x32_bf16 v[112:115], v[52:55], v[184:187], v[112:115]\n"
"v_mfma_f32_16x16x32_bf16 v[116:119], v[52:55], v[188:191], v[116:119]\nv_mfma_f32_16x16x32_bf16 v[120:123], v[52:55], a[56:59], v[120:123]\nv_mfma_f32_16x16x32_bf16 v[124:127], v[52:55], a[60:63], v[124:127]\nds_read_b128 v[52:55], v35 offset:16384\n"
"s_waitcnt lgkmcnt(3)\nv_mfma_f32_16x16x32_bf16 v[96:99], v[40:43], v[192:195], v[96:99]\nv_mfma_f32_16x16x32_bf16 v[100:103], v[40:43], v[196:199], v[100:103]\ns_add_u32 m0, s52, 12288\n"
"v_mfma_f32_16x16x32_bf16 v[104:107], v[40:43], a[64:67], v[104:107]\ns_add_u32 s53, s49, 384\nv_mfma_f32_16x16x32_bf16 v[108:111], v[40:43], a[68:71], v[108:111]\nds_read_b128 v[40:43], v32 offset:20480\n"
"buffer_load_dwordx4 v10, s[12:15], s53 offen lds\ns_waitcnt lgkmcnt(3)\nv_mfma_f32_16x16x32_bf16 v[112:115], v[44:47], v[192:195], v[112:115]\nv_mfma_f32_16x16x32_bf16 v[116:119], v[44:47], v[196:199], v[116:119]\n"
"v_mfma_f32_16x16x32_bf16 v[120:123], v[44:47], a[64:67], v[120:123]\nv_mfma_f32_16x16x32_bf16 v[124:127], v[44:47], a[68:71], v[124:127]\nds_read_b128 v[44:47], v33 offset:20480\ns_waitcnt lgkmcnt(3)\n"
"v_mfma_f32_16x16x32_bf16 v[96:99], v[48:51], v[200:203], v[96:99]\nv_mfma_f32_16x16x32_bf16 v[100:103], v[48:51], v[204:207], v[100:103]\nv_mfma_f32_16x16x32_bf16 v[104:107], v[48:51], a[72:75], v[104:107]\nv_mfma_f32_16x16x32_bf16 v[108:111], v[48:51], a[76:79], v[108:111]\n"
"ds_read_b128 v[48:51], v34 offset:20480\ns_waitcnt lgkmcnt(3)\nv_mfma_f32_16x16x32_bf16 v[112:115], v[52:55], v[200:203], v[112:115]\ns_add_u32 m0, s52, 16384\n"
"v_mfma_f32_16x16x32_bf16 v[116:119], v[52:55], v[204:207], v[116:119]\ns_add_u32 s53, s49, 512\nv_mfma_f32_16x16x32_bf16 v[120:123], v[52:55], a[72:75], v[120:123]\nbuffer_load_dwordx4 v10, s[12:15], s53 offen lds\n"
"v_mfma_f32_16x16x32_bf16 v[124:127], v[52:55], a[76:79], v[124:127]\nds_read_b128 v[52:55], v35 offset:20480\ns_waitcnt lgkmcnt(3)\nv_mfma_f32_16x16x32_bf16 v[96:99], v[40:43], v[208:211], v[96:99]\n"
"v_mfma_f32_16x16x32_bf16 v[100:103], v[40:43], v[212:215], v[100:103]\nv_mfma_f32_16x16x32_bf16 v[104:107], v[40:43], a[80:83], v[104:107]\nv_mfma_f32_16x16x32_bf16 v[108:111], v[40:43], a[84:87], v[108:111]\nds_read_b128 v[40:43], v32 offset:24576\n"
"s_waitcnt lgkmcnt(3)\nv_mfma_f32_16x16x32_bf16 v[112:115], v[44:47], v[208:211], v[112:115]\nv_mfma_f32_16x16x32_bf16 v[116:119], v[44:47], v[212:215], v[116:119]\nv_mfma_f32_16x16x32_bf16 v[120:123], v[44:47], a[80:83], v[120:123]\n"
"v_mfma_f32_16x16x32_bf16 v[124:127], v[44:47], a[84:87], v[124:127]\nds_read_b128 v[44:47], v33 offset:24576\ns_add_u32 m0, s52, 20480\ns_waitcnt lgkmcnt(3)\n"
"v_mfma_f32_16x16x32_bf16 v[96:99], v[48:51], v[216:219], v[96:99]\ns_add_u32 s54, s51, 32768\ns_cmp_eq_u32 s54, 163840\nv_mfma_f32_16x16x32_bf16 v[100:103], v[48:51], v[220:223], v[100:103]\n"
"v_mfma_f32_16x16x32_bf16 v[104:107], v[48:51], a[88:91], v[104:107]\nv_mfma_f32_16x16x32_bf16 v[108:111], v[48:51], a[92:95], v[108:111]\ns_cselect_b32 s54, 0, s54\ns_sub_u32 s54, s54, s51\n"
"ds_read_b128 v[48:51], v34 offset:24576\ns_add_u32 s53, s49, 640\ns_waitcnt lgkmcnt(3)\nv_mfma_f32_16x16x32_bf16 v[112:115], v[52:55], v[216:219], v[112:115]\n"
"buffer_load_dwordx4 v10, s[12:15], s53 offen lds\nv_mfma_f32_16x16x32_bf16 v[116:119], v[52:55], v[220:223], v[116:119]\nv_mfma_f32_16x16x32_bf16 v[120:123], v[52:55], a[88:91], v[120:123]\nv_mfma_f32_16x16x32_bf16 v[124:127], v[52:55], a[92:95], v[124:127]\n"
"ds_read_b128 v[52:55], v35 offset:24576\ns_waitcnt lgkmcnt(3)\nv_mfma_f32_16x16x32_bf16 v[96:99], v[40:43], v[224:227], v[96:99]\nv_add_u32 v32, s54, v32\n"
"v_mfma_f32_16x16x32_bf16 v[100:103], v[40:43], v[228:231], v[100:103]\nv_mfma_f32_16x16x32_bf16 v[104:107], v[40:43], a[96:99], v[104:107]\nv_mfma_f32_16x16x32_bf16 v[108:111], v[40:43], a[100:103], v[108:111]\nds_read_b128 v[40:43], v32 offset:0\n"
"s_waitcnt lgkmcnt(3)\nv_mfma_f32_16x16x32_bf16 v[112:115], v[44:47], v[224:227], v[112:115]\nv_add_u32 v33, s54, v33\nv_mfma_f32_16x16x32_bf16 v[116:119], v[44:47], v[228:231], v[116:119]\n"
"s_add_u32 m0, s52, 24576\nv_mfma_f32_16x16x32_bf16 v[120:123], v[44:47], a[96:99], v[120:123]\ns_add_u32 s53, s49, 768\nv_mfma_f32_16x16x32_bf16 v[124:127], v[44:47], a[100:103], v[124:127]\n"
"ds_read_b128 v[44:47], v33 offset:0\nbuffer_load_dwordx4 v10, s[12:15], s53 offen lds\ns_waitcnt lgkmcnt(3)\nv_mfma_f32_16x16x32_bf16 v[96:99], v[48:51], v[232:235], v[96:99]\n"
"v_add_u32 v34, s54, v34\nv_mfma_f32_16x16x32_bf16 v[100:103], v[48:51], v[236:239], v[100:103]\nv_mfma_f32_16x16x32_bf16 v[104:107], v[48:51], a[104:107], v[104:107]\nv_mfma_f32_16x16x32_bf16 v[108:111], v[48:51], a[108:111], v[108:111]\n"
"ds_read_b128 v[48:51], v34 offset:0\ns_waitcnt lgkmcnt(3)\nv_mfma_f32_16x16x32_bf16 v[112:115], v[52:55], v[232:235], v[112:115]\nv_add_u32 v35, s54, v35\n"
"v_mfma_f32_16x16x32_bf16 v[116:119], v[52:55], v[236:239], v[116:119]\nv_mfma_f32_16x16x32_bf16 v[120:123], v[52:55], a[104:107], v[120:123]\nv_mfma_f32_16x16x32_bf16 v[124:127], v[52:55], a[108:111], v[124:127]\nds_read_b128 v[52:55], v35 offset:0\n"
"s_mov_b32 s52, s47\ns_add_u32 s47, s51, s32\ns_add_u32 s51, s51, s54\ns_add_u32 s49, s49, 28672\n"
"s_cmp_lt_u32 s49, 1921024\ns_cbranch_scc1 fg1_loop\ns_nop 7\nv_cvt_pk_bf16_f32 v56, v96, v97\n"
"v_cvt_pk_bf16_f32 v57, v98, v99\nv_cvt_pk_bf16_f32 v58, v112, v113\nv_cvt_pk_bf16_f32 v59, v114, v115\ns_nop 0\n"
"s_nop 0\nbuffer_store_dwordx4 v[56:59], v15, s[20:23], s50 offen sc1\nv_cvt_pk_bf16_f32 v60, v100, v101\nv_cvt_pk_bf16_f32 v61, v102, v103\n"
"v_cvt_pk_bf16_f32 v62, v116, v117\nv_cvt_pk_bf16_f32 v63, v118, v119\ns_nop 0\ns_nop 0\n"
"buffer_store_dwordx4 v[60:63], v16, s[20:23], s50 offen sc1\nv_cvt_pk_bf16_f32 v56, v104, v105\nv_cvt_pk_bf16_f32 v57, v106, v107\nv_cvt_pk_bf16_f32 v58, v120, v121\n"
"v_cvt_pk_bf16_f32 v59, v122, v123\ns_nop 0\ns_nop 0\nbuffer_store_dwordx4 v[56:59], v17, s[20:23], s50 offen sc1\n"
"v_cvt_pk_bf16_f32 v60, v108, v109\nv_cvt_pk_bf16_f32 v61, v110, v111\nv_cvt_pk_bf16_f32 v62, v124, v125\nv_cvt_pk_bf16_f32 v63, v126, v127\n"
"s_nop 0\ns_nop 0\nbuffer_store_dwordx4 v[60:63], v18, s[20:23], s50 offen sc1\ns_add_u32 s48, s28, 64\n"
"v_mov_b32 v36, s48\ns_waitcnt vmcnt(0) lgkmcnt(0)\ns_barrier\nbuffer_store_dword v36, v19, s[24:27], s33 offen sc1\n"
"fg1_end:\n"
    :  : "{s[8:23]}"(s0), "{s[24:39]}"(s1), "{v[8:23]}"(v0) : "v24", "v25", "v26", "v27", "v28", "v29", "v30", "v31", "v32", "v33", "v34", "v35", "v36", "v37", "v38", "v39", "v40", "v41", "v42", "v43", "v44", "v45", "v46", "v47", "v48", "v49", "v50", "v51", "v52", "v53", "v54", "v55", "v56", "v57", "v58", "v59", "v60", "v61", "v62", "v63", "v64", "v65", "v66", "v67", "v68", "v69", "v70", "v71", "v72", "v73", "v74", "v75", "v76", "v77", "v78", "v79", "v80", "v81", "v82", "v83", "v84", "v85", "v86", "v87", "v88", "v89", "v90", "v91", "v92", "v93", "v94", "v95", "v96", "v97", "v98", "v99", "v100", "v101", "v102", "v103", "v104", "v105", "v106", "v107", "v108", "v109", "v110", "v111", "v112", "v113", "v114", "v115", "v116", "v117", "v118", "v119", "v120", "v121", "v122", "v123", "v124", "v125", "v126", "v127", "v128", "v129", "v130", "v131", "v132", "v133", "v134", "v135", "v136", "v137", "v138", "v139", "v140", "v141", "v142", "v143", "v144", "v145", "v146", "v147", "v148", "v149", "v150", "v151", "v152", "v153", "v154", "v155", "v156", "v157", "v158", "v159", "v160", "v161", "v162", "v163", "v164", "v165", "v166", "v167", "v168", "v169", "v170", "v171", "v172", "v173", "v174", "v175", "v176", "v177", "v178", "v179", "v180", "v181", "v182", "v183", "v184", "v185", "v186", "v187", "v188", "v189", "v190", "v191", "v192", "v193", "v194", "v195", "v196", "v197", "v198", "v199", "v200", "v201", "v202", "v203", "v204", "v205", "v206", "v207", "v208", "v209", "v210", "v211", "v212", "v213", "v214", "v215", "v216", "v217", "v218", "v219", "v220", "v221", "v222", "v223", "v224", "v225", "v226", "v227", "v228", "v229", "v230", "v231", "v232", "v233", "v234", "v235", "v236", "v237", "v238", "v239", "v240", "v241", "v242", "v243", "a0", "a1", "a2", "a3", "a4", "a5", "a6", "a7", "a8", "a9", "a10", "a11", "a12", "a13", "a14", "a15", "a16", "a17", "a18", "a19", "a20", "a21", "a22", "a23", "a24", "a25", "a26", "a27", "a28", "a29", "a30", "a31", "a32", "a33", "a34", "a35", "a36", "a37", "a38", "a39", "a40", "a41", "a42", "a43", "a44", "a45", "a46", "a47", "a48", "a49", "a50", "a51", "a52", "a53", "a54", "a55", "a56", "a57", "a58", "a59", "a60", "a61", "a62", "a63", "a64", "a65", "a66", "a67", "a68", "a69", "a70", "a71", "a72", "a73", "a74", "a75", "a76", "a77", "a78", "a79", "a80", "a81", "a82", "a83", "a84", "a85", "a86", "a87", "a88", "a89", "a90", "a91", "a92", "a93", "a94", "a95", "a96", "a97", "a98", "a99", "a100", "a101", "a102", "a103", "a104", "a105", "a106", "a107", "a108", "a109", "a110", "a111", "a112", "a113", "a114", "a115", "a116", "a117", "a118", "a119", "a120", "a121", "a122", "a123", "a124", "a125", "a126", "a127", "a128", "a129", "a130", "a131", "a132", "a133", "a134", "a135", "a136", "a137", "a138", "a139", "a140", "a141", "a142", "a143", "a144", "a145", "a146", "a147", "a148", "a149", "a150", "a151", "a152", "a153", "a154", "a155", "a156", "a157", "a158", "a159", "a160", "a161", "a162", "a163", "a164", "a165", "a166", "a167", "a168", "a169", "a170", "a171", "a172", "a173", "a174", "a175", "a176", "a177", "a178", "a179", "a180", "a181", "a182", "a183", "a184", "a185", "a186", "a187", "a188", "a189", "a190", "a191", "s40", "s41", "s42", "s43", "s44", "s45", "s46", "s47", "s48", "s49", "s50", "s51", "s52", "s53", "s54", "s55", "s56", "s57", "s58", "s59", "s60", "s61", "vcc", "scc", "memory");
    return o;
}
struct OCtx { u4 r; unsigned sg, ssx, sr, su, sum, nt, hx, p0, p1, p2, p3, p4; unsigned vg, vsx, vu, vum, vx, vt, vpx, vpm, vpr, vo, p5, p6, p7, p8, p9, pa; };
DI void o_asm(const OCtx& c) {
    const u16v s0 = {RFL(c.r[0]), RFL(c.r[1]), RFL(c.r[2]), RFL(c.r[3]), RFL(c.sg), RFL(c.ssx), RFL(c.sr), RFL(c.su), RFL(c.sum), RFL(c.nt), RFL(c.hx), RFL(c.p0), RFL(c.p1), RFL(c.p2), RFL(c.p3), RFL(c.p4)}, v0 = {c.vg, c.vsx, c.vu, c.vum, c.vx, c.vt, c.vpx, c.vpm, c.vpr, c.vo, c.p5, c.p6, c.p7, c.p8, c.p9, c.pa};
    asm volatile(
"s_nop 4\ns_xor_b32 s72, s18, 0\ns_mul_i32 s72, s72, 1835008\ns_add_u32 s72, s72, s12\n"
"buffer_load_dwordx4 a[0:3], v8, s[8:11], s72 offen offset:0 sc1\nbuffer_load_dwordx4 a[4:7], v8, s[8:11], s72 offen offset:512 sc1\nbuffer_load_dwordx4 a[8:11], v8, s[8:11], s72 offen offset:1024 sc1\nbuffer_load_dwordx4 a[12:15], v8, s[8:11], s72 offen offset:1536 sc1\n"
"buffer_load_dwordx4 a[16:19], v8, s[8:11], s72 offen offset:2048 sc1\nbuffer_load_dwordx4 a[20:23], v8, s[8:11], s72 offen offset:2560 sc1\nbuffer_load_dwordx4 a[24:27], v8, s[8:11], s72 offen offset:3072 sc1\nbuffer_load_dwordx4 a[28:31], v8, s[8:11], s72 offen offset:3584 sc1\n"
"s_add_u32 s72, s72, 4096\nbuffer_load_dwordx4 a[32:35], v8, s[8:11], s72 offen offset:0 sc1\nbuffer_load_dwordx4 a[36:39], v8, s[8:11], s72 offen offset:512 sc1\nbuffer_load_dwordx4 a[40:43], v8, s[8:11], s72 offen offset:1024 sc1\n"
"buffer_load_dwordx4 a[44:47], v8, s[8:11], s72 offen offset:1536 sc1\nbuffer_load_dwordx4 a[48:51], v8, s[8:11], s72 offen offset:2048 sc1\nbuffer_load_dwordx4 a[52:55], v8, s[8:11], s72 offen offset:2560 sc1\nbuffer_load_dwordx4 a[56:59], v8, s[8:11], s72 offen offset:3072 sc1\n"
"buffer_load_dwordx4 a[60:63], v8, s[8:11], s72 offen offset:3584 sc1\ns_add_u32 s72, s72, 4096\nbuffer_load_dwordx4 a[64:67], v8, s[8:11], s72 offen offset:0 sc1\nbuffer_load_dwordx4 a[68:71], v8, s[8:11], s72 offen offset:512 sc1\n"
"buffer_load_dwordx4 a[72:75], v8, s[8:11], s72 offen offset:1024 sc1\nbuffer_load_dwordx4 a[76:79], v8, s[8:11], s72 offen offset:1536 sc1\nbuffer_load_dwordx4 a[80:83], v8, s[8:11], s72 offen offset:2048 sc1\nbuffer_load_dwordx4 a[84:87], v8, s[8:11], s72 offen offset:2560 sc1\n"
"buffer_load_dwordx4 a[88:91], v8, s[8:11], s72 offen offset:3072 sc1\nbuffer_load_dwordx4 a[92:95], v8, s[8:11], s72 offen offset:3584 sc1\ns_add_u32 s72, s72, 4096\nbuffer_load_dwordx4 a[96:99], v8, s[8:11], s72 offen offset:0 sc1\n"
"buffer_load_dwordx4 a[100:103], v8, s[8:11], s72 offen offset:512 sc1\nbuffer_load_dwordx4 a[104:107], v8, s[8:11], s72 offen offset:1024 sc1\nbuffer_load_dwordx4 a[108:111], v8, s[8:11], s72 offen offset:1536 sc1\ns_xor_b32 s73, s18, 0\n"
"s_lshl_b32 s73, s73, 18\ns_add_u32 s73, s73, s13\nbuffer_load_dwordx4 v[64:67], v9, s[8:11], s73 offen sc1\ns_add_u32 s73, s73, 65536\n"
"buffer_load_dwordx4 v[68:71], v9, s[8:11], s73 offen sc1\ns_add_u32 s73, s73, 65536\nbuffer_load_dwordx4 v[72:75], v9, s[8:11], s73 offen sc1\ns_add_u32 s73, s73, 65536\n"
"buffer_load_dwordx4 v[76:79], v9, s[8:11], s73 offen sc1\ns_xor_b32 s72, s18, 1\ns_mul_i32 s72, s72, 1835008\ns_add_u32 s72, s72, s12\n"
"buffer_load_dwordx4 a[112:115], v8, s[8:11], s72 offen offset:0 sc1\nbuffer_load_dwordx4 a[116:119], v8, s[8:11], s72 offen offset:512 sc1\nbuffer_load_dwordx4 a[120:123], v8, s[8:11], s72 offen offset:1024 sc1\nbuffer_load_dwordx4 a[124:127], v8, s[8:11], s72 offen offset:1536 sc1\n"
"buffer_load_dwordx4 a[128:131], v8, s[8:11], s72 offen offset:2048 sc1\nbuffer_load_dwordx4 a[132:135], v8, s[8:11], s72 offen offset:2560 sc1\nbuffer_load_dwordx4 a[136:139], v8, s[8:11], s72 offen offset:3072 sc1\nbuffer_load_dwordx4 a[140:143], v8, s[8:11], s72 offen offset:3584 sc1\n"
"s_add_u32 s72, s72, 4096\nbuffer_load_dwordx4 a[144:147], v8, s[8:11], s72 offen offset:0 sc1\nbuffer_load_dwordx4 a[148:151], v8, s[8:11], s72 offen offset:512 sc1\nbuffer_load_dwordx4 a[152:155], v8, s[8:11], s72 offen offset:1024 sc1\n"
"buffer_load_dwordx4 a[156:159], v8, s[8:11], s72 offen offset:1536 sc1\nbuffer_load_dwordx4 a[160:163], v8, s[8:11], s72 offen offset:2048 sc1\nbuffer_load_dwordx4 a[164:167], v8, s[8:11], s72 offen offset:2560 sc1\nbuffer_load_dwordx4 a[168:171], v8, s[8:11], s72 offen offset:3072 sc1\n"
"buffer_load_dwordx4 a[172:175], v8, s[8:11], s72 offen offset:3584 sc1\ns_add_u32 s72, s72, 4096\nbuffer_load_dwordx4 a[176:179], v8, s[8:11], s72 offen offset:0 sc1\nbuffer_load_dwordx4 a[180:183], v8, s[8:11], s72 offen offset:512 sc1\n"
"buffer_load_dwordx4 a[184:187], v8, s[8:11], s72 offen offset:1024 sc1\nbuffer_load_dwordx4 a[188:191], v8, s[8:11], s72 offen offset:1536 sc1\nbuffer_load_dwordx4 a[192:195], v8, s[8:11], s72 offen offset:2048 sc1\nbuffer_load_dwordx4 a[196:199], v8, s[8:11], s72 offen offset:2560 sc1\n"
"buffer_load_dwordx4 a[200:203], v8, s[8:11], s72 offen offset:3072 sc1\nbuffer_load_dwordx4 a[204:207], v8, s[8:11], s72 offen offset:3584 sc1\ns_add_u32 s72, s72, 4096\nbuffer_load_dwordx4 a[208:211], v8, s[8:11], s72 offen offset:0 sc1\n"
"buffer_load_dwordx4 a[212:215], v8, s[8:11], s72 offen offset:512 sc1\nbuffer_load_dwordx4 a[216:219], v8, s[8:11], s72 offen offset:1024 sc1\nbuffer_load_dwordx4 a[220:223], v8, s[8:11], s72 offen offset:1536 sc1\ns_add_u32 s76, s15, 0\n"
"v_xor_b32 v24, 0, v12\ns_add_u32 s77, s15, 4096\nv_xor_b32 v25, 32, v12\ns_add_u32 s78, s15, 8192\n"
"v_xor_b32 v26, 64, v12\ns_add_u32 s79, s15, 12288\nv_xor_b32 v27, 96, v12\ns_mov_b32 s80, s16\n"
"v_cmp_gt_i32 s[40:41], 0, v13\nv_cmp_gt_i32 s[42:43], 1, v13\nv_cmp_gt_i32 s[44:45], 2, v13\nv_cmp_gt_i32 s[46:47], 3, v13\n"
"v_cmp_gt_i32 s[48:49], 4, v13\nv_cmp_gt_i32 s[50:51], 5, v13\nv_cmp_gt_i32 s[52:53], 6, v13\nv_cmp_gt_i32 s[54:55], 7, v13\n"
"v_cmp_gt_i32 s[56:57], 16, v13\nv_cmp_gt_i32 s[58:59], 17, v13\nv_cmp_gt_i32 s[60:61], 18, v13\nv_cmp_gt_i32 s[62:63], 19, v13\n"
"v_cmp_gt_i32 s[64:65], 20, v13\nv_cmp_gt_i32 s[66:67], 21, v13\nv_cmp_gt_i32 s[68:69], 22, v13\nv_cmp_gt_i32 s[70:71], 23, v13\n"
"s_barrier\nds_read_b128 v[128:131], v24 offset:0\nds_read_b128 v[132:135], v25 offset:0\nds_read_b128 v[136:139], v26 offset:0\n"
"ds_read_b128 v[140:143], v27 offset:0\nds_read_b128 v[144:147], v24 offset:4096\nds_read_b128 v[148:151], v25 offset:4096\nds_read_b128 v[152:155], v26 offset:4096\n"
"ds_read_b128 v[156:159], v27 offset:4096\nds_read_b128 v[160:163], v24 offset:8192\nds_read_b128 v[164:167], v25 offset:8192\nds_read_b128 v[168:171], v26 offset:8192\n"
"ds_read_b128 v[172:175], v27 offset:8192\ns_waitcnt lgkmcnt(0)\nds_read_b128 v[176:179], v24 offset:12288\nds_read_b128 v[180:183], v25 offset:12288\n"
"ds_read_b128 v[184:187], v26 offset:12288\nds_read_b128 v[188:191], v27 offset:12288\nds_read_b128 v[192:195], v24 offset:16384\nds_read_b128 v[196:199], v25 offset:16384\n"
"ds_read_b128 v[200:203], v26 offset:16384\nds_read_b128 v[204:207], v27 offset:16384\nds_read_b128 v[208:211], v24 offset:20480\nds_read_b128 v[212:215], v25 offset:20480\n"
"ds_read_b128 v[216:219], v26 offset:20480\nds_read_b128 v[220:223], v27 offset:20480\ns_waitcnt lgkmcnt(0)\nds_read_b128 v[224:227], v24 offset:24576\n"
"ds_read_b128 v[228:231], v25 offset:24576\nds_read_b128 v[232:235], v26 offset:24576\nds_read_b128 v[236:239], v27 offset:24576\nv_mov_b32 v31, 0xff800000\n"
"s_waitcnt lgkmcnt(0)\ns_waitcnt vmcnt(59)\nv_mfma_f32_32x32x16_bf16 v[40:55], a[0:3], v[128:131], 0\ns_xor_b32 s73, s18, 1\n"
"s_lshl_b32 s73, s73, 18\ns_add_u32 s73, s73, s13\nbuffer_load_dwordx4 v[80:83], v9, s[8:11], s73 offen sc1\ns_add_u32 s73, s73, 65536\n"
"buffer_load_dwordx4 v[84:87], v9, s[8:11], s73 offen sc1\ns_add_u32 s73, s73, 65536\nbuffer_load_dwordx4 v[88:91], v9, s[8:11], s73 offen sc1\ns_add_u32 s73, s73, 65536\n"
"buffer_load_dwordx4 v[92:95], v9, s[8:11], s73 offen sc1\ns_xor_b32 s72, s18, 2\ns_mul_i32 s72, s72, 1835008\ns_add_u32 s72, s72, s12\n"
"buffer_load_dwordx4 a[0:3], v8, s[8:11], s72 offen offset:0 sc1\ns_waitcnt vmcnt(63)\nv_mfma_f32_32x32x16_bf16 v[40:55], a[4:7], v[132:135], v[40:55]\nbuffer_load_dwordx4 a[4:7], v8, s[8:11], s72 offen offset:512 sc1\n"
"s_waitcnt vmcnt(63)\nv_mfma_f32_32x32x16_bf16 v[40:55], a[8:11], v[136:139], v[40:55]\nbuffer_load_dwordx4 a[8:11], v8, s[8:11], s72 offen offset:1024 sc1\ns_waitcnt vmcnt(63)\n"
"v_mfma_f32_32x32x16_bf16 v[40:55], a[12:15], v[140:143], v[40:55]\nbuffer_load_dwordx4 a[12:15], v8, s[8:11], s72 offen offset:1536 sc1\ns_waitcnt vmcnt(63)\nv_mfma_f32_32x32x16_bf16 v[40:55], a[16:19], v[144:147], v[40:55]\n"
"buffer_load_dwordx4 a[16:19], v8, s[8:11], s72 offen offset:2048 sc1\ns_waitcnt vmcnt(63)\nv_mfma_f32_32x32x16_bf16 v[40:55], a[20:23], v[148:151], v[40:55]\nbuffer_load_dwordx4 a[20:23], v8, s[8:11], s72 offen offset:2560 sc1\n"
"s_waitcnt vmcnt(63)\nv_mfma_f32_32x32x16_bf16 v[40:55], a[24:27], v[152:155], v[40:55]\nbuffer_load_dwordx4 a[24:27], v8, s[8:11], s72 offen offset:3072 sc1\ns_waitcnt vmcnt(63)\n"
"v_mfma_f32_32x32x16_bf16 v[40:55], a[28:31], v[156:159], v[40:55]\nbuffer_load_dwordx4 a[28:31], v8, s[8:11], s72 offen offset:3584 sc1\ns_waitcnt vmcnt(63)\nv_mfma_f32_32x32x16_bf16 v[40:55], a[32:35], v[160:163], v[40:55]\n"
"s_add_u32 s72, s72, 4096\nbuffer_load_dwordx4 a[32:35], v8, s[8:11], s72 offen offset:0 sc1\ns_waitcnt vmcnt(63)\nv_mfma_f32_32x32x16_bf16 v[40:55], a[36:39], v[164:167], v[40:55]\n"
"buffer_load_dwordx4 a[36:39], v8, s[8:11], s72 offen offset:512 sc1\ns_waitcnt vmcnt(63)\nv_mfma_f32_32x32x16_bf16 v[40:55], a[40:43], v[168:171], v[40:55]\nbuffer_load_dwordx4 a[40:43], v8, s[8:11], s72 offen offset:1024 sc1\n"
"s_waitcnt vmcnt(63)\nv_mfma_f32_32x32x16_bf16 v[40:55], a[44:47], v[172:175], v[40:55]\nbuffer_load_dwordx4 a[44:47], v8, s[8:11], s72 offen offset:1536 sc1\ns_waitcnt vmcnt(63)\n"
"v_mfma_f32_32x32x16_bf16 v[40:55], a[48:51], v[176:179], v[40:55]\nbuffer_load_dwordx4 a[48:51], v8, s[8:11], s72 offen offset:2048 sc1\ns_waitcnt vmcnt(63)\nv_mfma_f32_32x32x16_bf16 v[40:55], a[52:55], v[180:183], v[40:55]\n"
"buffer_load_dwordx4 a[52:55], v8, s[8:11], s72 offen offset:2560 sc1\ns_waitcnt vmcnt(63)\nv_mfma_f32_32x32x16_bf16 v[40:55], a[56:59], v[184:187], v[40:55]\nbuffer_load_dwordx4 a[56:59], v8, s[8:11], s72 offen offset:3072 sc1\n"
"s_waitcnt vmcnt(63)\nv_mfma_f32_32x32x16_bf16 v[40:55], a[60:63], v[188:191], v[40:55]\nbuffer_load_dwordx4 a[60:63], v8, s[8:11], s72 offen offset:3584 sc1\ns_waitcnt vmcnt(63)\n"
"v_mfma_f32_32x32x16_bf16 v[40:55], a[64:67], v[192:195], v[40:55]\ns_add_u32 s72, s72, 4096\nbuffer_load_dwordx4 a[64:67], v8, s[8:11], s72 offen offset:0 sc1\ns_waitcnt vmcnt(63)\n"
"v_mfma_f32_32x32x16_bf16 v[40:55], a[68:71], v[196:199], v[40:55]\nbuffer_load_dwordx4 a[68:71], v8, s[8:11], s72 offen offset:512 sc1\ns_waitcnt vmcnt(63)\nv_mfma_f32_32x32x16_bf16 v[40:55], a[72:75], v[200:203], v[40:55]\n"
"buffer_load_dwordx4 a[72:75], v8, s[8:11], s72 offen offset:1024 sc1\ns_waitcnt vmcnt(63)\nv_mfma_f32_32x32x16_bf16 v[40:55], a[76:79], v[204:207], v[40:55]\nbuffer_load_dwordx4 a[76:79], v8, s[8:11], s72 offen offset:1536 sc1\n"
"s_waitcnt vmcnt(63)\nv_mfma_f32_32x32x16_bf16 v[40:55], a[80:83], v[208:211], v[40:55]\nbuffer_load_dwordx4 a[80:83], v8, s[8:11], s72 offen offset:2048 sc1\ns_waitcnt vmcnt(63)\n"
"v_mfma_f32_32x32x16_bf16 v[40:55], a[84:87], v[212:215], v[40:55]\nbuffer_load_dwordx4 a[84:87], v8, s[8:11], s72 offen offset:2560 sc1\ns_waitcnt vmcnt(63)\nv_mfma_f32_32x32x16_bf16 v[40:55], a[88:91], v[216:219], v[40:55]\n"
"buffer_load_dwordx4 a[88:91], v8, s[8:11], s72 offen offset:3072 sc1\ns_waitcnt vmcnt(63)\nv_mfma_f32_32x32x16_bf16 v[40:55], a[92:95], v[220:223], v[40:55]\nbuffer_load_dwordx4 a[92:95], v8, s[8:11], s72 offen offset:3584 sc1\n"
"s_waitcnt vmcnt(63)\nv_mfma_f32_32x32x16_bf16 v[40:55], a[96:99], v[224:227], v[40:55]\ns_add_u32 s72, s72, 4096\nbuffer_load_dwordx4 a[96:99], v8, s[8:11], s72 offen offset:0 sc1\n"
"s_waitcnt vmcnt(63)\nv_mfma_f32_32x32x16_bf16 v[40:55], a[100:103], v[228:231], v[40:55]\nbuffer_load_dwordx4 a[100:103], v8, s[8:11], s72 offen offset:512 sc1\ns_waitcnt vmcnt(63)\n"
"v_mfma_f32_32x32x16_bf16 v[40:55], a[104:107], v[232:235], v[40:55]\nbuffer_load_dwordx4 a[104:107], v8, s[8:11], s72 offen offset:1024 sc1\ns_waitcnt vmcnt(63)\nv_mfma_f32_32x32x16_bf16 v[40:55], a[108:111], v[236:239], v[40:55]\n"
"buffer_load_dwordx4 a[108:111], v8, s[8:11], s72 offen offset:1536 sc1\ns_waitcnt vmcnt(60)\nv_add_f32 v64, v64, v68\nv_add_f32 v72, v72, v76\n"
"v_add_f32 v65, v65, v69\nv_add_f32 v73, v73, v77\nv_add_f32 v66, v66, v70\nv_add_f32 v74, v74, v78\n"
"v_add_f32 v67, v67, v71\nv_add_f32 v75, v75, v79\nv_add_f32 v64, v64, v72\nv_add_f32 v65, v65, v73\n"
"v_add_f32 v66, v66, v74\nv_add_f32 v67, v67, v75\nv_max3_f32 v28, v64, v65, v66\nv_max_f32 v28, v28, v67\n"
"v_cndmask_b32 v40, v40, v31, s[40:41]\nv_cndmask_b32 v41, v41, v31, s[42:43]\nv_cndmask_b32 v42, v42, v31, s[44:45]\nv_cndmask_b32 v43, v43, v31, s[46:47]\n"
"v_cndmask_b32 v44, v44, v31, s[48:49]\nv_cndmask_b32 v45, v45, v31, s[50:51]\nv_cndmask_b32 v46, v46, v31, s[52:53]\nv_cndmask_b32 v47, v47, v31, s[54:55]\n"
"v_cndmask_b32 v48, v48, v31, s[56:57]\nv_cndmask_b32 v49, v49, v31, s[58:59]\nv_cndmask_b32 v50, v50, v31, s[60:61]\nv_cndmask_b32 v51, v51, v31, s[62:63]\n"
"v_cndmask_b32 v52, v52, v31, s[64:65]\nv_cndmask_b32 v53, v53, v31, s[66:67]\nv_cndmask_b32 v54, v54, v31, s[68:69]\nv_cndmask_b32 v55, v55, v31, s[70:71]\n"
"v_max3_f32 v28, v28, v40, v41\nv_max3_f32 v28, v28, v42, v43\nv_max3_f32 v28, v28, v44, v45\nv_max3_f32 v28, v28, v46, v47\n"
"v_max3_f32 v28, v28, v48, v49\nv_max3_f32 v28, v28, v50, v51\nv_max3_f32 v28, v28, v52, v53\nv_max3_f32 v28, v28, v54, v55\n"
"v_mov_b32 v30, v28\ns_nop 1\nv_permlane32_swap_b32 v28, v30\nv_max_f32 v28, v28, v30\n"
"v_sub_f32 v40, v40, v28\nv_sub_f32 v41, v41, v28\nv_sub_f32 v42, v42, v28\nv_sub_f32 v43, v43, v28\n"
"v_sub_f32 v44, v44, v28\nv_sub_f32 v45, v45, v28\nv_sub_f32 v46, v46, v28\nv_sub_f32 v47, v47, v28\n"
"v_sub_f32 v48, v48, v28\nv_sub_f32 v49, v49, v28\nv_sub_f32 v50, v50, v28\nv_sub_f32 v51, v51, v28\n"
"v_sub_f32 v52, v52, v28\nv_sub_f32 v53, v53, v28\nv_sub_f32 v54, v54, v28\nv_sub_f32 v55, v55, v28\n"
"v_sub_f32 v64, v64, v28\nv_sub_f32 v65, v65, v28\nv_sub_f32 v66, v66, v28\nv_sub_f32 v67, v67, v28\n"
"v_exp_f32 v40, v40\nv_exp_f32 v41, v41\nv_exp_f32 v42, v42\nv_exp_f32 v43, v43\n"
"v_exp_f32 v44, v44\nv_exp_f32 v45, v45\nv_exp_f32 v46, v46\nv_exp_f32 v47, v47\n"
"v_exp_f32 v48, v48\nv_exp_f32 v49, v49\nv_exp_f32 v50, v50\nv_exp_f32 v51, v51\n"
"v_exp_f32 v52, v52\nv_exp_f32 v53, v53\nv_exp_f32 v54, v54\nv_exp_f32 v55, v55\n"
"v_exp_f32 v64, v64\nv_exp_f32 v65, v65\nv_exp_f32 v66, v66\nv_exp_f32 v67, v67\n"
"v_add_f32 v29, v40, v41\nv_add_f32 v30, v42, v43\nv_add_f32 v29, v29, v44\nv_add_f32 v30, v30, v45\n"
"v_add_f32 v29, v29, v46\nv_add_f32 v30, v30, v47\nv_add_f32 v29, v29, v48\nv_add_f32 v30, v30, v49\n"
"v_add_f32 v29, v29, v50\nv_add_f32 v30, v30, v51\nv_add_f32 v29, v29, v52\nv_add_f32 v30, v30, v53\n"
"v_add_f32 v29, v29, v54\nv_add_f32 v30, v30, v55\nv_add_f32 v29, v29, v64\nv_add_f32 v30, v30, v65\n"
"v_add_f32 v29, v29, v66\nv_add_f32 v30, v30, v67\nv_add_f32 v29, v29, v30\nv_mov_b32 v30, v29\n"
"s_nop 1\nv_permlane32_swap_b32 v29, v30\nv_add_f32 v29, v29, v30\nv_rcp_f32 v29, v29\n"
"s_nop 0\nv_mul_f32 v40, v40, v29\nv_mul_f32 v41, v41, v29\nv_mul_f32 v42, v42, v29\n"
"v_mul_f32 v43, v43, v29\nv_mul_f32 v44, v44, v29\nv_mul_f32 v45, v45, v29\nv_mul_f32 v46, v46, v29\n"
"v_mul_f32 v47, v47, v29\nv_mul_f32 v48, v48, v29\nv_mul_f32 v49, v49, v29\nv_mul_f32 v50, v50, v29\n"
"v_mul_f32 v51, v51, v29\nv_mul_f32 v52, v52, v29\nv_mul_f32 v53, v53, v29\nv_mul_f32 v54, v54, v29\n"
"v_mul_f32 v55, v55, v29\nv_mul_f32 v64, v64, v29\nv_mul_f32 v65, v65, v29\nv_mul_f32 v66, v66, v29\n"
"v_mul_f32 v67, v67, v29\nv_cvt_pk_bf16_f32 v56, v40, v41\nv_cvt_pk_bf16_f32 v57, v42, v43\nv_cvt_pk_bf16_f32 v58, v44, v45\n"
"v_cvt_pk_bf16_f32 v59, v46, v47\nv_cvt_pk_bf16_f32 v60, v48, v49\nv_cvt_pk_bf16_f32 v61, v50, v51\nv_cvt_pk_bf16_f32 v62, v52, v53\n"
"v_cvt_pk_bf16_f32 v63, v54, v55\nv_cvt_pk_bf16_f32 v32, v64, v65\nv_cvt_pk_bf16_f32 v36, v66, v67\nv_xor_b32 v30, 0, v14\n"
"ds_write_b128 v30, v[56:59]\nds_write_b128 v30, v[60:63] offset:1024\ns_waitcnt vmcnt(59)\nv_mfma_f32_32x32x16_bf16 v[40:55], a[112:115], v[128:131], 0\n"
"s_xor_b32 s73, s18, 2\ns_lshl_b32 s73, s73, 18\ns_add_u32 s73, s73, s13\nbuffer_load_dwordx4 v[96:99], v9, s[8:11], s73 offen sc1\n"
"s_add_u32 s73, s73, 65536\nbuffer_load_dwordx4 v[100:103], v9, s[8:11], s73 offen sc1\ns_add_u32 s73, s73, 65536\nbuffer_load_dwordx4 v[104:107], v9, s[8:11], s73 offen sc1\n"
"s_add_u32 s73, s73, 65536\nbuffer_load_dwordx4 v[108:111], v9, s[8:11], s73 offen sc1\ns_xor_b32 s72, s18, 3\ns_mul_i32 s72, s72, 1835008\n"
"s_add_u32 s72, s72, s12\nbuffer_load_dwordx4 a[112:115], v8, s[8:11], s72 offen offset:0 sc1\ns_waitcnt vmcnt(63)\nv_mfma_f32_32x32x16_bf16 v[40:55], a[116:119], v[132:135], v[40:55]\n"
"buffer_load_dwordx4 a[116:119], v8, s[8:11], s72 offen offset:512 sc1\ns_waitcnt vmcnt(63)\nv_mfma_f32_32x32x16_bf16 v[40:55], a[120:123], v[136:139], v[40:55]\nbuffer_load_dwordx4 a[120:123], v8, s[8:11], s72 offen offset:1024 sc1\n"
"s_waitcnt vmcnt(63)\nv_mfma_f32_32x32x16_bf16 v[40:55], a[124:127], v[140:143], v[40:55]\nbuffer_load_dwordx4 a[124:127], v8, s[8:11], s72 offen offset:1536 sc1\ns_waitcnt vmcnt(63)\n"
"v_mfma_f32_32x32x16_bf16 v[40:55], a[128:131], v[144:147], v[40:55]\nbuffer_load_dwordx4 a[128:131], v8, s[8:11], s72 offen offset:2048 sc1\ns_waitcnt vmcnt(63)\nv_mfma_f32_32x32x16_bf16 v[40:55], a[132:135], v[148:151], v[40:55]\n"
"buffer_load_dwordx4 a[132:135], v8, s[8:11], s72 offen offset:2560 sc1\ns_waitcnt vmcnt(63)\nv_mfma_f32_32x32x16_bf16 v[40:55], a[136:139], v[152:155], v[40:55]\nbuffer_load_dwordx4 a[136:139], v8, s[8:11], s72 offen offset:3072 sc1\n"
"s_waitcnt vmcnt(63)\nv_mfma_f32_32x32x16_bf16 v[40:55], a[140:143], v[156:159], v[40:55]\nbuffer_load_dwordx4 a[140:143], v8, s[8:11], s72 offen offset:3584 sc1\ns_waitcnt vmcnt(63)\n"
"v_mfma_f32_32x32x16_bf16 v[40:55], a[144:147], v[160:163], v[40:55]\ns_add_u32 s72, s72, 4096\nbuffer_load_dwordx4 a[144:147], v8, s[8:11], s72 offen offset:0 sc1\ns_waitcnt vmcnt(63)\n"
"v_mfma_f32_32x32x16_bf16 v[40:55], a[148:151], v[164:167], v[40:55]\nbuffer_load_dwordx4 a[148:151], v8, s[8:11], s72 offen offset:512 sc1\ns_waitcnt vmcnt(63)\nv_mfma_f32_32x32x16_bf16 v[40:55], a[152:155], v[168:171], v[40:55]\n"
"buffer_load_dwordx4 a[152:155], v8, s[8:11], s72 offen offset:1024 sc1\ns_waitcnt vmcnt(63)\nv_mfma_f32_32x32x16_bf16 v[40:55], a[156:159], v[172:175], v[40:55]\nbuffer_load_dwordx4 a[156:159], v8, s[8:11], s72 offen offset:1536 sc1\n"
"s_waitcnt vmcnt(63)\nv_mfma_f32_32x32x16_bf16 v[40:55], a[160:163], v[176:179], v[40:55]\nbuffer_load_dwordx4 a[160:163], v8, s[8:11], s72 offen offset:2048 sc1\ns_waitcnt vmcnt(63)\n"
"v_mfma_f32_32x32x16_bf16 v[40:55], a[164:167], v[180:183], v[40:55]\nbuffer_load_dwordx4 a[164:167], v8, s[8:11], s72 offen offset:2560 sc1\ns_waitcnt vmcnt(63)\nv_mfma_f32_32x32x16_bf16 v[40:55], a[168:171], v[184:187], v[40:55]\n"
"buffer_load_dwordx4 a[168:171], v8, s[8:11], s72 offen offset:3072 sc1\ns_waitcnt vmcnt(63)\nv_mfma_f32_32x32x16_bf16 v[40:55], a[172:175], v[188:191], v[40:55]\nbuffer_load_dwordx4 a[172:175], v8, s[8:11], s72 offen offset:3584 sc1\n"
"s_waitcnt vmcnt(63)\nv_mfma_f32_32x32x16_bf16 v[40:55], a[176:179], v[192:195], v[40:55]\ns_add_u32 s72, s72, 4096\nbuffer_load_dwordx4 a[176:179], v8, s[8:11], s72 offen offset:0 sc1\n"
"s_waitcnt vmcnt(63)\nv_mfma_f32_32x32x16_bf16 v[40:55], a[180:183], v[196:199], v[40:55]\nbuffer_load_dwordx4 a[180:183], v8, s[8:11], s72 offen offset:512 sc1\ns_waitcnt vmcnt(63)\n"
"v_mfma_f32_32x32x16_bf16 v[40:55], a[184:187], v[200:203], v[40:55]\nbuffer_load_dwordx4 a[184:187], v8, s[8:11], s72 offen offset:1024 sc1\ns_waitcnt vmcnt(63)\nv_mfma_f32_32x32x16_bf16 v[40:55], a[188:191], v[204:207], v[40:55]\n"
"buffer_load_dwordx4 a[188:191], v8, s[8:11], s72 offen offset:1536 sc1\ns_waitcnt vmcnt(63)\nv_mfma_f32_32x32x16_bf16 v[40:55], a[192:195], v[208:211], v[40:55]\nbuffer_load_dwordx4 a[192:195], v8, s[8:11], s72 offen offset:2048 sc1\n"
"s_waitcnt vmcnt(63)\nv_mfma_f32_32x32x16_bf16 v[40:55], a[196:199], v[212:215], v[40:55]\nbuffer_load_dwordx4 a[196:199], v8, s[8:11], s72 offen offset:2560 sc1\ns_waitcnt vmcnt(63)\n"
"v_mfma_f32_32x32x16_bf16 v[40:55], a[200:203], v[216:219], v[40:55]\nbuffer_load_dwordx4 a[200:203], v8, s[8:11], s72 offen offset:3072 sc1\ns_waitcnt vmcnt(63)\nv_mfma_f32_32x32x16_bf16 v[40:55], a[204:207], v[220:223], v[40:55]\n"
"buffer_load_dwordx4 a[204:207], v8, s[8:11], s72 offen offset:3584 sc1\ns_waitcnt vmcnt(63)\nv_mfma_f32_32x32x16_bf16 v[40:55], a[208:211], v[224:227], v[40:55]\ns_add_u32 s72, s72, 4096\n"
"buffer_load_dwordx4 a[208:211], v8, s[8:11], s72 offen offset:0 sc1\ns_waitcnt vmcnt(63)\nv_mfma_f32_32x32x16_bf16 v[40:55], a[212:215], v[228:231], v[40:55]\nbuffer_load_dwordx4 a[212:215], v8, s[8:11], s72 offen offset:512 sc1\n"
"s_waitcnt vmcnt(63)\nv_mfma_f32_32x32x16_bf16 v[40:55], a[216:219], v[232:235], v[40:55]\nbuffer_load_dwordx4 a[216:219], v8, s[8:11], s72 offen offset:1024 sc1\ns_waitcnt vmcnt(63)\n"
"v_mfma_f32_32x32x16_bf16 v[40:55], a[220:223], v[236:239], v[40:55]\nbuffer_load_dwordx4 a[220:223], v8, s[8:11], s72 offen offset:1536 sc1\ns_waitcnt vmcnt(60)\nv_add_f32 v80, v80, v84\n"
"v_add_f32 v88, v88, v92\nv_add_f32 v81, v81, v85\nv_add_f32 v89, v89, v93\nv_add_f32 v82, v82, v86\n"
"v_add_f32 v90, v90, v94\nv_add_f32 v83, v83, v87\nv_add_f32 v91, v91, v95\nv_add_f32 v80, v80, v88\n"
"v_add_f32 v81, v81, v89\nv_add_f32 v82, v82, v90\nv_add_f32 v83, v83, v91\nv_max3_f32 v28, v80, v81, v82\n"
"v_max_f32 v28, v28, v83\nv_cndmask_b32 v40, v40, v31, s[40:41]\nv_cndmask_b32 v41, v41, v31, s[42:43]\nv_cndmask_b32 v42, v42, v31, s[44:45]\n"
"v_cndmask_b32 v43, v43, v31, s[46:47]\nv_cndmask_b32 v44, v44, v31, s[48:49]\nv_cndmask_b32 v45, v45, v31, s[50:51]\nv_cndmask_b32 v46, v46, v31, s[52:53]\n"
"v_cndmask_b32 v47, v47, v31, s[54:55]\nv_cndmask_b32 v48, v48, v31, s[56:57]\nv_cndmask_b32 v49, v49, v31, s[58:59]\nv_cndmask_b32 v50, v50, v31, s[60:61]\n"
"v_cndmask_b32 v51, v51, v31, s[62:63]\nv_cndmask_b32 v52, v52, v31, s[64:65]\nv_cndmask_b32 v53, v53, v31, s[66:67]\nv_cndmask_b32 v54, v54, v31, s[68:69]\n"
"v_cndmask_b32 v55, v55, v31, s[70:71]\nv_max3_f32 v28, v28, v40, v41\nv_max3_f32 v28, v28, v42, v43\nv_max3_f32 v28, v28, v44, v45\n"
"v_max3_f32 v28, v28, v46, v47\nv_max3_f32 v28, v28, v48, v49\nv_max3_f32 v28, v28, v50, v51\nv_max3_f32 v28, v28, v52, v53\n"
"v_max3_f32 v28, v28, v54, v55\nv_mov_b32 v30, v28\ns_nop 1\nv_permlane32_swap_b32 v28, v30\n"
"v_max_f32 v28, v28, v30\nv_sub_f32 v40, v40, v28\nv_sub_f32 v41, v41, v28\nv_sub_f32 v42, v42, v28\n"
"v_sub_f32 v43, v43, v28\nv_sub_f32 v44, v44, v28\nv_sub_f32 v45, v45, v28\nv_sub_f32 v46, v46, v28\n"
"v_sub_f32 v47, v47, v28\nv_sub_f32 v48, v48, v28\nv_sub_f32 v49, v49, v28\nv_sub_f32 v50, v50, v28\n"
"v_sub_f32 v51, v51, v28\nv_sub_f32 v52, v52, v28\nv_sub_f32 v53, v53, v28\nv_sub_f32 v54, v54, v28\n"
"v_sub_f32 v55, v55, v28\nv_sub_f32 v80, v80, v28\nv_sub_f32 v81, v81, v28\nv_sub_f32 v82, v82, v28\n"
"v_sub_f32 v83, v83, v28\nv_exp_f32 v40, v40\nv_exp_f32 v41, v41\nv_exp_f32 v42, v42\n"
"v_exp_f32 v43, v43\nv_exp_f32 v44, v44\nv_exp_f32 v45, v45\nv_exp_f32 v46, v46\n"
"v_exp_f32 v47, v47\nv_exp_f32 v48, v48\nv_exp_f32 v49, v49\nv_exp_f32 v50, v50\n"
"v_exp_f32 v51, v51\nv_exp_f32 v52, v52\nv_exp_f32 v53, v53\nv_exp_f32 v54, v54\n"
"v_exp_f32 v55, v55\nv_exp_f32 v80, v80\nv_exp_f32 v81, v81\nv_exp_f32 v82, v82\n"
"v_exp_f32 v83, v83\nv_add_f32 v29, v40, v41\nv_add_f32 v30, v42, v43\nv_add_f32 v29, v29, v44\n"
"v_add_f32 v30, v30, v45\nv_add_f32 v29, v29, v46\nv_add_f32 v30, v30, v47\nv_add_f32 v29, v29, v48\n"
"v_add_f32 v30, v30, v49\nv_add_f32 v29, v29, v50\nv_add_f32 v30, v30, v51\nv_add_f32 v29, v29, v52\n"
"v_add_f32 v30, v30, v53\nv_add_f32 v29, v29, v54\nv_add_f32 v30, v30, v55\nv_add_f32 v29, v29, v80\n"
"v_add_f32 v30, v30, v81\nv_add_f32 v29, v29, v82\nv_add_f32 v30, v30, v83\nv_add_f32 v29, v29, v30\n"
"v_mov_b32 v30, v29\ns_nop 1\nv_permlane32_swap_b32 v29, v30\nv_add_f32 v29, v29, v30\n"
"v_rcp_f32 v29, v29\ns_nop 0\nv_mul_f32 v40, v40, v29\nv_mul_f32 v41, v41, v29\n"
"v_mul_f32 v42, v42, v29\nv_mul_f32 v43, v43, v29\nv_mul_f32 v44, v44, v29\nv_mul_f32 v45, v45, v29\n"
"v_mul_f32 v46, v46, v29\nv_mul_f32 v47, v47, v29\nv_mul_f32 v48, v48, v29\nv_mul_f32 v49, v49, v29\n"
"v_mul_f32 v50, v50, v29\nv_mul_f32 v51, v51, v29\nv_mul_f32 v52, v52, v29\nv_mul_f32 v53, v53, v29\n"
"v_mul_f32 v54, v54, v29\nv_mul_f32 v55, v55, v29\nv_mul_f32 v80, v80, v29\nv_mul_f32 v81, v81, v29\n"
"v_mul_f32 v82, v82, v29\nv_mul_f32 v83, v83, v29\nv_cvt_pk_bf16_f32 v56, v40, v41\nv_cvt_pk_bf16_f32 v57, v42, v43\n"
"v_cvt_pk_bf16_f32 v58, v44, v45\nv_cvt_pk_bf16_f32 v59, v46, v47\nv_cvt_pk_bf16_f32 v60, v48, v49\nv_cvt_pk_bf16_f32 v61, v50, v51\n"
"v_cvt_pk_bf16_f32 v62, v52, v53\nv_cvt_pk_bf16_f32 v63, v54, v55\nv_cvt_pk_bf16_f32 v33, v80, v81\nv_cvt_pk_bf16_f32 v37, v82, v83\n"
"v_xor_b32 v30, 2048, v14\nds_write_b128 v30, v[56:59]\nds_write_b128 v30, v[60:63] offset:1024\ns_waitcnt vmcnt(59)\n"
"v_mfma_f32_32x32x16_bf16 v[40:55], a[0:3], v[128:131], 0\ns_xor_b32 s73, s18, 3\ns_lshl_b32 s73, s73, 18\ns_add_u32 s73, s73, s13\n"
"buffer_load_dwordx4 v[112:115], v9, s[8:11], s73 offen sc1\ns_add_u32 s73, s73, 65536\nbuffer_load_dwordx4 v[116:119], v9, s[8:11], s73 offen sc1\ns_add_u32 s73, s73, 65536\n"
"buffer_load_dwordx4 v[120:123], v9, s[8:11], s73 offen sc1\ns_add_u32 s73, s73, 65536\nbuffer_load_dwordx4 v[124:127], v9, s[8:11], s73 offen sc1\nbuffer_load_dwordx4 a[0:3], v10, s[8:11], s76 offen offset:0 sc1\n"
"s_waitcnt vmcnt(63)\nv_mfma_f32_32x32x16_bf16 v[40:55], a[4:7], v[132:135], v[40:55]\nbuffer_load_dwordx4 a[4:7], v10, s[8:11], s76 offen offset:512 sc1\ns_waitcnt vmcnt(63)\n"
"v_mfma_f32_32x32x16_bf16 v[40:55], a[8:11], v[136:139], v[40:55]\nbuffer_load_dwordx4 a[8:11], v10, s[8:11], s76 offen offset:1024 sc1\ns_waitcnt vmcnt(63)\nv_mfma_f32_32x32x16_bf16 v[40:55], a[12:15], v[140:143], v[40:55]\n"
"buffer_load_dwordx4 a[12:15], v10, s[8:11], s76 offen offset:1536 sc1\ns_waitcnt vmcnt(63)\nv_mfma_f32_32x32x16_bf16 v[40:55], a[16:19], v[144:147], v[40:55]\nbuffer_load_dwordx4 a[16:19], v10, s[8:11], s76 offen offset:2048 sc1\n"
"s_waitcnt vmcnt(63)\nv_mfma_f32_32x32x16_bf16 v[40:55], a[20:23], v[148:151], v[40:55]\nbuffer_load_dwordx4 a[20:23], v10, s[8:11], s76 offen offset:2560 sc1\ns_waitcnt vmcnt(63)\n"
"v_mfma_f32_32x32x16_bf16 v[40:55], a[24:27], v[152:155], v[40:55]\nbuffer_load_dwordx4 a[24:27], v10, s[8:11], s76 offen offset:3072 sc1\ns_waitcnt vmcnt(63)\nv_mfma_f32_32x32x16_bf16 v[40:55], a[28:31], v[156:159], v[40:55]\n"
"buffer_load_dwordx4 a[28:31], v10, s[8:11], s76 offen offset:3584 sc1\ns_waitcnt vmcnt(63)\nv_mfma_f32_32x32x16_bf16 v[40:55], a[32:35], v[160:163], v[40:55]\nbuffer_load_dwordx4 a[32:35], v10, s[8:11], s77 offen offset:0 sc1\n"
"s_waitcnt vmcnt(63)\nv_mfma_f32_32x32x16_bf16 v[40:55], a[36:39], v[164:167], v[40:55]\nbuffer_load_dwordx4 a[36:39], v10, s[8:11], s77 offen offset:512 sc1\ns_waitcnt vmcnt(63)\n"
"v_mfma_f32_32x32x16_bf16 v[40:55], a[40:43], v[168:171], v[40:55]\nbuffer_load_dwordx4 a[40:43], v10, s[8:11], s77 offen offset:1024 sc1\ns_waitcnt vmcnt(63)\nv_mfma_f32_32x32x16_bf16 v[40:55], a[44:47], v[172:175], v[40:55]\n"
"buffer_load_dwordx4 a[44:47], v10, s[8:11], s77 offen offset:1536 sc1\ns_waitcnt vmcnt(63)\nv_mfma_f32_32x32x16_bf16 v[40:55], a[48:51], v[176:179], v[40:55]\nbuffer_load_dwordx4 a[48:51], v10, s[8:11], s77 offen offset:2048 sc1\n"
"s_waitcnt vmcnt(63)\nv_mfma_f32_32x32x16_bf16 v[40:55], a[52:55], v[180:183], v[40:55]\nbuffer_load_dwordx4 a[52:55], v10, s[8:11], s77 offen offset:2560 sc1\ns_waitcnt vmcnt(63)\n"
"v_mfma_f32_32x32x16_bf16 v[40:55], a[56:59], v[184:187], v[40:55]\nbuffer_load_dwordx4 a[56:59], v10, s[8:11], s77 offen offset:3072 sc1\ns_waitcnt vmcnt(63)\nv_mfma_f32_32x32x16_bf16 v[40:55], a[60:63], v[188:191], v[40:55]\n"
"buffer_load_dwordx4 a[60:63], v10, s[8:11], s77 offen offset:3584 sc1\ns_waitcnt vmcnt(63)\nv_mfma_f32_32x32x16_bf16 v[40:55], a[64:67], v[192:195], v[40:55]\nbuffer_load_dwordx4 a[64:67], v10, s[8:11], s78 offen offset:0 sc1\n"
"s_waitcnt vmcnt(63)\nv_mfma_f32_32x32x16_bf16 v[40:55], a[68:71], v[196:199], v[40:55]\nbuffer_load_dwordx4 a[68:71], v10, s[8:11], s78 offen offset:512 sc1\ns_waitcnt vmcnt(63)\n"
"v_mfma_f32_32x32x16_bf16 v[40:55], a[72:75], v[200:203], v[40:55]\nbuffer_load_dwordx4 a[72:75], v10, s[8:11], s78 offen offset:1024 sc1\ns_waitcnt vmcnt(63)\nv_mfma_f32_32x32x16_bf16 v[40:55], a[76:79], v[204:207], v[40:55]\n"
"buffer_load_dwordx4 a[76:79], v10, s[8:11], s78 offen offset:1536 sc1\ns_waitcnt vmcnt(63)\nv_mfma_f32_32x32x16_bf16 v[40:55], a[80:83], v[208:211], v[40:55]\nbuffer_load_dwordx4 a[80:83], v10, s[8:11], s78 offen offset:2048 sc1\n"
"s_waitcnt vmcnt(63)\nv_mfma_f32_32x32x16_bf16 v[40:55], a[84:87], v[212:215], v[40:55]\nbuffer_load_dwordx4 a[84:87], v10, s[8:11], s78 offen offset:2560 sc1\ns_waitcnt vmcnt(63)\n"
"v_mfma_f32_32x32x16_bf16 v[40:55], a[88:91], v[216:219], v[40:55]\nbuffer_load_dwordx4 a[88:91], v10, s[8:11], s78 offen offset:3072 sc1\ns_waitcnt vmcnt(63)\nv_mfma_f32_32x32x16_bf16 v[40:55], a[92:95], v[220:223], v[40:55]\n"
"buffer_load_dwordx4 a[92:95], v10, s[8:11], s78 offen offset:3584 sc1\ns_waitcnt vmcnt(63)\nv_mfma_f32_32x32x16_bf16 v[40:55], a[96:99], v[224:227], v[40:55]\nbuffer_load_dwordx4 a[96:99], v10, s[8:11], s79 offen offset:0 sc1\n"
"s_waitcnt vmcnt(63)\nv_mfma_f32_32x32x16_bf16 v[40:55], a[100:103], v[228:231], v[40:55]\nbuffer_load_dwordx4 a[100:103], v10, s[8:11], s79 offen offset:512 sc1\ns_waitcnt vmcnt(63)\n"
"v_mfma_f32_32x32x16_bf16 v[40:55], a[104:107], v[232:235], v[40:55]\nbuffer_load_dwordx4 a[104:107], v10, s[8:11], s79 offen offset:1024 sc1\ns_waitcnt vmcnt(63)\nv_mfma_f32_32x32x16_bf16 v[40:55], a[108:111], v[236:239], v[40:55]\n"
"buffer_load_dwordx4 a[108:111], v10, s[8:11], s79 offen offset:1536 sc1\ns_waitcnt vmcnt(60)\nv_add_f32 v96, v96, v100\nv_add_f32 v104, v104, v108\n"
"v_add_f32 v97, v97, v101\nv_add_f32 v105, v105, v109\nv_add_f32 v98, v98, v102\nv_add_f32 v106, v106, v110\n"
"v_add_f32 v99, v99, v103\nv_add_f32 v107, v107, v111\nv_add_f32 v96, v96, v104\nv_add_f32 v97, v97, v105\n"
"v_add_f32 v98, v98, v106\nv_add_f32 v99, v99, v107\nv_max3_f32 v28, v96, v97, v98\nv_max_f32 v28, v28, v99\n"
"v_cndmask_b32 v40, v40, v31, s[40:41]\nv_cndmask_b32 v41, v41, v31, s[42:43]\nv_cndmask_b32 v42, v42, v31, s[44:45]\nv_cndmask_b32 v43, v43, v31, s[46:47]\n"
"v_cndmask_b32 v44, v44, v31, s[48:49]\nv_cndmask_b32 v45, v45, v31, s[50:51]\nv_cndmask_b32 v46, v46, v31, s[52:53]\nv_cndmask_b32 v47, v47, v31, s[54:55]\n"
"v_cndmask_b32 v48, v48, v31, s[56:57]\nv_cndmask_b32 v49, v49, v31, s[58:59]\nv_cndmask_b32 v50, v50, v31, s[60:61]\nv_cndmask_b32 v51, v51, v31, s[62:63]\n"
"v_cndmask_b32 v52, v52, v31, s[64:65]\nv_cndmask_b32 v53, v53, v31, s[66:67]\nv_cndmask_b32 v54, v54, v31, s[68:69]\nv_cndmask_b32 v55, v55, v31, s[70:71]\n"
"v_max3_f32 v28, v28, v40, v41\nv_max3_f32 v28, v28, v42, v43\nv_max3_f32 v28, v28, v44, v45\nv_max3_f32 v28, v28, v46, v47\n"
"v_max3_f32 v28, v28, v48, v49\nv_max3_f32 v28, v28, v50, v51\nv_max3_f32 v28, v28, v52, v53\nv_max3_f32 v28, v28, v54, v55\n"
"v_mov_b32 v30, v28\ns_nop 1\nv_permlane32_swap_b32 v28, v30\nv_max_f32 v28, v28, v30\n"
"v_sub_f32 v40, v40, v28\nv_sub_f32 v41, v41, v28\nv_sub_f32 v42, v42, v28\nv_sub_f32 v43, v43, v28\n"
"v_sub_f32 v44, v44, v28\nv_sub_f32 v45, v45, v28\nv_sub_f32 v46, v46, v28\nv_sub_f32 v47, v47, v28\n"
"v_sub_f32 v48, v48, v28\nv_sub_f32 v49, v49, v28\nv_sub_f32 v50, v50, v28\nv_sub_f32 v51, v51, v28\n"
"v_sub_f32 v52, v52, v28\nv_sub_f32 v53, v53, v28\nv_sub_f32 v54, v54, v28\nv_sub_f32 v55, v55, v28\n"
"v_sub_f32 v96, v96, v28\nv_sub_f32 v97, v97, v28\nv_sub_f32 v98, v98, v28\nv_sub_f32 v99, v99, v28\n"
"v_exp_f32 v40, v40\nv_exp_f32 v41, v41\nv_exp_f32 v42, v42\nv_exp_f32 v43, v43\n"
"v_exp_f32 v44, v44\nv_exp_f32 v45, v45\nv_exp_f32 v46, v46\nv_exp_f32 v47, v47\n"
"v_exp_f32 v48, v48\nv_exp_f32 v49, v49\nv_exp_f32 v50, v50\nv_exp_f32 v51, v51\n"
"v_exp_f32 v52, v52\nv_exp_f32 v53, v53\nv_exp_f32 v54, v54\nv_exp_f32 v55, v55\n"
"v_exp_f32 v96, v96\nv_exp_f32 v97, v97\nv_exp_f32 v98, v98\nv_exp_f32 v99, v99\n"
"v_add_f32 v29, v40, v41\nv_add_f32 v30, v42, v43\nv_add_f32 v29, v29, v44\nv_add_f32 v30, v30, v45\n"
"v_add_f32 v29, v29, v46\nv_add_f32 v30, v30, v47\nv_add_f32 v29, v29, v48\nv_add_f32 v30, v30, v49\n"
"v_add_f32 v29, v29, v50\nv_add_f32 v30, v30, v51\nv_add_f32 v29, v29, v52\nv_add_f32 v30, v30, v53\n"
"v_add_f32 v29, v29, v54\nv_add_f32 v30, v30, v55\nv_add_f32 v29, v29, v96\nv_add_f32 v30, v30, v97\n"
"v_add_f32 v29, v29, v98\nv_add_f32 v30, v30, v99\nv_add_f32 v29, v29, v30\nv_mov_b32 v30, v29\n"
"s_nop 1\nv_permlane32_swap_b32 v29, v30\nv_add_f32 v29, v29, v30\nv_rcp_f32 v29, v29\n"
"s_nop 0\nv_mul_f32 v40, v40, v29\nv_mul_f32 v41, v41, v29\nv_mul_f32 v42, v42, v29\n"
"v_mul_f32 v43, v43, v29\nv_mul_f32 v44, v44, v29\nv_mul_f32 v45, v45, v29\nv_mul_f32 v46, v46, v29\n"
"v_mul_f32 v47, v47, v29\nv_mul_f32 v48, v48, v29\nv_mul_f32 v49, v49, v29\nv_mul_f32 v50, v50, v29\n"
"v_mul_f32 v51, v51, v29\nv_mul_f32 v52, v52, v29\nv_mul_f32 v53, v53, v29\nv_mul_f32 v54, v54, v29\n"
"v_mul_f32 v55, v55, v29\nv_mul_f32 v96, v96, v29\nv_mul_f32 v97, v97, v29\nv_mul_f32 v98, v98, v29\n"
"v_mul_f32 v99, v99, v29\nv_cvt_pk_bf16_f32 v56, v40, v41\nv_cvt_pk_bf16_f32 v57, v42, v43\nv_cvt_pk_bf16_f32 v58, v44, v45\n"
"v_cvt_pk_bf16_f32 v59, v46, v47\nv_cvt_pk_bf16_f32 v60, v48, v49\nv_cvt_pk_bf16_f32 v61, v50, v51\nv_cvt_pk_bf16_f32 v62, v52, v53\n"
"v_cvt_pk_bf16_f32 v63, v54, v55\nv_cvt_pk_bf16_f32 v34, v96, v97\nv_cvt_pk_bf16_f32 v38, v98, v99\nv_xor_b32 v30, 4096, v14\n"
"ds_write_b128 v30, v[56:59]\nds_write_b128 v30, v[60:63] offset:1024\ns_waitcnt vmcnt(59)\nv_mfma_f32_32x32x16_bf16 v[40:55], a[112:115], v[128:131], 0\n"
"buffer_load_dwordx4 a[112:115], v10, s[8:11], s79 offen offset:2048 sc1\ns_waitcnt vmcnt(59)\nv_mfma_f32_32x32x16_bf16 v[40:55], a[116:119], v[132:135], v[40:55]\nbuffer_load_dwordx4 a[116:119], v10, s[8:11], s79 offen offset:2560 sc1\n"
"s_waitcnt vmcnt(59)\nv_mfma_f32_32x32x16_bf16 v[40:55], a[120:123], v[136:139], v[40:55]\nbuffer_load_dwordx4 a[120:123], v10, s[8:11], s79 offen offset:3072 sc1\ns_waitcnt vmcnt(59)\n"
"v_mfma_f32_32x32x16_bf16 v[40:55], a[124:127], v[140:143], v[40:55]\nbuffer_load_dwordx4 a[124:127], v10, s[8:11], s79 offen offset:3584 sc1\ns_waitcnt vmcnt(59)\nv_mfma_f32_32x32x16_bf16 v[40:55], a[128:131], v[144:147], v[40:55]\n"
"buffer_load_dwordx4 a[128:131], v11, s[8:11], s80 offen offset:0 sc1\ns_waitcnt vmcnt(59)\nv_mfma_f32_32x32x16_bf16 v[40:55], a[132:135], v[148:151], v[40:55]\nbuffer_load_dwordx4 a[132:135], v11, s[8:11], s80 offen offset:256 sc1\n"
"s_waitcnt vmcnt(59)\nv_mfma_f32_32x32x16_bf16 v[40:55], a[136:139], v[152:155], v[40:55]\nbuffer_load_dwordx4 a[136:139], v11, s[8:11], s80 offen offset:1024 sc1\ns_waitcnt vmcnt(59)\n"
"v_mfma_f32_32x32x16_bf16 v[40:55], a[140:143], v[156:159], v[40:55]\nbuffer_load_dwordx4 a[140:143], v11, s[8:11], s80 offen offset:1280 sc1\ns_waitcnt vmcnt(59)\nv_mfma_f32_32x32x16_bf16 v[40:55], a[144:147], v[160:163], v[40:55]\n"
"buffer_load_dwordx4 a[144:147], v11, s[8:11], s80 offen offset:2048 sc1\ns_waitcnt vmcnt(59)\nv_mfma_f32_32x32x16_bf16 v[40:55], a[148:151], v[164:167], v[40:55]\nbuffer_load_dwordx4 a[148:151], v11, s[8:11], s80 offen offset:2304 sc1\n"
"s_waitcnt vmcnt(59)\nv_mfma_f32_32x32x16_bf16 v[40:55], a[152:155], v[168:171], v[40:55]\nbuffer_load_dwordx4 a[152:155], v11, s[8:11], s80 offen offset:3072 sc1\ns_waitcnt vmcnt(59)\n"
"v_mfma_f32_32x32x16_bf16 v[40:55], a[156:159], v[172:175], v[40:55]\nbuffer_load_dwordx4 a[156:159], v11, s[8:11], s80 offen offset:3328 sc1\ns_waitcnt vmcnt(59)\nv_mfma_f32_32x32x16_bf16 v[40:55], a[160:163], v[176:179], v[40:55]\n"
"s_waitcnt vmcnt(58)\nv_mfma_f32_32x32x16_bf16 v[40:55], a[164:167], v[180:183], v[40:55]\ns_waitcnt vmcnt(57)\nv_mfma_f32_32x32x16_bf16 v[40:55], a[168:171], v[184:187], v[40:55]\n"
"s_waitcnt vmcnt(56)\nv_mfma_f32_32x32x16_bf16 v[40:55], a[172:175], v[188:191], v[40:55]\ns_waitcnt vmcnt(55)\nv_mfma_f32_32x32x16_bf16 v[40:55], a[176:179], v[192:195], v[40:55]\n"
"s_waitcnt vmcnt(54)\nv_mfma_f32_32x32x16_bf16 v[40:55], a[180:183], v[196:199], v[40:55]\ns_waitcnt vmcnt(53)\nv_mfma_f32_32x32x16_bf16 v[40:55], a[184:187], v[200:203], v[40:55]\n"
"s_waitcnt vmcnt(52)\nv_mfma_f32_32x32x16_bf16 v[40:55], a[188:191], v[204:207], v[40:55]\ns_waitcnt vmcnt(51)\nv_mfma_f32_32x32x16_bf16 v[40:55], a[192:195], v[208:211], v[40:55]\n"
"s_waitcnt vmcnt(50)\nv_mfma_f32_32x32x16_bf16 v[40:55], a[196:199], v[212:215], v[40:55]\ns_waitcnt vmcnt(49)\nv_mfma_f32_32x32x16_bf16 v[40:55], a[200:203], v[216:219], v[40:55]\n"
"s_waitcnt vmcnt(48)\nv_mfma_f32_32x32x16_bf16 v[40:55], a[204:207], v[220:223], v[40:55]\ns_waitcnt vmcnt(47)\nv_mfma_f32_32x32x16_bf16 v[40:55], a[208:211], v[224:227], v[40:55]\n"
"s_waitcnt vmcnt(46)\nv_mfma_f32_32x32x16_bf16 v[40:55], a[212:215], v[228:231], v[40:55]\ns_waitcnt vmcnt(45)\nv_mfma_f32_32x32x16_bf16 v[40:55], a[216:219], v[232:235], v[40:55]\n"
"s_waitcnt vmcnt(44)\nv_mfma_f32_32x32x16_bf16 v[40:55], a[220:223], v[236:239], v[40:55]\ns_waitcnt vmcnt(40)\nv_add_f32 v112, v112, v116\n"
"v_add_f32 v120, v120, v124\nv_add_f32 v113, v113, v117\nv_add_f32 v121, v121, v125\nv_add_f32 v114, v114, v118\n"
"v_add_f32 v122, v122, v126\nv_add_f32 v115, v115, v119\nv_add_f32 v123, v123, v127\nv_add_f32 v112, v112, v120\n"
"v_add_f32 v113, v113, v121\nv_add_f32 v114, v114, v122\nv_add_f32 v115, v115, v123\nv_max3_f32 v28, v112, v113, v114\n"
"v_max_f32 v28, v28, v115\nv_cndmask_b32 v40, v40, v31, s[40:41]\nv_cndmask_b32 v41, v41, v31, s[42:43]\nv_cndmask_b32 v42, v42, v31, s[44:45]\n"
"v_cndmask_b32 v43, v43, v31, s[46:47]\nv_cndmask_b32 v44, v44, v31, s[48:49]\nv_cndmask_b32 v45, v45, v31, s[50:51]\nv_cndmask_b32 v46, v46, v31, s[52:53]\n"
"v_cndmask_b32 v47, v47, v31, s[54:55]\nv_cndmask_b32 v48, v48, v31, s[56:57]\nv_cndmask_b32 v49, v49, v31, s[58:59]\nv_cndmask_b32 v50, v50, v31, s[60:61]\n"
"v_cndmask_b32 v51, v51, v31, s[62:63]\nv_cndmask_b32 v52, v52, v31, s[64:65]\nv_cndmask_b32 v53, v53, v31, s[66:67]\nv_cndmask_b32 v54, v54, v31, s[68:69]\n"
"v_cndmask_b32 v55, v55, v31, s[70:71]\nv_max3_f32 v28, v28, v40, v41\nv_max3_f32 v28, v28, v42, v43\nv_max3_f32 v28, v28, v44, v45\n"
"v_max3_f32 v28, v28, v46, v47\nv_max3_f32 v28, v28, v48, v49\nv_max3_f32 v28, v28, v50, v51\nv_max3_f32 v28, v28, v52, v53\n"
"v_max3_f32 v28, v28, v54, v55\nv_mov_b32 v30, v28\ns_nop 1\nv_permlane32_swap_b32 v28, v30\n"
"v_max_f32 v28, v28, v30\nv_sub_f32 v40, v40, v28\nv_sub_f32 v41, v41, v28\nv_sub_f32 v42, v42, v28\n"
"v_sub_f32 v43, v43, v28\nv_sub_f32 v44, v44, v28\nv_sub_f32 v45, v45, v28\nv_sub_f32 v46, v46, v28\n"
"v_sub_f32 v47, v47, v28\nv_sub_f32 v48, v48, v28\nv_sub_f32 v49, v49, v28\nv_sub_f32 v50, v50, v28\n"
"v_sub_f32 v51, v51, v28\nv_sub_f32 v52, v52, v28\nv_sub_f32 v53, v53, v28\nv_sub_f32 v54, v54, v28\n"
"v_sub_f32 v55, v55, v28\nv_sub_f32 v112, v112, v28\nv_sub_f32 v113, v113, v28\nv_sub_f32 v114, v114, v28\n"
"v_sub_f32 v115, v115, v28\nv_exp_f32 v40, v40\nv_exp_f32 v41, v41\nv_exp_f32 v42, v42\n"
"v_exp_f32 v43, v43\nv_exp_f32 v44, v44\nv_exp_f32 v45, v45\nv_exp_f32 v46, v46\n"
"v_exp_f32 v47, v47\nv_exp_f32 v48, v48\nv_exp_f32 v49, v49\nv_exp_f32 v50, v50\n"
"v_exp_f32 v51, v51\nv_exp_f32 v52, v52\nv_exp_f32 v53, v53\nv_exp_f32 v54, v54\n"
"v_exp_f32 v55, v55\nv_exp_f32 v112, v112\nv_exp_f32 v113, v113\nv_exp_f32 v114, v114\n"
"v_exp_f32 v115, v115\nv_add_f32 v29, v40, v41\nv_add_f32 v30, v42, v43\nv_add_f32 v29, v29, v44\n"
"v_add_f32 v30, v30, v45\nv_add_f32 v29, v29, v46\nv_add_f32 v30, v30, v47\nv_add_f32 v29, v29, v48\n"
"v_add_f32 v30, v30, v49\nv_add_f32 v29, v29, v50\nv_add_f32 v30, v30, v51\nv_add_f32 v29, v29, v52\n"
"v_add_f32 v30, v30, v53\nv_add_f32 v29, v29, v54\nv_add_f32 v30, v30, v55\nv_add_f32 v29, v29, v112\n"
"v_add_f32 v30, v30, v113\nv_add_f32 v29, v29, v114\nv_add_f32 v30, v30, v115\nv_add_f32 v29, v29, v30\n"
"v_mov_b32 v30, v29\ns_nop 1\nv_permlane32_swap_b32 v29, v30\nv_add_f32 v29, v29, v30\n"
"v_rcp_f32 v29, v29\ns_nop 0\nv_mul_f32 v40, v40, v29\nv_mul_f32 v41, v41, v29\n"
"v_mul_f32 v42, v42, v29\nv_mul_f32 v43, v43, v29\nv_mul_f32 v44, v44, v29\nv_mul_f32 v45, v45, v29\n"
"v_mul_f32 v46, v46, v29\nv_mul_f32 v47, v47, v29\nv_mul_f32 v48, v48, v29\nv_mul_f32 v49, v49, v29\n"
"v_mul_f32 v50, v50, v29\nv_mul_f32 v51, v51, v29\nv_mul_f32 v52, v52, v29\nv_mul_f32 v53, v53, v29\n"
"v_mul_f32 v54, v54, v29\nv_mul_f32 v55, v55, v29\nv_mul_f32 v112, v112, v29\nv_mul_f32 v113, v113, v29\n"
"v_mul_f32 v114, v114, v29\nv_mul_f32 v115, v115, v29\nv_cvt_pk_bf16_f32 v56, v40, v41\nv_cvt_pk_bf16_f32 v57, v42, v43\n"
"v_cvt_pk_bf16_f32 v58, v44, v45\nv_cvt_pk_bf16_f32 v59, v46, v47\nv_cvt_pk_bf16_f32 v60, v48, v49\nv_cvt_pk_bf16_f32 v61, v50, v51\n"
"v_cvt_pk_bf16_f32 v62, v52, v53\nv_cvt_pk_bf16_f32 v63, v54, v55\nv_cvt_pk_bf16_f32 v35, v112, v113\nv_cvt_pk_bf16_f32 v39, v114, v115\n"
"v_xor_b32 v30, 6144, v14\nds_write_b128 v30, v[56:59]\nds_write_b128 v30, v[60:63] offset:1024\nds_write_b128 v15, v[32:35]\n"
"ds_write_b128 v15, v[36:39] offset:1024\ns_waitcnt lgkmcnt(0)\ns_barrier\nds_read_b128 v[96:99], v16 offset:0\n"
"ds_read_b128 v[100:103], v16 offset:1024\nds_read_b128 v[104:107], v16 offset:2048\nds_read_b128 v[108:111], v16 offset:3072\nds_read_b128 v[112:115], v16 offset:4096\n"
"ds_read_b128 v[116:119], v16 offset:5120\nds_read_b128 v[120:123], v16 offset:6144\nds_read_b128 v[124:127], v16 offset:7168\nds_read_b128 v[128:131], v16 offset:8192\n"
"ds_read_b128 v[132:135], v16 offset:9216\nds_read_b128 v[136:139], v16 offset:10240\nds_read_b128 v[140:143], v16 offset:11264\ns_waitcnt lgkmcnt(0)\n"
"ds_read_b128 v[144:147], v16 offset:12288\nds_read_b128 v[148:151], v16 offset:13312\nds_read_b128 v[152:155], v16 offset:14336\nds_read_b128 v[156:159], v16 offset:15360\n"
"ds_read_b128 v[160:163], v16 offset:16384\nds_read_b128 v[164:167], v16 offset:17408\nds_read_b128 v[168:171], v16 offset:18432\nds_read_b128 v[172:175], v16 offset:19456\n"
"ds_read_b128 v[176:179], v16 offset:20480\nds_read_b128 v[180:183], v16 offset:21504\nds_read_b128 v[184:187], v16 offset:22528\nds_read_b128 v[188:191], v16 offset:23552\n"
"s_waitcnt lgkmcnt(0)\nds_read_b128 v[192:195], v16 offset:24576\nds_read_b128 v[196:199], v16 offset:25600\nds_read_b128 v[200:203], v16 offset:26624\n"
"ds_read_b128 v[204:207], v16 offset:27648\nds_read_b128 v[208:211], v16 offset:28672\nds_read_b128 v[212:215], v16 offset:29696\nds_read_b128 v[216:219], v16 offset:30720\n"
"ds_read_b128 v[220:223], v16 offset:31744\nds_read_b128 v[224:227], v16 offset:32768\nds_read_b128 v[228:231], v16 offset:33792\nds_read_b128 v[232:235], v16 offset:34816\n"
"ds_read_b128 v[236:239], v16 offset:35840\ns_waitcnt lgkmcnt(0)\nds_read_b128 v[240:243], v16 offset:36864\nds_read_b128 v[244:247], v16 offset:37888\n"
"ds_read_b128 v[248:251], v16 offset:38912\nds_read_b128 v[252:255], v16 offset:39936\nv_mov_b32 v24, v17\ns_sub_u32 s72, s17, 1\n"
"s_waitcnt lgkmcnt(0)\nfo_loop:\ns_add_u32 s76, s76, 8388608\ns_add_u32 s77, s77, 8388608\n"
"s_add_u32 s78, s78, 8388608\ns_add_u32 s79, s79, 8388608\ns_add_u32 s80, s80, 2097152\ns_waitcnt vmcnt(39)\n"
"v_mfma_f32_32x32x16_bf16 v[40:55], a[0:3], v[96:99], 0\nbuffer_load_dwordx4 a[0:3], v10, s[8:11], s76 offen offset:0 sc1\ns_waitcnt vmcnt(39)\nv_mfma_f32_32x32x16_bf16 v[40:55], a[4:7], v[100:103], v[40:55]\n"
"buffer_load_dwordx4 a[4:7], v10, s[8:11], s76 offen offset:512 sc1\ns_waitcnt vmcnt(39)\nv_mfma_f32_32x32x16_bf16 v[40:55], a[8:11], v[104:107], v[40:55]\nbuffer_load_dwordx4 a[8:11], v10, s[8:11], s76 offen offset:1024 sc1\n"
"s_waitcnt vmcnt(39)\nv_mfma_f32_32x32x16_bf16 v[40:55], a[12:15], v[108:111], v[40:55]\nbuffer_load_dwordx4 a[12:15], v10, s[8:11], s76 offen offset:1536 sc1\ns_waitcnt vmcnt(39)\n"
"v_mfma_f32_32x32x16_bf16 v[40:55], a[16:19], v[112:115], v[40:55]\nbuffer_load_dwordx4 a[16:19], v10, s[8:11], s76 offen offset:2048 sc1\ns_waitcnt vmcnt(39)\nv_mfma_f32_32x32x16_bf16 v[40:55], a[20:23], v[116:119], v[40:55]\n"
"buffer_load_dwordx4 a[20:23], v10, s[8:11], s76 offen offset:2560 sc1\ns_waitcnt vmcnt(39)\nv_mfma_f32_32x32x16_bf16 v[40:55], a[24:27], v[120:123], v[40:55]\nbuffer_load_dwordx4 a[24:27], v10, s[8:11], s76 offen offset:3072 sc1\n"
"s_waitcnt vmcnt(39)\nv_mfma_f32_32x32x16_bf16 v[40:55], a[28:31], v[124:127], v[40:55]\nbuffer_load_dwordx4 a[28:31], v10, s[8:11], s76 offen offset:3584 sc1\ns_waitcnt vmcnt(39)\n"
"v_mfma_f32_32x32x16_bf16 v[40:55], a[32:35], v[128:131], v[40:55]\nbuffer_load_dwordx4 a[32:35], v10, s[8:11], s77 offen offset:0 sc1\ns_waitcnt vmcnt(39)\nv_mfma_f32_32x32x16_bf16 v[40:55], a[36:39], v[132:135], v[40:55]\n"
"buffer_load_dwordx4 a[36:39], v10, s[8:11], s77 offen offset:512 sc1\ns_waitcnt vmcnt(39)\nv_mfma_f32_32x32x16_bf16 v[40:55], a[40:43], v[136:139], v[40:55]\nbuffer_load_dwordx4 a[40:43], v10, s[8:11], s77 offen offset:1024 sc1\n"
"s_waitcnt vmcnt(39)\nv_mfma_f32_32x32x16_bf16 v[40:55], a[44:47], v[140:143], v[40:55]\nbuffer_load_dwordx4 a[44:47], v10, s[8:11], s77 offen offset:1536 sc1\ns_waitcnt vmcnt(39)\n"
"v_mfma_f32_32x32x16_bf16 v[40:55], a[48:51], v[144:147], v[40:55]\nbuffer_load_dwordx4 a[48:51], v10, s[8:11], s77 offen offset:2048 sc1\ns_waitcnt vmcnt(39)\nv_mfma_f32_32x32x16_bf16 v[40:55], a[52:55], v[148:151], v[40:55]\n"
"buffer_load_dwordx4 a[52:55], v10, s[8:11], s77 offen offset:2560 sc1\ns_waitcnt vmcnt(39)\nv_mfma_f32_32x32x16_bf16 v[40:55], a[56:59], v[152:155], v[40:55]\nbuffer_load_dwordx4 a[56:59], v10, s[8:11], s77 offen offset:3072 sc1\n"
"s_waitcnt vmcnt(39)\nv_mfma_f32_32x32x16_bf16 v[40:55], a[60:63], v[156:159], v[40:55]\nbuffer_load_dwordx4 a[60:63], v10, s[8:11], s77 offen offset:3584 sc1\ns_waitcnt vmcnt(39)\n"
"v_mfma_f32_32x32x16_bf16 v[40:55], a[64:67], v[160:163], v[40:55]\nbuffer_load_dwordx4 a[64:67], v10, s[8:11], s78 offen offset:0 sc1\ns_waitcnt vmcnt(39)\nv_mfma_f32_32x32x16_bf16 v[40:55], a[68:71], v[164:167], v[40:55]\n"
"buffer_load_dwordx4 a[68:71], v10, s[8:11], s78 offen offset:512 sc1\ns_waitcnt vmcnt(39)\nv_mfma_f32_32x32x16_bf16 v[40:55], a[72:75], v[168:171], v[40:55]\nbuffer_load_dwordx4 a[72:75], v10, s[8:11], s78 offen offset:1024 sc1\n"
"s_waitcnt vmcnt(39)\nv_mfma_f32_32x32x16_bf16 v[40:55], a[76:79], v[172:175], v[40:55]\nbuffer_load_dwordx4 a[76:79], v10, s[8:11], s78 offen offset:1536 sc1\ns_waitcnt vmcnt(39)\n"
"v_mfma_f32_32x32x16_bf16 v[40:55], a[80:83], v[176:179], v[40:55]\nbuffer_load_dwordx4 a[80:83], v10, s[8:11], s78 offen offset:2048 sc1\ns_waitcnt vmcnt(39)\nv_mfma_f32_32x32x16_bf16 v[40:55], a[84:87], v[180:183], v[40:55]\n"
"buffer_load_dwordx4 a[84:87], v10, s[8:11], s78 offen offset:2560 sc1\ns_waitcnt vmcnt(39)\nv_mfma_f32_32x32x16_bf16 v[40:55], a[88:91], v[184:187], v[40:55]\nbuffer_load_dwordx4 a[88:91], v10, s[8:11], s78 offen offset:3072 sc1\n"
"s_waitcnt vmcnt(39)\nv_mfma_f32_32x32x16_bf16 v[40:55], a[92:95], v[188:191], v[40:55]\nbuffer_load_dwordx4 a[92:95], v10, s[8:11], s78 offen offset:3584 sc1\ns_waitcnt vmcnt(39)\n"
"v_mfma_f32_32x32x16_bf16 v[40:55], a[96:99], v[192:195], v[40:55]\nbuffer_load_dwordx4 a[96:99], v10, s[8:11], s79 offen offset:0 sc1\ns_waitcnt vmcnt(39)\nv_mfma_f32_32x32x16_bf16 v[40:55], a[100:103], v[196:199], v[40:55]\n"
"buffer_load_dwordx4 a[100:103], v10, s[8:11], s79 offen offset:512 sc1\ns_waitcnt vmcnt(39)\nv_mfma_f32_32x32x16_bf16 v[40:55], a[104:107], v[200:203], v[40:55]\nbuffer_load_dwordx4 a[104:107], v10, s[8:11], s79 offen offset:1024 sc1\n"
"s_waitcnt vmcnt(39)\nv_mfma_f32_32x32x16_bf16 v[40:55], a[108:111], v[204:207], v[40:55]\nbuffer_load_dwordx4 a[108:111], v10, s[8:11], s79 offen offset:1536 sc1\ns_waitcnt vmcnt(39)\n"
"v_mfma_f32_32x32x16_bf16 v[40:55], a[112:115], v[208:211], v[40:55]\nbuffer_load_dwordx4 a[112:115], v10, s[8:11], s79 offen offset:2048 sc1\ns_waitcnt vmcnt(39)\nv_mfma_f32_32x32x16_bf16 v[40:55], a[116:119], v[212:215], v[40:55]\n"
"buffer_load_dwordx4 a[116:119], v10, s[8:11], s79 offen offset:2560 sc1\ns_waitcnt vmcnt(39)\nv_mfma_f32_32x32x16_bf16 v[40:55], a[120:123], v[216:219], v[40:55]\nbuffer_load_dwordx4 a[120:123], v10, s[8:11], s79 offen offset:3072 sc1\n"
"s_waitcnt vmcnt(39)\nv_mfma_f32_32x32x16_bf16 v[40:55], a[124:127], v[220:223], v[40:55]\nbuffer_load_dwordx4 a[124:127], v10, s[8:11], s79 offen offset:3584 sc1\ns_waitcnt vmcnt(39)\n"
"v_mfma_f32_32x32x16_bf16 v[40:55], a[128:131], v[224:227], v[40:55]\nbuffer_load_dwordx4 a[128:131], v11, s[8:11], s80 offen offset:0 sc1\ns_waitcnt vmcnt(39)\nv_mfma_f32_32x32x16_bf16 v[40:55], a[132:135], v[228:231], v[40:55]\n"
"buffer_load_dwordx4 a[132:135], v11, s[8:11], s80 offen offset:256 sc1\ns_waitcnt vmcnt(39)\nv_mfma_f32_32x32x16_bf16 v[40:55], a[136:139], v[232:235], v[40:55]\nbuffer_load_dwordx4 a[136:139], v11, s[8:11], s80 offen offset:1024 sc1\n"
"s_waitcnt vmcnt(39)\nv_mfma_f32_32x32x16_bf16 v[40:55], a[140:143], v[236:239], v[40:55]\nbuffer_load_dwordx4 a[140:143], v11, s[8:11], s80 offen offset:1280 sc1\ns_waitcnt vmcnt(39)\n"
"v_mfma_f32_32x32x16_bf16 v[40:55], a[144:147], v[240:243], v[40:55]\nbuffer_load_dwordx4 a[144:147], v11, s[8:11], s80 offen offset:2048 sc1\ns_waitcnt vmcnt(39)\nv_mfma_f32_32x32x16_bf16 v[40:55], a[148:151], v[244:247], v[40:55]\n"
"buffer_load_dwordx4 a[148:151], v11, s[8:11], s80 offen offset:2304 sc1\ns_waitcnt vmcnt(39)\nv_mfma_f32_32x32x16_bf16 v[40:55], a[152:155], v[248:251], v[40:55]\nbuffer_load_dwordx4 a[152:155], v11, s[8:11], s80 offen offset:3072 sc1\n"
"s_waitcnt vmcnt(39)\nv_mfma_f32_32x32x16_bf16 v[40:55], a[156:159], v[252:255], v[40:55]\nbuffer_load_dwordx4 a[156:159], v11, s[8:11], s80 offen offset:3328 sc1\ns_nop 11\n"
"v_cvt_pk_bf16_f32 v56, v40, v41\nv_cvt_pk_bf16_f32 v57, v42, v43\nv_cvt_pk_bf16_f32 v58, v44, v45\nv_cvt_pk_bf16_f32 v59, v46, v47\n"
"v_cvt_pk_bf16_f32 v60, v48, v49\nv_cvt_pk_bf16_f32 v61, v50, v51\nv_cvt_pk_bf16_f32 v62, v52, v53\nv_cvt_pk_bf16_f32 v63, v54, v55\n"
"ds_write_b128 v24, v[56:59]\nds_write_b128 v24, v[60:63] offset:32\nv_add_u32 v24, 256, v24\ns_sub_u32 s72, s72, 1\n"
"s_cmp_lg_u32 s72, 0\ns_cbranch_scc1 fo_loop\ns_waitcnt vmcnt(39)\nv_mfma_f32_32x32x16_bf16 v[40:55], a[0:3], v[96:99], 0\n"
"s_waitcnt vmcnt(38)\nv_mfma_f32_32x32x16_bf16 v[40:55], a[4:7], v[100:103], v[40:55]\ns_waitcnt vmcnt(37)\nv_mfma_f32_32x32x16_bf16 v[40:55], a[8:11], v[104:107], v[40:55]\n"
"s_waitcnt vmcnt(36)\nv_mfma_f32_32x32x16_bf16 v[40:55], a[12:15], v[108:111], v[40:55]\ns_waitcnt vmcnt(35)\nv_mfma_f32_32x32x16_bf16 v[40:55], a[16:19], v[112:115], v[40:55]\n"
"s_waitcnt vmcnt(34)\nv_mfma_f32_32x32x16_bf16 v[40:55], a[20:23], v[116:119], v[40:55]\ns_waitcnt vmcnt(33)\nv_mfma_f32_32x32x16_bf16 v[40:55], a[24:27], v[120:123], v[40:55]\n"
"s_waitcnt vmcnt(32)\nv_mfma_f32_32x32x16_bf16 v[40:55], a[28:31], v[124:127], v[40:55]\ns_waitcnt vmcnt(31)\nv_mfma_f32_32x32x16_bf16 v[40:55], a[32:35], v[128:131], v[40:55]\n"
"s_waitcnt vmcnt(30)\nv_mfma_f32_32x32x16_bf16 v[40:55], a[36:39], v[132:135], v[40:55]\ns_waitcnt vmcnt(29)\nv_mfma_f32_32x32x16_bf16 v[40:55], a[40:43], v[136:139], v[40:55]\n"
"s_waitcnt vmcnt(28)\nv_mfma_f32_32x32x16_bf16 v[40:55], a[44:47], v[140:143], v[40:55]\ns_waitcnt vmcnt(27)\nv_mfma_f32_32x32x16_bf16 v[40:55], a[48:51], v[144:147], v[40:55]\n"
"s_waitcnt vmcnt(26)\nv_mfma_f32_32x32x16_bf16 v[40:55], a[52:55], v[148:151], v[40:55]\ns_waitcnt vmcnt(25)\nv_mfma_f32_32x32x16_bf16 v[40:55], a[56:59], v[152:155], v[40:55]\n"
"s_waitcnt vmcnt(24)\nv_mfma_f32_32x32x16_bf16 v[40:55], a[60:63], v[156:159], v[40:55]\ns_waitcnt vmcnt(23)\nv_mfma_f32_32x32x16_bf16 v[40:55], a[64:67], v[160:163], v[40:55]\n"
"s_waitcnt vmcnt(22)\nv_mfma_f32_32x32x16_bf16 v[40:55], a[68:71], v[164:167], v[40:55]\ns_waitcnt vmcnt(21)\nv_mfma_f32_32x32x16_bf16 v[40:55], a[72:75], v[168:171], v[40:55]\n"
"s_waitcnt vmcnt(20)\nv_mfma_f32_32x32x16_bf16 v[40:55], a[76:79], v[172:175], v[40:55]\ns_waitcnt vmcnt(19)\nv_mfma_f32_32x32x16_bf16 v[40:55], a[80:83], v[176:179], v[40:55]\n"
"s_waitcnt vmcnt(18)\nv_mfma_f32_32x32x16_bf16 v[40:55], a[84:87], v[180:183], v[40:55]\ns_waitcnt vmcnt(17)\nv_mfma_f32_32x32x16_bf16 v[40:55], a[88:91], v[184:187], v[40:55]\n"
"s_waitcnt vmcnt(16)\nv_mfma_f32_32x32x16_bf16 v[40:55], a[92:95], v[188:191], v[40:55]\ns_waitcnt vmcnt(15)\nv_mfma_f32_32x32x16_bf16 v[40:55], a[96:99], v[192:195], v[40:55]\n"
"s_waitcnt vmcnt(14)\nv_mfma_f32_32x32x16_bf16 v[40:55], a[100:103], v[196:199], v[40:55]\ns_waitcnt vmcnt(13)\nv_mfma_f32_32x32x16_bf16 v[40:55], a[104:107], v[200:203], v[40:55]\n"
"s_waitcnt vmcnt(12)\nv_mfma_f32_32x32x16_bf16 v[40:55], a[108:111], v[204:207], v[40:55]\ns_waitcnt vmcnt(11)\nv_mfma_f32_32x32x16_bf16 v[40:55], a[112:115], v[208:211], v[40:55]\n"
"s_waitcnt vmcnt(10)\nv_mfma_f32_32x32x16_bf16 v[40:55], a[116:119], v[212:215], v[40:55]\ns_waitcnt vmcnt(9)\nv_mfma_f32_32x32x16_bf16 v[40:55], a[120:123], v[216:219], v[40:55]\n"
"s_waitcnt vmcnt(8)\nv_mfma_f32_32x32x16_bf16 v[40:55], a[124:127], v[220:223], v[40:55]\ns_waitcnt vmcnt(7)\nv_mfma_f32_32x32x16_bf16 v[40:55], a[128:131], v[224:227], v[40:55]\n"
"s_waitcnt vmcnt(6)\nv_mfma_f32_32x32x16_bf16 v[40:55], a[132:135], v[228:231], v[40:55]\ns_waitcnt vmcnt(5)\nv_mfma_f32_32x32x16_bf16 v[40:55], a[136:139], v[232:235], v[40:55]\n"
"s_waitcnt vmcnt(4)\nv_mfma_f32_32x32x16_bf16 v[40:55], a[140:143], v[236:239], v[40:55]\ns_waitcnt vmcnt(3)\nv_mfma_f32_32x32x16_bf16 v[40:55], a[144:147], v[240:243], v[40:55]\n"
"s_waitcnt vmcnt(2)\nv_mfma_f32_32x32x16_bf16 v[40:55], a[148:151], v[244:247], v[40:55]\ns_waitcnt vmcnt(1)\nv_mfma_f32_32x32x16_bf16 v[40:55], a[152:155], v[248:251], v[40:55]\n"
"s_waitcnt vmcnt(0)\nv_mfma_f32_32x32x16_bf16 v[40:55], a[156:159], v[252:255], v[40:55]\ns_nop 11\nv_cvt_pk_bf16_f32 v56, v40, v41\n"
"v_cvt_pk_bf16_f32 v57, v42, v43\nv_cvt_pk_bf16_f32 v58, v44, v45\nv_cvt_pk_bf16_f32 v59, v46, v47\nv_cvt_pk_bf16_f32 v60, v48, v49\n"
"v_cvt_pk_bf16_f32 v61, v50, v51\nv_cvt_pk_bf16_f32 v62, v52, v53\nv_cvt_pk_bf16_f32 v63, v54, v55\nds_write_b128 v24, v[56:59]\n"
"ds_write_b128 v24, v[60:63] offset:32\nv_add_u32 v24, 256, v24\n"
    :: "{s[8:23]}"(s0), "{v[8:23]}"(v0) : "v24", "v25", "v26", "v27", "v28", "v29", "v30", "v31", "v32", "v33", "v34", "v35", "v36", "v37", "v38", "v39", "v40", "v41", "v42", "v43", "v44", "v45", "v46", "v47", "v48", "v49", "v50", "v51", "v52", "v53", "v54", "v55", "v56", "v57", "v58", "v59", "v60", "v61", "v62", "v63", "v64", "v65", "v66", "v67", "v68", "v69", "v70", "v71", "v72", "v73", "v74", "v75", "v76", "v77", "v78", "v79", "v80", "v81", "v82", "v83", "v84", "v85", "v86", "v87", "v88", "v89", "v90", "v91", "v92", "v93", "v94", "v95", "v96", "v97", "v98", "v99", "v100", "v101", "v102", "v103", "v104", "v105", "v106", "v107", "v108", "v109", "v110", "v111", "v112", "v113", "v114", "v115", "v116", "v117", "v118", "v119", "v120", "v121", "v122", "v123", "v124", "v125", "v126", "v127", "v128", "v129", "v130", "v131", "v132", "v133", "v134", "v135", "v136", "v137", "v138", "v139", "v140", "v141", "v142", "v143", "v144", "v145", "v146", "v147", "v148", "v149", "v150", "v151", "v152", "v153", "v154", "v155", "v156", "v157", "v158", "v159", "v160", "v161", "v162", "v163", "v164", "v165", "v166", "v167", "v168", "v169", "v170", "v171", "v172", "v173", "v174", "v175", "v176", "v177", "v178", "v179", "v180", "v181", "v182", "v183", "v184", "v185", "v186", "v187", "v188", "v189", "v190", "v191", "v192", "v193", "v194", "v195", "v196", "v197", "v198", "v199", "v200", "v201", "v202", "v203", "v204", "v205", "v206", "v207", "v208", "v209", "v210", "v211", "v212", "v213", "v214", "v215", "v216", "v217", "v218", "v219", "v220", "v221", "v222", "v223", "v224", "v225", "v226", "v227", "v228", "v229", "v230", "v231", "v232", "v233", "v234", "v235", "v236", "v237", "v238", "v239", "v240", "v241", "v242", "v243", "v244", "v245", "v246", "v247", "v248", "v249", "v250", "v251", "v252", "v253", "v254", "v255", "a0", "a1", "a2", "a3", "a4", "a5", "a6", "a7", "a8", "a9", "a10", "a11", "a12", "a13", "a14", "a15", "a16", "a17", "a18", "a19", "a20", "a21", "a22", "a23", "a24", "a25", "a26", "a27", "a28", "a29", "a30", "a31", "a32", "a33", "a34", "a35", "a36", "a37", "a38", "a39", "a40", "a41", "a42", "a43", "a44", "a45", "a46", "a47", "a48", "a49", "a50", "a51", "a52", "a53", "a54", "a55", "a56", "a57", "a58", "a59", "a60", "a61", "a62", "a63", "a64", "a65", "a66", "a67", "a68", "a69", "a70", "a71", "a72", "a73", "a74", "a75", "a76", "a77", "a78", "a79", "a80", "a81", "a82", "a83", "a84", "a85", "a86", "a87", "a88", "a89", "a90", "a91", "a92", "a93", "a94", "a95", "a96", "a97", "a98", "a99", "a100", "a101", "a102", "a103", "a104", "a105", "a106", "a107", "a108", "a109", "a110", "a111", "a112", "a113", "a114", "a115", "a116", "a117", "a118", "a119", "a120", "a121", "a122", "a123", "a124", "a125", "a126", "a127", "a128", "a129", "a130", "a131", "a132", "a133", "a134", "a135", "a136", "a137", "a138", "a139", "a140", "a141", "a142", "a143", "a144", "a145", "a146", "a147", "a148", "a149", "a150", "a151", "a152", "a153", "a154", "a155", "a156", "a157", "a158", "a159", "a160", "a161", "a162", "a163", "a164", "a165", "a166", "a167", "a168", "a169", "a170", "a171", "a172", "a173", "a174", "a175", "a176", "a177", "a178", "a179", "a180", "a181", "a182", "a183", "a184", "a185", "a186", "a187", "a188", "a189", "a190", "a191", "a192", "a193", "a194", "a195", "a196", "a197", "a198", "a199", "a200", "a201", "a202", "a203", "a204", "a205", "a206", "a207", "a208", "a209", "a210", "a211", "a212", "a213", "a214", "a215", "a216", "a217", "a218", "a219", "a220", "a221", "a222", "a223", "s40", "s41", "s42", "s43", "s44", "s45", "s46", "s47", "s48", "s49", "s50", "s51", "s52", "s53", "s54", "s55", "s56", "s57", "s58", "s59", "s60", "s61", "s62", "s63", "s64", "s65", "s66", "s67", "s68", "s69", "s70", "s71", "s72", "s73", "s74", "s75", "s76", "s77", "s78", "s79", "s80", "s81", "vcc", "scc", "memory");
}
DI u4 pack8(const f16v& x, int s, float sc) { return (u4){pk2(x[8 * s] * sc, x[8 * s + 1] * sc), pk2(x[8 * s + 2] * sc, x[8 * s + 3] * sc), pk2(x[8 * s + 4] * sc, x[8 * s + 5] * sc), pk2(x[8 * s + 6] * sc, x[8 * s + 7] * sc)}; }
DI u4 pack8(const f16v& x, int s) { return (u4){pk2(x[8 * s], x[8 * s + 1]), pk2(x[8 * s + 2], x[8 * s + 3]), pk2(x[8 * s + 4], x[8 * s + 5]), pk2(x[8 * s + 6], x[8 * s + 7])}; }
DI void dma_pair(u4 srd, int v0, int v1, int soff, unsigned m) {
    asm volatile("s_mov_b32 m0, %4\n\ts_nop 0\n\tbuffer_load_dwordx4 %0, %2, %3 offen lds\n\tbuffer_load_dwordx4 %1, %2, %3 offen offset:1024 lds" :: "v"(v0), "v"(v1), "s"(srd), "s"(soff), "s"(m) : "memory");
}
DI void dma_one(u4 srd, int v0, int soff, unsigned m) {
    asm volatile("s_mov_b32 m0, %3\n\ts_nop 0\n\tbuffer_load_dwordx4 %0, %1, %2 offen lds" :: "v"(v0), "s"(srd), "s"(soff), "s"(m) : "memory");
}
DI void mV_va(f16v& c, u4 x, u4 y) { asm volatile("v_mfma_f32_32x32x16_bf16 %0, %1, %2, %0" : "+v"(c) : "v"(x), "a"(y)); }
DI void mV_va0(f16v& c, u4 x, u4 y) { asm volatile("v_mfma_f32_32x32x16_bf16 %0, %1, %2, 0" : "=&v"(c) : "v"(x), "a"(y)); }
DI void mV_av(f16v& c, u4 x, u4 y) { asm volatile("v_mfma_f32_32x32x16_bf16 %0, %1, %2, %0" : "+v"(c) : "a"(x), "v"(y)); }
DI void mV_av0(f16v& c, u4 x, u4 y) { asm volatile("v_mfma_f32_32x32x16_bf16 %0, %1, %2, 0" : "=&v"(c) : "a"(x), "v"(y)); }
DI void mV_vv(f16v& c, u4 x, u4 y) { asm volatile("v_mfma_f32_32x32x16_bf16 %0, %1, %2, %0" : "+v"(c) : "v"(x), "v"(y)); }
DI void mV_vv0(f16v& c, u4 x, u4 y) { asm volatile("v_mfma_f32_32x32x16_bf16 %0, %1, %2, 0" : "=&v"(c) : "v"(x), "v"(y)); }
template <int N, int PD, class RD, class MM, class EX> DI void pipe(RD rd, MM mm, EX ex) {
    u4 f[PD];
#pragma unroll
    for (int j = 0; j < PD; ++j) f[j] = rd(j);
    SB();
#pragma unroll
    for (int j = 0; j < N; ++j) { mm(j, f[j % PD]); if (j + PD < N) f[j % PD] = rd(j + PD); ex(j); SB(); }
}
DI int swzr(int row) { return ((row >> 2) & 1) | ((row & 3) << 1) | (row & 8); }
DI int swap23(int x) { return (x & ~12) | ((x & 4) << 1) | ((x & 8) >> 1); }
#define SLICE 32768
#define MFW32(x, y) asm volatile("s_nop 11" : "+v"(x), "+v"(y))
struct SideCtx {
    char LAS* sm; unsigned smb; int lane, wave;
    int vr0, vr1, vc0, vc1;
};
DI SideCtx make_ctx(int wave) {
    SideCtx c; c.sm = (char LAS*)smem_; c.smb = (unsigned)(size_t)c.sm; c.lane = fresh_lane(); c.wave = wave;
    asm volatile("" : "+s"(c.wave), "+s"(c.smb));
    const int l = c.lane;
    { int row = 4 * c.wave + (l >> 5); c.vr0 = row * 8192 + (((l & 31) ^ swzr(row)) * 16); row += 2; c.vr1 = row * 8192 + (((l & 31) ^ swzr(row)) * 16) - 1024; }
    { int row = l >> 3; c.vc0 = row * 896 + (((l & 7) ^ (((row >> 1) & 1) << 2)) * 16); c.vc1 = c.vc0 + 8 * 896 - 1024; }
    return c;
}
DI void dma_rows_q(const SideCtx& c, u4 srd, int h, int r0, int slot, int g) { dma_pair(srd, c.vr0, c.vr1, (r0 + 16 * g) * 8192 + h * 512, c.smb + slot * SLICE + (8 * g + 2 * c.wave) * 1024); }
DI void dma_cols_q(const SideCtx& c, u4 srd, int h, int c0, int slot, int g) { const int k = 8 * c.wave + 2 * g; dma_pair(srd, c.vc0, c.vc1, (h * 256 + 8 * k) * 896 + c0 * 2, c.smb + slot * SLICE + k * 1024); }
DI void dma_rows(const SideCtx& c, u4 srd, int h, int r0, int slot) {
#pragma unroll
    for (int g = 0; g < 4; ++g) dma_rows_q(c, srd, h, r0, slot, g);
}
DI void dma_cols(const SideCtx& c, u4 srd, int h, int c0, int slot) {
#pragma unroll
    for (int g = 0; g < 4; ++g) dma_cols_q(c, srd, h, c0, slot, g);
}
DI void set_flag(const SideCtx& c, u4 rhb, int off, unsigned tag) {
    waitvm<0>(); bar();
    if (c.wave == 0) sst32(tag, rhb, c.lane == 0 ? off : OOB, 0);
}

DI void slp(int n) {
#pragma nounroll
    for (int i = 0; i < n; ++i) asm volatile("s_sleep 1");
}
DI int xsrc(int wave, int lane, int k) { const int n = 64 * (7 * (wave & 1) + k) + lane, row = (n * 1171) >> 16, pos = n - 56 * row; return (row + 16 * (wave >> 1)) * 896 + ((pos ^ ((row >> 1) & 7)) * 16); }
DI int xrd(int row, int hi, int k) { return row * 896 + (((2 * k + hi) ^ ((row >> 1) & 7)) * 16); }
#define WSL(u) ((u) + 1)

DI int xswz(int type, int row) { return type ? ((row >> 1) & 1) | (((row >> 3) & 3) << 1) : (row >> 1) & 7; }
DI int xsrc8(int type, int wave, int lane) { const int row = 8 * wave + (lane >> 3); return row * 896 + (((lane & 7) ^ xswz(type, row)) * 16); }
template <int PART>
DI void gemm_first(int wave, int half, int type, int h, int sub) {
    CArgs& a = get_args();
    const SideCtx c = make_ctx(wave);
    h = __builtin_amdgcn_readfirstlane(h); sub = __builtin_amdgcn_readfirstlane(sub); half = __builtin_amdgcn_readfirstlane(half); asm volatile("" : "+s"(h), "+s"(sub), "+s"(half));
    if (PART == 0) {
        const u4 rw2 = mksrd(type ? a.Wo : a.Wq, 448 * 8192);
        const int c1 = sub == 3 ? 384 : sub * 128 + 64;
        if (type) { dma_cols(c, rw2, h, sub * 128, WSL(2)); dma_cols(c, rw2, h, c1, WSL(3)); } else { dma_rows(c, rw2, h, sub * 128, WSL(2)); dma_rows(c, rw2, h, c1, WSL(3)); }
    } else dma_rows(c, mksrd(type ? a.Wv : a.Wk, 448 * 8192), h, 256 * half, WSL(0));
}
template <int TYPE>
DI void gemm_role(unsigned tag, int vb, int wave, int half, int h, int sub) {
    CArgs& a = get_args();
    const SideCtx c = make_ctx(wave);
    const int lane = c.lane, g = lane >> 4, n = lane & 15;
    h = __builtin_amdgcn_readfirstlane(h); sub = __builtin_amdgcn_readfirstlane(sub); half = __builtin_amdgcn_readfirstlane(half); asm volatile("" : "+s"(h), "+s"(sub), "+s"(half));
    const int B = 4 * sub + c.wave, prow = 8 * (n >> 2) + (n & 3);
    GCtx x;
    x.rw1 = mksrd(TYPE ? a.Wv : a.Wk, 448 * 8192); x.rx = mksrd(a.X, half ? 0 : 2048 * 896); x.rhb = mksrd(a.HB, HB_SZ);
    x.rexp = mksrd(TYPE ? a.WvoF : a.WqkF, 28 * 16 * 14336); x.rout = mksrd(TYPE ? a.UF : a.GF, 16 * 64 * 28672);
    x.tag = tag; x.rot = 4 * half; x.sw1 = h * 512; x.mw = c.smb + c.wave * 2048; x.mx = c.smb + c.wave * 1024;
    x.sfg = O_FG - O_FW + (h * 2 + TYPE - TYPE * 16 - h) * 256; x.stay = half ? 2 : sub < 2; x.csc = __float_as_uint(CSCALE); x.ostr = TYPE ? 16384 : 28672;
    x.p1 = (unsigned)(u64)a.Au; x.p2 = (unsigned)((u64)a.Au >> 32); x.p5 = min(((TYPE * 16 + h) * 16 + B) * 3584, 2048 * 896 - 4096) + lane * 16; x.kb = 65536 + c.wave * 16384;
    x.vr0 = c.vr0; x.vr1 = c.vr1; x.vx = xsrc8(TYPE, c.wave, lane);
    x.wa = c.smb + SLICE + prow * 512 + ((g ^ swzr(prow)) * 16);
    { const int row = TYPE ? prow : n; x.xa = c.smb + row * 128 + ((g ^ xswz(TYPE, row)) * 16); }
    auto wof = [&](int blk) { return (TYPE ? 2 * blk * 16 + h : h * 28 + 2 * blk) * 14336 + lane * 16; };
    auto oof = [&](int blk, int i) { return blk > 13 ? OOB : (TYPE ? (2 * blk + i) * 1048576 + h * 1024 : h * 64 * 28672 + i * 14336 + blk * 1024) + lane * 16; };
    x.vexp = B > 13 ? OOB : wof(B); x.vimp = B > 5 ? OOB : wof(B + 8);
    x.vk = c.smb + (3 + (c.wave >> 1)) * SLICE + (8 * g + (n >> 2)) * 128 + (((4 * (c.wave & 1) + ((n & 3) >> 1)) ^ ((n >> 3) << 2)) * 16) + 8 * (n & 1);
    x.vo0 = oof(B, 0); x.vo1 = oof(B, 1); x.vo2 = oof(B + 8, 0); x.vo3 = oof(B + 8, 1);
    x.vfl = (c.wave == 0 && lane == 0) ? O_FW + half * 8192 + ((TYPE * 16 + h) * 4 + sub) * 64 : OOB;
    x.vpf = O_FW + (lane & 1) * 8192 + ((TYPE * 16 + h) * 4 + (sub | 2)) * 64; x.vpo = O_FW + 8192 + ((TYPE * 16 + h) * 4 + sub) * 64;
    PSTAMP(1);
    const unsigned tw = TYPE ? gemm_asm1(x) : gemm_asm0(x);
}

#define O_LAG 2
#define O_LAGN 24
DI void o_task(unsigned tag, int wave, int s) {
    {
        CArgs& a = get_args();
        const SideCtx c = make_ctx(wave);
        const int lane = c.lane, hi = lane >> 5, l31 = lane & 31, w = c.wave, pc = swap23(l31);
        s = __builtin_amdgcn_readfirstlane(s); asm volatile("" : "+s"(s));
        const u4 rhb = mksrd(a.HB, HB_SZ), rx = mksrd(a.X, 2048 * 896);
        bar();
        const int vx = xsrc8(0, w, lane);
#pragma unroll
        for (int k = 0; k < 7; ++k) dma_one(rx, vx, s * 28672 + k * 128, c.smb + k * 4096 + w * 1024);
        wait_flags(rhb, lane < 16 ? O_FH + (16 * w + lane) * 128 : OOB, tag + (s < O_LAGN ? s + O_LAG - (O_LAG - 2) * s / O_LAGN : s + 2));
        OCtx x;
        x.r = mksrd(a.HB, 0x7000000);
        x.sg = (unsigned)((char*)a.GF - a.HB) + s * 28672; x.ssx = (unsigned)((char*)a.SXM - a.HB) + s * 1024; x.hx = 4 * w + 2 * (w & 1);
        x.su = (unsigned)((char*)a.UF - a.HB) + s * 16384 + w * 2097152; x.sum = (unsigned)((char*)a.UM - a.HB) + s * 4096 + w * 524288; x.nt = w < 2 ? 4 : 3;
        x.sr = x.p0 = x.p1 = x.p2 = x.p3 = x.p4 = x.p5 = x.p6 = x.p7 = x.p8 = x.p9 = x.pa = 0;
        x.vg = (pc >> 4) * 14336 + hi * 256 + (pc & 15) * 16;
        x.vsx = (l31 >> 4) * 512 + hi * 256 + (l31 & 15) * 16;
        x.vu = (pc >> 4) * 1048576 + hi * 256 + (pc & 15) * 16; x.vum = (pc >> 4) * 262144 + hi * 512 + (pc & 15) * 16;
        x.vx = c.smb + l31 * 128 + ((hi ^ xswz(0, l31)) * 16); x.vt = l31 - 8 * hi;
        x.vpx = c.smb + 32768 + x.hx * 2048 + lane * 16; x.vpm = c.smb + 65536 + w * 2048 + lane * 16; x.vpr = c.smb + 32768 + lane * 16;
        x.vo = c.smb + 73728 + l31 * 912 + w * 64 + hi * 16;
        o_asm(x);
    }
    bar();
    const int tid = wave * 64 + fresh_lane();
    const u4 ro = mksrd(get_args().out, 2048 * 1792);
#pragma unroll
    for (int k = 0; k < 7; ++k) sst128(*(const u4 LAS*)(smem_ + 73728 + (tid >> 3) * 912 + (tid & 7) * 16 + k * 128), ro, (tid >> 3) * 1792 + (tid & 7) * 16, s * 32 * 1792 + 896 + k * 128);
}

DI void e_task(unsigned tag, int wave, int s, int hg) {
    CArgs& a = get_args();
    const SideCtx c = make_ctx(wave);
    const int lane = c.lane, hi = lane >> 5, l31 = lane & 31;
    s = __builtin_amdgcn_readfirstlane(s); hg = __builtin_amdgcn_readfirstlane(hg); asm volatile("" : "+s"(s), "+s"(hg));
    const int h = 4 * hg + c.wave;
    const u4 rhb = mksrd(a.HB, HB_SZ), rg = mksrd(a.GF, 16 * 64 * 28672), rx = mksrd(a.X, 2048 * 896);
    bar();
#pragma unroll
    for (int k = 0; k < 7; ++k) dma_one(rx, xsrc(c.wave, lane, k), s * 28672, c.smb + (7 * c.wave + k) * 1024);
    wait_flags(rhb, lane < 4 ? O_FG + (h * 8 + lane) * 64 : OOB, tag + s + 1);
    u4 gf[28];
    const int key = swap23(l31), o_g = (key >> 4) * 14336 + hi * 256 + (key & 15) * 16;
#pragma unroll
    for (int k = 0; k < 28; ++k) asm volatile("buffer_load_dwordx4 %0, %1, %2, %3 offen sc1" : "=a"(gf[k]) : "v"(o_g), "s"(rg), "s"((h * 64 + s) * 28672 + k * 512) : "memory");
    bar();
    f16v sx;
    pipe<28, 4>([&](int k) { return *(const u4 LAS*)(c.sm + xrd(l31, hi, k & 3) + (k >> 2) * 128); },
                [&](int k, u4 f) { if (k == 0) WAITV(21, "+a"(gf[0]), "+a"(gf[1]), "+a"(gf[2]), "+a"(gf[3]), "+a"(gf[4]), "+a"(gf[5]), "+a"(gf[6]));
                                   if (k == 7) WAITV(14, "+a"(gf[7]), "+a"(gf[8]), "+a"(gf[9]), "+a"(gf[10]), "+a"(gf[11]), "+a"(gf[12]), "+a"(gf[13]));
                                   if (k == 14) WAITV(7, "+a"(gf[14]), "+a"(gf[15]), "+a"(gf[16]), "+a"(gf[17]), "+a"(gf[18]), "+a"(gf[19]), "+a"(gf[20]));
                                   if (k == 21) WAITV(0, "+a"(gf[21]), "+a"(gf[22]), "+a"(gf[23]), "+a"(gf[24]), "+a"(gf[25]), "+a"(gf[26]), "+a"(gf[27]));
                                   if (k == 0) mV_av0(sx, gf[k], f); else mV_av(sx, gf[k], f); },
                [&](int) {});
    asm volatile("s_nop 11" : "+v"(sx));
    float m = -3.0e38f;
#pragma unroll
    for (int j = 0; j < 16; ++j) { if (16 * (j >> 3) + 8 * hi + (j & 7) > l31) sx[j] = -INFINITY; m = fmaxf(m, sx[j]); }
    m = x32max(m);
    float z = 0.f;
#pragma unroll
    for (int j = 0; j < 16; ++j) { sx[j] = __builtin_amdgcn_exp2f(sx[j] - m); z += sx[j]; }
    z = x32add(z);
    const u4 rex = mksrd(a.EX, 64 * 16 * 2048), rmz = mksrd(a.MZX, 64 * 16 * 256);
    sst128(pack8(sx, 0), rex, lane * 16, (s * 16 + h) * 2048); sst128(pack8(sx, 1), rex, lane * 16, (s * 16 + h) * 2048 + 1024);
    sst64((u2){__float_as_uint(m), __float_as_uint(z)}, rmz, lane < 32 ? lane * 8 : OOB, (s * 16 + h) * 256);
    set_flag(c, rhb, O_FE + (s * 4 + hg) * 64, tag);
}

#define A_LATE 1
#define X_NOU 0
#define P_XSLP 24
#define P_USLP 4
DI void x_task(unsigned tag, int wave, int s, int cb) {
    CArgs& a = get_args();
    const SideCtx c = make_ctx(wave);
    const int lane = c.lane, hi = lane >> 5, l31 = lane & 31, w = c.wave;
    s = __builtin_amdgcn_readfirstlane(s); cb = __builtin_amdgcn_readfirstlane(cb); asm volatile("" : "+s"(s), "+s"(cb));
    const u4 rhb = mksrd(a.HB, HB_SZ);
    const u4 ru = mksrd(a.UF, 16 * 64 * 28672), rex = mksrd(a.EX, 64 * 16 * 2048), rmz = mksrd(a.MZX, 64 * 16 * 256);
    const u4 rum = mksrd(a.UM, 28 * 64 * 4096), rsx = mksrd(a.SXM, 64 * 65536);
    wait_flags(rhb, lane < 8 ? O_FG + (((4 * w + (lane & 3)) * 2 + 1) * 4 + ((cb >> 1) & (lane < 4 ? 3 : 1))) * 64 : lane == 8 ? O_FE + (s * 4 + w) * 64 : OOB, lane < 8 ? tag + s + 1 : tag);
    u4 uf[4][2][2], ex[4][2]; u2 mz[4];
    const int o_u = (l31 >> 4) * 1048576 + hi * 256 + (l31 & 15) * 16;
#pragma unroll
    for (int k = 0; k < 4; ++k) {
        const int h = 4 * w + (k ^ (2 * (w & 1))), sh = s * 16 + h;
#pragma unroll
        for (int sg = 0; sg < 2; ++sg) {
            ex[k][sg] = sld128(rex, lane * 16, sh * 2048 + sg * 1024);
#pragma unroll
            for (int nt = 0; nt < 2; ++nt) uf[k][sg][nt] = sld128(ru, o_u, (4 * cb + 2 * nt) * 1048576 + sh * 1024 + sg * 512);
        }
        mz[k] = sld64(rmz, l31 * 8, sh * 256);
        asm volatile("s_sleep %0" :: "i"(P_XSLP));
    }
    waitvm<0>();
    f16v rr[4][2];
#pragma unroll
    for (int k = 0; k < 4; ++k) {
        asm volatile("" : "+v"(mz[k]), "+v"(ex[k][0]), "+v"(ex[k][1]), "+v"(uf[k][0][0]), "+v"(uf[k][0][1]), "+v"(uf[k][1][0]), "+v"(uf[k][1][1]));
        mV_vv0(rr[k][0], uf[k][0][0], ex[k][0]); mV_vv0(rr[k][1], uf[k][0][1], ex[k][0]);
        mV_vv(rr[k][0], uf[k][1][0], ex[k][1]); mV_vv(rr[k][1], uf[k][1][1], ex[k][1]);
        MFW32(rr[k][0], rr[k][1]);
    }
    wait_flags<2>(rhb, lane < 16 ? O_FH + (16 * w + lane) * 128 : s == 63 && lane < 32 ? O_FH + (16 * w + lane - 16) * 128 + 64 : OOB, tag + (s < 62 ? s + 2 : s + 1));
    u4 sp[4][4], um[2][2];
    const int o_sx = (l31 >> 4) * 512 + hi * 256 + (l31 & 15) * 16, o_um = (l31 >> 4) * 262144 + hi * 512 + (l31 & 15) * 16;
#pragma unroll
    for (int k = 0; k < 16; ++k) sp[k >> 2][k & 3] = sld128(rsx, o_sx, ((4 * w + ((k >> 2) ^ (2 * (w & 1)))) * 4 + (k & 3)) * 65536 + s * 1024);
    if (s >= 62) wait_flags<2>(rhb, lane < 4 ? O_FP + ((4 * cb + lane) * 4 + w) * 64 : OOB, tag + 64);
#pragma unroll
    for (int k = 0; k < 4; ++k) um[k >> 1][k & 1] = sld128(rum, o_um, (4 * cb + 2 * (k & 1)) * 262144 + s * 4096 + w * 1024 + (k >> 1) * 256);
    u2 pm[4]; float scx[4];
#pragma unroll
    for (int k = 0; k < 4; ++k) {
        if (k == 0) WAITV(16, "+v"(sp[0][0]), "+v"(sp[0][1]), "+v"(sp[0][2]), "+v"(sp[0][3]));
        if (k == 1) WAITV(12, "+v"(sp[1][0]), "+v"(sp[1][1]), "+v"(sp[1][2]), "+v"(sp[1][3]));
        if (k == 2) WAITV(8, "+v"(sp[2][0]), "+v"(sp[2][1]), "+v"(sp[2][2]), "+v"(sp[2][3]));
        if (k == 3) WAITV(4, "+v"(sp[3][0]), "+v"(sp[3][1]), "+v"(sp[3][2]), "+v"(sp[3][3]));
        const f4 sc = (__builtin_bit_cast(f4, sp[k][0]) + __builtin_bit_cast(f4, sp[k][1])) + (__builtin_bit_cast(f4, sp[k][2]) + __builtin_bit_cast(f4, sp[k][3]));
        const float mx = __uint_as_float(mz[k][0]);
        float m = fmaxf(x32max(fmaxf(fmaxf(sc[0], sc[1]), fmaxf(sc[2], sc[3]))), mx);
        float e[4];
#pragma unroll
        for (int j = 0; j < 4; ++j) e[j] = __builtin_amdgcn_exp2f(sc[j] - m);
        const float cx = __builtin_amdgcn_exp2f(mx - m);
        const float z = x32add((e[0] + e[1]) + (e[2] + e[3])) + cx * __uint_as_float(mz[k][1]);
        const float inv = __builtin_amdgcn_rcpf(z);
        scx[k] = cx * inv;
        pm[k] = (u2){pk2(e[0] * inv, e[1] * inv), pk2(e[2] * inv, e[3] * inv)};
    }
    WAITV(0, "+v"(um[0][0]), "+v"(um[0][1]), "+v"(um[1][0]), "+v"(um[1][1]));
    f16v d0, d1;
    {
        u4 p0 = {pm[0][0], pm[1][0], pm[2][0], pm[3][0]}, p1 = {pm[0][1], pm[1][1], pm[2][1], pm[3][1]};
        asm volatile("s_nop 1\n\tv_mfma_f32_32x32x16_bf16 %0, %2, %6, 0\n\tv_mfma_f32_32x32x16_bf16 %1, %3, %6, 0\n\tv_mfma_f32_32x32x16_bf16 %0, %4, %7, %0\n\tv_mfma_f32_32x32x16_bf16 %1, %5, %7, %1"
                     : "=&v"(d0), "=&v"(d1) : "v"(um[0][0]), "v"(um[0][1]), "v"(um[1][0]), "v"(um[1][1]), "v"(p0), "v"(p1));
    }
    f16v a0 = (rr[0][0] * scx[0] + rr[1][0] * scx[1]) + (rr[2][0] * scx[2] + rr[3][0] * scx[3]);
    f16v a1 = (rr[0][1] * scx[0] + rr[1][1] * scx[1]) + (rr[2][1] * scx[2] + rr[3][1] * scx[3]);
    asm volatile("s_nop 11" : "+v"(d0), "+v"(d1), "+v"(a0), "+v"(a1));
    a0 += d0; a1 += d1;
    bar();
#pragma unroll
    for (int g = 0; g < 4; ++g) {
        *(f4 LAS*)(c.sm + w * 8704 + l31 * 272 + (8 * g + 4 * hi) * 4) = (f4){a0[4 * g], a0[4 * g + 1], a0[4 * g + 2], a0[4 * g + 3]};
        *(f4 LAS*)(c.sm + w * 8704 + l31 * 272 + 128 + (8 * g + 4 * hi) * 4) = (f4){a1[4 * g], a1[4 * g + 1], a1[4 * g + 2], a1[4 * g + 3]};
    }
    bar();
    {
        const int t = w * 8 + (lane >> 3), c8 = lane & 7;
        f4 s0 = {0.f, 0.f, 0.f, 0.f}, s1 = s0;
#pragma unroll
        for (int p = 0; p < 4; ++p) { s0 += *(const f4 LAS*)(c.sm + p * 8704 + t * 272 + c8 * 32); s1 += *(const f4 LAS*)(c.sm + p * 8704 + t * 272 + c8 * 32 + 16); }
        sst128((u4){pk2(s0[0], s0[1]), pk2(s0[2], s0[3]), pk2(s1[0], s1[1]), pk2(s1[2], s1[3])}, mksrd(a.out, 2048 * 1792), t * 1792 + c8 * 16, s * 32 * 1792 + 896 + cb * 128);
    }
}

#define PA_UT 0
#define PA_TU 32896
#define PA_KH (PA_TU + 33280)
#define PA_KL (PA_KH + 7680)
#define PA_KT (PA_KL + 7680)
#define PA_RED (PA_KT + 24576)
typedef __attribute__((ext_vector_type(2))) _Float16 h2;
typedef __attribute__((ext_vector_type(8))) _Float16 h8;
DI unsigned pkh(float a, float b) { f2 v = {a, b}; return __builtin_bit_cast(unsigned, __builtin_convertvector(v, h2)); }
DI void parta_task(unsigned tag, int wave, int task) {
    CArgs& a = get_args();
    const SideCtx c = make_ctx(wave);
    char LAS* sm = c.sm;
    const int lane = c.lane, w = c.wave, tid = w * 64 + lane, hi = lane >> 5, l31 = lane & 31, l15 = lane & 15, kk = lane >> 4;
    task = __builtin_amdgcn_readfirstlane(task); asm volatile("" : "+s"(task));
    const int d0 = task * 8;
    const u4 rpa = mksrd(a.PA, 56 * 32768);
    bar();
    float scl[2];
#pragma unroll
    for (int g = 0; g < 2; ++g) {
        const int d = d0 + 4 * g + w;
        const float dt = bfr(expf(bf2f(a.Alogdt[d])));
        const u2 x0 = *(const u2*)(a.AlogA + d * 256 + lane * 4), x1 = *(const u2*)(a.Aim + d * 256 + lane * 4), x2 = *(const u2*)(a.ACr + d * 256 + lane * 4), x3 = *(const u2*)(a.ACi + d * 256 + lane * 4);
        f4 z[4]; unsigned mx = 0;
#pragma unroll
        for (int i = 0; i < 4; ++i) {
            auto el = [&](u2 x) { return bf2f((u16)(x[i >> 1] >> (16 * (i & 1)))); };
            z[i] = (f4){-bfr(expf(el(x0))) * dt * LOG2E, el(x1) * dt * 0.15915494309189535f, 2.f * el(x2) * dt, 2.f * el(x3) * dt};
            mx = max(mx, max(__float_as_uint(z[i][2]) & 0x7f800000u, __float_as_uint(z[i][3]) & 0x7f800000u));
        }
#pragma unroll
        for (int o = 1; o < 64; o <<= 1) mx = max(mx, (unsigned)__shfl_xor((int)mx, o));
        mx = min(max(mx, 0x00800000u), 0x7e800000u);
        scl[g] = __uint_as_float(mx);
        const float inv = __uint_as_float(0x7f000000u - mx);
#pragma unroll
        for (int i = 0; i < 4; ++i) { z[i][2] *= inv; z[i][3] *= inv; *(f4 LAS*)(sm + PA_RED + w * 9216 + g * 4096 + (i * 64 + lane) * 16) = z[i]; }
    }
    const u4 dsk4 = *(const u4*)(a.ADsk + d0);
    if (tid < 8) *(u4 LAS*)(sm + PA_UT + tid * 4112 + 4096) = (u4){0u, 0u, 0u, 0u};
    for (int i = tid; i < 2 * 31 * 5; i += 256) { const int x = i / 155, r = i % 155; *(u4 LAS*)(sm + (x ? PA_KL : PA_KH) + r * 16) = (u4){0u, 0u, 0u, 0u}; }
    float qpos[4];
    qpos[0] = (float)l15;
    qpos[1] = l15 < 8 ? (float)(256 + 2 * l15) : (float)(512 + ((l15 - 8) >> 2) * 256 + (l15 & 3) * 4);
    qpos[2] = l15 < 8 ? (float)(1024 + (l15 >> 1) * 256 + (l15 & 1) * 8) : 2048.f;
    qpos[3] = (float)(16 * l15);
    auto kgen = [&](int g) __attribute__((always_inline)) {
        const char LAS* pt = sm + PA_RED + w * 9216 + g * 4096 + kk * 16;
        f4 acc[3] = {{0.f, 0.f, 0.f, 0.f}, {0.f, 0.f, 0.f, 0.f}, {0.f, 0.f, 0.f, 0.f}};
#pragma nounroll
        for (int st = 0; st < 16; ++st) {
            u4 op[4];
#pragma unroll
            for (int i = 0; i < 4; ++i) {
                const f4 z = *(const f4 LAS*)(pt + st * 256 + i * 64);
#pragma unroll
                for (int t = 0; t < 4; ++t) {
                    const float ex = __builtin_amdgcn_exp2f(z[0] * qpos[t]), rv = __builtin_amdgcn_fractf(z[1] * qpos[t]);
                    const float er = ex * __builtin_amdgcn_cosf(rv), ei = ex * __builtin_amdgcn_sinf(rv);
                    op[t][i] = t < 3 ? pkh(er, ei) : pkh(z[2] * er - z[3] * ei, -(z[2] * ei + z[3] * er));
                }
            }
#pragma unroll
            for (int t = 0; t < 3; ++t) acc[t] = __builtin_amdgcn_mfma_f32_16x16x32_f16(__builtin_bit_cast(h8, op[3]), __builtin_bit_cast(h8, op[t]), acc[t], 0, 0, 0);
        }
        const float sc = scl[g];
#pragma unroll
        for (int t = 0; t < 3; ++t) *(f4 LAS*)(sm + PA_KT + (4 * g + w) * 3072 + ((t * 16 + l15) * 16 + 4 * kk) * 4) = acc[t] * sc;
    };
    kgen(0);
    if (A_LATE && task < 20) kgen(1);
    {
        const u4 ru = mksrd(a.Au, (X_NOU == 1 || (X_NOU == 2 && task < 20) || (X_NOU == 3 && task >= 20)) ? 0 : 2048 * 896);
#pragma unroll
        for (int i = 0; i < 8; ++i) { dma_one(ru, tid * 896, i * 256 * 896 + d0 * 2, c.smb + PA_TU + i * 4096 + w * 1024); asm volatile("s_sleep %0" :: "i"(P_USLP)); }
    }
    if (!(A_LATE && task < 20)) kgen(1);
    waitvm<0>(); bar();
    u4 uv[8];
#pragma unroll
    for (int i = 0; i < 8; ++i) uv[i] = *(const u4 LAS*)(sm + PA_TU + tid * 128 + i * 16);
#pragma unroll
    for (int ch = 0; ch < 8; ++ch) {
        u4 o;
#pragma unroll
        for (int j = 0; j < 4; ++j) o[j] = __builtin_amdgcn_perm(uv[6 - 2 * j][ch >> 1], uv[7 - 2 * j][ch >> 1], (ch & 1) ? 0x07060302u : 0x05040100u);
        *(u4 LAS*)(sm + PA_UT + ch * 4112 + (255 - tid) * 16) = o;
    }
    int toff[8];
#pragma unroll
    for (int j = 0; j < 8; ++j) {
        const int p = (int)bfr((float)(tid * 8 + j));
        int col, ar = (p & 255) >> 4;
        if (p < 256) col = p & 15; else if (p < 512) col = 16 + ((p & 15) >> 1); else if (p < 1024) col = 24 + ((p - 512) >> 8) * 4 + ((p & 15) >> 2);
        else if (p < 2048) col = 32 + ((p - 1024) >> 8) * 2 + ((p & 15) >> 3); else col = 40;
        toff[j] = (col * 16 + ar) * 4;
    }
    bar();
    if (tid < 32) *(u4 LAS*)(sm + PA_TU + ((tid >> 2) * 260 + 256 + (tid & 3)) * 16) = (u4){0u, 0u, 0u, 0u};
    {
#pragma nounroll
        for (int ch = 0; ch < 8; ++ch) {
            {
                const u4 x0 = *(const u4 LAS*)(sm + PA_UT + ch * 4112 + tid * 16), x1 = *(const u4 LAS*)(sm + PA_UT + ch * 4112 + tid * 16 + 16);
                float kv[8];
#pragma unroll
                for (int j = 0; j < 8; ++j) kv[j] = *(const float LAS*)(sm + PA_KT + ch * 3072 + toff[j]);
                const unsigned r[8] = {x0[0], x0[1], x0[2], x0[3], x1[0], x1[1], x1[2], x1[3]};
#pragma unroll
                for (int cp = 0; cp < 8; ++cp) {
                    u4 o;
#pragma unroll
                    for (int j = 0; j < 4; ++j) o[j] = (cp & 1) ? __builtin_amdgcn_alignbit(r[(cp >> 1) + j + 1], r[(cp >> 1) + j], 16) : r[(cp >> 1) + j];
                    *(u4 LAS*)(sm + PA_TU + (cp * 260 + tid) * 16) = o;
                }
                u4 h4, l4;
#pragma unroll
                for (int j = 0; j < 4; ++j) { h4[j] = pk2(kv[2 * j], kv[2 * j + 1]);
                    l4[j] = pk2(kv[2 * j] - __uint_as_float(h4[j] << 16), kv[2 * j + 1] - __uint_as_float(h4[j] & 0xffff0000u)); }
                const int o = (31 + (tid >> 2)) * 80 + (tid & 3) * 16;
                *(u4 LAS*)(sm + PA_KH + o) = h4; *(u4 LAS*)(sm + PA_KL + o) = l4;
            }
            bar();
            {
                f16v y0, y1;
                const int m00 = 2047 - l31 + 8 * hi, cp = m00 & 7;
                const char LAS* ab = sm + PA_TU + (cp * 260 + (m00 >> 3)) * 16 - 63 * 64;
                const char LAS* bb = sm + PA_KH + l31 * 80 + hi * 16;
                const char LAS* a1w = ab - w * 64; const char LAS* b1w = bb - w * 80;
                const char LAS* a2w = ab + w * 64; const char LAS* b2w = bb + w * 80;
                u4 fr[2][4];
                auto rdg = [&](int g, int sl) __attribute__((always_inline)) {
                    const int i = (g & 15) >> 1, ks = g & 1;
                    if (g < 16) { fr[sl][0] = *(const u4 LAS*)(b1w + (31 - 4 * i) * 80 + ks * 32); fr[sl][2] = *(const u4 LAS*)(a1w + (63 - 4 * i) * 64 + ks * 32);
                                  fr[sl][3] = *(const u4 LAS*)(a1w + (31 - 4 * i) * 64 + ks * 32); fr[sl][1] = *(const u4 LAS*)(b1w + 7680 + (31 - 4 * i) * 80 + ks * 32); }
                    else { fr[sl][0] = *(const u4 LAS*)(b2w + (32 + 4 * i) * 80 + ks * 32); fr[sl][3] = *(const u4 LAS*)(a2w + (32 + 4 * i) * 64 + ks * 32);
                           fr[sl][1] = *(const u4 LAS*)(b2w + 7680 + (32 + 4 * i) * 80 + ks * 32); }
                };
                rdg(0, 0); SB();
#pragma unroll
                for (int g = 0; g < 32; ++g) {
                    const int sl = g & 1;
                    if (g + 1 < 32) rdg(g + 1, sl ^ 1);
                    SB();
                    if (g == 0) { mV_vv0(y0, fr[sl][2], fr[sl][0]); mV_vv0(y1, fr[sl][3], fr[sl][0]); }
                    else { if (g < 16) mV_vv(y0, fr[sl][2], fr[sl][0]); mV_vv(y1, fr[sl][3], fr[sl][0]); }
                    if (g < 16) mV_vv(y0, fr[sl][2], fr[sl][1]);
                    mV_vv(y1, fr[sl][3], fr[sl][1]);
                    SB();
                }
                MFW32(y0, y1);
#pragma unroll
                for (int g = 0; g < 4; ++g) {
                    *(f4 LAS*)(sm + PA_RED + w * 9216 + l31 * 144 + (8 * g + 4 * hi) * 4) = (f4){y0[4 * g], y0[4 * g + 1], y0[4 * g + 2], y0[4 * g + 3]};
                    *(f4 LAS*)(sm + PA_RED + w * 9216 + (32 + l31) * 144 + (8 * g + 4 * hi) * 4) = (f4){y1[4 * g], y1[4 * g + 1], y1[4 * g + 2], y1[4 * g + 3]};
                }
            }
            bar();
            {
                f4 s0 = {0.f, 0.f, 0.f, 0.f}, s1 = s0;
#pragma unroll
                for (int p = 0; p < 4; ++p) { const char LAS* q_ = sm + PA_RED + p * 9216 + (tid >> 2) * 144 + (tid & 3) * 32; s0 += *(const f4 LAS*)q_; s1 += *(const f4 LAS*)(q_ + 16); }
                const u4 uu = *(const u4 LAS*)(sm + PA_UT + ch * 4112 + (255 - tid) * 16);
                const unsigned dw = (ch & 4) ? ((ch & 2) ? dsk4[3] : dsk4[2]) : ((ch & 2) ? dsk4[1] : dsk4[0]);
                const float dsk = __uint_as_float((ch & 1) ? dw & 0xffff0000u : dw << 16);
                float yv[8];
#pragma unroll
                for (int j = 0; j < 8; ++j) yv[j] = bfr(j < 4 ? s0[j & 3] : s1[j & 3]) + bfr(__uint_as_float((j & 1) ? uu[3 - (j >> 1)] << 16 : uu[3 - (j >> 1)] & 0xffff0000u) * dsk);
                sst128((u4){pk2(yv[0], yv[1]), pk2(yv[2], yv[3]), pk2(yv[4], yv[5]), pk2(yv[6], yv[7])}, rpa, tid * 16, (d0 + ch) * 4096);
            }
        }
    }
    set_flag(c, mksrd(a.HB, HB_SZ), O_FA + task * 64, tag);
}
#define P_ASLP 8
DI void pa_out(unsigned tag, int wave, int t) {
    CArgs& a = get_args();
    const SideCtx c = make_ctx(wave);
    const int lane = c.lane, tid = c.wave * 64 + lane;
    t = __builtin_amdgcn_readfirstlane(t); asm volatile("" : "+s"(t));
    const u4 rpa = mksrd(a.PA, 56 * 32768), ro = mksrd(a.out, 2048 * 1792);
    wait_flags(mksrd(a.HB, HB_SZ), lane < 8 ? O_FA + ((t & ~7) + lane) * 64 : OOB, tag);
    u4 v[8];
#pragma unroll
    for (int i = 0; i < 8; ++i) v[i] = sld128(rpa, (tid >> 5) * 4096 + (tid & 31) * 16, ((t >> 3) * 64 + i * 8) * 4096 + (t & 7) * 512);
    WAITV(0, "+v"(v[0]), "+v"(v[1]), "+v"(v[2]), "+v"(v[3]), "+v"(v[4]), "+v"(v[5]), "+v"(v[6]), "+v"(v[7]));
    bar();
#pragma unroll
    for (int i = 0; i < 8; ++i) *(u4 LAS*)(c.sm + (i * 8 + (tid >> 5)) * 528 + (tid & 31) * 16) = v[i];
    bar();
#pragma unroll
    for (int k = 0; k < 8; ++k) {
        const char LAS* p = c.sm + (tid & 7) * 8 * 528 + ((tid >> 3) + 32 * k) * 2;
        u4 o;
#pragma unroll
        for (int j = 0; j < 4; ++j) o[j] = (unsigned)*(const u16 LAS*)(p + (2 * j) * 528) | ((unsigned)*(const u16 LAS*)(p + (2 * j + 1) * 528) << 16);
        sst128(o, ro, (tid >> 3) * 1792 + (tid & 7) * 16, ((t & 7) * 256 + 32 * k) * 1792 + (t >> 3) * 128);
        asm volatile("s_sleep %0" :: "i"(P_ASLP));
    }
}
#define P_R 72
#define P_EPS 88
#define P_DM 4
#define P_TOUCH 1
#define P_XOR 4
#define L_SCR 28672

DI void chain_role(unsigned tag, int vb, int wave) {
    CArgs& a = get_args();
    const int nx = vb & 3, id = vb >> 3, W = wave;
    const int lane = fresh_lane(), q = lane >> 4, n = lane & 15;
    const u4 r = mksrd(a.HB, HB_SZ);
    for (int i = W * 64 + lane; i < 1792; i += 256) *(u4 LAS*)(smem_ + i * 16) = (u4){0u, 0u, 0u, 0u};
    {
        const char* b = id < 16 ? (W & 1 ? (const char*)a.X : (const char*)a.SXM + (nx * 16 + id) * 65536) : id < 20 ? (const char*)a.GF + (size_t)(nx * 4 + id - 16) * 64 * 28672
                      : W & 1 ? (const char*)a.UF + (size_t)(nx * 7 + id - 20) * 1048576 : (const char*)a.UM + (nx * 7 + id - 20) * 262144;
        const int st = id < 16 ? (W & 1 ? 28672 : 1024) : id < 20 ? 28672 : W & 1 ? 16384 : 4096;
        if (W < 2 && id < 27) { unsigned t = sld32(mksrd(b, 64 * st), lane * st, 0); WAITV(0, "+v"(t)); }
    }
    const int o_need = id < 16 ? O_FW + (lane & 4) * 2048 + ((nx * 4 + (id >> 2)) * 4 + (lane & 3)) * 64 : id < 20 ? O_FG + ((nx * 4 + id - 16) * 8 + (lane & 3)) * 64
                     : id < 27 ? O_FW + (lane & 4) * 2048 + ((16 + 4 * W + (lane & 3)) * 4 + ((nx * 7 + id - 20) >> 3)) * 64 : O_FW + 8192 + (W & 1) * 4096 + lane * 64;
    const unsigned want = tag + (id >= 16 && id < 20);
    unsigned win, t0;
    {
        unsigned sp = 0;
#pragma nounroll
        for (;;) {
            u4 v = sld128(r, O_PAR, 0); unsigned f = sld32(r, o_need, 0); WAITV(0, "+v"(v), "+v"(f));
            win = __builtin_amdgcn_readfirstlane(v[0]) ^ tag; t0 = __builtin_amdgcn_readfirstlane(v[1]);
            if (win <= 3u && (t0 ^ tag) == (unsigned)__builtin_amdgcn_readfirstlane(v[2]) && __all(f - want <= 72u)) break;
            asm volatile("s_sleep 2" ::: "memory");
            if (++sp > SPIN_MAX) __builtin_amdgcn_endpgm();
        }
    }
    const int wA = win * 8192, wB = wA ^ 16384, wF = 524288 + wA;
    auto copy_off = [&](int g) { return (2 + (g >> 1)) * 32768 + ((g & 1) ? wA : wB); };
    if (id == 27) {
        unsigned m[7];
#pragma unroll
        for (int k = 0; k < 7; ++k) m[k] = san(*(const unsigned*)(a.mem0 + (lane >> 3) * 448 + (4 * k + W) * 16 + 4 * (lane & 3) + 2 * ((lane >> 2) & 1)));
        bar();
#pragma unroll
        for (int k = 0; k < 7; ++k) sst32(m[k], r, copy_off(nx) + lane * 4, 3 * 131072 + (4 * k + W) * 256);
        return;
    }
    Ctx c;
    c.r = r; c.rts = r; c.t0 = t0; c.per = P_A + P_B; c.R = P_R; c.tol = 4; c.tag = tag; c.dm = P_DM; c.des = P_EPS; c.role = (id < 16 ? 0 : id < 20 ? 4 : 8) + W; c.tf0 = 0;
    c.idx = (lane & 1) ? 0xEEEE : 0x4444; c.vl16 = lane * 16;
    c.vg = c.vg3 = c.vtl = c.vtr = c.vsel = c.vraw = c.vfo = c.ve = c.vs = c.vrw = c.vrr = c.vr1 = c.vr2 = c.vdbg = 0;
    c.vtb = lane < 28 ? O_ANN + lane * 128 : OOB;
    c.vts = 32768 + W * 4096 + (STAMPS ? lane * 4 + (lane < 16 ? 0 : 32768) : 0);
    if (id < 16) {
        const int hb = nx * 16 + id, h = hb >> 2, part = hb & 3;
        c.rw = mksrd(a.WqkF, 16 * 28 * 14336); c.sw = h * 28 + part * 7; c.vsel = ((n >> 2) & 1) * 14336 + q * 256 + (4 * (n >> 3) + (n & 3)) * 16;
        c.rx = mksrd(a.SXM, 64 * 65536); c.sx = hb * 65536 + W * 512;
        c.rg = mksrd(a.X, 2048 * 896); c.sg = 0;
        c.vm = copy_off(nx) + (W >> 1) * 4096 + lane * 16;
        c.vtl = q * 1088 + n * 8 + (W >> 1) * 512;
        const int r_ = (lane >> 2) & 3, c_ = lane & 3;
        const int rec = (h >> 1) * 1024 + part * 256 + (h & 1) * 128;
        const int raw = (h >> 2) * 32768 + wF + (h & 3) * 2048 + part * 512;
        c.vfl = lane == 0 ? O_FH + hb * 128 + W * 64 : OOB; c.vpub = OOB;
        if (W < 2) {
            c.vg = (16 * W + (n & 7)) * 896 + (112 * part + 32 * (q >> 1) + 16 * (n >> 3) + 8 * (q & 1)) * 2; c.vg3 = (q >= 2 && n >= 8) ? OOB : c.vg + 128;
            c.vtr = c_ * 1088 + (8 * q + r_) * 8;
            c.vpub = (q >> 1) * 256 + n * 16 + (q & 1) * 8;
        } else if (W == 2) {
            c.vraw = lane < 16 ? (lane < 8 ? wA : wB) + rec + (lane & 7) * 16 : lane < 32 ? raw + (lane - 16) * 16 : id < 14 ? copy_off(nx) + id * 512 + (lane - 32) * 16 : OOB;
        } else {
            c.vg = copy_off(nx) + (7 * part + 2 * (q >> 1) + (n >> 3)) * 256 + (2 * (n & 7) + (q & 1)) * 16; c.vg3 = (q >= 2 && n >= 8) ? OOB : c.vg + 1024;
            c.vtr = c_ * 1088 + (16 * (q >> 1) + 4 * (r_ >> 1) + 2 * (q & 1) + (r_ & 1)) * 8;
            c.vpub = (n < 8 ? wA : wB) + rec + (n & 7) * 16 + q * 4;
            c.vraw = n < 8 ? raw + n * 32 + q * 8 : OOB;
        }
    } else if (id < 20) {
        const int h = nx * 4 + id - 16;
        c.rw = r; c.sw = 0; c.rx = r; c.sx = 0;
        c.rg = mksrd(a.GF, 16 * 64 * 28672); c.sg = h * 64 * 28672 + W * 4096;
        c.vm = copy_off(nx) + W * 2048 + lane * 16;
        c.vtl = W * 1280 + 2 * q * 160 + n * 4;
        c.vtr = ((lane >> 2) & 7) * 160 + (8 * W + 2 * (lane & 3)) * 4;
        const int rec = 32768 + (h >> 1) * 1024 + W * 256 + (h & 1) * 128;
        const int raw = (h >> 2) * 32768 + wF + (h & 3) * 2048 + W * 512 + 256;
        c.vpub = (lane < 32 ? wA : wB) + rec + (lane & 31) * 4;
        c.vraw = lane < 32 ? raw + lane * 8 : OOB;
        c.vsel = lane < 16 ? (lane < 8 ? wA : wB) + rec + (lane & 7) * 16 : lane < 32 ? raw + (lane - 16) * 16 : OOB;
        c.vfl = O_FG + (h * 8 + (lane & 7)) * 64;
    } else {
        const int pid = nx * 7 + id - 20;
        c.rw = mksrd(a.WvoF, 28 * 16 * 14336); c.sw = (pid * 16 + 4 * W) * 14336;
        c.rg = mksrd(a.UF, 16 * 64 * 28672); c.sg = pid * 1048576 + W * 4096;
        c.rx = mksrd(a.UM, 28 * 64 * 4096); c.sx = pid * 262144 + W * 1024;
        c.tf0 = P_A;
        c.vm = copy_off(nx) + W * 2048 + lane * 16;
        c.vpub = copy_off(W) + pid * 256 + lane * 4;
        c.vs = ((nx & 1) ? wA : wB) + W * 2048 + lane * 16; c.ve = c.vs + 32768;
        auto qb = [](int x) { return x * 256 + (x & 1) * 32 + (x >> 1) * 128; };
        c.vrw = 8192 + W * 1280 + qb(q) + n * 8;
        c.vrr = 8192 + qb(lane & 3) + ((lane >> 3) + 8 * ((lane >> 2) & 1)) * 8;
        c.vr1 = W * 32768 + wF + (n >> 3) * 2048 + (n & 7) * 32 + q * 8;
        c.vr2 = W * 32768 + wF + (n >> 3) * 2048 + q * 512 + 256 + (n & 7) * 32;
        c.vg = O_FG + (((4 * W + (lane & 3)) * 2 + 1) * 4 + (pid >> 3)) * 64;
        c.vfl = lane == 0 ? O_FP + (pid * 4 + W) * 64 : OOB;
        c.vsel = (pid == 0 && lane == 0) ? O_EPOCH : OOB;
        c.vfo = lane == 0 ? O_ANN + pid * 128 + W * 4 : OOB;
    }
    PSTAMP(5);
    asm volatile("s_waitcnt lgkmcnt(0)" ::: "memory");
    chain_asm(c);
}

__global__ __launch_bounds__(256, 1) void fused_kernel(Args a_) {
    CArgs& a = get_args();
    const int wave = __builtin_amdgcn_readfirstlane(threadIdx.x >> 6);
#define PT(k)
    const u4 rhb = mksrd(a.HB, HB_SZ);
    unsigned tag;
    asm volatile("s_load_dword %0, %1, %2" : "=s"(tag) : "s"(a.HB), "s"(O_EPOCH) : "memory");
    const int xcc = (__builtin_amdgcn_s_getreg((31 << 11) | 20) & 7) ^ P_XOR;
    int vb = xcc + (blockIdx.x & ~7);
    const bool prober = vb == 249 && wave;
    if (prober) { const int l = fresh_lane(); sst32(0u, rhb, l < 4 ? O_PROBE + l * 8192 : OOB, wave * 32768); }
    if (wave) {
        u64 pc; unsigned len;
        asm volatile("s_getpc_b64 %0\nfk_code_lo:\n\ts_max_i32 %1, fk_code_hi-fk_code_lo, 0" : "=s"(pc), "=s"(len) :: "scc");
        const u4 rc = mksrd((const void*)pc, len & ~15u);
        const int vo = (((blockIdx.x >> 3) * 3 + wave - 1) * 2) * 1024 + (threadIdx.x & 63) * 16;
#pragma unroll
        for (int i = 0; i < 2; ++i) dma_one(rc, vo + i * 1024, 0, (unsigned)(size_t)(char LAS*)smem_ + L_SCR + wave * 1024);
    }
    PT(4);
    unsigned win = 0;
#pragma nounroll
    for (int pass = 0;; ++pass) {
        gemm_first<0>(wave, (vb & 4) == 0, vb >> 7, 4 * (vb & 3) + ((vb >> 5) & 3), (vb >> 3) & 3);
        unsigned old = 0;
        PT(0);
        if (pass == 0) {
            asm volatile("s_waitcnt lgkmcnt(0)" : "+s"(tag) :: "memory");
            tag += 0x9E3779B4u; old = tag;
            if (wave == 0) asm volatile("s_nop 4\n\tbuffer_atomic_swap %0, %1, %2, %3 offen sc0 sc1" : "+v"(old) : "v"(threadIdx.x == 0 ? O_CNT : OOB), "s"(rhb), "s"((xcc * 32 + (int)(blockIdx.x >> 3)) * 128) : "memory");
        }
        if (pass == 0 && prober) {
            waitvm<18>();
            unsigned best = 1u << 20;
#pragma nounroll
            for (int k = 0; k < 4; ++k) {
                u64 c0, c1; unsigned d;
                asm volatile("s_memtime %0\n\ts_waitcnt lgkmcnt(0)\n\ts_load_dword %2, %3, %4\n\ts_waitcnt lgkmcnt(0)\n\ts_memtime %1\n\ts_waitcnt lgkmcnt(0)" : "=&s"(c0), "=&s"(c1), "=&s"(d) : "s"(a.HB), "s"(O_PROBE + wave * 32768 + ((k + wave) & 3) * 8192) : "memory");
                best = min(best, ((unsigned)(c1 - c0)) * 4 + ((k + wave) & 3));
            }
            *(unsigned LAS*)(smem_ + L_SCR + wave * 4) = best;
        }
        PT(1);
        gemm_first<1>(wave, (vb & 4) == 0, vb >> 7, 4 * (vb & 3) + ((vb >> 5) & 3), (vb >> 3) & 3);
        PT(2);
        WAITV(8, "+v"(old));
        PT(3);
        if (pass) { bar(); break; }
        if (threadIdx.x == 0) {
            unsigned slot = (unsigned)xcc * 32u + (blockIdx.x >> 3);
            if (old == tag)
                for (int k = 0; k < 256; ++k) { slot = (slot + 1u) & 255u; if (__hip_atomic_exchange((unsigned*)(a.HB + O_CNT) + slot * 32u, tag, __ATOMIC_RELAXED, __HIP_MEMORY_SCOPE_AGENT) != tag) break; }
            *(unsigned LAS*)(smem_ + L_SCR) = (slot >> 5) + 8u * (slot & 31u);
        }
        bar();
        const int got = __builtin_amdgcn_readfirstlane(*(const unsigned LAS*)(smem_ + L_SCR));
        if (got == vb) { if (prober) win = min(min(*(const unsigned LAS*)(smem_ + L_SCR + 4), *(const unsigned LAS*)(smem_ + L_SCR + 8)), *(const unsigned LAS*)(smem_ + L_SCR + 12)) & 3u; break; }
        vb = got; waitvm<0>(); bar();
    }
    vb = __builtin_amdgcn_readfirstlane(vb); tag = __builtin_amdgcn_readfirstlane(tag);
    const int res = vb & 7, idx = vb >> 3;
    if (res < 4) {
        const int lane = fresh_lane(), nb = res * 32 + idx;
        if (vb == 249 && wave == 3) { const unsigned t0 = rt_now(); sst128((u4){win ^ tag, t0, t0 ^ tag, 0u}, rhb, lane == 0 ? O_PAR : OOB, 0); }
        sst128((u4){FF, FF, FF, FF}, rhb, lane * 16, nb * 8192 + wave * 2048); sst128((u4){FF, FF, FF, FF}, rhb, lane * 16, nb * 8192 + wave * 2048 + 1024);
        if (nb == 0) sst128((u4){0u, 0u, 0u, 0u}, rhb, lane * 16, O_ANN + wave * 1024);
    }
    if (idx >> 4) gemm_role<1>(tag, vb, wave, res < 4, 4 * (res & 3) + ((idx >> 2) & 3), idx & 3); else gemm_role<0>(tag, vb, wave, res < 4, 4 * (res & 3) + ((idx >> 2) & 3), idx & 3);
    PSTAMP(3);
    if (res < 4 && idx < 28) chain_role(tag, vb, wave);
    const int f = res < 4 ? res * 5 + idx - 27 : ((idx & 2) ? 20 : 84) + (idx >> 2) * 8 + (idx & 1) * 4 + (res - 4);
    if (f < 56) { parta_task(tag, wave, f < 20 ? f : 20 + ((f - 20) & 3) * 9 + ((f - 20) >> 2)); PSTAMP(5); }
    if (f < 20) return;
#pragma nounroll
    for (int it = 0; it < 6; ++it) {
        int t = 0;
        if (f >= 84) t = it == 0 ? 0x3000 + 20 + f - 84 : 0;
        else if (f >= 56 && (it == 0 || f < 80)) t = it < 2 ? 0x1000 + f - 56 + 28 * it : 0;
        else {
            const int i = f < 56 ? f - 16 : f - 80;
            t = it == 1 ? 0x2000 + i : it == 2 ? (i < 8 ? 0x2000 + 40 + i : 0x4000 + i - 8) : it == 3 ? (i < 20 ? 0x3000 + i : 0x4000 + 12 + i) : it == 4 ? (i >= 36 ? 0x4000 + 16 + i : 0) : 0;
        }
        const int k = t >> 12, j = t & 4095;
        if (k == 1) o_task(tag, wave, j); else if (k == 2) e_task(tag, wave, 52 + (j >> 2), j & 3); else if (k == 3) x_task(tag, wave, 52 + j / 7, j % 7); else if (k == 4) pa_out(tag, wave, j);
    }
    PSTAMP(6);
}
__device__ __attribute__((used, noinline)) void fk_tail() { asm volatile("fk_code_hi:"); }

extern "C" void kernel_launch(void* const* d_in, const int* in_sizes, int n_in, void* d_out, int out_size, void* d_ws, size_t ws_size, hipStream_t stream) {
    Args a;
    a.Au = (const u16*)d_in[0]; a.AlogA = (const u16*)d_in[1]; a.Aim = (const u16*)d_in[2]; a.ACr = (const u16*)d_in[3]; a.ACi = (const u16*)d_in[4];
    a.Alogdt = (const u16*)d_in[5]; a.ADsk = (const u16*)d_in[6]; a.X = (const u16*)d_in[7]; a.mem0 = (const u16*)d_in[8];
    a.Wq = (const u16*)d_in[9]; a.Wk = (const u16*)d_in[10]; a.Wv = (const u16*)d_in[11]; a.Wo = (const u16*)d_in[12];
    a.out = (u16*)d_out;
    char* base = (char*)(((size_t)d_ws + (2u << 20) - 1) & ~(size_t)((2u << 20) - 1));
    a.HB = base;
    size_t off = HB_SZ;
    auto alloc = [&](size_t n) { char* p = base + off; off += (n + 4095) & ~(size_t)4095; return p; };
    a.TS = (unsigned*)alloc(8u << 20);
    a.WvoF = (u16*)alloc(28 * 16 * 14336); a.WqkF = (u16*)alloc(28 * 16 * 14336); a.GF = (u16*)alloc((size_t)16 * 64 * 28672); a.UF = (u16*)alloc((size_t)16 * 64 * 28672);
    a.EX = (u16*)alloc(64 * 16 * 2048); a.UM = (u16*)alloc(28 * 64 * 4096); a.MZX = (float*)alloc(64 * 16 * 256); a.SXM = (float*)alloc(64 * 65536); a.PA = (u16*)alloc(56 * 32768);
    (void)hipFuncSetAttribute((const void*)fused_kernel, hipFuncAttributeMaxDynamicSharedMemorySize, 160 * 1024);
    fused_kernel<<<256, 256, 160 * 1024, stream>>>(a);
}
```

```cpp
#define P_A 208
#define P_B 256
#define P_EPS 64
#define P_R 92
#include <hip/hip_runtime.h>
#include <stdint.h>
typedef unsigned short u16;
typedef unsigned long long u64;
typedef __attribute__((ext_vector_type(2))) __bf16 bf2;
typedef __attribute__((ext_vector_type(2))) float f2;
typedef __attribute__((ext_vector_type(4))) float f4;
typedef __attribute__((ext_vector_type(16))) float f16v;
typedef __attribute__((ext_vector_type(2))) unsigned u2;
typedef __attribute__((ext_vector_type(4))) unsigned u4;
typedef __attribute__((ext_vector_type(16))) unsigned u16v;
typedef __attribute__((ext_vector_type(4))) short s4;
#define LAS __attribute__((address_space(3)))
#define DI __device__ __forceinline__
#define SB() __builtin_amdgcn_sched_barrier(0)
#define PIN(x) asm volatile("" : "+v"(x))
#define WAITV(n, ...) asm volatile("s_waitcnt vmcnt(" #n ")" : __VA_ARGS__ :: "memory")
#define LOG2E 1.4426950408889634f
#define CSCALE (LOG2E * 0.0625f)
#define FF 0xFFFFFFFFu
#define OOB 0x7f000000
#define SPIN_MAX (1u << 18)

#define HB_SZ (2 << 20)
#define O_CTL (1 << 20)
#define O_ANN O_CTL
#define O_PAR (O_CTL + 4096)
#define O_FW (O_CTL + 8192)
#define O_FG (O_CTL + 24576)
#define O_FE (O_CTL + 32768)
#define O_FH (O_CTL + 49152)
#define O_FP (O_CTL + 57344)
#define O_FA (O_CTL + 65536)
#define O_CNT (O_CTL + 73728)
#define O_EPOCH (O_CNT + 32768)
#define O_PROBE (O_CTL + 131072)

struct Args {
    const u16 *Au, *AlogA, *Aim, *ACr, *ACi, *Alogdt, *ADsk, *X, *mem0, *Wq, *Wk, *Wv, *Wo;
    u16* out;
    char* HB;
    u16 *WvoF, *WqkF, *GF, *UF, *EX, *UM, *PA; float *MZX, *SXM;
    unsigned* TS;
};
typedef const Args __attribute__((address_space(4))) CArgs;
DI CArgs& get_args() { CArgs* p = (CArgs*)__builtin_amdgcn_kernarg_segment_ptr(); asm volatile("" : "+s"(p)); return *p; }
extern __shared__ __attribute__((aligned(16))) char smem_[];

DI unsigned pk2(float a, float b) { f2 v = {a, b}; return __builtin_bit_cast(unsigned, __builtin_convertvector(v, bf2)); }
DI float bfr(float a) { return __uint_as_float(pk2(a, a) & 0xffff0000u); }
DI float bf2f(u16 x) { return __uint_as_float((unsigned)x << 16); }
DI unsigned san(unsigned x) { return x < 0xFFFFFFFEu ? x : 0xFFFFFFFEu; }
DI void bar() { asm volatile("s_waitcnt lgkmcnt(0)\n\ts_barrier" ::: "memory"); SB(); }
DI float x32max(float a) { auto r = __builtin_amdgcn_permlane32_swap(__float_as_uint(a), __float_as_uint(a), false, false); return fmaxf(__uint_as_float(r[0]), __uint_as_float(r[1])); }
DI float x32add(float a) { auto r = __builtin_amdgcn_permlane32_swap(__float_as_uint(a), __float_as_uint(a), false, false); return __uint_as_float(r[0]) + __uint_as_float(r[1]); }
DI u2 trread(const char LAS* p) { return __builtin_bit_cast(u2, __builtin_amdgcn_ds_read_tr16_b64_v4i16((LAS s4*)p)); }
DI u4 mksrd(const void* p, unsigned bytes) { u64 x = (u64)p; return (u4){(unsigned)x, (unsigned)(x >> 32) & 0xffffu, bytes, 0x00020000u}; }
DI unsigned now32() { u64 t; asm volatile("s_memtime %0\n\ts_waitcnt lgkmcnt(0)" : "=s"(t) :: "memory"); return (unsigned)t; }
DI unsigned rt_now() { u64 t; asm volatile("s_memrealtime %0\n\ts_waitcnt lgkmcnt(0)" : "=s"(t) :: "memory"); return (unsigned)t; }
DI u4 ld128(u4 srd, int voff, int soff) { u4 v; asm volatile("buffer_load_dwordx4 %0, %1, %2, %3 offen sc1" : "=v"(v) : "v"(voff), "s"(srd), "s"(soff) : "memory"); return v; }
DI void st128(u4 v, u4 srd, int voff, int soff) { asm volatile("buffer_store_dwordx4 %0, %1, %2, %3 offen sc1\n\ts_nop 1" :: "v"(v), "v"(voff), "s"(srd), "s"(soff) : "memory"); }
DI void st32(unsigned v, u4 srd, int voff, int soff) { asm volatile("buffer_store_dword %0, %1, %2, %3 offen sc1" :: "v"(v), "v"(voff), "s"(srd), "s"(soff) : "memory"); }
DI u4 sld128(u4 srd, int voff, int soff) { u4 v; asm volatile("s_nop 4\n\tbuffer_load_dwordx4 %0, %1, %2, %3 offen sc1" : "=v"(v) : "v"(voff), "s"(srd), "s"(soff) : "memory"); return v; }
DI u2 sld64(u4 srd, int voff, int soff) { u2 v; asm volatile("s_nop 4\n\tbuffer_load_dwordx2 %0, %1, %2, %3 offen sc1" : "=v"(v) : "v"(voff), "s"(srd), "s"(soff) : "memory"); return v; }
DI unsigned sld32(u4 srd, int voff, int soff) { unsigned v; asm volatile("s_nop 4\n\tbuffer_load_dword %0, %1, %2, %3 offen sc1" : "=v"(v) : "v"(voff), "s"(srd), "s"(soff) : "memory"); return v; }
DI void sst128(u4 v, u4 srd, int voff, int soff) { asm volatile("s_nop 4\n\tbuffer_store_dwordx4 %0, %1, %2, %3 offen sc1\n\ts_nop 1" :: "v"(v), "v"(voff), "s"(srd), "s"(soff) : "memory"); }
DI void sst64(u2 v, u4 srd, int voff, int soff) { asm volatile("s_nop 4\n\tbuffer_store_dwordx2 %0, %1, %2, %3 offen sc1" :: "v"(v), "v"(voff), "s"(srd), "s"(soff) : "memory"); }
DI void sst32(unsigned v, u4 srd, int voff, int soff) { asm volatile("s_nop 4\n\tbuffer_store_dword %0, %1, %2, %3 offen sc1" :: "v"(v), "v"(voff), "s"(srd), "s"(soff) : "memory"); }
template <int N> DI void waitvm() { asm volatile("s_waitcnt vmcnt(%0)" :: "i"(N) : "memory"); }
DI int fresh_lane() { int l; asm volatile("v_mbcnt_lo_u32_b32 %0, -1, 0\n\tv_mbcnt_hi_u32_b32 %0, -1, %0" : "=v"(l)); __builtin_assume(l >= 0 && l < 64); return l; }
#define W_SLP 8
template <int SLP = W_SLP>
DI void wait_flags(u4 rhb, int voff_or_oob, unsigned want) {
    unsigned sp = 0;
#pragma nounroll
    for (;;) {
        unsigned v = sld32(rhb, voff_or_oob, 0); WAITV(0, "+v"(v));
        if (__all((v - want <= 72u) | (voff_or_oob >= OOB))) break;
        unsigned k = __builtin_amdgcn_readfirstlane(want - v);
        k = k > 64u ? (SLP < W_SLP ? 1u : 2u) : k;
        asm volatile("s_sleep %0" :: "i"(SLP) : "memory");
#pragma nounroll
        for (unsigned i = 1; i < k; ++i) asm volatile("s_sleep 30" ::: "memory");
        if (++sp > SPIN_MAX / 16) __builtin_amdgcn_endpgm();
    }
}
#define PSTAMP(k) do { } while (0)
#define RFL(x) (unsigned)__builtin_amdgcn_readfirstlane(x)
#define STAMPS 0
struct Ctx { u4 r, rw, rg, rx, rts; unsigned lv0, lv1, t0, tf0, per, R, tag, sw, sg, sx, des, role; unsigned idx, vl16, vm, vg, vg3, vtl, vtr, vsel, vpub, vraw, vfl, vfo, ve, vs, vrw, vrr, vr1, vr2, vts, vdbg, vtb; };
DI void chain_asm(const Ctx& c) {
    const u16v s0 = {RFL(c.r[0]), RFL(c.r[1]), RFL(c.r[2]), RFL(c.r[3]), RFL(c.rw[0]), RFL(c.rw[1]), RFL(c.rw[2]), RFL(c.rw[3]), RFL(c.rg[0]), RFL(c.rg[1]), RFL(c.rg[2]), RFL(c.rg[3]), RFL(c.rx[0]), RFL(c.rx[1]), RFL(c.rx[2]), RFL(c.rx[3])}, s1 = {RFL(c.rts[0]), RFL(c.rts[1]), RFL(c.rts[2]), RFL(c.rts[3]), RFL(c.lv0), RFL(c.lv1), RFL(c.t0), RFL(c.tf0), RFL(c.per), RFL(c.R), RFL(c.tag), RFL(c.sw), RFL(c.sg), RFL(c.sx), RFL(c.des), RFL(c.role)}, v0 = {c.idx, c.vl16, c.vm, c.vg, c.vg3, c.vtl, c.vtr, c.vsel, c.vpub, c.vraw, c.vfl, c.vfo, c.ve, c.vs, c.vrw, c.vrr}, v1 = {c.vr1, c.vr2, c.vts, c.vdbg, c.vtb, 0u, 0u, 0u, 0u, 0u, 0u, 0u, 0u, 0u, 0u, 0u};
    asm volatile(
"s_nop 4\ns_cmp_eq_u32 s39, 0\ns_cbranch_scc1 fk_jh0\ns_cmp_eq_u32 s39, 1\n"
"s_cbranch_scc1 fk_jh1\ns_cmp_eq_u32 s39, 2\ns_cbranch_scc1 fk_jh2\ns_cmp_eq_u32 s39, 3\n"
"s_cbranch_scc1 fk_jh3\ns_cmp_eq_u32 s39, 4\ns_cbranch_scc1 fk_jx0\ns_cmp_eq_u32 s39, 5\n"
"s_cbranch_scc1 fk_jx1\ns_cmp_eq_u32 s39, 6\ns_cbranch_scc1 fk_jx2\ns_cmp_eq_u32 s39, 7\n"
"s_cbranch_scc1 fk_jx3\ns_cmp_ge_u32 s39, 8\ns_cbranch_scc1 fk_jp0\ns_endpgm\n"
"fk_jh0:\ns_branch fk_h0\nfk_jh1:\ns_branch fk_h1\n"
"fk_jh2:\ns_branch fk_h2\nfk_jh3:\ns_branch fk_h3\n"
"fk_jx0:\ns_branch fk_x0\nfk_jx1:\ns_branch fk_x1\n"
"fk_jx2:\ns_branch fk_x2\nfk_jx3:\ns_branch fk_x3\n"
"fk_jp0:\ns_branch fk_p0\nfk_h0:\ns_mov_b32 s40, 0\n"
"s_mov_b32 s41, 0\ns_mov_b32 s42, 262144\ns_mov_b32 s43, 393216\ns_mov_b32 s44, s30\n"
"s_mov_b32 s45, 0\ns_bcnt1_i32_b64 s46, s[28:29]\ns_mov_b32 s92, 0\ns_mov_b32 s49, 0\n"
"s_mov_b32 s53, 0\ns_mov_b32 s50, s36\ns_mov_b32 s51, s37\nv_mov_b32 v131, v13\n"
"v_mov_b32 v132, v14\nv_mov_b32 v134, 24576\nv_mov_b32 v115, 0\nv_mov_b32 v120, -1\n"
"v_mov_b32 v121, -1\nv_mov_b32 v122, -1\nv_mov_b32 v123, -1\ns_add_u32 s47, s35, 0\n"
"s_and_b32 s52, s47, 1\ns_mul_i32 s52, s52, -14208\ns_mul_i32 s48, s47, 14336\ns_add_u32 s48, s48, s52\n"
"s_add_u32 s47, s48, 0\nbuffer_load_dwordx4 a[0:3], v15, s[12:15], s47 offen sc1\nbuffer_load_dwordx4 a[4:7], v15, s[12:15], s47 offen offset:1024 sc1\ns_add_u32 s47, s48, 2048\n"
"buffer_load_dwordx4 a[8:11], v15, s[12:15], s47 offen sc1\nbuffer_load_dwordx4 a[12:15], v15, s[12:15], s47 offen offset:1024 sc1\ns_add_u32 s47, s48, 4096\nbuffer_load_dwordx4 a[16:19], v15, s[12:15], s47 offen sc1\n"
"buffer_load_dwordx4 a[20:23], v15, s[12:15], s47 offen offset:1024 sc1\ns_add_u32 s47, s48, 6144\nbuffer_load_dwordx4 a[24:27], v15, s[12:15], s47 offen sc1\nbuffer_load_dwordx4 a[28:31], v15, s[12:15], s47 offen offset:1024 sc1\n"
"s_add_u32 s47, s35, 1\ns_and_b32 s52, s47, 1\ns_mul_i32 s52, s52, -14208\ns_mul_i32 s48, s47, 14336\n"
"s_add_u32 s48, s48, s52\ns_add_u32 s47, s48, 0\nbuffer_load_dwordx4 a[32:35], v15, s[12:15], s47 offen sc1\nbuffer_load_dwordx4 a[36:39], v15, s[12:15], s47 offen offset:1024 sc1\n"
"s_add_u32 s47, s48, 2048\nbuffer_load_dwordx4 a[40:43], v15, s[12:15], s47 offen sc1\nbuffer_load_dwordx4 a[44:47], v15, s[12:15], s47 offen offset:1024 sc1\ns_add_u32 s47, s48, 4096\n"
"buffer_load_dwordx4 a[48:51], v15, s[12:15], s47 offen sc1\nbuffer_load_dwordx4 a[52:55], v15, s[12:15], s47 offen offset:1024 sc1\ns_add_u32 s47, s48, 6144\nbuffer_load_dwordx4 a[56:59], v15, s[12:15], s47 offen sc1\n"
"buffer_load_dwordx4 a[60:63], v15, s[12:15], s47 offen offset:1024 sc1\ns_add_u32 s47, s35, 2\ns_and_b32 s52, s47, 1\ns_mul_i32 s52, s52, -14208\n"
"s_mul_i32 s48, s47, 14336\ns_add_u32 s48, s48, s52\ns_add_u32 s47, s48, 0\nbuffer_load_dwordx4 a[64:67], v15, s[12:15], s47 offen sc1\n"
"buffer_load_dwordx4 a[68:71], v15, s[12:15], s47 offen offset:1024 sc1\ns_add_u32 s47, s48, 2048\nbuffer_load_dwordx4 a[72:75], v15, s[12:15], s47 offen sc1\nbuffer_load_dwordx4 a[76:79], v15, s[12:15], s47 offen offset:1024 sc1\n"
"s_add_u32 s47, s48, 4096\nbuffer_load_dwordx4 a[80:83], v15, s[12:15], s47 offen sc1\nbuffer_load_dwordx4 a[84:87], v15, s[12:15], s47 offen offset:1024 sc1\ns_add_u32 s47, s48, 6144\n"
"buffer_load_dwordx4 a[88:91], v15, s[12:15], s47 offen sc1\nbuffer_load_dwordx4 a[92:95], v15, s[12:15], s47 offen offset:1024 sc1\ns_add_u32 s47, s35, 3\ns_and_b32 s52, s47, 1\n"
"s_mul_i32 s52, s52, -14208\ns_mul_i32 s48, s47, 14336\ns_add_u32 s48, s48, s52\ns_add_u32 s47, s48, 0\n"
"buffer_load_dwordx4 a[96:99], v15, s[12:15], s47 offen sc1\nbuffer_load_dwordx4 a[100:103], v15, s[12:15], s47 offen offset:1024 sc1\ns_add_u32 s47, s48, 2048\nbuffer_load_dwordx4 a[104:107], v15, s[12:15], s47 offen sc1\n"
"buffer_load_dwordx4 a[108:111], v15, s[12:15], s47 offen offset:1024 sc1\ns_add_u32 s47, s48, 4096\nbuffer_load_dwordx4 a[112:115], v15, s[12:15], s47 offen sc1\nbuffer_load_dwordx4 a[116:119], v15, s[12:15], s47 offen offset:1024 sc1\n"
"s_add_u32 s47, s48, 6144\nbuffer_load_dwordx4 a[120:123], v15, s[12:15], s47 offen sc1\nbuffer_load_dwordx4 a[124:127], v15, s[12:15], s47 offen offset:1024 sc1\ns_add_u32 s47, s50, 7168\n"
"buffer_load_dwordx4 v[72:75], v11, s[16:19], s50 offen\nbuffer_load_dwordx4 v[76:79], v12, s[16:19], s50 offen\nbuffer_load_dwordx4 v[80:83], v11, s[16:19], s47 offen\nbuffer_load_dwordx4 v[84:87], v12, s[16:19], s47 offen\n"
"s_add_u32 s50, s50, 28672\ns_waitcnt vmcnt(0)\ns_barrier\ns_memrealtime s[54:55]\n"
"s_waitcnt lgkmcnt(0)\ns_add_u32 s44, s54, 16\ns_mov_b32 s45, 8\n.p2align 6\n"
"top_h0:\nv_mov_b32 v56, 0\nv_mov_b32 v57, 0\nv_mov_b32 v58, 0\n"
"v_mov_b32 v59, 0\nv_mov_b32 v60, 0\nv_mov_b32 v61, 0\nv_mov_b32 v62, 0\n"
"v_mov_b32 v63, 0\nv_mov_b32 v64, 0\nv_mov_b32 v65, 0\nv_mov_b32 v66, 0\n"
"v_mov_b32 v67, 0\nv_mov_b32 v68, 0\nv_mov_b32 v69, 0\nv_mov_b32 v70, 0\n"
"v_mov_b32 v71, 0\nv_mov_b32 v128, 0\ns_mul_i32 s48, s45, 6\ns_and_b32 s48, s48, -4\n"
"s_getpc_b64 s[60:61]\nspc_h0:\ns_sub_u32 s48, sgo_h0-spc_h0, s48\ns_add_u32 s60, s60, s48\n"
"s_addc_u32 s61, s61, 0\nsf_h0:\ns_memrealtime s[54:55]\ns_nop 2\n"
"s_memrealtime s[56:57]\ns_nop 2\ns_memrealtime s[58:59]\nsg_h0:\n"
"s_waitcnt lgkmcnt(4)\ns_sub_u32 s47, s54, s44\ns_add_u32 s48, s47, 12\ns_cmp_ge_u32 s48, 12\n"
"s_cbranch_scc1 so_h0\ns_memrealtime s[54:55]\ns_waitcnt lgkmcnt(4)\ns_cmp_ge_u32 s56, s44\n"
"s_cbranch_scc1 sx_h0\ns_memrealtime s[56:57]\ns_waitcnt lgkmcnt(4)\ns_cmp_ge_u32 s58, s44\n"
"s_cbranch_scc1 sx_h0\ns_memrealtime s[58:59]\ns_waitcnt lgkmcnt(4)\ns_cmp_ge_u32 s54, s44\n"
"s_cbranch_scc1 sx_h0\ns_memrealtime s[54:55]\ns_waitcnt lgkmcnt(4)\ns_cmp_ge_u32 s56, s44\n"
"s_cbranch_scc1 sx_h0\ns_memrealtime s[56:57]\ns_waitcnt lgkmcnt(4)\ns_cmp_ge_u32 s58, s44\n"
"s_cbranch_scc1 sx_h0\ns_memrealtime s[58:59]\ns_waitcnt lgkmcnt(4)\ns_cmp_ge_u32 s54, s44\n"
"s_cbranch_scc1 sx_h0\ns_memrealtime s[54:55]\ns_waitcnt lgkmcnt(4)\ns_cmp_ge_u32 s56, s44\n"
"s_cbranch_scc1 sx_h0\ns_memrealtime s[56:57]\ns_waitcnt lgkmcnt(4)\ns_cmp_ge_u32 s58, s44\n"
"s_cbranch_scc1 sx_h0\ns_memrealtime s[58:59]\ns_waitcnt lgkmcnt(4)\ns_cmp_ge_u32 s54, s44\n"
"s_cbranch_scc1 sx_h0\ns_memrealtime s[54:55]\ns_waitcnt lgkmcnt(4)\ns_cmp_ge_u32 s56, s44\n"
"s_cbranch_scc1 sx_h0\ns_memrealtime s[56:57]\ns_waitcnt lgkmcnt(4)\ns_cmp_ge_u32 s58, s44\n"
"s_cbranch_scc1 sx_h0\ns_memrealtime s[58:59]\ns_waitcnt lgkmcnt(4)\ns_cmp_ge_u32 s54, s44\n"
"s_cbranch_scc1 sx_h0\ns_memrealtime s[54:55]\ns_waitcnt lgkmcnt(4)\ns_cmp_ge_u32 s56, s44\n"
"s_cbranch_scc1 sx_h0\ns_memrealtime s[56:57]\ns_waitcnt lgkmcnt(4)\ns_cmp_ge_u32 s58, s44\n"
"s_cbranch_scc1 sx_h0\ns_memrealtime s[58:59]\ns_waitcnt lgkmcnt(4)\ns_cmp_ge_u32 s54, s44\n"
"s_cbranch_scc1 sx_h0\ns_memrealtime s[54:55]\ns_waitcnt lgkmcnt(4)\ns_cmp_ge_u32 s56, s44\n"
"s_cbranch_scc1 sx_h0\ns_memrealtime s[56:57]\ns_branch sx_h0\nso_h0:\n"
"s_waitcnt lgkmcnt(0)\ns_cmp_lt_i32 s47, 0\ns_cbranch_scc1 sf_h0\ns_branch sgo_h0\n"
"sx_h0:\ns_setpc_b64 s[60:61]\ns_nop 0\ns_nop 0\n"
"s_nop 0\ns_nop 0\ns_nop 0\ns_nop 0\n"
"s_nop 0\ns_nop 0\ns_nop 0\ns_nop 0\n"
"s_nop 0\ns_nop 0\ns_nop 0\ns_nop 0\n"
"s_nop 0\ns_nop 0\ns_nop 0\ns_nop 0\n"
"s_nop 0\ns_nop 0\ns_nop 0\ns_nop 0\n"
"s_nop 0\ns_nop 0\nsgo_h0:\nbuffer_load_dwordx4 v[40:43], v10, s[8:11], s43 offen offset:0 sc1\n"
"buffer_load_dwordx4 v[44:47], v10, s[8:11], s43 offen offset:1024 sc1\ns_nop 15\ns_nop 3\nbuffer_load_dwordx4 v[48:51], v10, s[8:11], s43 offen offset:2048 sc1\n"
"s_nop 13\nbuffer_load_dwordx4 v[52:55], v10, s[8:11], s43 offen offset:3072 sc1\nlanded_h0:\ns_waitcnt vmcnt(3)\n"
"v_smfmac_f32_16x16x64_bf16 v[56:59], v[40:43], a[0:7], v8\nv_max3_u32 v128, v128, v40, v41\nv_smfmac_f32_16x16x64_bf16 v[60:63], v[40:43], a[32:39], v8\nv_max3_u32 v128, v128, v42, v43\n"
"v_smfmac_f32_16x16x64_bf16 v[64:67], v[40:43], a[64:71], v8\nv_smfmac_f32_16x16x64_bf16 v[68:71], v[40:43], a[96:103], v8\ns_waitcnt vmcnt(2)\nv_smfmac_f32_16x16x64_bf16 v[56:59], v[44:47], a[8:15], v8\n"
"v_max3_u32 v128, v128, v44, v45\nv_smfmac_f32_16x16x64_bf16 v[60:63], v[44:47], a[40:47], v8\nv_max3_u32 v128, v128, v46, v47\nv_smfmac_f32_16x16x64_bf16 v[64:67], v[44:47], a[72:79], v8\n"
"v_smfmac_f32_16x16x64_bf16 v[68:71], v[44:47], a[104:111], v8\ns_waitcnt vmcnt(1)\nv_smfmac_f32_16x16x64_bf16 v[56:59], v[48:51], a[16:23], v8\nv_max3_u32 v128, v128, v48, v49\n"
"v_smfmac_f32_16x16x64_bf16 v[60:63], v[48:51], a[48:55], v8\nv_max3_u32 v128, v128, v50, v51\nv_smfmac_f32_16x16x64_bf16 v[64:67], v[48:51], a[80:87], v8\nv_smfmac_f32_16x16x64_bf16 v[68:71], v[48:51], a[112:119], v8\n"
"s_waitcnt vmcnt(0)\nv_smfmac_f32_16x16x64_bf16 v[56:59], v[52:55], a[24:31], v8\nv_max3_u32 v128, v128, v52, v53\nv_smfmac_f32_16x16x64_bf16 v[60:63], v[52:55], a[56:63], v8\n"
"v_max3_u32 v128, v128, v54, v55\nv_smfmac_f32_16x16x64_bf16 v[64:67], v[52:55], a[88:95], v8\nv_cmp_eq_u32 vcc, -1, v128\nv_smfmac_f32_16x16x64_bf16 v[68:71], v[52:55], a[120:127], v8\n"
"v_add_f32 v129, v56, v57\nv_add_f32 v130, v58, v59\nv_cvt_pk_bf16_f32 v112, v129, v130\nv_add_f32 v129, v60, v61\n"
"v_add_f32 v130, v62, v63\nv_cvt_pk_bf16_f32 v113, v129, v130\nv_add_f32 v129, v64, v65\nv_add_f32 v130, v66, v67\n"
"v_cvt_pk_bf16_f32 v114, v129, v130\nv_add_f32 v129, v68, v69\nv_add_f32 v130, v70, v71\nv_cvt_pk_bf16_f32 v115, v129, v130\n"
"ds_write2_b64 v131, v[112:113], v[114:115] offset0:0 offset1:16\ns_cbranch_vccnz retry_h0\nv_mov_b32 v129, 0\ns_nop 1\n"
"v_mov_b32 v129, 0\ns_nop 1\ns_waitcnt lgkmcnt(0)\ns_barrier\n"
"s_nop 9\nds_read_b64_tr_b16 v[88:89], v132 offset:0\nds_read_b64_tr_b16 v[90:91], v132 offset:32\nds_read_b64_tr_b16 v[92:93], v132 offset:256\n"
"ds_read_b64_tr_b16 v[94:95], v132 offset:288\nds_read_b64_tr_b16 v[96:97], v132 offset:512\nds_read_b64_tr_b16 v[98:99], v132 offset:544\nds_read_b64_tr_b16 v[100:101], v132 offset:768\n"
"ds_read_b64_tr_b16 v[102:103], v132 offset:800\ns_waitcnt lgkmcnt(6)\nv_mfma_f32_16x16x32_bf16 v[104:107], v[88:91], v[72:75], 0\nv_mfma_f32_16x16x32_bf16 v[108:111], v[88:91], v[80:83], 0\n"
"s_waitcnt lgkmcnt(4)\nv_mfma_f32_16x16x32_bf16 v[104:107], v[92:95], v[76:79], v[104:107]\nv_mfma_f32_16x16x32_bf16 v[108:111], v[92:95], v[84:87], v[108:111]\ns_waitcnt lgkmcnt(2)\n"
"v_mfma_f32_16x16x32_bf16 v[104:107], v[96:99], v[72:75], v[104:107]\nv_mfma_f32_16x16x32_bf16 v[108:111], v[96:99], v[80:83], v[108:111]\ns_waitcnt lgkmcnt(0)\nv_mfma_f32_16x16x32_bf16 v[104:107], v[100:103], v[76:79], v[104:107]\n"
"v_mfma_f32_16x16x32_bf16 v[108:111], v[100:103], v[84:87], v[108:111]\ns_nop 7\nv_add_f32_dpp v124, v106, v104 row_ror:8 row_mask:0xf bank_mask:0x3\nv_add_f32_dpp v124, v108, v110 row_ror:8 row_mask:0xf bank_mask:0xc\n"
"v_add_f32_dpp v125, v107, v105 row_ror:8 row_mask:0xf bank_mask:0x3\nv_add_f32_dpp v125, v109, v111 row_ror:8 row_mask:0xf bank_mask:0xc\ns_add_u32 s47, s50, 7168\nbuffer_load_dwordx4 v[72:75], v11, s[16:19], s50 offen\n"
"buffer_load_dwordx4 v[76:79], v12, s[16:19], s50 offen\nbuffer_load_dwordx4 v[80:83], v11, s[16:19], s47 offen\nbuffer_load_dwordx4 v[84:87], v12, s[16:19], s47 offen\ns_add_u32 s50, s50, 28672\n"
"buffer_store_dwordx2 v[124:125], v16, s[20:23], s51 offen sc1\ns_add_u32 s51, s51, 1024\ns_add_u32 s47, s40, s34\nv_mov_b32 v135, s47\n"
"buffer_store_dword v135, v18, s[8:11], 0 offen sc1\nds_read_b32 v133, v134\nv_xor_b32 v131, 8192, v131\nv_xor_b32 v132, 8192, v132\n"
"s_waitcnt lgkmcnt(0)\nv_readfirstlane_b32 s47, v133\ns_sub_u32 s47, s47, s40\ns_cmp_eq_u32 s47, 1\n"
"s_cbranch_scc1 adopt_h0\nback_h0:\ns_mul_i32 s48, s46, 8\ns_add_u32 s45, s45, s48\n"
"s_add_u32 s45, s45, s32\ns_lshr_b32 s48, s45, 4\ns_lshl_b32 s48, s48, 2\ns_add_u32 s44, s44, s48\n"
"s_and_b32 s45, s45, 15\ns_add_u32 s52, s40, 1\ns_bitcmp1_b64 s[28:29], s52\ns_subb_u32 s46, s46, 0\n"
"s_add_u32 s40, s40, 1\ns_mov_b32 s43, s41\ns_add_u32 s41, s41, 131072\ns_and_b32 s41, s41, 524287\n"
"s_add_u32 s92, s92, 1\ns_xor_b32 s42, s41, 262144\ns_waitcnt lgkmcnt(0)\ns_cmp_lt_u32 s40, 64\n"
"s_cbranch_scc1 top_h0\ns_add_u32 s47, s34, 64\nv_mov_b32 v135, s47\ns_waitcnt vmcnt(0)\n"
"buffer_store_dword v135, v18, s[8:11], 0 offen sc1\ns_branch end_h0\nretry2_h0:\ns_sleep 3\n"
"retry_h0:\ns_add_u32 s53, s53, 1\ns_add_u32 s49, s49, 1\ns_cmp_gt_u32 s49, 65536\n"
"s_cbranch_scc1 end_h0\nbuffer_load_dwordx4 v[40:43], v10, s[8:11], s43 offen offset:0 sc1\nbuffer_load_dwordx4 v[44:47], v10, s[8:11], s43 offen offset:1024 sc1\nbuffer_load_dwordx4 v[48:51], v10, s[8:11], s43 offen offset:2048 sc1\n"
"buffer_load_dwordx4 v[52:55], v10, s[8:11], s43 offen offset:3072 sc1\nv_mov_b32 v128, 0\ns_add_u32 s47, s40, 1\nv_mov_b32 v133, s47\n"
"ds_write_b32 v134, v133\ns_waitcnt vmcnt(0)\nv_max3_u32 v128, v128, v40, v41\nv_max3_u32 v128, v128, v42, v43\n"
"v_max3_u32 v128, v128, v44, v45\nv_max3_u32 v128, v128, v46, v47\nv_max3_u32 v128, v128, v48, v49\nv_max3_u32 v128, v128, v50, v51\n"
"v_max3_u32 v128, v128, v52, v53\nv_max3_u32 v128, v128, v54, v55\nv_cmp_eq_u32 vcc, -1, v128\nv_mov_b32 v56, 0\n"
"v_mov_b32 v57, 0\nv_mov_b32 v58, 0\nv_mov_b32 v59, 0\nv_mov_b32 v60, 0\n"
"v_mov_b32 v61, 0\nv_mov_b32 v62, 0\nv_mov_b32 v63, 0\nv_mov_b32 v64, 0\n"
"v_mov_b32 v65, 0\nv_mov_b32 v66, 0\nv_mov_b32 v67, 0\nv_mov_b32 v68, 0\n"
"v_mov_b32 v69, 0\nv_mov_b32 v70, 0\nv_mov_b32 v71, 0\ns_cbranch_vccnz retry2_h0\n"
"s_branch landed_h0\nadopt_h0:\nbuffer_load_dword v116, v28, s[8:11], 0 offen sc1\ns_lshl_b32 s52, s40, 26\n"
"s_or_b32 s47, s52, 0x3ffffff\ns_waitcnt vmcnt(0)\nv_cmp_ge_u32 vcc, s47, v116\ns_nop 1\n"
"v_cndmask_b32 v133, 0, v116, vcc\ns_nop 1\nv_max_u32_dpp v133, v133, v133 row_ror:8 row_mask:0xf bank_mask:0xf\ns_nop 1\n"
"v_max_u32_dpp v133, v133, v133 row_ror:4 row_mask:0xf bank_mask:0xf\ns_nop 1\nv_max_u32_dpp v133, v133, v133 row_ror:2 row_mask:0xf bank_mask:0xf\ns_nop 1\n"
"v_max_u32_dpp v133, v133, v133 row_ror:1 row_mask:0xf bank_mask:0xf\ns_nop 0\nv_readlane_b32 s47, v133, 0\nv_readlane_b32 s48, v133, 16\n"
"s_max_u32 s48, s48, s47\ns_cmp_le_u32 s48, s52\ns_cbranch_scc1 back_h0\ns_bfe_u32 s52, s48, 1179656\n"
"s_add_u32 s52, s52, s30\ns_sub_u32 s45, s52, s44\ns_and_b32 s45, s45, 3\ns_sub_u32 s44, s52, s45\n"
"s_lshl_b32 s45, s45, 2\ns_and_b32 s46, s48, 255\ns_mov_b32 s92, 0\ns_branch back_h0\n"
"end_h0:\ns_branch fk_done\nfk_h1:\ns_mov_b32 s40, 0\n"
"s_mov_b32 s41, 0\ns_mov_b32 s42, 262144\ns_mov_b32 s43, 393216\ns_mov_b32 s44, s30\n"
"s_mov_b32 s45, 0\ns_bcnt1_i32_b64 s46, s[28:29]\ns_mov_b32 s92, 0\ns_mov_b32 s49, 0\n"
"s_mov_b32 s53, 0\ns_mov_b32 s50, s36\ns_mov_b32 s51, s37\nv_mov_b32 v131, v13\n"
"v_mov_b32 v132, v14\nv_mov_b32 v134, 24576\nv_mov_b32 v115, 0\nv_mov_b32 v120, -1\n"
"v_mov_b32 v121, -1\nv_mov_b32 v122, -1\nv_mov_b32 v123, -1\ns_add_u32 s47, s35, 4\n"
"s_and_b32 s52, s47, 1\ns_mul_i32 s52, s52, -14208\ns_mul_i32 s48, s47, 14336\ns_add_u32 s48, s48, s52\n"
"s_add_u32 s47, s48, 2048\nbuffer_load_dwordx4 a[8:11], v15, s[12:15], s47 offen sc1\nbuffer_load_dwordx4 a[12:15], v15, s[12:15], s47 offen offset:1024 sc1\ns_add_u32 s47, s48, 0\n"
"buffer_load_dwordx4 a[0:3], v15, s[12:15], s47 offen sc1\nbuffer_load_dwordx4 a[4:7], v15, s[12:15], s47 offen offset:1024 sc1\ns_add_u32 s47, s48, 6144\nbuffer_load_dwordx4 a[24:27], v15, s[12:15], s47 offen sc1\n"
"buffer_load_dwordx4 a[28:31], v15, s[12:15], s47 offen offset:1024 sc1\ns_add_u32 s47, s48, 4096\nbuffer_load_dwordx4 a[16:19], v15, s[12:15], s47 offen sc1\nbuffer_load_dwordx4 a[20:23], v15, s[12:15], s47 offen offset:1024 sc1\n"
"s_add_u32 s47, s35, 5\ns_and_b32 s52, s47, 1\ns_mul_i32 s52, s52, -14208\ns_mul_i32 s48, s47, 14336\n"
"s_add_u32 s48, s48, s52\ns_add_u32 s47, s48, 2048\nbuffer_load_dwordx4 a[40:43], v15, s[12:15], s47 offen sc1\nbuffer_load_dwordx4 a[44:47], v15, s[12:15], s47 offen offset:1024 sc1\n"
"s_add_u32 s47, s48, 0\nbuffer_load_dwordx4 a[32:35], v15, s[12:15], s47 offen sc1\nbuffer_load_dwordx4 a[36:39], v15, s[12:15], s47 offen offset:1024 sc1\ns_add_u32 s47, s48, 6144\n"
"buffer_load_dwordx4 a[56:59], v15, s[12:15], s47 offen sc1\nbuffer_load_dwordx4 a[60:63], v15, s[12:15], s47 offen offset:1024 sc1\ns_add_u32 s47, s48, 4096\nbuffer_load_dwordx4 a[48:51], v15, s[12:15], s47 offen sc1\n"
"buffer_load_dwordx4 a[52:55], v15, s[12:15], s47 offen offset:1024 sc1\ns_add_u32 s47, s35, 6\ns_and_b32 s52, s47, 1\ns_mul_i32 s52, s52, -14208\n"
"s_mul_i32 s48, s47, 14336\ns_add_u32 s48, s48, s52\ns_add_u32 s47, s48, 2048\nbuffer_load_dwordx4 a[72:75], v15, s[12:15], s47 offen sc1\n"
"buffer_load_dwordx4 a[76:79], v15, s[12:15], s47 offen offset:1024 sc1\ns_add_u32 s47, s48, 0\nbuffer_load_dwordx4 a[64:67], v15, s[12:15], s47 offen sc1\nbuffer_load_dwordx4 a[68:71], v15, s[12:15], s47 offen offset:1024 sc1\n"
"s_add_u32 s47, s48, 6144\nbuffer_load_dwordx4 a[88:91], v15, s[12:15], s47 offen sc1\nbuffer_load_dwordx4 a[92:95], v15, s[12:15], s47 offen offset:1024 sc1\ns_add_u32 s47, s48, 4096\n"
"buffer_load_dwordx4 a[80:83], v15, s[12:15], s47 offen sc1\nbuffer_load_dwordx4 a[84:87], v15, s[12:15], s47 offen offset:1024 sc1\ns_add_u32 s47, s50, 7168\nbuffer_load_dwordx4 v[72:75], v11, s[16:19], s50 offen\n"
"buffer_load_dwordx4 v[76:79], v12, s[16:19], s50 offen\nbuffer_load_dwordx4 v[80:83], v11, s[16:19], s47 offen\nbuffer_load_dwordx4 v[84:87], v12, s[16:19], s47 offen\ns_add_u32 s50, s50, 28672\n"
"s_waitcnt vmcnt(0)\ns_barrier\ns_memrealtime s[54:55]\ns_waitcnt lgkmcnt(0)\n"
"s_add_u32 s44, s54, 16\ns_mov_b32 s45, 8\n.p2align 6\ntop_h1:\n"
"v_mov_b32 v56, 0\nv_mov_b32 v57, 0\nv_mov_b32 v58, 0\nv_mov_b32 v59, 0\n"
"v_mov_b32 v60, 0\nv_mov_b32 v61, 0\nv_mov_b32 v62, 0\nv_mov_b32 v63, 0\n"
"v_mov_b32 v64, 0\nv_mov_b32 v65, 0\nv_mov_b32 v66, 0\nv_mov_b32 v67, 0\n"
"v_mov_b32 v128, 0\ns_mul_i32 s48, s45, 6\ns_and_b32 s48, s48, -4\ns_getpc_b64 s[60:61]\n"
"spc_h1:\ns_sub_u32 s48, sgo_h1-spc_h1, s48\ns_add_u32 s60, s60, s48\ns_addc_u32 s61, s61, 0\n"
"sf_h1:\ns_memrealtime s[54:55]\ns_nop 2\ns_memrealtime s[56:57]\n"
"s_nop 2\ns_memrealtime s[58:59]\nsg_h1:\ns_waitcnt lgkmcnt(4)\n"
"s_sub_u32 s47, s54, s44\ns_add_u32 s48, s47, 12\ns_cmp_ge_u32 s48, 12\ns_cbranch_scc1 so_h1\n"
"s_memrealtime s[54:55]\ns_waitcnt lgkmcnt(4)\ns_cmp_ge_u32 s56, s44\ns_cbranch_scc1 sx_h1\n"
"s_memrealtime s[56:57]\ns_waitcnt lgkmcnt(4)\ns_cmp_ge_u32 s58, s44\ns_cbranch_scc1 sx_h1\n"
"s_memrealtime s[58:59]\ns_waitcnt lgkmcnt(4)\ns_cmp_ge_u32 s54, s44\ns_cbranch_scc1 sx_h1\n"
"s_memrealtime s[54:55]\ns_waitcnt lgkmcnt(4)\ns_cmp_ge_u32 s56, s44\ns_cbranch_scc1 sx_h1\n"
"s_memrealtime s[56:57]\ns_waitcnt lgkmcnt(4)\ns_cmp_ge_u32 s58, s44\ns_cbranch_scc1 sx_h1\n"
"s_memrealtime s[58:59]\ns_waitcnt lgkmcnt(4)\ns_cmp_ge_u32 s54, s44\ns_cbranch_scc1 sx_h1\n"
"s_memrealtime s[54:55]\ns_waitcnt lgkmcnt(4)\ns_cmp_ge_u32 s56, s44\ns_cbranch_scc1 sx_h1\n"
"s_memrealtime s[56:57]\ns_waitcnt lgkmcnt(4)\ns_cmp_ge_u32 s58, s44\ns_cbranch_scc1 sx_h1\n"
"s_memrealtime s[58:59]\ns_waitcnt lgkmcnt(4)\ns_cmp_ge_u32 s54, s44\ns_cbranch_scc1 sx_h1\n"
"s_memrealtime s[54:55]\ns_waitcnt lgkmcnt(4)\ns_cmp_ge_u32 s56, s44\ns_cbranch_scc1 sx_h1\n"
"s_memrealtime s[56:57]\ns_waitcnt lgkmcnt(4)\ns_cmp_ge_u32 s58, s44\ns_cbranch_scc1 sx_h1\n"
"s_memrealtime s[58:59]\ns_waitcnt lgkmcnt(4)\ns_cmp_ge_u32 s54, s44\ns_cbranch_scc1 sx_h1\n"
"s_memrealtime s[54:55]\ns_waitcnt lgkmcnt(4)\ns_cmp_ge_u32 s56, s44\ns_cbranch_scc1 sx_h1\n"
"s_memrealtime s[56:57]\ns_waitcnt lgkmcnt(4)\ns_cmp_ge_u32 s58, s44\ns_cbranch_scc1 sx_h1\n"
"s_memrealtime s[58:59]\ns_waitcnt lgkmcnt(4)\ns_cmp_ge_u32 s54, s44\ns_cbranch_scc1 sx_h1\n"
"s_memrealtime s[54:55]\ns_waitcnt lgkmcnt(4)\ns_cmp_ge_u32 s56, s44\ns_cbranch_scc1 sx_h1\n"
"s_memrealtime s[56:57]\ns_branch sx_h1\nso_h1:\ns_waitcnt lgkmcnt(0)\n"
"s_cmp_lt_i32 s47, 0\ns_cbranch_scc1 sf_h1\ns_branch sgo_h1\nsx_h1:\n"
"s_setpc_b64 s[60:61]\ns_nop 0\ns_nop 0\ns_nop 0\n"
"s_nop 0\ns_nop 0\ns_nop 0\ns_nop 0\n"
"s_nop 0\ns_nop 0\ns_nop 0\ns_nop 0\n"
"s_nop 0\ns_nop 0\ns_nop 0\ns_nop 0\n"
"s_nop 0\ns_nop 0\ns_nop 0\ns_nop 0\n"
"s_nop 0\ns_nop 0\ns_nop 0\ns_nop 0\n"
"s_nop 0\nsgo_h1:\ns_nop 4\nbuffer_load_dwordx4 v[40:43], v10, s[8:11], s43 offen offset:1024 sc1\n"
"s_nop 4\nbuffer_load_dwordx4 v[44:47], v10, s[8:11], s43 offen offset:0 sc1\ns_nop 13\nbuffer_load_dwordx4 v[48:51], v10, s[8:11], s43 offen offset:3072 sc1\n"
"s_nop 13\nbuffer_load_dwordx4 v[52:55], v10, s[8:11], s43 offen offset:2048 sc1\nlanded_h1:\ns_waitcnt vmcnt(3)\n"
"v_smfmac_f32_16x16x64_bf16 v[56:59], v[40:43], a[8:15], v8\nv_max3_u32 v128, v128, v40, v41\nv_smfmac_f32_16x16x64_bf16 v[60:63], v[40:43], a[40:47], v8\nv_max3_u32 v128, v128, v42, v43\n"
"v_smfmac_f32_16x16x64_bf16 v[64:67], v[40:43], a[72:79], v8\ns_waitcnt vmcnt(2)\nv_smfmac_f32_16x16x64_bf16 v[56:59], v[44:47], a[0:7], v8\nv_max3_u32 v128, v128, v44, v45\n"
"v_smfmac_f32_16x16x64_bf16 v[60:63], v[44:47], a[32:39], v8\nv_max3_u32 v128, v128, v46, v47\nv_smfmac_f32_16x16x64_bf16 v[64:67], v[44:47], a[64:71], v8\ns_waitcnt vmcnt(1)\n"
"v_smfmac_f32_16x16x64_bf16 v[56:59], v[48:51], a[24:31], v8\nv_max3_u32 v128, v128, v48, v49\nv_smfmac_f32_16x16x64_bf16 v[60:63], v[48:51], a[56:63], v8\nv_max3_u32 v128, v128, v50, v51\n"
"v_smfmac_f32_16x16x64_bf16 v[64:67], v[48:51], a[88:95], v8\ns_waitcnt vmcnt(0)\nv_smfmac_f32_16x16x64_bf16 v[56:59], v[52:55], a[16:23], v8\nv_max3_u32 v128, v128, v52, v53\n"
"v_smfmac_f32_16x16x64_bf16 v[60:63], v[52:55], a[48:55], v8\nv_max3_u32 v128, v128, v54, v55\nv_smfmac_f32_16x16x64_bf16 v[64:67], v[52:55], a[80:87], v8\nv_add_f32 v129, v56, v57\n"
"v_add_f32 v130, v58, v59\nv_cvt_pk_bf16_f32 v112, v129, v130\nv_add_f32 v129, v60, v61\nv_add_f32 v130, v62, v63\n"
"v_cvt_pk_bf16_f32 v113, v129, v130\nv_cmp_eq_u32 vcc, -1, v128\nv_add_f32 v129, v64, v65\nv_add_f32 v130, v66, v67\n"
"v_cvt_pk_bf16_f32 v114, v129, v130\nds_write2_b64 v131, v[112:113], v[114:115] offset0:32 offset1:48\ns_cbranch_vccnz retry_h1\nv_mov_b32 v129, 0\n"
"s_nop 1\nv_mov_b32 v129, 0\ns_nop 1\ns_waitcnt lgkmcnt(0)\n"
"s_barrier\ns_nop 9\nds_read_b64_tr_b16 v[88:89], v132 offset:0\nds_read_b64_tr_b16 v[90:91], v132 offset:32\n"
"ds_read_b64_tr_b16 v[92:93], v132 offset:256\nds_read_b64_tr_b16 v[94:95], v132 offset:288\nds_read_b64_tr_b16 v[96:97], v132 offset:512\nds_read_b64_tr_b16 v[98:99], v132 offset:544\n"
"ds_read_b64_tr_b16 v[100:101], v132 offset:768\nds_read_b64_tr_b16 v[102:103], v132 offset:800\ns_waitcnt lgkmcnt(6)\nv_mfma_f32_16x16x32_bf16 v[104:107], v[88:91], v[72:75], 0\n"
"v_mfma_f32_16x16x32_bf16 v[108:111], v[88:91], v[80:83], 0\ns_waitcnt lgkmcnt(4)\nv_mfma_f32_16x16x32_bf16 v[104:107], v[92:95], v[76:79], v[104:107]\nv_mfma_f32_16x16x32_bf16 v[108:111], v[92:95], v[84:87], v[108:111]\n"
"s_waitcnt lgkmcnt(2)\nv_mfma_f32_16x16x32_bf16 v[104:107], v[96:99], v[72:75], v[104:107]\nv_mfma_f32_16x16x32_bf16 v[108:111], v[96:99], v[80:83], v[108:111]\ns_waitcnt lgkmcnt(0)\n"
"v_mfma_f32_16x16x32_bf16 v[104:107], v[100:103], v[76:79], v[104:107]\nv_mfma_f32_16x16x32_bf16 v[108:111], v[100:103], v[84:87], v[108:111]\ns_nop 7\nv_add_f32_dpp v124, v106, v104 row_ror:8 row_mask:0xf bank_mask:0x3\n"
"v_add_f32_dpp v124, v108, v110 row_ror:8 row_mask:0xf bank_mask:0xc\nv_add_f32_dpp v125, v107, v105 row_ror:8 row_mask:0xf bank_mask:0x3\nv_add_f32_dpp v125, v109, v111 row_ror:8 row_mask:0xf bank_mask:0xc\ns_add_u32 s47, s50, 7168\n"
"buffer_load_dwordx4 v[72:75], v11, s[16:19], s50 offen\nbuffer_load_dwordx4 v[76:79], v12, s[16:19], s50 offen\nbuffer_load_dwordx4 v[80:83], v11, s[16:19], s47 offen\nbuffer_load_dwordx4 v[84:87], v12, s[16:19], s47 offen\n"
"s_add_u32 s50, s50, 28672\nbuffer_store_dwordx2 v[124:125], v16, s[20:23], s51 offen sc1\ns_add_u32 s51, s51, 1024\nds_read_b32 v133, v134\n"
"v_xor_b32 v131, 8192, v131\nv_xor_b32 v132, 8192, v132\ns_waitcnt lgkmcnt(0)\nv_readfirstlane_b32 s47, v133\n"
"s_sub_u32 s47, s47, s40\ns_cmp_eq_u32 s47, 1\ns_cbranch_scc1 adopt_h1\nback_h1:\n"
"s_mul_i32 s48, s46, 8\ns_add_u32 s45, s45, s48\ns_add_u32 s45, s45, s32\ns_lshr_b32 s48, s45, 4\n"
"s_lshl_b32 s48, s48, 2\ns_add_u32 s44, s44, s48\ns_and_b32 s45, s45, 15\ns_add_u32 s52, s40, 1\n"
"s_bitcmp1_b64 s[28:29], s52\ns_subb_u32 s46, s46, 0\ns_add_u32 s40, s40, 1\ns_mov_b32 s43, s41\n"
"s_add_u32 s41, s41, 131072\ns_and_b32 s41, s41, 524287\ns_add_u32 s92, s92, 1\ns_xor_b32 s42, s41, 262144\n"
"s_waitcnt lgkmcnt(0)\ns_cmp_lt_u32 s40, 64\ns_cbranch_scc1 top_h1\ns_add_u32 s47, s34, 64\n"
"v_mov_b32 v135, s47\ns_waitcnt vmcnt(0)\nbuffer_store_dword v135, v18, s[8:11], 0 offen sc1\ns_branch end_h1\n"
"retry2_h1:\ns_sleep 3\nretry_h1:\ns_add_u32 s53, s53, 1\n"
"s_add_u32 s49, s49, 1\ns_cmp_gt_u32 s49, 65536\ns_cbranch_scc1 end_h1\nbuffer_load_dwordx4 v[40:43], v10, s[8:11], s43 offen offset:1024 sc1\n"
"buffer_load_dwordx4 v[44:47], v10, s[8:11], s43 offen offset:0 sc1\nbuffer_load_dwordx4 v[48:51], v10, s[8:11], s43 offen offset:3072 sc1\nbuffer_load_dwordx4 v[52:55], v10, s[8:11], s43 offen offset:2048 sc1\nv_mov_b32 v128, 0\n"
"s_add_u32 s47, s40, 1\nv_mov_b32 v133, s47\nds_write_b32 v134, v133\ns_waitcnt vmcnt(0)\n"
"v_max3_u32 v128, v128, v40, v41\nv_max3_u32 v128, v128, v42, v43\nv_max3_u32 v128, v128, v44, v45\nv_max3_u32 v128, v128, v46, v47\n"
"v_max3_u32 v128, v128, v48, v49\nv_max3_u32 v128, v128, v50, v51\nv_max3_u32 v128, v128, v52, v53\nv_max3_u32 v128, v128, v54, v55\n"
"v_cmp_eq_u32 vcc, -1, v128\nv_mov_b32 v56, 0\nv_mov_b32 v57, 0\nv_mov_b32 v58, 0\n"
"v_mov_b32 v59, 0\nv_mov_b32 v60, 0\nv_mov_b32 v61, 0\nv_mov_b32 v62, 0\n"
"v_mov_b32 v63, 0\nv_mov_b32 v64, 0\nv_mov_b32 v65, 0\nv_mov_b32 v66, 0\n"
"v_mov_b32 v67, 0\ns_cbranch_vccnz retry2_h1\ns_branch landed_h1\nadopt_h1:\n"
"buffer_load_dword v116, v28, s[8:11], 0 offen sc1\ns_lshl_b32 s52, s40, 26\ns_or_b32 s47, s52, 0x3ffffff\ns_waitcnt vmcnt(0)\n"
"v_cmp_ge_u32 vcc, s47, v116\ns_nop 1\nv_cndmask_b32 v133, 0, v116, vcc\ns_nop 1\n"
"v_max_u32_dpp v133, v133, v133 row_ror:8 row_mask:0xf bank_mask:0xf\ns_nop 1\nv_max_u32_dpp v133, v133, v133 row_ror:4 row_mask:0xf bank_mask:0xf\ns_nop 1\n"
"v_max_u32_dpp v133, v133, v133 row_ror:2 row_mask:0xf bank_mask:0xf\ns_nop 1\nv_max_u32_dpp v133, v133, v133 row_ror:1 row_mask:0xf bank_mask:0xf\ns_nop 0\n"
"v_readlane_b32 s47, v133, 0\nv_readlane_b32 s48, v133, 16\ns_max_u32 s48, s48, s47\ns_cmp_le_u32 s48, s52\n"
"s_cbranch_scc1 back_h1\ns_bfe_u32 s52, s48, 1179656\ns_add_u32 s52, s52, s30\ns_sub_u32 s45, s52, s44\n"
"s_and_b32 s45, s45, 3\ns_sub_u32 s44, s52, s45\ns_lshl_b32 s45, s45, 2\ns_and_b32 s46, s48, 255\n"
"s_mov_b32 s92, 0\ns_branch back_h1\nend_h1:\ns_branch fk_done\n"
"fk_h2:\ns_mov_b32 s40, 0\ns_mov_b32 s41, 0\ns_mov_b32 s42, 262144\n"
"s_mov_b32 s43, 393216\ns_mov_b32 s44, s30\ns_mov_b32 s45, 0\ns_bcnt1_i32_b64 s46, s[28:29]\n"
"s_mov_b32 s92, 0\ns_mov_b32 s49, 0\ns_mov_b32 s53, 0\ns_mov_b32 s50, s36\n"
"s_mov_b32 s51, s37\nv_mov_b32 v127, v13\nv_mov_b32 v128, v14\nv_mov_b32 v130, 24576\n"
"v_mov_b32 v111, 0\nv_mov_b32 v116, -1\nv_mov_b32 v117, -1\nv_mov_b32 v118, -1\n"
"v_mov_b32 v119, -1\ns_add_u32 s47, s35, 0\ns_and_b32 s52, s47, 1\ns_mul_i32 s52, s52, -14208\n"
"s_mul_i32 s48, s47, 14336\ns_add_u32 s48, s48, s52\ns_add_u32 s47, s48, 8192\nbuffer_load_dwordx4 a[0:3], v15, s[12:15], s47 offen sc1\n"
"buffer_load_dwordx4 a[4:7], v15, s[12:15], s47 offen offset:1024 sc1\ns_add_u32 s47, s48, 10240\nbuffer_load_dwordx4 a[8:11], v15, s[12:15], s47 offen sc1\nbuffer_load_dwordx4 a[12:15], v15, s[12:15], s47 offen offset:1024 sc1\n"
"s_add_u32 s47, s48, 12288\nbuffer_load_dwordx4 a[16:19], v15, s[12:15], s47 offen sc1\nbuffer_load_dwordx4 a[20:23], v15, s[12:15], s47 offen offset:1024 sc1\ns_add_u32 s47, s35, 1\n"
"s_and_b32 s52, s47, 1\ns_mul_i32 s52, s52, -14208\ns_mul_i32 s48, s47, 14336\ns_add_u32 s48, s48, s52\n"
"s_add_u32 s47, s48, 8192\nbuffer_load_dwordx4 a[32:35], v15, s[12:15], s47 offen sc1\nbuffer_load_dwordx4 a[36:39], v15, s[12:15], s47 offen offset:1024 sc1\ns_add_u32 s47, s48, 10240\n"
"buffer_load_dwordx4 a[40:43], v15, s[12:15], s47 offen sc1\nbuffer_load_dwordx4 a[44:47], v15, s[12:15], s47 offen offset:1024 sc1\ns_add_u32 s47, s48, 12288\nbuffer_load_dwordx4 a[48:51], v15, s[12:15], s47 offen sc1\n"
"buffer_load_dwordx4 a[52:55], v15, s[12:15], s47 offen offset:1024 sc1\ns_add_u32 s47, s35, 2\ns_and_b32 s52, s47, 1\ns_mul_i32 s52, s52, -14208\n"
"s_mul_i32 s48, s47, 14336\ns_add_u32 s48, s48, s52\ns_add_u32 s47, s48, 8192\nbuffer_load_dwordx4 a[64:67], v15, s[12:15], s47 offen sc1\n"
"buffer_load_dwordx4 a[68:71], v15, s[12:15], s47 offen offset:1024 sc1\ns_add_u32 s47, s48, 10240\nbuffer_load_dwordx4 a[72:75], v15, s[12:15], s47 offen sc1\nbuffer_load_dwordx4 a[76:79], v15, s[12:15], s47 offen offset:1024 sc1\n"
"s_add_u32 s47, s48, 12288\nbuffer_load_dwordx4 a[80:83], v15, s[12:15], s47 offen sc1\nbuffer_load_dwordx4 a[84:87], v15, s[12:15], s47 offen offset:1024 sc1\ns_add_u32 s47, s35, 3\n"
"s_and_b32 s52, s47, 1\ns_mul_i32 s52, s52, -14208\ns_mul_i32 s48, s47, 14336\ns_add_u32 s48, s48, s52\n"
"s_add_u32 s47, s48, 8192\nbuffer_load_dwordx4 a[96:99], v15, s[12:15], s47 offen sc1\nbuffer_load_dwordx4 a[100:103], v15, s[12:15], s47 offen offset:1024 sc1\ns_add_u32 s47, s48, 10240\n"
"buffer_load_dwordx4 a[104:107], v15, s[12:15], s47 offen sc1\nbuffer_load_dwordx4 a[108:111], v15, s[12:15], s47 offen offset:1024 sc1\ns_add_u32 s47, s48, 12288\nbuffer_load_dwordx4 a[112:115], v15, s[12:15], s47 offen sc1\n"
"buffer_load_dwordx4 a[116:119], v15, s[12:15], s47 offen offset:1024 sc1\ns_waitcnt vmcnt(0)\ns_barrier\ns_memrealtime s[54:55]\n"
"s_waitcnt lgkmcnt(0)\ns_add_u32 s44, s54, 16\ns_mov_b32 s45, 8\n.p2align 6\n"
"top_h2:\nv_mov_b32 v52, 0\nv_mov_b32 v53, 0\nv_mov_b32 v54, 0\n"
"v_mov_b32 v55, 0\nv_mov_b32 v56, 0\nv_mov_b32 v57, 0\nv_mov_b32 v58, 0\n"
"v_mov_b32 v59, 0\nv_mov_b32 v60, 0\nv_mov_b32 v61, 0\nv_mov_b32 v62, 0\n"
"v_mov_b32 v63, 0\nv_mov_b32 v64, 0\nv_mov_b32 v65, 0\nv_mov_b32 v66, 0\n"
"v_mov_b32 v67, 0\nv_mov_b32 v124, 0\ns_mul_i32 s48, s45, 6\ns_and_b32 s48, s48, -4\n"
"s_getpc_b64 s[60:61]\nspc_h2:\ns_sub_u32 s48, sgo_h2-spc_h2, s48\ns_add_u32 s60, s60, s48\n"
"s_addc_u32 s61, s61, 0\nsf_h2:\ns_memrealtime s[54:55]\ns_nop 2\n"
"s_memrealtime s[56:57]\ns_nop 2\ns_memrealtime s[58:59]\nsg_h2:\n"
"s_waitcnt lgkmcnt(4)\ns_sub_u32 s47, s54, s44\ns_add_u32 s48, s47, 12\ns_cmp_ge_u32 s48, 12\n"
"s_cbranch_scc1 so_h2\ns_memrealtime s[54:55]\ns_waitcnt lgkmcnt(4)\ns_cmp_ge_u32 s56, s44\n"
"s_cbranch_scc1 sx_h2\ns_memrealtime s[56:57]\ns_waitcnt lgkmcnt(4)\ns_cmp_ge_u32 s58, s44\n"
"s_cbranch_scc1 sx_h2\ns_memrealtime s[58:59]\ns_waitcnt lgkmcnt(4)\ns_cmp_ge_u32 s54, s44\n"
"s_cbranch_scc1 sx_h2\ns_memrealtime s[54:55]\ns_waitcnt lgkmcnt(4)\ns_cmp_ge_u32 s56, s44\n"
"s_cbranch_scc1 sx_h2\ns_memrealtime s[56:57]\ns_waitcnt lgkmcnt(4)\ns_cmp_ge_u32 s58, s44\n"
"s_cbranch_scc1 sx_h2\ns_memrealtime s[58:59]\ns_waitcnt lgkmcnt(4)\ns_cmp_ge_u32 s54, s44\n"
"s_cbranch_scc1 sx_h2\ns_memrealtime s[54:55]\ns_waitcnt lgkmcnt(4)\ns_cmp_ge_u32 s56, s44\n"
"s_cbranch_scc1 sx_h2\ns_memrealtime s[56:57]\ns_waitcnt lgkmcnt(4)\ns_cmp_ge_u32 s58, s44\n"
"s_cbranch_scc1 sx_h2\ns_memrealtime s[58:59]\ns_waitcnt lgkmcnt(4)\ns_cmp_ge_u32 s54, s44\n"
"s_cbranch_scc1 sx_h2\ns_memrealtime s[54:55]\ns_waitcnt lgkmcnt(4)\ns_cmp_ge_u32 s56, s44\n"
"s_cbranch_scc1 sx_h2\ns_memrealtime s[56:57]\ns_waitcnt lgkmcnt(4)\ns_cmp_ge_u32 s58, s44\n"
"s_cbranch_scc1 sx_h2\ns_memrealtime s[58:59]\ns_waitcnt lgkmcnt(4)\ns_cmp_ge_u32 s54, s44\n"
"s_cbranch_scc1 sx_h2\ns_memrealtime s[54:55]\ns_waitcnt lgkmcnt(4)\ns_cmp_ge_u32 s56, s44\n"
"s_cbranch_scc1 sx_h2\ns_memrealtime s[56:57]\ns_waitcnt lgkmcnt(4)\ns_cmp_ge_u32 s58, s44\n"
"s_cbranch_scc1 sx_h2\ns_memrealtime s[58:59]\ns_waitcnt lgkmcnt(4)\ns_cmp_ge_u32 s54, s44\n"
"s_cbranch_scc1 sx_h2\ns_memrealtime s[54:55]\ns_waitcnt lgkmcnt(4)\ns_cmp_ge_u32 s56, s44\n"
"s_cbranch_scc1 sx_h2\ns_memrealtime s[56:57]\ns_branch sx_h2\nso_h2:\n"
"s_waitcnt lgkmcnt(0)\ns_cmp_lt_i32 s47, 0\ns_cbranch_scc1 sf_h2\ns_branch sgo_h2\n"
"sx_h2:\ns_setpc_b64 s[60:61]\ns_nop 0\ns_nop 0\n"
"s_nop 0\ns_nop 0\ns_nop 0\ns_nop 0\n"
"s_nop 0\ns_nop 0\ns_nop 0\ns_nop 0\n"
"s_nop 0\ns_nop 0\ns_nop 0\ns_nop 0\n"
"s_nop 0\ns_nop 0\ns_nop 0\ns_nop 0\n"
"s_nop 0\ns_nop 0\ns_nop 0\ns_nop 0\n"
"s_nop 0\ns_nop 0\nsgo_h2:\ns_nop 8\n"
"buffer_load_dwordx4 v[40:43], v10, s[8:11], s43 offen offset:0 sc1\ns_nop 13\nbuffer_load_dwordx4 v[44:47], v10, s[8:11], s43 offen offset:1024 sc1\ns_nop 13\n"
"buffer_load_dwordx4 v[48:51], v10, s[8:11], s43 offen offset:2048 sc1\nlanded_h2:\ns_waitcnt vmcnt(2)\nv_smfmac_f32_16x16x64_bf16 v[52:55], v[40:43], a[0:7], v8\n"
"v_max3_u32 v124, v124, v40, v41\nv_smfmac_f32_16x16x64_bf16 v[56:59], v[40:43], a[32:39], v8\nv_max3_u32 v124, v124, v42, v43\nv_smfmac_f32_16x16x64_bf16 v[60:63], v[40:43], a[64:71], v8\n"
"v_smfmac_f32_16x16x64_bf16 v[64:67], v[40:43], a[96:103], v8\ns_waitcnt vmcnt(1)\nv_smfmac_f32_16x16x64_bf16 v[52:55], v[44:47], a[8:15], v8\nv_max3_u32 v124, v124, v44, v45\n"
"v_smfmac_f32_16x16x64_bf16 v[56:59], v[44:47], a[40:47], v8\nv_max3_u32 v124, v124, v46, v47\nv_smfmac_f32_16x16x64_bf16 v[60:63], v[44:47], a[72:79], v8\nv_smfmac_f32_16x16x64_bf16 v[64:67], v[44:47], a[104:111], v8\n"
"s_waitcnt vmcnt(0)\nv_smfmac_f32_16x16x64_bf16 v[52:55], v[48:51], a[16:23], v8\nv_max3_u32 v124, v124, v48, v49\nv_smfmac_f32_16x16x64_bf16 v[56:59], v[48:51], a[48:55], v8\n"
"v_max3_u32 v124, v124, v50, v51\nv_smfmac_f32_16x16x64_bf16 v[60:63], v[48:51], a[80:87], v8\nv_cmp_eq_u32 vcc, -1, v124\nv_smfmac_f32_16x16x64_bf16 v[64:67], v[48:51], a[112:119], v8\n"
"v_add_f32 v125, v52, v53\nv_add_f32 v126, v54, v55\nv_cvt_pk_bf16_f32 v108, v125, v126\nv_add_f32 v125, v56, v57\n"
"v_add_f32 v126, v58, v59\nv_cvt_pk_bf16_f32 v109, v125, v126\nv_add_f32 v125, v60, v61\nv_add_f32 v126, v62, v63\n"
"v_cvt_pk_bf16_f32 v110, v125, v126\nv_add_f32 v125, v64, v65\nv_add_f32 v126, v66, v67\nv_cvt_pk_bf16_f32 v111, v125, v126\n"
"ds_write2_b64 v127, v[108:109], v[110:111] offset0:0 offset1:16\ns_cbranch_vccnz retry_h2\nv_mov_b32 v125, 0\ns_nop 1\n"
"v_mov_b32 v125, 0\ns_nop 1\ns_waitcnt lgkmcnt(0)\ns_barrier\n"
"v_mov_b32 v125, 0\ns_nop 1\nv_mov_b32 v125, 0\ns_nop 1\n"
"v_mov_b32 v125, 0\ns_nop 1\nv_mov_b32 v125, 0\ns_nop 1\n"
"v_mov_b32 v125, 0\ns_nop 1\nv_mov_b32 v125, 0\ns_nop 1\n"
"v_mov_b32 v125, 0\ns_nop 1\nv_mov_b32 v125, 0\ns_nop 1\n"
"v_mov_b32 v125, 0\ns_nop 1\nv_mov_b32 v125, 0\ns_nop 1\n"
"v_mov_b32 v125, 0\ns_nop 1\nv_mov_b32 v125, 0\ns_nop 1\n"
"v_mov_b32 v125, 0\ns_nop 1\nv_mov_b32 v125, 0\ns_nop 1\n"
"v_mov_b32 v125, 0\ns_nop 1\nv_mov_b32 v125, 0\ns_nop 1\n"
"v_mov_b32 v125, 0\ns_nop 1\nv_mov_b32 v125, 0\ns_nop 1\n"
"v_mov_b32 v125, 0\ns_nop 1\nv_mov_b32 v125, 0\ns_nop 1\n"
"v_mov_b32 v125, 0\ns_nop 1\nv_mov_b32 v125, 0\nbuffer_store_dwordx4 v[116:119], v17, s[8:11], s42 offen sc1\n"
"ds_read_b32 v129, v130\nv_xor_b32 v127, 8192, v127\nv_xor_b32 v128, 8192, v128\ns_waitcnt lgkmcnt(0)\n"
"v_readfirstlane_b32 s47, v129\ns_sub_u32 s47, s47, s40\ns_cmp_eq_u32 s47, 1\ns_cbranch_scc1 adopt_h2\n"
"back_h2:\ns_mul_i32 s48, s46, 8\ns_add_u32 s45, s45, s48\ns_add_u32 s45, s45, s32\n"
"s_lshr_b32 s48, s45, 4\ns_lshl_b32 s48, s48, 2\ns_add_u32 s44, s44, s48\ns_and_b32 s45, s45, 15\n"
"s_add_u32 s52, s40, 1\ns_bitcmp1_b64 s[28:29], s52\ns_subb_u32 s46, s46, 0\ns_add_u32 s40, s40, 1\n"
"s_mov_b32 s43, s41\ns_add_u32 s41, s41, 131072\ns_and_b32 s41, s41, 524287\ns_add_u32 s92, s92, 1\n"
"s_xor_b32 s42, s41, 262144\ns_waitcnt lgkmcnt(0)\ns_cmp_lt_u32 s40, 64\ns_cbranch_scc1 top_h2\n"
"s_branch end_h2\nretry2_h2:\ns_sleep 3\nretry_h2:\n"
"s_add_u32 s53, s53, 1\ns_add_u32 s49, s49, 1\ns_cmp_gt_u32 s49, 65536\ns_cbranch_scc1 end_h2\n"
"buffer_load_dwordx4 v[40:43], v10, s[8:11], s43 offen offset:0 sc1\nbuffer_load_dwordx4 v[44:47], v10, s[8:11], s43 offen offset:1024 sc1\nbuffer_load_dwordx4 v[48:51], v10, s[8:11], s43 offen offset:2048 sc1\nv_mov_b32 v124, 0\n"
"s_add_u32 s47, s40, 1\nv_mov_b32 v129, s47\nds_write_b32 v130, v129\ns_waitcnt vmcnt(0)\n"
"v_max3_u32 v124, v124, v40, v41\nv_max3_u32 v124, v124, v42, v43\nv_max3_u32 v124, v124, v44, v45\nv_max3_u32 v124, v124, v46, v47\n"
"v_max3_u32 v124, v124, v48, v49\nv_max3_u32 v124, v124, v50, v51\nv_cmp_eq_u32 vcc, -1, v124\nv_mov_b32 v52, 0\n"
"v_mov_b32 v53, 0\nv_mov_b32 v54, 0\nv_mov_b32 v55, 0\nv_mov_b32 v56, 0\n"
"v_mov_b32 v57, 0\nv_mov_b32 v58, 0\nv_mov_b32 v59, 0\nv_mov_b32 v60, 0\n"
"v_mov_b32 v61, 0\nv_mov_b32 v62, 0\nv_mov_b32 v63, 0\nv_mov_b32 v64, 0\n"
"v_mov_b32 v65, 0\nv_mov_b32 v66, 0\nv_mov_b32 v67, 0\ns_cbranch_vccnz retry2_h2\n"
"s_branch landed_h2\nadopt_h2:\nbuffer_load_dword v112, v28, s[8:11], 0 offen sc1\ns_lshl_b32 s52, s40, 26\n"
"s_or_b32 s47, s52, 0x3ffffff\ns_waitcnt vmcnt(0)\nv_cmp_ge_u32 vcc, s47, v112\ns_nop 1\n"
"v_cndmask_b32 v129, 0, v112, vcc\ns_nop 1\nv_max_u32_dpp v129, v129, v129 row_ror:8 row_mask:0xf bank_mask:0xf\ns_nop 1\n"
"v_max_u32_dpp v129, v129, v129 row_ror:4 row_mask:0xf bank_mask:0xf\ns_nop 1\nv_max_u32_dpp v129, v129, v129 row_ror:2 row_mask:0xf bank_mask:0xf\ns_nop 1\n"
"v_max_u32_dpp v129, v129, v129 row_ror:1 row_mask:0xf bank_mask:0xf\ns_nop 0\nv_readlane_b32 s47, v129, 0\nv_readlane_b32 s48, v129, 16\n"
"s_max_u32 s48, s48, s47\ns_cmp_le_u32 s48, s52\ns_cbranch_scc1 back_h2\ns_bfe_u32 s52, s48, 1179656\n"
"s_add_u32 s52, s52, s30\ns_sub_u32 s45, s52, s44\ns_and_b32 s45, s45, 3\ns_sub_u32 s44, s52, s45\n"
"s_lshl_b32 s45, s45, 2\ns_and_b32 s46, s48, 255\ns_mov_b32 s92, 0\ns_branch back_h2\n"
"end_h2:\ns_branch fk_done\nfk_h3:\ns_mov_b32 s40, 0\n"
"s_mov_b32 s41, 0\ns_mov_b32 s42, 262144\ns_mov_b32 s43, 393216\ns_mov_b32 s44, s30\n"
"s_mov_b32 s45, 0\ns_bcnt1_i32_b64 s46, s[28:29]\ns_mov_b32 s92, 0\ns_mov_b32 s49, 0\n"
"s_mov_b32 s53, 0\ns_mov_b32 s50, s36\ns_mov_b32 s51, s37\nv_mov_b32 v127, v13\n"
"v_mov_b32 v128, v14\nv_mov_b32 v130, 24576\nv_mov_b32 v111, 0\nv_mov_b32 v116, -1\n"
"v_mov_b32 v117, -1\nv_mov_b32 v118, -1\nv_mov_b32 v119, -1\ns_add_u32 s47, s35, 4\n"
"s_and_b32 s52, s47, 1\ns_mul_i32 s52, s52, -14208\ns_mul_i32 s48, s47, 14336\ns_add_u32 s48, s48, s52\n"
"s_add_u32 s47, s48, 10240\nbuffer_load_dwordx4 a[8:11], v15, s[12:15], s47 offen sc1\nbuffer_load_dwordx4 a[12:15], v15, s[12:15], s47 offen offset:1024 sc1\ns_add_u32 s47, s48, 8192\n"
"buffer_load_dwordx4 a[0:3], v15, s[12:15], s47 offen sc1\nbuffer_load_dwordx4 a[4:7], v15, s[12:15], s47 offen offset:1024 sc1\ns_add_u32 s47, s48, 12288\nbuffer_load_dwordx4 a[16:19], v15, s[12:15], s47 offen sc1\n"
"buffer_load_dwordx4 a[20:23], v15, s[12:15], s47 offen offset:1024 sc1\ns_add_u32 s47, s35, 5\ns_and_b32 s52, s47, 1\ns_mul_i32 s52, s52, -14208\n"
"s_mul_i32 s48, s47, 14336\ns_add_u32 s48, s48, s52\ns_add_u32 s47, s48, 10240\nbuffer_load_dwordx4 a[40:43], v15, s[12:15], s47 offen sc1\n"
"buffer_load_dwordx4 a[44:47], v15, s[12:15], s47 offen offset:1024 sc1\ns_add_u32 s47, s48, 8192\nbuffer_load_dwordx4 a[32:35], v15, s[12:15], s47 offen sc1\nbuffer_load_dwordx4 a[36:39], v15, s[12:15], s47 offen offset:1024 sc1\n"
"s_add_u32 s47, s48, 12288\nbuffer_load_dwordx4 a[48:51], v15, s[12:15], s47 offen sc1\nbuffer_load_dwordx4 a[52:55], v15, s[12:15], s47 offen offset:1024 sc1\ns_add_u32 s47, s35, 6\n"
"s_and_b32 s52, s47, 1\ns_mul_i32 s52, s52, -14208\ns_mul_i32 s48, s47, 14336\ns_add_u32 s48, s48, s52\n"
"s_add_u32 s47, s48, 10240\nbuffer_load_dwordx4 a[72:75], v15, s[12:15], s47 offen sc1\nbuffer_load_dwordx4 a[76:79], v15, s[12:15], s47 offen offset:1024 sc1\ns_add_u32 s47, s48, 8192\n"
"buffer_load_dwordx4 a[64:67], v15, s[12:15], s47 offen sc1\nbuffer_load_dwordx4 a[68:71], v15, s[12:15], s47 offen offset:1024 sc1\ns_add_u32 s47, s48, 12288\nbuffer_load_dwordx4 a[80:83], v15, s[12:15], s47 offen sc1\n"
"buffer_load_dwordx4 a[84:87], v15, s[12:15], s47 offen offset:1024 sc1\nv_lshlrev_b32 v114, 2, v9\ns_mov_b32 s52, 1064960\nbuffer_load_dword v112, v114, s[8:11], s52 offen sc1\n"
"s_add_u32 s52, s52, 4096\nbuffer_load_dword v113, v114, s[8:11], s52 offen sc1\ns_waitcnt vmcnt(0)\nar_h3:\n"
"v_xor_b32 v112, s34, v112\nv_xor_b32 v113, s34, v113\nv_or_b32 v112, v112, v113\nv_cmp_ne_u32 vcc, 0, v112\n"
"s_cbranch_vccz arok_h3\ns_add_u32 s53, s53, 0\ns_add_u32 s49, s49, 1\ns_cmp_gt_u32 s49, 65536\n"
"s_cbranch_scc1 end_h3\ns_sleep 2\nv_lshlrev_b32 v114, 2, v9\ns_mov_b32 s52, 1064960\n"
"buffer_load_dword v112, v114, s[8:11], s52 offen sc1\ns_add_u32 s52, s52, 4096\nbuffer_load_dword v113, v114, s[8:11], s52 offen sc1\ns_waitcnt vmcnt(0)\n"
"s_branch ar_h3\narok_h3:\ns_barrier\ns_memrealtime s[54:55]\n"
"s_waitcnt lgkmcnt(0)\ns_add_u32 s44, s54, 16\ns_mov_b32 s45, 8\n.p2align 6\n"
"top_h3:\nv_mov_b32 v52, 0\nv_mov_b32 v53, 0\nv_mov_b32 v54, 0\n"
"v_mov_b32 v55, 0\nv_mov_b32 v56, 0\nv_mov_b32 v57, 0\nv_mov_b32 v58, 0\n"
"v_mov_b32 v59, 0\nv_mov_b32 v60, 0\nv_mov_b32 v61, 0\nv_mov_b32 v62, 0\n"
"v_mov_b32 v63, 0\nv_mov_b32 v124, 0\ns_mul_i32 s48, s45, 6\ns_and_b32 s48, s48, -4\n"
"s_getpc_b64 s[60:61]\nspc_h3:\ns_sub_u32 s48, sgo_h3-spc_h3, s48\ns_add_u32 s60, s60, s48\n"
"s_addc_u32 s61, s61, 0\nsf_h3:\ns_memrealtime s[54:55]\ns_nop 2\n"
"s_memrealtime s[56:57]\ns_nop 2\ns_memrealtime s[58:59]\nsg_h3:\n"
"s_waitcnt lgkmcnt(4)\ns_sub_u32 s47, s54, s44\ns_add_u32 s48, s47, 12\ns_cmp_ge_u32 s48, 12\n"
"s_cbranch_scc1 so_h3\ns_memrealtime s[54:55]\ns_waitcnt lgkmcnt(4)\ns_cmp_ge_u32 s56, s44\n"
"s_cbranch_scc1 sx_h3\ns_memrealtime s[56:57]\ns_waitcnt lgkmcnt(4)\ns_cmp_ge_u32 s58, s44\n"
"s_cbranch_scc1 sx_h3\ns_memrealtime s[58:59]\ns_waitcnt lgkmcnt(4)\ns_cmp_ge_u32 s54, s44\n"
"s_cbranch_scc1 sx_h3\ns_memrealtime s[54:55]\ns_waitcnt lgkmcnt(4)\ns_cmp_ge_u32 s56, s44\n"
"s_cbranch_scc1 sx_h3\ns_memrealtime s[56:57]\ns_waitcnt lgkmcnt(4)\ns_cmp_ge_u32 s58, s44\n"
"s_cbranch_scc1 sx_h3\ns_memrealtime s[58:59]\ns_waitcnt lgkmcnt(4)\ns_cmp_ge_u32 s54, s44\n"
"s_cbranch_scc1 sx_h3\ns_memrealtime s[54:55]\ns_waitcnt lgkmcnt(4)\ns_cmp_ge_u32 s56, s44\n"
"s_cbranch_scc1 sx_h3\ns_memrealtime s[56:57]\ns_waitcnt lgkmcnt(4)\ns_cmp_ge_u32 s58, s44\n"
"s_cbranch_scc1 sx_h3\ns_memrealtime s[58:59]\ns_waitcnt lgkmcnt(4)\ns_cmp_ge_u32 s54, s44\n"
"s_cbranch_scc1 sx_h3\ns_memrealtime s[54:55]\ns_waitcnt lgkmcnt(4)\ns_cmp_ge_u32 s56, s44\n"
"s_cbranch_scc1 sx_h3\ns_memrealtime s[56:57]\ns_waitcnt lgkmcnt(4)\ns_cmp_ge_u32 s58, s44\n"
"s_cbranch_scc1 sx_h3\ns_memrealtime s[58:59]\ns_waitcnt lgkmcnt(4)\ns_cmp_ge_u32 s54, s44\n"
"s_cbranch_scc1 sx_h3\ns_memrealtime s[54:55]\ns_waitcnt lgkmcnt(4)\ns_cmp_ge_u32 s56, s44\n"
"s_cbranch_scc1 sx_h3\ns_memrealtime s[56:57]\ns_waitcnt lgkmcnt(4)\ns_cmp_ge_u32 s58, s44\n"
"s_cbranch_scc1 sx_h3\ns_memrealtime s[58:59]\ns_waitcnt lgkmcnt(4)\ns_cmp_ge_u32 s54, s44\n"
"s_cbranch_scc1 sx_h3\ns_memrealtime s[54:55]\ns_waitcnt lgkmcnt(4)\ns_cmp_ge_u32 s56, s44\n"
"s_cbranch_scc1 sx_h3\ns_memrealtime s[56:57]\ns_branch sx_h3\nso_h3:\n"
"s_waitcnt lgkmcnt(0)\ns_cmp_lt_i32 s47, 0\ns_cbranch_scc1 sf_h3\ns_branch sgo_h3\n"
"sx_h3:\ns_setpc_b64 s[60:61]\ns_nop 0\ns_nop 0\n"
"s_nop 0\ns_nop 0\ns_nop 0\ns_nop 0\n"
"s_nop 0\ns_nop 0\ns_nop 0\ns_nop 0\n"
"s_nop 0\ns_nop 0\ns_nop 0\ns_nop 0\n"
"s_nop 0\ns_nop 0\ns_nop 0\ns_nop 0\n"
"s_nop 0\ns_nop 0\ns_nop 0\ns_nop 0\n"
"s_nop 0\ns_nop 0\nsgo_h3:\ns_nop 15\n"
"s_nop 2\nbuffer_load_dwordx4 v[40:43], v10, s[8:11], s43 offen offset:1024 sc1\ns_nop 13\nbuffer_load_dwordx4 v[44:47], v10, s[8:11], s43 offen offset:0 sc1\n"
"s_nop 13\nbuffer_load_dwordx4 v[48:51], v10, s[8:11], s43 offen offset:2048 sc1\ns_nop 3\nbuffer_load_dwordx4 v[68:71], v11, s[8:11], s43 offen sc1\n"
"buffer_load_dwordx4 v[72:75], v12, s[8:11], s43 offen sc1\nv_mov_b32 v125, 0\ns_nop 1\nv_mov_b32 v125, 0\n"
"s_nop 1\nv_mov_b32 v125, 0\ns_nop 1\nv_mov_b32 v125, 0\n"
"s_nop 1\nv_mov_b32 v125, 0\ns_nop 1\nv_mov_b32 v125, 0\n"
"s_nop 1\nv_mov_b32 v125, 0\ns_nop 1\nv_mov_b32 v125, 0\n"
"s_nop 1\nv_mov_b32 v125, 0\ns_nop 1\nv_mov_b32 v125, 0\n"
"s_nop 1\nv_mov_b32 v125, 0\ns_nop 1\nv_mov_b32 v125, 0\n"
"s_nop 1\nv_mov_b32 v125, 0\ns_nop 1\nv_mov_b32 v125, 0\n"
"s_nop 1\nv_mov_b32 v125, 0\ns_nop 1\nv_mov_b32 v125, 0\n"
"s_nop 1\nv_mov_b32 v125, 0\ns_nop 0\nlanded_h3:\n"
"s_waitcnt vmcnt(4)\nv_smfmac_f32_16x16x64_bf16 v[52:55], v[40:43], a[8:15], v8\nv_max3_u32 v124, v124, v40, v41\nv_smfmac_f32_16x16x64_bf16 v[56:59], v[40:43], a[40:47], v8\n"
"v_max3_u32 v124, v124, v42, v43\nv_smfmac_f32_16x16x64_bf16 v[60:63], v[40:43], a[72:79], v8\ns_waitcnt vmcnt(3)\nv_smfmac_f32_16x16x64_bf16 v[52:55], v[44:47], a[0:7], v8\n"
"v_max3_u32 v124, v124, v44, v45\nv_smfmac_f32_16x16x64_bf16 v[56:59], v[44:47], a[32:39], v8\nv_max3_u32 v124, v124, v46, v47\nv_smfmac_f32_16x16x64_bf16 v[60:63], v[44:47], a[64:71], v8\n"
"s_waitcnt vmcnt(2)\nv_smfmac_f32_16x16x64_bf16 v[52:55], v[48:51], a[16:23], v8\nv_max3_u32 v124, v124, v48, v49\nv_smfmac_f32_16x16x64_bf16 v[56:59], v[48:51], a[48:55], v8\n"
"v_max3_u32 v124, v124, v50, v51\nv_smfmac_f32_16x16x64_bf16 v[60:63], v[48:51], a[80:87], v8\nv_add_f32 v125, v52, v53\nv_add_f32 v126, v54, v55\n"
"v_cvt_pk_bf16_f32 v108, v125, v126\nv_add_f32 v125, v56, v57\nv_add_f32 v126, v58, v59\nv_cvt_pk_bf16_f32 v109, v125, v126\n"
"v_cmp_eq_u32 vcc, -1, v124\nv_add_f32 v125, v60, v61\nv_add_f32 v126, v62, v63\nv_cvt_pk_bf16_f32 v110, v125, v126\n"
"ds_write2_b64 v127, v[108:109], v[110:111] offset0:32 offset1:48\ns_cbranch_vccnz retry_h3\nv_mov_b32 v125, 0\ns_nop 1\n"
"v_mov_b32 v125, 0\ns_nop 1\ns_waitcnt lgkmcnt(0)\ns_barrier\n"
"ds_read_b64_tr_b16 v[84:85], v128 offset:0\nds_read_b64_tr_b16 v[86:87], v128 offset:64\nds_read_b64_tr_b16 v[88:89], v128 offset:256\nds_read_b64_tr_b16 v[90:91], v128 offset:320\n"
"ds_read_b64_tr_b16 v[92:93], v128 offset:512\nds_read_b64_tr_b16 v[94:95], v128 offset:576\nds_read_b64_tr_b16 v[96:97], v128 offset:768\nds_read_b64_tr_b16 v[98:99], v128 offset:832\n"
"mb_h3:\ns_waitcnt vmcnt(0)\nv_max3_u32 v124, v124, v68, v69\nv_max3_u32 v124, v124, v70, v71\n"
"v_max3_u32 v124, v124, v72, v73\nv_max3_u32 v124, v124, v74, v75\nv_cmp_eq_u32 vcc, -1, v124\ns_waitcnt lgkmcnt(6)\n"
"v_mfma_f32_16x16x32_bf16 v[100:103], v[84:87], v[68:71], 0\ns_waitcnt lgkmcnt(4)\nv_mfma_f32_16x16x32_bf16 v[100:103], v[88:91], v[72:75], v[100:103]\ns_waitcnt lgkmcnt(2)\n"
"v_mfma_f32_16x16x32_bf16 v[100:103], v[92:95], v[68:71], v[100:103]\ns_waitcnt lgkmcnt(0)\nv_mfma_f32_16x16x32_bf16 v[100:103], v[96:99], v[72:75], v[100:103]\ns_cbranch_vccnz mbr_h3\n"
"s_nop 6\nv_add_f32_dpp v120, v102, v100 row_ror:8 row_mask:0xf bank_mask:0x3\nv_add_f32_dpp v120, v100, v102 row_ror:8 row_mask:0xf bank_mask:0xc\nv_add_f32_dpp v121, v103, v101 row_ror:8 row_mask:0xf bank_mask:0x3\n"
"v_add_f32_dpp v121, v101, v103 row_ror:8 row_mask:0xf bank_mask:0xc\nv_cvt_pk_f16_f32 v125, v120, v121\nbuffer_store_dword v125, v16, s[8:11], s41 offen sc1\ns_nop 15\n"
"s_nop 15\ns_nop 15\nbuffer_store_dwordx2 v[120:121], v17, s[8:11], s41 offen sc1\nds_read_b32 v129, v130\n"
"v_xor_b32 v127, 8192, v127\nv_xor_b32 v128, 8192, v128\ns_waitcnt lgkmcnt(0)\nv_readfirstlane_b32 s47, v129\n"
"s_sub_u32 s47, s47, s40\ns_cmp_eq_u32 s47, 1\ns_cbranch_scc1 adopt_h3\nback_h3:\n"
"s_mul_i32 s48, s46, 8\ns_add_u32 s45, s45, s48\ns_add_u32 s45, s45, s32\ns_lshr_b32 s48, s45, 4\n"
"s_lshl_b32 s48, s48, 2\ns_add_u32 s44, s44, s48\ns_and_b32 s45, s45, 15\ns_add_u32 s52, s40, 1\n"
"s_bitcmp1_b64 s[28:29], s52\ns_subb_u32 s46, s46, 0\ns_add_u32 s40, s40, 1\ns_mov_b32 s43, s41\n"
"s_add_u32 s41, s41, 131072\ns_and_b32 s41, s41, 524287\ns_add_u32 s92, s92, 1\ns_xor_b32 s42, s41, 262144\n"
"s_waitcnt lgkmcnt(0)\ns_cmp_lt_u32 s40, 64\ns_cbranch_scc1 top_h3\ns_branch end_h3\n"
"retry2_h3:\ns_sleep 3\nretry_h3:\ns_add_u32 s53, s53, 1\n"
"s_add_u32 s49, s49, 1\ns_cmp_gt_u32 s49, 65536\ns_cbranch_scc1 end_h3\nbuffer_load_dwordx4 v[40:43], v10, s[8:11], s43 offen offset:1024 sc1\n"
"buffer_load_dwordx4 v[44:47], v10, s[8:11], s43 offen offset:0 sc1\nbuffer_load_dwordx4 v[48:51], v10, s[8:11], s43 offen offset:2048 sc1\nbuffer_load_dwordx4 v[68:71], v11, s[8:11], s43 offen sc1\nbuffer_load_dwordx4 v[72:75], v12, s[8:11], s43 offen sc1\n"
"v_mov_b32 v124, 0\ns_add_u32 s47, s40, 1\nv_mov_b32 v129, s47\nds_write_b32 v130, v129\n"
"s_waitcnt vmcnt(0)\nv_max3_u32 v124, v124, v40, v41\nv_max3_u32 v124, v124, v42, v43\nv_max3_u32 v124, v124, v44, v45\n"
"v_max3_u32 v124, v124, v46, v47\nv_max3_u32 v124, v124, v48, v49\nv_max3_u32 v124, v124, v50, v51\nv_cmp_eq_u32 vcc, -1, v124\n"
"v_mov_b32 v52, 0\nv_mov_b32 v53, 0\nv_mov_b32 v54, 0\nv_mov_b32 v55, 0\n"
"v_mov_b32 v56, 0\nv_mov_b32 v57, 0\nv_mov_b32 v58, 0\nv_mov_b32 v59, 0\n"
"v_mov_b32 v60, 0\nv_mov_b32 v61, 0\nv_mov_b32 v62, 0\nv_mov_b32 v63, 0\n"
"s_cbranch_vccnz retry2_h3\ns_branch landed_h3\nadopt_h3:\nbuffer_load_dword v112, v28, s[8:11], 0 offen sc1\n"
"s_lshl_b32 s52, s40, 26\ns_or_b32 s47, s52, 0x3ffffff\ns_waitcnt vmcnt(0)\nv_cmp_ge_u32 vcc, s47, v112\n"
"s_nop 1\nv_cndmask_b32 v129, 0, v112, vcc\ns_nop 1\nv_max_u32_dpp v129, v129, v129 row_ror:8 row_mask:0xf bank_mask:0xf\n"
"s_nop 1\nv_max_u32_dpp v129, v129, v129 row_ror:4 row_mask:0xf bank_mask:0xf\ns_nop 1\nv_max_u32_dpp v129, v129, v129 row_ror:2 row_mask:0xf bank_mask:0xf\n"
"s_nop 1\nv_max_u32_dpp v129, v129, v129 row_ror:1 row_mask:0xf bank_mask:0xf\ns_nop 0\nv_readlane_b32 s47, v129, 0\n"
"v_readlane_b32 s48, v129, 16\ns_max_u32 s48, s48, s47\ns_cmp_le_u32 s48, s52\ns_cbranch_scc1 back_h3\n"
"s_bfe_u32 s52, s48, 1179656\ns_add_u32 s52, s52, s30\ns_sub_u32 s45, s52, s44\ns_and_b32 s45, s45, 3\n"
"s_sub_u32 s44, s52, s45\ns_lshl_b32 s45, s45, 2\ns_and_b32 s46, s48, 255\ns_mov_b32 s92, 0\n"
"s_branch back_h3\nmbr_h3:\ns_add_u32 s53, s53, 1\ns_add_u32 s49, s49, 1\n"
"s_cmp_gt_u32 s49, 65536\ns_cbranch_scc1 end_h3\nbuffer_load_dwordx4 v[68:71], v11, s[8:11], s43 offen sc1\nbuffer_load_dwordx4 v[72:75], v12, s[8:11], s43 offen sc1\n"
"v_mov_b32 v124, 0\ns_branch mb_h3\nend_h3:\ns_branch fk_done\n"
"fk_x0:\ns_mov_b32 s40, 0\ns_mov_b32 s41, 0\ns_mov_b32 s42, 262144\n"
"s_mov_b32 s43, 393216\ns_mov_b32 s44, s30\ns_mov_b32 s45, 0\ns_bcnt1_i32_b64 s46, s[28:29]\n"
"s_mov_b32 s92, 0\ns_mov_b32 s49, 0\ns_mov_b32 s53, 0\ns_mov_b32 s50, s36\n"
"s_mov_b32 s51, s37\nv_mov_b32 v81, v13\nv_mov_b32 v82, v14\nv_mov_b32 v84, 24576\n"
"s_add_u32 s51, s50, 14336\nv_mov_b32 v64, -1\nv_mov_b32 v65, -1\nv_mov_b32 v66, -1\n"
"v_mov_b32 v67, -1\nbuffer_load_dwordx4 a[0:3], v9, s[16:19], s50 offen offset:0 sc1\nbuffer_load_dwordx4 a[4:7], v9, s[16:19], s50 offen offset:1024 sc1\nbuffer_load_dwordx4 a[8:11], v9, s[16:19], s50 offen offset:2048 sc1\n"
"buffer_load_dwordx4 a[12:15], v9, s[16:19], s50 offen offset:3072 sc1\nbuffer_load_dwordx4 a[16:19], v9, s[16:19], s51 offen offset:0 sc1\nbuffer_load_dwordx4 a[20:23], v9, s[16:19], s51 offen offset:1024 sc1\nbuffer_load_dwordx4 a[24:27], v9, s[16:19], s51 offen offset:2048 sc1\n"
"buffer_load_dwordx4 a[28:31], v9, s[16:19], s51 offen offset:3072 sc1\ns_add_u32 s50, s50, 28672\ns_add_u32 s51, s51, 28672\nbuffer_load_dword v85, v18, s[8:11], 0 offen sc1\n"
"s_waitcnt vmcnt(0)\nf0_x0:\ns_add_u32 s47, s40, 2\ns_min_u32 s47, s47, 64\n"
"s_add_u32 s47, s47, s34\nv_subrev_u32 v85, s47, v85\nv_cmp_lt_u32 vcc, 64, v85\ns_cbranch_vccz f0ok_x0\n"
"s_add_u32 s53, s53, 0\ns_add_u32 s49, s49, 1\ns_cmp_gt_u32 s49, 65536\ns_cbranch_scc1 end_x0\n"
"s_sleep 2\nbuffer_load_dword v85, v18, s[8:11], 0 offen sc1\ns_waitcnt vmcnt(0)\ns_branch f0_x0\n"
"f0ok_x0:\ns_barrier\ns_memrealtime s[54:55]\ns_waitcnt lgkmcnt(0)\n"
"s_add_u32 s44, s54, 16\ns_mov_b32 s45, 8\n.p2align 6\ntop_x0:\n"
"v_mov_b32 v48, 0\nv_mov_b32 v49, 0\nv_mov_b32 v50, 0\nv_mov_b32 v51, 0\n"
"v_mov_b32 v52, 0\nv_mov_b32 v53, 0\nv_mov_b32 v54, 0\nv_mov_b32 v55, 0\n"
"v_mov_b32 v78, 0\ns_mul_i32 s48, s45, 6\ns_and_b32 s48, s48, -4\ns_getpc_b64 s[60:61]\n"
"spc_x0:\ns_sub_u32 s48, sgo_x0-spc_x0, s48\ns_add_u32 s60, s60, s48\ns_addc_u32 s61, s61, 0\n"
"sf_x0:\ns_memrealtime s[54:55]\ns_nop 2\ns_memrealtime s[56:57]\n"
"s_nop 2\ns_memrealtime s[58:59]\nsg_x0:\ns_waitcnt lgkmcnt(4)\n"
"s_sub_u32 s47, s54, s44\ns_add_u32 s48, s47, 12\ns_cmp_ge_u32 s48, 12\ns_cbranch_scc1 so_x0\n"
"s_memrealtime s[54:55]\ns_waitcnt lgkmcnt(4)\ns_cmp_ge_u32 s56, s44\ns_cbranch_scc1 sx_x0\n"
"s_memrealtime s[56:57]\ns_waitcnt lgkmcnt(4)\ns_cmp_ge_u32 s58, s44\ns_cbranch_scc1 sx_x0\n"
"s_memrealtime s[58:59]\ns_waitcnt lgkmcnt(4)\ns_cmp_ge_u32 s54, s44\ns_cbranch_scc1 sx_x0\n"
"s_memrealtime s[54:55]\ns_waitcnt lgkmcnt(4)\ns_cmp_ge_u32 s56, s44\ns_cbranch_scc1 sx_x0\n"
"s_memrealtime s[56:57]\ns_waitcnt lgkmcnt(4)\ns_cmp_ge_u32 s58, s44\ns_cbranch_scc1 sx_x0\n"
"s_memrealtime s[58:59]\ns_waitcnt lgkmcnt(4)\ns_cmp_ge_u32 s54, s44\ns_cbranch_scc1 sx_x0\n"
"s_memrealtime s[54:55]\ns_waitcnt lgkmcnt(4)\ns_cmp_ge_u32 s56, s44\ns_cbranch_scc1 sx_x0\n"
"s_memrealtime s[56:57]\ns_waitcnt lgkmcnt(4)\ns_cmp_ge_u32 s58, s44\ns_cbranch_scc1 sx_x0\n"
"s_memrealtime s[58:59]\ns_waitcnt lgkmcnt(4)\ns_cmp_ge_u32 s54, s44\ns_cbranch_scc1 sx_x0\n"
"s_memrealtime s[54:55]\ns_waitcnt lgkmcnt(4)\ns_cmp_ge_u32 s56, s44\ns_cbranch_scc1 sx_x0\n"
"s_memrealtime s[56:57]\ns_waitcnt lgkmcnt(4)\ns_cmp_ge_u32 s58, s44\ns_cbranch_scc1 sx_x0\n"
"s_memrealtime s[58:59]\ns_waitcnt lgkmcnt(4)\ns_cmp_ge_u32 s54, s44\ns_cbranch_scc1 sx_x0\n"
"s_memrealtime s[54:55]\ns_waitcnt lgkmcnt(4)\ns_cmp_ge_u32 s56, s44\ns_cbranch_scc1 sx_x0\n"
"s_memrealtime s[56:57]\ns_waitcnt lgkmcnt(4)\ns_cmp_ge_u32 s58, s44\ns_cbranch_scc1 sx_x0\n"
"s_memrealtime s[58:59]\ns_waitcnt lgkmcnt(4)\ns_cmp_ge_u32 s54, s44\ns_cbranch_scc1 sx_x0\n"
"s_memrealtime s[54:55]\ns_waitcnt lgkmcnt(4)\ns_cmp_ge_u32 s56, s44\ns_cbranch_scc1 sx_x0\n"
"s_memrealtime s[56:57]\ns_branch sx_x0\nso_x0:\ns_waitcnt lgkmcnt(0)\n"
"s_cmp_lt_i32 s47, 0\ns_cbranch_scc1 sf_x0\ns_branch sgo_x0\nsx_x0:\n"
"s_setpc_b64 s[60:61]\ns_nop 0\ns_nop 0\ns_nop 0\n"
"s_nop 0\ns_nop 0\ns_nop 0\ns_nop 0\n"
"s_nop 0\ns_nop 0\ns_nop 0\ns_nop 0\n"
"s_nop 0\ns_nop 0\ns_nop 0\ns_nop 0\n"
"s_nop 0\ns_nop 0\ns_nop 0\ns_nop 0\n"
"s_nop 0\ns_nop 0\ns_nop 0\ns_nop 0\n"
"s_nop 0\nsgo_x0:\nbuffer_load_dwordx4 v[40:43], v10, s[8:11], s43 offen offset:0 sc1\nbuffer_load_dwordx4 v[44:47], v10, s[8:11], s43 offen offset:1024 sc1\n"
"landed_x0:\ns_waitcnt vmcnt(1)\nv_smfmac_f32_16x16x64_bf16 v[48:51], v[40:43], a[0:7], v8\nv_max3_u32 v78, v78, v40, v41\n"
"v_smfmac_f32_16x16x64_bf16 v[52:55], v[40:43], a[16:23], v8\nv_max3_u32 v78, v78, v42, v43\ns_waitcnt vmcnt(0)\nv_smfmac_f32_16x16x64_bf16 v[48:51], v[44:47], a[8:15], v8\n"
"v_max3_u32 v78, v78, v44, v45\nv_smfmac_f32_16x16x64_bf16 v[52:55], v[44:47], a[24:31], v8\nv_max3_u32 v78, v78, v46, v47\nv_cmp_eq_u32 vcc, -1, v78\n"
"s_nop 0\nv_add_f32 v56, v48, v49\nv_add_f32 v57, v50, v51\ns_nop 1\n"
"v_add_f32 v58, v52, v53\nv_add_f32 v59, v54, v55\nds_write2_b32 v81, v56, v57 offset0:0 offset1:40\nds_write2_b32 v81, v58, v59 offset0:16 offset1:56\n"
"s_cbranch_vccnz retry_x0\nv_mov_b32 v79, 0\ns_nop 1\nv_mov_b32 v79, 0\n"
"s_nop 1\nv_mov_b32 v79, 0\ns_waitcnt lgkmcnt(0)\ns_barrier\n"
"ds_read_b64 v[68:69], v82 offset:0\nds_read_b64 v[70:71], v82 offset:1280\nds_read_b64 v[72:73], v82 offset:2560\nds_read_b64 v[74:75], v82 offset:3840\n"
"v_mov_b32 v79, 0\ns_nop 1\nv_mov_b32 v79, 0\ns_nop 1\n"
"v_mov_b32 v79, 0\ns_nop 1\ns_waitcnt lgkmcnt(2)\nv_pk_add_f32 v[68:69], v[68:69], v[70:71]\n"
"s_waitcnt lgkmcnt(0)\nv_pk_add_f32 v[72:73], v[72:73], v[74:75]\nv_pk_add_f32 v[68:69], v[68:69], v[72:73]\nv_exp_f32 v79, v68\n"
"v_exp_f32 v80, v69\ns_nop 0\nv_cvt_pk_bf16_f32 v79, v79, v80\nbuffer_store_dword v79, v16, s[8:11], s41 offen sc1\n"
"s_barrier\nbuffer_load_dwordx4 a[0:3], v9, s[16:19], s50 offen offset:0 sc1\ns_nop 15\ns_nop 7\n"
"buffer_load_dwordx4 a[4:7], v9, s[16:19], s50 offen offset:1024 sc1\ns_nop 15\ns_nop 7\nbuffer_load_dwordx4 a[8:11], v9, s[16:19], s50 offen offset:2048 sc1\n"
"s_nop 15\ns_nop 7\nbuffer_load_dwordx4 a[12:15], v9, s[16:19], s50 offen offset:3072 sc1\ns_nop 15\n"
"s_nop 7\nbuffer_load_dwordx4 a[16:19], v9, s[16:19], s51 offen offset:0 sc1\ns_nop 15\ns_nop 7\n"
"buffer_load_dwordx4 a[20:23], v9, s[16:19], s51 offen offset:1024 sc1\ns_nop 15\ns_nop 7\nbuffer_load_dwordx4 a[24:27], v9, s[16:19], s51 offen offset:2048 sc1\n"
"s_nop 15\ns_nop 7\nbuffer_load_dwordx4 a[28:31], v9, s[16:19], s51 offen offset:3072 sc1\ns_nop 15\n"
"s_nop 7\ns_add_u32 s50, s50, 28672\ns_add_u32 s51, s51, 28672\nbuffer_store_dwordx2 v[68:69], v17, s[8:11], s41 offen sc1\n"
"buffer_store_dwordx4 v[64:67], v15, s[8:11], s42 offen sc1\nds_read_b32 v83, v84\nv_xor_b32 v81, 8192, v81\nv_xor_b32 v82, 8192, v82\n"
"s_waitcnt lgkmcnt(0)\nv_readfirstlane_b32 s47, v83\ns_sub_u32 s47, s47, s40\ns_cmp_eq_u32 s47, 1\n"
"s_cbranch_scc1 adopt_x0\nback_x0:\ns_mul_i32 s48, s46, 8\ns_add_u32 s45, s45, s48\n"
"s_add_u32 s45, s45, s32\ns_lshr_b32 s48, s45, 4\ns_lshl_b32 s48, s48, 2\ns_add_u32 s44, s44, s48\n"
"s_and_b32 s45, s45, 15\ns_add_u32 s52, s40, 1\ns_bitcmp1_b64 s[28:29], s52\ns_subb_u32 s46, s46, 0\n"
"s_add_u32 s40, s40, 1\ns_mov_b32 s43, s41\ns_add_u32 s41, s41, 131072\ns_and_b32 s41, s41, 524287\n"
"s_add_u32 s92, s92, 1\ns_xor_b32 s42, s41, 262144\ns_waitcnt lgkmcnt(0)\ns_cmp_lt_u32 s40, 63\n"
"s_cbranch_scc1 top_x0\ns_branch end_x0\nretry2_x0:\ns_sleep 3\n"
"retry_x0:\ns_add_u32 s53, s53, 1\ns_add_u32 s49, s49, 1\ns_cmp_gt_u32 s49, 65536\n"
"s_cbranch_scc1 end_x0\nbuffer_load_dwordx4 v[40:43], v10, s[8:11], s43 offen offset:0 sc1\nbuffer_load_dwordx4 v[44:47], v10, s[8:11], s43 offen offset:1024 sc1\nv_mov_b32 v78, 0\n"
"s_add_u32 s47, s40, 1\nv_mov_b32 v83, s47\nds_write_b32 v84, v83\ns_waitcnt vmcnt(0)\n"
"v_max3_u32 v78, v78, v40, v41\nv_max3_u32 v78, v78, v42, v43\nv_max3_u32 v78, v78, v44, v45\nv_max3_u32 v78, v78, v46, v47\n"
"v_cmp_eq_u32 vcc, -1, v78\nv_mov_b32 v48, 0\nv_mov_b32 v49, 0\nv_mov_b32 v50, 0\n"
"v_mov_b32 v51, 0\nv_mov_b32 v52, 0\nv_mov_b32 v53, 0\nv_mov_b32 v54, 0\n"
"v_mov_b32 v55, 0\ns_cbranch_vccnz retry2_x0\ns_branch landed_x0\nadopt_x0:\n"
"buffer_load_dword v60, v28, s[8:11], 0 offen sc1\ns_lshl_b32 s52, s40, 26\ns_or_b32 s47, s52, 0x3ffffff\ns_waitcnt vmcnt(0)\n"
"v_cmp_ge_u32 vcc, s47, v60\ns_nop 1\nv_cndmask_b32 v83, 0, v60, vcc\ns_nop 1\n"
"v_max_u32_dpp v83, v83, v83 row_ror:8 row_mask:0xf bank_mask:0xf\ns_nop 1\nv_max_u32_dpp v83, v83, v83 row_ror:4 row_mask:0xf bank_mask:0xf\ns_nop 1\n"
"v_max_u32_dpp v83, v83, v83 row_ror:2 row_mask:0xf bank_mask:0xf\ns_nop 1\nv_max_u32_dpp v83, v83, v83 row_ror:1 row_mask:0xf bank_mask:0xf\ns_nop 0\n"
"v_readlane_b32 s47, v83, 0\nv_readlane_b32 s48, v83, 16\ns_max_u32 s48, s48, s47\ns_cmp_le_u32 s48, s52\n"
"s_cbranch_scc1 back_x0\ns_bfe_u32 s52, s48, 1179656\ns_add_u32 s52, s52, s30\ns_sub_u32 s45, s52, s44\n"
"s_and_b32 s45, s45, 3\ns_sub_u32 s44, s52, s45\ns_lshl_b32 s45, s45, 2\ns_and_b32 s46, s48, 255\n"
"s_mov_b32 s92, 0\ns_branch back_x0\nend_x0:\ns_branch fk_done\n"
"fk_x1:\ns_mov_b32 s40, 0\ns_mov_b32 s41, 0\ns_mov_b32 s42, 262144\n"
"s_mov_b32 s43, 393216\ns_mov_b32 s44, s30\ns_mov_b32 s45, 0\ns_bcnt1_i32_b64 s46, s[28:29]\n"
"s_mov_b32 s92, 0\ns_mov_b32 s49, 0\ns_mov_b32 s53, 0\ns_mov_b32 s50, s36\n"
"s_mov_b32 s51, s37\nv_mov_b32 v81, v13\nv_mov_b32 v82, v14\nv_mov_b32 v84, 24576\n"
"s_add_u32 s51, s50, 14336\nv_mov_b32 v64, -1\nv_mov_b32 v65, -1\nv_mov_b32 v66, -1\n"
"v_mov_b32 v67, -1\nbuffer_load_dwordx4 a[8:11], v9, s[16:19], s50 offen offset:2048 sc1\nbuffer_load_dwordx4 a[12:15], v9, s[16:19], s50 offen offset:3072 sc1\nbuffer_load_dwordx4 a[0:3], v9, s[16:19], s50 offen offset:0 sc1\n"
"buffer_load_dwordx4 a[4:7], v9, s[16:19], s50 offen offset:1024 sc1\nbuffer_load_dwordx4 a[24:27], v9, s[16:19], s51 offen offset:2048 sc1\nbuffer_load_dwordx4 a[28:31], v9, s[16:19], s51 offen offset:3072 sc1\nbuffer_load_dwordx4 a[16:19], v9, s[16:19], s51 offen offset:0 sc1\n"
"buffer_load_dwordx4 a[20:23], v9, s[16:19], s51 offen offset:1024 sc1\ns_add_u32 s50, s50, 28672\ns_add_u32 s51, s51, 28672\nbuffer_load_dword v85, v18, s[8:11], 0 offen sc1\n"
"s_waitcnt vmcnt(0)\nf0_x1:\ns_add_u32 s47, s40, 2\ns_min_u32 s47, s47, 64\n"
"s_add_u32 s47, s47, s34\nv_subrev_u32 v85, s47, v85\nv_cmp_lt_u32 vcc, 64, v85\ns_cbranch_vccz f0ok_x1\n"
"s_add_u32 s53, s53, 0\ns_add_u32 s49, s49, 1\ns_cmp_gt_u32 s49, 65536\ns_cbranch_scc1 end_x1\n"
"s_sleep 2\nbuffer_load_dword v85, v18, s[8:11], 0 offen sc1\ns_waitcnt vmcnt(0)\ns_branch f0_x1\n"
"f0ok_x1:\ns_barrier\ns_memrealtime s[54:55]\ns_waitcnt lgkmcnt(0)\n"
"s_add_u32 s44, s54, 16\ns_mov_b32 s45, 8\n.p2align 6\ntop_x1:\n"
"v_mov_b32 v48, 0\nv_mov_b32 v49, 0\nv_mov_b32 v50, 0\nv_mov_b32 v51, 0\n"
"v_mov_b32 v52, 0\nv_mov_b32 v53, 0\nv_mov_b32 v54, 0\nv_mov_b32 v55, 0\n"
"v_mov_b32 v78, 0\ns_mul_i32 s48, s45, 6\ns_and_b32 s48, s48, -4\ns_getpc_b64 s[60:61]\n"
"spc_x1:\ns_sub_u32 s48, sgo_x1-spc_x1, s48\ns_add_u32 s60, s60, s48\ns_addc_u32 s61, s61, 0\n"
"sf_x1:\ns_memrealtime s[54:55]\ns_nop 2\ns_memrealtime s[56:57]\n"
"s_nop 2\ns_memrealtime s[58:59]\nsg_x1:\ns_waitcnt lgkmcnt(4)\n"
"s_sub_u32 s47, s54, s44\ns_add_u32 s48, s47, 12\ns_cmp_ge_u32 s48, 12\ns_cbranch_scc1 so_x1\n"
"s_memrealtime s[54:55]\ns_waitcnt lgkmcnt(4)\ns_cmp_ge_u32 s56, s44\ns_cbranch_scc1 sx_x1\n"
"s_memrealtime s[56:57]\ns_waitcnt lgkmcnt(4)\ns_cmp_ge_u32 s58, s44\ns_cbranch_scc1 sx_x1\n"
"s_memrealtime s[58:59]\ns_waitcnt lgkmcnt(4)\ns_cmp_ge_u32 s54, s44\ns_cbranch_scc1 sx_x1\n"
"s_memrealtime s[54:55]\ns_waitcnt lgkmcnt(4)\ns_cmp_ge_u32 s56, s44\ns_cbranch_scc1 sx_x1\n"
"s_memrealtime s[56:57]\ns_waitcnt lgkmcnt(4)\ns_cmp_ge_u32 s58, s44\ns_cbranch_scc1 sx_x1\n"
"s_memrealtime s[58:59]\ns_waitcnt lgkmcnt(4)\ns_cmp_ge_u32 s54, s44\ns_cbranch_scc1 sx_x1\n"
"s_memrealtime s[54:55]\ns_waitcnt lgkmcnt(4)\ns_cmp_ge_u32 s56, s44\ns_cbranch_scc1 sx_x1\n"
"s_memrealtime s[56:57]\ns_waitcnt lgkmcnt(4)\ns_cmp_ge_u32 s58, s44\ns_cbranch_scc1 sx_x1\n"
"s_memrealtime s[58:59]\ns_waitcnt lgkmcnt(4)\ns_cmp_ge_u32 s54, s44\ns_cbranch_scc1 sx_x1\n"
"s_memrealtime s[54:55]\ns_waitcnt lgkmcnt(4)\ns_cmp_ge_u32 s56, s44\ns_cbranch_scc1 sx_x1\n"
"s_memrealtime s[56:57]\ns_waitcnt lgkmcnt(4)\ns_cmp_ge_u32 s58, s44\ns_cbranch_scc1 sx_x1\n"
"s_memrealtime s[58:59]\ns_waitcnt lgkmcnt(4)\ns_cmp_ge_u32 s54, s44\ns_cbranch_scc1 sx_x1\n"
"s_memrealtime s[54:55]\ns_waitcnt lgkmcnt(4)\ns_cmp_ge_u32 s56, s44\ns_cbranch_scc1 sx_x1\n"
"s_memrealtime s[56:57]\ns_waitcnt lgkmcnt(4)\ns_cmp_ge_u32 s58, s44\ns_cbranch_scc1 sx_x1\n"
"s_memrealtime s[58:59]\ns_waitcnt lgkmcnt(4)\ns_cmp_ge_u32 s54, s44\ns_cbranch_scc1 sx_x1\n"
"s_memrealtime s[54:55]\ns_waitcnt lgkmcnt(4)\ns_cmp_ge_u32 s56, s44\ns_cbranch_scc1 sx_x1\n"
"s_memrealtime s[56:57]\ns_branch sx_x1\nso_x1:\ns_waitcnt lgkmcnt(0)\n"
"s_cmp_lt_i32 s47, 0\ns_cbranch_scc1 sf_x1\ns_branch sgo_x1\nsx_x1:\n"
"s_setpc_b64 s[60:61]\ns_nop 0\ns_nop 0\ns_nop 0\n"
"s_nop 0\ns_nop 0\ns_nop 0\ns_nop 0\n"
"s_nop 0\ns_nop 0\ns_nop 0\ns_nop 0\n"
"s_nop 0\ns_nop 0\ns_nop 0\ns_nop 0\n"
"s_nop 0\ns_nop 0\ns_nop 0\ns_nop 0\n"
"s_nop 0\ns_nop 0\ns_nop 0\ns_nop 0\n"
"s_nop 0\nsgo_x1:\ns_nop 5\nbuffer_load_dwordx4 v[40:43], v10, s[8:11], s43 offen offset:1024 sc1\n"
"buffer_load_dwordx4 v[44:47], v10, s[8:11], s43 offen offset:0 sc1\nlanded_x1:\ns_waitcnt vmcnt(1)\nv_smfmac_f32_16x16x64_bf16 v[48:51], v[40:43], a[8:15], v8\n"
"v_max3_u32 v78, v78, v40, v41\nv_smfmac_f32_16x16x64_bf16 v[52:55], v[40:43], a[24:31], v8\nv_max3_u32 v78, v78, v42, v43\ns_waitcnt vmcnt(0)\n"
"v_smfmac_f32_16x16x64_bf16 v[48:51], v[44:47], a[0:7], v8\nv_max3_u32 v78, v78, v44, v45\nv_smfmac_f32_16x16x64_bf16 v[52:55], v[44:47], a[16:23], v8\nv_max3_u32 v78, v78, v46, v47\n"
"v_cmp_eq_u32 vcc, -1, v78\ns_nop 0\nv_add_f32 v56, v48, v49\nv_add_f32 v57, v50, v51\n"
"s_nop 1\nv_add_f32 v58, v52, v53\nv_add_f32 v59, v54, v55\nds_write2_b32 v81, v56, v57 offset0:0 offset1:40\n"
"ds_write2_b32 v81, v58, v59 offset0:16 offset1:56\ns_cbranch_vccnz retry_x1\nv_mov_b32 v79, 0\ns_nop 1\n"
"v_mov_b32 v79, 0\ns_nop 1\nv_mov_b32 v79, 0\ns_waitcnt lgkmcnt(0)\n"
"s_barrier\nds_read_b64 v[68:69], v82 offset:0\nds_read_b64 v[70:71], v82 offset:1280\nds_read_b64 v[72:73], v82 offset:2560\n"
"ds_read_b64 v[74:75], v82 offset:3840\nv_mov_b32 v79, 0\ns_nop 1\nv_mov_b32 v79, 0\n"
"s_nop 1\nv_mov_b32 v79, 0\ns_nop 1\ns_waitcnt lgkmcnt(2)\n"
"v_pk_add_f32 v[68:69], v[68:69], v[70:71]\ns_waitcnt lgkmcnt(0)\nv_pk_add_f32 v[72:73], v[72:73], v[74:75]\nv_pk_add_f32 v[68:69], v[68:69], v[72:73]\n"
"v_exp_f32 v79, v68\nv_exp_f32 v80, v69\ns_nop 0\nv_cvt_pk_bf16_f32 v79, v79, v80\n"
"buffer_store_dword v79, v16, s[8:11], s41 offen sc1\ns_barrier\nbuffer_load_dwordx4 a[8:11], v9, s[16:19], s50 offen offset:2048 sc1\ns_nop 15\n"
"s_nop 7\nbuffer_load_dwordx4 a[12:15], v9, s[16:19], s50 offen offset:3072 sc1\ns_nop 15\ns_nop 7\n"
"buffer_load_dwordx4 a[0:3], v9, s[16:19], s50 offen offset:0 sc1\ns_nop 15\ns_nop 7\nbuffer_load_dwordx4 a[4:7], v9, s[16:19], s50 offen offset:1024 sc1\n"
"s_nop 15\ns_nop 7\nbuffer_load_dwordx4 a[24:27], v9, s[16:19], s51 offen offset:2048 sc1\ns_nop 15\n"
"s_nop 7\nbuffer_load_dwordx4 a[28:31], v9, s[16:19], s51 offen offset:3072 sc1\ns_nop 15\ns_nop 7\n"
"buffer_load_dwordx4 a[16:19], v9, s[16:19], s51 offen offset:0 sc1\ns_nop 15\ns_nop 7\nbuffer_load_dwordx4 a[20:23], v9, s[16:19], s51 offen offset:1024 sc1\n"
"s_nop 15\ns_nop 7\ns_add_u32 s50, s50, 28672\ns_add_u32 s51, s51, 28672\n"
"buffer_store_dwordx2 v[68:69], v17, s[8:11], s41 offen sc1\nbuffer_store_dwordx4 v[64:67], v15, s[8:11], s42 offen sc1\nds_read_b32 v83, v84\nv_xor_b32 v81, 8192, v81\n"
"v_xor_b32 v82, 8192, v82\ns_waitcnt lgkmcnt(0)\nv_readfirstlane_b32 s47, v83\ns_sub_u32 s47, s47, s40\n"
"s_cmp_eq_u32 s47, 1\ns_cbranch_scc1 adopt_x1\nback_x1:\ns_mul_i32 s48, s46, 8\n"
"s_add_u32 s45, s45, s48\ns_add_u32 s45, s45, s32\ns_lshr_b32 s48, s45, 4\ns_lshl_b32 s48, s48, 2\n"
"s_add_u32 s44, s44, s48\ns_and_b32 s45, s45, 15\ns_add_u32 s52, s40, 1\ns_bitcmp1_b64 s[28:29], s52\n"
"s_subb_u32 s46, s46, 0\ns_add_u32 s40, s40, 1\ns_mov_b32 s43, s41\ns_add_u32 s41, s41, 131072\n"
"s_and_b32 s41, s41, 524287\ns_add_u32 s92, s92, 1\ns_xor_b32 s42, s41, 262144\ns_waitcnt lgkmcnt(0)\n"
"s_cmp_lt_u32 s40, 63\ns_cbranch_scc1 top_x1\ns_branch end_x1\nretry2_x1:\n"
"s_sleep 3\nretry_x1:\ns_add_u32 s53, s53, 1\ns_add_u32 s49, s49, 1\n"
"s_cmp_gt_u32 s49, 65536\ns_cbranch_scc1 end_x1\nbuffer_load_dwordx4 v[40:43], v10, s[8:11], s43 offen offset:1024 sc1\nbuffer_load_dwordx4 v[44:47], v10, s[8:11], s43 offen offset:0 sc1\n"
"v_mov_b32 v78, 0\ns_add_u32 s47, s40, 1\nv_mov_b32 v83, s47\nds_write_b32 v84, v83\n"
"s_waitcnt vmcnt(0)\nv_max3_u32 v78, v78, v40, v41\nv_max3_u32 v78, v78, v42, v43\nv_max3_u32 v78, v78, v44, v45\n"
"v_max3_u32 v78, v78, v46, v47\nv_cmp_eq_u32 vcc, -1, v78\nv_mov_b32 v48, 0\nv_mov_b32 v49, 0\n"
"v_mov_b32 v50, 0\nv_mov_b32 v51, 0\nv_mov_b32 v52, 0\nv_mov_b32 v53, 0\n"
"v_mov_b32 v54, 0\nv_mov_b32 v55, 0\ns_cbranch_vccnz retry2_x1\ns_branch landed_x1\n"
"adopt_x1:\nbuffer_load_dword v60, v28, s[8:11], 0 offen sc1\ns_lshl_b32 s52, s40, 26\ns_or_b32 s47, s52, 0x3ffffff\n"
"s_waitcnt vmcnt(0)\nv_cmp_ge_u32 vcc, s47, v60\ns_nop 1\nv_cndmask_b32 v83, 0, v60, vcc\n"
"s_nop 1\nv_max_u32_dpp v83, v83, v83 row_ror:8 row_mask:0xf bank_mask:0xf\ns_nop 1\nv_max_u32_dpp v83, v83, v83 row_ror:4 row_mask:0xf bank_mask:0xf\n"
"s_nop 1\nv_max_u32_dpp v83, v83, v83 row_ror:2 row_mask:0xf bank_mask:0xf\ns_nop 1\nv_max_u32_dpp v83, v83, v83 row_ror:1 row_mask:0xf bank_mask:0xf\n"
"s_nop 0\nv_readlane_b32 s47, v83, 0\nv_readlane_b32 s48, v83, 16\ns_max_u32 s48, s48, s47\n"
"s_cmp_le_u32 s48, s52\ns_cbranch_scc1 back_x1\ns_bfe_u32 s52, s48, 1179656\ns_add_u32 s52, s52, s30\n"
"s_sub_u32 s45, s52, s44\ns_and_b32 s45, s45, 3\ns_sub_u32 s44, s52, s45\ns_lshl_b32 s45, s45, 2\n"
"s_and_b32 s46, s48, 255\ns_mov_b32 s92, 0\ns_branch back_x1\nend_x1:\n"
"s_branch fk_done\nfk_x2:\ns_mov_b32 s40, 0\ns_mov_b32 s41, 0\n"
"s_mov_b32 s42, 262144\ns_mov_b32 s43, 393216\ns_mov_b32 s44, s30\ns_mov_b32 s45, 0\n"
"s_bcnt1_i32_b64 s46, s[28:29]\ns_mov_b32 s92, 0\ns_mov_b32 s49, 0\ns_mov_b32 s53, 0\n"
"s_mov_b32 s50, s36\ns_mov_b32 s51, s37\nv_mov_b32 v81, v13\nv_mov_b32 v82, v14\n"
"v_mov_b32 v84, 24576\ns_add_u32 s51, s50, 14336\nv_mov_b32 v64, -1\nv_mov_b32 v65, -1\n"
"v_mov_b32 v66, -1\nv_mov_b32 v67, -1\nbuffer_load_dwordx4 a[0:3], v9, s[16:19], s50 offen offset:0 sc1\nbuffer_load_dwordx4 a[4:7], v9, s[16:19], s50 offen offset:1024 sc1\n"
"buffer_load_dwordx4 a[8:11], v9, s[16:19], s50 offen offset:2048 sc1\nbuffer_load_dwordx4 a[12:15], v9, s[16:19], s50 offen offset:3072 sc1\nbuffer_load_dwordx4 a[16:19], v9, s[16:19], s51 offen offset:0 sc1\nbuffer_load_dwordx4 a[20:23], v9, s[16:19], s51 offen offset:1024 sc1\n"
"buffer_load_dwordx4 a[24:27], v9, s[16:19], s51 offen offset:2048 sc1\nbuffer_load_dwordx4 a[28:31], v9, s[16:19], s51 offen offset:3072 sc1\ns_add_u32 s50, s50, 28672\ns_add_u32 s51, s51, 28672\n"
"buffer_load_dword v85, v18, s[8:11], 0 offen sc1\ns_waitcnt vmcnt(0)\nf0_x2:\ns_add_u32 s47, s40, 2\n"
"s_min_u32 s47, s47, 64\ns_add_u32 s47, s47, s34\nv_subrev_u32 v85, s47, v85\nv_cmp_lt_u32 vcc, 64, v85\n"
"s_cbranch_vccz f0ok_x2\ns_add_u32 s53, s53, 0\ns_add_u32 s49, s49, 1\ns_cmp_gt_u32 s49, 65536\n"
"s_cbranch_scc1 end_x2\ns_sleep 2\nbuffer_load_dword v85, v18, s[8:11], 0 offen sc1\ns_waitcnt vmcnt(0)\n"
"s_branch f0_x2\nf0ok_x2:\ns_barrier\ns_memrealtime s[54:55]\n"
"s_waitcnt lgkmcnt(0)\ns_add_u32 s44, s54, 16\ns_mov_b32 s45, 8\n.p2align 6\n"
"top_x2:\nv_mov_b32 v48, 0\nv_mov_b32 v49, 0\nv_mov_b32 v50, 0\n"
"v_mov_b32 v51, 0\nv_mov_b32 v52, 0\nv_mov_b32 v53, 0\nv_mov_b32 v54, 0\n"
"v_mov_b32 v55, 0\nv_mov_b32 v78, 0\ns_mul_i32 s48, s45, 6\ns_and_b32 s48, s48, -4\n"
"s_getpc_b64 s[60:61]\nspc_x2:\ns_sub_u32 s48, sgo_x2-spc_x2, s48\ns_add_u32 s60, s60, s48\n"
"s_addc_u32 s61, s61, 0\nsf_x2:\ns_memrealtime s[54:55]\ns_nop 2\n"
"s_memrealtime s[56:57]\ns_nop 2\ns_memrealtime s[58:59]\nsg_x2:\n"
"s_waitcnt lgkmcnt(4)\ns_sub_u32 s47, s54, s44\ns_add_u32 s48, s47, 12\ns_cmp_ge_u32 s48, 12\n"
"s_cbranch_scc1 so_x2\ns_memrealtime s[54:55]\ns_waitcnt lgkmcnt(4)\ns_cmp_ge_u32 s56, s44\n"
"s_cbranch_scc1 sx_x2\ns_memrealtime s[56:57]\ns_waitcnt lgkmcnt(4)\ns_cmp_ge_u32 s58, s44\n"
"s_cbranch_scc1 sx_x2\ns_memrealtime s[58:59]\ns_waitcnt lgkmcnt(4)\ns_cmp_ge_u32 s54, s44\n"
"s_cbranch_scc1 sx_x2\ns_memrealtime s[54:55]\ns_waitcnt lgkmcnt(4)\ns_cmp_ge_u32 s56, s44\n"
"s_cbranch_scc1 sx_x2\ns_memrealtime s[56:57]\ns_waitcnt lgkmcnt(4)\ns_cmp_ge_u32 s58, s44\n"
"s_cbranch_scc1 sx_x2\ns_memrealtime s[58:59]\ns_waitcnt lgkmcnt(4)\ns_cmp_ge_u32 s54, s44\n"
"s_cbranch_scc1 sx_x2\ns_memrealtime s[54:55]\ns_waitcnt lgkmcnt(4)\ns_cmp_ge_u32 s56, s44\n"
"s_cbranch_scc1 sx_x2\ns_memrealtime s[56:57]\ns_waitcnt lgkmcnt(4)\ns_cmp_ge_u32 s58, s44\n"
"s_cbranch_scc1 sx_x2\ns_memrealtime s[58:59]\ns_waitcnt lgkmcnt(4)\ns_cmp_ge_u32 s54, s44\n"
"s_cbranch_scc1 sx_x2\ns_memrealtime s[54:55]\ns_waitcnt lgkmcnt(4)\ns_cmp_ge_u32 s56, s44\n"
"s_cbranch_scc1 sx_x2\ns_memrealtime s[56:57]\ns_waitcnt lgkmcnt(4)\ns_cmp_ge_u32 s58, s44\n"
"s_cbranch_scc1 sx_x2\ns_memrealtime s[58:59]\ns_waitcnt lgkmcnt(4)\ns_cmp_ge_u32 s54, s44\n"
"s_cbranch_scc1 sx_x2\ns_memrealtime s[54:55]\ns_waitcnt lgkmcnt(4)\ns_cmp_ge_u32 s56, s44\n"
"s_cbranch_scc1 sx_x2\ns_memrealtime s[56:57]\ns_waitcnt lgkmcnt(4)\ns_cmp_ge_u32 s58, s44\n"
"s_cbranch_scc1 sx_x2\ns_memrealtime s[58:59]\ns_waitcnt lgkmcnt(4)\ns_cmp_ge_u32 s54, s44\n"
"s_cbranch_scc1 sx_x2\ns_memrealtime s[54:55]\ns_waitcnt lgkmcnt(4)\ns_cmp_ge_u32 s56, s44\n"
"s_cbranch_scc1 sx_x2\ns_memrealtime s[56:57]\ns_branch sx_x2\nso_x2:\n"
"s_waitcnt lgkmcnt(0)\ns_cmp_lt_i32 s47, 0\ns_cbranch_scc1 sf_x2\ns_branch sgo_x2\n"
"sx_x2:\ns_setpc_b64 s[60:61]\ns_nop 0\ns_nop 0\n"
"s_nop 0\ns_nop 0\ns_nop 0\ns_nop 0\n"
"s_nop 0\ns_nop 0\ns_nop 0\ns_nop 0\n"
"s_nop 0\ns_nop 0\ns_nop 0\ns_nop 0\n"
"s_nop 0\ns_nop 0\ns_nop 0\ns_nop 0\n"
"s_nop 0\ns_nop 0\ns_nop 0\ns_nop 0\n"
"s_nop 0\ns_nop 0\nsgo_x2:\ns_nop 11\n"
"buffer_load_dwordx4 v[40:43], v10, s[8:11], s43 offen offset:0 sc1\nbuffer_load_dwordx4 v[44:47], v10, s[8:11], s43 offen offset:1024 sc1\nv_mov_b32 v79, 0\ns_nop 1\n"
"v_mov_b32 v79, 0\ns_nop 1\nv_mov_b32 v79, 0\ns_nop 1\n"
"v_mov_b32 v79, 0\ns_nop 1\nv_mov_b32 v79, 0\ns_nop 1\n"
"v_mov_b32 v79, 0\ns_nop 1\nv_mov_b32 v79, 0\ns_nop 1\n"
"v_mov_b32 v79, 0\ns_nop 1\nv_mov_b32 v79, 0\ns_nop 1\n"
"v_mov_b32 v79, 0\ns_nop 1\nv_mov_b32 v79, 0\ns_nop 1\n"
"v_mov_b32 v79, 0\ns_nop 1\nv_mov_b32 v79, 0\ns_nop 1\n"
"v_mov_b32 v79, 0\ns_nop 1\nv_mov_b32 v79, 0\ns_nop 1\n"
"v_mov_b32 v79, 0\ns_nop 1\nv_mov_b32 v79, 0\ns_nop 1\n"
"v_mov_b32 v79, 0\ns_nop 1\nv_mov_b32 v79, 0\ns_nop 1\n"
"v_mov_b32 v79, 0\ns_nop 1\nv_mov_b32 v79, 0\ns_nop 1\n"
"v_mov_b32 v79, 0\ns_nop 1\nv_mov_b32 v79, 0\ns_nop 1\n"
"v_mov_b32 v79, 0\ns_nop 1\nv_mov_b32 v79, 0\ns_nop 1\n"
"v_mov_b32 v79, 0\ns_nop 1\nv_mov_b32 v79, 0\ns_nop 1\n"
"v_mov_b32 v79, 0\ns_nop 1\nv_mov_b32 v79, 0\nlanded_x2:\n"
"s_waitcnt vmcnt(1)\nv_smfmac_f32_16x16x64_bf16 v[48:51], v[40:43], a[0:7], v8\nv_max3_u32 v78, v78, v40, v41\nv_smfmac_f32_16x16x64_bf16 v[52:55], v[40:43], a[16:23], v8\n"
"v_max3_u32 v78, v78, v42, v43\ns_waitcnt vmcnt(0)\nv_smfmac_f32_16x16x64_bf16 v[48:51], v[44:47], a[8:15], v8\nv_max3_u32 v78, v78, v44, v45\n"
"v_smfmac_f32_16x16x64_bf16 v[52:55], v[44:47], a[24:31], v8\nv_max3_u32 v78, v78, v46, v47\nv_cmp_eq_u32 vcc, -1, v78\ns_nop 0\n"
"v_add_f32 v56, v48, v49\nv_add_f32 v57, v50, v51\ns_nop 1\nv_add_f32 v58, v52, v53\n"
"v_add_f32 v59, v54, v55\nds_write2_b32 v81, v56, v57 offset0:0 offset1:40\nds_write2_b32 v81, v58, v59 offset0:16 offset1:56\ns_cbranch_vccnz retry_x2\n"
"v_mov_b32 v79, 0\ns_nop 1\nv_mov_b32 v79, 0\ns_nop 1\n"
"v_mov_b32 v79, 0\ns_waitcnt lgkmcnt(0)\ns_barrier\nds_read_b64 v[68:69], v82 offset:0\n"
"ds_read_b64 v[70:71], v82 offset:1280\nds_read_b64 v[72:73], v82 offset:2560\nds_read_b64 v[74:75], v82 offset:3840\nv_mov_b32 v79, 0\n"
"s_nop 1\nv_mov_b32 v79, 0\ns_nop 1\nv_mov_b32 v79, 0\n"
"s_nop 1\ns_waitcnt lgkmcnt(2)\nv_pk_add_f32 v[68:69], v[68:69], v[70:71]\ns_waitcnt lgkmcnt(0)\n"
"v_pk_add_f32 v[72:73], v[72:73], v[74:75]\nv_pk_add_f32 v[68:69], v[68:69], v[72:73]\nv_exp_f32 v79, v68\nv_exp_f32 v80, v69\n"
"s_nop 0\nv_cvt_pk_bf16_f32 v79, v79, v80\nbuffer_store_dword v79, v16, s[8:11], s41 offen sc1\ns_barrier\n"
"buffer_load_dwordx4 a[0:3], v9, s[16:19], s50 offen offset:0 sc1\ns_nop 15\ns_nop 7\nbuffer_load_dwordx4 a[4:7], v9, s[16:19], s50 offen offset:1024 sc1\n"
"s_nop 15\ns_nop 7\nbuffer_load_dwordx4 a[8:11], v9, s[16:19], s50 offen offset:2048 sc1\ns_nop 15\n"
"s_nop 7\nbuffer_load_dwordx4 a[12:15], v9, s[16:19], s50 offen offset:3072 sc1\ns_nop 15\ns_nop 7\n"
"buffer_load_dwordx4 a[16:19], v9, s[16:19], s51 offen offset:0 sc1\ns_nop 15\ns_nop 7\nbuffer_load_dwordx4 a[20:23], v9, s[16:19], s51 offen offset:1024 sc1\n"
"s_nop 15\ns_nop 7\nbuffer_load_dwordx4 a[24:27], v9, s[16:19], s51 offen offset:2048 sc1\ns_nop 15\n"
"s_nop 7\nbuffer_load_dwordx4 a[28:31], v9, s[16:19], s51 offen offset:3072 sc1\ns_nop 15\ns_nop 7\n"
"s_add_u32 s50, s50, 28672\ns_add_u32 s51, s51, 28672\nbuffer_store_dwordx2 v[68:69], v17, s[8:11], s41 offen sc1\nbuffer_store_dwordx4 v[64:67], v15, s[8:11], s42 offen sc1\n"
"ds_read_b32 v83, v84\nv_xor_b32 v81, 8192, v81\nv_xor_b32 v82, 8192, v82\ns_waitcnt lgkmcnt(0)\n"
"v_readfirstlane_b32 s47, v83\ns_sub_u32 s47, s47, s40\ns_cmp_eq_u32 s47, 1\ns_cbranch_scc1 adopt_x2\n"
"back_x2:\ns_mul_i32 s48, s46, 8\ns_add_u32 s45, s45, s48\ns_add_u32 s45, s45, s32\n"
"s_lshr_b32 s48, s45, 4\ns_lshl_b32 s48, s48, 2\ns_add_u32 s44, s44, s48\ns_and_b32 s45, s45, 15\n"
"s_add_u32 s52, s40, 1\ns_bitcmp1_b64 s[28:29], s52\ns_subb_u32 s46, s46, 0\ns_add_u32 s40, s40, 1\n"
"s_mov_b32 s43, s41\ns_add_u32 s41, s41, 131072\ns_and_b32 s41, s41, 524287\ns_add_u32 s92, s92, 1\n"
"s_xor_b32 s42, s41, 262144\ns_waitcnt lgkmcnt(0)\ns_cmp_lt_u32 s40, 63\ns_cbranch_scc1 top_x2\n"
"s_branch end_x2\nretry2_x2:\ns_sleep 3\nretry_x2:\n"
"s_add_u32 s53, s53, 1\ns_add_u32 s49, s49, 1\ns_cmp_gt_u32 s49, 65536\ns_cbranch_scc1 end_x2\n"
"buffer_load_dwordx4 v[40:43], v10, s[8:11], s43 offen offset:0 sc1\nbuffer_load_dwordx4 v[44:47], v10, s[8:11], s43 offen offset:1024 sc1\nv_mov_b32 v78, 0\ns_add_u32 s47, s40, 1\n"
"v_mov_b32 v83, s47\nds_write_b32 v84, v83\ns_waitcnt vmcnt(0)\nv_max3_u32 v78, v78, v40, v41\n"
"v_max3_u32 v78, v78, v42, v43\nv_max3_u32 v78, v78, v44, v45\nv_max3_u32 v78, v78, v46, v47\nv_cmp_eq_u32 vcc, -1, v78\n"
"v_mov_b32 v48, 0\nv_mov_b32 v49, 0\nv_mov_b32 v50, 0\nv_mov_b32 v51, 0\n"
"v_mov_b32 v52, 0\nv_mov_b32 v53, 0\nv_mov_b32 v54, 0\nv_mov_b32 v55, 0\n"
"s_cbranch_vccnz retry2_x2\ns_branch landed_x2\nadopt_x2:\nbuffer_load_dword v60, v28, s[8:11], 0 offen sc1\n"
"s_lshl_b32 s52, s40, 26\ns_or_b32 s47, s52, 0x3ffffff\ns_waitcnt vmcnt(0)\nv_cmp_ge_u32 vcc, s47, v60\n"
"s_nop 1\nv_cndmask_b32 v83, 0, v60, vcc\ns_nop 1\nv_max_u32_dpp v83, v83, v83 row_ror:8 row_mask:0xf bank_mask:0xf\n"
"s_nop 1\nv_max_u32_dpp v83, v83, v83 row_ror:4 row_mask:0xf bank_mask:0xf\ns_nop 1\nv_max_u32_dpp v83, v83, v83 row_ror:2 row_mask:0xf bank_mask:0xf\n"
"s_nop 1\nv_max_u32_dpp v83, v83, v83 row_ror:1 row_mask:0xf bank_mask:0xf\ns_nop 0\nv_readlane_b32 s47, v83, 0\n"
"v_readlane_b32 s48, v83, 16\ns_max_u32 s48, s48, s47\ns_cmp_le_u32 s48, s52\ns_cbranch_scc1 back_x2\n"
"s_bfe_u32 s52, s48, 1179656\ns_add_u32 s52, s52, s30\ns_sub_u32 s45, s52, s44\ns_and_b32 s45, s45, 3\n"
"s_sub_u32 s44, s52, s45\ns_lshl_b32 s45, s45, 2\ns_and_b32 s46, s48, 255\ns_mov_b32 s92, 0\n"
"s_branch back_x2\nend_x2:\ns_branch fk_done\nfk_x3:\n"
"s_mov_b32 s40, 0\ns_mov_b32 s41, 0\ns_mov_b32 s42, 262144\ns_mov_b32 s43, 393216\n"
"s_mov_b32 s44, s30\ns_mov_b32 s45, 0\ns_bcnt1_i32_b64 s46, s[28:29]\ns_mov_b32 s92, 0\n"
"s_mov_b32 s49, 0\ns_mov_b32 s53, 0\ns_mov_b32 s50, s36\ns_mov_b32 s51, s37\n"
"v_mov_b32 v77, v13\nv_mov_b32 v78, v14\nv_mov_b32 v80, 24576\ns_add_u32 s51, s50, 14336\n"
"v_mov_b32 v60, -1\nv_mov_b32 v61, -1\nv_mov_b32 v62, -1\nv_mov_b32 v63, -1\n"
"buffer_load_dwordx4 a[0:3], v9, s[16:19], s50 offen offset:0 sc1\nbuffer_load_dwordx4 a[4:7], v9, s[16:19], s50 offen offset:1024 sc1\nbuffer_load_dwordx4 a[16:19], v9, s[16:19], s51 offen offset:0 sc1\nbuffer_load_dwordx4 a[20:23], v9, s[16:19], s51 offen offset:1024 sc1\n"
"s_add_u32 s50, s50, 28672\ns_add_u32 s51, s51, 28672\nv_lshlrev_b32 v58, 2, v9\ns_mov_b32 s52, 1064960\n"
"buffer_load_dword v56, v58, s[8:11], s52 offen sc1\ns_add_u32 s52, s52, 4096\nbuffer_load_dword v57, v58, s[8:11], s52 offen sc1\nbuffer_load_dword v81, v18, s[8:11], 0 offen sc1\n"
"s_waitcnt vmcnt(0)\nar_x3:\nv_xor_b32 v56, s34, v56\nv_xor_b32 v57, s34, v57\n"
"v_or_b32 v56, v56, v57\nv_cmp_ne_u32 vcc, 0, v56\ns_cbranch_vccz arok_x3\ns_add_u32 s53, s53, 0\n"
"s_add_u32 s49, s49, 1\ns_cmp_gt_u32 s49, 65536\ns_cbranch_scc1 end_x3\ns_sleep 2\n"
"v_lshlrev_b32 v58, 2, v9\ns_mov_b32 s52, 1064960\nbuffer_load_dword v56, v58, s[8:11], s52 offen sc1\ns_add_u32 s52, s52, 4096\n"
"buffer_load_dword v57, v58, s[8:11], s52 offen sc1\ns_waitcnt vmcnt(0)\ns_branch ar_x3\narok_x3:\n"
"f0_x3:\ns_add_u32 s47, s40, 2\ns_min_u32 s47, s47, 64\ns_add_u32 s47, s47, s34\n"
"v_subrev_u32 v81, s47, v81\nv_cmp_lt_u32 vcc, 64, v81\ns_cbranch_vccz f0ok_x3\ns_add_u32 s53, s53, 0\n"
"s_add_u32 s49, s49, 1\ns_cmp_gt_u32 s49, 65536\ns_cbranch_scc1 end_x3\ns_sleep 2\n"
"buffer_load_dword v81, v18, s[8:11], 0 offen sc1\ns_waitcnt vmcnt(0)\ns_branch f0_x3\nf0ok_x3:\n"
"s_barrier\ns_memrealtime s[54:55]\ns_waitcnt lgkmcnt(0)\ns_add_u32 s44, s54, 16\n"
"s_mov_b32 s45, 8\n.p2align 6\ntop_x3:\nv_mov_b32 v44, 0\n"
"v_mov_b32 v45, 0\nv_mov_b32 v46, 0\nv_mov_b32 v47, 0\nv_mov_b32 v48, 0\n"
"v_mov_b32 v49, 0\nv_mov_b32 v50, 0\nv_mov_b32 v51, 0\nv_mov_b32 v74, 0\n"
"s_mul_i32 s48, s45, 6\ns_and_b32 s48, s48, -4\ns_getpc_b64 s[60:61]\nspc_x3:\n"
"s_sub_u32 s48, sgo_x3-spc_x3, s48\ns_add_u32 s60, s60, s48\ns_addc_u32 s61, s61, 0\nsf_x3:\n"
"s_memrealtime s[54:55]\ns_nop 2\ns_memrealtime s[56:57]\ns_nop 2\n"
"s_memrealtime s[58:59]\nsg_x3:\ns_waitcnt lgkmcnt(4)\ns_sub_u32 s47, s54, s44\n"
"s_add_u32 s48, s47, 12\ns_cmp_ge_u32 s48, 12\ns_cbranch_scc1 so_x3\ns_memrealtime s[54:55]\n"
"s_waitcnt lgkmcnt(4)\ns_cmp_ge_u32 s56, s44\ns_cbranch_scc1 sx_x3\ns_memrealtime s[56:57]\n"
"s_waitcnt lgkmcnt(4)\ns_cmp_ge_u32 s58, s44\ns_cbranch_scc1 sx_x3\ns_memrealtime s[58:59]\n"
"s_waitcnt lgkmcnt(4)\ns_cmp_ge_u32 s54, s44\ns_cbranch_scc1 sx_x3\ns_memrealtime s[54:55]\n"
"s_waitcnt lgkmcnt(4)\ns_cmp_ge_u32 s56, s44\ns_cbranch_scc1 sx_x3\ns_memrealtime s[56:57]\n"
"s_waitcnt lgkmcnt(4)\ns_cmp_ge_u32 s58, s44\ns_cbranch_scc1 sx_x3\ns_memrealtime s[58:59]\n"
"s_waitcnt lgkmcnt(4)\ns_cmp_ge_u32 s54, s44\ns_cbranch_scc1 sx_x3\ns_memrealtime s[54:55]\n"
"s_waitcnt lgkmcnt(4)\ns_cmp_ge_u32 s56, s44\ns_cbranch_scc1 sx_x3\ns_memrealtime s[56:57]\n"
"s_waitcnt lgkmcnt(4)\ns_cmp_ge_u32 s58, s44\ns_cbranch_scc1 sx_x3\ns_memrealtime s[58:59]\n"
"s_waitcnt lgkmcnt(4)\ns_cmp_ge_u32 s54, s44\ns_cbranch_scc1 sx_x3\ns_memrealtime s[54:55]\n"
"s_waitcnt lgkmcnt(4)\ns_cmp_ge_u32 s56, s44\ns_cbranch_scc1 sx_x3\ns_memrealtime s[56:57]\n"
"s_waitcnt lgkmcnt(4)\ns_cmp_ge_u32 s58, s44\ns_cbranch_scc1 sx_x3\ns_memrealtime s[58:59]\n"
"s_waitcnt lgkmcnt(4)\ns_cmp_ge_u32 s54, s44\ns_cbranch_scc1 sx_x3\ns_memrealtime s[54:55]\n"
"s_waitcnt lgkmcnt(4)\ns_cmp_ge_u32 s56, s44\ns_cbranch_scc1 sx_x3\ns_memrealtime s[56:57]\n"
"s_waitcnt lgkmcnt(4)\ns_cmp_ge_u32 s58, s44\ns_cbranch_scc1 sx_x3\ns_memrealtime s[58:59]\n"
"s_waitcnt lgkmcnt(4)\ns_cmp_ge_u32 s54, s44\ns_cbranch_scc1 sx_x3\ns_memrealtime s[54:55]\n"
"s_waitcnt lgkmcnt(4)\ns_cmp_ge_u32 s56, s44\ns_cbranch_scc1 sx_x3\ns_memrealtime s[56:57]\n"
"s_branch sx_x3\nso_x3:\ns_waitcnt lgkmcnt(0)\ns_cmp_lt_i32 s47, 0\n"
"s_cbranch_scc1 sf_x3\ns_branch sgo_x3\nsx_x3:\ns_setpc_b64 s[60:61]\n"
"s_nop 0\ns_nop 0\ns_nop 0\ns_nop 0\n"
"s_nop 0\ns_nop 0\ns_nop 0\ns_nop 0\n"
"s_nop 0\ns_nop 0\ns_nop 0\ns_nop 0\n"
"s_nop 0\ns_nop 0\ns_nop 0\ns_nop 0\n"
"s_nop 0\ns_nop 0\ns_nop 0\ns_nop 0\n"
"s_nop 0\ns_nop 0\ns_nop 0\ns_nop 0\n"
"sgo_x3:\ns_nop 15\ns_nop 1\nbuffer_load_dwordx4 v[40:43], v10, s[8:11], s43 offen offset:0 sc1\n"
"v_mov_b32 v75, 0\ns_nop 1\nv_mov_b32 v75, 0\ns_nop 1\n"
"v_mov_b32 v75, 0\ns_nop 1\nv_mov_b32 v75, 0\ns_nop 1\n"
"v_mov_b32 v75, 0\ns_nop 1\nv_mov_b32 v75, 0\ns_nop 1\n"
"v_mov_b32 v75, 0\ns_nop 1\nv_mov_b32 v75, 0\ns_nop 1\n"
"v_mov_b32 v75, 0\ns_nop 1\nv_mov_b32 v75, 0\ns_nop 1\n"
"v_mov_b32 v75, 0\ns_nop 1\nv_mov_b32 v75, 0\ns_nop 1\n"
"v_mov_b32 v75, 0\ns_nop 1\nv_mov_b32 v75, 0\ns_nop 1\n"
"v_mov_b32 v75, 0\ns_nop 1\nv_mov_b32 v75, 0\ns_nop 1\n"
"v_mov_b32 v75, 0\ns_nop 1\nv_mov_b32 v75, 0\ns_nop 1\n"
"v_mov_b32 v75, 0\ns_nop 1\nv_mov_b32 v75, 0\ns_nop 1\n"
"v_mov_b32 v75, 0\ns_nop 1\nv_mov_b32 v75, 0\ns_nop 1\n"
"v_mov_b32 v75, 0\ns_nop 1\nv_mov_b32 v75, 0\ns_nop 1\n"
"v_mov_b32 v75, 0\ns_nop 1\nv_mov_b32 v75, 0\ns_nop 1\n"
"v_mov_b32 v75, 0\ns_nop 1\nv_mov_b32 v75, 0\ns_nop 1\n"
"v_mov_b32 v75, 0\ns_nop 1\nv_mov_b32 v75, 0\ns_nop 1\n"
"v_mov_b32 v75, 0\ns_nop 1\nv_mov_b32 v75, 0\ns_nop 0\n"
"landed_x3:\ns_waitcnt vmcnt(0)\nv_smfmac_f32_16x16x64_bf16 v[44:47], v[40:43], a[0:7], v8\nv_max3_u32 v74, v74, v40, v41\n"
"v_smfmac_f32_16x16x64_bf16 v[48:51], v[40:43], a[16:23], v8\nv_max3_u32 v74, v74, v42, v43\nv_cmp_eq_u32 vcc, -1, v74\ns_nop 0\n"
"v_add_f32 v52, v44, v45\nv_add_f32 v53, v46, v47\ns_nop 1\nv_add_f32 v54, v48, v49\n"
"v_add_f32 v55, v50, v51\nds_write2_b32 v77, v52, v53 offset0:0 offset1:40\nds_write2_b32 v77, v54, v55 offset0:16 offset1:56\ns_cbranch_vccnz retry_x3\n"
"v_mov_b32 v75, 0\ns_nop 1\nv_mov_b32 v75, 0\ns_nop 1\n"
"v_mov_b32 v75, 0\ns_waitcnt lgkmcnt(0)\ns_barrier\nds_read_b64 v[64:65], v78 offset:0\n"
"ds_read_b64 v[66:67], v78 offset:1280\nds_read_b64 v[68:69], v78 offset:2560\nds_read_b64 v[70:71], v78 offset:3840\nv_mov_b32 v75, 0\n"
"s_nop 1\nv_mov_b32 v75, 0\ns_nop 1\nv_mov_b32 v75, 0\n"
"s_nop 1\ns_waitcnt lgkmcnt(2)\nv_pk_add_f32 v[64:65], v[64:65], v[66:67]\ns_waitcnt lgkmcnt(0)\n"
"v_pk_add_f32 v[68:69], v[68:69], v[70:71]\nv_pk_add_f32 v[64:65], v[64:65], v[68:69]\nv_exp_f32 v75, v64\nv_exp_f32 v76, v65\n"
"s_nop 0\nv_cvt_pk_bf16_f32 v75, v75, v76\nbuffer_store_dword v75, v16, s[8:11], s41 offen sc1\ns_barrier\n"
"buffer_load_dword v81, v18, s[8:11], 0 offen sc1\nbuffer_load_dwordx4 a[0:3], v9, s[16:19], s50 offen offset:0 sc1\ns_nop 15\ns_nop 7\n"
"buffer_load_dwordx4 a[4:7], v9, s[16:19], s50 offen offset:1024 sc1\ns_nop 15\ns_nop 7\nbuffer_load_dwordx4 a[16:19], v9, s[16:19], s51 offen offset:0 sc1\n"
"s_nop 15\ns_nop 7\nbuffer_load_dwordx4 a[20:23], v9, s[16:19], s51 offen offset:1024 sc1\ns_nop 15\n"
"s_nop 7\ns_add_u32 s50, s50, 28672\ns_add_u32 s51, s51, 28672\nbuffer_store_dwordx2 v[64:65], v17, s[8:11], s41 offen sc1\n"
"buffer_store_dwordx4 v[60:63], v15, s[8:11], s42 offen sc1\nds_read_b32 v79, v80\nv_xor_b32 v77, 8192, v77\nv_xor_b32 v78, 8192, v78\n"
"s_waitcnt lgkmcnt(0)\nv_readfirstlane_b32 s47, v79\ns_sub_u32 s47, s47, s40\ns_cmp_eq_u32 s47, 1\n"
"s_cbranch_scc1 adopt_x3\nback_x3:\ns_waitcnt vmcnt(6)\nfl_x3:\n"
"s_add_u32 s47, s40, 4\ns_min_u32 s47, s47, 64\ns_add_u32 s47, s47, s34\nv_subrev_u32 v81, s47, v81\n"
"v_cmp_lt_u32 vcc, 64, v81\ns_cbranch_vccz flok_x3\ns_add_u32 s53, s53, 0\ns_add_u32 s49, s49, 1\n"
"s_cmp_gt_u32 s49, 65536\ns_cbranch_scc1 end_x3\ns_sleep 2\nbuffer_load_dword v81, v18, s[8:11], 0 offen sc1\n"
"s_waitcnt vmcnt(0)\ns_branch fl_x3\nflok_x3:\ns_mul_i32 s48, s46, 8\n"
"s_add_u32 s45, s45, s48\ns_add_u32 s45, s45, s32\ns_lshr_b32 s48, s45, 4\ns_lshl_b32 s48, s48, 2\n"
"s_add_u32 s44, s44, s48\ns_and_b32 s45, s45, 15\ns_add_u32 s52, s40, 1\ns_bitcmp1_b64 s[28:29], s52\n"
"s_subb_u32 s46, s46, 0\ns_add_u32 s40, s40, 1\ns_mov_b32 s43, s41\ns_add_u32 s41, s41, 131072\n"
"s_and_b32 s41, s41, 524287\ns_add_u32 s92, s92, 1\ns_xor_b32 s42, s41, 262144\ns_waitcnt lgkmcnt(0)\n"
"s_cmp_lt_u32 s40, 63\ns_cbranch_scc1 top_x3\ns_branch end_x3\nretry2_x3:\n"
"s_sleep 3\nretry_x3:\ns_add_u32 s53, s53, 1\ns_add_u32 s49, s49, 1\n"
"s_cmp_gt_u32 s49, 65536\ns_cbranch_scc1 end_x3\nbuffer_load_dwordx4 v[40:43], v10, s[8:11], s43 offen offset:0 sc1\nv_mov_b32 v74, 0\n"
"s_add_u32 s47, s40, 1\nv_mov_b32 v79, s47\nds_write_b32 v80, v79\ns_waitcnt vmcnt(0)\n"
"v_max3_u32 v74, v74, v40, v41\nv_max3_u32 v74, v74, v42, v43\nv_cmp_eq_u32 vcc, -1, v74\nv_mov_b32 v44, 0\n"
"v_mov_b32 v45, 0\nv_mov_b32 v46, 0\nv_mov_b32 v47, 0\nv_mov_b32 v48, 0\n"
"v_mov_b32 v49, 0\nv_mov_b32 v50, 0\nv_mov_b32 v51, 0\ns_cbranch_vccnz retry2_x3\n"
"s_branch landed_x3\nadopt_x3:\nbuffer_load_dword v56, v28, s[8:11], 0 offen sc1\ns_lshl_b32 s52, s40, 26\n"
"s_or_b32 s47, s52, 0x3ffffff\ns_waitcnt vmcnt(0)\nv_cmp_ge_u32 vcc, s47, v56\ns_nop 1\n"
"v_cndmask_b32 v79, 0, v56, vcc\ns_nop 1\nv_max_u32_dpp v79, v79, v79 row_ror:8 row_mask:0xf bank_mask:0xf\ns_nop 1\n"
"v_max_u32_dpp v79, v79, v79 row_ror:4 row_mask:0xf bank_mask:0xf\ns_nop 1\nv_max_u32_dpp v79, v79, v79 row_ror:2 row_mask:0xf bank_mask:0xf\ns_nop 1\n"
"v_max_u32_dpp v79, v79, v79 row_ror:1 row_mask:0xf bank_mask:0xf\ns_nop 0\nv_readlane_b32 s47, v79, 0\nv_readlane_b32 s48, v79, 16\n"
"s_max_u32 s48, s48, s47\ns_cmp_le_u32 s48, s52\ns_cbranch_scc1 back_x3\ns_bfe_u32 s52, s48, 1179656\n"
"s_add_u32 s52, s52, s30\ns_sub_u32 s45, s52, s44\ns_and_b32 s45, s45, 3\ns_sub_u32 s44, s52, s45\n"
"s_lshl_b32 s45, s45, 2\ns_and_b32 s46, s48, 255\ns_mov_b32 s92, 0\ns_branch back_x3\n"
"end_x3:\ns_branch fk_done\nfk_p0:\ns_mov_b32 s40, 0\n"
"s_mov_b32 s41, 0\ns_mov_b32 s42, 262144\ns_mov_b32 s43, 393216\ns_mov_b32 s44, s30\n"
"s_mov_b32 s45, 0\ns_bcnt1_i32_b64 s46, s[28:29]\ns_mov_b32 s92, 0\ns_mov_b32 s49, 0\n"
"s_mov_b32 s53, 0\ns_mov_b32 s50, s36\ns_mov_b32 s51, s37\nv_mov_b32 v222, 24576\n"
"s_mov_b32 s62, 0x00ff00ff\ns_mov_b32 s63, 0x00ff00ff\nv_cndmask_b32 v223, 0, -1, s[62:63]\nv_not_b32 v224, v223\n"
"v_bfe_u32 v219, v9, 4, 1\nv_lshlrev_b32 v220, 4, v219\nv_mov_b32 v218, 0x3c00\nv_lshlrev_b32 v218, v220, v218\n"
"v_bfe_u32 v220, v9, 5, 3\nv_cmp_eq_u32 vcc, 0, v220\ns_nop 1\nv_cndmask_b32 v168, 0, v218, vcc\n"
"v_cmp_eq_u32 vcc, 2, v220\ns_nop 1\nv_cndmask_b32 v169, 0, v218, vcc\nv_cmp_eq_u32 vcc, 4, v220\n"
"s_nop 1\nv_cndmask_b32 v170, 0, v218, vcc\nv_cmp_eq_u32 vcc, 6, v220\ns_nop 1\n"
"v_cndmask_b32 v171, 0, v218, vcc\nv_cmp_eq_u32 vcc, 1, v220\ns_nop 1\nv_cndmask_b32 v172, 0, v218, vcc\n"
"v_cmp_eq_u32 vcc, 3, v220\ns_nop 1\nv_cndmask_b32 v173, 0, v218, vcc\nv_cmp_eq_u32 vcc, 5, v220\n"
"s_nop 1\nv_cndmask_b32 v174, 0, v218, vcc\nv_cmp_eq_u32 vcc, 7, v220\ns_nop 1\n"
"v_cndmask_b32 v175, 0, v218, vcc\nv_mov_b32 v218, 0x3f803f80\nv_cmp_eq_u32 vcc, 0, v219\ns_nop 1\n"
"v_cndmask_b32 v176, 0, v218, vcc\nv_cndmask_b32 v177, 0, v218, vcc\nv_cndmask_b32 v178, 0, v218, vcc\nv_cndmask_b32 v179, 0, v218, vcc\n"
"v_cndmask_b32 v214, 0, v218, vcc\nv_cmp_eq_u32 vcc, 1, v219\ns_nop 1\nv_cndmask_b32 v180, 0, v218, vcc\n"
"v_cndmask_b32 v181, 0, v218, vcc\nv_cndmask_b32 v182, 0, v218, vcc\nv_cndmask_b32 v183, 0, v218, vcc\nv_cndmask_b32 v215, 0, v218, vcc\n"
"s_sub_u32 s93, s39, 8\ns_lshl_b32 s47, s93, 1\ns_add_u32 s47, s47, 0\ns_sub_u32 s48, s47, 7\n"
"s_cmp_ge_u32 s47, 7\ns_cselect_b32 s47, s48, s47\ns_lshl_b32 s47, s47, 10\nv_add_u32 v226, s47, v9\n"
"s_lshl_b32 s47, s47, 1\ns_add_u32 s81, s47, s35\ns_lshl_b32 s47, s93, 1\ns_add_u32 s47, s47, 1\n"
"s_sub_u32 s48, s47, 7\ns_cmp_ge_u32 s47, 7\ns_cselect_b32 s47, s48, s47\ns_lshl_b32 s47, s47, 10\n"
"v_add_u32 v227, s47, v9\ns_lshl_b32 s47, s47, 1\ns_add_u32 s82, s47, s35\ns_lshl_b32 s47, s93, 1\n"
"s_add_u32 s47, s47, 2\ns_sub_u32 s48, s47, 7\ns_cmp_ge_u32 s47, 7\ns_cselect_b32 s47, s48, s47\n"
"s_lshl_b32 s47, s47, 10\nv_add_u32 v228, s47, v9\ns_lshl_b32 s47, s47, 1\ns_add_u32 s83, s47, s35\n"
"s_lshl_b32 s47, s93, 1\ns_add_u32 s47, s47, 3\ns_sub_u32 s48, s47, 7\ns_cmp_ge_u32 s47, 7\n"
"s_cselect_b32 s47, s48, s47\ns_lshl_b32 s47, s47, 10\nv_add_u32 v229, s47, v9\ns_lshl_b32 s47, s47, 1\n"
"s_add_u32 s84, s47, s35\ns_lshl_b32 s47, s93, 1\ns_add_u32 s47, s47, 4\ns_sub_u32 s48, s47, 7\n"
"s_cmp_ge_u32 s47, 7\ns_cselect_b32 s47, s48, s47\ns_lshl_b32 s47, s47, 10\nv_add_u32 v230, s47, v9\n"
"s_lshl_b32 s47, s47, 1\ns_add_u32 s85, s47, s35\ns_lshl_b32 s47, s93, 1\ns_add_u32 s47, s47, 5\n"
"s_sub_u32 s48, s47, 7\ns_cmp_ge_u32 s47, 7\ns_cselect_b32 s47, s48, s47\ns_lshl_b32 s47, s47, 10\n"
"v_add_u32 v231, s47, v9\ns_lshl_b32 s47, s47, 1\ns_add_u32 s86, s47, s35\ns_lshl_b32 s47, s93, 1\n"
"s_add_u32 s47, s47, 6\ns_sub_u32 s48, s47, 7\ns_cmp_ge_u32 s47, 7\ns_cselect_b32 s47, s48, s47\n"
"s_lshl_b32 s47, s47, 10\nv_add_u32 v232, s47, v9\ns_lshl_b32 s47, s47, 1\ns_add_u32 s87, s47, s35\n"
"s_add_u32 s47, s81, 0\nbuffer_load_dwordx4 a[0:3], v9, s[12:15], s47 offen sc1\nbuffer_load_dwordx4 a[4:7], v9, s[12:15], s47 offen offset:1024 sc1\ns_add_u32 s47, s82, 0\n"
"buffer_load_dwordx4 a[8:11], v9, s[12:15], s47 offen sc1\nbuffer_load_dwordx4 a[12:15], v9, s[12:15], s47 offen offset:1024 sc1\ns_add_u32 s47, s83, 0\nbuffer_load_dwordx4 a[16:19], v9, s[12:15], s47 offen sc1\n"
"buffer_load_dwordx4 a[20:23], v9, s[12:15], s47 offen offset:1024 sc1\ns_add_u32 s47, s84, 0\nbuffer_load_dwordx4 a[24:27], v9, s[12:15], s47 offen sc1\nbuffer_load_dwordx4 a[28:31], v9, s[12:15], s47 offen offset:1024 sc1\n"
"s_add_u32 s47, s85, 0\nbuffer_load_dwordx4 a[32:35], v9, s[12:15], s47 offen sc1\nbuffer_load_dwordx4 a[36:39], v9, s[12:15], s47 offen offset:1024 sc1\ns_add_u32 s47, s86, 0\n"
"buffer_load_dwordx4 a[40:43], v9, s[12:15], s47 offen sc1\nbuffer_load_dwordx4 a[44:47], v9, s[12:15], s47 offen offset:1024 sc1\ns_add_u32 s47, s87, 0\nbuffer_load_dwordx4 a[48:51], v9, s[12:15], s47 offen sc1\n"
"buffer_load_dwordx4 a[52:55], v9, s[12:15], s47 offen offset:1024 sc1\ns_mul_i32 s47, s93, 2016\ns_getpc_b64 s[60:61]\ndp_p0:\n"
"s_add_u32 s47, s47, va0_p0-dp_p0+4\ns_add_u32 s60, s60, s47\ns_addc_u32 s61, s61, 0\ns_mov_b64 exec, 0\n"
"s_setpc_b64 s[60:61]\nwarm_p0:\ns_mov_b64 exec, -1\ns_waitcnt vmcnt(16)\n"
"s_add_u32 s47, s81, 14336\nbuffer_load_dwordx4 a[56:59], v9, s[12:15], s47 offen sc1\nbuffer_load_dwordx4 a[60:63], v9, s[12:15], s47 offen offset:1024 sc1\ns_add_u32 s47, s82, 14336\n"
"buffer_load_dwordx4 a[64:67], v9, s[12:15], s47 offen sc1\nbuffer_load_dwordx4 a[68:71], v9, s[12:15], s47 offen offset:1024 sc1\ns_add_u32 s47, s83, 14336\nbuffer_load_dwordx4 a[72:75], v9, s[12:15], s47 offen sc1\n"
"buffer_load_dwordx4 a[76:79], v9, s[12:15], s47 offen offset:1024 sc1\ns_add_u32 s47, s84, 14336\nbuffer_load_dwordx4 a[80:83], v9, s[12:15], s47 offen sc1\nbuffer_load_dwordx4 a[84:87], v9, s[12:15], s47 offen offset:1024 sc1\n"
"s_add_u32 s47, s85, 14336\nbuffer_load_dwordx4 a[88:91], v9, s[12:15], s47 offen sc1\nbuffer_load_dwordx4 a[92:95], v9, s[12:15], s47 offen offset:1024 sc1\ns_add_u32 s47, s86, 14336\n"
"buffer_load_dwordx4 a[96:99], v9, s[12:15], s47 offen sc1\nbuffer_load_dwordx4 a[100:103], v9, s[12:15], s47 offen offset:1024 sc1\ns_add_u32 s47, s87, 14336\nbuffer_load_dwordx4 a[104:107], v9, s[12:15], s47 offen sc1\n"
"buffer_load_dwordx4 a[108:111], v9, s[12:15], s47 offen offset:1024 sc1\ns_add_u32 s47, s81, 28672\nbuffer_load_dwordx4 a[112:115], v9, s[12:15], s47 offen sc1\nbuffer_load_dwordx4 a[116:119], v9, s[12:15], s47 offen offset:1024 sc1\n"
"s_add_u32 s47, s82, 28672\nbuffer_load_dwordx4 a[120:123], v9, s[12:15], s47 offen sc1\nbuffer_load_dwordx4 a[124:127], v9, s[12:15], s47 offen offset:1024 sc1\ns_add_u32 s47, s83, 28672\n"
"buffer_load_dwordx4 a[128:131], v9, s[12:15], s47 offen sc1\nbuffer_load_dwordx4 a[132:135], v9, s[12:15], s47 offen offset:1024 sc1\ns_add_u32 s47, s84, 28672\nbuffer_load_dwordx4 a[136:139], v9, s[12:15], s47 offen sc1\n"
"buffer_load_dwordx4 a[140:143], v9, s[12:15], s47 offen offset:1024 sc1\ns_add_u32 s47, s85, 28672\nbuffer_load_dwordx4 a[144:147], v9, s[12:15], s47 offen sc1\nbuffer_load_dwordx4 a[148:151], v9, s[12:15], s47 offen offset:1024 sc1\n"
"s_add_u32 s47, s86, 28672\nbuffer_load_dwordx4 a[152:155], v9, s[12:15], s47 offen sc1\nbuffer_load_dwordx4 a[156:159], v9, s[12:15], s47 offen offset:1024 sc1\ns_add_u32 s47, s87, 28672\n"
"buffer_load_dwordx4 a[160:163], v9, s[12:15], s47 offen sc1\nbuffer_load_dwordx4 a[164:167], v9, s[12:15], s47 offen offset:1024 sc1\ns_add_u32 s47, s81, 43008\nbuffer_load_dwordx4 a[168:171], v9, s[12:15], s47 offen sc1\n"
"buffer_load_dwordx4 a[172:175], v9, s[12:15], s47 offen offset:1024 sc1\ns_add_u32 s47, s82, 43008\nbuffer_load_dwordx4 a[176:179], v9, s[12:15], s47 offen sc1\nbuffer_load_dwordx4 a[180:183], v9, s[12:15], s47 offen offset:1024 sc1\n"
"s_add_u32 s47, s83, 43008\nbuffer_load_dwordx4 a[184:187], v9, s[12:15], s47 offen sc1\nbuffer_load_dwordx4 a[188:191], v9, s[12:15], s47 offen offset:1024 sc1\ns_add_u32 s47, s84, 43008\n"
"buffer_load_dwordx4 a[192:195], v9, s[12:15], s47 offen sc1\nbuffer_load_dwordx4 a[196:199], v9, s[12:15], s47 offen offset:1024 sc1\ns_add_u32 s47, s85, 43008\nbuffer_load_dwordx4 a[200:203], v9, s[12:15], s47 offen sc1\n"
"buffer_load_dwordx4 a[204:207], v9, s[12:15], s47 offen offset:1024 sc1\ns_add_u32 s47, s86, 43008\nbuffer_load_dwordx4 a[208:211], v9, s[12:15], s47 offen sc1\nbuffer_load_dwordx4 a[212:215], v9, s[12:15], s47 offen offset:1024 sc1\n"
"s_add_u32 s47, s87, 43008\nbuffer_load_dwordx4 a[216:219], v9, s[12:15], s47 offen sc1\nbuffer_load_dwordx4 a[220:223], v9, s[12:15], s47 offen offset:1024 sc1\nv_readfirstlane_b32 s93, v13\n"
"v_lshlrev_b32 v166, 2, v9\ns_mov_b32 s52, 1064960\nbuffer_load_dword v164, v166, s[8:11], s52 offen sc1\ns_add_u32 s52, s52, 4096\n"
"buffer_load_dword v165, v166, s[8:11], s52 offen sc1\nbuffer_load_dword v218, v11, s[8:11], 0 offen sc1\ns_mov_b64 s[94:95], 1\ns_barrier\n"
"s_waitcnt vmcnt(0)\nar_p0:\nv_xor_b32 v164, s34, v164\nv_xor_b32 v165, s34, v165\n"
"v_or_b32 v164, v164, v165\nv_cmp_ne_u32 vcc, 0, v164\ns_cbranch_vccz arok_p0\ns_add_u32 s53, s53, 0\n"
"s_add_u32 s49, s49, 1\ns_cmp_gt_u32 s49, 65536\ns_cbranch_scc1 end_p0\ns_sleep 2\n"
"v_lshlrev_b32 v166, 2, v9\ns_mov_b32 s52, 1064960\nbuffer_load_dword v164, v166, s[8:11], s52 offen sc1\ns_add_u32 s52, s52, 4096\n"
"buffer_load_dword v165, v166, s[8:11], s52 offen sc1\ns_waitcnt vmcnt(0)\ns_branch ar_p0\narok_p0:\n"
"fl_p0:\ns_add_u32 s47, s40, 2\ns_min_u32 s47, s47, 64\ns_add_u32 s47, s47, s34\n"
"v_subrev_u32 v218, s47, v218\nv_cmp_lt_u32 vcc, 64, v218\ns_cbranch_vccz flok_p0\ns_add_u32 s53, s53, 0\n"
"s_add_u32 s49, s49, 1\ns_cmp_gt_u32 s49, 65536\ns_cbranch_scc1 end_p0\ns_sleep 2\n"
"buffer_load_dword v218, v11, s[8:11], 0 offen sc1\ns_waitcnt vmcnt(0)\ns_branch fl_p0\nflok_p0:\n"
"buffer_load_dwordx4 v[100:103], v9, s[16:19], s50 offen offset:0 sc1\nbuffer_load_dwordx4 v[104:107], v9, s[16:19], s50 offen offset:1024 sc1\nbuffer_load_dwordx4 v[108:111], v9, s[16:19], s50 offen offset:2048 sc1\nbuffer_load_dwordx4 v[112:115], v9, s[16:19], s50 offen offset:3072 sc1\n"
"s_add_u32 s50, s50, 16384\ns_memrealtime s[54:55]\ns_waitcnt lgkmcnt(0)\ns_add_u32 s44, s54, 12\n"
"s_mov_b32 s45, 8\ns_mov_b32 s42, 1\ns_mul_i32 s48, s45, 6\ns_and_b32 s48, s48, -4\n"
"s_getpc_b64 s[88:89]\nmpc_p0:\ns_sub_u32 s48, mgo_p0-mpc_p0, s48\ns_add_u32 s88, s88, s48\n"
"s_addc_u32 s89, s89, 0\ns_branch mf_p0\n.p2align 6\ntop_p0:\n"
"mf_p0:\ns_memrealtime s[54:55]\ns_nop 2\ns_memrealtime s[56:57]\n"
"s_nop 2\ns_memrealtime s[58:59]\nmg_p0:\ns_waitcnt lgkmcnt(4)\n"
"s_sub_u32 s47, s54, s44\ns_add_u32 s48, s47, 12\ns_cmp_ge_u32 s48, 12\ns_cbranch_scc1 mo_p0\n"
"s_memrealtime s[54:55]\ns_waitcnt lgkmcnt(4)\ns_cmp_ge_u32 s56, s44\ns_cbranch_scc1 mx_p0\n"
"s_memrealtime s[56:57]\ns_waitcnt lgkmcnt(4)\ns_cmp_ge_u32 s58, s44\ns_cbranch_scc1 mx_p0\n"
"s_memrealtime s[58:59]\ns_waitcnt lgkmcnt(4)\ns_cmp_ge_u32 s54, s44\ns_cbranch_scc1 mx_p0\n"
"s_memrealtime s[54:55]\ns_waitcnt lgkmcnt(4)\ns_cmp_ge_u32 s56, s44\ns_cbranch_scc1 mx_p0\n"
"s_memrealtime s[56:57]\ns_waitcnt lgkmcnt(4)\ns_cmp_ge_u32 s58, s44\ns_cbranch_scc1 mx_p0\n"
"s_memrealtime s[58:59]\ns_waitcnt lgkmcnt(4)\ns_cmp_ge_u32 s54, s44\ns_cbranch_scc1 mx_p0\n"
"s_memrealtime s[54:55]\ns_waitcnt lgkmcnt(4)\ns_cmp_ge_u32 s56, s44\ns_cbranch_scc1 mx_p0\n"
"s_memrealtime s[56:57]\ns_waitcnt lgkmcnt(4)\ns_cmp_ge_u32 s58, s44\ns_cbranch_scc1 mx_p0\n"
"s_memrealtime s[58:59]\ns_waitcnt lgkmcnt(4)\ns_cmp_ge_u32 s54, s44\ns_cbranch_scc1 mx_p0\n"
"s_memrealtime s[54:55]\ns_waitcnt lgkmcnt(4)\ns_cmp_ge_u32 s56, s44\ns_cbranch_scc1 mx_p0\n"
"s_memrealtime s[56:57]\ns_waitcnt lgkmcnt(4)\ns_cmp_ge_u32 s58, s44\ns_cbranch_scc1 mx_p0\n"
"s_memrealtime s[58:59]\ns_waitcnt lgkmcnt(4)\ns_cmp_ge_u32 s54, s44\ns_cbranch_scc1 mx_p0\n"
"s_memrealtime s[54:55]\ns_waitcnt lgkmcnt(4)\ns_cmp_ge_u32 s56, s44\ns_cbranch_scc1 mx_p0\n"
"s_memrealtime s[56:57]\ns_waitcnt lgkmcnt(4)\ns_cmp_ge_u32 s58, s44\ns_cbranch_scc1 mx_p0\n"
"s_memrealtime s[58:59]\ns_waitcnt lgkmcnt(4)\ns_cmp_ge_u32 s54, s44\ns_cbranch_scc1 mx_p0\n"
"s_memrealtime s[54:55]\ns_waitcnt lgkmcnt(4)\ns_cmp_ge_u32 s56, s44\ns_cbranch_scc1 mx_p0\n"
"s_memrealtime s[56:57]\ns_branch mx_p0\nmo_p0:\ns_waitcnt lgkmcnt(0)\n"
"s_cmp_lt_i32 s47, 0\ns_cbranch_scc1 mf_p0\ns_branch mgo_p0\nmx_p0:\n"
"s_setpc_b64 s[88:89]\ns_nop 0\ns_nop 0\ns_nop 0\n"
"s_nop 0\ns_nop 0\ns_nop 0\ns_nop 0\n"
"s_nop 0\ns_nop 0\ns_nop 0\ns_nop 0\n"
"s_nop 0\ns_nop 0\ns_nop 0\ns_nop 0\n"
"s_nop 0\ns_nop 0\ns_nop 0\ns_nop 0\n"
"s_nop 0\ns_nop 0\ns_nop 0\ns_nop 0\n"
"s_nop 0\nmgo_p0:\ntop2_p0:\nbuffer_load_dwordx4 v[40:43], v10, s[8:11], s43 offen sc1\n"
"buffer_load_dwordx4 v[44:47], v12, s[8:11], s43 offen sc1\nv_mov_b32 v68, 0\nv_mov_b32 v69, 0\nv_mov_b32 v70, 0\n"
"v_mov_b32 v71, 0\nv_mov_b32 v72, 0\nv_mov_b32 v73, 0\nv_mov_b32 v74, 0\n"
"v_mov_b32 v75, 0\nv_mov_b32 v76, 0\nv_mov_b32 v77, 0\nv_mov_b32 v78, 0\n"
"v_mov_b32 v79, 0\nv_mov_b32 v80, 0\nv_mov_b32 v81, 0\nv_mov_b32 v82, 0\n"
"v_mov_b32 v83, 0\nv_mov_b32 v218, 0\ns_cmp_eq_u32 s42, 0\ns_cbranch_scc1 np_p0\n"
"s_mov_b32 s42, 0\ns_and_b32 s47, s46, 15\ns_mul_i32 s47, s47, 4\ns_add_u32 s47, s47, s31\n"
"s_add_u32 s47, s47, s45\ns_and_b32 s91, s47, 15\ns_lshr_b32 s47, s47, 4\ns_lshl_b32 s47, s47, 2\n"
"s_add_u32 s90, s44, s47\ns_mul_i32 s47, s91, 6\ns_add_u32 s47, s47, s93\ns_lshr_b32 s47, s47, 4\n"
"s_mul_i32 s47, s47, 244\ns_getpc_b64 s[60:61]\nep1_p0:\ns_add_u32 s47, s47, sgo_p0-ep1_p0\n"
"s_add_u32 s60, s60, s47\ns_addc_u32 s61, s61, 0\ns_mul_i32 s48, s46, 8\ns_add_u32 s45, s45, s48\n"
"s_add_u32 s45, s45, s32\ns_lshr_b32 s48, s45, 4\ns_lshl_b32 s48, s48, 2\ns_add_u32 s44, s44, s48\n"
"s_and_b32 s45, s45, 15\ns_add_u32 s52, s40, 1\ns_bitcmp1_b64 s[28:29], s52\ns_subb_u32 s46, s46, 0\n"
"s_mul_i32 s48, s45, 6\ns_and_b32 s48, s48, -4\ns_getpc_b64 s[88:89]\nmpc1_p0:\n"
"s_sub_u32 s48, mgo_p0-mpc1_p0, s48\ns_add_u32 s88, s88, s48\ns_addc_u32 s89, s89, -1\nnp_p0:\n"
"s_waitcnt vmcnt(1)\nds_write_b128 v226, v[40:43]\nv_max3_u32 v218, v218, v40, v41\nv_max3_u32 v218, v218, v42, v43\n"
"s_waitcnt vmcnt(0)\nds_write_b128 v227, v[44:47]\nv_max3_u32 v218, v218, v44, v45\nv_max3_u32 v218, v218, v46, v47\n"
"v_cmp_eq_u32 vcc, -1, v218\nv_smfmac_f32_16x16x64_bf16 v[68:71], v[40:43], a[0:7], v8\nv_mov_b32 v84, v100\nv_mov_b32 v85, v101\n"
"v_mov_b32 v86, v102\nv_mov_b32 v87, v103\nv_mov_b32 v88, v104\nv_smfmac_f32_16x16x64_bf16 v[72:75], v[40:43], a[56:63], v8\n"
"v_mov_b32 v89, v105\nv_mov_b32 v90, v106\nv_mov_b32 v91, v107\nv_mov_b32 v92, v108\n"
"v_mov_b32 v93, v109\ns_cbranch_vccnz retry1_p0\ns_waitcnt lgkmcnt(0)\ns_barrier\n"
"ds_read_b32 v221, v222\nds_read_b128 v[48:51], v228\nds_read_b128 v[52:55], v229\nds_read_b128 v[56:59], v230\n"
"ds_read_b128 v[60:63], v231\nds_read_b128 v[64:67], v232\nv_smfmac_f32_16x16x64_bf16 v[76:79], v[40:43], a[112:119], v8\nv_mov_b32 v94, v110\n"
"v_smfmac_f32_16x16x64_bf16 v[80:83], v[40:43], a[168:175], v8\nv_mov_b32 v95, v111\nv_smfmac_f32_16x16x64_bf16 v[68:71], v[44:47], a[8:15], v8\nv_mov_b32 v96, v112\n"
"v_smfmac_f32_16x16x64_bf16 v[72:75], v[44:47], a[64:71], v8\nv_mov_b32 v97, v113\ns_waitcnt lgkmcnt(0)\nv_readfirstlane_b32 s47, v221\n"
"s_memrealtime s[54:55]\nv_smfmac_f32_16x16x64_bf16 v[76:79], v[44:47], a[120:127], v8\nv_mov_b32 v98, v114\ns_memrealtime s[56:57]\n"
"v_smfmac_f32_16x16x64_bf16 v[80:83], v[44:47], a[176:183], v8\nv_mov_b32 v99, v115\ns_cmp_eq_u32 s40, 63\ns_cbranch_scc1 last_p0\n"
"s_memrealtime s[58:59]\ns_sub_u32 s47, s47, s40\ns_cmp_eq_u32 s47, 1\ns_cbranch_scc1 adopt_p0\n"
"s_waitcnt lgkmcnt(4)\ns_sub_u32 s47, s54, s90\ns_cmp_ge_i32 s47, 0\ns_cbranch_scc1 so_p0\n"
"s_memrealtime s[54:55]\nv_smfmac_f32_16x16x64_bf16 v[68:71], v[48:51], a[16:23], v8\ns_waitcnt lgkmcnt(4)\ns_cmp_ge_u32 s56, s90\n"
"s_cbranch_scc1 sx1_p0\ns_memrealtime s[56:57]\nv_smfmac_f32_16x16x64_bf16 v[72:75], v[48:51], a[72:79], v8\ns_waitcnt lgkmcnt(4)\n"
"s_cmp_ge_u32 s58, s90\ns_cbranch_scc1 sx2_p0\ns_memrealtime s[58:59]\nv_smfmac_f32_16x16x64_bf16 v[76:79], v[48:51], a[128:135], v8\n"
"s_waitcnt lgkmcnt(4)\ns_cmp_ge_u32 s54, s90\ns_cbranch_scc1 sx3_p0\ns_memrealtime s[54:55]\n"
"v_smfmac_f32_16x16x64_bf16 v[80:83], v[48:51], a[184:191], v8\ns_waitcnt lgkmcnt(4)\ns_cmp_ge_u32 s56, s90\ns_cbranch_scc1 sx4_p0\n"
"s_memrealtime s[56:57]\nv_smfmac_f32_16x16x64_bf16 v[68:71], v[52:55], a[24:31], v8\ns_waitcnt lgkmcnt(4)\ns_cmp_ge_u32 s58, s90\n"
"s_cbranch_scc1 sx5_p0\ns_memrealtime s[58:59]\nv_smfmac_f32_16x16x64_bf16 v[72:75], v[52:55], a[80:87], v8\ns_waitcnt lgkmcnt(4)\n"
"s_cmp_ge_u32 s54, s90\ns_cbranch_scc1 sx6_p0\ns_memrealtime s[54:55]\nv_smfmac_f32_16x16x64_bf16 v[76:79], v[52:55], a[136:143], v8\n"
"s_waitcnt lgkmcnt(4)\ns_cmp_ge_u32 s56, s90\ns_cbranch_scc1 sx7_p0\ns_memrealtime s[56:57]\n"
"v_smfmac_f32_16x16x64_bf16 v[80:83], v[52:55], a[192:199], v8\ns_waitcnt lgkmcnt(4)\ns_cmp_ge_u32 s58, s90\ns_cbranch_scc1 sx8_p0\n"
"s_memrealtime s[58:59]\nv_smfmac_f32_16x16x64_bf16 v[68:71], v[56:59], a[32:39], v8\ns_waitcnt lgkmcnt(4)\ns_cmp_ge_u32 s54, s90\n"
"s_cbranch_scc1 sx9_p0\ns_memrealtime s[54:55]\nv_smfmac_f32_16x16x64_bf16 v[72:75], v[56:59], a[88:95], v8\ns_waitcnt lgkmcnt(4)\n"
"s_cmp_ge_u32 s56, s90\ns_cbranch_scc1 sx10_p0\ns_memrealtime s[56:57]\nv_smfmac_f32_16x16x64_bf16 v[76:79], v[56:59], a[144:151], v8\n"
"s_waitcnt lgkmcnt(4)\ns_cmp_ge_u32 s58, s90\ns_cbranch_scc1 sx11_p0\ns_memrealtime s[58:59]\n"
"v_smfmac_f32_16x16x64_bf16 v[80:83], v[56:59], a[200:207], v8\ns_waitcnt lgkmcnt(4)\ns_cmp_ge_u32 s54, s90\ns_cbranch_scc1 sx12_p0\n"
"s_memrealtime s[54:55]\nv_smfmac_f32_16x16x64_bf16 v[68:71], v[60:63], a[40:47], v8\ns_waitcnt lgkmcnt(4)\ns_cmp_ge_u32 s56, s90\n"
"s_cbranch_scc1 sx13_p0\ns_memrealtime s[56:57]\nv_smfmac_f32_16x16x64_bf16 v[72:75], v[60:63], a[96:103], v8\ns_waitcnt lgkmcnt(4)\n"
"s_cmp_ge_u32 s58, s90\ns_cbranch_scc1 sx14_p0\ns_memrealtime s[58:59]\nv_smfmac_f32_16x16x64_bf16 v[76:79], v[60:63], a[152:159], v8\n"
"s_waitcnt lgkmcnt(4)\ns_cmp_ge_u32 s54, s90\ns_cbranch_scc1 sx15_p0\ns_memrealtime s[54:55]\n"
"v_smfmac_f32_16x16x64_bf16 v[80:83], v[60:63], a[208:215], v8\ns_waitcnt lgkmcnt(4)\ns_cmp_ge_u32 s56, s90\ns_cbranch_scc1 sx16_p0\n"
"s_memrealtime s[56:57]\nv_smfmac_f32_16x16x64_bf16 v[68:71], v[64:67], a[48:55], v8\ns_waitcnt lgkmcnt(4)\ns_cmp_ge_u32 s58, s90\n"
"s_cbranch_scc1 sx17_p0\ns_memrealtime s[58:59]\nv_smfmac_f32_16x16x64_bf16 v[72:75], v[64:67], a[104:111], v8\ns_waitcnt lgkmcnt(4)\n"
"s_cmp_ge_u32 s54, s90\ns_cbranch_scc1 sx18_p0\ns_memrealtime s[54:55]\nv_smfmac_f32_16x16x64_bf16 v[76:79], v[64:67], a[160:167], v8\n"
"s_waitcnt lgkmcnt(4)\ns_cmp_ge_u32 s56, s90\ns_cbranch_scc1 sx19_p0\ns_memrealtime s[56:57]\n"
"v_smfmac_f32_16x16x64_bf16 v[80:83], v[64:67], a[216:223], v8\ns_waitcnt lgkmcnt(4)\ns_cmp_ge_u32 s58, s90\ns_cbranch_scc1 sx20_p0\n"
"s_memrealtime s[58:59]\nsp_p0:\ns_waitcnt lgkmcnt(4)\ns_sub_u32 s47, s54, s90\n"
"s_cmp_ge_i32 s47, 0\ns_cbranch_scc1 sx20_p0\ns_memrealtime s[54:55]\ns_waitcnt lgkmcnt(4)\n"
"s_sub_u32 s47, s56, s90\ns_cmp_ge_i32 s47, 0\ns_cbranch_scc1 sx20_p0\ns_memrealtime s[56:57]\n"
"s_waitcnt lgkmcnt(4)\ns_sub_u32 s47, s58, s90\ns_cmp_ge_i32 s47, 0\ns_cbranch_scc1 sx20_p0\n"
"s_memrealtime s[58:59]\ns_waitcnt lgkmcnt(4)\ns_sub_u32 s47, s54, s90\ns_cmp_ge_i32 s47, 0\n"
"s_cbranch_scc1 sx20_p0\ns_memrealtime s[54:55]\ns_waitcnt lgkmcnt(4)\ns_sub_u32 s47, s56, s90\n"
"s_cmp_ge_i32 s47, 0\ns_cbranch_scc1 sx20_p0\ns_memrealtime s[56:57]\ns_waitcnt lgkmcnt(4)\n"
"s_sub_u32 s47, s58, s90\ns_cmp_ge_i32 s47, 0\ns_cbranch_scc1 sx20_p0\ns_memrealtime s[58:59]\n"
"s_branch sp_p0\nso_p0:\ns_cmp_gt_i32 s47, 4\ns_cbranch_scc1 late_p0\n"
"s_branch sgo_p0\nsx1_p0:\ns_add_u32 s60, s60, 252\ns_addc_u32 s61, s61, 0\n"
"s_setpc_b64 s[60:61]\nsx2_p0:\ns_add_u32 s60, s60, 504\ns_addc_u32 s61, s61, 0\n"
"s_setpc_b64 s[60:61]\nsx3_p0:\ns_add_u32 s60, s60, 756\ns_addc_u32 s61, s61, 0\n"
"s_setpc_b64 s[60:61]\nsx4_p0:\ns_add_u32 s60, s60, 1008\ns_addc_u32 s61, s61, 0\n"
"s_setpc_b64 s[60:61]\nsx5_p0:\ns_add_u32 s60, s60, 1260\ns_addc_u32 s61, s61, 0\n"
"s_setpc_b64 s[60:61]\nsx6_p0:\ns_add_u32 s60, s60, 1512\ns_addc_u32 s61, s61, 0\n"
"s_setpc_b64 s[60:61]\nsx7_p0:\ns_add_u32 s60, s60, 1764\ns_addc_u32 s61, s61, 0\n"
"s_setpc_b64 s[60:61]\nsx8_p0:\ns_add_u32 s60, s60, 2016\ns_addc_u32 s61, s61, 0\n"
"s_setpc_b64 s[60:61]\nsx9_p0:\ns_add_u32 s60, s60, 2268\ns_addc_u32 s61, s61, 0\n"
"s_setpc_b64 s[60:61]\nsx10_p0:\ns_add_u32 s60, s60, 2520\ns_addc_u32 s61, s61, 0\n"
"s_setpc_b64 s[60:61]\nsx11_p0:\ns_add_u32 s60, s60, 2772\ns_addc_u32 s61, s61, 0\n"
"s_setpc_b64 s[60:61]\nsx12_p0:\ns_add_u32 s60, s60, 3024\ns_addc_u32 s61, s61, 0\n"
"s_setpc_b64 s[60:61]\nsx13_p0:\ns_add_u32 s60, s60, 3276\ns_addc_u32 s61, s61, 0\n"
"s_setpc_b64 s[60:61]\nsx14_p0:\ns_add_u32 s60, s60, 3528\ns_addc_u32 s61, s61, 0\n"
"s_setpc_b64 s[60:61]\nsx15_p0:\ns_add_u32 s60, s60, 3780\ns_addc_u32 s61, s61, 0\n"
"s_setpc_b64 s[60:61]\nsx16_p0:\ns_add_u32 s60, s60, 4032\ns_addc_u32 s61, s61, 0\n"
"s_setpc_b64 s[60:61]\nsx17_p0:\ns_add_u32 s60, s60, 4284\ns_addc_u32 s61, s61, 0\n"
"s_setpc_b64 s[60:61]\nsx18_p0:\ns_add_u32 s60, s60, 4536\ns_addc_u32 s61, s61, 0\n"
"s_setpc_b64 s[60:61]\nsx19_p0:\ns_add_u32 s60, s60, 4788\ns_addc_u32 s61, s61, 0\n"
"s_setpc_b64 s[60:61]\nsx20_p0:\ns_add_u32 s60, s60, 5040\ns_addc_u32 s61, s61, 0\n"
"s_setpc_b64 s[60:61]\ntl8_p0:\nv_smfmac_f32_16x16x64_bf16 v[68:71], v[56:59], a[32:39], v8\ntl9_p0:\n"
"v_smfmac_f32_16x16x64_bf16 v[72:75], v[56:59], a[88:95], v8\ntl10_p0:\nv_smfmac_f32_16x16x64_bf16 v[76:79], v[56:59], a[144:151], v8\ntl11_p0:\n"
"v_smfmac_f32_16x16x64_bf16 v[80:83], v[56:59], a[200:207], v8\ntl12_p0:\nv_smfmac_f32_16x16x64_bf16 v[68:71], v[60:63], a[40:47], v8\ntl13_p0:\n"
"v_smfmac_f32_16x16x64_bf16 v[72:75], v[60:63], a[96:103], v8\ntl14_p0:\nv_smfmac_f32_16x16x64_bf16 v[76:79], v[60:63], a[152:159], v8\ntl15_p0:\n"
"v_smfmac_f32_16x16x64_bf16 v[80:83], v[60:63], a[208:215], v8\ntl16_p0:\nv_smfmac_f32_16x16x64_bf16 v[68:71], v[64:67], a[48:55], v8\ntl17_p0:\n"
"v_smfmac_f32_16x16x64_bf16 v[72:75], v[64:67], a[104:111], v8\ntl18_p0:\nv_smfmac_f32_16x16x64_bf16 v[76:79], v[64:67], a[160:167], v8\ntl19_p0:\n"
"v_smfmac_f32_16x16x64_bf16 v[80:83], v[64:67], a[216:223], v8\ntl20_p0:\nv_add_f32 v219, v68, v69\nv_add_f32 v220, v70, v71\n"
"v_cvt_pk_bf16_f32 v156, v219, v220\ntl21_p0:\nv_add_f32 v219, v72, v73\nv_add_f32 v220, v74, v75\n"
"v_cvt_pk_bf16_f32 v157, v219, v220\ntl22_p0:\nv_add_f32 v219, v76, v77\nv_add_f32 v220, v78, v79\n"
"v_cvt_pk_bf16_f32 v158, v219, v220\ntl23_p0:\nv_add_f32 v219, v80, v81\nv_add_f32 v220, v82, v83\n"
"v_cvt_pk_bf16_f32 v159, v219, v220\ntl24_p0:\nbuffer_load_dwordx4 v[100:103], v9, s[16:19], s50 offen offset:0 sc1\nbuffer_load_dwordx4 v[104:107], v9, s[16:19], s50 offen offset:1024 sc1\n"
"buffer_load_dwordx4 v[108:111], v9, s[16:19], s50 offen offset:2048 sc1\nbuffer_load_dwordx4 v[112:115], v9, s[16:19], s50 offen offset:3072 sc1\ns_add_u32 s50, s50, 16384\nbuffer_store_dwordx4 v[156:159], v9, s[20:23], s51 offen sc1\n"
"s_waitcnt vmcnt(8)\nv_mfma_f32_16x16x32_bf16 v[132:135], v[84:87], v[116:119], 0\nv_mfma_f32_16x16x32_bf16 v[136:139], v[88:91], v[116:119], 0\nv_mfma_f32_16x16x32_bf16 v[148:151], v[176:179], v[116:119], 0\n"
"s_waitcnt vmcnt(7)\nv_mfma_f32_16x16x32_bf16 v[140:143], v[92:95], v[120:123], 0\nv_mfma_f32_16x16x32_bf16 v[144:147], v[96:99], v[120:123], 0\nv_mfma_f32_16x16x32_bf16 v[148:151], v[180:183], v[120:123], v[148:151]\n"
"v_cndmask_b32 v132, v136, v132, s[62:63]\nv_cndmask_b32 v133, v137, v133, s[62:63]\nv_cndmask_b32 v134, v138, v134, s[62:63]\nv_cndmask_b32 v135, v139, v135, s[62:63]\n"
"s_waitcnt vmcnt(6)\nv_mfma_f32_16x16x32_f16 v[152:155], v[168:171], v[124:127], 0\ns_waitcnt vmcnt(5)\nv_mfma_f32_16x16x32_f16 v[152:155], v[172:175], v[128:131], v[152:155]\n"
"v_cndmask_b32 v140, v144, v140, s[62:63]\nv_cndmask_b32 v141, v145, v141, s[62:63]\nv_cndmask_b32 v142, v146, v142, s[62:63]\nv_cndmask_b32 v143, v147, v143, s[62:63]\n"
"s_nop 2\nv_exp_f32 v152, v152\nv_exp_f32 v153, v153\nv_exp_f32 v154, v154\n"
"v_exp_f32 v155, v155\nv_cvt_pk_bf16_f32 v200, v152, v153\nv_cvt_pk_bf16_f32 v201, v154, v155\nv_and_b32 v160, v200, v223\n"
"v_and_b32 v161, v200, v224\nv_mfma_f32_16x16x16_bf16 v[148:151], v[214:215], v[200:201], v[148:151]\nv_and_b32 v162, v201, v223\nv_and_b32 v163, v201, v224\n"
"v_mfma_f32_16x16x16_bf16 v[132:135], v[156:157], v[160:161], v[132:135]\ns_nop 0\nv_mfma_f32_16x16x16_bf16 v[140:143], v[158:159], v[162:163], v[140:143]\nv_rcp_f32 v202, v148\n"
"v_rcp_f32 v203, v149\nv_mul_f32 v132, v132, v202\nv_mul_f32 v133, v133, v202\nv_mul_f32 v134, v134, v202\n"
"v_mul_f32 v135, v135, v202\nv_fmac_f32 v132, v140, v203\nv_fmac_f32 v133, v141, v203\nv_fmac_f32 v134, v142, v203\n"
"v_fmac_f32 v135, v143, v203\nv_add_f32_dpp v204, v132, v132 row_ror:8 row_mask:0xf bank_mask:0x3\nv_add_f32_dpp v205, v133, v133 row_ror:8 row_mask:0xf bank_mask:0x3\nv_add_f32_dpp v204, v134, v134 row_ror:8 row_mask:0xf bank_mask:0xc\n"
"v_add_f32_dpp v205, v135, v135 row_ror:8 row_mask:0xf bank_mask:0xc\nv_subrev_u32 v219, 0x23800000, v148\nv_subrev_u32 v220, 0x23800000, v149\nv_max_u32 v219, v219, v220\n"
"v_cmp_le_u32 vcc, 0x38000000, v219\nds_write_b64 v22, v[204:205]\ns_or_b64 s[56:57], vcc, s[94:95]\ns_cbranch_scc1 slow_p0\n"
"join_p0:\nv_mov_b32 v219, 0\ns_nop 1\nv_mov_b32 v219, 0\n"
"s_waitcnt lgkmcnt(0)\ns_barrier\nds_read_b64 v[206:207], v23 offset:0\nds_read_b64 v[208:209], v23 offset:1280\n"
"ds_read_b64 v[210:211], v23 offset:2560\nds_read_b64 v[212:213], v23 offset:3840\nds_read_b32 v221, v222\nv_mov_b32 v219, 0\n"
"s_add_u32 s40, s40, 1\ns_mov_b32 s43, s41\ns_add_u32 s41, s41, 131072\nv_mov_b32 v219, 0\n"
"s_and_b32 s41, s41, 524287\ns_add_u32 s92, s92, 1\ns_add_u32 s51, s51, 4096\nv_mov_b32 v219, 0\n"
"s_mov_b32 s42, 1\ns_waitcnt lgkmcnt(3)\nv_pk_add_f32 v[206:207], v[206:207], v[208:209]\ns_waitcnt lgkmcnt(1)\n"
"v_pk_add_f32 v[210:211], v[210:211], v[212:213]\nv_pk_add_f32 v[206:207], v[206:207], v[210:211]\nv_cvt_pk_bf16_f32 v220, v206, v207\nbuffer_store_dword v220, v16, s[8:11], s43 offen sc1\n"
"s_waitcnt lgkmcnt(0)\ns_memrealtime s[54:55]\nv_readfirstlane_b32 s47, v221\ns_nop 1\n"
"s_memrealtime s[56:57]\ns_sub_u32 s47, s47, s40\ns_cmp_lg_u32 s47, 1\ns_memrealtime s[58:59]\n"
"s_cbranch_scc1 mg_p0\ns_lshl_b32 s52, s40, 26\ns_cmp_le_u32 s92, 2\ns_cselect_b32 s48, s33, s38\n"
"s_waitcnt lgkmcnt(0)\ns_sub_u32 s47, s54, s30\ns_add_u32 s47, s47, s48\ns_min_u32 s47, s47, 0x3ffff\n"
"s_lshl_b32 s47, s47, 8\ns_or_b32 s47, s47, s52\ns_sub_u32 s52, s92, 2\ns_cmp_lt_u32 s52, 6\n"
"s_cselect_b32 s48, 1, 0\ns_cmp_gt_u32 s40, 5\ns_cselect_b32 s48, s48, 0\ns_add_u32 s48, s48, s46\n"
"s_min_u32 s48, s48, 15\ns_or_b32 s47, s47, s48\nv_mov_b32 v225, s47\nbuffer_store_dword v225, v19, s[8:11], 0 offen sc1\n"
"s_cmp_lg_u32 s40, 1\ns_cbranch_scc1 rec_p0\nbuffer_load_dword v218, v11, s[8:11], 0 offen sc1\ns_waitcnt vmcnt(0)\n"
"f1_p0:\ns_add_u32 s47, s40, 2\ns_min_u32 s47, s47, 64\ns_add_u32 s47, s47, s34\n"
"v_subrev_u32 v218, s47, v218\nv_cmp_lt_u32 vcc, 64, v218\ns_cbranch_vccz f1ok_p0\ns_add_u32 s53, s53, 0\n"
"s_add_u32 s49, s49, 1\ns_cmp_gt_u32 s49, 65536\ns_cbranch_scc1 end_p0\ns_sleep 2\n"
"buffer_load_dword v218, v11, s[8:11], 0 offen sc1\ns_waitcnt vmcnt(0)\ns_branch f1_p0\nf1ok_p0:\n"
"s_branch top2_p0\nlast_p0:\nv_smfmac_f32_16x16x64_bf16 v[68:71], v[48:51], a[16:23], v8\nv_smfmac_f32_16x16x64_bf16 v[72:75], v[48:51], a[72:79], v8\n"
"v_smfmac_f32_16x16x64_bf16 v[76:79], v[48:51], a[128:135], v8\nv_smfmac_f32_16x16x64_bf16 v[80:83], v[48:51], a[184:191], v8\nv_smfmac_f32_16x16x64_bf16 v[68:71], v[52:55], a[24:31], v8\nv_smfmac_f32_16x16x64_bf16 v[72:75], v[52:55], a[80:87], v8\n"
"v_smfmac_f32_16x16x64_bf16 v[76:79], v[52:55], a[136:143], v8\nv_smfmac_f32_16x16x64_bf16 v[80:83], v[52:55], a[192:199], v8\nv_smfmac_f32_16x16x64_bf16 v[68:71], v[56:59], a[32:39], v8\nv_smfmac_f32_16x16x64_bf16 v[72:75], v[56:59], a[88:95], v8\n"
"v_smfmac_f32_16x16x64_bf16 v[76:79], v[56:59], a[144:151], v8\nv_smfmac_f32_16x16x64_bf16 v[80:83], v[56:59], a[200:207], v8\nv_smfmac_f32_16x16x64_bf16 v[68:71], v[60:63], a[40:47], v8\nv_smfmac_f32_16x16x64_bf16 v[72:75], v[60:63], a[96:103], v8\n"
"v_smfmac_f32_16x16x64_bf16 v[76:79], v[60:63], a[152:159], v8\nv_smfmac_f32_16x16x64_bf16 v[80:83], v[60:63], a[208:215], v8\nv_smfmac_f32_16x16x64_bf16 v[68:71], v[64:67], a[48:55], v8\nv_smfmac_f32_16x16x64_bf16 v[72:75], v[64:67], a[104:111], v8\n"
"v_smfmac_f32_16x16x64_bf16 v[76:79], v[64:67], a[160:167], v8\nv_smfmac_f32_16x16x64_bf16 v[80:83], v[64:67], a[216:223], v8\nv_add_f32 v219, v68, v69\nv_add_f32 v220, v70, v71\n"
"v_cvt_pk_bf16_f32 v156, v219, v220\nv_add_f32 v219, v72, v73\nv_add_f32 v220, v74, v75\nv_cvt_pk_bf16_f32 v157, v219, v220\n"
"v_add_f32 v219, v76, v77\nv_add_f32 v220, v78, v79\nv_cvt_pk_bf16_f32 v158, v219, v220\nv_add_f32 v219, v80, v81\n"
"v_add_f32 v220, v82, v83\nv_cvt_pk_bf16_f32 v159, v219, v220\nbuffer_store_dwordx4 v[156:159], v9, s[20:23], s51 offen sc1\ns_add_u32 s47, s34, 64\n"
"v_mov_b32 v219, s47\ns_waitcnt vmcnt(0)\nbuffer_store_dword v219, v18, s[8:11], 0 offen sc1\nv_mov_b32 v220, s34\n"
"buffer_store_dword v220, v15, s[8:11], 0 offen sc1\ns_branch end_p0\nretry1_p0:\ns_add_u32 s53, s53, 256\n"
"s_add_u32 s49, s49, 1\ns_cmp_gt_u32 s49, 65536\ns_cbranch_scc1 end_p0\ns_add_u32 s47, s40, 1\n"
"v_mov_b32 v221, s47\nds_write_b32 v222, v221\ns_sleep 2\ns_branch top2_p0\n"
"rec_p0:\ns_sleep 6\ns_branch top2_p0\nadopt_p0:\n"
"buffer_load_dword v164, v28, s[8:11], 0 offen sc1\nv_smfmac_f32_16x16x64_bf16 v[68:71], v[48:51], a[16:23], v8\nv_smfmac_f32_16x16x64_bf16 v[72:75], v[48:51], a[72:79], v8\nv_smfmac_f32_16x16x64_bf16 v[76:79], v[48:51], a[128:135], v8\n"
"v_smfmac_f32_16x16x64_bf16 v[80:83], v[48:51], a[184:191], v8\nv_smfmac_f32_16x16x64_bf16 v[68:71], v[52:55], a[24:31], v8\nv_smfmac_f32_16x16x64_bf16 v[72:75], v[52:55], a[80:87], v8\nv_smfmac_f32_16x16x64_bf16 v[76:79], v[52:55], a[136:143], v8\n"
"v_smfmac_f32_16x16x64_bf16 v[80:83], v[52:55], a[192:199], v8\nv_smfmac_f32_16x16x64_bf16 v[68:71], v[56:59], a[32:39], v8\nv_smfmac_f32_16x16x64_bf16 v[72:75], v[56:59], a[88:95], v8\nv_smfmac_f32_16x16x64_bf16 v[76:79], v[56:59], a[144:151], v8\n"
"v_smfmac_f32_16x16x64_bf16 v[80:83], v[56:59], a[200:207], v8\nv_smfmac_f32_16x16x64_bf16 v[68:71], v[60:63], a[40:47], v8\nv_smfmac_f32_16x16x64_bf16 v[72:75], v[60:63], a[96:103], v8\nv_smfmac_f32_16x16x64_bf16 v[76:79], v[60:63], a[152:159], v8\n"
"v_smfmac_f32_16x16x64_bf16 v[80:83], v[60:63], a[208:215], v8\nv_smfmac_f32_16x16x64_bf16 v[68:71], v[64:67], a[48:55], v8\nv_smfmac_f32_16x16x64_bf16 v[72:75], v[64:67], a[104:111], v8\nv_smfmac_f32_16x16x64_bf16 v[76:79], v[64:67], a[160:167], v8\n"
"v_smfmac_f32_16x16x64_bf16 v[80:83], v[64:67], a[216:223], v8\ns_lshl_b32 s52, s40, 26\ns_or_b32 s47, s52, 0x3ffffff\ns_waitcnt vmcnt(0)\n"
"v_cmp_ge_u32 vcc, s47, v164\ns_nop 1\nv_cndmask_b32 v221, 0, v164, vcc\ns_nop 1\n"
"v_max_u32_dpp v221, v221, v221 row_ror:8 row_mask:0xf bank_mask:0xf\ns_nop 1\nv_max_u32_dpp v221, v221, v221 row_ror:4 row_mask:0xf bank_mask:0xf\ns_nop 1\n"
"v_max_u32_dpp v221, v221, v221 row_ror:2 row_mask:0xf bank_mask:0xf\ns_nop 1\nv_max_u32_dpp v221, v221, v221 row_ror:1 row_mask:0xf bank_mask:0xf\ns_nop 0\n"
"v_readlane_b32 s47, v221, 0\nv_readlane_b32 s48, v221, 16\ns_max_u32 s48, s48, s47\ns_cmp_le_u32 s48, s52\n"
"s_cbranch_scc1 back3_p0\ns_bfe_u32 s52, s48, 1179656\ns_add_u32 s52, s52, s30\ns_sub_u32 s45, s52, s44\n"
"s_and_b32 s45, s45, 3\ns_sub_u32 s44, s52, s45\ns_lshl_b32 s45, s45, 2\ns_and_b32 s46, s48, 255\n"
"s_mov_b32 s92, 0\ns_branch back2_p0\nback2_p0:\ns_and_b32 s47, s46, 15\n"
"s_mul_i32 s47, s47, 4\ns_add_u32 s47, s47, s31\ns_add_u32 s47, s47, s45\ns_and_b32 s91, s47, 15\n"
"s_lshr_b32 s47, s47, 4\ns_lshl_b32 s47, s47, 2\ns_add_u32 s90, s44, s47\ns_mul_i32 s47, s91, 6\n"
"s_add_u32 s47, s47, s93\ns_lshr_b32 s47, s47, 4\ns_mul_i32 s47, s47, 244\ns_getpc_b64 s[60:61]\n"
"ep2_p0:\ns_add_u32 s47, s47, sgo_p0-ep2_p0\ns_add_u32 s60, s60, s47\ns_addc_u32 s61, s61, 0\n"
"s_mul_i32 s48, s46, 8\ns_add_u32 s45, s45, s48\ns_add_u32 s45, s45, s32\ns_lshr_b32 s48, s45, 4\n"
"s_lshl_b32 s48, s48, 2\ns_add_u32 s44, s44, s48\ns_and_b32 s45, s45, 15\ns_add_u32 s52, s40, 1\n"
"s_bitcmp1_b64 s[28:29], s52\ns_subb_u32 s46, s46, 0\ns_mul_i32 s48, s45, 6\ns_and_b32 s48, s48, -4\n"
"s_getpc_b64 s[88:89]\nmpc2_p0:\ns_sub_u32 s48, mgo_p0-mpc2_p0, s48\ns_add_u32 s88, s88, s48\n"
"s_addc_u32 s89, s89, -1\nback3_p0:\ns_waitcnt lgkmcnt(0)\ns_memrealtime s[54:55]\n"
"s_nop 2\ns_memrealtime s[56:57]\ns_nop 2\ns_memrealtime s[58:59]\n"
"s_waitcnt lgkmcnt(0)\ns_sub_u32 s47, s58, s90\ns_cmp_lt_i32 s47, 0\ns_cbranch_scc1 sp_p0\n"
"s_cmp_le_i32 s47, 0\ns_cbranch_scc1 sgn_p0\ns_add_u32 s53, s53, 16777216\ns_mov_b64 s[94:95], 1\n"
"s_add_u32 s52, s90, 20\nlw2_p0:\ns_memrealtime s[54:55]\ns_waitcnt lgkmcnt(0)\n"
"s_sub_u32 s47, s54, s52\ns_cmp_lt_i32 s47, 0\ns_cbranch_scc1 lw2_p0\ns_branch sgn_p0\n"
"late_p0:\ns_add_u32 s53, s53, 16777216\ns_mov_b64 s[94:95], 1\ns_add_u32 s52, s90, 20\n"
"lw_p0:\ns_memrealtime s[54:55]\ns_waitcnt lgkmcnt(0)\ns_sub_u32 s47, s54, s52\n"
"s_cmp_lt_i32 s47, 0\ns_cbranch_scc1 lw_p0\ns_branch sgo_p0\nslow_p0:\n"
"s_mov_b64 s[94:95], 0\ns_cbranch_vccnz slow2_p0\ns_add_u32 s47, s40, 2\nv_mov_b32 v221, s47\n"
"ds_write_b32 v222, v221\ns_branch join_p0\nslow2_p0:\nv_max3_u32 v218, v124, v125, v126\n"
"v_max_u32 v218, v218, v127\nv_max3_u32 v218, v218, v128, v129\nv_max3_u32 v218, v218, v130, v131\nv_max3_u32 v218, v218, v116, v117\n"
"v_max3_u32 v218, v218, v118, v119\nv_max3_u32 v218, v218, v120, v121\nv_max3_u32 v218, v218, v122, v123\nv_cmp_eq_u32 vcc, -1, v218\n"
"s_cbranch_vccz exact_p0\ns_branch spin_p0\nspin2_p0:\ns_sleep 3\n"
"spin_p0:\ns_add_u32 s53, s53, 1\ns_add_u32 s49, s49, 1\ns_cmp_gt_u32 s49, 65536\n"
"s_cbranch_scc1 end_p0\nbuffer_load_dwordx4 v[116:119], v20, s[8:11], s41 offen offset:0 sc1\nbuffer_load_dwordx4 v[120:123], v20, s[8:11], s41 offen offset:1024 sc1\nbuffer_load_dwordx4 v[124:127], v21, s[8:11], s41 offen offset:0 sc1\n"
"buffer_load_dwordx4 v[128:131], v21, s[8:11], s41 offen offset:1024 sc1\ns_waitcnt vmcnt(0)\nv_max3_u32 v218, v124, v125, v126\nv_max_u32 v218, v218, v127\n"
"v_max3_u32 v218, v218, v128, v129\nv_max3_u32 v218, v218, v130, v131\nv_max3_u32 v218, v218, v116, v117\nv_max3_u32 v218, v218, v118, v119\n"
"v_max3_u32 v218, v218, v120, v121\nv_max3_u32 v218, v218, v122, v123\nv_cmp_eq_u32 vcc, -1, v218\ns_cbranch_vccnz spin2_p0\n"
"s_add_u32 s47, s40, 2\nv_mov_b32 v221, s47\nds_write_b32 v222, v221\nv_mfma_f32_16x16x32_bf16 v[132:135], v[84:87], v[116:119], 0\n"
"v_mfma_f32_16x16x32_bf16 v[136:139], v[88:91], v[116:119], 0\nv_mfma_f32_16x16x32_bf16 v[148:151], v[176:179], v[116:119], 0\nv_mfma_f32_16x16x32_bf16 v[140:143], v[92:95], v[120:123], 0\nv_mfma_f32_16x16x32_bf16 v[144:147], v[96:99], v[120:123], 0\n"
"v_mfma_f32_16x16x32_bf16 v[148:151], v[180:183], v[120:123], v[148:151]\nv_cndmask_b32 v132, v136, v132, s[62:63]\nv_cndmask_b32 v133, v137, v133, s[62:63]\nv_cndmask_b32 v134, v138, v134, s[62:63]\n"
"v_cndmask_b32 v135, v139, v135, s[62:63]\nv_mfma_f32_16x16x32_f16 v[152:155], v[168:171], v[124:127], 0\nv_mfma_f32_16x16x32_f16 v[152:155], v[172:175], v[128:131], v[152:155]\nv_cndmask_b32 v140, v144, v140, s[62:63]\n"
"v_cndmask_b32 v141, v145, v141, s[62:63]\nv_cndmask_b32 v142, v146, v142, s[62:63]\nv_cndmask_b32 v143, v147, v143, s[62:63]\ns_nop 2\n"
"v_exp_f32 v152, v152\nv_exp_f32 v153, v153\nv_exp_f32 v154, v154\nv_exp_f32 v155, v155\n"
"v_cvt_pk_bf16_f32 v200, v152, v153\nv_cvt_pk_bf16_f32 v201, v154, v155\nv_and_b32 v160, v200, v223\nv_and_b32 v161, v200, v224\n"
"v_mfma_f32_16x16x16_bf16 v[148:151], v[214:215], v[200:201], v[148:151]\nv_and_b32 v162, v201, v223\nv_and_b32 v163, v201, v224\nv_mfma_f32_16x16x16_bf16 v[132:135], v[156:157], v[160:161], v[132:135]\n"
"s_nop 0\nv_mfma_f32_16x16x16_bf16 v[140:143], v[158:159], v[162:163], v[140:143]\nv_rcp_f32 v202, v148\nv_rcp_f32 v203, v149\n"
"v_mul_f32 v132, v132, v202\nv_mul_f32 v133, v133, v202\nv_mul_f32 v134, v134, v202\nv_mul_f32 v135, v135, v202\n"
"v_fmac_f32 v132, v140, v203\nv_fmac_f32 v133, v141, v203\nv_fmac_f32 v134, v142, v203\nv_fmac_f32 v135, v143, v203\n"
"v_add_f32_dpp v204, v132, v132 row_ror:8 row_mask:0xf bank_mask:0x3\nv_add_f32_dpp v205, v133, v133 row_ror:8 row_mask:0xf bank_mask:0x3\nv_add_f32_dpp v204, v134, v134 row_ror:8 row_mask:0xf bank_mask:0xc\nv_add_f32_dpp v205, v135, v135 row_ror:8 row_mask:0xf bank_mask:0xc\n"
"v_subrev_u32 v219, 0x23800000, v148\nv_subrev_u32 v220, 0x23800000, v149\nv_max_u32 v219, v219, v220\nv_cmp_le_u32 vcc, 0x38000000, v219\n"
"ds_write_b64 v22, v[204:205]\ns_cbranch_vccz join_p0\nexact_p0:\ns_add_u32 s53, s53, 65536\n"
"ex0_p0:\ns_add_u32 s47, s41, 0\nbuffer_load_dwordx2 v[184:185], v24, s[8:11], s47 offen offset:0 sc1\nbuffer_load_dwordx2 v[186:187], v24, s[8:11], s47 offen offset:512 sc1\n"
"buffer_load_dwordx2 v[188:189], v24, s[8:11], s47 offen offset:1024 sc1\nbuffer_load_dwordx2 v[190:191], v24, s[8:11], s47 offen offset:1536 sc1\nbuffer_load_dwordx4 v[192:195], v25, s[8:11], s47 offen sc1\nbuffer_load_dwordx4 v[196:199], v25, s[8:11], s47 offen offset:16 sc1\n"
"v_mov_b32 v218, 0\ns_waitcnt vmcnt(0)\nv_max3_u32 v218, v218, v184, v185\nv_max3_u32 v218, v218, v186, v187\n"
"v_max3_u32 v218, v218, v188, v189\nv_max3_u32 v218, v218, v190, v191\nv_max3_u32 v218, v218, v192, v193\nv_max3_u32 v218, v218, v194, v195\n"
"v_max3_u32 v218, v218, v196, v197\nv_max3_u32 v218, v218, v198, v199\nv_cmp_eq_u32 vcc, -1, v218\ns_cbranch_vccz exok0_p0\n"
"s_add_u32 s53, s53, 0\ns_add_u32 s49, s49, 1\ns_cmp_gt_u32 s49, 65536\ns_cbranch_scc1 end_p0\n"
"s_branch ex0_p0\nexok0_p0:\nv_add_f32 v184, v184, v186\nv_add_f32 v188, v188, v190\n"
"v_add_f32 v184, v184, v188\nv_add_f32 v185, v185, v187\nv_add_f32 v189, v189, v191\nv_add_f32 v185, v185, v189\n"
"v_max_f32 v225, v184, v185\nv_max3_f32 v225, v225, v192, v193\nv_max3_f32 v225, v225, v194, v195\nv_max3_f32 v225, v225, v196, v197\n"
"v_max3_f32 v225, v225, v198, v199\nv_mov_b32 v219, v225\ns_nop 1\nv_permlane16_swap_b32 v225, v219\n"
"v_max_f32 v225, v225, v219\nv_mov_b32 v219, v225\ns_nop 1\nv_permlane32_swap_b32 v225, v219\n"
"v_max_f32 v225, v225, v219\nv_sub_f32 v184, v184, v225\nv_exp_f32 v152, v184\nv_sub_f32 v185, v185, v225\n"
"v_exp_f32 v153, v185\nv_sub_f32 v192, v192, v225\nv_exp_f32 v192, v192\nv_sub_f32 v193, v193, v225\n"
"v_exp_f32 v193, v193\nv_sub_f32 v194, v194, v225\nv_exp_f32 v194, v194\nv_sub_f32 v195, v195, v225\n"
"v_exp_f32 v195, v195\nv_sub_f32 v196, v196, v225\nv_exp_f32 v196, v196\nv_sub_f32 v197, v197, v225\n"
"v_exp_f32 v197, v197\nv_sub_f32 v198, v198, v225\nv_exp_f32 v198, v198\nv_sub_f32 v199, v199, v225\n"
"v_exp_f32 v199, v199\ns_nop 0\nv_cvt_pk_bf16_f32 v116, v192, v193\nv_cvt_pk_bf16_f32 v117, v194, v195\n"
"v_cvt_pk_bf16_f32 v118, v196, v197\nv_cvt_pk_bf16_f32 v119, v198, v199\nex1_p0:\ns_add_u32 s47, s41, 4096\n"
"buffer_load_dwordx2 v[184:185], v24, s[8:11], s47 offen offset:0 sc1\nbuffer_load_dwordx2 v[186:187], v24, s[8:11], s47 offen offset:512 sc1\nbuffer_load_dwordx2 v[188:189], v24, s[8:11], s47 offen offset:1024 sc1\nbuffer_load_dwordx2 v[190:191], v24, s[8:11], s47 offen offset:1536 sc1\n"
"buffer_load_dwordx4 v[192:195], v25, s[8:11], s47 offen sc1\nbuffer_load_dwordx4 v[196:199], v25, s[8:11], s47 offen offset:16 sc1\nv_mov_b32 v218, 0\ns_waitcnt vmcnt(0)\n"
"v_max3_u32 v218, v218, v184, v185\nv_max3_u32 v218, v218, v186, v187\nv_max3_u32 v218, v218, v188, v189\nv_max3_u32 v218, v218, v190, v191\n"
"v_max3_u32 v218, v218, v192, v193\nv_max3_u32 v218, v218, v194, v195\nv_max3_u32 v218, v218, v196, v197\nv_max3_u32 v218, v218, v198, v199\n"
"v_cmp_eq_u32 vcc, -1, v218\ns_cbranch_vccz exok1_p0\ns_add_u32 s53, s53, 0\ns_add_u32 s49, s49, 1\n"
"s_cmp_gt_u32 s49, 65536\ns_cbranch_scc1 end_p0\ns_branch ex1_p0\nexok1_p0:\n"
"v_add_f32 v184, v184, v186\nv_add_f32 v188, v188, v190\nv_add_f32 v184, v184, v188\nv_add_f32 v185, v185, v187\n"
"v_add_f32 v189, v189, v191\nv_add_f32 v185, v185, v189\nv_max_f32 v225, v184, v185\nv_max3_f32 v225, v225, v192, v193\n"
"v_max3_f32 v225, v225, v194, v195\nv_max3_f32 v225, v225, v196, v197\nv_max3_f32 v225, v225, v198, v199\nv_mov_b32 v219, v225\n"
"s_nop 1\nv_permlane16_swap_b32 v225, v219\nv_max_f32 v225, v225, v219\nv_mov_b32 v219, v225\n"
"s_nop 1\nv_permlane32_swap_b32 v225, v219\nv_max_f32 v225, v225, v219\nv_sub_f32 v184, v184, v225\n"
"v_exp_f32 v154, v184\nv_sub_f32 v185, v185, v225\nv_exp_f32 v155, v185\nv_sub_f32 v192, v192, v225\n"
"v_exp_f32 v192, v192\nv_sub_f32 v193, v193, v225\nv_exp_f32 v193, v193\nv_sub_f32 v194, v194, v225\n"
"v_exp_f32 v194, v194\nv_sub_f32 v195, v195, v225\nv_exp_f32 v195, v195\nv_sub_f32 v196, v196, v225\n"
"v_exp_f32 v196, v196\nv_sub_f32 v197, v197, v225\nv_exp_f32 v197, v197\nv_sub_f32 v198, v198, v225\n"
"v_exp_f32 v198, v198\nv_sub_f32 v199, v199, v225\nv_exp_f32 v199, v199\ns_nop 0\n"
"v_cvt_pk_bf16_f32 v120, v192, v193\nv_cvt_pk_bf16_f32 v121, v194, v195\nv_cvt_pk_bf16_f32 v122, v196, v197\nv_cvt_pk_bf16_f32 v123, v198, v199\n"
"s_add_u32 s47, s40, 2\nv_mov_b32 v221, s47\nds_write_b32 v222, v221\nv_mfma_f32_16x16x32_bf16 v[132:135], v[84:87], v[116:119], 0\n"
"v_mfma_f32_16x16x32_bf16 v[136:139], v[88:91], v[116:119], 0\nv_mfma_f32_16x16x32_bf16 v[148:151], v[176:179], v[116:119], 0\nv_mfma_f32_16x16x32_bf16 v[140:143], v[92:95], v[120:123], 0\nv_mfma_f32_16x16x32_bf16 v[144:147], v[96:99], v[120:123], 0\n"
"v_mfma_f32_16x16x32_bf16 v[148:151], v[180:183], v[120:123], v[148:151]\nv_cndmask_b32 v132, v136, v132, s[62:63]\nv_cndmask_b32 v133, v137, v133, s[62:63]\nv_cndmask_b32 v134, v138, v134, s[62:63]\n"
"v_cndmask_b32 v135, v139, v135, s[62:63]\ns_nop 0\nv_cndmask_b32 v140, v144, v140, s[62:63]\nv_cndmask_b32 v141, v145, v141, s[62:63]\n"
"v_cndmask_b32 v142, v146, v142, s[62:63]\nv_cndmask_b32 v143, v147, v143, s[62:63]\nv_cvt_pk_bf16_f32 v200, v152, v153\nv_cvt_pk_bf16_f32 v201, v154, v155\n"
"v_and_b32 v160, v200, v223\nv_and_b32 v161, v200, v224\nv_mfma_f32_16x16x16_bf16 v[148:151], v[214:215], v[200:201], v[148:151]\nv_and_b32 v162, v201, v223\n"
"v_and_b32 v163, v201, v224\nv_mfma_f32_16x16x16_bf16 v[132:135], v[156:157], v[160:161], v[132:135]\ns_nop 0\nv_mfma_f32_16x16x16_bf16 v[140:143], v[158:159], v[162:163], v[140:143]\n"
"v_rcp_f32 v202, v148\nv_rcp_f32 v203, v149\nv_mul_f32 v132, v132, v202\nv_mul_f32 v133, v133, v202\n"
"v_mul_f32 v134, v134, v202\nv_mul_f32 v135, v135, v202\nv_fmac_f32 v132, v140, v203\nv_fmac_f32 v133, v141, v203\n"
"v_fmac_f32 v134, v142, v203\nv_fmac_f32 v135, v143, v203\nv_add_f32_dpp v204, v132, v132 row_ror:8 row_mask:0xf bank_mask:0x3\nv_add_f32_dpp v205, v133, v133 row_ror:8 row_mask:0xf bank_mask:0x3\n"
"v_add_f32_dpp v204, v134, v134 row_ror:8 row_mask:0xf bank_mask:0xc\nv_add_f32_dpp v205, v135, v135 row_ror:8 row_mask:0xf bank_mask:0xc\nds_write_b64 v22, v[204:205]\ns_branch join_p0\n"
".p2align 6\nva0_p0:\ns_branch warm_p0\ns_nop 1\n"
"s_nop 1\ns_nop 1\ns_nop 1\ns_nop 1\n"
"s_nop 1\ns_nop 1\ns_nop 1\ns_nop 1\n"
"s_nop 1\ns_nop 1\ns_nop 1\ns_nop 1\n"
"s_nop 1\ns_nop 1\ns_nop 1\ns_nop 1\n"
"s_nop 1\ns_nop 1\ns_nop 1\ns_nop 1\n"
"s_nop 1\nsgo_p0:\nbuffer_load_dwordx4 v[116:119], v20, s[8:11], s41 offen offset:0 sc1\nbuffer_load_dwordx4 v[120:123], v20, s[8:11], s41 offen offset:1024 sc1\n"
"v_smfmac_f32_16x16x64_bf16 v[68:71], v[48:51], a[16:23], v8\nv_smfmac_f32_16x16x64_bf16 v[72:75], v[48:51], a[72:79], v8\nv_smfmac_f32_16x16x64_bf16 v[76:79], v[48:51], a[128:135], v8\nv_smfmac_f32_16x16x64_bf16 v[80:83], v[48:51], a[184:191], v8\n"
"v_smfmac_f32_16x16x64_bf16 v[68:71], v[52:55], a[24:31], v8\nv_smfmac_f32_16x16x64_bf16 v[72:75], v[52:55], a[80:87], v8\nv_smfmac_f32_16x16x64_bf16 v[76:79], v[52:55], a[136:143], v8\nv_smfmac_f32_16x16x64_bf16 v[80:83], v[52:55], a[192:199], v8\n"
"buffer_load_dwordx4 v[124:127], v21, s[8:11], s41 offen offset:0 sc1\nbuffer_load_dwordx4 v[128:131], v21, s[8:11], s41 offen offset:1024 sc1\ns_cbranch_execnz tl8_p0\ns_nop 0\n"
"s_nop 0\ns_nop 0\ns_nop 0\ns_nop 0\n"
"s_nop 0\ns_nop 0\ns_nop 0\ns_nop 0\n"
"s_nop 0\ns_nop 0\ns_nop 0\ns_nop 0\n"
"s_nop 0\ns_nop 0\nva1_p0:\ns_nop 0\n"
"s_nop 1\ns_nop 1\ns_nop 1\ns_nop 1\n"
"s_nop 1\ns_nop 1\ns_nop 1\ns_nop 1\n"
"s_nop 1\ns_nop 1\ns_nop 1\ns_nop 1\n"
"s_nop 1\ns_nop 1\ns_nop 1\ns_nop 1\n"
"s_nop 1\ns_nop 1\ns_nop 1\ns_nop 1\n"
"v_smfmac_f32_16x16x64_bf16 v[68:71], v[48:51], a[16:23], v8\nbuffer_load_dwordx4 v[116:119], v20, s[8:11], s41 offen offset:0 sc1\nbuffer_load_dwordx4 v[120:123], v20, s[8:11], s41 offen offset:1024 sc1\nv_smfmac_f32_16x16x64_bf16 v[72:75], v[48:51], a[72:79], v8\n"
"v_smfmac_f32_16x16x64_bf16 v[76:79], v[48:51], a[128:135], v8\nv_smfmac_f32_16x16x64_bf16 v[80:83], v[48:51], a[184:191], v8\nv_smfmac_f32_16x16x64_bf16 v[68:71], v[52:55], a[24:31], v8\nv_smfmac_f32_16x16x64_bf16 v[72:75], v[52:55], a[80:87], v8\n"
"v_smfmac_f32_16x16x64_bf16 v[76:79], v[52:55], a[136:143], v8\nv_smfmac_f32_16x16x64_bf16 v[80:83], v[52:55], a[192:199], v8\nv_smfmac_f32_16x16x64_bf16 v[68:71], v[56:59], a[32:39], v8\nbuffer_load_dwordx4 v[124:127], v21, s[8:11], s41 offen offset:0 sc1\n"
"buffer_load_dwordx4 v[128:131], v21, s[8:11], s41 offen offset:1024 sc1\ns_cbranch_execnz tl9_p0\ns_nop 0\ns_nop 0\n"
"s_nop 0\ns_nop 0\ns_nop 0\ns_nop 0\n"
"s_nop 0\ns_nop 0\ns_nop 0\ns_nop 0\n"
"s_nop 0\ns_nop 0\ns_nop 0\ns_nop 0\n"
"s_nop 0\nva2_p0:\ns_nop 0\ns_nop 1\n"
"s_nop 1\ns_nop 1\ns_nop 1\ns_nop 1\n"
"s_nop 1\ns_nop 1\ns_nop 1\ns_nop 1\n"
"s_nop 1\ns_nop 1\ns_nop 1\ns_nop 1\n"
"s_nop 1\ns_nop 1\ns_nop 1\ns_nop 1\n"
"s_nop 1\nv_smfmac_f32_16x16x64_bf16 v[68:71], v[48:51], a[16:23], v8\nv_smfmac_f32_16x16x64_bf16 v[72:75], v[48:51], a[72:79], v8\nbuffer_load_dwordx4 v[116:119], v20, s[8:11], s41 offen offset:0 sc1\n"
"buffer_load_dwordx4 v[120:123], v20, s[8:11], s41 offen offset:1024 sc1\nv_smfmac_f32_16x16x64_bf16 v[76:79], v[48:51], a[128:135], v8\nv_smfmac_f32_16x16x64_bf16 v[80:83], v[48:51], a[184:191], v8\nv_smfmac_f32_16x16x64_bf16 v[68:71], v[52:55], a[24:31], v8\n"
"v_smfmac_f32_16x16x64_bf16 v[72:75], v[52:55], a[80:87], v8\nv_smfmac_f32_16x16x64_bf16 v[76:79], v[52:55], a[136:143], v8\nv_smfmac_f32_16x16x64_bf16 v[80:83], v[52:55], a[192:199], v8\nv_smfmac_f32_16x16x64_bf16 v[68:71], v[56:59], a[32:39], v8\n"
"v_smfmac_f32_16x16x64_bf16 v[72:75], v[56:59], a[88:95], v8\nbuffer_load_dwordx4 v[124:127], v21, s[8:11], s41 offen offset:0 sc1\nbuffer_load_dwordx4 v[128:131], v21, s[8:11], s41 offen offset:1024 sc1\ns_cbranch_execnz tl10_p0\n"
"s_nop 0\ns_nop 0\ns_nop 0\ns_nop 0\n"
"s_nop 0\ns_nop 0\ns_nop 0\ns_nop 0\n"
"s_nop 0\ns_nop 0\ns_nop 0\ns_nop 0\n"
"s_nop 0\ns_nop 0\ns_nop 0\nva3_p0:\n"
"s_nop 0\ns_nop 1\ns_nop 1\ns_nop 1\n"
"s_nop 1\ns_nop 1\ns_nop 1\ns_nop 1\n"
"s_nop 1\ns_nop 1\ns_nop 1\ns_nop 1\n"
"s_nop 1\ns_nop 1\ns_nop 1\ns_nop 1\n"
"s_nop 1\nv_smfmac_f32_16x16x64_bf16 v[68:71], v[48:51], a[16:23], v8\nv_smfmac_f32_16x16x64_bf16 v[72:75], v[48:51], a[72:79], v8\nv_smfmac_f32_16x16x64_bf16 v[76:79], v[48:51], a[128:135], v8\n"
"buffer_load_dwordx4 v[116:119], v20, s[8:11], s41 offen offset:0 sc1\nbuffer_load_dwordx4 v[120:123], v20, s[8:11], s41 offen offset:1024 sc1\nv_smfmac_f32_16x16x64_bf16 v[80:83], v[48:51], a[184:191], v8\nv_smfmac_f32_16x16x64_bf16 v[68:71], v[52:55], a[24:31], v8\n"
"v_smfmac_f32_16x16x64_bf16 v[72:75], v[52:55], a[80:87], v8\nv_smfmac_f32_16x16x64_bf16 v[76:79], v[52:55], a[136:143], v8\nv_smfmac_f32_16x16x64_bf16 v[80:83], v[52:55], a[192:199], v8\nv_smfmac_f32_16x16x64_bf16 v[68:71], v[56:59], a[32:39], v8\n"
"v_smfmac_f32_16x16x64_bf16 v[72:75], v[56:59], a[88:95], v8\nv_smfmac_f32_16x16x64_bf16 v[76:79], v[56:59], a[144:151], v8\nbuffer_load_dwordx4 v[124:127], v21, s[8:11], s41 offen offset:0 sc1\nbuffer_load_dwordx4 v[128:131], v21, s[8:11], s41 offen offset:1024 sc1\n"
"s_cbranch_execnz tl11_p0\ns_nop 0\ns_nop 0\ns_nop 0\n"
"s_nop 0\ns_nop 0\ns_nop 0\ns_nop 0\n"
"s_nop 0\ns_nop 0\ns_nop 0\ns_nop 0\n"
"s_nop 0\ns_nop 0\ns_nop 0\ns_nop 0\n"
"va4_p0:\ns_nop 0\ns_nop 1\ns_nop 1\n"
"s_nop 1\ns_nop 1\ns_nop 1\ns_nop 1\n"
"s_nop 1\ns_nop 1\ns_nop 1\ns_nop 1\n"
"s_nop 1\ns_nop 1\ns_nop 1\ns_nop 1\n"
"v_smfmac_f32_16x16x64_bf16 v[68:71], v[48:51], a[16:23], v8\nv_smfmac_f32_16x16x64_bf16 v[72:75], v[48:51], a[72:79], v8\nv_smfmac_f32_16x16x64_bf16 v[76:79], v[48:51], a[128:135], v8\nv_smfmac_f32_16x16x64_bf16 v[80:83], v[48:51], a[184:191], v8\n"
"buffer_load_dwordx4 v[116:119], v20, s[8:11], s41 offen offset:0 sc1\nbuffer_load_dwordx4 v[120:123], v20, s[8:11], s41 offen offset:1024 sc1\nv_smfmac_f32_16x16x64_bf16 v[68:71], v[52:55], a[24:31], v8\nv_smfmac_f32_16x16x64_bf16 v[72:75], v[52:55], a[80:87], v8\n"
"v_smfmac_f32_16x16x64_bf16 v[76:79], v[52:55], a[136:143], v8\nv_smfmac_f32_16x16x64_bf16 v[80:83], v[52:55], a[192:199], v8\nv_smfmac_f32_16x16x64_bf16 v[68:71], v[56:59], a[32:39], v8\nv_smfmac_f32_16x16x64_bf16 v[72:75], v[56:59], a[88:95], v8\n"
"v_smfmac_f32_16x16x64_bf16 v[76:79], v[56:59], a[144:151], v8\nv_smfmac_f32_16x16x64_bf16 v[80:83], v[56:59], a[200:207], v8\nbuffer_load_dwordx4 v[124:127], v21, s[8:11], s41 offen offset:0 sc1\nbuffer_load_dwordx4 v[128:131], v21, s[8:11], s41 offen offset:1024 sc1\n"
"s_cbranch_execnz tl12_p0\ns_nop 0\ns_nop 0\ns_nop 0\n"
"s_nop 0\ns_nop 0\ns_nop 0\ns_nop 0\n"
"s_nop 0\ns_nop 0\ns_nop 0\ns_nop 0\n"
"s_nop 0\ns_nop 0\ns_nop 0\ns_nop 0\n"
"va5_p0:\ns_nop 0\ns_nop 1\ns_nop 1\n"
"s_nop 1\ns_nop 1\ns_nop 1\ns_nop 1\n"
"s_nop 1\ns_nop 1\ns_nop 1\ns_nop 1\n"
"s_nop 1\ns_nop 1\nv_smfmac_f32_16x16x64_bf16 v[68:71], v[48:51], a[16:23], v8\nv_smfmac_f32_16x16x64_bf16 v[72:75], v[48:51], a[72:79], v8\n"
"v_smfmac_f32_16x16x64_bf16 v[76:79], v[48:51], a[128:135], v8\nv_smfmac_f32_16x16x64_bf16 v[80:83], v[48:51], a[184:191], v8\nv_smfmac_f32_16x16x64_bf16 v[68:71], v[52:55], a[24:31], v8\nbuffer_load_dwordx4 v[116:119], v20, s[8:11], s41 offen offset:0 sc1\n"
"buffer_load_dwordx4 v[120:123], v20, s[8:11], s41 offen offset:1024 sc1\nv_smfmac_f32_16x16x64_bf16 v[72:75], v[52:55], a[80:87], v8\nv_smfmac_f32_16x16x64_bf16 v[76:79], v[52:55], a[136:143], v8\nv_smfmac_f32_16x16x64_bf16 v[80:83], v[52:55], a[192:199], v8\n"
"v_smfmac_f32_16x16x64_bf16 v[68:71], v[56:59], a[32:39], v8\nv_smfmac_f32_16x16x64_bf16 v[72:75], v[56:59], a[88:95], v8\nv_smfmac_f32_16x16x64_bf16 v[76:79], v[56:59], a[144:151], v8\nv_smfmac_f32_16x16x64_bf16 v[80:83], v[56:59], a[200:207], v8\n"
"v_smfmac_f32_16x16x64_bf16 v[68:71], v[60:63], a[40:47], v8\nbuffer_load_dwordx4 v[124:127], v21, s[8:11], s41 offen offset:0 sc1\nbuffer_load_dwordx4 v[128:131], v21, s[8:11], s41 offen offset:1024 sc1\ns_cbranch_execnz tl13_p0\n"
"s_nop 0\ns_nop 0\ns_nop 0\ns_nop 0\n"
"s_nop 0\ns_nop 0\ns_nop 0\ns_nop 0\n"
"s_nop 0\ns_nop 0\ns_nop 0\ns_nop 0\n"
"s_nop 0\ns_nop 0\ns_nop 0\nva6_p0:\n"
"s_nop 0\ns_nop 1\ns_nop 1\ns_nop 1\n"
"s_nop 1\ns_nop 1\ns_nop 1\ns_nop 1\n"
"s_nop 1\ns_nop 1\ns_nop 1\nv_smfmac_f32_16x16x64_bf16 v[68:71], v[48:51], a[16:23], v8\n"
"v_smfmac_f32_16x16x64_bf16 v[72:75], v[48:51], a[72:79], v8\nv_smfmac_f32_16x16x64_bf16 v[76:79], v[48:51], a[128:135], v8\nv_smfmac_f32_16x16x64_bf16 v[80:83], v[48:51], a[184:191], v8\nv_smfmac_f32_16x16x64_bf16 v[68:71], v[52:55], a[24:31], v8\n"
"v_smfmac_f32_16x16x64_bf16 v[72:75], v[52:55], a[80:87], v8\nbuffer_load_dwordx4 v[116:119], v20, s[8:11], s41 offen offset:0 sc1\nbuffer_load_dwordx4 v[120:123], v20, s[8:11], s41 offen offset:1024 sc1\nv_smfmac_f32_16x16x64_bf16 v[76:79], v[52:55], a[136:143], v8\n"
"v_smfmac_f32_16x16x64_bf16 v[80:83], v[52:55], a[192:199], v8\nv_smfmac_f32_16x16x64_bf16 v[68:71], v[56:59], a[32:39], v8\nv_smfmac_f32_16x16x64_bf16 v[72:75], v[56:59], a[88:95], v8\nv_smfmac_f32_16x16x64_bf16 v[76:79], v[56:59], a[144:151], v8\n"
"v_smfmac_f32_16x16x64_bf16 v[80:83], v[56:59], a[200:207], v8\nv_smfmac_f32_16x16x64_bf16 v[68:71], v[60:63], a[40:47], v8\nv_smfmac_f32_16x16x64_bf16 v[72:75], v[60:63], a[96:103], v8\nbuffer_load_dwordx4 v[124:127], v21, s[8:11], s41 offen offset:0 sc1\n"
"buffer_load_dwordx4 v[128:131], v21, s[8:11], s41 offen offset:1024 sc1\ns_nop 0\ns_cbranch_execnz tl14_p0\ns_nop 0\n"
"s_nop 0\ns_nop 0\ns_nop 0\ns_nop 0\n"
"s_nop 0\ns_nop 0\ns_nop 0\ns_nop 0\n"
"s_nop 0\ns_nop 0\ns_nop 0\ns_nop 0\n"
"s_nop 0\nva7_p0:\ns_nop 0\ns_nop 1\n"
"s_nop 1\ns_nop 1\ns_nop 1\ns_nop 1\n"
"s_nop 1\ns_nop 1\ns_nop 1\nv_smfmac_f32_16x16x64_bf16 v[68:71], v[48:51], a[16:23], v8\n"
"v_smfmac_f32_16x16x64_bf16 v[72:75], v[48:51], a[72:79], v8\nv_smfmac_f32_16x16x64_bf16 v[76:79], v[48:51], a[128:135], v8\nv_smfmac_f32_16x16x64_bf16 v[80:83], v[48:51], a[184:191], v8\nv_smfmac_f32_16x16x64_bf16 v[68:71], v[52:55], a[24:31], v8\n"
"v_smfmac_f32_16x16x64_bf16 v[72:75], v[52:55], a[80:87], v8\nv_smfmac_f32_16x16x64_bf16 v[76:79], v[52:55], a[136:143], v8\nbuffer_load_dwordx4 v[116:119], v20, s[8:11], s41 offen offset:0 sc1\nbuffer_load_dwordx4 v[120:123], v20, s[8:11], s41 offen offset:1024 sc1\n"
"v_smfmac_f32_16x16x64_bf16 v[80:83], v[52:55], a[192:199], v8\nv_smfmac_f32_16x16x64_bf16 v[68:71], v[56:59], a[32:39], v8\nv_smfmac_f32_16x16x64_bf16 v[72:75], v[56:59], a[88:95], v8\nv_smfmac_f32_16x16x64_bf16 v[76:79], v[56:59], a[144:151], v8\n"
"v_smfmac_f32_16x16x64_bf16 v[80:83], v[56:59], a[200:207], v8\nv_smfmac_f32_16x16x64_bf16 v[68:71], v[60:63], a[40:47], v8\nv_smfmac_f32_16x16x64_bf16 v[72:75], v[60:63], a[96:103], v8\nv_smfmac_f32_16x16x64_bf16 v[76:79], v[60:63], a[152:159], v8\n"
"buffer_load_dwordx4 v[124:127], v21, s[8:11], s41 offen offset:0 sc1\nbuffer_load_dwordx4 v[128:131], v21, s[8:11], s41 offen offset:1024 sc1\ns_nop 4\ns_cbranch_execnz tl15_p0\n"
"s_nop 0\ns_nop 0\ns_nop 0\ns_nop 0\n"
"s_nop 0\ns_nop 0\ns_nop 0\ns_nop 0\n"
"s_nop 0\ns_nop 0\ns_nop 0\ns_nop 0\n"
"s_nop 0\ns_nop 0\nva8_p0:\ns_branch warm_p0\n"
"s_nop 1\ns_nop 1\ns_nop 1\ns_nop 1\n"
"s_nop 1\ns_nop 1\nv_smfmac_f32_16x16x64_bf16 v[68:71], v[48:51], a[16:23], v8\nv_smfmac_f32_16x16x64_bf16 v[72:75], v[48:51], a[72:79], v8\n"
"v_smfmac_f32_16x16x64_bf16 v[76:79], v[48:51], a[128:135], v8\nv_smfmac_f32_16x16x64_bf16 v[80:83], v[48:51], a[184:191], v8\nv_smfmac_f32_16x16x64_bf16 v[68:71], v[52:55], a[24:31], v8\nv_smfmac_f32_16x16x64_bf16 v[72:75], v[52:55], a[80:87], v8\n"
"v_smfmac_f32_16x16x64_bf16 v[76:79], v[52:55], a[136:143], v8\nv_smfmac_f32_16x16x64_bf16 v[80:83], v[52:55], a[192:199], v8\nbuffer_load_dwordx4 v[116:119], v20, s[8:11], s41 offen offset:0 sc1\nbuffer_load_dwordx4 v[120:123], v20, s[8:11], s41 offen offset:1024 sc1\n"
"v_smfmac_f32_16x16x64_bf16 v[68:71], v[56:59], a[32:39], v8\nv_smfmac_f32_16x16x64_bf16 v[72:75], v[56:59], a[88:95], v8\nv_smfmac_f32_16x16x64_bf16 v[76:79], v[56:59], a[144:151], v8\nv_smfmac_f32_16x16x64_bf16 v[80:83], v[56:59], a[200:207], v8\n"
"v_smfmac_f32_16x16x64_bf16 v[68:71], v[60:63], a[40:47], v8\nv_smfmac_f32_16x16x64_bf16 v[72:75], v[60:63], a[96:103], v8\nv_smfmac_f32_16x16x64_bf16 v[76:79], v[60:63], a[152:159], v8\nv_smfmac_f32_16x16x64_bf16 v[80:83], v[60:63], a[208:215], v8\n"
"buffer_load_dwordx4 v[124:127], v21, s[8:11], s41 offen offset:0 sc1\nbuffer_load_dwordx4 v[128:131], v21, s[8:11], s41 offen offset:1024 sc1\ns_nop 8\ns_cbranch_execnz tl16_p0\n"
"s_nop 0\ns_nop 0\ns_nop 0\ns_nop 0\n"
"s_nop 0\ns_nop 0\ns_nop 0\ns_nop 0\n"
"s_nop 0\ns_nop 0\ns_nop 0\ns_nop 0\n"
"s_nop 0\ns_nop 0\nva9_p0:\ns_nop 0\n"
"s_nop 1\ns_nop 1\ns_nop 1\ns_nop 1\n"
"v_smfmac_f32_16x16x64_bf16 v[68:71], v[48:51], a[16:23], v8\nv_smfmac_f32_16x16x64_bf16 v[72:75], v[48:51], a[72:79], v8\nv_smfmac_f32_16x16x64_bf16 v[76:79], v[48:51], a[128:135], v8\nv_smfmac_f32_16x16x64_bf16 v[80:83], v[48:51], a[184:191], v8\n"
"v_smfmac_f32_16x16x64_bf16 v[68:71], v[52:55], a[24:31], v8\nv_smfmac_f32_16x16x64_bf16 v[72:75], v[52:55], a[80:87], v8\nv_smfmac_f32_16x16x64_bf16 v[76:79], v[52:55], a[136:143], v8\nv_smfmac_f32_16x16x64_bf16 v[80:83], v[52:55], a[192:199], v8\n"
"v_smfmac_f32_16x16x64_bf16 v[68:71], v[56:59], a[32:39], v8\nbuffer_load_dwordx4 v[116:119], v20, s[8:11], s41 offen offset:0 sc1\nbuffer_load_dwordx4 v[120:123], v20, s[8:11], s41 offen offset:1024 sc1\nv_smfmac_f32_16x16x64_bf16 v[72:75], v[56:59], a[88:95], v8\n"
"v_smfmac_f32_16x16x64_bf16 v[76:79], v[56:59], a[144:151], v8\nv_smfmac_f32_16x16x64_bf16 v[80:83], v[56:59], a[200:207], v8\nv_smfmac_f32_16x16x64_bf16 v[68:71], v[60:63], a[40:47], v8\nv_smfmac_f32_16x16x64_bf16 v[72:75], v[60:63], a[96:103], v8\n"
"v_smfmac_f32_16x16x64_bf16 v[76:79], v[60:63], a[152:159], v8\nv_smfmac_f32_16x16x64_bf16 v[80:83], v[60:63], a[208:215], v8\nv_smfmac_f32_16x16x64_bf16 v[68:71], v[64:67], a[48:55], v8\nbuffer_load_dwordx4 v[124:127], v21, s[8:11], s41 offen offset:0 sc1\n"
"buffer_load_dwordx4 v[128:131], v21, s[8:11], s41 offen offset:1024 sc1\ns_nop 12\ns_cbranch_execnz tl17_p0\ns_nop 0\n"
"s_nop 0\ns_nop 0\ns_nop 0\ns_nop 0\n"
"s_nop 0\ns_nop 0\ns_nop 0\ns_nop 0\n"
"s_nop 0\ns_nop 0\ns_nop 0\ns_nop 0\n"
"s_nop 0\nva10_p0:\ns_nop 0\ns_nop 1\n"
"s_nop 1\nv_smfmac_f32_16x16x64_bf16 v[68:71], v[48:51], a[16:23], v8\nv_smfmac_f32_16x16x64_bf16 v[72:75], v[48:51], a[72:79], v8\nv_smfmac_f32_16x16x64_bf16 v[76:79], v[48:51], a[128:135], v8\n"
"v_smfmac_f32_16x16x64_bf16 v[80:83], v[48:51], a[184:191], v8\nv_smfmac_f32_16x16x64_bf16 v[68:71], v[52:55], a[24:31], v8\nv_smfmac_f32_16x16x64_bf16 v[72:75], v[52:55], a[80:87], v8\nv_smfmac_f32_16x16x64_bf16 v[76:79], v[52:55], a[136:143], v8\n"
"v_smfmac_f32_16x16x64_bf16 v[80:83], v[52:55], a[192:199], v8\nv_smfmac_f32_16x16x64_bf16 v[68:71], v[56:59], a[32:39], v8\nv_smfmac_f32_16x16x64_bf16 v[72:75], v[56:59], a[88:95], v8\nbuffer_load_dwordx4 v[116:119], v20, s[8:11], s41 offen offset:0 sc1\n"
"buffer_load_dwordx4 v[120:123], v20, s[8:11], s41 offen offset:1024 sc1\nv_smfmac_f32_16x16x64_bf16 v[76:79], v[56:59], a[144:151], v8\nv_smfmac_f32_16x16x64_bf16 v[80:83], v[56:59], a[200:207], v8\nv_smfmac_f32_16x16x64_bf16 v[68:71], v[60:63], a[40:47], v8\n"
"v_smfmac_f32_16x16x64_bf16 v[72:75], v[60:63], a[96:103], v8\nv_smfmac_f32_16x16x64_bf16 v[76:79], v[60:63], a[152:159], v8\nv_smfmac_f32_16x16x64_bf16 v[80:83], v[60:63], a[208:215], v8\nv_smfmac_f32_16x16x64_bf16 v[68:71], v[64:67], a[48:55], v8\n"
"v_smfmac_f32_16x16x64_bf16 v[72:75], v[64:67], a[104:111], v8\nbuffer_load_dwordx4 v[124:127], v21, s[8:11], s41 offen offset:0 sc1\nbuffer_load_dwordx4 v[128:131], v21, s[8:11], s41 offen offset:1024 sc1\ns_nop 15\n"
"s_nop 0\ns_cbranch_execnz tl18_p0\ns_nop 0\ns_nop 0\n"
"s_nop 0\ns_nop 0\ns_nop 0\ns_nop 0\n"
"s_nop 0\ns_nop 0\ns_nop 0\ns_nop 0\n"
"s_nop 0\ns_nop 0\ns_nop 0\nva11_p0:\n"
"s_nop 0\nv_smfmac_f32_16x16x64_bf16 v[68:71], v[48:51], a[16:23], v8\nv_smfmac_f32_16x16x64_bf16 v[72:75], v[48:51], a[72:79], v8\nv_smfmac_f32_16x16x64_bf16 v[76:79], v[48:51], a[128:135], v8\n"
"v_smfmac_f32_16x16x64_bf16 v[80:83], v[48:51], a[184:191], v8\nv_smfmac_f32_16x16x64_bf16 v[68:71], v[52:55], a[24:31], v8\nv_smfmac_f32_16x16x64_bf16 v[72:75], v[52:55], a[80:87], v8\nv_smfmac_f32_16x16x64_bf16 v[76:79], v[52:55], a[136:143], v8\n"
"v_smfmac_f32_16x16x64_bf16 v[80:83], v[52:55], a[192:199], v8\nv_smfmac_f32_16x16x64_bf16 v[68:71], v[56:59], a[32:39], v8\nv_smfmac_f32_16x16x64_bf16 v[72:75], v[56:59], a[88:95], v8\nv_smfmac_f32_16x16x64_bf16 v[76:79], v[56:59], a[144:151], v8\n"
"buffer_load_dwordx4 v[116:119], v20, s[8:11], s41 offen offset:0 sc1\nbuffer_load_dwordx4 v[120:123], v20, s[8:11], s41 offen offset:1024 sc1\nv_smfmac_f32_16x16x64_bf16 v[80:83], v[56:59], a[200:207], v8\nv_smfmac_f32_16x16x64_bf16 v[68:71], v[60:63], a[40:47], v8\n"
"v_smfmac_f32_16x16x64_bf16 v[72:75], v[60:63], a[96:103], v8\nv_smfmac_f32_16x16x64_bf16 v[76:79], v[60:63], a[152:159], v8\nv_smfmac_f32_16x16x64_bf16 v[80:83], v[60:63], a[208:215], v8\nv_smfmac_f32_16x16x64_bf16 v[68:71], v[64:67], a[48:55], v8\n"
"v_smfmac_f32_16x16x64_bf16 v[72:75], v[64:67], a[104:111], v8\nv_smfmac_f32_16x16x64_bf16 v[76:79], v[64:67], a[160:167], v8\nbuffer_load_dwordx4 v[124:127], v21, s[8:11], s41 offen offset:0 sc1\nbuffer_load_dwordx4 v[128:131], v21, s[8:11], s41 offen offset:1024 sc1\n"
"s_nop 15\ns_nop 4\ns_cbranch_execnz tl19_p0\ns_nop 0\n"
"s_nop 0\ns_nop 0\ns_nop 0\ns_nop 0\n"
"s_nop 0\ns_nop 0\ns_nop 0\ns_nop 0\n"
"s_nop 0\ns_nop 0\ns_nop 0\ns_nop 0\n"
"va12_p0:\ns_nop 0\nv_smfmac_f32_16x16x64_bf16 v[72:75], v[48:51], a[72:79], v8\nv_smfmac_f32_16x16x64_bf16 v[76:79], v[48:51], a[128:135], v8\n"
"v_smfmac_f32_16x16x64_bf16 v[80:83], v[48:51], a[184:191], v8\nv_smfmac_f32_16x16x64_bf16 v[68:71], v[52:55], a[24:31], v8\nv_smfmac_f32_16x16x64_bf16 v[72:75], v[52:55], a[80:87], v8\nv_smfmac_f32_16x16x64_bf16 v[76:79], v[52:55], a[136:143], v8\n"
"v_smfmac_f32_16x16x64_bf16 v[80:83], v[52:55], a[192:199], v8\nv_smfmac_f32_16x16x64_bf16 v[68:71], v[56:59], a[32:39], v8\nv_smfmac_f32_16x16x64_bf16 v[72:75], v[56:59], a[88:95], v8\nv_smfmac_f32_16x16x64_bf16 v[76:79], v[56:59], a[144:151], v8\n"
"v_smfmac_f32_16x16x64_bf16 v[80:83], v[56:59], a[200:207], v8\nbuffer_load_dwordx4 v[116:119], v20, s[8:11], s41 offen offset:0 sc1\nbuffer_load_dwordx4 v[120:123], v20, s[8:11], s41 offen offset:1024 sc1\nv_smfmac_f32_16x16x64_bf16 v[68:71], v[60:63], a[40:47], v8\n"
"v_smfmac_f32_16x16x64_bf16 v[72:75], v[60:63], a[96:103], v8\nv_smfmac_f32_16x16x64_bf16 v[76:79], v[60:63], a[152:159], v8\nv_smfmac_f32_16x16x64_bf16 v[80:83], v[60:63], a[208:215], v8\nv_smfmac_f32_16x16x64_bf16 v[68:71], v[64:67], a[48:55], v8\n"
"v_smfmac_f32_16x16x64_bf16 v[72:75], v[64:67], a[104:111], v8\nv_smfmac_f32_16x16x64_bf16 v[76:79], v[64:67], a[160:167], v8\nv_smfmac_f32_16x16x64_bf16 v[80:83], v[64:67], a[216:223], v8\nbuffer_load_dwordx4 v[124:127], v21, s[8:11], s41 offen offset:0 sc1\n"
"buffer_load_dwordx4 v[128:131], v21, s[8:11], s41 offen offset:1024 sc1\ns_nop 15\ns_nop 8\ns_cbranch_execnz tl20_p0\n"
"s_nop 0\ns_nop 0\ns_nop 0\ns_nop 0\n"
"s_nop 0\ns_nop 0\ns_nop 0\ns_nop 0\n"
"s_nop 0\ns_nop 0\ns_nop 0\ns_nop 0\n"
"s_nop 0\nva13_p0:\ns_nop 0\nv_smfmac_f32_16x16x64_bf16 v[76:79], v[48:51], a[128:135], v8\n"
"v_smfmac_f32_16x16x64_bf16 v[80:83], v[48:51], a[184:191], v8\nv_smfmac_f32_16x16x64_bf16 v[68:71], v[52:55], a[24:31], v8\nv_smfmac_f32_16x16x64_bf16 v[72:75], v[52:55], a[80:87], v8\nv_smfmac_f32_16x16x64_bf16 v[76:79], v[52:55], a[136:143], v8\n"
"v_smfmac_f32_16x16x64_bf16 v[80:83], v[52:55], a[192:199], v8\nv_smfmac_f32_16x16x64_bf16 v[68:71], v[56:59], a[32:39], v8\nv_smfmac_f32_16x16x64_bf16 v[72:75], v[56:59], a[88:95], v8\nv_smfmac_f32_16x16x64_bf16 v[76:79], v[56:59], a[144:151], v8\n"
"v_smfmac_f32_16x16x64_bf16 v[80:83], v[56:59], a[200:207], v8\nv_smfmac_f32_16x16x64_bf16 v[68:71], v[60:63], a[40:47], v8\nbuffer_load_dwordx4 v[116:119], v20, s[8:11], s41 offen offset:0 sc1\nbuffer_load_dwordx4 v[120:123], v20, s[8:11], s41 offen offset:1024 sc1\n"
"v_smfmac_f32_16x16x64_bf16 v[72:75], v[60:63], a[96:103], v8\nv_smfmac_f32_16x16x64_bf16 v[76:79], v[60:63], a[152:159], v8\nv_smfmac_f32_16x16x64_bf16 v[80:83], v[60:63], a[208:215], v8\nv_smfmac_f32_16x16x64_bf16 v[68:71], v[64:67], a[48:55], v8\n"
"v_smfmac_f32_16x16x64_bf16 v[72:75], v[64:67], a[104:111], v8\nv_smfmac_f32_16x16x64_bf16 v[76:79], v[64:67], a[160:167], v8\nv_smfmac_f32_16x16x64_bf16 v[80:83], v[64:67], a[216:223], v8\nv_add_f32 v219, v68, v69\n"
"v_add_f32 v220, v70, v71\nv_cvt_pk_bf16_f32 v156, v219, v220\ns_nop 0\nbuffer_load_dwordx4 v[124:127], v21, s[8:11], s41 offen offset:0 sc1\n"
"buffer_load_dwordx4 v[128:131], v21, s[8:11], s41 offen offset:1024 sc1\ns_nop 15\ns_nop 11\ns_cbranch_execnz tl21_p0\n"
"s_nop 0\ns_nop 0\ns_nop 0\ns_nop 0\n"
"s_nop 0\ns_nop 0\ns_nop 0\ns_nop 0\n"
"s_nop 0\ns_nop 0\nva14_p0:\ns_nop 0\n"
"v_smfmac_f32_16x16x64_bf16 v[80:83], v[48:51], a[184:191], v8\nv_smfmac_f32_16x16x64_bf16 v[68:71], v[52:55], a[24:31], v8\nv_smfmac_f32_16x16x64_bf16 v[72:75], v[52:55], a[80:87], v8\nv_smfmac_f32_16x16x64_bf16 v[76:79], v[52:55], a[136:143], v8\n"
"v_smfmac_f32_16x16x64_bf16 v[80:83], v[52:55], a[192:199], v8\nv_smfmac_f32_16x16x64_bf16 v[68:71], v[56:59], a[32:39], v8\nv_smfmac_f32_16x16x64_bf16 v[72:75], v[56:59], a[88:95], v8\nv_smfmac_f32_16x16x64_bf16 v[76:79], v[56:59], a[144:151], v8\n"
"v_smfmac_f32_16x16x64_bf16 v[80:83], v[56:59], a[200:207], v8\nv_smfmac_f32_16x16x64_bf16 v[68:71], v[60:63], a[40:47], v8\nv_smfmac_f32_16x16x64_bf16 v[72:75], v[60:63], a[96:103], v8\nbuffer_load_dwordx4 v[116:119], v20, s[8:11], s41 offen offset:0 sc1\n"
"buffer_load_dwordx4 v[120:123], v20, s[8:11], s41 offen offset:1024 sc1\nv_smfmac_f32_16x16x64_bf16 v[76:79], v[60:63], a[152:159], v8\nv_smfmac_f32_16x16x64_bf16 v[80:83], v[60:63], a[208:215], v8\nv_smfmac_f32_16x16x64_bf16 v[68:71], v[64:67], a[48:55], v8\n"
"v_smfmac_f32_16x16x64_bf16 v[72:75], v[64:67], a[104:111], v8\nv_smfmac_f32_16x16x64_bf16 v[76:79], v[64:67], a[160:167], v8\nv_smfmac_f32_16x16x64_bf16 v[80:83], v[64:67], a[216:223], v8\nv_add_f32 v219, v68, v69\n"
"v_add_f32 v220, v70, v71\nv_cvt_pk_bf16_f32 v156, v219, v220\ns_nop 0\nv_add_f32 v219, v72, v73\n"
"v_add_f32 v220, v74, v75\nv_cvt_pk_bf16_f32 v157, v219, v220\ns_nop 0\nbuffer_load_dwordx4 v[124:127], v21, s[8:11], s41 offen offset:0 sc1\n"
"buffer_load_dwordx4 v[128:131], v21, s[8:11], s41 offen offset:1024 sc1\ns_nop 15\ns_nop 14\ns_cbranch_execnz tl22_p0\n"
"s_nop 0\ns_nop 0\ns_nop 0\ns_nop 0\n"
"s_nop 0\ns_nop 0\ns_nop 0\nva15_p0:\n"
"s_nop 0\nv_smfmac_f32_16x16x64_bf16 v[68:71], v[52:55], a[24:31], v8\nv_smfmac_f32_16x16x64_bf16 v[72:75], v[52:55], a[80:87], v8\nv_smfmac_f32_16x16x64_bf16 v[76:79], v[52:55], a[136:143], v8\n"
"v_smfmac_f32_16x16x64_bf16 v[80:83], v[52:55], a[192:199], v8\nv_smfmac_f32_16x16x64_bf16 v[68:71], v[56:59], a[32:39], v8\nv_smfmac_f32_16x16x64_bf16 v[72:75], v[56:59], a[88:95], v8\nv_smfmac_f32_16x16x64_bf16 v[76:79], v[56:59], a[144:151], v8\n"
"v_smfmac_f32_16x16x64_bf16 v[80:83], v[56:59], a[200:207], v8\nv_smfmac_f32_16x16x64_bf16 v[68:71], v[60:63], a[40:47], v8\nv_smfmac_f32_16x16x64_bf16 v[72:75], v[60:63], a[96:103], v8\nv_smfmac_f32_16x16x64_bf16 v[76:79], v[60:63], a[152:159], v8\n"
"buffer_load_dwordx4 v[116:119], v20, s[8:11], s41 offen offset:0 sc1\nbuffer_load_dwordx4 v[120:123], v20, s[8:11], s41 offen offset:1024 sc1\nv_smfmac_f32_16x16x64_bf16 v[80:83], v[60:63], a[208:215], v8\nv_smfmac_f32_16x16x64_bf16 v[68:71], v[64:67], a[48:55], v8\n"
"v_smfmac_f32_16x16x64_bf16 v[72:75], v[64:67], a[104:111], v8\nv_smfmac_f32_16x16x64_bf16 v[76:79], v[64:67], a[160:167], v8\nv_smfmac_f32_16x16x64_bf16 v[80:83], v[64:67], a[216:223], v8\nv_add_f32 v219, v68, v69\n"
"v_add_f32 v220, v70, v71\nv_cvt_pk_bf16_f32 v156, v219, v220\ns_nop 0\nv_add_f32 v219, v72, v73\n"
"v_add_f32 v220, v74, v75\nv_cvt_pk_bf16_f32 v157, v219, v220\ns_nop 0\nv_add_f32 v219, v76, v77\n"
"v_add_f32 v220, v78, v79\nv_cvt_pk_bf16_f32 v158, v219, v220\ns_nop 0\nbuffer_load_dwordx4 v[124:127], v21, s[8:11], s41 offen offset:0 sc1\n"
"buffer_load_dwordx4 v[128:131], v21, s[8:11], s41 offen offset:1024 sc1\ns_nop 15\ns_nop 15\ns_nop 1\n"
"s_cbranch_execnz tl23_p0\ns_nop 0\ns_nop 0\ns_nop 0\n"
"va16_p0:\ns_branch warm_p0\nv_smfmac_f32_16x16x64_bf16 v[72:75], v[52:55], a[80:87], v8\nv_smfmac_f32_16x16x64_bf16 v[76:79], v[52:55], a[136:143], v8\n"
"v_smfmac_f32_16x16x64_bf16 v[80:83], v[52:55], a[192:199], v8\nv_smfmac_f32_16x16x64_bf16 v[68:71], v[56:59], a[32:39], v8\nv_smfmac_f32_16x16x64_bf16 v[72:75], v[56:59], a[88:95], v8\nv_smfmac_f32_16x16x64_bf16 v[76:79], v[56:59], a[144:151], v8\n"
"v_smfmac_f32_16x16x64_bf16 v[80:83], v[56:59], a[200:207], v8\nv_smfmac_f32_16x16x64_bf16 v[68:71], v[60:63], a[40:47], v8\nv_smfmac_f32_16x16x64_bf16 v[72:75], v[60:63], a[96:103], v8\nv_smfmac_f32_16x16x64_bf16 v[76:79], v[60:63], a[152:159], v8\n"
"v_smfmac_f32_16x16x64_bf16 v[80:83], v[60:63], a[208:215], v8\nbuffer_load_dwordx4 v[116:119], v20, s[8:11], s41 offen offset:0 sc1\nbuffer_load_dwordx4 v[120:123], v20, s[8:11], s41 offen offset:1024 sc1\nv_smfmac_f32_16x16x64_bf16 v[68:71], v[64:67], a[48:55], v8\n"
"v_smfmac_f32_16x16x64_bf16 v[72:75], v[64:67], a[104:111], v8\nv_smfmac_f32_16x16x64_bf16 v[76:79], v[64:67], a[160:167], v8\nv_smfmac_f32_16x16x64_bf16 v[80:83], v[64:67], a[216:223], v8\nv_add_f32 v219, v68, v69\n"
"v_add_f32 v220, v70, v71\nv_cvt_pk_bf16_f32 v156, v219, v220\ns_nop 0\nv_add_f32 v219, v72, v73\n"
"v_add_f32 v220, v74, v75\nv_cvt_pk_bf16_f32 v157, v219, v220\ns_nop 0\nv_add_f32 v219, v76, v77\n"
"v_add_f32 v220, v78, v79\nv_cvt_pk_bf16_f32 v158, v219, v220\ns_nop 0\nv_add_f32 v219, v80, v81\n"
"v_add_f32 v220, v82, v83\nv_cvt_pk_bf16_f32 v159, v219, v220\ns_nop 0\nbuffer_load_dwordx4 v[124:127], v21, s[8:11], s41 offen offset:0 sc1\n"
"buffer_load_dwordx4 v[128:131], v21, s[8:11], s41 offen offset:1024 sc1\ns_nop 15\ns_nop 15\ns_nop 4\n"
"s_cbranch_execnz tl24_p0\nva17_p0:\ns_nop 0\nv_smfmac_f32_16x16x64_bf16 v[76:79], v[52:55], a[136:143], v8\n"
"v_smfmac_f32_16x16x64_bf16 v[80:83], v[52:55], a[192:199], v8\nv_smfmac_f32_16x16x64_bf16 v[68:71], v[56:59], a[32:39], v8\nv_smfmac_f32_16x16x64_bf16 v[72:75], v[56:59], a[88:95], v8\nv_smfmac_f32_16x16x64_bf16 v[76:79], v[56:59], a[144:151], v8\n"
"v_smfmac_f32_16x16x64_bf16 v[80:83], v[56:59], a[200:207], v8\nv_smfmac_f32_16x16x64_bf16 v[68:71], v[60:63], a[40:47], v8\nv_smfmac_f32_16x16x64_bf16 v[72:75], v[60:63], a[96:103], v8\nv_smfmac_f32_16x16x64_bf16 v[76:79], v[60:63], a[152:159], v8\n"
"v_smfmac_f32_16x16x64_bf16 v[80:83], v[60:63], a[208:215], v8\nv_smfmac_f32_16x16x64_bf16 v[68:71], v[64:67], a[48:55], v8\nbuffer_load_dwordx4 v[116:119], v20, s[8:11], s41 offen offset:0 sc1\nbuffer_load_dwordx4 v[120:123], v20, s[8:11], s41 offen offset:1024 sc1\n"
"v_smfmac_f32_16x16x64_bf16 v[72:75], v[64:67], a[104:111], v8\nv_smfmac_f32_16x16x64_bf16 v[76:79], v[64:67], a[160:167], v8\nv_smfmac_f32_16x16x64_bf16 v[80:83], v[64:67], a[216:223], v8\nv_add_f32 v219, v68, v69\n"
"v_add_f32 v220, v70, v71\nv_cvt_pk_bf16_f32 v156, v219, v220\ns_nop 0\nv_add_f32 v219, v72, v73\n"
"v_add_f32 v220, v74, v75\nv_cvt_pk_bf16_f32 v157, v219, v220\ns_nop 0\nv_add_f32 v219, v76, v77\n"
"v_add_f32 v220, v78, v79\nv_cvt_pk_bf16_f32 v158, v219, v220\ns_nop 0\nv_add_f32 v219, v80, v81\n"
"v_add_f32 v220, v82, v83\nv_cvt_pk_bf16_f32 v159, v219, v220\ns_nop 0\ns_nop 1\n"
"s_nop 1\nbuffer_load_dwordx4 v[124:127], v21, s[8:11], s41 offen offset:0 sc1\nbuffer_load_dwordx4 v[128:131], v21, s[8:11], s41 offen offset:1024 sc1\ns_nop 15\n"
"s_nop 15\ns_nop 4\ns_cbranch_execnz tl24_p0\nva18_p0:\n"
"s_nop 0\nv_smfmac_f32_16x16x64_bf16 v[80:83], v[52:55], a[192:199], v8\nv_smfmac_f32_16x16x64_bf16 v[68:71], v[56:59], a[32:39], v8\nv_smfmac_f32_16x16x64_bf16 v[72:75], v[56:59], a[88:95], v8\n"
"v_smfmac_f32_16x16x64_bf16 v[76:79], v[56:59], a[144:151], v8\nv_smfmac_f32_16x16x64_bf16 v[80:83], v[56:59], a[200:207], v8\nv_smfmac_f32_16x16x64_bf16 v[68:71], v[60:63], a[40:47], v8\nv_smfmac_f32_16x16x64_bf16 v[72:75], v[60:63], a[96:103], v8\n"
"v_smfmac_f32_16x16x64_bf16 v[76:79], v[60:63], a[152:159], v8\nv_smfmac_f32_16x16x64_bf16 v[80:83], v[60:63], a[208:215], v8\nv_smfmac_f32_16x16x64_bf16 v[68:71], v[64:67], a[48:55], v8\nv_smfmac_f32_16x16x64_bf16 v[72:75], v[64:67], a[104:111], v8\n"
"buffer_load_dwordx4 v[116:119], v20, s[8:11], s41 offen offset:0 sc1\nbuffer_load_dwordx4 v[120:123], v20, s[8:11], s41 offen offset:1024 sc1\nv_smfmac_f32_16x16x64_bf16 v[76:79], v[64:67], a[160:167], v8\nv_smfmac_f32_16x16x64_bf16 v[80:83], v[64:67], a[216:223], v8\n"
"v_add_f32 v219, v68, v69\nv_add_f32 v220, v70, v71\nv_cvt_pk_bf16_f32 v156, v219, v220\ns_nop 0\n"
"v_add_f32 v219, v72, v73\nv_add_f32 v220, v74, v75\nv_cvt_pk_bf16_f32 v157, v219, v220\ns_nop 0\n"
"v_add_f32 v219, v76, v77\nv_add_f32 v220, v78, v79\nv_cvt_pk_bf16_f32 v158, v219, v220\ns_nop 0\n"
"v_add_f32 v219, v80, v81\nv_add_f32 v220, v82, v83\nv_cvt_pk_bf16_f32 v159, v219, v220\ns_nop 0\n"
"s_nop 1\ns_nop 1\ns_nop 1\ns_nop 1\n"
"buffer_load_dwordx4 v[124:127], v21, s[8:11], s41 offen offset:0 sc1\nbuffer_load_dwordx4 v[128:131], v21, s[8:11], s41 offen offset:1024 sc1\ns_nop 15\ns_nop 15\n"
"s_nop 4\ns_cbranch_execnz tl24_p0\nva19_p0:\ns_nop 0\n"
"v_smfmac_f32_16x16x64_bf16 v[68:71], v[56:59], a[32:39], v8\nv_smfmac_f32_16x16x64_bf16 v[72:75], v[56:59], a[88:95], v8\nv_smfmac_f32_16x16x64_bf16 v[76:79], v[56:59], a[144:151], v8\nv_smfmac_f32_16x16x64_bf16 v[80:83], v[56:59], a[200:207], v8\n"
"v_smfmac_f32_16x16x64_bf16 v[68:71], v[60:63], a[40:47], v8\nv_smfmac_f32_16x16x64_bf16 v[72:75], v[60:63], a[96:103], v8\nv_smfmac_f32_16x16x64_bf16 v[76:79], v[60:63], a[152:159], v8\nv_smfmac_f32_16x16x64_bf16 v[80:83], v[60:63], a[208:215], v8\n"
"v_smfmac_f32_16x16x64_bf16 v[68:71], v[64:67], a[48:55], v8\nv_smfmac_f32_16x16x64_bf16 v[72:75], v[64:67], a[104:111], v8\nv_smfmac_f32_16x16x64_bf16 v[76:79], v[64:67], a[160:167], v8\nbuffer_load_dwordx4 v[116:119], v20, s[8:11], s41 offen offset:0 sc1\n"
"buffer_load_dwordx4 v[120:123], v20, s[8:11], s41 offen offset:1024 sc1\nv_smfmac_f32_16x16x64_bf16 v[80:83], v[64:67], a[216:223], v8\nv_add_f32 v219, v68, v69\nv_add_f32 v220, v70, v71\n"
"v_cvt_pk_bf16_f32 v156, v219, v220\ns_nop 0\nv_add_f32 v219, v72, v73\nv_add_f32 v220, v74, v75\n"
"v_cvt_pk_bf16_f32 v157, v219, v220\ns_nop 0\nv_add_f32 v219, v76, v77\nv_add_f32 v220, v78, v79\n"
"v_cvt_pk_bf16_f32 v158, v219, v220\ns_nop 0\nv_add_f32 v219, v80, v81\nv_add_f32 v220, v82, v83\n"
"v_cvt_pk_bf16_f32 v159, v219, v220\ns_nop 0\ns_nop 1\ns_nop 1\n"
"s_nop 1\ns_nop 1\ns_nop 1\ns_nop 1\n"
"buffer_load_dwordx4 v[124:127], v21, s[8:11], s41 offen offset:0 sc1\nbuffer_load_dwordx4 v[128:131], v21, s[8:11], s41 offen offset:1024 sc1\ns_nop 15\ns_nop 15\n"
"s_nop 4\ns_cbranch_execnz tl24_p0\nva20_p0:\ns_nop 0\n"
"v_smfmac_f32_16x16x64_bf16 v[72:75], v[56:59], a[88:95], v8\nv_smfmac_f32_16x16x64_bf16 v[76:79], v[56:59], a[144:151], v8\nv_smfmac_f32_16x16x64_bf16 v[80:83], v[56:59], a[200:207], v8\nv_smfmac_f32_16x16x64_bf16 v[68:71], v[60:63], a[40:47], v8\n"
"v_smfmac_f32_16x16x64_bf16 v[72:75], v[60:63], a[96:103], v8\nv_smfmac_f32_16x16x64_bf16 v[76:79], v[60:63], a[152:159], v8\nv_smfmac_f32_16x16x64_bf16 v[80:83], v[60:63], a[208:215], v8\nv_smfmac_f32_16x16x64_bf16 v[68:71], v[64:67], a[48:55], v8\n"
"v_smfmac_f32_16x16x64_bf16 v[72:75], v[64:67], a[104:111], v8\nv_smfmac_f32_16x16x64_bf16 v[76:79], v[64:67], a[160:167], v8\nv_smfmac_f32_16x16x64_bf16 v[80:83], v[64:67], a[216:223], v8\nsgn_p0:\n"
"buffer_load_dwordx4 v[116:119], v20, s[8:11], s41 offen offset:0 sc1\nbuffer_load_dwordx4 v[120:123], v20, s[8:11], s41 offen offset:1024 sc1\nv_add_f32 v219, v68, v69\nv_add_f32 v220, v70, v71\n"
"v_cvt_pk_bf16_f32 v156, v219, v220\ns_nop 0\nv_add_f32 v219, v72, v73\nv_add_f32 v220, v74, v75\n"
"v_cvt_pk_bf16_f32 v157, v219, v220\ns_nop 0\nv_add_f32 v219, v76, v77\nv_add_f32 v220, v78, v79\n"
"v_cvt_pk_bf16_f32 v158, v219, v220\ns_nop 0\nv_add_f32 v219, v80, v81\nv_add_f32 v220, v82, v83\n"
"v_cvt_pk_bf16_f32 v159, v219, v220\ns_nop 0\ns_nop 1\ns_nop 1\n"
"s_nop 1\ns_nop 1\ns_nop 1\ns_nop 1\n"
"s_nop 1\ns_nop 1\nbuffer_load_dwordx4 v[124:127], v21, s[8:11], s41 offen offset:0 sc1\nbuffer_load_dwordx4 v[128:131], v21, s[8:11], s41 offen offset:1024 sc1\n"
"s_nop 15\ns_nop 15\ns_nop 4\ns_cbranch_execnz tl24_p0\n"
"va21_p0:\ns_nop 0\nv_smfmac_f32_16x16x64_bf16 v[76:79], v[56:59], a[144:151], v8\nv_smfmac_f32_16x16x64_bf16 v[80:83], v[56:59], a[200:207], v8\n"
"v_smfmac_f32_16x16x64_bf16 v[68:71], v[60:63], a[40:47], v8\nv_smfmac_f32_16x16x64_bf16 v[72:75], v[60:63], a[96:103], v8\nv_smfmac_f32_16x16x64_bf16 v[76:79], v[60:63], a[152:159], v8\nv_smfmac_f32_16x16x64_bf16 v[80:83], v[60:63], a[208:215], v8\n"
"v_smfmac_f32_16x16x64_bf16 v[68:71], v[64:67], a[48:55], v8\nv_smfmac_f32_16x16x64_bf16 v[72:75], v[64:67], a[104:111], v8\nv_smfmac_f32_16x16x64_bf16 v[76:79], v[64:67], a[160:167], v8\nv_smfmac_f32_16x16x64_bf16 v[80:83], v[64:67], a[216:223], v8\n"
"s_nop 1\ns_nop 1\nbuffer_load_dwordx4 v[116:119], v20, s[8:11], s41 offen offset:0 sc1\nbuffer_load_dwordx4 v[120:123], v20, s[8:11], s41 offen offset:1024 sc1\n"
"v_add_f32 v219, v68, v69\nv_add_f32 v220, v70, v71\nv_cvt_pk_bf16_f32 v156, v219, v220\ns_nop 0\n"
"v_add_f32 v219, v72, v73\nv_add_f32 v220, v74, v75\nv_cvt_pk_bf16_f32 v157, v219, v220\ns_nop 0\n"
"v_add_f32 v219, v76, v77\nv_add_f32 v220, v78, v79\nv_cvt_pk_bf16_f32 v158, v219, v220\ns_nop 0\n"
"v_add_f32 v219, v80, v81\nv_add_f32 v220, v82, v83\nv_cvt_pk_bf16_f32 v159, v219, v220\ns_nop 0\n"
"s_nop 1\ns_nop 1\ns_nop 1\ns_nop 1\n"
"s_nop 1\ns_nop 1\ns_nop 1\ns_nop 1\n"
"buffer_load_dwordx4 v[124:127], v21, s[8:11], s41 offen offset:0 sc1\nbuffer_load_dwordx4 v[128:131], v21, s[8:11], s41 offen offset:1024 sc1\ns_nop 15\ns_nop 15\n"
"s_nop 4\ns_cbranch_execnz tl24_p0\nva22_p0:\ns_nop 0\n"
"v_smfmac_f32_16x16x64_bf16 v[80:83], v[56:59], a[200:207], v8\nv_smfmac_f32_16x16x64_bf16 v[68:71], v[60:63], a[40:47], v8\nv_smfmac_f32_16x16x64_bf16 v[72:75], v[60:63], a[96:103], v8\nv_smfmac_f32_16x16x64_bf16 v[76:79], v[60:63], a[152:159], v8\n"
"v_smfmac_f32_16x16x64_bf16 v[80:83], v[60:63], a[208:215], v8\nv_smfmac_f32_16x16x64_bf16 v[68:71], v[64:67], a[48:55], v8\nv_smfmac_f32_16x16x64_bf16 v[72:75], v[64:67], a[104:111], v8\nv_smfmac_f32_16x16x64_bf16 v[76:79], v[64:67], a[160:167], v8\n"
"v_smfmac_f32_16x16x64_bf16 v[80:83], v[64:67], a[216:223], v8\ns_nop 1\ns_nop 1\ns_nop 1\n"
"s_nop 1\nbuffer_load_dwordx4 v[116:119], v20, s[8:11], s41 offen offset:0 sc1\nbuffer_load_dwordx4 v[120:123], v20, s[8:11], s41 offen offset:1024 sc1\nv_add_f32 v219, v68, v69\n"
"v_add_f32 v220, v70, v71\nv_cvt_pk_bf16_f32 v156, v219, v220\ns_nop 0\nv_add_f32 v219, v72, v73\n"
"v_add_f32 v220, v74, v75\nv_cvt_pk_bf16_f32 v157, v219, v220\ns_nop 0\nv_add_f32 v219, v76, v77\n"
"v_add_f32 v220, v78, v79\nv_cvt_pk_bf16_f32 v158, v219, v220\ns_nop 0\nv_add_f32 v219, v80, v81\n"
"v_add_f32 v220, v82, v83\nv_cvt_pk_bf16_f32 v159, v219, v220\ns_nop 0\ns_nop 1\n"
"s_nop 1\ns_nop 1\ns_nop 1\ns_nop 1\n"
"s_nop 1\ns_nop 1\ns_nop 1\nbuffer_load_dwordx4 v[124:127], v21, s[8:11], s41 offen offset:0 sc1\n"
"buffer_load_dwordx4 v[128:131], v21, s[8:11], s41 offen offset:1024 sc1\ns_nop 15\ns_nop 15\ns_nop 4\n"
"s_cbranch_execnz tl24_p0\nva23_p0:\ns_nop 0\nv_smfmac_f32_16x16x64_bf16 v[68:71], v[60:63], a[40:47], v8\n"
"v_smfmac_f32_16x16x64_bf16 v[72:75], v[60:63], a[96:103], v8\nv_smfmac_f32_16x16x64_bf16 v[76:79], v[60:63], a[152:159], v8\nv_smfmac_f32_16x16x64_bf16 v[80:83], v[60:63], a[208:215], v8\nv_smfmac_f32_16x16x64_bf16 v[68:71], v[64:67], a[48:55], v8\n"
"v_smfmac_f32_16x16x64_bf16 v[72:75], v[64:67], a[104:111], v8\nv_smfmac_f32_16x16x64_bf16 v[76:79], v[64:67], a[160:167], v8\nv_smfmac_f32_16x16x64_bf16 v[80:83], v[64:67], a[216:223], v8\ns_nop 1\n"
"s_nop 1\ns_nop 1\ns_nop 1\ns_nop 1\n"
"s_nop 1\nbuffer_load_dwordx4 v[116:119], v20, s[8:11], s41 offen offset:0 sc1\nbuffer_load_dwordx4 v[120:123], v20, s[8:11], s41 offen offset:1024 sc1\nv_add_f32 v219, v68, v69\n"
"v_add_f32 v220, v70, v71\nv_cvt_pk_bf16_f32 v156, v219, v220\ns_nop 0\nv_add_f32 v219, v72, v73\n"
"v_add_f32 v220, v74, v75\nv_cvt_pk_bf16_f32 v157, v219, v220\ns_nop 0\nv_add_f32 v219, v76, v77\n"
"v_add_f32 v220, v78, v79\nv_cvt_pk_bf16_f32 v158, v219, v220\ns_nop 0\nv_add_f32 v219, v80, v81\n"
"v_add_f32 v220, v82, v83\nv_cvt_pk_bf16_f32 v159, v219, v220\ns_nop 0\ns_nop 1\n"
"s_nop 1\ns_nop 1\ns_nop 1\ns_nop 1\n"
"s_nop 1\ns_nop 1\ns_nop 1\nbuffer_load_dwordx4 v[124:127], v21, s[8:11], s41 offen offset:0 sc1\n"
"buffer_load_dwordx4 v[128:131], v21, s[8:11], s41 offen offset:1024 sc1\ns_nop 15\ns_nop 15\ns_nop 4\n"
"s_cbranch_execnz tl24_p0\nva24_p0:\ns_branch warm_p0\nv_smfmac_f32_16x16x64_bf16 v[72:75], v[60:63], a[96:103], v8\n"
"v_smfmac_f32_16x16x64_bf16 v[76:79], v[60:63], a[152:159], v8\nv_smfmac_f32_16x16x64_bf16 v[80:83], v[60:63], a[208:215], v8\nv_smfmac_f32_16x16x64_bf16 v[68:71], v[64:67], a[48:55], v8\nv_smfmac_f32_16x16x64_bf16 v[72:75], v[64:67], a[104:111], v8\n"
"v_smfmac_f32_16x16x64_bf16 v[76:79], v[64:67], a[160:167], v8\nv_smfmac_f32_16x16x64_bf16 v[80:83], v[64:67], a[216:223], v8\ns_nop 1\ns_nop 1\n"
"s_nop 1\ns_nop 1\ns_nop 1\ns_nop 1\n"
"s_nop 1\ns_nop 1\nbuffer_load_dwordx4 v[116:119], v20, s[8:11], s41 offen offset:0 sc1\nbuffer_load_dwordx4 v[120:123], v20, s[8:11], s41 offen offset:1024 sc1\n"
"v_add_f32 v219, v68, v69\nv_add_f32 v220, v70, v71\nv_cvt_pk_bf16_f32 v156, v219, v220\ns_nop 0\n"
"v_add_f32 v219, v72, v73\nv_add_f32 v220, v74, v75\nv_cvt_pk_bf16_f32 v157, v219, v220\ns_nop 0\n"
"v_add_f32 v219, v76, v77\nv_add_f32 v220, v78, v79\nv_cvt_pk_bf16_f32 v158, v219, v220\ns_nop 0\n"
"v_add_f32 v219, v80, v81\nv_add_f32 v220, v82, v83\nv_cvt_pk_bf16_f32 v159, v219, v220\ns_nop 0\n"
"s_nop 1\ns_nop 1\ns_nop 1\ns_nop 1\n"
"s_nop 1\ns_nop 1\ns_nop 1\ns_nop 1\n"
"buffer_load_dwordx4 v[124:127], v21, s[8:11], s41 offen offset:0 sc1\nbuffer_load_dwordx4 v[128:131], v21, s[8:11], s41 offen offset:1024 sc1\ns_nop 15\ns_nop 15\n"
"s_nop 4\ns_cbranch_execnz tl24_p0\nva25_p0:\ns_nop 0\n"
"v_smfmac_f32_16x16x64_bf16 v[76:79], v[60:63], a[152:159], v8\nv_smfmac_f32_16x16x64_bf16 v[80:83], v[60:63], a[208:215], v8\nv_smfmac_f32_16x16x64_bf16 v[68:71], v[64:67], a[48:55], v8\nv_smfmac_f32_16x16x64_bf16 v[72:75], v[64:67], a[104:111], v8\n"
"v_smfmac_f32_16x16x64_bf16 v[76:79], v[64:67], a[160:167], v8\nv_smfmac_f32_16x16x64_bf16 v[80:83], v[64:67], a[216:223], v8\ns_nop 1\ns_nop 1\n"
"s_nop 1\ns_nop 1\ns_nop 1\ns_nop 1\n"
"s_nop 1\ns_nop 1\ns_nop 1\ns_nop 1\n"
"buffer_load_dwordx4 v[116:119], v20, s[8:11], s41 offen offset:0 sc1\nbuffer_load_dwordx4 v[120:123], v20, s[8:11], s41 offen offset:1024 sc1\nv_add_f32 v219, v68, v69\nv_add_f32 v220, v70, v71\n"
"v_cvt_pk_bf16_f32 v156, v219, v220\ns_nop 0\nv_add_f32 v219, v72, v73\nv_add_f32 v220, v74, v75\n"
"v_cvt_pk_bf16_f32 v157, v219, v220\ns_nop 0\nv_add_f32 v219, v76, v77\nv_add_f32 v220, v78, v79\n"
"v_cvt_pk_bf16_f32 v158, v219, v220\ns_nop 0\nv_add_f32 v219, v80, v81\nv_add_f32 v220, v82, v83\n"
"v_cvt_pk_bf16_f32 v159, v219, v220\ns_nop 0\ns_nop 1\ns_nop 1\n"
"s_nop 1\ns_nop 1\ns_nop 1\ns_nop 1\n"
"s_nop 1\ns_nop 1\nbuffer_load_dwordx4 v[124:127], v21, s[8:11], s41 offen offset:0 sc1\nbuffer_load_dwordx4 v[128:131], v21, s[8:11], s41 offen offset:1024 sc1\n"
"s_nop 15\ns_nop 15\ns_nop 4\ns_cbranch_execnz tl24_p0\n"
"va26_p0:\ns_nop 0\nv_smfmac_f32_16x16x64_bf16 v[80:83], v[60:63], a[208:215], v8\nv_smfmac_f32_16x16x64_bf16 v[68:71], v[64:67], a[48:55], v8\n"
"v_smfmac_f32_16x16x64_bf16 v[72:75], v[64:67], a[104:111], v8\nv_smfmac_f32_16x16x64_bf16 v[76:79], v[64:67], a[160:167], v8\nv_smfmac_f32_16x16x64_bf16 v[80:83], v[64:67], a[216:223], v8\ns_nop 1\n"
"s_nop 1\ns_nop 1\ns_nop 1\ns_nop 1\n"
"s_nop 1\ns_nop 1\ns_nop 1\ns_nop 1\n"
"s_nop 1\ns_nop 1\ns_nop 1\nbuffer_load_dwordx4 v[116:119], v20, s[8:11], s41 offen offset:0 sc1\n"
"buffer_load_dwordx4 v[120:123], v20, s[8:11], s41 offen offset:1024 sc1\nv_add_f32 v219, v68, v69\nv_add_f32 v220, v70, v71\nv_cvt_pk_bf16_f32 v156, v219, v220\n"
"s_nop 0\nv_add_f32 v219, v72, v73\nv_add_f32 v220, v74, v75\nv_cvt_pk_bf16_f32 v157, v219, v220\n"
"s_nop 0\nv_add_f32 v219, v76, v77\nv_add_f32 v220, v78, v79\nv_cvt_pk_bf16_f32 v158, v219, v220\n"
"s_nop 0\nv_add_f32 v219, v80, v81\nv_add_f32 v220, v82, v83\nv_cvt_pk_bf16_f32 v159, v219, v220\n"
"s_nop 0\ns_nop 1\ns_nop 1\ns_nop 1\n"
"s_nop 1\ns_nop 1\ns_nop 1\ns_nop 1\n"
"s_nop 1\nbuffer_load_dwordx4 v[124:127], v21, s[8:11], s41 offen offset:0 sc1\nbuffer_load_dwordx4 v[128:131], v21, s[8:11], s41 offen offset:1024 sc1\ns_nop 15\n"
"s_nop 15\ns_nop 4\ns_cbranch_execnz tl24_p0\nva27_p0:\n"
"s_nop 0\nv_smfmac_f32_16x16x64_bf16 v[68:71], v[64:67], a[48:55], v8\nv_smfmac_f32_16x16x64_bf16 v[72:75], v[64:67], a[104:111], v8\nv_smfmac_f32_16x16x64_bf16 v[76:79], v[64:67], a[160:167], v8\n"
"v_smfmac_f32_16x16x64_bf16 v[80:83], v[64:67], a[216:223], v8\ns_nop 1\ns_nop 1\ns_nop 1\n"
"s_nop 1\ns_nop 1\ns_nop 1\ns_nop 1\n"
"s_nop 1\ns_nop 1\ns_nop 1\ns_nop 1\n"
"s_nop 1\ns_nop 1\ns_nop 1\nbuffer_load_dwordx4 v[116:119], v20, s[8:11], s41 offen offset:0 sc1\n"
"buffer_load_dwordx4 v[120:123], v20, s[8:11], s41 offen offset:1024 sc1\nv_add_f32 v219, v68, v69\nv_add_f32 v220, v70, v71\nv_cvt_pk_bf16_f32 v156, v219, v220\n"
"s_nop 0\nv_add_f32 v219, v72, v73\nv_add_f32 v220, v74, v75\nv_cvt_pk_bf16_f32 v157, v219, v220\n"
"s_nop 0\nv_add_f32 v219, v76, v77\nv_add_f32 v220, v78, v79\nv_cvt_pk_bf16_f32 v158, v219, v220\n"
"s_nop 0\nv_add_f32 v219, v80, v81\nv_add_f32 v220, v82, v83\nv_cvt_pk_bf16_f32 v159, v219, v220\n"
"s_nop 0\ns_nop 1\ns_nop 1\ns_nop 1\n"
"s_nop 1\ns_nop 1\ns_nop 1\ns_nop 1\n"
"s_nop 1\nbuffer_load_dwordx4 v[124:127], v21, s[8:11], s41 offen offset:0 sc1\nbuffer_load_dwordx4 v[128:131], v21, s[8:11], s41 offen offset:1024 sc1\ns_nop 15\n"
"s_nop 15\ns_nop 4\ns_cbranch_execnz tl24_p0\nva28_p0:\n"
"s_nop 0\nv_smfmac_f32_16x16x64_bf16 v[72:75], v[64:67], a[104:111], v8\nv_smfmac_f32_16x16x64_bf16 v[76:79], v[64:67], a[160:167], v8\nv_smfmac_f32_16x16x64_bf16 v[80:83], v[64:67], a[216:223], v8\n"
"s_nop 1\ns_nop 1\ns_nop 1\ns_nop 1\n"
"s_nop 1\ns_nop 1\ns_nop 1\ns_nop 1\n"
"s_nop 1\ns_nop 1\ns_nop 1\ns_nop 1\n"
"s_nop 1\ns_nop 1\ns_nop 1\ns_nop 1\n"
"buffer_load_dwordx4 v[116:119], v20, s[8:11], s41 offen offset:0 sc1\nbuffer_load_dwordx4 v[120:123], v20, s[8:11], s41 offen offset:1024 sc1\nv_add_f32 v219, v68, v69\nv_add_f32 v220, v70, v71\n"
"v_cvt_pk_bf16_f32 v156, v219, v220\ns_nop 0\nv_add_f32 v219, v72, v73\nv_add_f32 v220, v74, v75\n"
"v_cvt_pk_bf16_f32 v157, v219, v220\ns_nop 0\nv_add_f32 v219, v76, v77\nv_add_f32 v220, v78, v79\n"
"v_cvt_pk_bf16_f32 v158, v219, v220\ns_nop 0\nv_add_f32 v219, v80, v81\nv_add_f32 v220, v82, v83\n"
"v_cvt_pk_bf16_f32 v159, v219, v220\ns_nop 0\ns_nop 1\ns_nop 1\n"
"s_nop 1\ns_nop 1\ns_nop 1\ns_nop 1\n"
"s_nop 1\ns_nop 1\nbuffer_load_dwordx4 v[124:127], v21, s[8:11], s41 offen offset:0 sc1\nbuffer_load_dwordx4 v[128:131], v21, s[8:11], s41 offen offset:1024 sc1\n"
"s_nop 15\ns_nop 15\ns_nop 4\ns_cbranch_execnz tl24_p0\n"
"va29_p0:\ns_nop 0\nv_smfmac_f32_16x16x64_bf16 v[76:79], v[64:67], a[160:167], v8\nv_smfmac_f32_16x16x64_bf16 v[80:83], v[64:67], a[216:223], v8\n"
"s_nop 1\ns_nop 1\ns_nop 1\ns_nop 1\n"
"s_nop 1\ns_nop 1\ns_nop 1\ns_nop 1\n"
"s_nop 1\ns_nop 1\ns_nop 1\ns_nop 1\n"
"s_nop 1\ns_nop 1\ns_nop 1\ns_nop 1\n"
"s_nop 1\ns_nop 1\nbuffer_load_dwordx4 v[116:119], v20, s[8:11], s41 offen offset:0 sc1\nbuffer_load_dwordx4 v[120:123], v20, s[8:11], s41 offen offset:1024 sc1\n"
"v_add_f32 v219, v68, v69\nv_add_f32 v220, v70, v71\nv_cvt_pk_bf16_f32 v156, v219, v220\ns_nop 0\n"
"v_add_f32 v219, v72, v73\nv_add_f32 v220, v74, v75\nv_cvt_pk_bf16_f32 v157, v219, v220\ns_nop 0\n"
"v_add_f32 v219, v76, v77\nv_add_f32 v220, v78, v79\nv_cvt_pk_bf16_f32 v158, v219, v220\ns_nop 0\n"
"v_add_f32 v219, v80, v81\nv_add_f32 v220, v82, v83\nv_cvt_pk_bf16_f32 v159, v219, v220\ns_nop 0\n"
"s_nop 1\ns_nop 1\ns_nop 1\ns_nop 1\n"
"s_nop 1\ns_nop 1\ns_nop 1\ns_nop 1\n"
"buffer_load_dwordx4 v[124:127], v21, s[8:11], s41 offen offset:0 sc1\nbuffer_load_dwordx4 v[128:131], v21, s[8:11], s41 offen offset:1024 sc1\ns_nop 15\ns_nop 15\n"
"s_nop 4\ns_cbranch_execnz tl24_p0\nva30_p0:\ns_nop 0\n"
"v_smfmac_f32_16x16x64_bf16 v[80:83], v[64:67], a[216:223], v8\ns_nop 1\ns_nop 1\ns_nop 1\n"
"s_nop 1\ns_nop 1\ns_nop 1\ns_nop 1\n"
"s_nop 1\ns_nop 1\ns_nop 1\ns_nop 1\n"
"s_nop 1\ns_nop 1\ns_nop 1\ns_nop 1\n"
"s_nop 1\ns_nop 1\ns_nop 1\ns_nop 1\n"
"s_nop 1\nbuffer_load_dwordx4 v[116:119], v20, s[8:11], s41 offen offset:0 sc1\nbuffer_load_dwordx4 v[120:123], v20, s[8:11], s41 offen offset:1024 sc1\nv_add_f32 v219, v68, v69\n"
"v_add_f32 v220, v70, v71\nv_cvt_pk_bf16_f32 v156, v219, v220\ns_nop 0\nv_add_f32 v219, v72, v73\n"
"v_add_f32 v220, v74, v75\nv_cvt_pk_bf16_f32 v157, v219, v220\ns_nop 0\nv_add_f32 v219, v76, v77\n"
"v_add_f32 v220, v78, v79\nv_cvt_pk_bf16_f32 v158, v219, v220\ns_nop 0\nv_add_f32 v219, v80, v81\n"
"v_add_f32 v220, v82, v83\nv_cvt_pk_bf16_f32 v159, v219, v220\ns_nop 0\ns_nop 1\n"
"s_nop 1\ns_nop 1\ns_nop 1\ns_nop 1\n"
"s_nop 1\ns_nop 1\ns_nop 1\nbuffer_load_dwordx4 v[124:127], v21, s[8:11], s41 offen offset:0 sc1\n"
"buffer_load_dwordx4 v[128:131], v21, s[8:11], s41 offen offset:1024 sc1\ns_nop 15\ns_nop 15\ns_nop 4\n"
"s_cbranch_execnz tl24_p0\nva31_p0:\ns_nop 0\ns_nop 1\n"
"s_nop 1\ns_nop 1\ns_nop 1\ns_nop 1\n"
"s_nop 1\ns_nop 1\ns_nop 1\ns_nop 1\n"
"s_nop 1\ns_nop 1\ns_nop 1\ns_nop 1\n"
"s_nop 1\ns_nop 1\ns_nop 1\ns_nop 1\n"
"s_nop 1\ns_nop 1\ns_nop 1\ns_nop 1\n"
"s_nop 1\nbuffer_load_dwordx4 v[116:119], v20, s[8:11], s41 offen offset:0 sc1\nbuffer_load_dwordx4 v[120:123], v20, s[8:11], s41 offen offset:1024 sc1\nv_add_f32 v219, v68, v69\n"
"v_add_f32 v220, v70, v71\nv_cvt_pk_bf16_f32 v156, v219, v220\ns_nop 0\nv_add_f32 v219, v72, v73\n"
"v_add_f32 v220, v74, v75\nv_cvt_pk_bf16_f32 v157, v219, v220\ns_nop 0\nv_add_f32 v219, v76, v77\n"
"v_add_f32 v220, v78, v79\nv_cvt_pk_bf16_f32 v158, v219, v220\ns_nop 0\nv_add_f32 v219, v80, v81\n"
"v_add_f32 v220, v82, v83\nv_cvt_pk_bf16_f32 v159, v219, v220\ns_nop 0\ns_nop 1\n"
"s_nop 1\ns_nop 1\ns_nop 1\ns_nop 1\n"
"s_nop 1\ns_nop 1\ns_nop 1\nbuffer_load_dwordx4 v[124:127], v21, s[8:11], s41 offen offset:0 sc1\n"
"buffer_load_dwordx4 v[128:131], v21, s[8:11], s41 offen offset:1024 sc1\ns_nop 15\ns_nop 15\ns_nop 4\n"
"s_cbranch_execnz tl24_p0\nva32_p0:\ns_branch warm_p0\nend_p0:\n"
"s_branch fk_done\nfk_done:\ns_endpgm\n"
    :: "{s[8:23]}"(s0), "{s[24:39]}"(s1), "{v[8:23]}"(v0), "{v[24:39]}"(v1) : "v40", "v41", "v42", "v43", "v44", "v45", "v46", "v47", "v48", "v49", "v50", "v51", "v52", "v53", "v54", "v55", "v56", "v57", "v58", "v59", "v60", "v61", "v62", "v63", "v64", "v65", "v66", "v67", "v68", "v69", "v70", "v71", "v72", "v73", "v74", "v75", "v76", "v77", "v78", "v79", "v80", "v81", "v82", "v83", "v84", "v85", "v86", "v87", "v88", "v89", "v90", "v91", "v92", "v93", "v94", "v95", "v96", "v97", "v98", "v99", "v100", "v101", "v102", "v103", "v104", "v105", "v106", "v107", "v108", "v109", "v110", "v111", "v112", "v113", "v114", "v115", "v116", "v117", "v118", "v119", "v120", "v121", "v122", "v123", "v124", "v125", "v126", "v127", "v128", "v129", "v130", "v131", "v132", "v133", "v134", "v135", "v136", "v137", "v138", "v139", "v140", "v141", "v142", "v143", "v144", "v145", "v146", "v147", "v148", "v149", "v150", "v151", "v152", "v153", "v154", "v155", "v156", "v157", "v158", "v159", "v160", "v161", "v162", "v163", "v164", "v165", "v166", "v167", "v168", "v169", "v170", "v171", "v172", "v173", "v174", "v175", "v176", "v177", "v178", "v179", "v180", "v181", "v182", "v183", "v184", "v185", "v186", "v187", "v188", "v189", "v190", "v191", "v192", "v193", "v194", "v195", "v196", "v197", "v198", "v199", "v200", "v201", "v202", "v203", "v204", "v205", "v206", "v207", "v208", "v209", "v210", "v211", "v212", "v213", "v214", "v215", "v216", "v217", "v218", "v219", "v220", "v221", "v222", "v223", "v224", "v225", "v226", "v227", "v228", "v229", "v230", "v231", "v232", "v233", "v234", "v235", "v236", "v237", "v238", "v239", "v240", "v241", "v242", "v243", "v244", "v245", "v246", "v247", "v248", "v249", "v250", "v251", "v252", "v253", "v254", "v255", "a0", "a1", "a2", "a3", "a4", "a5", "a6", "a7", "a8", "a9", "a10", "a11", "a12", "a13", "a14", "a15", "a16", "a17", "a18", "a19", "a20", "a21", "a22", "a23", "a24", "a25", "a26", "a27", "a28", "a29", "a30", "a31", "a32", "a33", "a34", "a35", "a36", "a37", "a38", "a39", "a40", "a41", "a42", "a43", "a44", "a45", "a46", "a47", "a48", "a49", "a50", "a51", "a52", "a53", "a54", "a55", "a56", "a57", "a58", "a59", "a60", "a61", "a62", "a63", "a64", "a65", "a66", "a67", "a68", "a69", "a70", "a71", "a72", "a73", "a74", "a75", "a76", "a77", "a78", "a79", "a80", "a81", "a82", "a83", "a84", "a85", "a86", "a87", "a88", "a89", "a90", "a91", "a92", "a93", "a94", "a95", "a96", "a97", "a98", "a99", "a100", "a101", "a102", "a103", "a104", "a105", "a106", "a107", "a108", "a109", "a110", "a111", "a112", "a113", "a114", "a115", "a116", "a117", "a118", "a119", "a120", "a121", "a122", "a123", "a124", "a125", "a126", "a127", "a128", "a129", "a130", "a131", "a132", "a133", "a134", "a135", "a136", "a137", "a138", "a139", "a140", "a141", "a142", "a143", "a144", "a145", "a146", "a147", "a148", "a149", "a150", "a151", "a152", "a153", "a154", "a155", "a156", "a157", "a158", "a159", "a160", "a161", "a162", "a163", "a164", "a165", "a166", "a167", "a168", "a169", "a170", "a171", "a172", "a173", "a174", "a175", "a176", "a177", "a178", "a179", "a180", "a181", "a182", "a183", "a184", "a185", "a186", "a187", "a188", "a189", "a190", "a191", "a192", "a193", "a194", "a195", "a196", "a197", "a198", "a199", "a200", "a201", "a202", "a203", "a204", "a205", "a206", "a207", "a208", "a209", "a210", "a211", "a212", "a213", "a214", "a215", "a216", "a217", "a218", "a219", "a220", "a221", "a222", "a223", "a224", "a225", "a226", "a227", "a228", "a229", "a230", "a231", "a232", "a233", "a234", "a235", "a236", "a237", "a238", "a239", "a240", "a241", "a242", "a243", "a244", "a245", "a246", "a247", "a248", "a249", "a250", "a251", "a252", "a253", "a254", "a255", "s40", "s41", "s42", "s43", "s44", "s45", "s46", "s47", "s48", "s49", "s50", "s51", "s52", "s53", "s54", "s55", "s56", "s57", "s58", "s59", "s60", "s61", "s62", "s63", "s64", "s65", "s66", "s67", "s68", "s69", "s70", "s71", "s72", "s73", "s74", "s75", "s76", "s77", "s78", "s79", "s80", "s81", "s82", "s83", "s84", "s85", "s86", "s87", "s88", "s89", "s90", "s91", "s92", "s93", "s94", "s95", "vcc", "scc", "memory");
    __builtin_unreachable();
}
struct GCtx { u4 rw1, rx, rexp, rout, rhb; unsigned tag, rot, sw1, mw, mx, sfg, stay, csc, ostr, kb, p1, p2; unsigned vr0, vr1, vx, wa, xa, vexp, vimp, vo0, vo1, vo2, vo3, vfl, vpf, vpo, vk, p5; };
DI unsigned gemm_asm0(const GCtx& c) {
    const u16v s0 = {RFL(c.rw1[0]), RFL(c.rw1[1]), RFL(c.rw1[2]), RFL(c.rw1[3]), RFL(c.rx[0]), RFL(c.rx[1]), RFL(c.rx[2]), RFL(c.rx[3]), RFL(c.rexp[0]), RFL(c.rexp[1]), RFL(c.rexp[2]), RFL(c.rexp[3]), RFL(c.rout[0]), RFL(c.rout[1]), RFL(c.rout[2]), RFL(c.rout[3])}, s1 = {RFL(c.rhb[0]), RFL(c.rhb[1]), RFL(c.rhb[2]), RFL(c.rhb[3]), RFL(c.tag), RFL(c.rot), RFL(c.sw1), RFL(c.mw), RFL(c.mx), RFL(c.sfg), RFL(c.stay), RFL(c.csc), RFL(c.ostr), RFL(c.kb), RFL(c.p1), RFL(c.p2)}, v0 = {c.vr0, c.vr1, c.vx, c.wa, c.xa, c.vexp, c.vimp, c.vo0, c.vo1, c.vo2, c.vo3, c.vfl, c.vpf, c.vpo, c.vk, c.p5};
    unsigned o = 0;
    asm volatile(
"s_nop 4\ns_add_u32 s40, s29, 0\ns_add_u32 s41, s29, 1\ns_add_u32 s42, s29, 2\n"
"s_add_u32 s43, s29, 3\nv_xor_b32 v24, 0, v11\nv_xor_b32 v25, 64, v11\nv_xor_b32 v26, 128, v11\n"
"v_xor_b32 v27, 192, v11\nv_xor_b32 v28, 2064, v11\nv_xor_b32 v29, 2128, v11\nv_xor_b32 v30, 2192, v11\n"
"v_xor_b32 v31, 2256, v11\nv_add_u32 v40, s37, v24\nv_add_u32 v41, s37, v25\nv_add_u32 v42, s37, v26\n"
"v_add_u32 v43, s37, v27\nv_add_u32 v44, s37, v28\nv_add_u32 v45, s37, v29\nv_add_u32 v46, s37, v30\n"
"v_add_u32 v47, s37, v31\nds_read_b128 a[128:131], v40 offset:0\nds_read_b128 a[132:135], v41 offset:0\nds_read_b128 a[136:139], v42 offset:0\n"
"ds_read_b128 a[140:143], v43 offset:0\ns_lshl_b32 s53, s41, 19\ns_add_u32 s53, s53, s30\ns_add_u32 m0, s31, 65536\n"
"s_add_u32 s53, s53, 0\nbuffer_load_dwordx4 v8, s[8:11], s53 offen lds\nbuffer_load_dwordx4 v9, s[8:11], s53 offen offset:1024 lds\nds_read_b128 a[144:147], v40 offset:256\n"
"ds_read_b128 a[148:151], v41 offset:256\nds_read_b128 a[152:155], v42 offset:256\nds_read_b128 a[156:159], v43 offset:256\ns_lshl_b32 s53, s41, 19\n"
"s_add_u32 s53, s53, s30\ns_add_u32 m0, s31, 73728\ns_add_u32 s53, s53, 131072\nbuffer_load_dwordx4 v8, s[8:11], s53 offen lds\n"
"buffer_load_dwordx4 v9, s[8:11], s53 offen offset:1024 lds\ns_waitcnt lgkmcnt(0)\nds_read_b128 a[160:163], v44 offset:0\nds_read_b128 a[164:167], v45 offset:0\n"
"ds_read_b128 a[168:171], v46 offset:0\nds_read_b128 a[172:175], v47 offset:0\ns_lshl_b32 s53, s41, 19\ns_add_u32 s53, s53, s30\n"
"s_add_u32 m0, s31, 81920\ns_add_u32 s53, s53, 262144\nbuffer_load_dwordx4 v8, s[8:11], s53 offen lds\nbuffer_load_dwordx4 v9, s[8:11], s53 offen offset:1024 lds\n"
"ds_read_b128 a[176:179], v44 offset:256\nds_read_b128 a[180:183], v45 offset:256\nds_read_b128 a[184:187], v46 offset:256\nds_read_b128 a[188:191], v47 offset:256\n"
"s_lshl_b32 s53, s41, 19\ns_add_u32 s53, s53, s30\ns_add_u32 m0, s31, 90112\ns_add_u32 s53, s53, 393216\n"
"buffer_load_dwordx4 v8, s[8:11], s53 offen lds\nbuffer_load_dwordx4 v9, s[8:11], s53 offen offset:1024 lds\ns_waitcnt lgkmcnt(0)\nv_add_u32 v40, 65536, v24\n"
"v_add_u32 v41, 65536, v25\nv_add_u32 v42, 65536, v26\nv_add_u32 v43, 65536, v27\nv_add_u32 v44, 65536, v28\n"
"v_add_u32 v45, 65536, v29\nv_add_u32 v46, 65536, v30\nv_add_u32 v47, 65536, v31\nv_xor_b32 v32, 0, v12\n"
"v_xor_b32 v33, 2048, v12\nv_xor_b32 v34, 64, v12\nv_xor_b32 v35, 2112, v12\ns_mov_b32 s55, 0\n"
"s_waitcnt vmcnt(8)\ns_barrier\nds_read_b128 v[96:99], v24 offset:0\nds_read_b128 v[100:103], v28 offset:0\n"
"ds_read_b128 v[104:107], v25 offset:0\nds_read_b128 v[108:111], v29 offset:0\nds_read_b128 v[112:115], v26 offset:0\nds_read_b128 v[116:119], v30 offset:0\n"
"ds_read_b128 v[120:123], v27 offset:0\nds_read_b128 v[124:127], v31 offset:0\ns_waitcnt lgkmcnt(6)\nv_mfma_f32_16x16x32_bf16 v[64:67], v[96:99], a[128:131], 0\n"
"s_lshl_b32 s53, s42, 19\ns_add_u32 s53, s53, s30\nv_mfma_f32_16x16x32_bf16 v[68:71], v[96:99], a[160:163], 0\nds_read_b128 v[96:99], v24 offset:256\n"
"s_add_u32 m0, s31, 98304\ns_add_u32 s53, s53, 0\nv_mfma_f32_16x16x32_bf16 v[72:75], v[100:103], a[128:131], 0\nbuffer_load_dwordx4 v8, s[8:11], s53 offen lds\n"
"v_mfma_f32_16x16x32_bf16 v[76:79], v[100:103], a[160:163], 0\nds_read_b128 v[100:103], v28 offset:256\nbuffer_load_dwordx4 v9, s[8:11], s53 offen offset:1024 lds\ns_waitcnt lgkmcnt(6)\n"
"v_mfma_f32_16x16x32_bf16 v[64:67], v[104:107], a[132:135], v[64:67]\ns_lshl_b32 s53, s42, 19\ns_add_u32 s53, s53, s30\nv_mfma_f32_16x16x32_bf16 v[68:71], v[104:107], a[164:167], v[68:71]\n"
"ds_read_b128 v[104:107], v25 offset:256\ns_add_u32 m0, s31, 106496\ns_add_u32 s53, s53, 131072\nv_mfma_f32_16x16x32_bf16 v[72:75], v[108:111], a[132:135], v[72:75]\n"
"buffer_load_dwordx4 v8, s[8:11], s53 offen lds\nv_mfma_f32_16x16x32_bf16 v[76:79], v[108:111], a[164:167], v[76:79]\nds_read_b128 v[108:111], v29 offset:256\nbuffer_load_dwordx4 v9, s[8:11], s53 offen offset:1024 lds\n"
"s_waitcnt lgkmcnt(6)\nv_mfma_f32_16x16x32_bf16 v[64:67], v[112:115], a[136:139], v[64:67]\nv_mfma_f32_16x16x32_bf16 v[68:71], v[112:115], a[168:171], v[68:71]\nds_read_b128 v[112:115], v26 offset:256\n"
"v_mfma_f32_16x16x32_bf16 v[72:75], v[116:119], a[136:139], v[72:75]\nv_mfma_f32_16x16x32_bf16 v[76:79], v[116:119], a[168:171], v[76:79]\nds_read_b128 v[116:119], v30 offset:256\ns_waitcnt lgkmcnt(6)\n"
"v_mfma_f32_16x16x32_bf16 v[64:67], v[120:123], a[140:143], v[64:67]\nv_mfma_f32_16x16x32_bf16 v[68:71], v[120:123], a[172:175], v[68:71]\nds_read_b128 v[120:123], v27 offset:256\nv_mfma_f32_16x16x32_bf16 v[72:75], v[124:127], a[140:143], v[72:75]\n"
"v_mfma_f32_16x16x32_bf16 v[76:79], v[124:127], a[172:175], v[76:79]\nds_read_b128 v[124:127], v31 offset:256\ns_waitcnt lgkmcnt(6)\nv_mfma_f32_16x16x32_bf16 v[64:67], v[96:99], a[144:147], v[64:67]\n"
"v_mfma_f32_16x16x32_bf16 v[68:71], v[96:99], a[176:179], v[68:71]\nds_read_b128 v[96:99], v24 offset:16384\nv_mfma_f32_16x16x32_bf16 v[72:75], v[100:103], a[144:147], v[72:75]\nv_mfma_f32_16x16x32_bf16 v[76:79], v[100:103], a[176:179], v[76:79]\n"
"ds_read_b128 v[100:103], v28 offset:16384\ns_waitcnt lgkmcnt(6)\nv_mfma_f32_16x16x32_bf16 v[64:67], v[104:107], a[148:151], v[64:67]\nv_mfma_f32_16x16x32_bf16 v[68:71], v[104:107], a[180:183], v[68:71]\n"
"ds_read_b128 v[104:107], v25 offset:16384\nv_mfma_f32_16x16x32_bf16 v[72:75], v[108:111], a[148:151], v[72:75]\nv_mfma_f32_16x16x32_bf16 v[76:79], v[108:111], a[180:183], v[76:79]\nds_read_b128 v[108:111], v29 offset:16384\n"
"s_waitcnt lgkmcnt(6)\nv_mfma_f32_16x16x32_bf16 v[64:67], v[112:115], a[152:155], v[64:67]\nv_mfma_f32_16x16x32_bf16 v[68:71], v[112:115], a[184:187], v[68:71]\nds_read_b128 v[112:115], v26 offset:16384\n"
"v_mfma_f32_16x16x32_bf16 v[72:75], v[116:119], a[152:155], v[72:75]\nv_mfma_f32_16x16x32_bf16 v[76:79], v[116:119], a[184:187], v[76:79]\nds_read_b128 v[116:119], v30 offset:16384\ns_waitcnt lgkmcnt(6)\n"
"v_mfma_f32_16x16x32_bf16 v[64:67], v[120:123], a[156:159], v[64:67]\nv_mfma_f32_16x16x32_bf16 v[68:71], v[120:123], a[188:191], v[68:71]\nds_read_b128 v[120:123], v27 offset:16384\nv_mfma_f32_16x16x32_bf16 v[72:75], v[124:127], a[156:159], v[72:75]\n"
"v_mfma_f32_16x16x32_bf16 v[76:79], v[124:127], a[188:191], v[76:79]\nds_read_b128 v[124:127], v31 offset:16384\ns_waitcnt lgkmcnt(6)\nv_mfma_f32_16x16x32_bf16 v[80:83], v[96:99], a[128:131], 0\n"
"s_lshl_b32 s53, s42, 19\ns_add_u32 s53, s53, s30\nv_mfma_f32_16x16x32_bf16 v[84:87], v[96:99], a[160:163], 0\nds_read_b128 v[96:99], v24 offset:16640\n"
"s_add_u32 m0, s31, 114688\ns_add_u32 s53, s53, 262144\nv_mfma_f32_16x16x32_bf16 v[88:91], v[100:103], a[128:131], 0\nbuffer_load_dwordx4 v8, s[8:11], s53 offen lds\n"
"v_mfma_f32_16x16x32_bf16 v[92:95], v[100:103], a[160:163], 0\nds_read_b128 v[100:103], v28 offset:16640\nbuffer_load_dwordx4 v9, s[8:11], s53 offen offset:1024 lds\ns_waitcnt lgkmcnt(6)\n"
"v_mfma_f32_16x16x32_bf16 v[80:83], v[104:107], a[132:135], v[80:83]\nv_mul_f32 v64, s35, v64\nv_mul_f32 v65, s35, v65\nv_mfma_f32_16x16x32_bf16 v[84:87], v[104:107], a[164:167], v[84:87]\n"
"ds_read_b128 v[104:107], v25 offset:16640\nv_mul_f32 v66, s35, v66\nv_mul_f32 v67, s35, v67\nv_mfma_f32_16x16x32_bf16 v[88:91], v[108:111], a[132:135], v[88:91]\n"
"v_cvt_pk_bf16_f32 v128, v64, v65\nv_cvt_pk_bf16_f32 v129, v66, v67\nv_mfma_f32_16x16x32_bf16 v[92:95], v[108:111], a[164:167], v[92:95]\nds_read_b128 v[108:111], v29 offset:16640\n"
"v_mul_f32 v72, s35, v72\nv_mul_f32 v73, s35, v73\ns_waitcnt lgkmcnt(6)\nv_mfma_f32_16x16x32_bf16 v[80:83], v[112:115], a[136:139], v[80:83]\n"
"v_mul_f32 v74, s35, v74\nv_mul_f32 v75, s35, v75\nv_mfma_f32_16x16x32_bf16 v[84:87], v[112:115], a[168:171], v[84:87]\nds_read_b128 v[112:115], v26 offset:16640\n"
"v_cvt_pk_bf16_f32 v130, v72, v73\nv_cvt_pk_bf16_f32 v131, v74, v75\nv_mfma_f32_16x16x32_bf16 v[88:91], v[116:119], a[136:139], v[88:91]\nv_mul_f32 v68, s35, v68\n"
"v_mul_f32 v69, s35, v69\nv_mfma_f32_16x16x32_bf16 v[92:95], v[116:119], a[168:171], v[92:95]\nds_read_b128 v[116:119], v30 offset:16640\nv_mul_f32 v70, s35, v70\n"
"v_mul_f32 v71, s35, v71\ns_waitcnt lgkmcnt(6)\nv_mfma_f32_16x16x32_bf16 v[80:83], v[120:123], a[140:143], v[80:83]\nv_cvt_pk_bf16_f32 v132, v68, v69\n"
"v_cvt_pk_bf16_f32 v133, v70, v71\nv_mfma_f32_16x16x32_bf16 v[84:87], v[120:123], a[172:175], v[84:87]\nds_read_b128 v[120:123], v27 offset:16640\nv_mul_f32 v76, s35, v76\n"
"v_mul_f32 v77, s35, v77\nv_mfma_f32_16x16x32_bf16 v[88:91], v[124:127], a[140:143], v[88:91]\nv_mul_f32 v78, s35, v78\nv_mul_f32 v79, s35, v79\n"
"v_mfma_f32_16x16x32_bf16 v[92:95], v[124:127], a[172:175], v[92:95]\nds_read_b128 v[124:127], v31 offset:16640\nv_cvt_pk_bf16_f32 v134, v76, v77\nv_cvt_pk_bf16_f32 v135, v78, v79\n"
"s_waitcnt vmcnt(6)\ns_barrier\ns_waitcnt lgkmcnt(6)\nv_mfma_f32_16x16x32_bf16 v[80:83], v[96:99], a[144:147], v[80:83]\n"
"s_lshl_b32 s54, s40, 11\ns_add_u32 s54, s54, 0\nv_mfma_f32_16x16x32_bf16 v[84:87], v[96:99], a[176:179], v[84:87]\nds_read_b128 v[96:99], v24 offset:32768\n"
"buffer_store_dwordx4 v[128:131], v13, s[16:19], s54 offen sc1\nv_mfma_f32_16x16x32_bf16 v[88:91], v[100:103], a[144:147], v[88:91]\ns_add_u32 s54, s54, 14336\nv_mfma_f32_16x16x32_bf16 v[92:95], v[100:103], a[176:179], v[92:95]\n"
"ds_read_b128 v[100:103], v28 offset:32768\nbuffer_store_dwordx4 v[132:135], v13, s[16:19], s54 offen sc1\ns_waitcnt lgkmcnt(6)\nv_mfma_f32_16x16x32_bf16 v[80:83], v[104:107], a[148:151], v[80:83]\n"
"s_lshl_b32 s53, s42, 19\ns_add_u32 s53, s53, s30\nv_mfma_f32_16x16x32_bf16 v[84:87], v[104:107], a[180:183], v[84:87]\nds_read_b128 v[104:107], v25 offset:32768\n"
"s_add_u32 m0, s31, 122880\ns_add_u32 s53, s53, 393216\nv_mfma_f32_16x16x32_bf16 v[88:91], v[108:111], a[148:151], v[88:91]\nbuffer_load_dwordx4 v8, s[8:11], s53 offen lds\n"
"v_mfma_f32_16x16x32_bf16 v[92:95], v[108:111], a[180:183], v[92:95]\nds_read_b128 v[108:111], v29 offset:32768\nbuffer_load_dwordx4 v9, s[8:11], s53 offen offset:1024 lds\ns_waitcnt lgkmcnt(6)\n"
"v_mfma_f32_16x16x32_bf16 v[80:83], v[112:115], a[152:155], v[80:83]\nv_mfma_f32_16x16x32_bf16 v[84:87], v[112:115], a[184:187], v[84:87]\nds_read_b128 v[112:115], v26 offset:32768\nv_mfma_f32_16x16x32_bf16 v[88:91], v[116:119], a[152:155], v[88:91]\n"
"v_mfma_f32_16x16x32_bf16 v[92:95], v[116:119], a[184:187], v[92:95]\nds_read_b128 v[116:119], v30 offset:32768\ns_waitcnt lgkmcnt(6)\nv_mfma_f32_16x16x32_bf16 v[80:83], v[120:123], a[156:159], v[80:83]\n"
"v_mfma_f32_16x16x32_bf16 v[84:87], v[120:123], a[188:191], v[84:87]\nds_read_b128 v[120:123], v27 offset:32768\nv_mfma_f32_16x16x32_bf16 v[88:91], v[124:127], a[156:159], v[88:91]\nv_mfma_f32_16x16x32_bf16 v[92:95], v[124:127], a[188:191], v[92:95]\n"
"ds_read_b128 v[124:127], v31 offset:32768\ns_waitcnt lgkmcnt(6)\nv_mfma_f32_16x16x32_bf16 v[64:67], v[96:99], a[128:131], 0\ns_lshl_b32 s53, s43, 19\n"
"s_add_u32 s53, s53, s30\nv_mfma_f32_16x16x32_bf16 v[68:71], v[96:99], a[160:163], 0\nds_read_b128 v[96:99], v24 offset:33024\ns_add_u32 m0, s31, 131072\n"
"s_add_u32 s53, s53, 0\nv_mfma_f32_16x16x32_bf16 v[72:75], v[100:103], a[128:131], 0\nbuffer_load_dwordx4 v8, s[8:11], s53 offen lds\nv_mfma_f32_16x16x32_bf16 v[76:79], v[100:103], a[160:163], 0\n"
"ds_read_b128 v[100:103], v28 offset:33024\nbuffer_load_dwordx4 v9, s[8:11], s53 offen offset:1024 lds\ns_waitcnt lgkmcnt(6)\nv_mfma_f32_16x16x32_bf16 v[64:67], v[104:107], a[132:135], v[64:67]\n"
"v_mul_f32 v80, s35, v80\nv_mul_f32 v81, s35, v81\nv_mfma_f32_16x16x32_bf16 v[68:71], v[104:107], a[164:167], v[68:71]\nds_read_b128 v[104:107], v25 offset:33024\n"
"v_mul_f32 v82, s35, v82\nv_mul_f32 v83, s35, v83\nv_mfma_f32_16x16x32_bf16 v[72:75], v[108:111], a[132:135], v[72:75]\nv_cvt_pk_bf16_f32 v136, v80, v81\n"
"v_cvt_pk_bf16_f32 v137, v82, v83\nv_mfma_f32_16x16x32_bf16 v[76:79], v[108:111], a[164:167], v[76:79]\nds_read_b128 v[108:111], v29 offset:33024\nv_mul_f32 v88, s35, v88\n"
"v_mul_f32 v89, s35, v89\ns_waitcnt lgkmcnt(6)\nv_mfma_f32_16x16x32_bf16 v[64:67], v[112:115], a[136:139], v[64:67]\nv_mul_f32 v90, s35, v90\n"
"v_mul_f32 v91, s35, v91\nv_mfma_f32_16x16x32_bf16 v[68:71], v[112:115], a[168:171], v[68:71]\nds_read_b128 v[112:115], v26 offset:33024\nv_cvt_pk_bf16_f32 v138, v88, v89\n"
"v_cvt_pk_bf16_f32 v139, v90, v91\nv_mfma_f32_16x16x32_bf16 v[72:75], v[116:119], a[136:139], v[72:75]\nv_mul_f32 v84, s35, v84\nv_mul_f32 v85, s35, v85\n"
"v_mfma_f32_16x16x32_bf16 v[76:79], v[116:119], a[168:171], v[76:79]\nds_read_b128 v[116:119], v30 offset:33024\nv_mul_f32 v86, s35, v86\nv_mul_f32 v87, s35, v87\n"
"s_waitcnt lgkmcnt(6)\nv_mfma_f32_16x16x32_bf16 v[64:67], v[120:123], a[140:143], v[64:67]\nv_cvt_pk_bf16_f32 v140, v84, v85\nv_cvt_pk_bf16_f32 v141, v86, v87\n"
"v_mfma_f32_16x16x32_bf16 v[68:71], v[120:123], a[172:175], v[68:71]\nds_read_b128 v[120:123], v27 offset:33024\nv_mul_f32 v92, s35, v92\nv_mul_f32 v93, s35, v93\n"
"v_mfma_f32_16x16x32_bf16 v[72:75], v[124:127], a[140:143], v[72:75]\nv_mul_f32 v94, s35, v94\nv_mul_f32 v95, s35, v95\nv_mfma_f32_16x16x32_bf16 v[76:79], v[124:127], a[172:175], v[76:79]\n"
"ds_read_b128 v[124:127], v31 offset:33024\nv_cvt_pk_bf16_f32 v142, v92, v93\nv_cvt_pk_bf16_f32 v143, v94, v95\ns_waitcnt lgkmcnt(6)\n"
"v_mfma_f32_16x16x32_bf16 v[64:67], v[96:99], a[144:147], v[64:67]\ns_lshl_b32 s54, s40, 11\ns_add_u32 s54, s54, 1024\nv_mfma_f32_16x16x32_bf16 v[68:71], v[96:99], a[176:179], v[68:71]\n"
"ds_read_b128 v[96:99], v24 offset:49152\nbuffer_store_dwordx4 v[136:139], v13, s[16:19], s54 offen sc1\nv_mfma_f32_16x16x32_bf16 v[72:75], v[100:103], a[144:147], v[72:75]\ns_add_u32 s54, s54, 14336\n"
"v_mfma_f32_16x16x32_bf16 v[76:79], v[100:103], a[176:179], v[76:79]\nds_read_b128 v[100:103], v28 offset:49152\nbuffer_store_dwordx4 v[140:143], v13, s[16:19], s54 offen sc1\ns_waitcnt lgkmcnt(6)\n"
"v_mfma_f32_16x16x32_bf16 v[64:67], v[104:107], a[148:151], v[64:67]\ns_lshl_b32 s53, s43, 19\ns_add_u32 s53, s53, s30\nv_mfma_f32_16x16x32_bf16 v[68:71], v[104:107], a[180:183], v[68:71]\n"
"ds_read_b128 v[104:107], v25 offset:49152\ns_add_u32 m0, s31, 139264\ns_add_u32 s53, s53, 131072\nv_mfma_f32_16x16x32_bf16 v[72:75], v[108:111], a[148:151], v[72:75]\n"
"buffer_load_dwordx4 v8, s[8:11], s53 offen lds\nv_mfma_f32_16x16x32_bf16 v[76:79], v[108:111], a[180:183], v[76:79]\nds_read_b128 v[108:111], v29 offset:49152\nbuffer_load_dwordx4 v9, s[8:11], s53 offen offset:1024 lds\n"
"s_waitcnt lgkmcnt(6)\nv_mfma_f32_16x16x32_bf16 v[64:67], v[112:115], a[152:155], v[64:67]\nv_mfma_f32_16x16x32_bf16 v[68:71], v[112:115], a[184:187], v[68:71]\nds_read_b128 v[112:115], v26 offset:49152\n"
"v_mfma_f32_16x16x32_bf16 v[72:75], v[116:119], a[152:155], v[72:75]\nv_mfma_f32_16x16x32_bf16 v[76:79], v[116:119], a[184:187], v[76:79]\nds_read_b128 v[116:119], v30 offset:49152\ns_waitcnt lgkmcnt(6)\n"
"v_mfma_f32_16x16x32_bf16 v[64:67], v[120:123], a[156:159], v[64:67]\nv_mfma_f32_16x16x32_bf16 v[68:71], v[120:123], a[188:191], v[68:71]\nds_read_b128 v[120:123], v27 offset:49152\nv_mfma_f32_16x16x32_bf16 v[72:75], v[124:127], a[156:159], v[72:75]\n"
"v_mfma_f32_16x16x32_bf16 v[76:79], v[124:127], a[188:191], v[76:79]\nds_read_b128 v[124:127], v31 offset:49152\ns_waitcnt lgkmcnt(6)\nv_mfma_f32_16x16x32_bf16 v[80:83], v[96:99], a[128:131], 0\n"
"s_lshl_b32 s53, s43, 19\ns_add_u32 s53, s53, s30\nv_mfma_f32_16x16x32_bf16 v[84:87], v[96:99], a[160:163], 0\nds_read_b128 v[96:99], v24 offset:49408\n"
"s_add_u32 m0, s31, 147456\ns_add_u32 s53, s53, 262144\nv_mfma_f32_16x16x32_bf16 v[88:91], v[100:103], a[128:131], 0\nbuffer_load_dwordx4 v8, s[8:11], s53 offen lds\n"
"v_mfma_f32_16x16x32_bf16 v[92:95], v[100:103], a[160:163], 0\nds_read_b128 v[100:103], v28 offset:49408\nbuffer_load_dwordx4 v9, s[8:11], s53 offen offset:1024 lds\ns_waitcnt lgkmcnt(6)\n"
"v_mfma_f32_16x16x32_bf16 v[80:83], v[104:107], a[132:135], v[80:83]\nv_mul_f32 v64, s35, v64\nv_mul_f32 v65, s35, v65\nv_mfma_f32_16x16x32_bf16 v[84:87], v[104:107], a[164:167], v[84:87]\n"
"ds_read_b128 v[104:107], v25 offset:49408\nv_mul_f32 v66, s35, v66\nv_mul_f32 v67, s35, v67\nv_mfma_f32_16x16x32_bf16 v[88:91], v[108:111], a[132:135], v[88:91]\n"
"v_cvt_pk_bf16_f32 v144, v64, v65\nv_cvt_pk_bf16_f32 v145, v66, v67\nv_mfma_f32_16x16x32_bf16 v[92:95], v[108:111], a[164:167], v[92:95]\nds_read_b128 v[108:111], v29 offset:49408\n"
"v_mul_f32 v72, s35, v72\nv_mul_f32 v73, s35, v73\ns_waitcnt lgkmcnt(6)\nv_mfma_f32_16x16x32_bf16 v[80:83], v[112:115], a[136:139], v[80:83]\n"
"v_mul_f32 v74, s35, v74\nv_mul_f32 v75, s35, v75\nv_mfma_f32_16x16x32_bf16 v[84:87], v[112:115], a[168:171], v[84:87]\nds_read_b128 v[112:115], v26 offset:49408\n"
"v_cvt_pk_bf16_f32 v146, v72, v73\nv_cvt_pk_bf16_f32 v147, v74, v75\nv_mfma_f32_16x16x32_bf16 v[88:91], v[116:119], a[136:139], v[88:91]\nv_mul_f32 v68, s35, v68\n"
"v_mul_f32 v69, s35, v69\nv_mfma_f32_16x16x32_bf16 v[92:95], v[116:119], a[168:171], v[92:95]\nds_read_b128 v[116:119], v30 offset:49408\nv_mul_f32 v70, s35, v70\n"
"v_mul_f32 v71, s35, v71\ns_waitcnt lgkmcnt(6)\nv_mfma_f32_16x16x32_bf16 v[80:83], v[120:123], a[140:143], v[80:83]\nv_cvt_pk_bf16_f32 v148, v68, v69\n"
"v_cvt_pk_bf16_f32 v149, v70, v71\nv_mfma_f32_16x16x32_bf16 v[84:87], v[120:123], a[172:175], v[84:87]\nds_read_b128 v[120:123], v27 offset:49408\nv_mul_f32 v76, s35, v76\n"
"v_mul_f32 v77, s35, v77\nv_mfma_f32_16x16x32_bf16 v[88:91], v[124:127], a[140:143], v[88:91]\nv_mul_f32 v78, s35, v78\nv_mul_f32 v79, s35, v79\n"
"v_mfma_f32_16x16x32_bf16 v[92:95], v[124:127], a[172:175], v[92:95]\nds_read_b128 v[124:127], v31 offset:49408\nv_cvt_pk_bf16_f32 v150, v76, v77\nv_cvt_pk_bf16_f32 v151, v78, v79\n"
"s_waitcnt vmcnt(8)\ns_barrier\ns_waitcnt lgkmcnt(6)\nv_mfma_f32_16x16x32_bf16 v[80:83], v[96:99], a[144:147], v[80:83]\n"
"s_lshl_b32 s54, s41, 11\ns_add_u32 s54, s54, 0\nv_mfma_f32_16x16x32_bf16 v[84:87], v[96:99], a[176:179], v[84:87]\nds_read_b128 v[96:99], v40 offset:0\n"
"buffer_store_dwordx4 v[144:147], v13, s[16:19], s54 offen sc1\nv_mfma_f32_16x16x32_bf16 v[88:91], v[100:103], a[144:147], v[88:91]\ns_add_u32 s54, s54, 14336\nv_mfma_f32_16x16x32_bf16 v[92:95], v[100:103], a[176:179], v[92:95]\n"
"ds_read_b128 v[100:103], v44 offset:0\nbuffer_store_dwordx4 v[148:151], v13, s[16:19], s54 offen sc1\ns_waitcnt lgkmcnt(6)\nv_mfma_f32_16x16x32_bf16 v[80:83], v[104:107], a[148:151], v[80:83]\n"
"s_lshl_b32 s53, s43, 19\ns_add_u32 s53, s53, s30\nv_mfma_f32_16x16x32_bf16 v[84:87], v[104:107], a[180:183], v[84:87]\nds_read_b128 v[104:107], v41 offset:0\n"
"s_add_u32 m0, s31, 155648\ns_add_u32 s53, s53, 393216\nv_mfma_f32_16x16x32_bf16 v[88:91], v[108:111], a[148:151], v[88:91]\nbuffer_load_dwordx4 v8, s[8:11], s53 offen lds\n"
"v_mfma_f32_16x16x32_bf16 v[92:95], v[108:111], a[180:183], v[92:95]\nds_read_b128 v[108:111], v45 offset:0\nbuffer_load_dwordx4 v9, s[8:11], s53 offen offset:1024 lds\ns_waitcnt lgkmcnt(6)\n"
"v_mfma_f32_16x16x32_bf16 v[80:83], v[112:115], a[152:155], v[80:83]\nv_mfma_f32_16x16x32_bf16 v[84:87], v[112:115], a[184:187], v[84:87]\nds_read_b128 v[112:115], v42 offset:0\nv_mfma_f32_16x16x32_bf16 v[88:91], v[116:119], a[152:155], v[88:91]\n"
"v_mfma_f32_16x16x32_bf16 v[92:95], v[116:119], a[184:187], v[92:95]\nds_read_b128 v[116:119], v46 offset:0\ns_waitcnt lgkmcnt(6)\nv_mfma_f32_16x16x32_bf16 v[80:83], v[120:123], a[156:159], v[80:83]\n"
"v_mfma_f32_16x16x32_bf16 v[84:87], v[120:123], a[188:191], v[84:87]\nds_read_b128 v[120:123], v43 offset:0\nv_mfma_f32_16x16x32_bf16 v[88:91], v[124:127], a[156:159], v[88:91]\nv_mfma_f32_16x16x32_bf16 v[92:95], v[124:127], a[188:191], v[92:95]\n"
"ds_read_b128 v[124:127], v47 offset:0\ns_waitcnt lgkmcnt(6)\nv_mfma_f32_16x16x32_bf16 v[64:67], v[96:99], a[128:131], 0\ns_add_u32 m0, s32, 0\n"
"v_mfma_f32_16x16x32_bf16 v[68:71], v[96:99], a[160:163], 0\nds_read_b128 v[96:99], v40 offset:256\ns_mov_b32 s53, 0\nv_mfma_f32_16x16x32_bf16 v[72:75], v[100:103], a[128:131], 0\n"
"buffer_load_dwordx4 v10, s[12:15], s53 offen lds\nv_mfma_f32_16x16x32_bf16 v[76:79], v[100:103], a[160:163], 0\nds_read_b128 v[100:103], v44 offset:256\nv_mul_f32 v80, s35, v80\n"
"v_mul_f32 v81, s35, v81\ns_waitcnt lgkmcnt(6)\nv_mfma_f32_16x16x32_bf16 v[64:67], v[104:107], a[132:135], v[64:67]\nv_mul_f32 v82, s35, v82\n"
"v_mul_f32 v83, s35, v83\nv_mfma_f32_16x16x32_bf16 v[68:71], v[104:107], a[164:167], v[68:71]\nds_read_b128 v[104:107], v41 offset:256\nv_cvt_pk_bf16_f32 v152, v80, v81\n"
"v_cvt_pk_bf16_f32 v153, v82, v83\nv_mfma_f32_16x16x32_bf16 v[72:75], v[108:111], a[132:135], v[72:75]\nv_mul_f32 v88, s35, v88\nv_mul_f32 v89, s35, v89\n"
"v_mfma_f32_16x16x32_bf16 v[76:79], v[108:111], a[164:167], v[76:79]\nds_read_b128 v[108:111], v45 offset:256\nv_mul_f32 v90, s35, v90\nv_mul_f32 v91, s35, v91\n"
"s_waitcnt lgkmcnt(6)\nv_mfma_f32_16x16x32_bf16 v[64:67], v[112:115], a[136:139], v[64:67]\nv_cvt_pk_bf16_f32 v154, v88, v89\nv_cvt_pk_bf16_f32 v155, v90, v91\n"
"v_mfma_f32_16x16x32_bf16 v[68:71], v[112:115], a[168:171], v[68:71]\nds_read_b128 v[112:115], v42 offset:256\nv_mul_f32 v84, s35, v84\nv_mul_f32 v85, s35, v85\n"
"v_mfma_f32_16x16x32_bf16 v[72:75], v[116:119], a[136:139], v[72:75]\nv_mul_f32 v86, s35, v86\nv_mul_f32 v87, s35, v87\nv_mfma_f32_16x16x32_bf16 v[76:79], v[116:119], a[168:171], v[76:79]\n"
"ds_read_b128 v[116:119], v46 offset:256\nv_cvt_pk_bf16_f32 v156, v84, v85\nv_cvt_pk_bf16_f32 v157, v86, v87\ns_waitcnt lgkmcnt(6)\n"
"v_mfma_f32_16x16x32_bf16 v[64:67], v[120:123], a[140:143], v[64:67]\nv_mul_f32 v92, s35, v92\nv_mul_f32 v93, s35, v93\nv_mfma_f32_16x16x32_bf16 v[68:71], v[120:123], a[172:175], v[68:71]\n"
"ds_read_b128 v[120:123], v43 offset:256\nv_mul_f32 v94, s35, v94\nv_mul_f32 v95, s35, v95\nv_mfma_f32_16x16x32_bf16 v[72:75], v[124:127], a[140:143], v[72:75]\n"
"v_cvt_pk_bf16_f32 v158, v92, v93\nv_cvt_pk_bf16_f32 v159, v94, v95\nv_mfma_f32_16x16x32_bf16 v[76:79], v[124:127], a[172:175], v[76:79]\nds_read_b128 v[124:127], v47 offset:256\n"
"s_lshl_b32 s54, s41, 11\ns_add_u32 s54, s54, 1024\ns_waitcnt lgkmcnt(6)\nv_mfma_f32_16x16x32_bf16 v[64:67], v[96:99], a[144:147], v[64:67]\n"
"buffer_store_dwordx4 v[152:155], v13, s[16:19], s54 offen sc1\nv_mfma_f32_16x16x32_bf16 v[68:71], v[96:99], a[176:179], v[68:71]\nds_read_b128 v[96:99], v40 offset:16384\ns_add_u32 s54, s54, 14336\n"
"v_mfma_f32_16x16x32_bf16 v[72:75], v[100:103], a[144:147], v[72:75]\nbuffer_store_dwordx4 v[156:159], v13, s[16:19], s54 offen sc1\nv_mfma_f32_16x16x32_bf16 v[76:79], v[100:103], a[176:179], v[76:79]\nds_read_b128 v[100:103], v44 offset:16384\n"
"s_add_u32 m0, s32, 4096\ns_waitcnt lgkmcnt(6)\nv_mfma_f32_16x16x32_bf16 v[64:67], v[104:107], a[148:151], v[64:67]\ns_mov_b32 s53, 128\n"
"v_mfma_f32_16x16x32_bf16 v[68:71], v[104:107], a[180:183], v[68:71]\nds_read_b128 v[104:107], v41 offset:16384\nbuffer_load_dwordx4 v10, s[12:15], s53 offen lds\nv_mfma_f32_16x16x32_bf16 v[72:75], v[108:111], a[148:151], v[72:75]\n"
"s_add_u32 m0, s32, 8192\nv_mfma_f32_16x16x32_bf16 v[76:79], v[108:111], a[180:183], v[76:79]\nds_read_b128 v[108:111], v45 offset:16384\ns_mov_b32 s53, 256\n"
"s_waitcnt lgkmcnt(6)\nv_mfma_f32_16x16x32_bf16 v[64:67], v[112:115], a[152:155], v[64:67]\nbuffer_load_dwordx4 v10, s[12:15], s53 offen lds\nv_mfma_f32_16x16x32_bf16 v[68:71], v[112:115], a[184:187], v[68:71]\n"
"ds_read_b128 v[112:115], v42 offset:16384\ns_add_u32 m0, s32, 12288\nv_mfma_f32_16x16x32_bf16 v[72:75], v[116:119], a[152:155], v[72:75]\ns_mov_b32 s53, 384\n"
"v_mfma_f32_16x16x32_bf16 v[76:79], v[116:119], a[184:187], v[76:79]\nds_read_b128 v[116:119], v46 offset:16384\nbuffer_load_dwordx4 v10, s[12:15], s53 offen lds\ns_waitcnt lgkmcnt(6)\n"
"v_mfma_f32_16x16x32_bf16 v[64:67], v[120:123], a[156:159], v[64:67]\nv_mfma_f32_16x16x32_bf16 v[68:71], v[120:123], a[188:191], v[68:71]\nds_read_b128 v[120:123], v43 offset:16384\nv_mfma_f32_16x16x32_bf16 v[72:75], v[124:127], a[156:159], v[72:75]\n"
"v_mfma_f32_16x16x32_bf16 v[76:79], v[124:127], a[188:191], v[76:79]\nds_read_b128 v[124:127], v47 offset:16384\ns_waitcnt lgkmcnt(6)\nv_mfma_f32_16x16x32_bf16 v[80:83], v[96:99], a[128:131], 0\n"
"s_add_u32 m0, s32, 16384\nv_mfma_f32_16x16x32_bf16 v[84:87], v[96:99], a[160:163], 0\nds_read_b128 v[96:99], v40 offset:16640\ns_mov_b32 s53, 512\n"
"v_mfma_f32_16x16x32_bf16 v[88:91], v[100:103], a[128:131], 0\nbuffer_load_dwordx4 v10, s[12:15], s53 offen lds\nv_mfma_f32_16x16x32_bf16 v[92:95], v[100:103], a[160:163], 0\nds_read_b128 v[100:103], v44 offset:16640\n"
"v_mul_f32 v64, s35, v64\nv_mul_f32 v65, s35, v65\ns_waitcnt lgkmcnt(6)\nv_mfma_f32_16x16x32_bf16 v[80:83], v[104:107], a[132:135], v[80:83]\n"
"v_mul_f32 v66, s35, v66\nv_mul_f32 v67, s35, v67\nv_mfma_f32_16x16x32_bf16 v[84:87], v[104:107], a[164:167], v[84:87]\nds_read_b128 v[104:107], v41 offset:16640\n"
"v_cvt_pk_bf16_f32 v160, v64, v65\nv_cvt_pk_bf16_f32 v161, v66, v67\nv_mfma_f32_16x16x32_bf16 v[88:91], v[108:111], a[132:135], v[88:91]\nv_mul_f32 v72, s35, v72\n"
"v_mul_f32 v73, s35, v73\nv_mfma_f32_16x16x32_bf16 v[92:95], v[108:111], a[164:167], v[92:95]\nds_read_b128 v[108:111], v45 offset:16640\nv_mul_f32 v74, s35, v74\n"
"v_mul_f32 v75, s35, v75\ns_waitcnt lgkmcnt(6)\nv_mfma_f32_16x16x32_bf16 v[80:83], v[112:115], a[136:139], v[80:83]\nv_cvt_pk_bf16_f32 v162, v72, v73\n"
"v_cvt_pk_bf16_f32 v163, v74, v75\nv_mfma_f32_16x16x32_bf16 v[84:87], v[112:115], a[168:171], v[84:87]\nds_read_b128 v[112:115], v42 offset:16640\nv_mul_f32 v68, s35, v68\n"
"v_mul_f32 v69, s35, v69\nv_mfma_f32_16x16x32_bf16 v[88:91], v[116:119], a[136:139], v[88:91]\nv_mul_f32 v70, s35, v70\nv_mul_f32 v71, s35, v71\n"
"v_mfma_f32_16x16x32_bf16 v[92:95], v[116:119], a[168:171], v[92:95]\nds_read_b128 v[116:119], v46 offset:16640\nv_cvt_pk_bf16_f32 v164, v68, v69\nv_cvt_pk_bf16_f32 v165, v70, v71\n"
"s_waitcnt lgkmcnt(6)\nv_mfma_f32_16x16x32_bf16 v[80:83], v[120:123], a[140:143], v[80:83]\nv_mul_f32 v76, s35, v76\nv_mul_f32 v77, s35, v77\n"
"v_mfma_f32_16x16x32_bf16 v[84:87], v[120:123], a[172:175], v[84:87]\nds_read_b128 v[120:123], v43 offset:16640\nv_mul_f32 v78, s35, v78\nv_mul_f32 v79, s35, v79\n"
"v_mfma_f32_16x16x32_bf16 v[88:91], v[124:127], a[140:143], v[88:91]\nv_cvt_pk_bf16_f32 v166, v76, v77\nv_cvt_pk_bf16_f32 v167, v78, v79\nv_mfma_f32_16x16x32_bf16 v[92:95], v[124:127], a[172:175], v[92:95]\n"
"ds_read_b128 v[124:127], v47 offset:16640\ns_lshl_b32 s54, s42, 11\ns_add_u32 s54, s54, 0\ns_waitcnt vmcnt(7)\n"
"s_barrier\ns_waitcnt lgkmcnt(6)\nv_mfma_f32_16x16x32_bf16 v[80:83], v[96:99], a[144:147], v[80:83]\nbuffer_store_dwordx4 v[160:163], v13, s[16:19], s54 offen sc1\n"
"v_mfma_f32_16x16x32_bf16 v[84:87], v[96:99], a[176:179], v[84:87]\nds_read_b128 v[96:99], v40 offset:32768\ns_add_u32 s54, s54, 14336\nv_mfma_f32_16x16x32_bf16 v[88:91], v[100:103], a[144:147], v[88:91]\n"
"buffer_store_dwordx4 v[164:167], v13, s[16:19], s54 offen sc1\nv_mfma_f32_16x16x32_bf16 v[92:95], v[100:103], a[176:179], v[92:95]\nds_read_b128 v[100:103], v44 offset:32768\ns_add_u32 m0, s32, 20480\n"
"s_waitcnt lgkmcnt(6)\nv_mfma_f32_16x16x32_bf16 v[80:83], v[104:107], a[148:151], v[80:83]\ns_mov_b32 s53, 640\nv_mfma_f32_16x16x32_bf16 v[84:87], v[104:107], a[180:183], v[84:87]\n"
"ds_read_b128 v[104:107], v41 offset:32768\nbuffer_load_dwordx4 v10, s[12:15], s53 offen lds\nv_mfma_f32_16x16x32_bf16 v[88:91], v[108:111], a[148:151], v[88:91]\ns_add_u32 m0, s32, 24576\n"
"v_mfma_f32_16x16x32_bf16 v[92:95], v[108:111], a[180:183], v[92:95]\nds_read_b128 v[108:111], v45 offset:32768\ns_mov_b32 s53, 768\ns_waitcnt lgkmcnt(6)\n"
"v_mfma_f32_16x16x32_bf16 v[80:83], v[112:115], a[152:155], v[80:83]\nbuffer_load_dwordx4 v10, s[12:15], s53 offen lds\nv_mfma_f32_16x16x32_bf16 v[84:87], v[112:115], a[184:187], v[84:87]\nds_read_b128 v[112:115], v42 offset:32768\n"
"v_mfma_f32_16x16x32_bf16 v[88:91], v[116:119], a[152:155], v[88:91]\nv_mfma_f32_16x16x32_bf16 v[92:95], v[116:119], a[184:187], v[92:95]\nds_read_b128 v[116:119], v46 offset:32768\ns_waitcnt lgkmcnt(6)\n"
"v_mfma_f32_16x16x32_bf16 v[80:83], v[120:123], a[156:159], v[80:83]\nv_mfma_f32_16x16x32_bf16 v[84:87], v[120:123], a[188:191], v[84:87]\nds_read_b128 v[120:123], v43 offset:32768\nv_mfma_f32_16x16x32_bf16 v[88:91], v[124:127], a[156:159], v[88:91]\n"
"v_mfma_f32_16x16x32_bf16 v[92:95], v[124:127], a[188:191], v[92:95]\nds_read_b128 v[124:127], v47 offset:32768\ns_cmp_lg_u32 s34, 2\ns_cbranch_scc1 fg0_main\n"
"s_nop 7\nv_mul_f32 v80, s35, v80\nv_mul_f32 v81, s35, v81\nv_mul_f32 v82, s35, v82\n"
"v_mul_f32 v83, s35, v83\nv_cvt_pk_bf16_f32 v168, v80, v81\nv_cvt_pk_bf16_f32 v169, v82, v83\nv_mul_f32 v88, s35, v88\n"
"v_mul_f32 v89, s35, v89\nv_mul_f32 v90, s35, v90\nv_mul_f32 v91, s35, v91\nv_cvt_pk_bf16_f32 v170, v88, v89\n"
"v_cvt_pk_bf16_f32 v171, v90, v91\nv_mul_f32 v84, s35, v84\nv_mul_f32 v85, s35, v85\nv_mul_f32 v86, s35, v86\n"
"v_mul_f32 v87, s35, v87\nv_cvt_pk_bf16_f32 v172, v84, v85\nv_cvt_pk_bf16_f32 v173, v86, v87\nv_mul_f32 v92, s35, v92\n"
"v_mul_f32 v93, s35, v93\nv_mul_f32 v94, s35, v94\nv_mul_f32 v95, s35, v95\nv_cvt_pk_bf16_f32 v174, v92, v93\n"
"v_cvt_pk_bf16_f32 v175, v94, v95\ns_lshl_b32 s54, s42, 11\ns_add_u32 s54, s54, 1024\nbuffer_store_dwordx4 v[168:171], v13, s[16:19], s54 offen sc1\n"
"s_add_u32 s54, s54, 14336\nbuffer_store_dwordx4 v[172:175], v13, s[16:19], s54 offen sc1\nv_mov_b32 v36, s28\ns_waitcnt vmcnt(0) lgkmcnt(0)\n"
"s_barrier\nbuffer_store_dword v36, v19, s[24:27], 0 offen sc1\ns_branch fg0_end\nfg0_main:\n"
"s_waitcnt lgkmcnt(6)\nv_mfma_f32_16x16x32_bf16 v[64:67], v[96:99], a[128:131], 0\ns_add_u32 m0, s32, 32768\nv_mfma_f32_16x16x32_bf16 v[68:71], v[96:99], a[160:163], 0\n"
"ds_read_b128 v[96:99], v40 offset:33024\ns_mov_b32 s53, 28672\nv_mfma_f32_16x16x32_bf16 v[72:75], v[100:103], a[128:131], 0\nbuffer_load_dwordx4 v10, s[12:15], s53 offen lds\n"
"v_mfma_f32_16x16x32_bf16 v[76:79], v[100:103], a[160:163], 0\nds_read_b128 v[100:103], v44 offset:33024\nv_mul_f32 v80, s35, v80\nv_mul_f32 v81, s35, v81\n"
"s_waitcnt lgkmcnt(6)\nv_mfma_f32_16x16x32_bf16 v[64:67], v[104:107], a[132:135], v[64:67]\nv_mul_f32 v82, s35, v82\nv_mul_f32 v83, s35, v83\n"
"v_mfma_f32_16x16x32_bf16 v[68:71], v[104:107], a[164:167], v[68:71]\nds_read_b128 v[104:107], v41 offset:33024\nv_cvt_pk_bf16_f32 v168, v80, v81\nv_cvt_pk_bf16_f32 v169, v82, v83\n"
"v_mfma_f32_16x16x32_bf16 v[72:75], v[108:111], a[132:135], v[72:75]\nv_mul_f32 v88, s35, v88\nv_mul_f32 v89, s35, v89\nv_mfma_f32_16x16x32_bf16 v[76:79], v[108:111], a[164:167], v[76:79]\n"
"ds_read_b128 v[108:111], v45 offset:33024\nv_mul_f32 v90, s35, v90\nv_mul_f32 v91, s35, v91\ns_waitcnt lgkmcnt(6)\n"
"v_mfma_f32_16x16x32_bf16 v[64:67], v[112:115], a[136:139], v[64:67]\nv_cvt_pk_bf16_f32 v170, v88, v89\nv_cvt_pk_bf16_f32 v171, v90, v91\nv_mfma_f32_16x16x32_bf16 v[68:71], v[112:115], a[168:171], v[68:71]\n"
"ds_read_b128 v[112:115], v42 offset:33024\nv_mul_f32 v84, s35, v84\nv_mul_f32 v85, s35, v85\nv_mfma_f32_16x16x32_bf16 v[72:75], v[116:119], a[136:139], v[72:75]\n"
"v_mul_f32 v86, s35, v86\nv_mul_f32 v87, s35, v87\nv_mfma_f32_16x16x32_bf16 v[76:79], v[116:119], a[168:171], v[76:79]\nds_read_b128 v[116:119], v46 offset:33024\n"
"v_cvt_pk_bf16_f32 v172, v84, v85\nv_cvt_pk_bf16_f32 v173, v86, v87\ns_waitcnt lgkmcnt(6)\nv_mfma_f32_16x16x32_bf16 v[64:67], v[120:123], a[140:143], v[64:67]\n"
"v_mul_f32 v92, s35, v92\nv_mul_f32 v93, s35, v93\nv_mfma_f32_16x16x32_bf16 v[68:71], v[120:123], a[172:175], v[68:71]\nds_read_b128 v[120:123], v43 offset:33024\n"
"v_mul_f32 v94, s35, v94\nv_mul_f32 v95, s35, v95\nv_mfma_f32_16x16x32_bf16 v[72:75], v[124:127], a[140:143], v[72:75]\nv_cvt_pk_bf16_f32 v174, v92, v93\n"
"v_cvt_pk_bf16_f32 v175, v94, v95\nv_mfma_f32_16x16x32_bf16 v[76:79], v[124:127], a[172:175], v[76:79]\nds_read_b128 v[124:127], v47 offset:33024\ns_lshl_b32 s54, s42, 11\n"
"s_add_u32 s54, s54, 1024\ns_waitcnt lgkmcnt(6)\nv_mfma_f32_16x16x32_bf16 v[64:67], v[96:99], a[144:147], v[64:67]\nbuffer_store_dwordx4 v[168:171], v13, s[16:19], s54 offen sc1\n"
"v_mfma_f32_16x16x32_bf16 v[68:71], v[96:99], a[176:179], v[68:71]\nds_read_b128 v[96:99], v40 offset:49152\ns_add_u32 s54, s54, 14336\nv_mfma_f32_16x16x32_bf16 v[72:75], v[100:103], a[144:147], v[72:75]\n"
"buffer_store_dwordx4 v[172:175], v13, s[16:19], s54 offen sc1\nv_mfma_f32_16x16x32_bf16 v[76:79], v[100:103], a[176:179], v[76:79]\nds_read_b128 v[100:103], v44 offset:49152\ns_add_u32 m0, s32, 36864\n"
"s_waitcnt lgkmcnt(6)\nv_mfma_f32_16x16x32_bf16 v[64:67], v[104:107], a[148:151], v[64:67]\ns_mov_b32 s53, 28800\nv_mfma_f32_16x16x32_bf16 v[68:71], v[104:107], a[180:183], v[68:71]\n"
"ds_read_b128 v[104:107], v41 offset:49152\nbuffer_load_dwordx4 v10, s[12:15], s53 offen lds\nv_mfma_f32_16x16x32_bf16 v[72:75], v[108:111], a[148:151], v[72:75]\ns_add_u32 m0, s32, 40960\n"
"v_mfma_f32_16x16x32_bf16 v[76:79], v[108:111], a[180:183], v[76:79]\nds_read_b128 v[108:111], v45 offset:49152\ns_mov_b32 s53, 28928\ns_waitcnt lgkmcnt(6)\n"
"v_mfma_f32_16x16x32_bf16 v[64:67], v[112:115], a[152:155], v[64:67]\nbuffer_load_dwordx4 v10, s[12:15], s53 offen lds\nv_mfma_f32_16x16x32_bf16 v[68:71], v[112:115], a[184:187], v[68:71]\nds_read_b128 v[112:115], v42 offset:49152\n"
"s_add_u32 m0, s32, 45056\nv_mfma_f32_16x16x32_bf16 v[72:75], v[116:119], a[152:155], v[72:75]\ns_mov_b32 s53, 29056\nv_mfma_f32_16x16x32_bf16 v[76:79], v[116:119], a[184:187], v[76:79]\n"
"ds_read_b128 v[116:119], v46 offset:49152\nbuffer_load_dwordx4 v10, s[12:15], s53 offen lds\ns_waitcnt lgkmcnt(6)\nv_mfma_f32_16x16x32_bf16 v[64:67], v[120:123], a[156:159], v[64:67]\n"
"v_mfma_f32_16x16x32_bf16 v[68:71], v[120:123], a[188:191], v[68:71]\nds_read_b128 v[120:123], v43 offset:49152\nv_mfma_f32_16x16x32_bf16 v[72:75], v[124:127], a[156:159], v[72:75]\nv_mfma_f32_16x16x32_bf16 v[76:79], v[124:127], a[188:191], v[76:79]\n"
"ds_read_b128 v[124:127], v47 offset:49152\ns_waitcnt lgkmcnt(6)\nv_mfma_f32_16x16x32_bf16 v[80:83], v[96:99], a[128:131], 0\ns_add_u32 m0, s32, 49152\n"
"v_mfma_f32_16x16x32_bf16 v[84:87], v[96:99], a[160:163], 0\nds_read_b128 v[96:99], v40 offset:49408\ns_mov_b32 s53, 29184\nv_mfma_f32_16x16x32_bf16 v[88:91], v[100:103], a[128:131], 0\n"
"buffer_load_dwordx4 v10, s[12:15], s53 offen lds\nv_mfma_f32_16x16x32_bf16 v[92:95], v[100:103], a[160:163], 0\nds_read_b128 v[100:103], v44 offset:49408\nv_mul_f32 v64, s35, v64\n"
"v_mul_f32 v65, s35, v65\ns_waitcnt lgkmcnt(6)\nv_mfma_f32_16x16x32_bf16 v[80:83], v[104:107], a[132:135], v[80:83]\nv_mul_f32 v66, s35, v66\n"
"v_mul_f32 v67, s35, v67\nv_mfma_f32_16x16x32_bf16 v[84:87], v[104:107], a[164:167], v[84:87]\nds_read_b128 v[104:107], v41 offset:49408\nv_cvt_pk_bf16_f32 v176, v64, v65\n"
"v_cvt_pk_bf16_f32 v177, v66, v67\nv_mfma_f32_16x16x32_bf16 v[88:91], v[108:111], a[132:135], v[88:91]\nv_mul_f32 v72, s35, v72\nv_mul_f32 v73, s35, v73\n"
"v_mfma_f32_16x16x32_bf16 v[92:95], v[108:111], a[164:167], v[92:95]\nds_read_b128 v[108:111], v45 offset:49408\nv_mul_f32 v74, s35, v74\nv_mul_f32 v75, s35, v75\n"
"s_waitcnt lgkmcnt(6)\nv_mfma_f32_16x16x32_bf16 v[80:83], v[112:115], a[136:139], v[80:83]\nv_cvt_pk_bf16_f32 v178, v72, v73\nv_cvt_pk_bf16_f32 v179, v74, v75\n"
"v_mfma_f32_16x16x32_bf16 v[84:87], v[112:115], a[168:171], v[84:87]\nds_read_b128 v[112:115], v42 offset:49408\nv_mul_f32 v68, s35, v68\nv_mul_f32 v69, s35, v69\n"
"v_mfma_f32_16x16x32_bf16 v[88:91], v[116:119], a[136:139], v[88:91]\nv_mul_f32 v70, s35, v70\nv_mul_f32 v71, s35, v71\nv_mfma_f32_16x16x32_bf16 v[92:95], v[116:119], a[168:171], v[92:95]\n"
"ds_read_b128 v[116:119], v46 offset:49408\nv_cvt_pk_bf16_f32 v180, v68, v69\nv_cvt_pk_bf16_f32 v181, v70, v71\ns_waitcnt lgkmcnt(6)\n"
"v_mfma_f32_16x16x32_bf16 v[80:83], v[120:123], a[140:143], v[80:83]\nv_mul_f32 v76, s35, v76\nv_mul_f32 v77, s35, v77\nv_mfma_f32_16x16x32_bf16 v[84:87], v[120:123], a[172:175], v[84:87]\n"
"ds_read_b128 v[120:123], v43 offset:49408\nv_mul_f32 v78, s35, v78\nv_mul_f32 v79, s35, v79\nv_mfma_f32_16x16x32_bf16 v[88:91], v[124:127], a[140:143], v[88:91]\n"
"v_cvt_pk_bf16_f32 v182, v76, v77\nv_cvt_pk_bf16_f32 v183, v78, v79\nv_mfma_f32_16x16x32_bf16 v[92:95], v[124:127], a[172:175], v[92:95]\nds_read_b128 v[124:127], v47 offset:49408\n"
"s_lshl_b32 s54, s43, 11\ns_add_u32 s54, s54, 0\ns_waitcnt lgkmcnt(6)\nv_mfma_f32_16x16x32_bf16 v[80:83], v[96:99], a[144:147], v[80:83]\n"
"buffer_store_dwordx4 v[176:179], v13, s[16:19], s54 offen sc1\nv_mfma_f32_16x16x32_bf16 v[84:87], v[96:99], a[176:179], v[84:87]\ns_add_u32 s54, s54, 14336\nv_mfma_f32_16x16x32_bf16 v[88:91], v[100:103], a[144:147], v[88:91]\n"
"buffer_store_dwordx4 v[180:183], v13, s[16:19], s54 offen sc1\nv_mfma_f32_16x16x32_bf16 v[92:95], v[100:103], a[176:179], v[92:95]\ns_add_u32 m0, s32, 53248\ns_waitcnt lgkmcnt(4)\n"
"v_mfma_f32_16x16x32_bf16 v[80:83], v[104:107], a[148:151], v[80:83]\ns_mov_b32 s53, 29312\nv_mfma_f32_16x16x32_bf16 v[84:87], v[104:107], a[180:183], v[84:87]\nbuffer_load_dwordx4 v10, s[12:15], s53 offen lds\n"
"v_mfma_f32_16x16x32_bf16 v[88:91], v[108:111], a[148:151], v[88:91]\ns_add_u32 m0, s32, 57344\nv_mfma_f32_16x16x32_bf16 v[92:95], v[108:111], a[180:183], v[92:95]\ns_mov_b32 s53, 29440\n"
"s_waitcnt lgkmcnt(2)\nv_mfma_f32_16x16x32_bf16 v[80:83], v[112:115], a[152:155], v[80:83]\nbuffer_load_dwordx4 v10, s[12:15], s53 offen lds\nv_mfma_f32_16x16x32_bf16 v[84:87], v[112:115], a[184:187], v[84:87]\n"
"buffer_load_dword v37, v21, s[24:27], 0 offen sc1\nv_mfma_f32_16x16x32_bf16 v[88:91], v[116:119], a[152:155], v[88:91]\nv_mfma_f32_16x16x32_bf16 v[92:95], v[116:119], a[184:187], v[92:95]\ns_waitcnt lgkmcnt(0)\n"
"v_mfma_f32_16x16x32_bf16 v[80:83], v[120:123], a[156:159], v[80:83]\nv_mfma_f32_16x16x32_bf16 v[84:87], v[120:123], a[188:191], v[84:87]\nv_mfma_f32_16x16x32_bf16 v[88:91], v[124:127], a[156:159], v[88:91]\nv_mfma_f32_16x16x32_bf16 v[92:95], v[124:127], a[188:191], v[92:95]\n"
"s_nop 7\nv_mul_f32 v80, s35, v80\nv_mul_f32 v81, s35, v81\nv_mul_f32 v82, s35, v82\n"
"v_mul_f32 v83, s35, v83\nv_cvt_pk_bf16_f32 v184, v80, v81\nv_cvt_pk_bf16_f32 v185, v82, v83\nv_mul_f32 v88, s35, v88\n"
"v_mul_f32 v89, s35, v89\nv_mul_f32 v90, s35, v90\nv_mul_f32 v91, s35, v91\nv_cvt_pk_bf16_f32 v186, v88, v89\n"
"v_cvt_pk_bf16_f32 v187, v90, v91\nv_mul_f32 v84, s35, v84\nv_mul_f32 v85, s35, v85\nv_mul_f32 v86, s35, v86\n"
"v_mul_f32 v87, s35, v87\nv_cvt_pk_bf16_f32 v188, v84, v85\nv_cvt_pk_bf16_f32 v189, v86, v87\nv_mul_f32 v92, s35, v92\n"
"v_mul_f32 v93, s35, v93\nv_mul_f32 v94, s35, v94\nv_mul_f32 v95, s35, v95\nv_cvt_pk_bf16_f32 v190, v92, v93\n"
"v_cvt_pk_bf16_f32 v191, v94, v95\ns_lshl_b32 s54, s43, 11\ns_add_u32 s54, s54, 1024\nbuffer_store_dwordx4 v[184:187], v13, s[16:19], s54 offen sc1\n"
"s_add_u32 s54, s54, 14336\nbuffer_store_dwordx4 v[188:191], v13, s[16:19], s54 offen sc1\ns_waitcnt vmcnt(2)\nv_mov_b32 v36, s28\n"
"v_cmp_ne_u32 vcc, s28, v37\ns_cbranch_vccz fg0_e1\ns_waitcnt vmcnt(0)\ns_barrier\n"
"buffer_store_dword v36, v19, s[24:27], 0 offen sc1\nfg0_po:\nv_cmp_ne_u32 vcc, s28, v37\ns_cbranch_vccz fg0_pook\n"
"s_add_u32 s55, s55, 1\ns_cmp_gt_u32 s55, 262144\ns_cbranch_scc0 fg0_po2\ns_endpgm\n"
"fg0_po2:\ns_sleep 1\nbuffer_load_dword v37, v21, s[24:27], 0 offen sc1\ns_waitcnt vmcnt(0)\n"
"s_branch fg0_po\nfg0_pook:\ns_mov_b32 s53, 8192\nbuffer_load_dwordx4 v[192:195], v13, s[16:19], s53 offen sc1\n"
"s_mov_b32 s53, 22528\nbuffer_load_dwordx4 v[196:199], v13, s[16:19], s53 offen sc1\ns_mov_b32 s53, 9216\nbuffer_load_dwordx4 v[200:203], v13, s[16:19], s53 offen sc1\n"
"s_mov_b32 s53, 23552\nbuffer_load_dwordx4 v[204:207], v13, s[16:19], s53 offen sc1\ns_mov_b32 s53, 10240\nbuffer_load_dwordx4 v[208:211], v13, s[16:19], s53 offen sc1\n"
"s_mov_b32 s53, 24576\nbuffer_load_dwordx4 v[212:215], v13, s[16:19], s53 offen sc1\ns_mov_b32 s53, 11264\nbuffer_load_dwordx4 v[216:219], v13, s[16:19], s53 offen sc1\n"
"s_mov_b32 s53, 25600\nbuffer_load_dwordx4 v[220:223], v13, s[16:19], s53 offen sc1\ns_mov_b32 s53, 12288\nbuffer_load_dwordx4 v[224:227], v13, s[16:19], s53 offen sc1\n"
"s_mov_b32 s53, 26624\nbuffer_load_dwordx4 v[228:231], v13, s[16:19], s53 offen sc1\ns_mov_b32 s53, 13312\nbuffer_load_dwordx4 v[232:235], v13, s[16:19], s53 offen sc1\n"
"s_mov_b32 s53, 27648\nbuffer_load_dwordx4 v[236:239], v13, s[16:19], s53 offen sc1\nbuffer_store_dword v36, v19, s[24:27], 0 offen sc1\ns_branch fg0_e2\n"
"fg0_e1:\ns_mov_b32 s53, 8192\nbuffer_load_dwordx4 v[192:195], v13, s[16:19], s53 offen sc1\ns_mov_b32 s53, 22528\n"
"buffer_load_dwordx4 v[196:199], v13, s[16:19], s53 offen sc1\ns_mov_b32 s53, 9216\nbuffer_load_dwordx4 v[200:203], v13, s[16:19], s53 offen sc1\ns_mov_b32 s53, 23552\n"
"buffer_load_dwordx4 v[204:207], v13, s[16:19], s53 offen sc1\ns_mov_b32 s53, 10240\nbuffer_load_dwordx4 v[208:211], v13, s[16:19], s53 offen sc1\ns_mov_b32 s53, 24576\n"
"buffer_load_dwordx4 v[212:215], v13, s[16:19], s53 offen sc1\ns_mov_b32 s53, 11264\nbuffer_load_dwordx4 v[216:219], v13, s[16:19], s53 offen sc1\ns_mov_b32 s53, 25600\n"
"buffer_load_dwordx4 v[220:223], v13, s[16:19], s53 offen sc1\ns_mov_b32 s53, 12288\nbuffer_load_dwordx4 v[224:227], v13, s[16:19], s53 offen sc1\ns_mov_b32 s53, 26624\n"
"buffer_load_dwordx4 v[228:231], v13, s[16:19], s53 offen sc1\ns_mov_b32 s53, 13312\nbuffer_load_dwordx4 v[232:235], v13, s[16:19], s53 offen sc1\ns_mov_b32 s53, 27648\n"
"buffer_load_dwordx4 v[236:239], v13, s[16:19], s53 offen sc1\ns_waitcnt vmcnt(12)\ns_barrier\nbuffer_store_dword v36, v19, s[24:27], 0 offen sc1\n"
"fg0_e2:\ns_add_u32 m0, s32, 65536\ns_mov_b32 s53, 57344\nbuffer_load_dwordx4 v10, s[12:15], s53 offen lds\n"
"s_add_u32 m0, s32, 69632\ns_mov_b32 s53, 57472\nbuffer_load_dwordx4 v10, s[12:15], s53 offen lds\ns_add_u32 m0, s32, 73728\n"
"s_mov_b32 s53, 57600\nbuffer_load_dwordx4 v10, s[12:15], s53 offen lds\ns_add_u32 m0, s32, 77824\ns_mov_b32 s53, 57728\n"
"buffer_load_dwordx4 v10, s[12:15], s53 offen lds\ns_add_u32 m0, s32, 81920\ns_mov_b32 s53, 57856\nbuffer_load_dwordx4 v10, s[12:15], s53 offen lds\n"
"s_add_u32 m0, s32, 86016\ns_mov_b32 s53, 57984\nbuffer_load_dwordx4 v10, s[12:15], s53 offen lds\ns_add_u32 m0, s32, 90112\n"
"s_mov_b32 s53, 58112\nbuffer_load_dwordx4 v10, s[12:15], s53 offen lds\ns_sub_u32 s48, s28, 1\ns_mov_b32 s49, 86016\n"
"s_mov_b32 s50, 0\ns_mov_b32 s51, 0\ns_add_u32 s52, s32, 98304\ns_add_u32 s47, s32, 131072\n"
"ds_read_b128 v[40:43], v32 offset:0\nds_read_b128 v[44:47], v33 offset:0\nds_read_b128 v[48:51], v34 offset:0\nds_read_b128 v[52:55], v35 offset:0\n"
"s_barrier\ns_waitcnt lgkmcnt(3)\nv_mfma_f32_16x16x32_bf16 v[64:67], v[128:131], v[40:43], 0\nv_mov_b32 v36, s48\n"
"s_add_u32 s48, s48, 1\ns_nop 0\nbuffer_store_dword v36, v19, s[24:27], s33 offen sc1\nv_mfma_f32_16x16x32_bf16 v[68:71], v[132:135], v[40:43], 0\n"
"ds_read_b128 v[40:43], v32 offset:4096\ns_waitcnt lgkmcnt(3)\nv_mfma_f32_16x16x32_bf16 v[72:75], v[128:131], v[44:47], 0\nv_mfma_f32_16x16x32_bf16 v[76:79], v[132:135], v[44:47], 0\n"
"ds_read_b128 v[44:47], v33 offset:4096\ns_waitcnt lgkmcnt(3)\nv_mfma_f32_16x16x32_bf16 v[64:67], v[136:139], v[48:51], v[64:67]\ns_add_u32 m0, s52, 0\n"
"v_mfma_f32_16x16x32_bf16 v[68:71], v[140:143], v[48:51], v[68:71]\nds_read_b128 v[48:51], v34 offset:4096\ns_add_u32 s53, s49, 0\ns_waitcnt lgkmcnt(3)\n"
"v_mfma_f32_16x16x32_bf16 v[72:75], v[136:139], v[52:55], v[72:75]\nbuffer_load_dwordx4 v10, s[12:15], s53 offen lds\nv_mfma_f32_16x16x32_bf16 v[76:79], v[140:143], v[52:55], v[76:79]\nds_read_b128 v[52:55], v35 offset:4096\n"
"s_add_u32 m0, s52, 4096\ns_waitcnt lgkmcnt(3)\nv_mfma_f32_16x16x32_bf16 v[64:67], v[144:147], v[40:43], v[64:67]\ns_add_u32 s53, s49, 128\n"
"v_mfma_f32_16x16x32_bf16 v[68:71], v[148:151], v[40:43], v[68:71]\nds_read_b128 v[40:43], v32 offset:8192\nbuffer_load_dwordx4 v10, s[12:15], s53 offen lds\ns_waitcnt lgkmcnt(3)\n"
"v_mfma_f32_16x16x32_bf16 v[72:75], v[144:147], v[44:47], v[72:75]\ns_add_u32 m0, s52, 8192\nv_mfma_f32_16x16x32_bf16 v[76:79], v[148:151], v[44:47], v[76:79]\nds_read_b128 v[44:47], v33 offset:8192\n"
"s_add_u32 s53, s49, 256\ns_waitcnt lgkmcnt(3)\nv_mfma_f32_16x16x32_bf16 v[64:67], v[152:155], v[48:51], v[64:67]\nbuffer_load_dwordx4 v10, s[12:15], s53 offen lds\n"
"v_mfma_f32_16x16x32_bf16 v[68:71], v[156:159], v[48:51], v[68:71]\nds_read_b128 v[48:51], v34 offset:8192\ns_add_u32 m0, s52, 12288\ns_waitcnt lgkmcnt(3)\n"
"v_mfma_f32_16x16x32_bf16 v[72:75], v[152:155], v[52:55], v[72:75]\ns_add_u32 s53, s49, 384\nv_mfma_f32_16x16x32_bf16 v[76:79], v[156:159], v[52:55], v[76:79]\nds_read_b128 v[52:55], v35 offset:8192\n"
"buffer_load_dwordx4 v10, s[12:15], s53 offen lds\ns_waitcnt lgkmcnt(3)\nv_mfma_f32_16x16x32_bf16 v[64:67], v[160:163], v[40:43], v[64:67]\ns_add_u32 m0, s52, 16384\n"
"v_mfma_f32_16x16x32_bf16 v[68:71], v[164:167], v[40:43], v[68:71]\nds_read_b128 v[40:43], v32 offset:12288\ns_add_u32 s53, s49, 512\ns_waitcnt lgkmcnt(3)\n"
"v_mfma_f32_16x16x32_bf16 v[72:75], v[160:163], v[44:47], v[72:75]\nbuffer_load_dwordx4 v10, s[12:15], s53 offen lds\nv_mfma_f32_16x16x32_bf16 v[76:79], v[164:167], v[44:47], v[76:79]\nds_read_b128 v[44:47], v33 offset:12288\n"
"s_add_u32 m0, s52, 20480\ns_waitcnt lgkmcnt(3)\nv_mfma_f32_16x16x32_bf16 v[64:67], v[168:171], v[48:51], v[64:67]\ns_add_u32 s53, s49, 640\n"
"v_mfma_f32_16x16x32_bf16 v[68:71], v[172:175], v[48:51], v[68:71]\nds_read_b128 v[48:51], v34 offset:12288\nbuffer_load_dwordx4 v10, s[12:15], s53 offen lds\ns_waitcnt lgkmcnt(3)\n"
"v_mfma_f32_16x16x32_bf16 v[72:75], v[168:171], v[52:55], v[72:75]\ns_add_u32 m0, s52, 24576\nv_mfma_f32_16x16x32_bf16 v[76:79], v[172:175], v[52:55], v[76:79]\nds_read_b128 v[52:55], v35 offset:12288\n"
"s_add_u32 s53, s49, 768\ns_waitcnt lgkmcnt(3)\nv_mfma_f32_16x16x32_bf16 v[64:67], v[176:179], v[40:43], v[64:67]\nbuffer_load_dwordx4 v10, s[12:15], s53 offen lds\n"
"v_mfma_f32_16x16x32_bf16 v[68:71], v[180:183], v[40:43], v[68:71]\nds_read_b128 v[40:43], v32 offset:16384\ns_waitcnt lgkmcnt(3)\nv_mfma_f32_16x16x32_bf16 v[72:75], v[176:179], v[44:47], v[72:75]\n"
"v_mfma_f32_16x16x32_bf16 v[76:79], v[180:183], v[44:47], v[76:79]\nds_read_b128 v[44:47], v33 offset:16384\ns_waitcnt lgkmcnt(3)\nv_mfma_f32_16x16x32_bf16 v[64:67], v[184:187], v[48:51], v[64:67]\n"
"v_mfma_f32_16x16x32_bf16 v[68:71], v[188:191], v[48:51], v[68:71]\nds_read_b128 v[48:51], v34 offset:16384\ns_waitcnt lgkmcnt(3)\nv_mfma_f32_16x16x32_bf16 v[72:75], v[184:187], v[52:55], v[72:75]\n"
"v_mfma_f32_16x16x32_bf16 v[76:79], v[188:191], v[52:55], v[76:79]\nds_read_b128 v[52:55], v35 offset:16384\ns_waitcnt vmcnt(16)\ns_waitcnt lgkmcnt(3)\n"
"v_mfma_f32_16x16x32_bf16 v[64:67], v[192:195], v[40:43], v[64:67]\nv_mfma_f32_16x16x32_bf16 v[68:71], v[196:199], v[40:43], v[68:71]\nds_read_b128 v[40:43], v32 offset:20480\ns_waitcnt lgkmcnt(3)\n"
"v_mfma_f32_16x16x32_bf16 v[72:75], v[192:195], v[44:47], v[72:75]\nv_mfma_f32_16x16x32_bf16 v[76:79], v[196:199], v[44:47], v[76:79]\nds_read_b128 v[44:47], v33 offset:20480\ns_waitcnt lgkmcnt(3)\n"
"v_mfma_f32_16x16x32_bf16 v[64:67], v[200:203], v[48:51], v[64:67]\nv_mfma_f32_16x16x32_bf16 v[68:71], v[204:207], v[48:51], v[68:71]\nds_read_b128 v[48:51], v34 offset:20480\ns_waitcnt lgkmcnt(3)\n"
"v_mfma_f32_16x16x32_bf16 v[72:75], v[200:203], v[52:55], v[72:75]\nv_mfma_f32_16x16x32_bf16 v[76:79], v[204:207], v[52:55], v[76:79]\nds_read_b128 v[52:55], v35 offset:20480\ns_waitcnt lgkmcnt(3)\n"
"v_mfma_f32_16x16x32_bf16 v[64:67], v[208:211], v[40:43], v[64:67]\nv_mfma_f32_16x16x32_bf16 v[68:71], v[212:215], v[40:43], v[68:71]\nds_read_b128 v[40:43], v32 offset:24576\ns_waitcnt lgkmcnt(3)\n"
"v_mfma_f32_16x16x32_bf16 v[72:75], v[208:211], v[44:47], v[72:75]\nv_mfma_f32_16x16x32_bf16 v[76:79], v[212:215], v[44:47], v[76:79]\nds_read_b128 v[44:47], v33 offset:24576\ns_waitcnt lgkmcnt(3)\n"
"v_mfma_f32_16x16x32_bf16 v[64:67], v[216:219], v[48:51], v[64:67]\ns_add_u32 s54, s51, 32768\ns_cmp_eq_u32 s54, 163840\nv_mfma_f32_16x16x32_bf16 v[68:71], v[220:223], v[48:51], v[68:71]\n"
"s_cselect_b32 s54, 0, s54\ns_sub_u32 s54, s54, s51\nds_read_b128 v[48:51], v34 offset:24576\ns_waitcnt lgkmcnt(3)\n"
"v_mfma_f32_16x16x32_bf16 v[72:75], v[216:219], v[52:55], v[72:75]\nv_mfma_f32_16x16x32_bf16 v[76:79], v[220:223], v[52:55], v[76:79]\nds_read_b128 v[52:55], v35 offset:24576\ns_waitcnt lgkmcnt(3)\n"
"v_mfma_f32_16x16x32_bf16 v[64:67], v[224:227], v[40:43], v[64:67]\nv_add_u32 v32, s54, v32\nv_mfma_f32_16x16x32_bf16 v[68:71], v[228:231], v[40:43], v[68:71]\nds_read_b128 v[40:43], v32 offset:0\n"
"s_waitcnt lgkmcnt(3)\nv_mfma_f32_16x16x32_bf16 v[72:75], v[224:227], v[44:47], v[72:75]\nv_add_u32 v33, s54, v33\nv_mfma_f32_16x16x32_bf16 v[76:79], v[228:231], v[44:47], v[76:79]\n"
"ds_read_b128 v[44:47], v33 offset:0\ns_waitcnt lgkmcnt(3)\nv_mfma_f32_16x16x32_bf16 v[64:67], v[232:235], v[48:51], v[64:67]\nv_add_u32 v34, s54, v34\n"
"v_mfma_f32_16x16x32_bf16 v[68:71], v[236:239], v[48:51], v[68:71]\nds_read_b128 v[48:51], v34 offset:0\ns_waitcnt lgkmcnt(3)\nv_mfma_f32_16x16x32_bf16 v[72:75], v[232:235], v[52:55], v[72:75]\n"
"v_add_u32 v35, s54, v35\nv_mfma_f32_16x16x32_bf16 v[76:79], v[236:239], v[52:55], v[76:79]\nds_read_b128 v[52:55], v35 offset:0\ns_mov_b32 s52, s47\n"
"s_add_u32 s47, s51, s32\ns_add_u32 s51, s51, s54\ns_add_u32 s49, s49, 28672\ns_waitcnt vmcnt(8)\n"
"s_barrier\ns_waitcnt lgkmcnt(3)\nv_mfma_f32_16x16x32_bf16 v[96:99], v[128:131], v[40:43], 0\nv_mov_b32 v36, s48\n"
"s_add_u32 s48, s48, 1\ns_nop 0\nbuffer_store_dword v36, v19, s[24:27], s33 offen sc1\nv_mfma_f32_16x16x32_bf16 v[100:103], v[132:135], v[40:43], 0\n"
"ds_read_b128 v[40:43], v32 offset:4096\ns_waitcnt lgkmcnt(3)\nv_mfma_f32_16x16x32_bf16 v[104:107], v[128:131], v[44:47], 0\nv_mfma_f32_16x16x32_bf16 v[108:111], v[132:135], v[44:47], 0\n"
"ds_read_b128 v[44:47], v33 offset:4096\nv_cvt_pk_bf16_f32 v56, v64, v65\ns_waitcnt lgkmcnt(3)\nv_mfma_f32_16x16x32_bf16 v[96:99], v[136:139], v[48:51], v[96:99]\n"
"v_cvt_pk_bf16_f32 v57, v66, v67\nv_mfma_f32_16x16x32_bf16 v[100:103], v[140:143], v[48:51], v[100:103]\nds_read_b128 v[48:51], v34 offset:4096\nv_cvt_pk_bf16_f32 v58, v68, v69\n"
"s_waitcnt lgkmcnt(3)\nv_mfma_f32_16x16x32_bf16 v[104:107], v[136:139], v[52:55], v[104:107]\nv_cvt_pk_bf16_f32 v59, v70, v71\nv_mfma_f32_16x16x32_bf16 v[108:111], v[140:143], v[52:55], v[108:111]\n"
"ds_read_b128 v[52:55], v35 offset:4096\ns_waitcnt lgkmcnt(3)\nv_mfma_f32_16x16x32_bf16 v[96:99], v[144:147], v[40:43], v[96:99]\nv_mfma_f32_16x16x32_bf16 v[100:103], v[148:151], v[40:43], v[100:103]\n"
"ds_read_b128 v[40:43], v32 offset:8192\nbuffer_store_dwordx4 v[56:59], v15, s[20:23], s50 offen sc1\ns_waitcnt lgkmcnt(3)\nv_mfma_f32_16x16x32_bf16 v[104:107], v[144:147], v[44:47], v[104:107]\n"
"v_cvt_pk_bf16_f32 v60, v72, v73\nv_mfma_f32_16x16x32_bf16 v[108:111], v[148:151], v[44:47], v[108:111]\nds_read_b128 v[44:47], v33 offset:8192\nv_cvt_pk_bf16_f32 v61, v74, v75\n"
"s_waitcnt lgkmcnt(3)\nv_mfma_f32_16x16x32_bf16 v[96:99], v[152:155], v[48:51], v[96:99]\nv_cvt_pk_bf16_f32 v62, v76, v77\nv_mfma_f32_16x16x32_bf16 v[100:103], v[156:159], v[48:51], v[100:103]\n"
"ds_read_b128 v[48:51], v34 offset:8192\nv_cvt_pk_bf16_f32 v63, v78, v79\ns_waitcnt lgkmcnt(3)\nv_mfma_f32_16x16x32_bf16 v[104:107], v[152:155], v[52:55], v[104:107]\n"
"v_mfma_f32_16x16x32_bf16 v[108:111], v[156:159], v[52:55], v[108:111]\nds_read_b128 v[52:55], v35 offset:8192\ns_waitcnt lgkmcnt(3)\nv_mfma_f32_16x16x32_bf16 v[96:99], v[160:163], v[40:43], v[96:99]\n"
"buffer_store_dwordx4 v[60:63], v16, s[20:23], s50 offen sc1\nv_mfma_f32_16x16x32_bf16 v[100:103], v[164:167], v[40:43], v[100:103]\nds_read_b128 v[40:43], v32 offset:12288\ns_add_u32 s50, s50, s36\n"
"s_waitcnt lgkmcnt(3)\nv_mfma_f32_16x16x32_bf16 v[104:107], v[160:163], v[44:47], v[104:107]\ns_add_u32 m0, s52, 0\nv_mfma_f32_16x16x32_bf16 v[108:111], v[164:167], v[44:47], v[108:111]\n"
"ds_read_b128 v[44:47], v33 offset:12288\ns_add_u32 s53, s49, 0\ns_waitcnt lgkmcnt(3)\nv_mfma_f32_16x16x32_bf16 v[96:99], v[168:171], v[48:51], v[96:99]\n"
"buffer_load_dwordx4 v10, s[12:15], s53 offen lds\nv_mfma_f32_16x16x32_bf16 v[100:103], v[172:175], v[48:51], v[100:103]\nds_read_b128 v[48:51], v34 offset:12288\ns_add_u32 m0, s52, 4096\n"
"s_waitcnt lgkmcnt(3)\nv_mfma_f32_16x16x32_bf16 v[104:107], v[168:171], v[52:55], v[104:107]\ns_add_u32 s53, s49, 128\nv_mfma_f32_16x16x32_bf16 v[108:111], v[172:175], v[52:55], v[108:111]\n"
"ds_read_b128 v[52:55], v35 offset:12288\nbuffer_load_dwordx4 v10, s[12:15], s53 offen lds\ns_waitcnt lgkmcnt(3)\nv_mfma_f32_16x16x32_bf16 v[96:99], v[176:179], v[40:43], v[96:99]\n"
"s_add_u32 m0, s52, 8192\nv_mfma_f32_16x16x32_bf16 v[100:103], v[180:183], v[40:43], v[100:103]\nds_read_b128 v[40:43], v32 offset:16384\ns_add_u32 s53, s49, 256\n"
"s_waitcnt lgkmcnt(3)\nv_mfma_f32_16x16x32_bf16 v[104:107], v[176:179], v[44:47], v[104:107]\nbuffer_load_dwordx4 v10, s[12:15], s53 offen lds\nv_mfma_f32_16x16x32_bf16 v[108:111], v[180:183], v[44:47], v[108:111]\n"
"ds_read_b128 v[44:47], v33 offset:16384\ns_add_u32 m0, s52, 12288\ns_waitcnt lgkmcnt(3)\nv_mfma_f32_16x16x32_bf16 v[96:99], v[184:187], v[48:51], v[96:99]\n"
"s_add_u32 s53, s49, 384\nv_mfma_f32_16x16x32_bf16 v[100:103], v[188:191], v[48:51], v[100:103]\nds_read_b128 v[48:51], v34 offset:16384\nbuffer_load_dwordx4 v10, s[12:15], s53 offen lds\n"
"s_waitcnt lgkmcnt(3)\nv_mfma_f32_16x16x32_bf16 v[104:107], v[184:187], v[52:55], v[104:107]\ns_add_u32 m0, s52, 16384\nv_mfma_f32_16x16x32_bf16 v[108:111], v[188:191], v[52:55], v[108:111]\n"
"ds_read_b128 v[52:55], v35 offset:16384\ns_add_u32 s53, s49, 512\ns_waitcnt lgkmcnt(3)\nv_mfma_f32_16x16x32_bf16 v[96:99], v[192:195], v[40:43], v[96:99]\n"
"buffer_load_dwordx4 v10, s[12:15], s53 offen lds\nv_mfma_f32_16x16x32_bf16 v[100:103], v[196:199], v[40:43], v[100:103]\nds_read_b128 v[40:43], v32 offset:20480\ns_add_u32 m0, s52, 20480\n"
"s_waitcnt lgkmcnt(3)\nv_mfma_f32_16x16x32_bf16 v[104:107], v[192:195], v[44:47], v[104:107]\ns_add_u32 s53, s49, 640\nv_mfma_f32_16x16x32_bf16 v[108:111], v[196:199], v[44:47], v[108:111]\n"
"ds_read_b128 v[44:47], v33 offset:20480\nbuffer_load_dwordx4 v10, s[12:15], s53 offen lds\ns_waitcnt lgkmcnt(3)\nv_mfma_f32_16x16x32_bf16 v[96:99], v[200:203], v[48:51], v[96:99]\n"
"s_add_u32 m0, s52, 24576\nv_mfma_f32_16x16x32_bf16 v[100:103], v[204:207], v[48:51], v[100:103]\nds_read_b128 v[48:51], v34 offset:20480\ns_add_u32 s53, s49, 768\n"
"s_waitcnt lgkmcnt(3)\nv_mfma_f32_16x16x32_bf16 v[104:107], v[200:203], v[52:55], v[104:107]\nbuffer_load_dwordx4 v10, s[12:15], s53 offen lds\nv_mfma_f32_16x16x32_bf16 v[108:111], v[204:207], v[52:55], v[108:111]\n"
"ds_read_b128 v[52:55], v35 offset:20480\ns_waitcnt lgkmcnt(3)\nv_mfma_f32_16x16x32_bf16 v[96:99], v[208:211], v[40:43], v[96:99]\nv_mfma_f32_16x16x32_bf16 v[100:103], v[212:215], v[40:43], v[100:103]\n"
"ds_read_b128 v[40:43], v32 offset:24576\ns_waitcnt lgkmcnt(3)\nv_mfma_f32_16x16x32_bf16 v[104:107], v[208:211], v[44:47], v[104:107]\nv_mfma_f32_16x16x32_bf16 v[108:111], v[212:215], v[44:47], v[108:111]\n"
"ds_read_b128 v[44:47], v33 offset:24576\ns_waitcnt lgkmcnt(3)\nv_mfma_f32_16x16x32_bf16 v[96:99], v[216:219], v[48:51], v[96:99]\ns_add_u32 s54, s51, 32768\n"
"s_cmp_eq_u32 s54, 163840\nv_mfma_f32_16x16x32_bf16 v[100:103], v[220:223], v[48:51], v[100:103]\ns_cselect_b32 s54, 0, s54\ns_sub_u32 s54, s54, s51\n"
"ds_read_b128 v[48:51], v34 offset:24576\ns_waitcnt lgkmcnt(3)\nv_mfma_f32_16x16x32_bf16 v[104:107], v[216:219], v[52:55], v[104:107]\nv_mfma_f32_16x16x32_bf16 v[108:111], v[220:223], v[52:55], v[108:111]\n"
"ds_read_b128 v[52:55], v35 offset:24576\ns_waitcnt lgkmcnt(3)\nv_mfma_f32_16x16x32_bf16 v[96:99], v[224:227], v[40:43], v[96:99]\nv_add_u32 v32, s54, v32\n"
"v_mfma_f32_16x16x32_bf16 v[100:103], v[228:231], v[40:43], v[100:103]\nds_read_b128 v[40:43], v32 offset:0\ns_waitcnt lgkmcnt(3)\nv_mfma_f32_16x16x32_bf16 v[104:107], v[224:227], v[44:47], v[104:107]\n"
"v_add_u32 v33, s54, v33\nv_mfma_f32_16x16x32_bf16 v[108:111], v[228:231], v[44:47], v[108:111]\nds_read_b128 v[44:47], v33 offset:0\ns_waitcnt lgkmcnt(3)\n"
"v_mfma_f32_16x16x32_bf16 v[96:99], v[232:235], v[48:51], v[96:99]\nv_add_u32 v34, s54, v34\nv_mfma_f32_16x16x32_bf16 v[100:103], v[236:239], v[48:51], v[100:103]\nds_read_b128 v[48:51], v34 offset:0\n"
"s_waitcnt lgkmcnt(3)\nv_mfma_f32_16x16x32_bf16 v[104:107], v[232:235], v[52:55], v[104:107]\nv_add_u32 v35, s54, v35\nv_mfma_f32_16x16x32_bf16 v[108:111], v[236:239], v[52:55], v[108:111]\n"
"ds_read_b128 v[52:55], v35 offset:0\ns_mov_b32 s52, s47\ns_add_u32 s47, s51, s32\ns_add_u32 s51, s51, s54\n"
"s_add_u32 s49, s49, 28672\ns_waitcnt vmcnt(7)\ns_barrier\ns_waitcnt lgkmcnt(3)\n"
"v_mfma_f32_16x16x32_bf16 v[64:67], v[128:131], v[40:43], 0\nv_mov_b32 v36, s48\ns_add_u32 s48, s48, 1\ns_nop 0\n"
"buffer_store_dword v36, v19, s[24:27], s33 offen sc1\nv_mfma_f32_16x16x32_bf16 v[68:71], v[132:135], v[40:43], 0\nds_read_b128 v[40:43], v32 offset:4096\ns_waitcnt lgkmcnt(3)\n"
"v_mfma_f32_16x16x32_bf16 v[72:75], v[128:131], v[44:47], 0\nv_mfma_f32_16x16x32_bf16 v[76:79], v[132:135], v[44:47], 0\nds_read_b128 v[44:47], v33 offset:4096\nv_cvt_pk_bf16_f32 v56, v96, v97\n"
"s_waitcnt lgkmcnt(3)\nv_mfma_f32_16x16x32_bf16 v[64:67], v[136:139], v[48:51], v[64:67]\nv_cvt_pk_bf16_f32 v57, v98, v99\nv_mfma_f32_16x16x32_bf16 v[68:71], v[140:143], v[48:51], v[68:71]\n"
"ds_read_b128 v[48:51], v34 offset:4096\nv_cvt_pk_bf16_f32 v58, v100, v101\ns_waitcnt lgkmcnt(3)\nv_mfma_f32_16x16x32_bf16 v[72:75], v[136:139], v[52:55], v[72:75]\n"
"v_cvt_pk_bf16_f32 v59, v102, v103\nv_mfma_f32_16x16x32_bf16 v[76:79], v[140:143], v[52:55], v[76:79]\nds_read_b128 v[52:55], v35 offset:4096\ns_waitcnt lgkmcnt(3)\n"
"v_mfma_f32_16x16x32_bf16 v[64:67], v[144:147], v[40:43], v[64:67]\nv_mfma_f32_16x16x32_bf16 v[68:71], v[148:151], v[40:43], v[68:71]\nds_read_b128 v[40:43], v32 offset:8192\nbuffer_store_dwordx4 v[56:59], v15, s[20:23], s50 offen sc1\n"
"s_waitcnt lgkmcnt(3)\nv_mfma_f32_16x16x32_bf16 v[72:75], v[144:147], v[44:47], v[72:75]\nv_cvt_pk_bf16_f32 v60, v104, v105\nv_mfma_f32_16x16x32_bf16 v[76:79], v[148:151], v[44:47], v[76:79]\n"
"ds_read_b128 v[44:47], v33 offset:8192\nv_cvt_pk_bf16_f32 v61, v106, v107\ns_waitcnt lgkmcnt(3)\nv_mfma_f32_16x16x32_bf16 v[64:67], v[152:155], v[48:51], v[64:67]\n"
"v_cvt_pk_bf16_f32 v62, v108, v109\nv_mfma_f32_16x16x32_bf16 v[68:71], v[156:159], v[48:51], v[68:71]\nds_read_b128 v[48:51], v34 offset:8192\nv_cvt_pk_bf16_f32 v63, v110, v111\n"
"s_waitcnt lgkmcnt(3)\nv_mfma_f32_16x16x32_bf16 v[72:75], v[152:155], v[52:55], v[72:75]\nv_mfma_f32_16x16x32_bf16 v[76:79], v[156:159], v[52:55], v[76:79]\nds_read_b128 v[52:55], v35 offset:8192\n"
"s_waitcnt lgkmcnt(3)\nv_mfma_f32_16x16x32_bf16 v[64:67], v[160:163], v[40:43], v[64:67]\nbuffer_store_dwordx4 v[60:63], v16, s[20:23], s50 offen sc1\nv_mfma_f32_16x16x32_bf16 v[68:71], v[164:167], v[40:43], v[68:71]\n"
"ds_read_b128 v[40:43], v32 offset:12288\ns_add_u32 s50, s50, s36\ns_waitcnt lgkmcnt(3)\nv_mfma_f32_16x16x32_bf16 v[72:75], v[160:163], v[44:47], v[72:75]\n"
"s_add_u32 m0, s52, 0\nv_mfma_f32_16x16x32_bf16 v[76:79], v[164:167], v[44:47], v[76:79]\nds_read_b128 v[44:47], v33 offset:12288\ns_add_u32 s53, s49, 0\n"
"s_waitcnt lgkmcnt(3)\nv_mfma_f32_16x16x32_bf16 v[64:67], v[168:171], v[48:51], v[64:67]\nbuffer_load_dwordx4 v10, s[12:15], s53 offen lds\nv_mfma_f32_16x16x32_bf16 v[68:71], v[172:175], v[48:51], v[68:71]\n"
"ds_read_b128 v[48:51], v34 offset:12288\ns_add_u32 m0, s52, 4096\ns_waitcnt lgkmcnt(3)\nv_mfma_f32_16x16x32_bf16 v[72:75], v[168:171], v[52:55], v[72:75]\n"
"s_add_u32 s53, s49, 128\nv_mfma_f32_16x16x32_bf16 v[76:79], v[172:175], v[52:55], v[76:79]\nds_read_b128 v[52:55], v35 offset:12288\nbuffer_load_dwordx4 v10, s[12:15], s53 offen lds\n"
"s_waitcnt lgkmcnt(3)\nv_mfma_f32_16x16x32_bf16 v[64:67], v[176:179], v[40:43], v[64:67]\ns_add_u32 m0, s52, 8192\nv_mfma_f32_16x16x32_bf16 v[68:71], v[180:183], v[40:43], v[68:71]\n"
"ds_read_b128 v[40:43], v32 offset:16384\ns_add_u32 s53, s49, 256\ns_waitcnt lgkmcnt(3)\nv_mfma_f32_16x16x32_bf16 v[72:75], v[176:179], v[44:47], v[72:75]\n"
"buffer_load_dwordx4 v10, s[12:15], s53 offen lds\nv_mfma_f32_16x16x32_bf16 v[76:79], v[180:183], v[44:47], v[76:79]\nds_read_b128 v[44:47], v33 offset:16384\ns_add_u32 m0, s52, 12288\n"
"s_waitcnt lgkmcnt(3)\nv_mfma_f32_16x16x32_bf16 v[64:67], v[184:187], v[48:51], v[64:67]\ns_add_u32 s53, s49, 384\nv_mfma_f32_16x16x32_bf16 v[68:71], v[188:191], v[48:51], v[68:71]\n"
"ds_read_b128 v[48:51], v34 offset:16384\nbuffer_load_dwordx4 v10, s[12:15], s53 offen lds\ns_waitcnt lgkmcnt(3)\nv_mfma_f32_16x16x32_bf16 v[72:75], v[184:187], v[52:55], v[72:75]\n"
"s_add_u32 m0, s52, 16384\nv_mfma_f32_16x16x32_bf16 v[76:79], v[188:191], v[52:55], v[76:79]\nds_read_b128 v[52:55], v35 offset:16384\ns_add_u32 s53, s49, 512\n"
"s_waitcnt lgkmcnt(3)\nv_mfma_f32_16x16x32_bf16 v[64:67], v[192:195], v[40:43], v[64:67]\nbuffer_load_dwordx4 v10, s[12:15], s53 offen lds\nv_mfma_f32_16x16x32_bf16 v[68:71], v[196:199], v[40:43], v[68:71]\n"
"ds_read_b128 v[40:43], v32 offset:20480\ns_add_u32 m0, s52, 20480\ns_waitcnt lgkmcnt(3)\nv_mfma_f32_16x16x32_bf16 v[72:75], v[192:195], v[44:47], v[72:75]\n"
"s_add_u32 s53, s49, 640\nv_mfma_f32_16x16x32_bf16 v[76:79], v[196:199], v[44:47], v[76:79]\nds_read_b128 v[44:47], v33 offset:20480\nbuffer_load_dwordx4 v10, s[12:15], s53 offen lds\n"
"s_waitcnt lgkmcnt(3)\nv_mfma_f32_16x16x32_bf16 v[64:67], v[200:203], v[48:51], v[64:67]\ns_add_u32 m0, s52, 24576\nv_mfma_f32_16x16x32_bf16 v[68:71], v[204:207], v[48:51], v[68:71]\n"
"ds_read_b128 v[48:51], v34 offset:20480\ns_add_u32 s53, s49, 768\ns_waitcnt lgkmcnt(3)\nv_mfma_f32_16x16x32_bf16 v[72:75], v[200:203], v[52:55], v[72:75]\n"
"buffer_load_dwordx4 v10, s[12:15], s53 offen lds\nv_mfma_f32_16x16x32_bf16 v[76:79], v[204:207], v[52:55], v[76:79]\nds_read_b128 v[52:55], v35 offset:20480\ns_waitcnt lgkmcnt(3)\n"
"v_mfma_f32_16x16x32_bf16 v[64:67], v[208:211], v[40:43], v[64:67]\nv_mfma_f32_16x16x32_bf16 v[68:71], v[212:215], v[40:43], v[68:71]\nds_read_b128 v[40:43], v32 offset:24576\ns_waitcnt lgkmcnt(3)\n"
"v_mfma_f32_16x16x32_bf16 v[72:75], v[208:211], v[44:47], v[72:75]\nv_mfma_f32_16x16x32_bf16 v[76:79], v[212:215], v[44:47], v[76:79]\nds_read_b128 v[44:47], v33 offset:24576\ns_waitcnt lgkmcnt(3)\n"
"v_mfma_f32_16x16x32_bf16 v[64:67], v[216:219], v[48:51], v[64:67]\ns_add_u32 s54, s51, 32768\ns_cmp_eq_u32 s54, 163840\nv_mfma_f32_16x16x32_bf16 v[68:71], v[220:223], v[48:51], v[68:71]\n"
"s_cselect_b32 s54, 0, s54\ns_sub_u32 s54, s54, s51\nds_read_b128 v[48:51], v34 offset:24576\ns_waitcnt lgkmcnt(3)\n"
"v_mfma_f32_16x16x32_bf16 v[72:75], v[216:219], v[52:55], v[72:75]\nv_mfma_f32_16x16x32_bf16 v[76:79], v[220:223], v[52:55], v[76:79]\nds_read_b128 v[52:55], v35 offset:24576\ns_waitcnt lgkmcnt(3)\n"
"v_mfma_f32_16x16x32_bf16 v[64:67], v[224:227], v[40:43], v[64:67]\nv_add_u32 v32, s54, v32\nv_mfma_f32_16x16x32_bf16 v[68:71], v[228:231], v[40:43], v[68:71]\nds_read_b128 v[40:43], v32 offset:0\n"
"s_waitcnt lgkmcnt(3)\nv_mfma_f32_16x16x32_bf16 v[72:75], v[224:227], v[44:47], v[72:75]\nv_add_u32 v33, s54, v33\nv_mfma_f32_16x16x32_bf16 v[76:79], v[228:231], v[44:47], v[76:79]\n"
"ds_read_b128 v[44:47], v33 offset:0\ns_waitcnt lgkmcnt(3)\nv_mfma_f32_16x16x32_bf16 v[64:67], v[232:235], v[48:51], v[64:67]\nv_add_u32 v34, s54, v34\n"
"v_mfma_f32_16x16x32_bf16 v[68:71], v[236:239], v[48:51], v[68:71]\nds_read_b128 v[48:51], v34 offset:0\ns_waitcnt lgkmcnt(3)\nv_mfma_f32_16x16x32_bf16 v[72:75], v[232:235], v[52:55], v[72:75]\n"
"v_add_u32 v35, s54, v35\nv_mfma_f32_16x16x32_bf16 v[76:79], v[236:239], v[52:55], v[76:79]\nds_read_b128 v[52:55], v35 offset:0\ns_mov_b32 s52, s47\n"
"s_add_u32 s47, s51, s32\ns_add_u32 s51, s51, s54\ns_add_u32 s49, s49, 28672\ns_waitcnt vmcnt(7)\n"
"s_barrier\ns_waitcnt lgkmcnt(3)\nv_mfma_f32_16x16x32_bf16 v[96:99], v[128:131], v[40:43], 0\nv_mov_b32 v36, s48\n"
"s_add_u32 s48, s48, 1\ns_nop 0\nbuffer_store_dword v36, v19, s[24:27], s33 offen sc1\nv_mfma_f32_16x16x32_bf16 v[100:103], v[132:135], v[40:43], 0\n"
"ds_read_b128 v[40:43], v32 offset:4096\ns_waitcnt lgkmcnt(3)\nv_mfma_f32_16x16x32_bf16 v[104:107], v[128:131], v[44:47], 0\nv_mfma_f32_16x16x32_bf16 v[108:111], v[132:135], v[44:47], 0\n"
"ds_read_b128 v[44:47], v33 offset:4096\nv_cvt_pk_bf16_f32 v56, v64, v65\ns_waitcnt lgkmcnt(3)\nv_mfma_f32_16x16x32_bf16 v[96:99], v[136:139], v[48:51], v[96:99]\n"
"v_cvt_pk_bf16_f32 v57, v66, v67\nv_mfma_f32_16x16x32_bf16 v[100:103], v[140:143], v[48:51], v[100:103]\nds_read_b128 v[48:51], v34 offset:4096\nv_cvt_pk_bf16_f32 v58, v68, v69\n"
"s_waitcnt lgkmcnt(3)\nv_mfma_f32_16x16x32_bf16 v[104:107], v[136:139], v[52:55], v[104:107]\nv_cvt_pk_bf16_f32 v59, v70, v71\nv_mfma_f32_16x16x32_bf16 v[108:111], v[140:143], v[52:55], v[108:111]\n"
"ds_read_b128 v[52:55], v35 offset:4096\ns_waitcnt lgkmcnt(3)\nv_mfma_f32_16x16x32_bf16 v[96:99], v[144:147], v[40:43], v[96:99]\nv_mfma_f32_16x16x32_bf16 v[100:103], v[148:151], v[40:43], v[100:103]\n"
"ds_read_b128 v[40:43], v32 offset:8192\nbuffer_store_dwordx4 v[56:59], v15, s[20:23], s50 offen sc1\ns_waitcnt lgkmcnt(3)\nv_mfma_f32_16x16x32_bf16 v[104:107], v[144:147], v[44:47], v[104:107]\n"
"v_cvt_pk_bf16_f32 v60, v72, v73\nv_mfma_f32_16x16x32_bf16 v[108:111], v[148:151], v[44:47], v[108:111]\nds_read_b128 v[44:47], v33 offset:8192\nv_cvt_pk_bf16_f32 v61, v74, v75\n"
"s_waitcnt lgkmcnt(3)\nv_mfma_f32_16x16x32_bf16 v[96:99], v[152:155], v[48:51], v[96:99]\nv_cvt_pk_bf16_f32 v62, v76, v77\nv_mfma_f32_16x16x32_bf16 v[100:103], v[156:159], v[48:51], v[100:103]\n"
"ds_read_b128 v[48:51], v34 offset:8192\nv_cvt_pk_bf16_f32 v63, v78, v79\ns_waitcnt lgkmcnt(3)\nv_mfma_f32_16x16x32_bf16 v[104:107], v[152:155], v[52:55], v[104:107]\n"
"v_mfma_f32_16x16x32_bf16 v[108:111], v[156:159], v[52:55], v[108:111]\nds_read_b128 v[52:55], v35 offset:8192\ns_waitcnt lgkmcnt(3)\nv_mfma_f32_16x16x32_bf16 v[96:99], v[160:163], v[40:43], v[96:99]\n"
"buffer_store_dwordx4 v[60:63], v16, s[20:23], s50 offen sc1\nv_mfma_f32_16x16x32_bf16 v[100:103], v[164:167], v[40:43], v[100:103]\nds_read_b128 v[40:43], v32 offset:12288\ns_add_u32 s50, s50, s36\n"
"s_waitcnt lgkmcnt(3)\nv_mfma_f32_16x16x32_bf16 v[104:107], v[160:163], v[44:47], v[104:107]\ns_add_u32 m0, s52, 0\nv_mfma_f32_16x16x32_bf16 v[108:111], v[164:167], v[44:47], v[108:111]\n"
"ds_read_b128 v[44:47], v33 offset:12288\ns_add_u32 s53, s49, 0\ns_waitcnt lgkmcnt(3)\nv_mfma_f32_16x16x32_bf16 v[96:99], v[168:171], v[48:51], v[96:99]\n"
"buffer_load_dwordx4 v10, s[12:15], s53 offen lds\nv_mfma_f32_16x16x32_bf16 v[100:103], v[172:175], v[48:51], v[100:103]\nds_read_b128 v[48:51], v34 offset:12288\ns_add_u32 m0, s52, 4096\n"
"s_waitcnt lgkmcnt(3)\nv_mfma_f32_16x16x32_bf16 v[104:107], v[168:171], v[52:55], v[104:107]\ns_add_u32 s53, s49, 128\nv_mfma_f32_16x16x32_bf16 v[108:111], v[172:175], v[52:55], v[108:111]\n"
"ds_read_b128 v[52:55], v35 offset:12288\nbuffer_load_dwordx4 v10, s[12:15], s53 offen lds\ns_waitcnt lgkmcnt(3)\nv_mfma_f32_16x16x32_bf16 v[96:99], v[176:179], v[40:43], v[96:99]\n"
"s_add_u32 m0, s52, 8192\nv_mfma_f32_16x16x32_bf16 v[100:103], v[180:183], v[40:43], v[100:103]\nds_read_b128 v[40:43], v32 offset:16384\ns_add_u32 s53, s49, 256\n"
"s_waitcnt lgkmcnt(3)\nv_mfma_f32_16x16x32_bf16 v[104:107], v[176:179], v[44:47], v[104:107]\nbuffer_load_dwordx4 v10, s[12:15], s53 offen lds\nv_mfma_f32_16x16x32_bf16 v[108:111], v[180:183], v[44:47], v[108:111]\n"
"ds_read_b128 v[44:47], v33 offset:16384\ns_add_u32 m0, s52, 12288\ns_waitcnt lgkmcnt(3)\nv_mfma_f32_16x16x32_bf16 v[96:99], v[184:187], v[48:51], v[96:99]\n"
"s_add_u32 s53, s49, 384\nv_mfma_f32_16x16x32_bf16 v[100:103], v[188:191], v[48:51], v[100:103]\nds_read_b128 v[48:51], v34 offset:16384\nbuffer_load_dwordx4 v10, s[12:15], s53 offen lds\n"
"s_waitcnt lgkmcnt(3)\nv_mfma_f32_16x16x32_bf16 v[104:107], v[184:187], v[52:55], v[104:107]\ns_add_u32 m0, s52, 16384\nv_mfma_f32_16x16x32_bf16 v[108:111], v[188:191], v[52:55], v[108:111]\n"
"ds_read_b128 v[52:55], v35 offset:16384\ns_add_u32 s53, s49, 512\ns_waitcnt lgkmcnt(3)\nv_mfma_f32_16x16x32_bf16 v[96:99], v[192:195], v[40:43], v[96:99]\n"
"buffer_load_dwordx4 v10, s[12:15], s53 offen lds\nv_mfma_f32_16x16x32_bf16 v[100:103], v[196:199], v[40:43], v[100:103]\nds_read_b128 v[40:43], v32 offset:20480\ns_add_u32 m0, s52, 20480\n"
"s_waitcnt lgkmcnt(3)\nv_mfma_f32_16x16x32_bf16 v[104:107], v[192:195], v[44:47], v[104:107]\ns_add_u32 s53, s49, 640\nv_mfma_f32_16x16x32_bf16 v[108:111], v[196:199], v[44:47], v[108:111]\n"
"ds_read_b128 v[44:47], v33 offset:20480\nbuffer_load_dwordx4 v10, s[12:15], s53 offen lds\ns_waitcnt lgkmcnt(3)\nv_mfma_f32_16x16x32_bf16 v[96:99], v[200:203], v[48:51], v[96:99]\n"
"s_add_u32 m0, s52, 24576\nv_mfma_f32_16x16x32_bf16 v[100:103], v[204:207], v[48:51], v[100:103]\nds_read_b128 v[48:51], v34 offset:20480\ns_add_u32 s53, s49, 768\n"
"s_waitcnt lgkmcnt(3)\nv_mfma_f32_16x16x32_bf16 v[104:107], v[200:203], v[52:55], v[104:107]\nbuffer_load_dwordx4 v10, s[12:15], s53 offen lds\nv_mfma_f32_16x16x32_bf16 v[108:111], v[204:207], v[52:55], v[108:111]\n"
"ds_read_b128 v[52:55], v35 offset:20480\ns_waitcnt lgkmcnt(3)\nv_mfma_f32_16x16x32_bf16 v[96:99], v[208:211], v[40:43], v[96:99]\nv_mfma_f32_16x16x32_bf16 v[100:103], v[212:215], v[40:43], v[100:103]\n"
"ds_read_b128 v[40:43], v32 offset:24576\ns_waitcnt lgkmcnt(3)\nv_mfma_f32_16x16x32_bf16 v[104:107], v[208:211], v[44:47], v[104:107]\nv_mfma_f32_16x16x32_bf16 v[108:111], v[212:215], v[44:47], v[108:111]\n"
"ds_read_b128 v[44:47], v33 offset:24576\ns_waitcnt lgkmcnt(3)\nv_mfma_f32_16x16x32_bf16 v[96:99], v[216:219], v[48:51], v[96:99]\ns_add_u32 s54, s51, 32768\n"
"s_cmp_eq_u32 s54, 163840\nv_mfma_f32_16x16x32_bf16 v[100:103], v[220:223], v[48:51], v[100:103]\ns_cselect_b32 s54, 0, s54\ns_sub_u32 s54, s54, s51\n"
"ds_read_b128 v[48:51], v34 offset:24576\ns_waitcnt lgkmcnt(3)\nv_mfma_f32_16x16x32_bf16 v[104:107], v[216:219], v[52:55], v[104:107]\nv_mfma_f32_16x16x32_bf16 v[108:111], v[220:223], v[52:55], v[108:111]\n"
"ds_read_b128 v[52:55], v35 offset:24576\ns_waitcnt lgkmcnt(3)\nv_mfma_f32_16x16x32_bf16 v[96:99], v[224:227], v[40:43], v[96:99]\nv_add_u32 v32, s54, v32\n"
"v_mfma_f32_16x16x32_bf16 v[100:103], v[228:231], v[40:43], v[100:103]\nds_read_b128 v[40:43], v32 offset:0\ns_waitcnt lgkmcnt(3)\nv_mfma_f32_16x16x32_bf16 v[104:107], v[224:227], v[44:47], v[104:107]\n"
"v_add_u32 v33, s54, v33\nv_mfma_f32_16x16x32_bf16 v[108:111], v[228:231], v[44:47], v[108:111]\nds_read_b128 v[44:47], v33 offset:0\ns_waitcnt lgkmcnt(3)\n"
"v_mfma_f32_16x16x32_bf16 v[96:99], v[232:235], v[48:51], v[96:99]\nv_add_u32 v34, s54, v34\nv_mfma_f32_16x16x32_bf16 v[100:103], v[236:239], v[48:51], v[100:103]\nds_read_b128 v[48:51], v34 offset:0\n"
"s_waitcnt lgkmcnt(3)\nv_mfma_f32_16x16x32_bf16 v[104:107], v[232:235], v[52:55], v[104:107]\nv_add_u32 v35, s54, v35\nv_mfma_f32_16x16x32_bf16 v[108:111], v[236:239], v[52:55], v[108:111]\n"
"ds_read_b128 v[52:55], v35 offset:0\ns_mov_b32 s52, s47\ns_add_u32 s47, s51, s32\ns_add_u32 s51, s51, s54\n"
"s_add_u32 s49, s49, 28672\ns_waitcnt vmcnt(7)\ns_barrier\ns_waitcnt lgkmcnt(3)\n"
"v_mfma_f32_16x16x32_bf16 v[64:67], v[128:131], v[40:43], 0\nv_mov_b32 v36, s48\ns_add_u32 s48, s48, 1\ns_nop 0\n"
"buffer_store_dword v36, v19, s[24:27], s33 offen sc1\nv_mfma_f32_16x16x32_bf16 v[68:71], v[132:135], v[40:43], 0\nds_read_b128 v[40:43], v32 offset:4096\ns_waitcnt lgkmcnt(3)\n"
"v_mfma_f32_16x16x32_bf16 v[72:75], v[128:131], v[44:47], 0\nv_mfma_f32_16x16x32_bf16 v[76:79], v[132:135], v[44:47], 0\nds_read_b128 v[44:47], v33 offset:4096\nv_cvt_pk_bf16_f32 v56, v96, v97\n"
"s_waitcnt lgkmcnt(3)\nv_mfma_f32_16x16x32_bf16 v[64:67], v[136:139], v[48:51], v[64:67]\nv_cvt_pk_bf16_f32 v57, v98, v99\nv_mfma_f32_16x16x32_bf16 v[68:71], v[140:143], v[48:51], v[68:71]\n"
"ds_read_b128 v[48:51], v34 offset:4096\nv_cvt_pk_bf16_f32 v58, v100, v101\ns_waitcnt lgkmcnt(3)\nv_mfma_f32_16x16x32_bf16 v[72:75], v[136:139], v[52:55], v[72:75]\n"
"v_cvt_pk_bf16_f32 v59, v102, v103\nv_mfma_f32_16x16x32_bf16 v[76:79], v[140:143], v[52:55], v[76:79]\nds_read_b128 v[52:55], v35 offset:4096\ns_waitcnt lgkmcnt(3)\n"
"v_mfma_f32_16x16x32_bf16 v[64:67], v[144:147], v[40:43], v[64:67]\nv_mfma_f32_16x16x32_bf16 v[68:71], v[148:151], v[40:43], v[68:71]\nds_read_b128 v[40:43], v32 offset:8192\nbuffer_store_dwordx4 v[56:59], v15, s[20:23], s50 offen sc1\n"
"s_waitcnt lgkmcnt(3)\nv_mfma_f32_16x16x32_bf16 v[72:75], v[144:147], v[44:47], v[72:75]\nv_cvt_pk_bf16_f32 v60, v104, v105\nv_mfma_f32_16x16x32_bf16 v[76:79], v[148:151], v[44:47], v[76:79]\n"
"ds_read_b128 v[44:47], v33 offset:8192\nv_cvt_pk_bf16_f32 v61, v106, v107\ns_waitcnt lgkmcnt(3)\nv_mfma_f32_16x16x32_bf16 v[64:67], v[152:155], v[48:51], v[64:67]\n"
"v_cvt_pk_bf16_f32 v62, v108, v109\nv_mfma_f32_16x16x32_bf16 v[68:71], v[156:159], v[48:51], v[68:71]\nds_read_b128 v[48:51], v34 offset:8192\nv_cvt_pk_bf16_f32 v63, v110, v111\n"
"s_waitcnt lgkmcnt(3)\nv_mfma_f32_16x16x32_bf16 v[72:75], v[152:155], v[52:55], v[72:75]\nv_mfma_f32_16x16x32_bf16 v[76:79], v[156:159], v[52:55], v[76:79]\nds_read_b128 v[52:55], v35 offset:8192\n"
"s_waitcnt lgkmcnt(3)\nv_mfma_f32_16x16x32_bf16 v[64:67], v[160:163], v[40:43], v[64:67]\nbuffer_store_dwordx4 v[60:63], v16, s[20:23], s50 offen sc1\nv_mfma_f32_16x16x32_bf16 v[68:71], v[164:167], v[40:43], v[68:71]\n"
"ds_read_b128 v[40:43], v32 offset:12288\ns_add_u32 s50, s50, s36\ns_waitcnt lgkmcnt(3)\nv_mfma_f32_16x16x32_bf16 v[72:75], v[160:163], v[44:47], v[72:75]\n"
"s_add_u32 m0, s52, 0\nv_mfma_f32_16x16x32_bf16 v[76:79], v[164:167], v[44:47], v[76:79]\nds_read_b128 v[44:47], v33 offset:12288\ns_add_u32 s53, s49, 0\n"
"s_waitcnt lgkmcnt(3)\nv_mfma_f32_16x16x32_bf16 v[64:67], v[168:171], v[48:51], v[64:67]\nbuffer_load_dwordx4 v10, s[12:15], s53 offen lds\nv_mfma_f32_16x16x32_bf16 v[68:71], v[172:175], v[48:51], v[68:71]\n"
"ds_read_b128 v[48:51], v34 offset:12288\ns_add_u32 m0, s52, 4096\ns_waitcnt lgkmcnt(3)\nv_mfma_f32_16x16x32_bf16 v[72:75], v[168:171], v[52:55], v[72:75]\n"
"s_add_u32 s53, s49, 128\nv_mfma_f32_16x16x32_bf16 v[76:79], v[172:175], v[52:55], v[76:79]\nds_read_b128 v[52:55], v35 offset:12288\nbuffer_load_dwordx4 v10, s[12:15], s53 offen lds\n"
"s_waitcnt lgkmcnt(3)\nv_mfma_f32_16x16x32_bf16 v[64:67], v[176:179], v[40:43], v[64:67]\ns_add_u32 m0, s52, 8192\nv_mfma_f32_16x16x32_bf16 v[68:71], v[180:183], v[40:43], v[68:71]\n"
"ds_read_b128 v[40:43], v32 offset:16384\ns_add_u32 s53, s49, 256\ns_waitcnt lgkmcnt(3)\nv_mfma_f32_16x16x32_bf16 v[72:75], v[176:179], v[44:47], v[72:75]\n"
"buffer_load_dwordx4 v10, s[12:15], s53 offen lds\nv_mfma_f32_16x16x32_bf16 v[76:79], v[180:183], v[44:47], v[76:79]\nds_read_b128 v[44:47], v33 offset:16384\ns_add_u32 m0, s52, 12288\n"
"s_waitcnt lgkmcnt(3)\nv_mfma_f32_16x16x32_bf16 v[64:67], v[184:187], v[48:51], v[64:67]\ns_add_u32 s53, s49, 384\nv_mfma_f32_16x16x32_bf16 v[68:71], v[188:191], v[48:51], v[68:71]\n"
"ds_read_b128 v[48:51], v34 offset:16384\nbuffer_load_dwordx4 v10, s[12:15], s53 offen lds\ns_waitcnt lgkmcnt(3)\nv_mfma_f32_16x16x32_bf16 v[72:75], v[184:187], v[52:55], v[72:75]\n"
"s_add_u32 m0, s52, 16384\nv_mfma_f32_16x16x32_bf16 v[76:79], v[188:191], v[52:55], v[76:79]\nds_read_b128 v[52:55], v35 offset:16384\ns_add_u32 s53, s49, 512\n"
"s_waitcnt lgkmcnt(3)\nv_mfma_f32_16x16x32_bf16 v[64:67], v[192:195], v[40:43], v[64:67]\nbuffer_load_dwordx4 v10, s[12:15], s53 offen lds\nv_mfma_f32_16x16x32_bf16 v[68:71], v[196:199], v[40:43], v[68:71]\n"
"ds_read_b128 v[40:43], v32 offset:20480\ns_add_u32 m0, s52, 20480\ns_waitcnt lgkmcnt(3)\nv_mfma_f32_16x16x32_bf16 v[72:75], v[192:195], v[44:47], v[72:75]\n"
"s_add_u32 s53, s49, 640\nv_mfma_f32_16x16x32_bf16 v[76:79], v[196:199], v[44:47], v[76:79]\nds_read_b128 v[44:47], v33 offset:20480\nbuffer_load_dwordx4 v10, s[12:15], s53 offen lds\n"
"s_waitcnt lgkmcnt(3)\nv_mfma_f32_16x16x32_bf16 v[64:67], v[200:203], v[48:51], v[64:67]\ns_add_u32 m0, s52, 24576\nv_mfma_f32_16x16x32_bf16 v[68:71], v[204:207], v[48:51], v[68:71]\n"
"ds_read_b128 v[48:51], v34 offset:20480\ns_add_u32 s53, s49, 768\ns_waitcnt lgkmcnt(3)\nv_mfma_f32_16x16x32_bf16 v[72:75], v[200:203], v[52:55], v[72:75]\n"
"buffer_load_dwordx4 v10, s[12:15], s53 offen lds\nv_mfma_f32_16x16x32_bf16 v[76:79], v[204:207], v[52:55], v[76:79]\nds_read_b128 v[52:55], v35 offset:20480\ns_waitcnt lgkmcnt(3)\n"
"v_mfma_f32_16x16x32_bf16 v[64:67], v[208:211], v[40:43], v[64:67]\nv_mfma_f32_16x16x32_bf16 v[68:71], v[212:215], v[40:43], v[68:71]\nds_read_b128 v[40:43], v32 offset:24576\ns_waitcnt lgkmcnt(3)\n"
"v_mfma_f32_16x16x32_bf16 v[72:75], v[208:211], v[44:47], v[72:75]\nv_mfma_f32_16x16x32_bf16 v[76:79], v[212:215], v[44:47], v[76:79]\nds_read_b128 v[44:47], v33 offset:24576\ns_waitcnt lgkmcnt(3)\n"
"v_mfma_f32_16x16x32_bf16 v[64:67], v[216:219], v[48:51], v[64:67]\ns_add_u32 s54, s51, 32768\ns_cmp_eq_u32 s54, 163840\nv_mfma_f32_16x16x32_bf16 v[68:71], v[220:223], v[48:51], v[68:71]\n"
"s_cselect_b32 s54, 0, s54\ns_sub_u32 s54, s54, s51\nds_read_b128 v[48:51], v34 offset:24576\ns_waitcnt lgkmcnt(3)\n"
"v_mfma_f32_16x16x32_bf16 v[72:75], v[216:219], v[52:55], v[72:75]\nv_mfma_f32_16x16x32_bf16 v[76:79], v[220:223], v[52:55], v[76:79]\nds_read_b128 v[52:55], v35 offset:24576\ns_waitcnt lgkmcnt(3)\n"
"v_mfma_f32_16x16x32_bf16 v[64:67], v[224:227], v[40:43], v[64:67]\nv_add_u32 v32, s54, v32\nv_mfma_f32_16x16x32_bf16 v[68:71], v[228:231], v[40:43], v[68:71]\nds_read_b128 v[40:43], v32 offset:0\n"
"s_waitcnt lgkmcnt(3)\nv_mfma_f32_16x16x32_bf16 v[72:75], v[224:227], v[44:47], v[72:75]\nv_add_u32 v33, s54, v33\nv_mfma_f32_16x16x32_bf16 v[76:79], v[228:231], v[44:47], v[76:79]\n"
"ds_read_b128 v[44:47], v33 offset:0\ns_waitcnt lgkmcnt(3)\nv_mfma_f32_16x16x32_bf16 v[64:67], v[232:235], v[48:51], v[64:67]\nv_add_u32 v34, s54, v34\n"
"v_mfma_f32_16x16x32_bf16 v[68:71], v[236:239], v[48:51], v[68:71]\nds_read_b128 v[48:51], v34 offset:0\ns_waitcnt lgkmcnt(3)\nv_mfma_f32_16x16x32_bf16 v[72:75], v[232:235], v[52:55], v[72:75]\n"
"v_add_u32 v35, s54, v35\nv_mfma_f32_16x16x32_bf16 v[76:79], v[236:239], v[52:55], v[76:79]\nds_read_b128 v[52:55], v35 offset:0\ns_mov_b32 s52, s47\n"
"s_add_u32 s47, s51, s32\ns_add_u32 s51, s51, s54\ns_add_u32 s49, s49, 28672\ns_waitcnt vmcnt(7)\n"
"s_barrier\ns_waitcnt lgkmcnt(3)\nv_mfma_f32_16x16x32_bf16 v[96:99], v[128:131], v[40:43], 0\nv_mov_b32 v36, s48\n"
"s_add_u32 s48, s48, 1\ns_nop 0\nbuffer_store_dword v36, v19, s[24:27], s33 offen sc1\nv_mfma_f32_16x16x32_bf16 v[100:103], v[132:135], v[40:43], 0\n"
"ds_read_b128 v[40:43], v32 offset:4096\ns_waitcnt lgkmcnt(3)\nv_mfma_f32_16x16x32_bf16 v[104:107], v[128:131], v[44:47], 0\nv_mfma_f32_16x16x32_bf16 v[108:111], v[132:135], v[44:47], 0\n"
"ds_read_b128 v[44:47], v33 offset:4096\nv_cvt_pk_bf16_f32 v56, v64, v65\ns_waitcnt lgkmcnt(3)\nv_mfma_f32_16x16x32_bf16 v[96:99], v[136:139], v[48:51], v[96:99]\n"
"v_cvt_pk_bf16_f32 v57, v66, v67\nv_mfma_f32_16x16x32_bf16 v[100:103], v[140:143], v[48:51], v[100:103]\nds_read_b128 v[48:51], v34 offset:4096\nv_cvt_pk_bf16_f32 v58, v68, v69\n"
"s_waitcnt lgkmcnt(3)\nv_mfma_f32_16x16x32_bf16 v[104:107], v[136:139], v[52:55], v[104:107]\nv_cvt_pk_bf16_f32 v59, v70, v71\nv_mfma_f32_16x16x32_bf16 v[108:111], v[140:143], v[52:55], v[108:111]\n"
"ds_read_b128 v[52:55], v35 offset:4096\ns_waitcnt lgkmcnt(3)\nv_mfma_f32_16x16x32_bf16 v[96:99], v[144:147], v[40:43], v[96:99]\nv_mfma_f32_16x16x32_bf16 v[100:103], v[148:151], v[40:43], v[100:103]\n"
"ds_read_b128 v[40:43], v32 offset:8192\nbuffer_store_dwordx4 v[56:59], v15, s[20:23], s50 offen sc1\ns_waitcnt lgkmcnt(3)\nv_mfma_f32_16x16x32_bf16 v[104:107], v[144:147], v[44:47], v[104:107]\n"
"v_cvt_pk_bf16_f32 v60, v72, v73\nv_mfma_f32_16x16x32_bf16 v[108:111], v[148:151], v[44:47], v[108:111]\nds_read_b128 v[44:47], v33 offset:8192\nv_cvt_pk_bf16_f32 v61, v74, v75\n"
"s_waitcnt lgkmcnt(3)\nv_mfma_f32_16x16x32_bf16 v[96:99], v[152:155], v[48:51], v[96:99]\nv_cvt_pk_bf16_f32 v62, v76, v77\nv_mfma_f32_16x16x32_bf16 v[100:103], v[156:159], v[48:51], v[100:103]\n"
"ds_read_b128 v[48:51], v34 offset:8192\nv_cvt_pk_bf16_f32 v63, v78, v79\ns_waitcnt lgkmcnt(3)\nv_mfma_f32_16x16x32_bf16 v[104:107], v[152:155], v[52:55], v[104:107]\n"
"v_mfma_f32_16x16x32_bf16 v[108:111], v[156:159], v[52:55], v[108:111]\nds_read_b128 v[52:55], v35 offset:8192\ns_waitcnt lgkmcnt(3)\nv_mfma_f32_16x16x32_bf16 v[96:99], v[160:163], v[40:43], v[96:99]\n"
"buffer_store_dwordx4 v[60:63], v16, s[20:23], s50 offen sc1\nv_mfma_f32_16x16x32_bf16 v[100:103], v[164:167], v[40:43], v[100:103]\nds_read_b128 v[40:43], v32 offset:12288\ns_add_u32 s50, s50, s36\n"
"s_waitcnt lgkmcnt(3)\nv_mfma_f32_16x16x32_bf16 v[104:107], v[160:163], v[44:47], v[104:107]\ns_add_u32 m0, s52, 0\nv_mfma_f32_16x16x32_bf16 v[108:111], v[164:167], v[44:47], v[108:111]\n"
"ds_read_b128 v[44:47], v33 offset:12288\ns_add_u32 s53, s49, 0\ns_waitcnt lgkmcnt(3)\nv_mfma_f32_16x16x32_bf16 v[96:99], v[168:171], v[48:51], v[96:99]\n"
"buffer_load_dwordx4 v10, s[12:15], s53 offen lds\nv_mfma_f32_16x16x32_bf16 v[100:103], v[172:175], v[48:51], v[100:103]\nds_read_b128 v[48:51], v34 offset:12288\ns_add_u32 m0, s52, 4096\n"
"s_waitcnt lgkmcnt(3)\nv_mfma_f32_16x16x32_bf16 v[104:107], v[168:171], v[52:55], v[104:107]\ns_add_u32 s53, s49, 128\nv_mfma_f32_16x16x32_bf16 v[108:111], v[172:175], v[52:55], v[108:111]\n"
"ds_read_b128 v[52:55], v35 offset:12288\nbuffer_load_dwordx4 v10, s[12:15], s53 offen lds\ns_waitcnt lgkmcnt(3)\nv_mfma_f32_16x16x32_bf16 v[96:99], v[176:179], v[40:43], v[96:99]\n"
"s_add_u32 m0, s52, 8192\nv_mfma_f32_16x16x32_bf16 v[100:103], v[180:183], v[40:43], v[100:103]\nds_read_b128 v[40:43], v32 offset:16384\ns_add_u32 s53, s49, 256\n"
"s_waitcnt lgkmcnt(3)\nv_mfma_f32_16x16x32_bf16 v[104:107], v[176:179], v[44:47], v[104:107]\nbuffer_load_dwordx4 v10, s[12:15], s53 offen lds\nv_mfma_f32_16x16x32_bf16 v[108:111], v[180:183], v[44:47], v[108:111]\n"
"ds_read_b128 v[44:47], v33 offset:16384\ns_add_u32 m0, s52, 12288\ns_waitcnt lgkmcnt(3)\nv_mfma_f32_16x16x32_bf16 v[96:99], v[184:187], v[48:51], v[96:99]\n"
"s_add_u32 s53, s49, 384\nv_mfma_f32_16x16x32_bf16 v[100:103], v[188:191], v[48:51], v[100:103]\nds_read_b128 v[48:51], v34 offset:16384\nbuffer_load_dwordx4 v10, s[12:15], s53 offen lds\n"
"s_waitcnt lgkmcnt(3)\nv_mfma_f32_16x16x32_bf16 v[104:107], v[184:187], v[52:55], v[104:107]\ns_add_u32 m0, s52, 16384\nv_mfma_f32_16x16x32_bf16 v[108:111], v[188:191], v[52:55], v[108:111]\n"
"ds_read_b128 v[52:55], v35 offset:16384\ns_add_u32 s53, s49, 512\ns_waitcnt lgkmcnt(3)\nv_mfma_f32_16x16x32_bf16 v[96:99], v[192:195], v[40:43], v[96:99]\n"
"buffer_load_dwordx4 v10, s[12:15], s53 offen lds\nv_mfma_f32_16x16x32_bf16 v[100:103], v[196:199], v[40:43], v[100:103]\nds_read_b128 v[40:43], v32 offset:20480\ns_add_u32 m0, s52, 20480\n"
"s_waitcnt lgkmcnt(3)\nv_mfma_f32_16x16x32_bf16 v[104:107], v[192:195], v[44:47], v[104:107]\ns_add_u32 s53, s49, 640\nv_mfma_f32_16x16x32_bf16 v[108:111], v[196:199], v[44:47], v[108:111]\n"
"ds_read_b128 v[44:47], v33 offset:20480\nbuffer_load_dwordx4 v10, s[12:15], s53 offen lds\ns_waitcnt lgkmcnt(3)\nv_mfma_f32_16x16x32_bf16 v[96:99], v[200:203], v[48:51], v[96:99]\n"
"s_add_u32 m0, s52, 24576\nv_mfma_f32_16x16x32_bf16 v[100:103], v[204:207], v[48:51], v[100:103]\nds_read_b128 v[48:51], v34 offset:20480\ns_add_u32 s53, s49, 768\n"
"s_waitcnt lgkmcnt(3)\nv_mfma_f32_16x16x32_bf16 v[104:107], v[200:203], v[52:55], v[104:107]\nbuffer_load_dwordx4 v10, s[12:15], s53 offen lds\nv_mfma_f32_16x16x32_bf16 v[108:111], v[204:207], v[52:55], v[108:111]\n"
"ds_read_b128 v[52:55], v35 offset:20480\ns_waitcnt lgkmcnt(3)\nv_mfma_f32_16x16x32_bf16 v[96:99], v[208:211], v[40:43], v[96:99]\nv_mfma_f32_16x16x32_bf16 v[100:103], v[212:215], v[40:43], v[100:103]\n"
"ds_read_b128 v[40:43], v32 offset:24576\ns_waitcnt lgkmcnt(3)\nv_mfma_f32_16x16x32_bf16 v[104:107], v[208:211], v[44:47], v[104:107]\nv_mfma_f32_16x16x32_bf16 v[108:111], v[212:215], v[44:47], v[108:111]\n"
"ds_read_b128 v[44:47], v33 offset:24576\ns_waitcnt lgkmcnt(3)\nv_mfma_f32_16x16x32_bf16 v[96:99], v[216:219], v[48:51], v[96:99]\ns_add_u32 s54, s51, 32768\n"
"s_cmp_eq_u32 s54, 163840\nv_mfma_f32_16x16x32_bf16 v[100:103], v[220:223], v[48:51], v[100:103]\ns_cselect_b32 s54, 0, s54\ns_sub_u32 s54, s54, s51\n"
"ds_read_b128 v[48:51], v34 offset:24576\ns_waitcnt lgkmcnt(3)\nv_mfma_f32_16x16x32_bf16 v[104:107], v[216:219], v[52:55], v[104:107]\nv_mfma_f32_16x16x32_bf16 v[108:111], v[220:223], v[52:55], v[108:111]\n"
"ds_read_b128 v[52:55], v35 offset:24576\ns_waitcnt lgkmcnt(3)\nv_mfma_f32_16x16x32_bf16 v[96:99], v[224:227], v[40:43], v[96:99]\nv_add_u32 v32, s54, v32\n"
"v_mfma_f32_16x16x32_bf16 v[100:103], v[228:231], v[40:43], v[100:103]\nds_read_b128 v[40:43], v32 offset:0\ns_waitcnt lgkmcnt(3)\nv_mfma_f32_16x16x32_bf16 v[104:107], v[224:227], v[44:47], v[104:107]\n"
"v_add_u32 v33, s54, v33\nv_mfma_f32_16x16x32_bf16 v[108:111], v[228:231], v[44:47], v[108:111]\nds_read_b128 v[44:47], v33 offset:0\ns_waitcnt lgkmcnt(3)\n"
"v_mfma_f32_16x16x32_bf16 v[96:99], v[232:235], v[48:51], v[96:99]\nv_add_u32 v34, s54, v34\nv_mfma_f32_16x16x32_bf16 v[100:103], v[236:239], v[48:51], v[100:103]\nds_read_b128 v[48:51], v34 offset:0\n"
"s_waitcnt lgkmcnt(3)\nv_mfma_f32_16x16x32_bf16 v[104:107], v[232:235], v[52:55], v[104:107]\nv_add_u32 v35, s54, v35\nv_mfma_f32_16x16x32_bf16 v[108:111], v[236:239], v[52:55], v[108:111]\n"
"ds_read_b128 v[52:55], v35 offset:0\ns_mov_b32 s52, s47\ns_add_u32 s47, s51, s32\ns_add_u32 s51, s51, s54\n"
"s_add_u32 s49, s49, 28672\ns_waitcnt vmcnt(7)\ns_barrier\ns_waitcnt lgkmcnt(3)\n"
"v_mfma_f32_16x16x32_bf16 v[64:67], v[128:131], v[40:43], 0\nv_mov_b32 v36, s48\ns_add_u32 s48, s48, 1\ns_nop 0\n"
"buffer_store_dword v36, v19, s[24:27], s33 offen sc1\nv_mfma_f32_16x16x32_bf16 v[68:71], v[132:135], v[40:43], 0\nds_read_b128 v[40:43], v32 offset:4096\ns_waitcnt lgkmcnt(3)\n"
"v_mfma_f32_16x16x32_bf16 v[72:75], v[128:131], v[44:47], 0\nv_mfma_f32_16x16x32_bf16 v[76:79], v[132:135], v[44:47], 0\nds_read_b128 v[44:47], v33 offset:4096\nv_cvt_pk_bf16_f32 v56, v96, v97\n"
"s_waitcnt lgkmcnt(3)\nv_mfma_f32_16x16x32_bf16 v[64:67], v[136:139], v[48:51], v[64:67]\nv_cvt_pk_bf16_f32 v57, v98, v99\nv_mfma_f32_16x16x32_bf16 v[68:71], v[140:143], v[48:51], v[68:71]\n"
"ds_read_b128 v[48:51], v34 offset:4096\nv_cvt_pk_bf16_f32 v58, v100, v101\ns_waitcnt lgkmcnt(3)\nv_mfma_f32_16x16x32_bf16 v[72:75], v[136:139], v[52:55], v[72:75]\n"
"v_cvt_pk_bf16_f32 v59, v102, v103\nv_mfma_f32_16x16x32_bf16 v[76:79], v[140:143], v[52:55], v[76:79]\nds_read_b128 v[52:55], v35 offset:4096\ns_waitcnt lgkmcnt(3)\n"
"v_mfma_f32_16x16x32_bf16 v[64:67], v[144:147], v[40:43], v[64:67]\nv_mfma_f32_16x16x32_bf16 v[68:71], v[148:151], v[40:43], v[68:71]\nds_read_b128 v[40:43], v32 offset:8192\nbuffer_store_dwordx4 v[56:59], v15, s[20:23], s50 offen sc1\n"
"s_waitcnt lgkmcnt(3)\nv_mfma_f32_16x16x32_bf16 v[72:75], v[144:147], v[44:47], v[72:75]\nv_cvt_pk_bf16_f32 v60, v104, v105\nv_mfma_f32_16x16x32_bf16 v[76:79], v[148:151], v[44:47], v[76:79]\n"
"ds_read_b128 v[44:47], v33 offset:8192\nv_cvt_pk_bf16_f32 v61, v106, v107\ns_waitcnt lgkmcnt(3)\nv_mfma_f32_16x16x32_bf16 v[64:67], v[152:155], v[48:51], v[64:67]\n"
"v_cvt_pk_bf16_f32 v62, v108, v109\nv_mfma_f32_16x16x32_bf16 v[68:71], v[156:159], v[48:51], v[68:71]\nds_read_b128 v[48:51], v34 offset:8192\nv_cvt_pk_bf16_f32 v63, v110, v111\n"
"s_waitcnt lgkmcnt(3)\nv_mfma_f32_16x16x32_bf16 v[72:75], v[152:155], v[52:55], v[72:75]\nv_mfma_f32_16x16x32_bf16 v[76:79], v[156:159], v[52:55], v[76:79]\nds_read_b128 v[52:55], v35 offset:8192\n"
"s_waitcnt lgkmcnt(3)\nv_mfma_f32_16x16x32_bf16 v[64:67], v[160:163], v[40:43], v[64:67]\nbuffer_store_dwordx4 v[60:63], v16, s[20:23], s50 offen sc1\nv_mfma_f32_16x16x32_bf16 v[68:71], v[164:167], v[40:43], v[68:71]\n"
"ds_read_b128 v[40:43], v32 offset:12288\ns_add_u32 s50, s50, s36\ns_waitcnt lgkmcnt(3)\nv_mfma_f32_16x16x32_bf16 v[72:75], v[160:163], v[44:47], v[72:75]\n"
"s_add_u32 m0, s52, 0\nv_mfma_f32_16x16x32_bf16 v[76:79], v[164:167], v[44:47], v[76:79]\nds_read_b128 v[44:47], v33 offset:12288\ns_add_u32 s53, s49, 0\n"
"s_waitcnt lgkmcnt(3)\nv_mfma_f32_16x16x32_bf16 v[64:67], v[168:171], v[48:51], v[64:67]\nbuffer_load_dwordx4 v10, s[12:15], s53 offen lds\nv_mfma_f32_16x16x32_bf16 v[68:71], v[172:175], v[48:51], v[68:71]\n"
"ds_read_b128 v[48:51], v34 offset:12288\ns_add_u32 m0, s52, 4096\ns_waitcnt lgkmcnt(3)\nv_mfma_f32_16x16x32_bf16 v[72:75], v[168:171], v[52:55], v[72:75]\n"
"s_add_u32 s53, s49, 128\nv_mfma_f32_16x16x32_bf16 v[76:79], v[172:175], v[52:55], v[76:79]\nds_read_b128 v[52:55], v35 offset:12288\nbuffer_load_dwordx4 v10, s[12:15], s53 offen lds\n"
"s_waitcnt lgkmcnt(3)\nv_mfma_f32_16x16x32_bf16 v[64:67], v[176:179], v[40:43], v[64:67]\ns_add_u32 m0, s52, 8192\nv_mfma_f32_16x16x32_bf16 v[68:71], v[180:183], v[40:43], v[68:71]\n"
"ds_read_b128 v[40:43], v32 offset:16384\ns_add_u32 s53, s49, 256\ns_waitcnt lgkmcnt(3)\nv_mfma_f32_16x16x32_bf16 v[72:75], v[176:179], v[44:47], v[72:75]\n"
"buffer_load_dwordx4 v10, s[12:15], s53 offen lds\nv_mfma_f32_16x16x32_bf16 v[76:79], v[180:183], v[44:47], v[76:79]\nds_read_b128 v[44:47], v33 offset:16384\ns_add_u32 m0, s52, 12288\n"
"s_waitcnt lgkmcnt(3)\nv_mfma_f32_16x16x32_bf16 v[64:67], v[184:187], v[48:51], v[64:67]\ns_add_u32 s53, s49, 384\nv_mfma_f32_16x16x32_bf16 v[68:71], v[188:191], v[48:51], v[68:71]\n"
"ds_read_b128 v[48:51], v34 offset:16384\nbuffer_load_dwordx4 v10, s[12:15], s53 offen lds\ns_waitcnt lgkmcnt(3)\nv_mfma_f32_16x16x32_bf16 v[72:75], v[184:187], v[52:55], v[72:75]\n"
"s_add_u32 m0, s52, 16384\nv_mfma_f32_16x16x32_bf16 v[76:79], v[188:191], v[52:55], v[76:79]\nds_read_b128 v[52:55], v35 offset:16384\ns_add_u32 s53, s49, 512\n"
"s_waitcnt lgkmcnt(3)\nv_mfma_f32_16x16x32_bf16 v[64:67], v[192:195], v[40:43], v[64:67]\nbuffer_load_dwordx4 v10, s[12:15], s53 offen lds\nv_mfma_f32_16x16x32_bf16 v[68:71], v[196:199], v[40:43], v[68:71]\n"
"ds_read_b128 v[40:43], v32 offset:20480\ns_add_u32 m0, s52, 20480\ns_waitcnt lgkmcnt(3)\nv_mfma_f32_16x16x32_bf16 v[72:75], v[192:195], v[44:47], v[72:75]\n"
"s_add_u32 s53, s49, 640\nv_mfma_f32_16x16x32_bf16 v[76:79], v[196:199], v[44:47], v[76:79]\nds_read_b128 v[44:47], v33 offset:20480\nbuffer_load_dwordx4 v10, s[12:15], s53 offen lds\n"
"s_waitcnt lgkmcnt(3)\nv_mfma_f32_16x16x32_bf16 v[64:67], v[200:203], v[48:51], v[64:67]\ns_add_u32 m0, s52, 24576\nv_mfma_f32_16x16x32_bf16 v[68:71], v[204:207], v[48:51], v[68:71]\n"
"ds_read_b128 v[48:51], v34 offset:20480\ns_add_u32 s53, s49, 768\ns_waitcnt lgkmcnt(3)\nv_mfma_f32_16x16x32_bf16 v[72:75], v[200:203], v[52:55], v[72:75]\n"
"buffer_load_dwordx4 v10, s[12:15], s53 offen lds\nv_mfma_f32_16x16x32_bf16 v[76:79], v[204:207], v[52:55], v[76:79]\nds_read_b128 v[52:55], v35 offset:20480\nglobal_load_dwordx4 v[240:243], v23, s[38:39] offset:0\n"
"s_waitcnt lgkmcnt(3)\nv_mfma_f32_16x16x32_bf16 v[64:67], v[208:211], v[40:43], v[64:67]\nv_mfma_f32_16x16x32_bf16 v[68:71], v[212:215], v[40:43], v[68:71]\nds_read_b128 v[40:43], v32 offset:24576\n"
"s_waitcnt lgkmcnt(3)\nv_mfma_f32_16x16x32_bf16 v[72:75], v[208:211], v[44:47], v[72:75]\nv_mfma_f32_16x16x32_bf16 v[76:79], v[212:215], v[44:47], v[76:79]\nds_read_b128 v[44:47], v33 offset:24576\n"
"s_waitcnt lgkmcnt(3)\nv_mfma_f32_16x16x32_bf16 v[64:67], v[216:219], v[48:51], v[64:67]\ns_add_u32 s54, s51, 32768\ns_cmp_eq_u32 s54, 163840\n"
"v_mfma_f32_16x16x32_bf16 v[68:71], v[220:223], v[48:51], v[68:71]\ns_cselect_b32 s54, 0, s54\ns_sub_u32 s54, s54, s51\nds_read_b128 v[48:51], v34 offset:24576\n"
"s_waitcnt lgkmcnt(3)\nv_mfma_f32_16x16x32_bf16 v[72:75], v[216:219], v[52:55], v[72:75]\nv_mfma_f32_16x16x32_bf16 v[76:79], v[220:223], v[52:55], v[76:79]\nds_read_b128 v[52:55], v35 offset:24576\n"
"s_waitcnt lgkmcnt(3)\nv_mfma_f32_16x16x32_bf16 v[64:67], v[224:227], v[40:43], v[64:67]\nv_add_u32 v32, s54, v32\nv_mfma_f32_16x16x32_bf16 v[68:71], v[228:231], v[40:43], v[68:71]\n"
"ds_read_b128 v[40:43], v32 offset:0\ns_waitcnt lgkmcnt(3)\nv_mfma_f32_16x16x32_bf16 v[72:75], v[224:227], v[44:47], v[72:75]\nv_add_u32 v33, s54, v33\n"
"v_mfma_f32_16x16x32_bf16 v[76:79], v[228:231], v[44:47], v[76:79]\nds_read_b128 v[44:47], v33 offset:0\ns_waitcnt lgkmcnt(3)\nv_mfma_f32_16x16x32_bf16 v[64:67], v[232:235], v[48:51], v[64:67]\n"
"v_add_u32 v34, s54, v34\nv_mfma_f32_16x16x32_bf16 v[68:71], v[236:239], v[48:51], v[68:71]\nds_read_b128 v[48:51], v34 offset:0\ns_waitcnt lgkmcnt(3)\n"
"v_mfma_f32_16x16x32_bf16 v[72:75], v[232:235], v[52:55], v[72:75]\nv_add_u32 v35, s54, v35\nv_mfma_f32_16x16x32_bf16 v[76:79], v[236:239], v[52:55], v[76:79]\nds_read_b128 v[52:55], v35 offset:0\n"
"s_mov_b32 s52, s47\ns_add_u32 s47, s51, s32\ns_add_u32 s51, s51, s54\ns_add_u32 s49, s49, 28672\n"
"s_waitcnt vmcnt(8)\ns_barrier\ns_waitcnt lgkmcnt(3)\nv_mfma_f32_16x16x32_bf16 v[96:99], v[128:131], v[40:43], 0\n"
"v_mov_b32 v36, s48\ns_add_u32 s48, s48, 1\ns_nop 0\nbuffer_store_dword v36, v19, s[24:27], s33 offen sc1\n"
"buffer_load_dword v37, v20, s[24:27], 0 offen sc1\nv_mfma_f32_16x16x32_bf16 v[100:103], v[132:135], v[40:43], 0\nds_read_b128 v[40:43], v32 offset:4096\ns_waitcnt lgkmcnt(3)\n"
"v_mfma_f32_16x16x32_bf16 v[104:107], v[128:131], v[44:47], 0\nv_mfma_f32_16x16x32_bf16 v[108:111], v[132:135], v[44:47], 0\nds_read_b128 v[44:47], v33 offset:4096\nv_cvt_pk_bf16_f32 v56, v64, v65\n"
"s_waitcnt lgkmcnt(3)\nv_mfma_f32_16x16x32_bf16 v[96:99], v[136:139], v[48:51], v[96:99]\nv_cvt_pk_bf16_f32 v57, v66, v67\nv_mfma_f32_16x16x32_bf16 v[100:103], v[140:143], v[48:51], v[100:103]\n"
"ds_read_b128 v[48:51], v34 offset:4096\nv_cvt_pk_bf16_f32 v58, v68, v69\ns_waitcnt lgkmcnt(3)\nv_mfma_f32_16x16x32_bf16 v[104:107], v[136:139], v[52:55], v[104:107]\n"
"v_cvt_pk_bf16_f32 v59, v70, v71\nv_mfma_f32_16x16x32_bf16 v[108:111], v[140:143], v[52:55], v[108:111]\nds_read_b128 v[52:55], v35 offset:4096\ns_waitcnt lgkmcnt(3)\n"
"v_mfma_f32_16x16x32_bf16 v[96:99], v[144:147], v[40:43], v[96:99]\nv_mfma_f32_16x16x32_bf16 v[100:103], v[148:151], v[40:43], v[100:103]\nds_read_b128 v[40:43], v32 offset:8192\nbuffer_store_dwordx4 v[56:59], v15, s[20:23], s50 offen sc1\n"
"s_waitcnt lgkmcnt(3)\nv_mfma_f32_16x16x32_bf16 v[104:107], v[144:147], v[44:47], v[104:107]\nv_cvt_pk_bf16_f32 v60, v72, v73\nv_mfma_f32_16x16x32_bf16 v[108:111], v[148:151], v[44:47], v[108:111]\n"
"ds_read_b128 v[44:47], v33 offset:8192\nv_cvt_pk_bf16_f32 v61, v74, v75\ns_waitcnt lgkmcnt(3)\nv_mfma_f32_16x16x32_bf16 v[96:99], v[152:155], v[48:51], v[96:99]\n"
"v_cvt_pk_bf16_f32 v62, v76, v77\nv_mfma_f32_16x16x32_bf16 v[100:103], v[156:159], v[48:51], v[100:103]\nds_read_b128 v[48:51], v34 offset:8192\nv_cvt_pk_bf16_f32 v63, v78, v79\n"
"s_waitcnt lgkmcnt(3)\nv_mfma_f32_16x16x32_bf16 v[104:107], v[152:155], v[52:55], v[104:107]\nv_mfma_f32_16x16x32_bf16 v[108:111], v[156:159], v[52:55], v[108:111]\nds_read_b128 v[52:55], v35 offset:8192\n"
"s_waitcnt lgkmcnt(3)\nv_mfma_f32_16x16x32_bf16 v[96:99], v[160:163], v[40:43], v[96:99]\nbuffer_store_dwordx4 v[60:63], v16, s[20:23], s50 offen sc1\nv_mfma_f32_16x16x32_bf16 v[100:103], v[164:167], v[40:43], v[100:103]\n"
"ds_read_b128 v[40:43], v32 offset:12288\ns_add_u32 s50, s50, s36\ns_waitcnt lgkmcnt(3)\nv_mfma_f32_16x16x32_bf16 v[104:107], v[160:163], v[44:47], v[104:107]\n"
"s_add_u32 m0, s52, 0\nv_mfma_f32_16x16x32_bf16 v[108:111], v[164:167], v[44:47], v[108:111]\nds_read_b128 v[44:47], v33 offset:12288\ns_add_u32 s53, s49, 0\n"
"s_waitcnt lgkmcnt(3)\nv_mfma_f32_16x16x32_bf16 v[96:99], v[168:171], v[48:51], v[96:99]\nbuffer_load_dwordx4 v10, s[12:15], s53 offen lds\nv_mfma_f32_16x16x32_bf16 v[100:103], v[172:175], v[48:51], v[100:103]\n"
"ds_read_b128 v[48:51], v34 offset:12288\ns_add_u32 m0, s52, 4096\ns_waitcnt lgkmcnt(3)\nv_mfma_f32_16x16x32_bf16 v[104:107], v[168:171], v[52:55], v[104:107]\n"
"s_add_u32 s53, s49, 128\nv_mfma_f32_16x16x32_bf16 v[108:111], v[172:175], v[52:55], v[108:111]\nds_read_b128 v[52:55], v35 offset:12288\nbuffer_load_dwordx4 v10, s[12:15], s53 offen lds\n"
"s_waitcnt lgkmcnt(3)\nv_mfma_f32_16x16x32_bf16 v[96:99], v[176:179], v[40:43], v[96:99]\ns_add_u32 m0, s52, 8192\nv_mfma_f32_16x16x32_bf16 v[100:103], v[180:183], v[40:43], v[100:103]\n"
"ds_read_b128 v[40:43], v32 offset:16384\ns_add_u32 s53, s49, 256\ns_waitcnt lgkmcnt(3)\nv_mfma_f32_16x16x32_bf16 v[104:107], v[176:179], v[44:47], v[104:107]\n"
"buffer_load_dwordx4 v10, s[12:15], s53 offen lds\nv_mfma_f32_16x16x32_bf16 v[108:111], v[180:183], v[44:47], v[108:111]\nds_read_b128 v[44:47], v33 offset:16384\ns_add_u32 m0, s52, 12288\n"
"s_waitcnt lgkmcnt(3)\nv_mfma_f32_16x16x32_bf16 v[96:99], v[184:187], v[48:51], v[96:99]\ns_add_u32 s53, s49, 384\nv_mfma_f32_16x16x32_bf16 v[100:103], v[188:191], v[48:51], v[100:103]\n"
"ds_read_b128 v[48:51], v34 offset:16384\nbuffer_load_dwordx4 v10, s[12:15], s53 offen lds\ns_waitcnt lgkmcnt(3)\nv_mfma_f32_16x16x32_bf16 v[104:107], v[184:187], v[52:55], v[104:107]\n"
"s_add_u32 m0, s52, 16384\nv_mfma_f32_16x16x32_bf16 v[108:111], v[188:191], v[52:55], v[108:111]\nds_read_b128 v[52:55], v35 offset:16384\ns_add_u32 s53, s49, 512\n"
"s_waitcnt lgkmcnt(3)\nv_mfma_f32_16x16x32_bf16 v[96:99], v[192:195], v[40:43], v[96:99]\nbuffer_load_dwordx4 v10, s[12:15], s53 offen lds\nv_mfma_f32_16x16x32_bf16 v[100:103], v[196:199], v[40:43], v[100:103]\n"
"ds_read_b128 v[40:43], v32 offset:20480\ns_add_u32 m0, s52, 20480\ns_waitcnt lgkmcnt(3)\nv_mfma_f32_16x16x32_bf16 v[104:107], v[192:195], v[44:47], v[104:107]\n"
"s_add_u32 s53, s49, 640\nv_mfma_f32_16x16x32_bf16 v[108:111], v[196:199], v[44:47], v[108:111]\nds_read_b128 v[44:47], v33 offset:20480\nbuffer_load_dwordx4 v10, s[12:15], s53 offen lds\n"
"s_waitcnt lgkmcnt(3)\nv_mfma_f32_16x16x32_bf16 v[96:99], v[200:203], v[48:51], v[96:99]\ns_add_u32 m0, s52, 24576\nv_mfma_f32_16x16x32_bf16 v[100:103], v[204:207], v[48:51], v[100:103]\n"
"ds_read_b128 v[48:51], v34 offset:20480\ns_add_u32 s53, s49, 768\ns_waitcnt lgkmcnt(3)\nv_mfma_f32_16x16x32_bf16 v[104:107], v[200:203], v[52:55], v[104:107]\n"
"buffer_load_dwordx4 v10, s[12:15], s53 offen lds\nv_mfma_f32_16x16x32_bf16 v[108:111], v[204:207], v[52:55], v[108:111]\nds_read_b128 v[52:55], v35 offset:20480\nglobal_load_dwordx4 v[240:243], v23, s[38:39] offset:1024\n"
"s_waitcnt lgkmcnt(3)\nv_mfma_f32_16x16x32_bf16 v[96:99], v[208:211], v[40:43], v[96:99]\nv_mfma_f32_16x16x32_bf16 v[100:103], v[212:215], v[40:43], v[100:103]\nds_read_b128 v[40:43], v32 offset:24576\n"
"s_waitcnt lgkmcnt(3)\nv_mfma_f32_16x16x32_bf16 v[104:107], v[208:211], v[44:47], v[104:107]\nv_mfma_f32_16x16x32_bf16 v[108:111], v[212:215], v[44:47], v[108:111]\nds_read_b128 v[44:47], v33 offset:24576\n"
"s_waitcnt lgkmcnt(3)\nv_mfma_f32_16x16x32_bf16 v[96:99], v[216:219], v[48:51], v[96:99]\ns_add_u32 s54, s51, 32768\ns_cmp_eq_u32 s54, 163840\n"
"v_mfma_f32_16x16x32_bf16 v[100:103], v[220:223], v[48:51], v[100:103]\ns_cselect_b32 s54, 0, s54\ns_sub_u32 s54, s54, s51\nds_read_b128 v[48:51], v34 offset:24576\n"
"s_waitcnt lgkmcnt(3)\nv_mfma_f32_16x16x32_bf16 v[104:107], v[216:219], v[52:55], v[104:107]\nv_mfma_f32_16x16x32_bf16 v[108:111], v[220:223], v[52:55], v[108:111]\nds_read_b128 v[52:55], v35 offset:24576\n"
"s_waitcnt lgkmcnt(3)\nv_mfma_f32_16x16x32_bf16 v[96:99], v[224:227], v[40:43], v[96:99]\nv_add_u32 v32, s54, v32\nv_mfma_f32_16x16x32_bf16 v[100:103], v[228:231], v[40:43], v[100:103]\n"
"ds_read_b128 v[40:43], v32 offset:0\ns_waitcnt lgkmcnt(3)\nv_mfma_f32_16x16x32_bf16 v[104:107], v[224:227], v[44:47], v[104:107]\nv_add_u32 v33, s54, v33\n"
"v_mfma_f32_16x16x32_bf16 v[108:111], v[228:231], v[44:47], v[108:111]\nds_read_b128 v[44:47], v33 offset:0\ns_waitcnt lgkmcnt(3)\nv_mfma_f32_16x16x32_bf16 v[96:99], v[232:235], v[48:51], v[96:99]\n"
"v_add_u32 v34, s54, v34\nv_mfma_f32_16x16x32_bf16 v[100:103], v[236:239], v[48:51], v[100:103]\nds_read_b128 v[48:51], v34 offset:0\ns_waitcnt lgkmcnt(3)\n"
"v_mfma_f32_16x16x32_bf16 v[104:107], v[232:235], v[52:55], v[104:107]\nv_add_u32 v35, s54, v35\nv_mfma_f32_16x16x32_bf16 v[108:111], v[236:239], v[52:55], v[108:111]\nds_read_b128 v[52:55], v35 offset:0\n"
"s_mov_b32 s52, s47\ns_add_u32 s47, s51, s32\ns_add_u32 s51, s51, s54\ns_add_u32 s49, s49, 28672\n"
"s_waitcnt vmcnt(8)\nfg0_pf:\nv_cmp_ne_u32 vcc, s28, v37\ns_cbranch_vccz fg0_pfok\n"
"s_add_u32 s55, s55, 1\ns_cmp_gt_u32 s55, 262144\ns_cbranch_scc0 fg0_pf2\ns_endpgm\n"
"fg0_pf2:\ns_sleep 1\nbuffer_load_dword v37, v20, s[24:27], 0 offen sc1\ns_waitcnt vmcnt(0)\n"
"s_branch fg0_pf\nfg0_pfok:\ns_barrier\ns_waitcnt lgkmcnt(3)\n"
"v_mfma_f32_16x16x32_bf16 v[64:67], v[128:131], v[40:43], 0\nv_mov_b32 v36, s48\ns_add_u32 s48, s48, 1\ns_nop 0\n"
"buffer_store_dword v36, v19, s[24:27], s33 offen sc1\nv_mfma_f32_16x16x32_bf16 v[68:71], v[132:135], v[40:43], 0\nds_read_b128 v[40:43], v32 offset:4096\ns_waitcnt lgkmcnt(3)\n"
"v_mfma_f32_16x16x32_bf16 v[72:75], v[128:131], v[44:47], 0\nv_mfma_f32_16x16x32_bf16 v[76:79], v[132:135], v[44:47], 0\nds_read_b128 v[44:47], v33 offset:4096\nv_cvt_pk_bf16_f32 v56, v96, v97\n"
"s_waitcnt lgkmcnt(3)\nv_mfma_f32_16x16x32_bf16 v[64:67], v[136:139], v[48:51], v[64:67]\nv_cvt_pk_bf16_f32 v57, v98, v99\nv_mfma_f32_16x16x32_bf16 v[68:71], v[140:143], v[48:51], v[68:71]\n"
"ds_read_b128 v[48:51], v34 offset:4096\nv_cvt_pk_bf16_f32 v58, v100, v101\ns_waitcnt lgkmcnt(3)\nv_mfma_f32_16x16x32_bf16 v[72:75], v[136:139], v[52:55], v[72:75]\n"
"v_cvt_pk_bf16_f32 v59, v102, v103\nv_mfma_f32_16x16x32_bf16 v[76:79], v[140:143], v[52:55], v[76:79]\nds_read_b128 v[52:55], v35 offset:4096\ns_waitcnt lgkmcnt(3)\n"
"v_mfma_f32_16x16x32_bf16 v[64:67], v[144:147], v[40:43], v[64:67]\nv_mfma_f32_16x16x32_bf16 v[68:71], v[148:151], v[40:43], v[68:71]\nds_read_b128 v[40:43], v32 offset:8192\nbuffer_store_dwordx4 v[56:59], v15, s[20:23], s50 offen sc1\n"
"s_waitcnt lgkmcnt(3)\nv_mfma_f32_16x16x32_bf16 v[72:75], v[144:147], v[44:47], v[72:75]\nv_cvt_pk_bf16_f32 v60, v104, v105\nv_mfma_f32_16x16x32_bf16 v[76:79], v[148:151], v[44:47], v[76:79]\n"
"ds_read_b128 v[44:47], v33 offset:8192\nv_cvt_pk_bf16_f32 v61, v106, v107\ns_waitcnt lgkmcnt(3)\nv_mfma_f32_16x16x32_bf16 v[64:67], v[152:155], v[48:51], v[64:67]\n"
"v_cvt_pk_bf16_f32 v62, v108, v109\nv_mfma_f32_16x16x32_bf16 v[68:71], v[156:159], v[48:51], v[68:71]\nds_read_b128 v[48:51], v34 offset:8192\nv_cvt_pk_bf16_f32 v63, v110, v111\n"
"s_waitcnt lgkmcnt(3)\nv_mfma_f32_16x16x32_bf16 v[72:75], v[152:155], v[52:55], v[72:75]\nv_mfma_f32_16x16x32_bf16 v[76:79], v[156:159], v[52:55], v[76:79]\nds_read_b128 v[52:55], v35 offset:8192\n"
"s_waitcnt lgkmcnt(3)\nv_mfma_f32_16x16x32_bf16 v[64:67], v[160:163], v[40:43], v[64:67]\nbuffer_store_dwordx4 v[60:63], v16, s[20:23], s50 offen sc1\nv_mfma_f32_16x16x32_bf16 v[68:71], v[164:167], v[40:43], v[68:71]\n"
"ds_read_b128 v[40:43], v32 offset:12288\ns_add_u32 s50, s50, s36\ns_waitcnt lgkmcnt(3)\nv_mfma_f32_16x16x32_bf16 v[72:75], v[160:163], v[44:47], v[72:75]\n"
"s_add_u32 m0, s52, 0\nv_mfma_f32_16x16x32_bf16 v[76:79], v[164:167], v[44:47], v[76:79]\nds_read_b128 v[44:47], v33 offset:12288\ns_add_u32 s53, s49, 0\n"
"s_waitcnt lgkmcnt(3)\nv_mfma_f32_16x16x32_bf16 v[64:67], v[168:171], v[48:51], v[64:67]\nbuffer_load_dwordx4 v10, s[12:15], s53 offen lds\nv_mfma_f32_16x16x32_bf16 v[68:71], v[172:175], v[48:51], v[68:71]\n"
"ds_read_b128 v[48:51], v34 offset:12288\ns_add_u32 m0, s52, 4096\ns_waitcnt lgkmcnt(3)\nv_mfma_f32_16x16x32_bf16 v[72:75], v[168:171], v[52:55], v[72:75]\n"
"s_add_u32 s53, s49, 128\nv_mfma_f32_16x16x32_bf16 v[76:79], v[172:175], v[52:55], v[76:79]\nds_read_b128 v[52:55], v35 offset:12288\nbuffer_load_dwordx4 v10, s[12:15], s53 offen lds\n"
"s_waitcnt lgkmcnt(3)\nv_mfma_f32_16x16x32_bf16 v[64:67], v[176:179], v[40:43], v[64:67]\ns_add_u32 m0, s52, 8192\nv_mfma_f32_16x16x32_bf16 v[68:71], v[180:183], v[40:43], v[68:71]\n"
"ds_read_b128 v[40:43], v32 offset:16384\ns_add_u32 s53, s49, 256\ns_waitcnt lgkmcnt(3)\nv_mfma_f32_16x16x32_bf16 v[72:75], v[176:179], v[44:47], v[72:75]\n"
"buffer_load_dwordx4 v10, s[12:15], s53 offen lds\nv_mfma_f32_16x16x32_bf16 v[76:79], v[180:183], v[44:47], v[76:79]\nds_read_b128 v[44:47], v33 offset:16384\ns_add_u32 m0, s52, 12288\n"
"s_waitcnt lgkmcnt(3)\nv_mfma_f32_16x16x32_bf16 v[64:67], v[184:187], v[48:51], v[64:67]\ns_add_u32 s53, s49, 384\nv_mfma_f32_16x16x32_bf16 v[68:71], v[188:191], v[48:51], v[68:71]\n"
"ds_read_b128 v[48:51], v34 offset:16384\nbuffer_load_dwordx4 v10, s[12:15], s53 offen lds\ns_waitcnt lgkmcnt(3)\nv_mfma_f32_16x16x32_bf16 v[72:75], v[184:187], v[52:55], v[72:75]\n"
"s_add_u32 m0, s52, 16384\nv_mfma_f32_16x16x32_bf16 v[76:79], v[188:191], v[52:55], v[76:79]\nds_read_b128 v[52:55], v35 offset:16384\ns_add_u32 s53, s49, 512\n"
"s_waitcnt lgkmcnt(3)\nv_mfma_f32_16x16x32_bf16 v[64:67], v[192:195], v[40:43], v[64:67]\nbuffer_load_dwordx4 v10, s[12:15], s53 offen lds\nv_mfma_f32_16x16x32_bf16 v[68:71], v[196:199], v[40:43], v[68:71]\n"
"ds_read_b128 v[40:43], v32 offset:20480\ns_add_u32 m0, s52, 20480\ns_waitcnt lgkmcnt(3)\nv_mfma_f32_16x16x32_bf16 v[72:75], v[192:195], v[44:47], v[72:75]\n"
"s_add_u32 s53, s49, 640\nv_mfma_f32_16x16x32_bf16 v[76:79], v[196:199], v[44:47], v[76:79]\nds_read_b128 v[44:47], v33 offset:20480\nbuffer_load_dwordx4 v10, s[12:15], s53 offen lds\n"
"s_waitcnt lgkmcnt(3)\nv_mfma_f32_16x16x32_bf16 v[64:67], v[200:203], v[48:51], v[64:67]\ns_add_u32 m0, s52, 24576\nv_mfma_f32_16x16x32_bf16 v[68:71], v[204:207], v[48:51], v[68:71]\n"
"ds_read_b128 v[48:51], v34 offset:20480\ns_add_u32 s53, s49, 768\ns_waitcnt lgkmcnt(3)\nv_mfma_f32_16x16x32_bf16 v[72:75], v[200:203], v[52:55], v[72:75]\n"
"buffer_load_dwordx4 v10, s[12:15], s53 offen lds\nv_mfma_f32_16x16x32_bf16 v[76:79], v[204:207], v[52:55], v[76:79]\nds_read_b128 v[52:55], v35 offset:20480\nglobal_load_dwordx4 v[240:243], v23, s[38:39] offset:2048\n"
"s_waitcnt lgkmcnt(3)\nv_mfma_f32_16x16x32_bf16 v[64:67], v[208:211], v[40:43], v[64:67]\ns_mov_b32 s53, 0\nbuffer_load_dwordx4 a[0:3], v14, s[16:19], s53 offen sc1\n"
"v_mfma_f32_16x16x32_bf16 v[68:71], v[212:215], v[40:43], v[68:71]\nds_read_b128 v[40:43], v32 offset:24576\ns_mov_b32 s53, 14336\nbuffer_load_dwordx4 a[4:7], v14, s[16:19], s53 offen sc1\n"
"s_waitcnt lgkmcnt(3)\nv_mfma_f32_16x16x32_bf16 v[72:75], v[208:211], v[44:47], v[72:75]\ns_mov_b32 s53, 1024\nbuffer_load_dwordx4 a[8:11], v14, s[16:19], s53 offen sc1\n"
"v_mfma_f32_16x16x32_bf16 v[76:79], v[212:215], v[44:47], v[76:79]\nds_read_b128 v[44:47], v33 offset:24576\ns_mov_b32 s53, 15360\nbuffer_load_dwordx4 a[12:15], v14, s[16:19], s53 offen sc1\n"
"s_waitcnt lgkmcnt(3)\nv_mfma_f32_16x16x32_bf16 v[64:67], v[216:219], v[48:51], v[64:67]\ns_add_u32 s54, s51, 32768\ns_cmp_eq_u32 s54, 163840\n"
"v_mfma_f32_16x16x32_bf16 v[68:71], v[220:223], v[48:51], v[68:71]\ns_cselect_b32 s54, 0, s54\ns_sub_u32 s54, s54, s51\nds_read_b128 v[48:51], v34 offset:24576\n"
"s_mov_b32 s53, 2048\nbuffer_load_dwordx4 a[16:19], v14, s[16:19], s53 offen sc1\ns_waitcnt lgkmcnt(3)\nv_mfma_f32_16x16x32_bf16 v[72:75], v[216:219], v[52:55], v[72:75]\n"
"v_mfma_f32_16x16x32_bf16 v[76:79], v[220:223], v[52:55], v[76:79]\nds_read_b128 v[52:55], v35 offset:24576\ns_waitcnt lgkmcnt(3)\nv_mfma_f32_16x16x32_bf16 v[64:67], v[224:227], v[40:43], v[64:67]\n"
"v_add_u32 v32, s54, v32\nv_mfma_f32_16x16x32_bf16 v[68:71], v[228:231], v[40:43], v[68:71]\nds_read_b128 v[40:43], v32 offset:0\ns_waitcnt lgkmcnt(3)\n"
"v_mfma_f32_16x16x32_bf16 v[72:75], v[224:227], v[44:47], v[72:75]\nv_add_u32 v33, s54, v33\nv_mfma_f32_16x16x32_bf16 v[76:79], v[228:231], v[44:47], v[76:79]\nds_read_b128 v[44:47], v33 offset:0\n"
"s_waitcnt lgkmcnt(3)\nv_mfma_f32_16x16x32_bf16 v[64:67], v[232:235], v[48:51], v[64:67]\nv_add_u32 v34, s54, v34\nv_mfma_f32_16x16x32_bf16 v[68:71], v[236:239], v[48:51], v[68:71]\n"
"ds_read_b128 v[48:51], v34 offset:0\ns_waitcnt lgkmcnt(3)\nv_mfma_f32_16x16x32_bf16 v[72:75], v[232:235], v[52:55], v[72:75]\nv_add_u32 v35, s54, v35\n"
"v_mfma_f32_16x16x32_bf16 v[76:79], v[236:239], v[52:55], v[76:79]\nds_read_b128 v[52:55], v35 offset:0\ns_mov_b32 s52, s47\ns_add_u32 s47, s51, s32\n"
"s_add_u32 s51, s51, s54\ns_add_u32 s49, s49, 28672\ns_waitcnt vmcnt(13)\ns_barrier\n"
"s_waitcnt lgkmcnt(3)\nv_mfma_f32_16x16x32_bf16 v[96:99], v[128:131], v[40:43], 0\nv_mov_b32 v36, s48\ns_add_u32 s48, s48, 1\n"
"s_nop 0\nbuffer_store_dword v36, v19, s[24:27], s33 offen sc1\nv_mfma_f32_16x16x32_bf16 v[100:103], v[132:135], v[40:43], 0\nds_read_b128 v[40:43], v32 offset:4096\n"
"s_waitcnt lgkmcnt(3)\nv_mfma_f32_16x16x32_bf16 v[104:107], v[128:131], v[44:47], 0\nv_mfma_f32_16x16x32_bf16 v[108:111], v[132:135], v[44:47], 0\nds_read_b128 v[44:47], v33 offset:4096\n"
"v_cvt_pk_bf16_f32 v56, v64, v65\ns_waitcnt lgkmcnt(3)\nv_mfma_f32_16x16x32_bf16 v[96:99], v[136:139], v[48:51], v[96:99]\nv_cvt_pk_bf16_f32 v57, v66, v67\n"
"v_mfma_f32_16x16x32_bf16 v[100:103], v[140:143], v[48:51], v[100:103]\nds_read_b128 v[48:51], v34 offset:4096\nv_cvt_pk_bf16_f32 v58, v68, v69\ns_waitcnt lgkmcnt(3)\n"
"v_mfma_f32_16x16x32_bf16 v[104:107], v[136:139], v[52:55], v[104:107]\nv_cvt_pk_bf16_f32 v59, v70, v71\nv_mfma_f32_16x16x32_bf16 v[108:111], v[140:143], v[52:55], v[108:111]\nds_read_b128 v[52:55], v35 offset:4096\n"
"s_waitcnt lgkmcnt(3)\nv_mfma_f32_16x16x32_bf16 v[96:99], v[144:147], v[40:43], v[96:99]\nv_mfma_f32_16x16x32_bf16 v[100:103], v[148:151], v[40:43], v[100:103]\nds_read_b128 v[40:43], v32 offset:8192\n"
"buffer_store_dwordx4 v[56:59], v15, s[20:23], s50 offen sc1\ns_waitcnt lgkmcnt(3)\nv_mfma_f32_16x16x32_bf16 v[104:107], v[144:147], v[44:47], v[104:107]\nv_cvt_pk_bf16_f32 v60, v72, v73\n"
"v_mfma_f32_16x16x32_bf16 v[108:111], v[148:151], v[44:47], v[108:111]\nds_read_b128 v[44:47], v33 offset:8192\nv_cvt_pk_bf16_f32 v61, v74, v75\ns_waitcnt lgkmcnt(3)\n"
"v_mfma_f32_16x16x32_bf16 v[96:99], v[152:155], v[48:51], v[96:99]\nv_cvt_pk_bf16_f32 v62, v76, v77\nv_mfma_f32_16x16x32_bf16 v[100:103], v[156:159], v[48:51], v[100:103]\nds_read_b128 v[48:51], v34 offset:8192\n"
"v_cvt_pk_bf16_f32 v63, v78, v79\ns_waitcnt lgkmcnt(3)\nv_mfma_f32_16x16x32_bf16 v[104:107], v[152:155], v[52:55], v[104:107]\nv_mfma_f32_16x16x32_bf16 v[108:111], v[156:159], v[52:55], v[108:111]\n"
"ds_read_b128 v[52:55], v35 offset:8192\ns_waitcnt lgkmcnt(3)\nv_mfma_f32_16x16x32_bf16 v[96:99], v[160:163], v[40:43], v[96:99]\nbuffer_store_dwordx4 v[60:63], v16, s[20:23], s50 offen sc1\n"
"v_mfma_f32_16x16x32_bf16 v[100:103], v[164:167], v[40:43], v[100:103]\nds_read_b128 v[40:43], v32 offset:12288\ns_add_u32 s50, s50, s36\ns_waitcnt lgkmcnt(3)\n"
"v_mfma_f32_16x16x32_bf16 v[104:107], v[160:163], v[44:47], v[104:107]\ns_add_u32 m0, s52, 0\nv_mfma_f32_16x16x32_bf16 v[108:111], v[164:167], v[44:47], v[108:111]\nds_read_b128 v[44:47], v33 offset:12288\n"
"s_add_u32 s53, s49, 0\ns_waitcnt lgkmcnt(3)\nv_mfma_f32_16x16x32_bf16 v[96:99], v[168:171], v[48:51], v[96:99]\nbuffer_load_dwordx4 v10, s[12:15], s53 offen lds\n"
"v_mfma_f32_16x16x32_bf16 v[100:103], v[172:175], v[48:51], v[100:103]\nds_read_b128 v[48:51], v34 offset:12288\ns_add_u32 m0, s52, 4096\ns_waitcnt lgkmcnt(3)\n"
"v_mfma_f32_16x16x32_bf16 v[104:107], v[168:171], v[52:55], v[104:107]\ns_add_u32 s53, s49, 128\nv_mfma_f32_16x16x32_bf16 v[108:111], v[172:175], v[52:55], v[108:111]\nds_read_b128 v[52:55], v35 offset:12288\n"
"buffer_load_dwordx4 v10, s[12:15], s53 offen lds\ns_waitcnt lgkmcnt(3)\nv_mfma_f32_16x16x32_bf16 v[96:99], v[176:179], v[40:43], v[96:99]\ns_add_u32 m0, s52, 8192\n"
"v_mfma_f32_16x16x32_bf16 v[100:103], v[180:183], v[40:43], v[100:103]\nds_read_b128 v[40:43], v32 offset:16384\ns_add_u32 s53, s49, 256\ns_waitcnt lgkmcnt(3)\n"
"v_mfma_f32_16x16x32_bf16 v[104:107], v[176:179], v[44:47], v[104:107]\nbuffer_load_dwordx4 v10, s[12:15], s53 offen lds\nv_mfma_f32_16x16x32_bf16 v[108:111], v[180:183], v[44:47], v[108:111]\nds_read_b128 v[44:47], v33 offset:16384\n"
"s_add_u32 m0, s52, 12288\ns_waitcnt lgkmcnt(3)\nv_mfma_f32_16x16x32_bf16 v[96:99], v[184:187], v[48:51], v[96:99]\ns_add_u32 s53, s49, 384\n"
"v_mfma_f32_16x16x32_bf16 v[100:103], v[188:191], v[48:51], v[100:103]\nds_read_b128 v[48:51], v34 offset:16384\nbuffer_load_dwordx4 v10, s[12:15], s53 offen lds\ns_waitcnt lgkmcnt(3)\n"
"v_mfma_f32_16x16x32_bf16 v[104:107], v[184:187], v[52:55], v[104:107]\ns_add_u32 m0, s52, 16384\nv_mfma_f32_16x16x32_bf16 v[108:111], v[188:191], v[52:55], v[108:111]\nds_read_b128 v[52:55], v35 offset:16384\n"
"s_add_u32 s53, s49, 512\ns_waitcnt lgkmcnt(3)\nv_mfma_f32_16x16x32_bf16 v[96:99], v[192:195], v[40:43], v[96:99]\nbuffer_load_dwordx4 v10, s[12:15], s53 offen lds\n"
"v_mfma_f32_16x16x32_bf16 v[100:103], v[196:199], v[40:43], v[100:103]\nds_read_b128 v[40:43], v32 offset:20480\ns_add_u32 m0, s52, 20480\ns_waitcnt lgkmcnt(3)\n"
"v_mfma_f32_16x16x32_bf16 v[104:107], v[192:195], v[44:47], v[104:107]\ns_add_u32 s53, s49, 640\nv_mfma_f32_16x16x32_bf16 v[108:111], v[196:199], v[44:47], v[108:111]\nds_read_b128 v[44:47], v33 offset:20480\n"
"buffer_load_dwordx4 v10, s[12:15], s53 offen lds\ns_waitcnt lgkmcnt(3)\nv_mfma_f32_16x16x32_bf16 v[96:99], v[200:203], v[48:51], v[96:99]\ns_add_u32 m0, s52, 24576\n"
"v_mfma_f32_16x16x32_bf16 v[100:103], v[204:207], v[48:51], v[100:103]\nds_read_b128 v[48:51], v34 offset:20480\ns_add_u32 s53, s49, 768\ns_waitcnt lgkmcnt(3)\n"
"v_mfma_f32_16x16x32_bf16 v[104:107], v[200:203], v[52:55], v[104:107]\nbuffer_load_dwordx4 v10, s[12:15], s53 offen lds\nv_mfma_f32_16x16x32_bf16 v[108:111], v[204:207], v[52:55], v[108:111]\nds_read_b128 v[52:55], v35 offset:20480\n"
"global_load_dwordx4 v[240:243], v23, s[38:39] offset:3072\ns_waitcnt lgkmcnt(3)\nv_mfma_f32_16x16x32_bf16 v[96:99], v[208:211], v[40:43], v[96:99]\ns_mov_b32 s53, 16384\n"
"buffer_load_dwordx4 a[20:23], v14, s[16:19], s53 offen sc1\nv_mfma_f32_16x16x32_bf16 v[100:103], v[212:215], v[40:43], v[100:103]\nds_read_b128 v[40:43], v32 offset:24576\ns_mov_b32 s53, 3072\n"
"buffer_load_dwordx4 a[24:27], v14, s[16:19], s53 offen sc1\ns_waitcnt lgkmcnt(3)\nv_mfma_f32_16x16x32_bf16 v[104:107], v[208:211], v[44:47], v[104:107]\ns_mov_b32 s53, 17408\n"
"buffer_load_dwordx4 a[28:31], v14, s[16:19], s53 offen sc1\nv_mfma_f32_16x16x32_bf16 v[108:111], v[212:215], v[44:47], v[108:111]\nds_read_b128 v[44:47], v33 offset:24576\ns_mov_b32 s53, 4096\n"
"buffer_load_dwordx4 a[32:35], v14, s[16:19], s53 offen sc1\ns_waitcnt lgkmcnt(3)\nv_mfma_f32_16x16x32_bf16 v[96:99], v[216:219], v[48:51], v[96:99]\ns_add_u32 s54, s51, 32768\n"
"s_cmp_eq_u32 s54, 163840\nv_mfma_f32_16x16x32_bf16 v[100:103], v[220:223], v[48:51], v[100:103]\ns_cselect_b32 s54, 0, s54\ns_sub_u32 s54, s54, s51\n"
"ds_read_b128 v[48:51], v34 offset:24576\ns_mov_b32 s53, 18432\nbuffer_load_dwordx4 a[36:39], v14, s[16:19], s53 offen sc1\ns_waitcnt lgkmcnt(3)\n"
"v_mfma_f32_16x16x32_bf16 v[104:107], v[216:219], v[52:55], v[104:107]\nv_mfma_f32_16x16x32_bf16 v[108:111], v[220:223], v[52:55], v[108:111]\nds_read_b128 v[52:55], v35 offset:24576\ns_waitcnt lgkmcnt(3)\n"
"v_mfma_f32_16x16x32_bf16 v[96:99], v[224:227], v[40:43], v[96:99]\nv_add_u32 v32, s54, v32\nv_mfma_f32_16x16x32_bf16 v[100:103], v[228:231], v[40:43], v[100:103]\nds_read_b128 v[40:43], v32 offset:0\n"
"s_waitcnt lgkmcnt(3)\nv_mfma_f32_16x16x32_bf16 v[104:107], v[224:227], v[44:47], v[104:107]\nv_add_u32 v33, s54, v33\nv_mfma_f32_16x16x32_bf16 v[108:111], v[228:231], v[44:47], v[108:111]\n"
"ds_read_b128 v[44:47], v33 offset:0\ns_waitcnt lgkmcnt(3)\nv_mfma_f32_16x16x32_bf16 v[96:99], v[232:235], v[48:51], v[96:99]\nv_add_u32 v34, s54, v34\n"
"v_mfma_f32_16x16x32_bf16 v[100:103], v[236:239], v[48:51], v[100:103]\nds_read_b128 v[48:51], v34 offset:0\ns_waitcnt lgkmcnt(3)\nv_mfma_f32_16x16x32_bf16 v[104:107], v[232:235], v[52:55], v[104:107]\n"
"v_add_u32 v35, s54, v35\nv_mfma_f32_16x16x32_bf16 v[108:111], v[236:239], v[52:55], v[108:111]\nds_read_b128 v[52:55], v35 offset:0\ns_mov_b32 s52, s47\n"
"s_add_u32 s47, s51, s32\ns_add_u32 s51, s51, s54\ns_add_u32 s49, s49, 28672\ns_waitcnt vmcnt(13)\n"
"s_barrier\ns_waitcnt lgkmcnt(3)\nv_mfma_f32_16x16x32_bf16 v[64:67], v[128:131], v[40:43], 0\nv_mov_b32 v36, s48\n"
"s_add_u32 s48, s48, 1\ns_nop 0\nbuffer_store_dword v36, v19, s[24:27], s33 offen sc1\nv_mfma_f32_16x16x32_bf16 v[68:71], v[132:135], v[40:43], 0\n"
"ds_read_b128 v[40:43], v32 offset:4096\ns_waitcnt lgkmcnt(3)\nv_mfma_f32_16x16x32_bf16 v[72:75], v[128:131], v[44:47], 0\nv_mfma_f32_16x16x32_bf16 v[76:79], v[132:135], v[44:47], 0\n"
"ds_read_b128 v[44:47], v33 offset:4096\nv_cvt_pk_bf16_f32 v56, v96, v97\ns_waitcnt lgkmcnt(3)\nv_mfma_f32_16x16x32_bf16 v[64:67], v[136:139], v[48:51], v[64:67]\n"
"v_cvt_pk_bf16_f32 v57, v98, v99\nv_mfma_f32_16x16x32_bf16 v[68:71], v[140:143], v[48:51], v[68:71]\nds_read_b128 v[48:51], v34 offset:4096\nv_cvt_pk_bf16_f32 v58, v100, v101\n"
"s_waitcnt lgkmcnt(3)\nv_mfma_f32_16x16x32_bf16 v[72:75], v[136:139], v[52:55], v[72:75]\nv_cvt_pk_bf16_f32 v59, v102, v103\nv_mfma_f32_16x16x32_bf16 v[76:79], v[140:143], v[52:55], v[76:79]\n"
"ds_read_b128 v[52:55], v35 offset:4096\ns_waitcnt lgkmcnt(3)\nv_mfma_f32_16x16x32_bf16 v[64:67], v[144:147], v[40:43], v[64:67]\nv_mfma_f32_16x16x32_bf16 v[68:71], v[148:151], v[40:43], v[68:71]\n"
"ds_read_b128 v[40:43], v32 offset:8192\nbuffer_store_dwordx4 v[56:59], v15, s[20:23], s50 offen sc1\ns_waitcnt lgkmcnt(3)\nv_mfma_f32_16x16x32_bf16 v[72:75], v[144:147], v[44:47], v[72:75]\n"
"v_cvt_pk_bf16_f32 v60, v104, v105\nv_mfma_f32_16x16x32_bf16 v[76:79], v[148:151], v[44:47], v[76:79]\nds_read_b128 v[44:47], v33 offset:8192\nv_cvt_pk_bf16_f32 v61, v106, v107\n"
"s_waitcnt lgkmcnt(3)\nv_mfma_f32_16x16x32_bf16 v[64:67], v[152:155], v[48:51], v[64:67]\nv_cvt_pk_bf16_f32 v62, v108, v109\nv_mfma_f32_16x16x32_bf16 v[68:71], v[156:159], v[48:51], v[68:71]\n"
"ds_read_b128 v[48:51], v34 offset:8192\nv_cvt_pk_bf16_f32 v63, v110, v111\ns_waitcnt lgkmcnt(3)\nv_mfma_f32_16x16x32_bf16 v[72:75], v[152:155], v[52:55], v[72:75]\n"
"v_mfma_f32_16x16x32_bf16 v[76:79], v[156:159], v[52:55], v[76:79]\nds_read_b128 v[52:55], v35 offset:8192\ns_waitcnt lgkmcnt(3)\nv_mfma_f32_16x16x32_bf16 v[64:67], v[160:163], v[40:43], v[64:67]\n"
"buffer_store_dwordx4 v[60:63], v16, s[20:23], s50 offen sc1\nv_mfma_f32_16x16x32_bf16 v[68:71], v[164:167], v[40:43], v[68:71]\nds_read_b128 v[40:43], v32 offset:12288\ns_add_u32 s50, s50, s36\n"
"s_waitcnt lgkmcnt(3)\nv_mfma_f32_16x16x32_bf16 v[72:75], v[160:163], v[44:47], v[72:75]\ns_add_u32 m0, s52, 0\nv_mfma_f32_16x16x32_bf16 v[76:79], v[164:167], v[44:47], v[76:79]\n"
"ds_read_b128 v[44:47], v33 offset:12288\ns_add_u32 s53, s49, 0\ns_waitcnt lgkmcnt(3)\nv_mfma_f32_16x16x32_bf16 v[64:67], v[168:171], v[48:51], v[64:67]\n"
"buffer_load_dwordx4 v10, s[12:15], s53 offen lds\nv_mfma_f32_16x16x32_bf16 v[68:71], v[172:175], v[48:51], v[68:71]\nds_read_b128 v[48:51], v34 offset:12288\ns_add_u32 m0, s52, 4096\n"
"s_waitcnt lgkmcnt(3)\nv_mfma_f32_16x16x32_bf16 v[72:75], v[168:171], v[52:55], v[72:75]\ns_add_u32 s53, s49, 128\nv_mfma_f32_16x16x32_bf16 v[76:79], v[172:175], v[52:55], v[76:79]\n"
"ds_read_b128 v[52:55], v35 offset:12288\nbuffer_load_dwordx4 v10, s[12:15], s53 offen lds\ns_waitcnt lgkmcnt(3)\nv_mfma_f32_16x16x32_bf16 v[64:67], v[176:179], v[40:43], v[64:67]\n"
"s_add_u32 m0, s52, 8192\nv_mfma_f32_16x16x32_bf16 v[68:71], v[180:183], v[40:43], v[68:71]\nds_read_b128 v[40:43], v32 offset:16384\ns_add_u32 s53, s49, 256\n"
"s_waitcnt lgkmcnt(3)\nv_mfma_f32_16x16x32_bf16 v[72:75], v[176:179], v[44:47], v[72:75]\nbuffer_load_dwordx4 v10, s[12:15], s53 offen lds\nv_mfma_f32_16x16x32_bf16 v[76:79], v[180:183], v[44:47], v[76:79]\n"
"ds_read_b128 v[44:47], v33 offset:16384\ns_add_u32 m0, s52, 12288\ns_waitcnt lgkmcnt(3)\nv_mfma_f32_16x16x32_bf16 v[64:67], v[184:187], v[48:51], v[64:67]\n"
"s_add_u32 s53, s49, 384\nv_mfma_f32_16x16x32_bf16 v[68:71], v[188:191], v[48:51], v[68:71]\nds_read_b128 v[48:51], v34 offset:16384\nbuffer_load_dwordx4 v10, s[12:15], s53 offen lds\n"
"s_waitcnt lgkmcnt(3)\nv_mfma_f32_16x16x32_bf16 v[72:75], v[184:187], v[52:55], v[72:75]\ns_add_u32 m0, s52, 16384\nv_mfma_f32_16x16x32_bf16 v[76:79], v[188:191], v[52:55], v[76:79]\n"
"ds_read_b128 v[52:55], v35 offset:16384\ns_add_u32 s53, s49, 512\ns_waitcnt lgkmcnt(3)\nv_mfma_f32_16x16x32_bf16 v[64:67], v[192:195], v[40:43], v[64:67]\n"
"buffer_load_dwordx4 v10, s[12:15], s53 offen lds\nv_mfma_f32_16x16x32_bf16 v[68:71], v[196:199], v[40:43], v[68:71]\nds_read_b128 v[40:43], v32 offset:20480\ns_add_u32 m0, s52, 20480\n"
"s_waitcnt lgkmcnt(3)\nv_mfma_f32_16x16x32_bf16 v[72:75], v[192:195], v[44:47], v[72:75]\ns_add_u32 s53, s49, 640\nv_mfma_f32_16x16x32_bf16 v[76:79], v[196:199], v[44:47], v[76:79]\n"
"ds_read_b128 v[44:47], v33 offset:20480\nbuffer_load_dwordx4 v10, s[12:15], s53 offen lds\ns_waitcnt lgkmcnt(3)\nv_mfma_f32_16x16x32_bf16 v[64:67], v[200:203], v[48:51], v[64:67]\n"
"s_add_u32 m0, s52, 24576\nv_mfma_f32_16x16x32_bf16 v[68:71], v[204:207], v[48:51], v[68:71]\nds_read_b128 v[48:51], v34 offset:20480\ns_add_u32 s53, s49, 768\n"
"s_waitcnt lgkmcnt(3)\nv_mfma_f32_16x16x32_bf16 v[72:75], v[200:203], v[52:55], v[72:75]\nbuffer_load_dwordx4 v10, s[12:15], s53 offen lds\nv_mfma_f32_16x16x32_bf16 v[76:79], v[204:207], v[52:55], v[76:79]\n"
"ds_read_b128 v[52:55], v35 offset:20480\ns_mov_b32 s53, 5120\nbuffer_load_dwordx4 a[40:43], v14, s[16:19], s53 offen sc1\ns_waitcnt lgkmcnt(3)\n"
"v_mfma_f32_16x16x32_bf16 v[64:67], v[208:211], v[40:43], v[64:67]\ns_mov_b32 s53, 19456\nbuffer_load_dwordx4 a[44:47], v14, s[16:19], s53 offen sc1\nv_mfma_f32_16x16x32_bf16 v[68:71], v[212:215], v[40:43], v[68:71]\n"
"ds_read_b128 v[40:43], v32 offset:24576\ns_mov_b32 s53, 6144\nbuffer_load_dwordx4 a[48:51], v14, s[16:19], s53 offen sc1\ns_waitcnt lgkmcnt(3)\n"
"v_mfma_f32_16x16x32_bf16 v[72:75], v[208:211], v[44:47], v[72:75]\ns_mov_b32 s53, 20480\nbuffer_load_dwordx4 a[52:55], v14, s[16:19], s53 offen sc1\nv_mfma_f32_16x16x32_bf16 v[76:79], v[212:215], v[44:47], v[76:79]\n"
"ds_read_b128 v[44:47], v33 offset:24576\ns_mov_b32 s53, 7168\nbuffer_load_dwordx4 a[56:59], v14, s[16:19], s53 offen sc1\ns_waitcnt lgkmcnt(3)\n"
"v_mfma_f32_16x16x32_bf16 v[64:67], v[216:219], v[48:51], v[64:67]\ns_add_u32 s54, s51, 32768\ns_cmp_eq_u32 s54, 163840\nv_mfma_f32_16x16x32_bf16 v[68:71], v[220:223], v[48:51], v[68:71]\n"
"s_cselect_b32 s54, 0, s54\ns_sub_u32 s54, s54, s51\nds_read_b128 v[48:51], v34 offset:24576\ns_waitcnt lgkmcnt(3)\n"
"v_mfma_f32_16x16x32_bf16 v[72:75], v[216:219], v[52:55], v[72:75]\nv_mfma_f32_16x16x32_bf16 v[76:79], v[220:223], v[52:55], v[76:79]\nds_read_b128 v[52:55], v35 offset:24576\ns_waitcnt lgkmcnt(3)\n"
"v_mfma_f32_16x16x32_bf16 v[64:67], v[224:227], v[40:43], v[64:67]\nv_add_u32 v32, s54, v32\nv_mfma_f32_16x16x32_bf16 v[68:71], v[228:231], v[40:43], v[68:71]\nds_read_b128 v[40:43], v32 offset:0\n"
"s_waitcnt lgkmcnt(3)\nv_mfma_f32_16x16x32_bf16 v[72:75], v[224:227], v[44:47], v[72:75]\nv_add_u32 v33, s54, v33\nv_mfma_f32_16x16x32_bf16 v[76:79], v[228:231], v[44:47], v[76:79]\n"
"ds_read_b128 v[44:47], v33 offset:0\ns_waitcnt lgkmcnt(3)\nv_mfma_f32_16x16x32_bf16 v[64:67], v[232:235], v[48:51], v[64:67]\nv_add_u32 v34, s54, v34\n"
"v_mfma_f32_16x16x32_bf16 v[68:71], v[236:239], v[48:51], v[68:71]\nds_read_b128 v[48:51], v34 offset:0\ns_waitcnt lgkmcnt(3)\nv_mfma_f32_16x16x32_bf16 v[72:75], v[232:235], v[52:55], v[72:75]\n"
"v_add_u32 v35, s54, v35\nv_mfma_f32_16x16x32_bf16 v[76:79], v[236:239], v[52:55], v[76:79]\nds_read_b128 v[52:55], v35 offset:0\ns_mov_b32 s52, s47\n"
"s_add_u32 s47, s51, s32\ns_add_u32 s51, s51, s54\ns_add_u32 s49, s49, 28672\ns_waitcnt vmcnt(12)\n"
"s_barrier\ns_waitcnt lgkmcnt(3)\nv_mfma_f32_16x16x32_bf16 v[96:99], v[128:131], v[40:43], 0\nv_mov_b32 v36, s48\n"
"s_add_u32 s48, s48, 1\ns_nop 0\nbuffer_store_dword v36, v19, s[24:27], s33 offen sc1\nv_mfma_f32_16x16x32_bf16 v[100:103], v[132:135], v[40:43], 0\n"
"ds_read_b128 v[40:43], v32 offset:4096\ns_waitcnt lgkmcnt(3)\nv_mfma_f32_16x16x32_bf16 v[104:107], v[128:131], v[44:47], 0\nv_mfma_f32_16x16x32_bf16 v[108:111], v[132:135], v[44:47], 0\n"
"ds_read_b128 v[44:47], v33 offset:4096\nv_cvt_pk_bf16_f32 v56, v64, v65\ns_waitcnt lgkmcnt(3)\nv_mfma_f32_16x16x32_bf16 v[96:99], v[136:139], v[48:51], v[96:99]\n"
"v_cvt_pk_bf16_f32 v57, v66, v67\nv_mfma_f32_16x16x32_bf16 v[100:103], v[140:143], v[48:51], v[100:103]\nds_read_b128 v[48:51], v34 offset:4096\nv_cvt_pk_bf16_f32 v58, v68, v69\n"
"s_waitcnt lgkmcnt(3)\nv_mfma_f32_16x16x32_bf16 v[104:107], v[136:139], v[52:55], v[104:107]\nv_cvt_pk_bf16_f32 v59, v70, v71\nv_mfma_f32_16x16x32_bf16 v[108:111], v[140:143], v[52:55], v[108:111]\n"
"ds_read_b128 v[52:55], v35 offset:4096\ns_waitcnt lgkmcnt(3)\nv_mfma_f32_16x16x32_bf16 v[96:99], v[144:147], v[40:43], v[96:99]\nv_mfma_f32_16x16x32_bf16 v[100:103], v[148:151], v[40:43], v[100:103]\n"
"ds_read_b128 v[40:43], v32 offset:8192\nbuffer_store_dwordx4 v[56:59], v15, s[20:23], s50 offen sc1\ns_waitcnt lgkmcnt(3)\nv_mfma_f32_16x16x32_bf16 v[104:107], v[144:147], v[44:47], v[104:107]\n"
"v_cvt_pk_bf16_f32 v60, v72, v73\nv_mfma_f32_16x16x32_bf16 v[108:111], v[148:151], v[44:47], v[108:111]\nds_read_b128 v[44:47], v33 offset:8192\nv_cvt_pk_bf16_f32 v61, v74, v75\n"
"s_waitcnt lgkmcnt(3)\nv_mfma_f32_16x16x32_bf16 v[96:99], v[152:155], v[48:51], v[96:99]\nv_cvt_pk_bf16_f32 v62, v76, v77\nv_mfma_f32_16x16x32_bf16 v[100:103], v[156:159], v[48:51], v[100:103]\n"
"ds_read_b128 v[48:51], v34 offset:8192\nv_cvt_pk_bf16_f32 v63, v78, v79\ns_waitcnt lgkmcnt(3)\nv_mfma_f32_16x16x32_bf16 v[104:107], v[152:155], v[52:55], v[104:107]\n"
"v_mfma_f32_16x16x32_bf16 v[108:111], v[156:159], v[52:55], v[108:111]\nds_read_b128 v[52:55], v35 offset:8192\ns_waitcnt lgkmcnt(3)\nv_mfma_f32_16x16x32_bf16 v[96:99], v[160:163], v[40:43], v[96:99]\n"
"buffer_store_dwordx4 v[60:63], v16, s[20:23], s50 offen sc1\nv_mfma_f32_16x16x32_bf16 v[100:103], v[164:167], v[40:43], v[100:103]\nds_read_b128 v[40:43], v32 offset:12288\ns_add_u32 s50, s50, s36\n"
"s_waitcnt lgkmcnt(3)\nv_mfma_f32_16x16x32_bf16 v[104:107], v[160:163], v[44:47], v[104:107]\ns_add_u32 m0, s52, 0\nv_mfma_f32_16x16x32_bf16 v[108:111], v[164:167], v[44:47], v[108:111]\n"
"ds_read_b128 v[44:47], v33 offset:12288\ns_add_u32 s53, s49, 0\ns_waitcnt lgkmcnt(3)\nv_mfma_f32_16x16x32_bf16 v[96:99], v[168:171], v[48:51], v[96:99]\n"
"buffer_load_dwordx4 v10, s[12:15], s53 offen lds\nv_mfma_f32_16x16x32_bf16 v[100:103], v[172:175], v[48:51], v[100:103]\nds_read_b128 v[48:51], v34 offset:12288\ns_add_u32 m0, s52, 4096\n"
"s_waitcnt lgkmcnt(3)\nv_mfma_f32_16x16x32_bf16 v[104:107], v[168:171], v[52:55], v[104:107]\ns_add_u32 s53, s49, 128\nv_mfma_f32_16x16x32_bf16 v[108:111], v[172:175], v[52:55], v[108:111]\n"
"ds_read_b128 v[52:55], v35 offset:12288\nbuffer_load_dwordx4 v10, s[12:15], s53 offen lds\ns_waitcnt lgkmcnt(3)\nv_mfma_f32_16x16x32_bf16 v[96:99], v[176:179], v[40:43], v[96:99]\n"
"s_add_u32 m0, s52, 8192\nv_mfma_f32_16x16x32_bf16 v[100:103], v[180:183], v[40:43], v[100:103]\nds_read_b128 v[40:43], v32 offset:16384\ns_add_u32 s53, s49, 256\n"
"s_waitcnt lgkmcnt(3)\nv_mfma_f32_16x16x32_bf16 v[104:107], v[176:179], v[44:47], v[104:107]\nbuffer_load_dwordx4 v10, s[12:15], s53 offen lds\nv_mfma_f32_16x16x32_bf16 v[108:111], v[180:183], v[44:47], v[108:111]\n"
"ds_read_b128 v[44:47], v33 offset:16384\ns_add_u32 m0, s52, 12288\ns_waitcnt lgkmcnt(3)\nv_mfma_f32_16x16x32_bf16 v[96:99], v[184:187], v[48:51], v[96:99]\n"
"s_add_u32 s53, s49, 384\nv_mfma_f32_16x16x32_bf16 v[100:103], v[188:191], v[48:51], v[100:103]\nds_read_b128 v[48:51], v34 offset:16384\nbuffer_load_dwordx4 v10, s[12:15], s53 offen lds\n"
"s_waitcnt lgkmcnt(3)\nv_mfma_f32_16x16x32_bf16 v[104:107], v[184:187], v[52:55], v[104:107]\ns_add_u32 m0, s52, 16384\nv_mfma_f32_16x16x32_bf16 v[108:111], v[188:191], v[52:55], v[108:111]\n"
"ds_read_b128 v[52:55], v35 offset:16384\ns_add_u32 s53, s49, 512\ns_waitcnt lgkmcnt(3)\nv_mfma_f32_16x16x32_bf16 v[96:99], v[192:195], v[40:43], v[96:99]\n"
"buffer_load_dwordx4 v10, s[12:15], s53 offen lds\nv_mfma_f32_16x16x32_bf16 v[100:103], v[196:199], v[40:43], v[100:103]\nds_read_b128 v[40:43], v32 offset:20480\ns_add_u32 m0, s52, 20480\n"
"s_waitcnt lgkmcnt(3)\nv_mfma_f32_16x16x32_bf16 v[104:107], v[192:195], v[44:47], v[104:107]\ns_add_u32 s53, s49, 640\nv_mfma_f32_16x16x32_bf16 v[108:111], v[196:199], v[44:47], v[108:111]\n"
"ds_read_b128 v[44:47], v33 offset:20480\nbuffer_load_dwordx4 v10, s[12:15], s53 offen lds\ns_waitcnt lgkmcnt(3)\nv_mfma_f32_16x16x32_bf16 v[96:99], v[200:203], v[48:51], v[96:99]\n"
"s_add_u32 m0, s52, 24576\nv_mfma_f32_16x16x32_bf16 v[100:103], v[204:207], v[48:51], v[100:103]\nds_read_b128 v[48:51], v34 offset:20480\ns_add_u32 s53, s49, 768\n"
"s_waitcnt lgkmcnt(3)\nv_mfma_f32_16x16x32_bf16 v[104:107], v[200:203], v[52:55], v[104:107]\nbuffer_load_dwordx4 v10, s[12:15], s53 offen lds\nv_mfma_f32_16x16x32_bf16 v[108:111], v[204:207], v[52:55], v[108:111]\n"
"ds_read_b128 v[52:55], v35 offset:20480\ns_mov_b32 s53, 21504\nbuffer_load_dwordx4 a[60:63], v14, s[16:19], s53 offen sc1\ns_waitcnt lgkmcnt(3)\n"
"v_mfma_f32_16x16x32_bf16 v[96:99], v[208:211], v[40:43], v[96:99]\ns_mov_b32 s53, 8192\nbuffer_load_dwordx4 a[64:67], v14, s[16:19], s53 offen sc1\nv_mfma_f32_16x16x32_bf16 v[100:103], v[212:215], v[40:43], v[100:103]\n"
"ds_read_b128 v[40:43], v32 offset:24576\ns_mov_b32 s53, 22528\nbuffer_load_dwordx4 a[68:71], v14, s[16:19], s53 offen sc1\ns_waitcnt lgkmcnt(3)\n"
"v_mfma_f32_16x16x32_bf16 v[104:107], v[208:211], v[44:47], v[104:107]\ns_mov_b32 s53, 9216\nbuffer_load_dwordx4 a[72:75], v14, s[16:19], s53 offen sc1\nv_mfma_f32_16x16x32_bf16 v[108:111], v[212:215], v[44:47], v[108:111]\n"
"ds_read_b128 v[44:47], v33 offset:24576\ns_mov_b32 s53, 23552\nbuffer_load_dwordx4 a[76:79], v14, s[16:19], s53 offen sc1\ns_waitcnt lgkmcnt(3)\n"
"v_mfma_f32_16x16x32_bf16 v[96:99], v[216:219], v[48:51], v[96:99]\ns_add_u32 s54, s51, 32768\ns_cmp_eq_u32 s54, 163840\nv_mfma_f32_16x16x32_bf16 v[100:103], v[220:223], v[48:51], v[100:103]\n"
"s_cselect_b32 s54, 0, s54\ns_sub_u32 s54, s54, s51\nds_read_b128 v[48:51], v34 offset:24576\ns_waitcnt lgkmcnt(3)\n"
"v_mfma_f32_16x16x32_bf16 v[104:107], v[216:219], v[52:55], v[104:107]\nv_mfma_f32_16x16x32_bf16 v[108:111], v[220:223], v[52:55], v[108:111]\nds_read_b128 v[52:55], v35 offset:24576\ns_waitcnt lgkmcnt(3)\n"
"v_mfma_f32_16x16x32_bf16 v[96:99], v[224:227], v[40:43], v[96:99]\nv_add_u32 v32, s54, v32\nv_mfma_f32_16x16x32_bf16 v[100:103], v[228:231], v[40:43], v[100:103]\nds_read_b128 v[40:43], v32 offset:0\n"
"s_waitcnt lgkmcnt(3)\nv_mfma_f32_16x16x32_bf16 v[104:107], v[224:227], v[44:47], v[104:107]\nv_add_u32 v33, s54, v33\nv_mfma_f32_16x16x32_bf16 v[108:111], v[228:231], v[44:47], v[108:111]\n"
"ds_read_b128 v[44:47], v33 offset:0\ns_waitcnt lgkmcnt(3)\nv_mfma_f32_16x16x32_bf16 v[96:99], v[232:235], v[48:51], v[96:99]\nv_add_u32 v34, s54, v34\n"
"v_mfma_f32_16x16x32_bf16 v[100:103], v[236:239], v[48:51], v[100:103]\nds_read_b128 v[48:51], v34 offset:0\ns_waitcnt lgkmcnt(3)\nv_mfma_f32_16x16x32_bf16 v[104:107], v[232:235], v[52:55], v[104:107]\n"
"v_add_u32 v35, s54, v35\nv_mfma_f32_16x16x32_bf16 v[108:111], v[236:239], v[52:55], v[108:111]\nds_read_b128 v[52:55], v35 offset:0\ns_mov_b32 s52, s47\n"
"s_add_u32 s47, s51, s32\ns_add_u32 s51, s51, s54\ns_add_u32 s49, s49, 28672\ns_waitcnt vmcnt(12)\n"
"s_barrier\ns_waitcnt lgkmcnt(3)\nv_mfma_f32_16x16x32_bf16 v[64:67], v[128:131], v[40:43], 0\nv_mov_b32 v36, s48\n"
"s_add_u32 s48, s48, 1\ns_nop 0\nbuffer_store_dword v36, v19, s[24:27], s33 offen sc1\nv_mfma_f32_16x16x32_bf16 v[68:71], v[132:135], v[40:43], 0\n"
"ds_read_b128 v[40:43], v32 offset:4096\ns_waitcnt lgkmcnt(3)\nv_mfma_f32_16x16x32_bf16 v[72:75], v[128:131], v[44:47], 0\nv_mfma_f32_16x16x32_bf16 v[76:79], v[132:135], v[44:47], 0\n"
"ds_read_b128 v[44:47], v33 offset:4096\nv_cvt_pk_bf16_f32 v56, v96, v97\ns_waitcnt lgkmcnt(3)\nv_mfma_f32_16x16x32_bf16 v[64:67], v[136:139], v[48:51], v[64:67]\n"
"v_cvt_pk_bf16_f32 v57, v98, v99\nv_mfma_f32_16x16x32_bf16 v[68:71], v[140:143], v[48:51], v[68:71]\nds_read_b128 v[48:51], v34 offset:4096\nv_cvt_pk_bf16_f32 v58, v100, v101\n"
"s_waitcnt lgkmcnt(3)\nv_mfma_f32_16x16x32_bf16 v[72:75], v[136:139], v[52:55], v[72:75]\nv_cvt_pk_bf16_f32 v59, v102, v103\nv_mfma_f32_16x16x32_bf16 v[76:79], v[140:143], v[52:55], v[76:79]\n"
"ds_read_b128 v[52:55], v35 offset:4096\ns_waitcnt lgkmcnt(3)\nv_mfma_f32_16x16x32_bf16 v[64:67], v[144:147], v[40:43], v[64:67]\nv_mfma_f32_16x16x32_bf16 v[68:71], v[148:151], v[40:43], v[68:71]\n"
"ds_read_b128 v[40:43], v32 offset:8192\nbuffer_store_dwordx4 v[56:59], v15, s[20:23], s50 offen sc1\ns_waitcnt lgkmcnt(3)\nv_mfma_f32_16x16x32_bf16 v[72:75], v[144:147], v[44:47], v[72:75]\n"
"v_cvt_pk_bf16_f32 v60, v104, v105\nv_mfma_f32_16x16x32_bf16 v[76:79], v[148:151], v[44:47], v[76:79]\nds_read_b128 v[44:47], v33 offset:8192\nv_cvt_pk_bf16_f32 v61, v106, v107\n"
"s_waitcnt lgkmcnt(3)\nv_mfma_f32_16x16x32_bf16 v[64:67], v[152:155], v[48:51], v[64:67]\nv_cvt_pk_bf16_f32 v62, v108, v109\nv_mfma_f32_16x16x32_bf16 v[68:71], v[156:159], v[48:51], v[68:71]\n"
"ds_read_b128 v[48:51], v34 offset:8192\nv_cvt_pk_bf16_f32 v63, v110, v111\ns_waitcnt lgkmcnt(3)\nv_mfma_f32_16x16x32_bf16 v[72:75], v[152:155], v[52:55], v[72:75]\n"
"v_mfma_f32_16x16x32_bf16 v[76:79], v[156:159], v[52:55], v[76:79]\nds_read_b128 v[52:55], v35 offset:8192\ns_waitcnt lgkmcnt(3)\nv_mfma_f32_16x16x32_bf16 v[64:67], v[160:163], v[40:43], v[64:67]\n"
"buffer_store_dwordx4 v[60:63], v16, s[20:23], s50 offen sc1\nv_mfma_f32_16x16x32_bf16 v[68:71], v[164:167], v[40:43], v[68:71]\nds_read_b128 v[40:43], v32 offset:12288\ns_add_u32 s50, s50, s36\n"
"s_waitcnt lgkmcnt(3)\nv_mfma_f32_16x16x32_bf16 v[72:75], v[160:163], v[44:47], v[72:75]\ns_add_u32 m0, s52, 0\nv_mfma_f32_16x16x32_bf16 v[76:79], v[164:167], v[44:47], v[76:79]\n"
"ds_read_b128 v[44:47], v33 offset:12288\ns_add_u32 s53, s49, 0\ns_waitcnt lgkmcnt(3)\nv_mfma_f32_16x16x32_bf16 v[64:67], v[168:171], v[48:51], v[64:67]\n"
"buffer_load_dwordx4 v10, s[12:15], s53 offen lds\nv_mfma_f32_16x16x32_bf16 v[68:71], v[172:175], v[48:51], v[68:71]\nds_read_b128 v[48:51], v34 offset:12288\ns_add_u32 m0, s52, 4096\n"
"s_waitcnt lgkmcnt(3)\nv_mfma_f32_16x16x32_bf16 v[72:75], v[168:171], v[52:55], v[72:75]\ns_add_u32 s53, s49, 128\nv_mfma_f32_16x16x32_bf16 v[76:79], v[172:175], v[52:55], v[76:79]\n"
"ds_read_b128 v[52:55], v35 offset:12288\nbuffer_load_dwordx4 v10, s[12:15], s53 offen lds\ns_waitcnt lgkmcnt(3)\nv_mfma_f32_16x16x32_bf16 v[64:67], v[176:179], v[40:43], v[64:67]\n"
"s_add_u32 m0, s52, 8192\nv_mfma_f32_16x16x32_bf16 v[68:71], v[180:183], v[40:43], v[68:71]\nds_read_b128 v[40:43], v32 offset:16384\ns_add_u32 s53, s49, 256\n"
"s_waitcnt lgkmcnt(3)\nv_mfma_f32_16x16x32_bf16 v[72:75], v[176:179], v[44:47], v[72:75]\nbuffer_load_dwordx4 v10, s[12:15], s53 offen lds\nv_mfma_f32_16x16x32_bf16 v[76:79], v[180:183], v[44:47], v[76:79]\n"
"ds_read_b128 v[44:47], v33 offset:16384\ns_add_u32 m0, s52, 12288\ns_waitcnt lgkmcnt(3)\nv_mfma_f32_16x16x32_bf16 v[64:67], v[184:187], v[48:51], v[64:67]\n"
"s_add_u32 s53, s49, 384\nv_mfma_f32_16x16x32_bf16 v[68:71], v[188:191], v[48:51], v[68:71]\nds_read_b128 v[48:51], v34 offset:16384\nbuffer_load_dwordx4 v10, s[12:15], s53 offen lds\n"
"s_waitcnt lgkmcnt(3)\nv_mfma_f32_16x16x32_bf16 v[72:75], v[184:187], v[52:55], v[72:75]\ns_add_u32 m0, s52, 16384\nv_mfma_f32_16x16x32_bf16 v[76:79], v[188:191], v[52:55], v[76:79]\n"
"ds_read_b128 v[52:55], v35 offset:16384\ns_add_u32 s53, s49, 512\ns_waitcnt lgkmcnt(3)\nv_mfma_f32_16x16x32_bf16 v[64:67], v[192:195], v[40:43], v[64:67]\n"
"buffer_load_dwordx4 v10, s[12:15], s53 offen lds\nv_mfma_f32_16x16x32_bf16 v[68:71], v[196:199], v[40:43], v[68:71]\nds_read_b128 v[40:43], v32 offset:20480\ns_add_u32 m0, s52, 20480\n"
"s_waitcnt lgkmcnt(3)\nv_mfma_f32_16x16x32_bf16 v[72:75], v[192:195], v[44:47], v[72:75]\ns_add_u32 s53, s49, 640\nv_mfma_f32_16x16x32_bf16 v[76:79], v[196:199], v[44:47], v[76:79]\n"
"ds_read_b128 v[44:47], v33 offset:20480\nbuffer_load_dwordx4 v10, s[12:15], s53 offen lds\ns_waitcnt lgkmcnt(3)\nv_mfma_f32_16x16x32_bf16 v[64:67], v[200:203], v[48:51], v[64:67]\n"
"s_add_u32 m0, s52, 24576\nv_mfma_f32_16x16x32_bf16 v[68:71], v[204:207], v[48:51], v[68:71]\nds_read_b128 v[48:51], v34 offset:20480\ns_add_u32 s53, s49, 768\n"
"s_waitcnt lgkmcnt(3)\nv_mfma_f32_16x16x32_bf16 v[72:75], v[200:203], v[52:55], v[72:75]\nbuffer_load_dwordx4 v10, s[12:15], s53 offen lds\nv_mfma_f32_16x16x32_bf16 v[76:79], v[204:207], v[52:55], v[76:79]\n"
"ds_read_b128 v[52:55], v35 offset:20480\ns_mov_b32 s53, 10240\nbuffer_load_dwordx4 a[80:83], v14, s[16:19], s53 offen sc1\ns_waitcnt lgkmcnt(3)\n"
"v_mfma_f32_16x16x32_bf16 v[64:67], v[208:211], v[40:43], v[64:67]\ns_mov_b32 s53, 24576\nbuffer_load_dwordx4 a[84:87], v14, s[16:19], s53 offen sc1\nv_mfma_f32_16x16x32_bf16 v[68:71], v[212:215], v[40:43], v[68:71]\n"
"ds_read_b128 v[40:43], v32 offset:24576\ns_mov_b32 s53, 11264\nbuffer_load_dwordx4 a[88:91], v14, s[16:19], s53 offen sc1\ns_waitcnt lgkmcnt(3)\n"
"v_mfma_f32_16x16x32_bf16 v[72:75], v[208:211], v[44:47], v[72:75]\ns_mov_b32 s53, 25600\nbuffer_load_dwordx4 a[92:95], v14, s[16:19], s53 offen sc1\nv_mfma_f32_16x16x32_bf16 v[76:79], v[212:215], v[44:47], v[76:79]\n"
"ds_read_b128 v[44:47], v33 offset:24576\ns_mov_b32 s53, 12288\nbuffer_load_dwordx4 a[96:99], v14, s[16:19], s53 offen sc1\ns_waitcnt lgkmcnt(3)\n"
"v_mfma_f32_16x16x32_bf16 v[64:67], v[216:219], v[48:51], v[64:67]\ns_add_u32 s54, s51, 32768\ns_cmp_eq_u32 s54, 163840\nv_mfma_f32_16x16x32_bf16 v[68:71], v[220:223], v[48:51], v[68:71]\n"
"s_cselect_b32 s54, 0, s54\ns_sub_u32 s54, s54, s51\nds_read_b128 v[48:51], v34 offset:24576\ns_waitcnt lgkmcnt(3)\n"
"v_mfma_f32_16x16x32_bf16 v[72:75], v[216:219], v[52:55], v[72:75]\nv_mfma_f32_16x16x32_bf16 v[76:79], v[220:223], v[52:55], v[76:79]\nds_read_b128 v[52:55], v35 offset:24576\ns_waitcnt lgkmcnt(3)\n"
"v_mfma_f32_16x16x32_bf16 v[64:67], v[224:227], v[40:43], v[64:67]\nv_add_u32 v32, s54, v32\nv_mfma_f32_16x16x32_bf16 v[68:71], v[228:231], v[40:43], v[68:71]\nds_read_b128 v[40:43], v32 offset:0\n"
"s_waitcnt lgkmcnt(3)\nv_mfma_f32_16x16x32_bf16 v[72:75], v[224:227], v[44:47], v[72:75]\nv_add_u32 v33, s54, v33\nv_mfma_f32_16x16x32_bf16 v[76:79], v[228:231], v[44:47], v[76:79]\n"
"ds_read_b128 v[44:47], v33 offset:0\ns_waitcnt lgkmcnt(3)\nv_mfma_f32_16x16x32_bf16 v[64:67], v[232:235], v[48:51], v[64:67]\nv_add_u32 v34, s54, v34\n"
"v_mfma_f32_16x16x32_bf16 v[68:71], v[236:239], v[48:51], v[68:71]\nds_read_b128 v[48:51], v34 offset:0\ns_waitcnt lgkmcnt(3)\nv_mfma_f32_16x16x32_bf16 v[72:75], v[232:235], v[52:55], v[72:75]\n"
"v_add_u32 v35, s54, v35\nv_mfma_f32_16x16x32_bf16 v[76:79], v[236:239], v[52:55], v[76:79]\nds_read_b128 v[52:55], v35 offset:0\ns_mov_b32 s52, s47\n"
"s_add_u32 s47, s51, s32\ns_add_u32 s51, s51, s54\ns_add_u32 s49, s49, 28672\ns_waitcnt vmcnt(12)\n"
"s_barrier\ns_waitcnt lgkmcnt(3)\nv_mfma_f32_16x16x32_bf16 v[96:99], v[128:131], v[40:43], 0\nv_mov_b32 v36, s48\n"
"s_add_u32 s48, s48, 1\ns_nop 0\nbuffer_store_dword v36, v19, s[24:27], s33 offen sc1\nv_mfma_f32_16x16x32_bf16 v[100:103], v[132:135], v[40:43], 0\n"
"ds_read_b128 v[40:43], v32 offset:4096\ns_waitcnt lgkmcnt(3)\nv_mfma_f32_16x16x32_bf16 v[104:107], v[128:131], v[44:47], 0\nv_mfma_f32_16x16x32_bf16 v[108:111], v[132:135], v[44:47], 0\n"
"ds_read_b128 v[44:47], v33 offset:4096\nv_cvt_pk_bf16_f32 v56, v64, v65\ns_waitcnt lgkmcnt(3)\nv_mfma_f32_16x16x32_bf16 v[96:99], v[136:139], v[48:51], v[96:99]\n"
"v_cvt_pk_bf16_f32 v57, v66, v67\nv_mfma_f32_16x16x32_bf16 v[100:103], v[140:143], v[48:51], v[100:103]\nds_read_b128 v[48:51], v34 offset:4096\nv_cvt_pk_bf16_f32 v58, v68, v69\n"
"s_waitcnt lgkmcnt(3)\nv_mfma_f32_16x16x32_bf16 v[104:107], v[136:139], v[52:55], v[104:107]\nv_cvt_pk_bf16_f32 v59, v70, v71\nv_mfma_f32_16x16x32_bf16 v[108:111], v[140:143], v[52:55], v[108:111]\n"
"ds_read_b128 v[52:55], v35 offset:4096\ns_waitcnt lgkmcnt(3)\nv_mfma_f32_16x16x32_bf16 v[96:99], v[144:147], v[40:43], v[96:99]\nv_mfma_f32_16x16x32_bf16 v[100:103], v[148:151], v[40:43], v[100:103]\n"
"ds_read_b128 v[40:43], v32 offset:8192\nbuffer_store_dwordx4 v[56:59], v15, s[20:23], s50 offen sc1\ns_waitcnt lgkmcnt(3)\nv_mfma_f32_16x16x32_bf16 v[104:107], v[144:147], v[44:47], v[104:107]\n"
"v_cvt_pk_bf16_f32 v60, v72, v73\nv_mfma_f32_16x16x32_bf16 v[108:111], v[148:151], v[44:47], v[108:111]\nds_read_b128 v[44:47], v33 offset:8192\nv_cvt_pk_bf16_f32 v61, v74, v75\n"
"s_waitcnt lgkmcnt(3)\nv_mfma_f32_16x16x32_bf16 v[96:99], v[152:155], v[48:51], v[96:99]\nv_cvt_pk_bf16_f32 v62, v76, v77\nv_mfma_f32_16x16x32_bf16 v[100:103], v[156:159], v[48:51], v[100:103]\n"
"ds_read_b128 v[48:51], v34 offset:8192\nv_cvt_pk_bf16_f32 v63, v78, v79\ns_waitcnt lgkmcnt(3)\nv_mfma_f32_16x16x32_bf16 v[104:107], v[152:155], v[52:55], v[104:107]\n"
"v_mfma_f32_16x16x32_bf16 v[108:111], v[156:159], v[52:55], v[108:111]\nds_read_b128 v[52:55], v35 offset:8192\ns_waitcnt lgkmcnt(3)\nv_mfma_f32_16x16x32_bf16 v[96:99], v[160:163], v[40:43], v[96:99]\n"
"buffer_store_dwordx4 v[60:63], v16, s[20:23], s50 offen sc1\nv_mfma_f32_16x16x32_bf16 v[100:103], v[164:167], v[40:43], v[100:103]\nds_read_b128 v[40:43], v32 offset:12288\ns_add_u32 s50, s50, s36\n"
"s_waitcnt lgkmcnt(3)\nv_mfma_f32_16x16x32_bf16 v[104:107], v[160:163], v[44:47], v[104:107]\ns_add_u32 m0, s52, 0\nv_mfma_f32_16x16x32_bf16 v[108:111], v[164:167], v[44:47], v[108:111]\n"
"ds_read_b128 v[44:47], v33 offset:12288\ns_add_u32 s53, s49, 0\ns_waitcnt lgkmcnt(3)\nv_mfma_f32_16x16x32_bf16 v[96:99], v[168:171], v[48:51], v[96:99]\n"
"buffer_load_dwordx4 v10, s[12:15], s53 offen lds\nv_mfma_f32_16x16x32_bf16 v[100:103], v[172:175], v[48:51], v[100:103]\nds_read_b128 v[48:51], v34 offset:12288\ns_add_u32 m0, s52, 4096\n"
"s_waitcnt lgkmcnt(3)\nv_mfma_f32_16x16x32_bf16 v[104:107], v[168:171], v[52:55], v[104:107]\ns_add_u32 s53, s49, 128\nv_mfma_f32_16x16x32_bf16 v[108:111], v[172:175], v[52:55], v[108:111]\n"
"ds_read_b128 v[52:55], v35 offset:12288\nbuffer_load_dwordx4 v10, s[12:15], s53 offen lds\ns_waitcnt lgkmcnt(3)\nv_mfma_f32_16x16x32_bf16 v[96:99], v[176:179], v[40:43], v[96:99]\n"
"s_add_u32 m0, s52, 8192\nv_mfma_f32_16x16x32_bf16 v[100:103], v[180:183], v[40:43], v[100:103]\nds_read_b128 v[40:43], v32 offset:16384\ns_add_u32 s53, s49, 256\n"
"s_waitcnt lgkmcnt(3)\nv_mfma_f32_16x16x32_bf16 v[104:107], v[176:179], v[44:47], v[104:107]\nbuffer_load_dwordx4 v10, s[12:15], s53 offen lds\nv_mfma_f32_16x16x32_bf16 v[108:111], v[180:183], v[44:47], v[108:111]\n"
"ds_read_b128 v[44:47], v33 offset:16384\ns_add_u32 m0, s52, 12288\ns_waitcnt lgkmcnt(3)\nv_mfma_f32_16x16x32_bf16 v[96:99], v[184:187], v[48:51], v[96:99]\n"
"s_add_u32 s53, s49, 384\nv_mfma_f32_16x16x32_bf16 v[100:103], v[188:191], v[48:51], v[100:103]\nds_read_b128 v[48:51], v34 offset:16384\nbuffer_load_dwordx4 v10, s[12:15], s53 offen lds\n"
"s_waitcnt lgkmcnt(3)\nv_mfma_f32_16x16x32_bf16 v[104:107], v[184:187], v[52:55], v[104:107]\ns_add_u32 m0, s52, 16384\nv_mfma_f32_16x16x32_bf16 v[108:111], v[188:191], v[52:55], v[108:111]\n"
"ds_read_b128 v[52:55], v35 offset:16384\ns_add_u32 s53, s49, 512\ns_waitcnt lgkmcnt(3)\nv_mfma_f32_16x16x32_bf16 v[96:99], v[192:195], v[40:43], v[96:99]\n"
"buffer_load_dwordx4 v10, s[12:15], s53 offen lds\nv_mfma_f32_16x16x32_bf16 v[100:103], v[196:199], v[40:43], v[100:103]\nds_read_b128 v[40:43], v32 offset:20480\ns_add_u32 m0, s52, 20480\n"
"s_waitcnt lgkmcnt(3)\nv_mfma_f32_16x16x32_bf16 v[104:107], v[192:195], v[44:47], v[104:107]\ns_add_u32 s53, s49, 640\nv_mfma_f32_16x16x32_bf16 v[108:111], v[196:199], v[44:47], v[108:111]\n"
"ds_read_b128 v[44:47], v33 offset:20480\nbuffer_load_dwordx4 v10, s[12:15], s53 offen lds\ns_waitcnt lgkmcnt(3)\nv_mfma_f32_16x16x32_bf16 v[96:99], v[200:203], v[48:51], v[96:99]\n"
"s_add_u32 m0, s52, 24576\nv_mfma_f32_16x16x32_bf16 v[100:103], v[204:207], v[48:51], v[100:103]\nds_read_b128 v[48:51], v34 offset:20480\ns_add_u32 s53, s49, 768\n"
"s_waitcnt lgkmcnt(3)\nv_mfma_f32_16x16x32_bf16 v[104:107], v[200:203], v[52:55], v[104:107]\nbuffer_load_dwordx4 v10, s[12:15], s53 offen lds\nv_mfma_f32_16x16x32_bf16 v[108:111], v[204:207], v[52:55], v[108:111]\n"
"ds_read_b128 v[52:55], v35 offset:20480\ns_mov_b32 s53, 26624\nbuffer_load_dwordx4 a[100:103], v14, s[16:19], s53 offen sc1\ns_waitcnt lgkmcnt(3)\n"
"v_mfma_f32_16x16x32_bf16 v[96:99], v[208:211], v[40:43], v[96:99]\ns_mov_b32 s53, 13312\nbuffer_load_dwordx4 a[104:107], v14, s[16:19], s53 offen sc1\nv_mfma_f32_16x16x32_bf16 v[100:103], v[212:215], v[40:43], v[100:103]\n"
"ds_read_b128 v[40:43], v32 offset:24576\ns_mov_b32 s53, 27648\nbuffer_load_dwordx4 a[108:111], v14, s[16:19], s53 offen sc1\ns_waitcnt lgkmcnt(3)\n"
"v_mfma_f32_16x16x32_bf16 v[104:107], v[208:211], v[44:47], v[104:107]\nv_mfma_f32_16x16x32_bf16 v[108:111], v[212:215], v[44:47], v[108:111]\nds_read_b128 v[44:47], v33 offset:24576\ns_waitcnt lgkmcnt(3)\n"
"v_mfma_f32_16x16x32_bf16 v[96:99], v[216:219], v[48:51], v[96:99]\ns_add_u32 s54, s51, 32768\ns_cmp_eq_u32 s54, 163840\nv_mfma_f32_16x16x32_bf16 v[100:103], v[220:223], v[48:51], v[100:103]\n"
"s_cselect_b32 s54, 0, s54\ns_sub_u32 s54, s54, s51\nds_read_b128 v[48:51], v34 offset:24576\ns_waitcnt lgkmcnt(3)\n"
"v_mfma_f32_16x16x32_bf16 v[104:107], v[216:219], v[52:55], v[104:107]\nv_mfma_f32_16x16x32_bf16 v[108:111], v[220:223], v[52:55], v[108:111]\nds_read_b128 v[52:55], v35 offset:24576\ns_waitcnt lgkmcnt(3)\n"
"v_mfma_f32_16x16x32_bf16 v[96:99], v[224:227], v[40:43], v[96:99]\nv_add_u32 v32, s54, v32\nv_mfma_f32_16x16x32_bf16 v[100:103], v[228:231], v[40:43], v[100:103]\nds_read_b128 v[40:43], v32 offset:0\n"
"s_waitcnt lgkmcnt(3)\nv_mfma_f32_16x16x32_bf16 v[104:107], v[224:227], v[44:47], v[104:107]\nv_add_u32 v33, s54, v33\nv_mfma_f32_16x16x32_bf16 v[108:111], v[228:231], v[44:47], v[108:111]\n"
"ds_read_b128 v[44:47], v33 offset:0\ns_waitcnt lgkmcnt(3)\nv_mfma_f32_16x16x32_bf16 v[96:99], v[232:235], v[48:51], v[96:99]\nv_add_u32 v34, s54, v34\n"
"v_mfma_f32_16x16x32_bf16 v[100:103], v[236:239], v[48:51], v[100:103]\nds_read_b128 v[48:51], v34 offset:0\ns_waitcnt lgkmcnt(3)\nv_mfma_f32_16x16x32_bf16 v[104:107], v[232:235], v[52:55], v[104:107]\n"
"v_add_u32 v35, s54, v35\nv_mfma_f32_16x16x32_bf16 v[108:111], v[236:239], v[52:55], v[108:111]\nds_read_b128 v[52:55], v35 offset:0\ns_mov_b32 s52, s47\n"
"s_add_u32 s47, s51, s32\ns_add_u32 s51, s51, s54\ns_add_u32 s49, s49, 28672\ns_cmp_lg_u32 s34, 0\n"
"s_cbranch_scc1 fg0_stay\ns_nop 7\nv_cvt_pk_bf16_f32 v56, v96, v97\nv_cvt_pk_bf16_f32 v57, v98, v99\n"
"v_cvt_pk_bf16_f32 v58, v100, v101\nv_cvt_pk_bf16_f32 v59, v102, v103\ns_nop 0\ns_nop 0\n"
"buffer_store_dwordx4 v[56:59], v15, s[20:23], s50 offen sc1\nv_cvt_pk_bf16_f32 v60, v104, v105\nv_cvt_pk_bf16_f32 v61, v106, v107\nv_cvt_pk_bf16_f32 v62, v108, v109\n"
"v_cvt_pk_bf16_f32 v63, v110, v111\ns_nop 0\ns_nop 0\nbuffer_store_dwordx4 v[60:63], v16, s[20:23], s50 offen sc1\n"
"s_add_u32 s48, s28, 64\nv_mov_b32 v36, s48\ns_waitcnt vmcnt(0) lgkmcnt(0)\ns_barrier\n"
"buffer_store_dword v36, v19, s[24:27], s33 offen sc1\ns_branch fg0_end\nfg0_stay:\ns_waitcnt vmcnt(0)\n"
"s_barrier\ns_waitcnt lgkmcnt(3)\nv_mfma_f32_16x16x32_bf16 v[64:67], v[128:131], v[40:43], 0\nv_mov_b32 v36, s48\n"
"s_add_u32 s48, s48, 1\ns_nop 0\nbuffer_store_dword v36, v19, s[24:27], s33 offen sc1\nv_mfma_f32_16x16x32_bf16 v[68:71], v[132:135], v[40:43], 0\n"
"v_mfma_f32_16x16x32_bf16 v[72:75], a[0:3], v[40:43], 0\nv_mfma_f32_16x16x32_bf16 v[76:79], a[4:7], v[40:43], 0\nds_read_b128 v[40:43], v32 offset:4096\nv_cvt_pk_bf16_f32 v56, v96, v97\n"
"s_waitcnt lgkmcnt(3)\nv_mfma_f32_16x16x32_bf16 v[80:83], v[128:131], v[44:47], 0\nv_cvt_pk_bf16_f32 v57, v98, v99\nv_mfma_f32_16x16x32_bf16 v[84:87], v[132:135], v[44:47], 0\n"
"v_cvt_pk_bf16_f32 v58, v100, v101\nv_mfma_f32_16x16x32_bf16 v[88:91], a[0:3], v[44:47], 0\nv_cvt_pk_bf16_f32 v59, v102, v103\nv_mfma_f32_16x16x32_bf16 v[92:95], a[4:7], v[44:47], 0\n"
"ds_read_b128 v[44:47], v33 offset:4096\ns_waitcnt lgkmcnt(3)\nv_mfma_f32_16x16x32_bf16 v[64:67], v[136:139], v[48:51], v[64:67]\nv_mfma_f32_16x16x32_bf16 v[68:71], v[140:143], v[48:51], v[68:71]\n"
"buffer_store_dwordx4 v[56:59], v15, s[20:23], s50 offen sc1\nv_mfma_f32_16x16x32_bf16 v[72:75], a[8:11], v[48:51], v[72:75]\nv_cvt_pk_bf16_f32 v60, v104, v105\nv_mfma_f32_16x16x32_bf16 v[76:79], a[12:15], v[48:51], v[76:79]\n"
"ds_read_b128 v[48:51], v34 offset:4096\nv_cvt_pk_bf16_f32 v61, v106, v107\ns_waitcnt lgkmcnt(3)\nv_mfma_f32_16x16x32_bf16 v[80:83], v[136:139], v[52:55], v[80:83]\n"
"v_cvt_pk_bf16_f32 v62, v108, v109\nv_mfma_f32_16x16x32_bf16 v[84:87], v[140:143], v[52:55], v[84:87]\nv_cvt_pk_bf16_f32 v63, v110, v111\nv_mfma_f32_16x16x32_bf16 v[88:91], a[8:11], v[52:55], v[88:91]\n"
"v_mfma_f32_16x16x32_bf16 v[92:95], a[12:15], v[52:55], v[92:95]\nds_read_b128 v[52:55], v35 offset:4096\ns_waitcnt lgkmcnt(3)\nv_mfma_f32_16x16x32_bf16 v[64:67], v[144:147], v[40:43], v[64:67]\n"
"buffer_store_dwordx4 v[60:63], v16, s[20:23], s50 offen sc1\nv_mfma_f32_16x16x32_bf16 v[68:71], v[148:151], v[40:43], v[68:71]\ns_add_u32 s50, s50, s36\nv_mfma_f32_16x16x32_bf16 v[72:75], a[16:19], v[40:43], v[72:75]\n"
"s_add_u32 m0, s52, 0\nv_mfma_f32_16x16x32_bf16 v[76:79], a[20:23], v[40:43], v[76:79]\nds_read_b128 v[40:43], v32 offset:8192\ns_add_u32 s53, s49, 0\n"
"s_waitcnt lgkmcnt(3)\nv_mfma_f32_16x16x32_bf16 v[80:83], v[144:147], v[44:47], v[80:83]\nbuffer_load_dwordx4 v10, s[12:15], s53 offen lds\nv_mfma_f32_16x16x32_bf16 v[84:87], v[148:151], v[44:47], v[84:87]\n"
"v_mfma_f32_16x16x32_bf16 v[88:91], a[16:19], v[44:47], v[88:91]\nv_mfma_f32_16x16x32_bf16 v[92:95], a[20:23], v[44:47], v[92:95]\nds_read_b128 v[44:47], v33 offset:8192\ns_waitcnt lgkmcnt(3)\n"
"v_mfma_f32_16x16x32_bf16 v[64:67], v[152:155], v[48:51], v[64:67]\nv_mfma_f32_16x16x32_bf16 v[68:71], v[156:159], v[48:51], v[68:71]\nv_mfma_f32_16x16x32_bf16 v[72:75], a[24:27], v[48:51], v[72:75]\nv_mfma_f32_16x16x32_bf16 v[76:79], a[28:31], v[48:51], v[76:79]\n"
"ds_read_b128 v[48:51], v34 offset:8192\ns_waitcnt lgkmcnt(3)\nv_mfma_f32_16x16x32_bf16 v[80:83], v[152:155], v[52:55], v[80:83]\nv_mfma_f32_16x16x32_bf16 v[84:87], v[156:159], v[52:55], v[84:87]\n"
"s_add_u32 m0, s52, 4096\nv_mfma_f32_16x16x32_bf16 v[88:91], a[24:27], v[52:55], v[88:91]\ns_add_u32 s53, s49, 128\nv_mfma_f32_16x16x32_bf16 v[92:95], a[28:31], v[52:55], v[92:95]\n"
"ds_read_b128 v[52:55], v35 offset:8192\nbuffer_load_dwordx4 v10, s[12:15], s53 offen lds\ns_waitcnt lgkmcnt(3)\nv_mfma_f32_16x16x32_bf16 v[64:67], v[160:163], v[40:43], v[64:67]\n"
"v_mfma_f32_16x16x32_bf16 v[68:71], v[164:167], v[40:43], v[68:71]\nv_mfma_f32_16x16x32_bf16 v[72:75], a[32:35], v[40:43], v[72:75]\nv_mfma_f32_16x16x32_bf16 v[76:79], a[36:39], v[40:43], v[76:79]\nds_read_b128 v[40:43], v32 offset:12288\n"
"s_waitcnt lgkmcnt(3)\nv_mfma_f32_16x16x32_bf16 v[80:83], v[160:163], v[44:47], v[80:83]\nv_mfma_f32_16x16x32_bf16 v[84:87], v[164:167], v[44:47], v[84:87]\nv_mfma_f32_16x16x32_bf16 v[88:91], a[32:35], v[44:47], v[88:91]\n"
"v_mfma_f32_16x16x32_bf16 v[92:95], a[36:39], v[44:47], v[92:95]\nds_read_b128 v[44:47], v33 offset:12288\ns_waitcnt lgkmcnt(3)\nv_mfma_f32_16x16x32_bf16 v[64:67], v[168:171], v[48:51], v[64:67]\n"
"s_add_u32 m0, s52, 8192\nv_mfma_f32_16x16x32_bf16 v[68:71], v[172:175], v[48:51], v[68:71]\ns_add_u32 s53, s49, 256\nv_mfma_f32_16x16x32_bf16 v[72:75], a[40:43], v[48:51], v[72:75]\n"
"buffer_load_dwordx4 v10, s[12:15], s53 offen lds\nv_mfma_f32_16x16x32_bf16 v[76:79], a[44:47], v[48:51], v[76:79]\nds_read_b128 v[48:51], v34 offset:12288\ns_waitcnt lgkmcnt(3)\n"
"v_mfma_f32_16x16x32_bf16 v[80:83], v[168:171], v[52:55], v[80:83]\nv_mfma_f32_16x16x32_bf16 v[84:87], v[172:175], v[52:55], v[84:87]\nv_mfma_f32_16x16x32_bf16 v[88:91], a[40:43], v[52:55], v[88:91]\nv_mfma_f32_16x16x32_bf16 v[92:95], a[44:47], v[52:55], v[92:95]\n"
"ds_read_b128 v[52:55], v35 offset:12288\ns_waitcnt lgkmcnt(3)\nv_mfma_f32_16x16x32_bf16 v[64:67], v[176:179], v[40:43], v[64:67]\nv_mfma_f32_16x16x32_bf16 v[68:71], v[180:183], v[40:43], v[68:71]\n"
"v_mfma_f32_16x16x32_bf16 v[72:75], a[48:51], v[40:43], v[72:75]\nv_mfma_f32_16x16x32_bf16 v[76:79], a[52:55], v[40:43], v[76:79]\nds_read_b128 v[40:43], v32 offset:16384\ns_add_u32 m0, s52, 12288\n"
"s_waitcnt lgkmcnt(3)\nv_mfma_f32_16x16x32_bf16 v[80:83], v[176:179], v[44:47], v[80:83]\ns_add_u32 s53, s49, 384\nv_mfma_f32_16x16x32_bf16 v[84:87], v[180:183], v[44:47], v[84:87]\n"
"buffer_load_dwordx4 v10, s[12:15], s53 offen lds\nv_mfma_f32_16x16x32_bf16 v[88:91], a[48:51], v[44:47], v[88:91]\nv_mfma_f32_16x16x32_bf16 v[92:95], a[52:55], v[44:47], v[92:95]\nds_read_b128 v[44:47], v33 offset:16384\n"
"s_waitcnt lgkmcnt(3)\nv_mfma_f32_16x16x32_bf16 v[64:67], v[184:187], v[48:51], v[64:67]\nv_mfma_f32_16x16x32_bf16 v[68:71], v[188:191], v[48:51], v[68:71]\nv_mfma_f32_16x16x32_bf16 v[72:75], a[56:59], v[48:51], v[72:75]\n"
"v_mfma_f32_16x16x32_bf16 v[76:79], a[60:63], v[48:51], v[76:79]\nds_read_b128 v[48:51], v34 offset:16384\ns_waitcnt lgkmcnt(3)\nv_mfma_f32_16x16x32_bf16 v[80:83], v[184:187], v[52:55], v[80:83]\n"
"v_mfma_f32_16x16x32_bf16 v[84:87], v[188:191], v[52:55], v[84:87]\nv_mfma_f32_16x16x32_bf16 v[88:91], a[56:59], v[52:55], v[88:91]\ns_add_u32 m0, s52, 16384\nv_mfma_f32_16x16x32_bf16 v[92:95], a[60:63], v[52:55], v[92:95]\n"
"ds_read_b128 v[52:55], v35 offset:16384\ns_add_u32 s53, s49, 512\ns_waitcnt lgkmcnt(3)\nv_mfma_f32_16x16x32_bf16 v[64:67], v[192:195], v[40:43], v[64:67]\n"
"buffer_load_dwordx4 v10, s[12:15], s53 offen lds\nv_mfma_f32_16x16x32_bf16 v[68:71], v[196:199], v[40:43], v[68:71]\nv_mfma_f32_16x16x32_bf16 v[72:75], a[64:67], v[40:43], v[72:75]\nv_mfma_f32_16x16x32_bf16 v[76:79], a[68:71], v[40:43], v[76:79]\n"
"ds_read_b128 v[40:43], v32 offset:20480\ns_waitcnt lgkmcnt(3)\nv_mfma_f32_16x16x32_bf16 v[80:83], v[192:195], v[44:47], v[80:83]\nv_mfma_f32_16x16x32_bf16 v[84:87], v[196:199], v[44:47], v[84:87]\n"
"v_mfma_f32_16x16x32_bf16 v[88:91], a[64:67], v[44:47], v[88:91]\nv_mfma_f32_16x16x32_bf16 v[92:95], a[68:71], v[44:47], v[92:95]\nds_read_b128 v[44:47], v33 offset:20480\ns_waitcnt lgkmcnt(3)\n"
"v_mfma_f32_16x16x32_bf16 v[64:67], v[200:203], v[48:51], v[64:67]\nv_mfma_f32_16x16x32_bf16 v[68:71], v[204:207], v[48:51], v[68:71]\ns_add_u32 m0, s52, 20480\nv_mfma_f32_16x16x32_bf16 v[72:75], a[72:75], v[48:51], v[72:75]\n"
"s_add_u32 s53, s49, 640\nv_mfma_f32_16x16x32_bf16 v[76:79], a[76:79], v[48:51], v[76:79]\nds_read_b128 v[48:51], v34 offset:20480\nbuffer_load_dwordx4 v10, s[12:15], s53 offen lds\n"
"s_waitcnt lgkmcnt(3)\nv_mfma_f32_16x16x32_bf16 v[80:83], v[200:203], v[52:55], v[80:83]\nv_mfma_f32_16x16x32_bf16 v[84:87], v[204:207], v[52:55], v[84:87]\nv_mfma_f32_16x16x32_bf16 v[88:91], a[72:75], v[52:55], v[88:91]\n"
"v_mfma_f32_16x16x32_bf16 v[92:95], a[76:79], v[52:55], v[92:95]\nds_read_b128 v[52:55], v35 offset:20480\ns_waitcnt lgkmcnt(3)\nv_mfma_f32_16x16x32_bf16 v[64:67], v[208:211], v[40:43], v[64:67]\n"
"v_mfma_f32_16x16x32_bf16 v[68:71], v[212:215], v[40:43], v[68:71]\nv_mfma_f32_16x16x32_bf16 v[72:75], a[80:83], v[40:43], v[72:75]\nv_mfma_f32_16x16x32_bf16 v[76:79], a[84:87], v[40:43], v[76:79]\nds_read_b128 v[40:43], v32 offset:24576\n"
"s_waitcnt lgkmcnt(3)\nv_mfma_f32_16x16x32_bf16 v[80:83], v[208:211], v[44:47], v[80:83]\ns_add_u32 m0, s52, 24576\nv_mfma_f32_16x16x32_bf16 v[84:87], v[212:215], v[44:47], v[84:87]\n"
"s_add_u32 s53, s49, 768\nv_mfma_f32_16x16x32_bf16 v[88:91], a[80:83], v[44:47], v[88:91]\nbuffer_load_dwordx4 v10, s[12:15], s53 offen lds\nv_mfma_f32_16x16x32_bf16 v[92:95], a[84:87], v[44:47], v[92:95]\n"
"ds_read_b128 v[44:47], v33 offset:24576\ns_waitcnt lgkmcnt(3)\nv_mfma_f32_16x16x32_bf16 v[64:67], v[216:219], v[48:51], v[64:67]\ns_add_u32 s54, s51, 32768\n"
"s_cmp_eq_u32 s54, 163840\nv_mfma_f32_16x16x32_bf16 v[68:71], v[220:223], v[48:51], v[68:71]\nv_mfma_f32_16x16x32_bf16 v[72:75], a[88:91], v[48:51], v[72:75]\nv_mfma_f32_16x16x32_bf16 v[76:79], a[92:95], v[48:51], v[76:79]\n"
"s_cselect_b32 s54, 0, s54\ns_sub_u32 s54, s54, s51\nds_read_b128 v[48:51], v34 offset:24576\ns_waitcnt lgkmcnt(3)\n"
"v_mfma_f32_16x16x32_bf16 v[80:83], v[216:219], v[52:55], v[80:83]\nv_mfma_f32_16x16x32_bf16 v[84:87], v[220:223], v[52:55], v[84:87]\nv_mfma_f32_16x16x32_bf16 v[88:91], a[88:91], v[52:55], v[88:91]\nv_mfma_f32_16x16x32_bf16 v[92:95], a[92:95], v[52:55], v[92:95]\n"
"ds_read_b128 v[52:55], v35 offset:24576\ns_waitcnt lgkmcnt(3)\nv_mfma_f32_16x16x32_bf16 v[64:67], v[224:227], v[40:43], v[64:67]\nv_add_u32 v32, s54, v32\n"
"v_mfma_f32_16x16x32_bf16 v[68:71], v[228:231], v[40:43], v[68:71]\nv_mfma_f32_16x16x32_bf16 v[72:75], a[96:99], v[40:43], v[72:75]\nv_mfma_f32_16x16x32_bf16 v[76:79], a[100:103], v[40:43], v[76:79]\nds_read_b128 v[40:43], v32 offset:0\n"
"s_waitcnt lgkmcnt(3)\nv_mfma_f32_16x16x32_bf16 v[80:83], v[224:227], v[44:47], v[80:83]\nv_add_u32 v33, s54, v33\nv_mfma_f32_16x16x32_bf16 v[84:87], v[228:231], v[44:47], v[84:87]\n"
"v_mfma_f32_16x16x32_bf16 v[88:91], a[96:99], v[44:47], v[88:91]\nv_mfma_f32_16x16x32_bf16 v[92:95], a[100:103], v[44:47], v[92:95]\nds_read_b128 v[44:47], v33 offset:0\ns_waitcnt lgkmcnt(3)\n"
"v_mfma_f32_16x16x32_bf16 v[64:67], v[232:235], v[48:51], v[64:67]\nv_add_u32 v34, s54, v34\nv_mfma_f32_16x16x32_bf16 v[68:71], v[236:239], v[48:51], v[68:71]\nv_mfma_f32_16x16x32_bf16 v[72:75], a[104:107], v[48:51], v[72:75]\n"
"v_mfma_f32_16x16x32_bf16 v[76:79], a[108:111], v[48:51], v[76:79]\nds_read_b128 v[48:51], v34 offset:0\ns_waitcnt lgkmcnt(3)\nv_mfma_f32_16x16x32_bf16 v[80:83], v[232:235], v[52:55], v[80:83]\n"
"v_add_u32 v35, s54, v35\nv_mfma_f32_16x16x32_bf16 v[84:87], v[236:239], v[52:55], v[84:87]\nv_mfma_f32_16x16x32_bf16 v[88:91], a[104:107], v[52:55], v[88:91]\nv_mfma_f32_16x16x32_bf16 v[92:95], a[108:111], v[52:55], v[92:95]\n"
"ds_read_b128 v[52:55], v35 offset:0\ns_mov_b32 s52, s47\ns_add_u32 s47, s51, s32\ns_add_u32 s51, s51, s54\n"
"s_add_u32 s49, s49, 28672\ns_waitcnt vmcnt(7)\ns_barrier\ns_waitcnt lgkmcnt(3)\n"
"v_mfma_f32_16x16x32_bf16 v[96:99], v[128:131], v[40:43], 0\nv_mov_b32 v36, s48\ns_add_u32 s48, s48, 1\ns_nop 0\n"
"buffer_store_dword v36, v19, s[24:27], s33 offen sc1\nv_mfma_f32_16x16x32_bf16 v[100:103], v[132:135], v[40:43], 0\nv_mfma_f32_16x16x32_bf16 v[104:107], a[0:3], v[40:43], 0\nv_mfma_f32_16x16x32_bf16 v[108:111], a[4:7], v[40:43], 0\n"
"ds_read_b128 v[40:43], v32 offset:4096\nv_cvt_pk_bf16_f32 v56, v64, v65\ns_waitcnt lgkmcnt(3)\nv_mfma_f32_16x16x32_bf16 v[112:115], v[128:131], v[44:47], 0\n"
"v_cvt_pk_bf16_f32 v57, v66, v67\nv_mfma_f32_16x16x32_bf16 v[116:119], v[132:135], v[44:47], 0\nv_cvt_pk_bf16_f32 v58, v68, v69\nv_mfma_f32_16x16x32_bf16 v[120:123], a[0:3], v[44:47], 0\n"
"v_cvt_pk_bf16_f32 v59, v70, v71\nv_mfma_f32_16x16x32_bf16 v[124:127], a[4:7], v[44:47], 0\nds_read_b128 v[44:47], v33 offset:4096\ns_waitcnt lgkmcnt(3)\n"
"v_mfma_f32_16x16x32_bf16 v[96:99], v[136:139], v[48:51], v[96:99]\nv_mfma_f32_16x16x32_bf16 v[100:103], v[140:143], v[48:51], v[100:103]\nbuffer_store_dwordx4 v[56:59], v15, s[20:23], s50 offen sc1\nv_mfma_f32_16x16x32_bf16 v[104:107], a[8:11], v[48:51], v[104:107]\n"
"v_cvt_pk_bf16_f32 v60, v80, v81\nv_mfma_f32_16x16x32_bf16 v[108:111], a[12:15], v[48:51], v[108:111]\nds_read_b128 v[48:51], v34 offset:4096\nv_cvt_pk_bf16_f32 v61, v82, v83\n"
"s_waitcnt lgkmcnt(3)\nv_mfma_f32_16x16x32_bf16 v[112:115], v[136:139], v[52:55], v[112:115]\nv_cvt_pk_bf16_f32 v62, v84, v85\nv_mfma_f32_16x16x32_bf16 v[116:119], v[140:143], v[52:55], v[116:119]\n"
"v_cvt_pk_bf16_f32 v63, v86, v87\nv_mfma_f32_16x16x32_bf16 v[120:123], a[8:11], v[52:55], v[120:123]\nv_mfma_f32_16x16x32_bf16 v[124:127], a[12:15], v[52:55], v[124:127]\nds_read_b128 v[52:55], v35 offset:4096\n"
"s_waitcnt lgkmcnt(3)\nv_mfma_f32_16x16x32_bf16 v[96:99], v[144:147], v[40:43], v[96:99]\nbuffer_store_dwordx4 v[60:63], v16, s[20:23], s50 offen sc1\nv_mfma_f32_16x16x32_bf16 v[100:103], v[148:151], v[40:43], v[100:103]\n"
"v_cvt_pk_bf16_f32 v56, v72, v73\nv_mfma_f32_16x16x32_bf16 v[104:107], a[16:19], v[40:43], v[104:107]\nv_cvt_pk_bf16_f32 v57, v74, v75\nv_mfma_f32_16x16x32_bf16 v[108:111], a[20:23], v[40:43], v[108:111]\n"
"ds_read_b128 v[40:43], v32 offset:8192\nv_cvt_pk_bf16_f32 v58, v76, v77\ns_waitcnt lgkmcnt(3)\nv_mfma_f32_16x16x32_bf16 v[112:115], v[144:147], v[44:47], v[112:115]\n"
"v_cvt_pk_bf16_f32 v59, v78, v79\nv_mfma_f32_16x16x32_bf16 v[116:119], v[148:151], v[44:47], v[116:119]\nv_mfma_f32_16x16x32_bf16 v[120:123], a[16:19], v[44:47], v[120:123]\nv_mfma_f32_16x16x32_bf16 v[124:127], a[20:23], v[44:47], v[124:127]\n"
"ds_read_b128 v[44:47], v33 offset:8192\nbuffer_store_dwordx4 v[56:59], v17, s[20:23], s50 offen sc1\ns_waitcnt lgkmcnt(3)\nv_mfma_f32_16x16x32_bf16 v[96:99], v[152:155], v[48:51], v[96:99]\n"
"v_cvt_pk_bf16_f32 v60, v88, v89\nv_mfma_f32_16x16x32_bf16 v[100:103], v[156:159], v[48:51], v[100:103]\nv_cvt_pk_bf16_f32 v61, v90, v91\nv_mfma_f32_16x16x32_bf16 v[104:107], a[24:27], v[48:51], v[104:107]\n"
"v_cvt_pk_bf16_f32 v62, v92, v93\nv_mfma_f32_16x16x32_bf16 v[108:111], a[28:31], v[48:51], v[108:111]\nds_read_b128 v[48:51], v34 offset:8192\nv_cvt_pk_bf16_f32 v63, v94, v95\n"
"s_waitcnt lgkmcnt(3)\nv_mfma_f32_16x16x32_bf16 v[112:115], v[152:155], v[52:55], v[112:115]\nv_mfma_f32_16x16x32_bf16 v[116:119], v[156:159], v[52:55], v[116:119]\nv_mfma_f32_16x16x32_bf16 v[120:123], a[24:27], v[52:55], v[120:123]\n"
"buffer_store_dwordx4 v[60:63], v18, s[20:23], s50 offen sc1\nv_mfma_f32_16x16x32_bf16 v[124:127], a[28:31], v[52:55], v[124:127]\nds_read_b128 v[52:55], v35 offset:8192\ns_add_u32 s50, s50, s36\n"
"s_waitcnt lgkmcnt(3)\nv_mfma_f32_16x16x32_bf16 v[96:99], v[160:163], v[40:43], v[96:99]\ns_add_u32 m0, s52, 0\nv_mfma_f32_16x16x32_bf16 v[100:103], v[164:167], v[40:43], v[100:103]\n"
"s_add_u32 s53, s49, 0\nv_mfma_f32_16x16x32_bf16 v[104:107], a[32:35], v[40:43], v[104:107]\nbuffer_load_dwordx4 v10, s[12:15], s53 offen lds\nv_mfma_f32_16x16x32_bf16 v[108:111], a[36:39], v[40:43], v[108:111]\n"
"ds_read_b128 v[40:43], v32 offset:12288\ns_waitcnt lgkmcnt(3)\nv_mfma_f32_16x16x32_bf16 v[112:115], v[160:163], v[44:47], v[112:115]\nv_mfma_f32_16x16x32_bf16 v[116:119], v[164:167], v[44:47], v[116:119]\n"
"v_mfma_f32_16x16x32_bf16 v[120:123], a[32:35], v[44:47], v[120:123]\nv_mfma_f32_16x16x32_bf16 v[124:127], a[36:39], v[44:47], v[124:127]\nds_read_b128 v[44:47], v33 offset:12288\ns_waitcnt lgkmcnt(3)\n"
"v_mfma_f32_16x16x32_bf16 v[96:99], v[168:171], v[48:51], v[96:99]\nv_mfma_f32_16x16x32_bf16 v[100:103], v[172:175], v[48:51], v[100:103]\nv_mfma_f32_16x16x32_bf16 v[104:107], a[40:43], v[48:51], v[104:107]\nv_mfma_f32_16x16x32_bf16 v[108:111], a[44:47], v[48:51], v[108:111]\n"
"ds_read_b128 v[48:51], v34 offset:12288\ns_add_u32 m0, s52, 4096\ns_waitcnt lgkmcnt(3)\nv_mfma_f32_16x16x32_bf16 v[112:115], v[168:171], v[52:55], v[112:115]\n"
"s_add_u32 s53, s49, 128\nv_mfma_f32_16x16x32_bf16 v[116:119], v[172:175], v[52:55], v[116:119]\nbuffer_load_dwordx4 v10, s[12:15], s53 offen lds\nv_mfma_f32_16x16x32_bf16 v[120:123], a[40:43], v[52:55], v[120:123]\n"
"v_mfma_f32_16x16x32_bf16 v[124:127], a[44:47], v[52:55], v[124:127]\nds_read_b128 v[52:55], v35 offset:12288\ns_waitcnt lgkmcnt(3)\nv_mfma_f32_16x16x32_bf16 v[96:99], v[176:179], v[40:43], v[96:99]\n"
"v_mfma_f32_16x16x32_bf16 v[100:103], v[180:183], v[40:43], v[100:103]\nv_mfma_f32_16x16x32_bf16 v[104:107], a[48:51], v[40:43], v[104:107]\nv_mfma_f32_16x16x32_bf16 v[108:111], a[52:55], v[40:43], v[108:111]\nds_read_b128 v[40:43], v32 offset:16384\n"
"s_waitcnt lgkmcnt(3)\nv_mfma_f32_16x16x32_bf16 v[112:115], v[176:179], v[44:47], v[112:115]\nv_mfma_f32_16x16x32_bf16 v[116:119], v[180:183], v[44:47], v[116:119]\nv_mfma_f32_16x16x32_bf16 v[120:123], a[48:51], v[44:47], v[120:123]\n"
"s_add_u32 m0, s52, 8192\nv_mfma_f32_16x16x32_bf16 v[124:127], a[52:55], v[44:47], v[124:127]\nds_read_b128 v[44:47], v33 offset:16384\ns_add_u32 s53, s49, 256\n"
"s_waitcnt lgkmcnt(3)\nv_mfma_f32_16x16x32_bf16 v[96:99], v[184:187], v[48:51], v[96:99]\nbuffer_load_dwordx4 v10, s[12:15], s53 offen lds\nv_mfma_f32_16x16x32_bf16 v[100:103], v[188:191], v[48:51], v[100:103]\n"
"v_mfma_f32_16x16x32_bf16 v[104:107], a[56:59], v[48:51], v[104:107]\nv_mfma_f32_16x16x32_bf16 v[108:111], a[60:63], v[48:51], v[108:111]\nds_read_b128 v[48:51], v34 offset:16384\ns_waitcnt lgkmcnt(3)\n"
"v_mfma_f32_16x16x32_bf16 v[112:115], v[184:187], v[52:55], v[112:115]\nv_mfma_f32_16x16x32_bf16 v[116:119], v[188:191], v[52:55], v[116:119]\nv_mfma_f32_16x16x32_bf16 v[120:123], a[56:59], v[52:55], v[120:123]\nv_mfma_f32_16x16x32_bf16 v[124:127], a[60:63], v[52:55], v[124:127]\n"
"ds_read_b128 v[52:55], v35 offset:16384\ns_waitcnt lgkmcnt(3)\nv_mfma_f32_16x16x32_bf16 v[96:99], v[192:195], v[40:43], v[96:99]\nv_mfma_f32_16x16x32_bf16 v[100:103], v[196:199], v[40:43], v[100:103]\n"
"s_add_u32 m0, s52, 12288\nv_mfma_f32_16x16x32_bf16 v[104:107], a[64:67], v[40:43], v[104:107]\ns_add_u32 s53, s49, 384\nv_mfma_f32_16x16x32_bf16 v[108:111], a[68:71], v[40:43], v[108:111]\n"
"ds_read_b128 v[40:43], v32 offset:20480\nbuffer_load_dwordx4 v10, s[12:15], s53 offen lds\ns_waitcnt lgkmcnt(3)\nv_mfma_f32_16x16x32_bf16 v[112:115], v[192:195], v[44:47], v[112:115]\n"
"v_mfma_f32_16x16x32_bf16 v[116:119], v[196:199], v[44:47], v[116:119]\nv_mfma_f32_16x16x32_bf16 v[120:123], a[64:67], v[44:47], v[120:123]\nv_mfma_f32_16x16x32_bf16 v[124:127], a[68:71], v[44:47], v[124:127]\nds_read_b128 v[44:47], v33 offset:20480\n"
"s_waitcnt lgkmcnt(3)\nv_mfma_f32_16x16x32_bf16 v[96:99], v[200:203], v[48:51], v[96:99]\nv_mfma_f32_16x16x32_bf16 v[100:103], v[204:207], v[48:51], v[100:103]\nv_mfma_f32_16x16x32_bf16 v[104:107], a[72:75], v[48:51], v[104:107]\n"
"v_mfma_f32_16x16x32_bf16 v[108:111], a[76:79], v[48:51], v[108:111]\nds_read_b128 v[48:51], v34 offset:20480\ns_waitcnt lgkmcnt(3)\nv_mfma_f32_16x16x32_bf16 v[112:115], v[200:203], v[52:55], v[112:115]\n"
"s_add_u32 m0, s52, 16384\nv_mfma_f32_16x16x32_bf16 v[116:119], v[204:207], v[52:55], v[116:119]\ns_add_u32 s53, s49, 512\nv_mfma_f32_16x16x32_bf16 v[120:123], a[72:75], v[52:55], v[120:123]\n"
"buffer_load_dwordx4 v10, s[12:15], s53 offen lds\nv_mfma_f32_16x16x32_bf16 v[124:127], a[76:79], v[52:55], v[124:127]\nds_read_b128 v[52:55], v35 offset:20480\ns_waitcnt lgkmcnt(3)\n"
"v_mfma_f32_16x16x32_bf16 v[96:99], v[208:211], v[40:43], v[96:99]\nv_mfma_f32_16x16x32_bf16 v[100:103], v[212:215], v[40:43], v[100:103]\nv_mfma_f32_16x16x32_bf16 v[104:107], a[80:83], v[40:43], v[104:107]\nv_mfma_f32_16x16x32_bf16 v[108:111], a[84:87], v[40:43], v[108:111]\n"
"ds_read_b128 v[40:43], v32 offset:24576\ns_waitcnt lgkmcnt(3)\nv_mfma_f32_16x16x32_bf16 v[112:115], v[208:211], v[44:47], v[112:115]\nv_mfma_f32_16x16x32_bf16 v[116:119], v[212:215], v[44:47], v[116:119]\n"
"v_mfma_f32_16x16x32_bf16 v[120:123], a[80:83], v[44:47], v[120:123]\nv_mfma_f32_16x16x32_bf16 v[124:127], a[84:87], v[44:47], v[124:127]\nds_read_b128 v[44:47], v33 offset:24576\ns_add_u32 m0, s52, 20480\n"
"s_waitcnt lgkmcnt(3)\nv_mfma_f32_16x16x32_bf16 v[96:99], v[216:219], v[48:51], v[96:99]\ns_add_u32 s54, s51, 32768\ns_cmp_eq_u32 s54, 163840\n"
"v_mfma_f32_16x16x32_bf16 v[100:103], v[220:223], v[48:51], v[100:103]\nv_mfma_f32_16x16x32_bf16 v[104:107], a[88:91], v[48:51], v[104:107]\nv_mfma_f32_16x16x32_bf16 v[108:111], a[92:95], v[48:51], v[108:111]\ns_cselect_b32 s54, 0, s54\n"
"s_sub_u32 s54, s54, s51\nds_read_b128 v[48:51], v34 offset:24576\ns_add_u32 s53, s49, 640\ns_waitcnt lgkmcnt(3)\n"
"v_mfma_f32_16x16x32_bf16 v[112:115], v[216:219], v[52:55], v[112:115]\nbuffer_load_dwordx4 v10, s[12:15], s53 offen lds\nv_mfma_f32_16x16x32_bf16 v[116:119], v[220:223], v[52:55], v[116:119]\nv_mfma_f32_16x16x32_bf16 v[120:123], a[88:91], v[52:55], v[120:123]\n"
"v_mfma_f32_16x16x32_bf16 v[124:127], a[92:95], v[52:55], v[124:127]\nds_read_b128 v[52:55], v35 offset:24576\ns_waitcnt lgkmcnt(3)\nv_mfma_f32_16x16x32_bf16 v[96:99], v[224:227], v[40:43], v[96:99]\n"
"v_add_u32 v32, s54, v32\nv_mfma_f32_16x16x32_bf16 v[100:103], v[228:231], v[40:43], v[100:103]\nv_mfma_f32_16x16x32_bf16 v[104:107], a[96:99], v[40:43], v[104:107]\nv_mfma_f32_16x16x32_bf16 v[108:111], a[100:103], v[40:43], v[108:111]\n"
"ds_read_b128 v[40:43], v32 offset:0\ns_waitcnt lgkmcnt(3)\nv_mfma_f32_16x16x32_bf16 v[112:115], v[224:227], v[44:47], v[112:115]\nv_add_u32 v33, s54, v33\n"
"v_mfma_f32_16x16x32_bf16 v[116:119], v[228:231], v[44:47], v[116:119]\ns_add_u32 m0, s52, 24576\nv_mfma_f32_16x16x32_bf16 v[120:123], a[96:99], v[44:47], v[120:123]\ns_add_u32 s53, s49, 768\n"
"v_mfma_f32_16x16x32_bf16 v[124:127], a[100:103], v[44:47], v[124:127]\nds_read_b128 v[44:47], v33 offset:0\nbuffer_load_dwordx4 v10, s[12:15], s53 offen lds\ns_waitcnt lgkmcnt(3)\n"
"v_mfma_f32_16x16x32_bf16 v[96:99], v[232:235], v[48:51], v[96:99]\nv_add_u32 v34, s54, v34\nv_mfma_f32_16x16x32_bf16 v[100:103], v[236:239], v[48:51], v[100:103]\nv_mfma_f32_16x16x32_bf16 v[104:107], a[104:107], v[48:51], v[104:107]\n"
"v_mfma_f32_16x16x32_bf16 v[108:111], a[108:111], v[48:51], v[108:111]\nds_read_b128 v[48:51], v34 offset:0\ns_waitcnt lgkmcnt(3)\nv_mfma_f32_16x16x32_bf16 v[112:115], v[232:235], v[52:55], v[112:115]\n"
"v_add_u32 v35, s54, v35\nv_mfma_f32_16x16x32_bf16 v[116:119], v[236:239], v[52:55], v[116:119]\nv_mfma_f32_16x16x32_bf16 v[120:123], a[104:107], v[52:55], v[120:123]\nv_mfma_f32_16x16x32_bf16 v[124:127], a[108:111], v[52:55], v[124:127]\n"
"ds_read_b128 v[52:55], v35 offset:0\ns_mov_b32 s52, s47\ns_add_u32 s47, s51, s32\ns_add_u32 s51, s51, s54\n"
"s_add_u32 s49, s49, 28672\n.p2align 6\nfg0_loop:\ns_waitcnt vmcnt(7)\n"
"s_barrier\ns_waitcnt lgkmcnt(3)\nv_mfma_f32_16x16x32_bf16 v[64:67], v[128:131], v[40:43], 0\nv_mov_b32 v36, s48\n"
"s_add_u32 s48, s48, 1\ns_nop 0\nbuffer_store_dword v36, v19, s[24:27], s33 offen sc1\nv_mfma_f32_16x16x32_bf16 v[68:71], v[132:135], v[40:43], 0\n"
"v_mfma_f32_16x16x32_bf16 v[72:75], a[0:3], v[40:43], 0\nv_mfma_f32_16x16x32_bf16 v[76:79], a[4:7], v[40:43], 0\nds_read_b128 v[40:43], v32 offset:4096\nv_cvt_pk_bf16_f32 v56, v96, v97\n"
"s_waitcnt lgkmcnt(3)\nv_mfma_f32_16x16x32_bf16 v[80:83], v[128:131], v[44:47], 0\nv_cvt_pk_bf16_f32 v57, v98, v99\nv_mfma_f32_16x16x32_bf16 v[84:87], v[132:135], v[44:47], 0\n"
"v_cvt_pk_bf16_f32 v58, v100, v101\nv_mfma_f32_16x16x32_bf16 v[88:91], a[0:3], v[44:47], 0\nv_cvt_pk_bf16_f32 v59, v102, v103\nv_mfma_f32_16x16x32_bf16 v[92:95], a[4:7], v[44:47], 0\n"
"ds_read_b128 v[44:47], v33 offset:4096\ns_waitcnt lgkmcnt(3)\nv_mfma_f32_16x16x32_bf16 v[64:67], v[136:139], v[48:51], v[64:67]\nv_mfma_f32_16x16x32_bf16 v[68:71], v[140:143], v[48:51], v[68:71]\n"
"buffer_store_dwordx4 v[56:59], v15, s[20:23], s50 offen sc1\nv_mfma_f32_16x16x32_bf16 v[72:75], a[8:11], v[48:51], v[72:75]\nv_cvt_pk_bf16_f32 v60, v112, v113\nv_mfma_f32_16x16x32_bf16 v[76:79], a[12:15], v[48:51], v[76:79]\n"
"ds_read_b128 v[48:51], v34 offset:4096\nv_cvt_pk_bf16_f32 v61, v114, v115\ns_waitcnt lgkmcnt(3)\nv_mfma_f32_16x16x32_bf16 v[80:83], v[136:139], v[52:55], v[80:83]\n"
"v_cvt_pk_bf16_f32 v62, v116, v117\nv_mfma_f32_16x16x32_bf16 v[84:87], v[140:143], v[52:55], v[84:87]\nv_cvt_pk_bf16_f32 v63, v118, v119\nv_mfma_f32_16x16x32_bf16 v[88:91], a[8:11], v[52:55], v[88:91]\n"
"v_mfma_f32_16x16x32_bf16 v[92:95], a[12:15], v[52:55], v[92:95]\nds_read_b128 v[52:55], v35 offset:4096\ns_waitcnt lgkmcnt(3)\nv_mfma_f32_16x16x32_bf16 v[64:67], v[144:147], v[40:43], v[64:67]\n"
"buffer_store_dwordx4 v[60:63], v16, s[20:23], s50 offen sc1\nv_mfma_f32_16x16x32_bf16 v[68:71], v[148:151], v[40:43], v[68:71]\nv_cvt_pk_bf16_f32 v56, v104, v105\nv_mfma_f32_16x16x32_bf16 v[72:75], a[16:19], v[40:43], v[72:75]\n"
"v_cvt_pk_bf16_f32 v57, v106, v107\nv_mfma_f32_16x16x32_bf16 v[76:79], a[20:23], v[40:43], v[76:79]\nds_read_b128 v[40:43], v32 offset:8192\nv_cvt_pk_bf16_f32 v58, v108, v109\n"
"s_waitcnt lgkmcnt(3)\nv_mfma_f32_16x16x32_bf16 v[80:83], v[144:147], v[44:47], v[80:83]\nv_cvt_pk_bf16_f32 v59, v110, v111\nv_mfma_f32_16x16x32_bf16 v[84:87], v[148:151], v[44:47], v[84:87]\n"
"v_mfma_f32_16x16x32_bf16 v[88:91], a[16:19], v[44:47], v[88:91]\nv_mfma_f32_16x16x32_bf16 v[92:95], a[20:23], v[44:47], v[92:95]\nds_read_b128 v[44:47], v33 offset:8192\nbuffer_store_dwordx4 v[56:59], v17, s[20:23], s50 offen sc1\n"
"s_waitcnt lgkmcnt(3)\nv_mfma_f32_16x16x32_bf16 v[64:67], v[152:155], v[48:51], v[64:67]\nv_cvt_pk_bf16_f32 v60, v120, v121\nv_mfma_f32_16x16x32_bf16 v[68:71], v[156:159], v[48:51], v[68:71]\n"
"v_cvt_pk_bf16_f32 v61, v122, v123\nv_mfma_f32_16x16x32_bf16 v[72:75], a[24:27], v[48:51], v[72:75]\nv_cvt_pk_bf16_f32 v62, v124, v125\nv_mfma_f32_16x16x32_bf16 v[76:79], a[28:31], v[48:51], v[76:79]\n"
"ds_read_b128 v[48:51], v34 offset:8192\nv_cvt_pk_bf16_f32 v63, v126, v127\ns_waitcnt lgkmcnt(3)\nv_mfma_f32_16x16x32_bf16 v[80:83], v[152:155], v[52:55], v[80:83]\n"
"v_mfma_f32_16x16x32_bf16 v[84:87], v[156:159], v[52:55], v[84:87]\nv_mfma_f32_16x16x32_bf16 v[88:91], a[24:27], v[52:55], v[88:91]\nbuffer_store_dwordx4 v[60:63], v18, s[20:23], s50 offen sc1\nv_mfma_f32_16x16x32_bf16 v[92:95], a[28:31], v[52:55], v[92:95]\n"
"ds_read_b128 v[52:55], v35 offset:8192\ns_add_u32 s50, s50, s36\ns_waitcnt lgkmcnt(3)\nv_mfma_f32_16x16x32_bf16 v[64:67], v[160:163], v[40:43], v[64:67]\n"
"s_add_u32 m0, s52, 0\nv_mfma_f32_16x16x32_bf16 v[68:71], v[164:167], v[40:43], v[68:71]\ns_add_u32 s53, s49, 0\nv_mfma_f32_16x16x32_bf16 v[72:75], a[32:35], v[40:43], v[72:75]\n"
"buffer_load_dwordx4 v10, s[12:15], s53 offen lds\nv_mfma_f32_16x16x32_bf16 v[76:79], a[36:39], v[40:43], v[76:79]\nds_read_b128 v[40:43], v32 offset:12288\ns_waitcnt lgkmcnt(3)\n"
"v_mfma_f32_16x16x32_bf16 v[80:83], v[160:163], v[44:47], v[80:83]\nv_mfma_f32_16x16x32_bf16 v[84:87], v[164:167], v[44:47], v[84:87]\nv_mfma_f32_16x16x32_bf16 v[88:91], a[32:35], v[44:47], v[88:91]\nv_mfma_f32_16x16x32_bf16 v[92:95], a[36:39], v[44:47], v[92:95]\n"
"ds_read_b128 v[44:47], v33 offset:12288\ns_waitcnt lgkmcnt(3)\nv_mfma_f32_16x16x32_bf16 v[64:67], v[168:171], v[48:51], v[64:67]\nv_mfma_f32_16x16x32_bf16 v[68:71], v[172:175], v[48:51], v[68:71]\n"
"v_mfma_f32_16x16x32_bf16 v[72:75], a[40:43], v[48:51], v[72:75]\nv_mfma_f32_16x16x32_bf16 v[76:79], a[44:47], v[48:51], v[76:79]\nds_read_b128 v[48:51], v34 offset:12288\ns_add_u32 m0, s52, 4096\n"
"s_waitcnt lgkmcnt(3)\nv_mfma_f32_16x16x32_bf16 v[80:83], v[168:171], v[52:55], v[80:83]\ns_add_u32 s53, s49, 128\nv_mfma_f32_16x16x32_bf16 v[84:87], v[172:175], v[52:55], v[84:87]\n"
"buffer_load_dwordx4 v10, s[12:15], s53 offen lds\nv_mfma_f32_16x16x32_bf16 v[88:91], a[40:43], v[52:55], v[88:91]\nv_mfma_f32_16x16x32_bf16 v[92:95], a[44:47], v[52:55], v[92:95]\nds_read_b128 v[52:55], v35 offset:12288\n"
"s_waitcnt lgkmcnt(3)\nv_mfma_f32_16x16x32_bf16 v[64:67], v[176:179], v[40:43], v[64:67]\nv_mfma_f32_16x16x32_bf16 v[68:71], v[180:183], v[40:43], v[68:71]\nv_mfma_f32_16x16x32_bf16 v[72:75], a[48:51], v[40:43], v[72:75]\n"
"v_mfma_f32_16x16x32_bf16 v[76:79], a[52:55], v[40:43], v[76:79]\nds_read_b128 v[40:43], v32 offset:16384\ns_waitcnt lgkmcnt(3)\nv_mfma_f32_16x16x32_bf16 v[80:83], v[176:179], v[44:47], v[80:83]\n"
"v_mfma_f32_16x16x32_bf16 v[84:87], v[180:183], v[44:47], v[84:87]\nv_mfma_f32_16x16x32_bf16 v[88:91], a[48:51], v[44:47], v[88:91]\ns_add_u32 m0, s52, 8192\nv_mfma_f32_16x16x32_bf16 v[92:95], a[52:55], v[44:47], v[92:95]\n"
"ds_read_b128 v[44:47], v33 offset:16384\ns_add_u32 s53, s49, 256\ns_waitcnt lgkmcnt(3)\nv_mfma_f32_16x16x32_bf16 v[64:67], v[184:187], v[48:51], v[64:67]\n"
"buffer_load_dwordx4 v10, s[12:15], s53 offen lds\nv_mfma_f32_16x16x32_bf16 v[68:71], v[188:191], v[48:51], v[68:71]\nv_mfma_f32_16x16x32_bf16 v[72:75], a[56:59], v[48:51], v[72:75]\nv_mfma_f32_16x16x32_bf16 v[76:79], a[60:63], v[48:51], v[76:79]\n"
"ds_read_b128 v[48:51], v34 offset:16384\ns_waitcnt lgkmcnt(3)\nv_mfma_f32_16x16x32_bf16 v[80:83], v[184:187], v[52:55], v[80:83]\nv_mfma_f32_16x16x32_bf16 v[84:87], v[188:191], v[52:55], v[84:87]\n"
"v_mfma_f32_16x16x32_bf16 v[88:91], a[56:59], v[52:55], v[88:91]\nv_mfma_f32_16x16x32_bf16 v[92:95], a[60:63], v[52:55], v[92:95]\nds_read_b128 v[52:55], v35 offset:16384\ns_waitcnt lgkmcnt(3)\n"
"v_mfma_f32_16x16x32_bf16 v[64:67], v[192:195], v[40:43], v[64:67]\nv_mfma_f32_16x16x32_bf16 v[68:71], v[196:199], v[40:43], v[68:71]\ns_add_u32 m0, s52, 12288\nv_mfma_f32_16x16x32_bf16 v[72:75], a[64:67], v[40:43], v[72:75]\n"
"s_add_u32 s53, s49, 384\nv_mfma_f32_16x16x32_bf16 v[76:79], a[68:71], v[40:43], v[76:79]\nds_read_b128 v[40:43], v32 offset:20480\nbuffer_load_dwordx4 v10, s[12:15], s53 offen lds\n"
"s_waitcnt lgkmcnt(3)\nv_mfma_f32_16x16x32_bf16 v[80:83], v[192:195], v[44:47], v[80:83]\nv_mfma_f32_16x16x32_bf16 v[84:87], v[196:199], v[44:47], v[84:87]\nv_mfma_f32_16x16x32_bf16 v[88:91], a[64:67], v[44:47], v[88:91]\n"
"v_mfma_f32_16x16x32_bf16 v[92:95], a[68:71], v[44:47], v[92:95]\nds_read_b128 v[44:47], v33 offset:20480\ns_waitcnt lgkmcnt(3)\nv_mfma_f32_16x16x32_bf16 v[64:67], v[200:203], v[48:51], v[64:67]\n"
"v_mfma_f32_16x16x32_bf16 v[68:71], v[204:207], v[48:51], v[68:71]\nv_mfma_f32_16x16x32_bf16 v[72:75], a[72:75], v[48:51], v[72:75]\nv_mfma_f32_16x16x32_bf16 v[76:79], a[76:79], v[48:51], v[76:79]\nds_read_b128 v[48:51], v34 offset:20480\n"
"s_waitcnt lgkmcnt(3)\nv_mfma_f32_16x16x32_bf16 v[80:83], v[200:203], v[52:55], v[80:83]\ns_add_u32 m0, s52, 16384\nv_mfma_f32_16x16x32_bf16 v[84:87], v[204:207], v[52:55], v[84:87]\n"
"s_add_u32 s53, s49, 512\nv_mfma_f32_16x16x32_bf16 v[88:91], a[72:75], v[52:55], v[88:91]\nbuffer_load_dwordx4 v10, s[12:15], s53 offen lds\nv_mfma_f32_16x16x32_bf16 v[92:95], a[76:79], v[52:55], v[92:95]\n"
"ds_read_b128 v[52:55], v35 offset:20480\ns_waitcnt lgkmcnt(3)\nv_mfma_f32_16x16x32_bf16 v[64:67], v[208:211], v[40:43], v[64:67]\nv_mfma_f32_16x16x32_bf16 v[68:71], v[212:215], v[40:43], v[68:71]\n"
"v_mfma_f32_16x16x32_bf16 v[72:75], a[80:83], v[40:43], v[72:75]\nv_mfma_f32_16x16x32_bf16 v[76:79], a[84:87], v[40:43], v[76:79]\nds_read_b128 v[40:43], v32 offset:24576\ns_waitcnt lgkmcnt(3)\n"
"v_mfma_f32_16x16x32_bf16 v[80:83], v[208:211], v[44:47], v[80:83]\nv_mfma_f32_16x16x32_bf16 v[84:87], v[212:215], v[44:47], v[84:87]\nv_mfma_f32_16x16x32_bf16 v[88:91], a[80:83], v[44:47], v[88:91]\nv_mfma_f32_16x16x32_bf16 v[92:95], a[84:87], v[44:47], v[92:95]\n"
"ds_read_b128 v[44:47], v33 offset:24576\ns_add_u32 m0, s52, 20480\ns_waitcnt lgkmcnt(3)\nv_mfma_f32_16x16x32_bf16 v[64:67], v[216:219], v[48:51], v[64:67]\n"
"s_add_u32 s54, s51, 32768\ns_cmp_eq_u32 s54, 163840\nv_mfma_f32_16x16x32_bf16 v[68:71], v[220:223], v[48:51], v[68:71]\nv_mfma_f32_16x16x32_bf16 v[72:75], a[88:91], v[48:51], v[72:75]\n"
"v_mfma_f32_16x16x32_bf16 v[76:79], a[92:95], v[48:51], v[76:79]\ns_cselect_b32 s54, 0, s54\ns_sub_u32 s54, s54, s51\nds_read_b128 v[48:51], v34 offset:24576\n"
"s_add_u32 s53, s49, 640\ns_waitcnt lgkmcnt(3)\nv_mfma_f32_16x16x32_bf16 v[80:83], v[216:219], v[52:55], v[80:83]\nbuffer_load_dwordx4 v10, s[12:15], s53 offen lds\n"
"v_mfma_f32_16x16x32_bf16 v[84:87], v[220:223], v[52:55], v[84:87]\nv_mfma_f32_16x16x32_bf16 v[88:91], a[88:91], v[52:55], v[88:91]\nv_mfma_f32_16x16x32_bf16 v[92:95], a[92:95], v[52:55], v[92:95]\nds_read_b128 v[52:55], v35 offset:24576\n"
"s_waitcnt lgkmcnt(3)\nv_mfma_f32_16x16x32_bf16 v[64:67], v[224:227], v[40:43], v[64:67]\nv_add_u32 v32, s54, v32\nv_mfma_f32_16x16x32_bf16 v[68:71], v[228:231], v[40:43], v[68:71]\n"
"v_mfma_f32_16x16x32_bf16 v[72:75], a[96:99], v[40:43], v[72:75]\nv_mfma_f32_16x16x32_bf16 v[76:79], a[100:103], v[40:43], v[76:79]\nds_read_b128 v[40:43], v32 offset:0\ns_waitcnt lgkmcnt(3)\n"
"v_mfma_f32_16x16x32_bf16 v[80:83], v[224:227], v[44:47], v[80:83]\nv_add_u32 v33, s54, v33\nv_mfma_f32_16x16x32_bf16 v[84:87], v[228:231], v[44:47], v[84:87]\ns_add_u32 m0, s52, 24576\n"
"v_mfma_f32_16x16x32_bf16 v[88:91], a[96:99], v[44:47], v[88:91]\ns_add_u32 s53, s49, 768\nv_mfma_f32_16x16x32_bf16 v[92:95], a[100:103], v[44:47], v[92:95]\nds_read_b128 v[44:47], v33 offset:0\n"
"buffer_load_dwordx4 v10, s[12:15], s53 offen lds\ns_waitcnt lgkmcnt(3)\nv_mfma_f32_16x16x32_bf16 v[64:67], v[232:235], v[48:51], v[64:67]\nv_add_u32 v34, s54, v34\n"
"v_mfma_f32_16x16x32_bf16 v[68:71], v[236:239], v[48:51], v[68:71]\nv_mfma_f32_16x16x32_bf16 v[72:75], a[104:107], v[48:51], v[72:75]\nv_mfma_f32_16x16x32_bf16 v[76:79], a[108:111], v[48:51], v[76:79]\nds_read_b128 v[48:51], v34 offset:0\n"
"s_waitcnt lgkmcnt(3)\nv_mfma_f32_16x16x32_bf16 v[80:83], v[232:235], v[52:55], v[80:83]\nv_add_u32 v35, s54, v35\nv_mfma_f32_16x16x32_bf16 v[84:87], v[236:239], v[52:55], v[84:87]\n"
"v_mfma_f32_16x16x32_bf16 v[88:91], a[104:107], v[52:55], v[88:91]\nv_mfma_f32_16x16x32_bf16 v[92:95], a[108:111], v[52:55], v[92:95]\nds_read_b128 v[52:55], v35 offset:0\ns_mov_b32 s52, s47\n"
"s_add_u32 s47, s51, s32\ns_add_u32 s51, s51, s54\ns_add_u32 s49, s49, 28672\ns_waitcnt vmcnt(7)\n"
"s_barrier\ns_waitcnt lgkmcnt(3)\nv_mfma_f32_16x16x32_bf16 v[96:99], v[128:131], v[40:43], 0\ns_add_u32 s48, s48, 1\n"
"v_mfma_f32_16x16x32_bf16 v[100:103], v[132:135], v[40:43], 0\nv_mfma_f32_16x16x32_bf16 v[104:107], a[0:3], v[40:43], 0\nv_mfma_f32_16x16x32_bf16 v[108:111], a[4:7], v[40:43], 0\nds_read_b128 v[40:43], v32 offset:4096\n"
"v_cvt_pk_bf16_f32 v56, v64, v65\ns_waitcnt lgkmcnt(3)\nv_mfma_f32_16x16x32_bf16 v[112:115], v[128:131], v[44:47], 0\nv_cvt_pk_bf16_f32 v57, v66, v67\n"
"v_mfma_f32_16x16x32_bf16 v[116:119], v[132:135], v[44:47], 0\nv_cvt_pk_bf16_f32 v58, v68, v69\nv_mfma_f32_16x16x32_bf16 v[120:123], a[0:3], v[44:47], 0\nv_cvt_pk_bf16_f32 v59, v70, v71\n"
"v_mfma_f32_16x16x32_bf16 v[124:127], a[4:7], v[44:47], 0\nds_read_b128 v[44:47], v33 offset:4096\ns_waitcnt lgkmcnt(3)\nv_mfma_f32_16x16x32_bf16 v[96:99], v[136:139], v[48:51], v[96:99]\n"
"v_mfma_f32_16x16x32_bf16 v[100:103], v[140:143], v[48:51], v[100:103]\nbuffer_store_dwordx4 v[56:59], v15, s[20:23], s50 offen sc1\nv_mfma_f32_16x16x32_bf16 v[104:107], a[8:11], v[48:51], v[104:107]\nv_cvt_pk_bf16_f32 v60, v80, v81\n"
"v_mfma_f32_16x16x32_bf16 v[108:111], a[12:15], v[48:51], v[108:111]\nds_read_b128 v[48:51], v34 offset:4096\nv_cvt_pk_bf16_f32 v61, v82, v83\ns_waitcnt lgkmcnt(3)\n"
"v_mfma_f32_16x16x32_bf16 v[112:115], v[136:139], v[52:55], v[112:115]\nv_cvt_pk_bf16_f32 v62, v84, v85\nv_mfma_f32_16x16x32_bf16 v[116:119], v[140:143], v[52:55], v[116:119]\nv_cvt_pk_bf16_f32 v63, v86, v87\n"
"v_mfma_f32_16x16x32_bf16 v[120:123], a[8:11], v[52:55], v[120:123]\nv_mfma_f32_16x16x32_bf16 v[124:127], a[12:15], v[52:55], v[124:127]\nds_read_b128 v[52:55], v35 offset:4096\ns_waitcnt lgkmcnt(3)\n"
"v_mfma_f32_16x16x32_bf16 v[96:99], v[144:147], v[40:43], v[96:99]\nbuffer_store_dwordx4 v[60:63], v16, s[20:23], s50 offen sc1\nv_mfma_f32_16x16x32_bf16 v[100:103], v[148:151], v[40:43], v[100:103]\nv_cvt_pk_bf16_f32 v56, v72, v73\n"
"v_mfma_f32_16x16x32_bf16 v[104:107], a[16:19], v[40:43], v[104:107]\nv_cvt_pk_bf16_f32 v57, v74, v75\nv_mfma_f32_16x16x32_bf16 v[108:111], a[20:23], v[40:43], v[108:111]\nds_read_b128 v[40:43], v32 offset:8192\n"
"v_cvt_pk_bf16_f32 v58, v76, v77\ns_waitcnt lgkmcnt(3)\nv_mfma_f32_16x16x32_bf16 v[112:115], v[144:147], v[44:47], v[112:115]\nv_cvt_pk_bf16_f32 v59, v78, v79\n"
"v_mfma_f32_16x16x32_bf16 v[116:119], v[148:151], v[44:47], v[116:119]\nv_mfma_f32_16x16x32_bf16 v[120:123], a[16:19], v[44:47], v[120:123]\nv_mfma_f32_16x16x32_bf16 v[124:127], a[20:23], v[44:47], v[124:127]\nds_read_b128 v[44:47], v33 offset:8192\n"
"buffer_store_dwordx4 v[56:59], v17, s[20:23], s50 offen sc1\ns_waitcnt lgkmcnt(3)\nv_mfma_f32_16x16x32_bf16 v[96:99], v[152:155], v[48:51], v[96:99]\nv_cvt_pk_bf16_f32 v60, v88, v89\n"
"v_mfma_f32_16x16x32_bf16 v[100:103], v[156:159], v[48:51], v[100:103]\nv_cvt_pk_bf16_f32 v61, v90, v91\nv_mfma_f32_16x16x32_bf16 v[104:107], a[24:27], v[48:51], v[104:107]\nv_cvt_pk_bf16_f32 v62, v92, v93\n"
"v_mfma_f32_16x16x32_bf16 v[108:111], a[28:31], v[48:51], v[108:111]\nds_read_b128 v[48:51], v34 offset:8192\nv_cvt_pk_bf16_f32 v63, v94, v95\ns_waitcnt lgkmcnt(3)\n"
"v_mfma_f32_16x16x32_bf16 v[112:115], v[152:155], v[52:55], v[112:115]\nv_mfma_f32_16x16x32_bf16 v[116:119], v[156:159], v[52:55], v[116:119]\nv_mfma_f32_16x16x32_bf16 v[120:123], a[24:27], v[52:55], v[120:123]\nbuffer_store_dwordx4 v[60:63], v18, s[20:23], s50 offen sc1\n"
"v_mfma_f32_16x16x32_bf16 v[124:127], a[28:31], v[52:55], v[124:127]\nds_read_b128 v[52:55], v35 offset:8192\ns_add_u32 s50, s50, s36\ns_waitcnt lgkmcnt(3)\n"
"v_mfma_f32_16x16x32_bf16 v[96:99], v[160:163], v[40:43], v[96:99]\ns_add_u32 m0, s52, 0\nv_mfma_f32_16x16x32_bf16 v[100:103], v[164:167], v[40:43], v[100:103]\ns_add_u32 s53, s49, 0\n"
"v_mfma_f32_16x16x32_bf16 v[104:107], a[32:35], v[40:43], v[104:107]\nbuffer_load_dwordx4 v10, s[12:15], s53 offen lds\nv_mfma_f32_16x16x32_bf16 v[108:111], a[36:39], v[40:43], v[108:111]\nds_read_b128 v[40:43], v32 offset:12288\n"
"s_waitcnt lgkmcnt(3)\nv_mfma_f32_16x16x32_bf16 v[112:115], v[160:163], v[44:47], v[112:115]\nv_mfma_f32_16x16x32_bf16 v[116:119], v[164:167], v[44:47], v[116:119]\nv_mfma_f32_16x16x32_bf16 v[120:123], a[32:35], v[44:47], v[120:123]\n"
"v_mfma_f32_16x16x32_bf16 v[124:127], a[36:39], v[44:47], v[124:127]\nds_read_b128 v[44:47], v33 offset:12288\ns_waitcnt lgkmcnt(3)\nv_mfma_f32_16x16x32_bf16 v[96:99], v[168:171], v[48:51], v[96:99]\n"
"v_mfma_f32_16x16x32_bf16 v[100:103], v[172:175], v[48:51], v[100:103]\nv_mfma_f32_16x16x32_bf16 v[104:107], a[40:43], v[48:51], v[104:107]\nv_mfma_f32_16x16x32_bf16 v[108:111], a[44:47], v[48:51], v[108:111]\nds_read_b128 v[48:51], v34 offset:12288\n"
"s_add_u32 m0, s52, 4096\ns_waitcnt lgkmcnt(3)\nv_mfma_f32_16x16x32_bf16 v[112:115], v[168:171], v[52:55], v[112:115]\ns_add_u32 s53, s49, 128\n"
"v_mfma_f32_16x16x32_bf16 v[116:119], v[172:175], v[52:55], v[116:119]\nbuffer_load_dwordx4 v10, s[12:15], s53 offen lds\nv_mfma_f32_16x16x32_bf16 v[120:123], a[40:43], v[52:55], v[120:123]\nv_mfma_f32_16x16x32_bf16 v[124:127], a[44:47], v[52:55], v[124:127]\n"
"ds_read_b128 v[52:55], v35 offset:12288\ns_waitcnt lgkmcnt(3)\nv_mfma_f32_16x16x32_bf16 v[96:99], v[176:179], v[40:43], v[96:99]\nv_mfma_f32_16x16x32_bf16 v[100:103], v[180:183], v[40:43], v[100:103]\n"
"v_mfma_f32_16x16x32_bf16 v[104:107], a[48:51], v[40:43], v[104:107]\nv_mfma_f32_16x16x32_bf16 v[108:111], a[52:55], v[40:43], v[108:111]\nds_read_b128 v[40:43], v32 offset:16384\ns_waitcnt lgkmcnt(3)\n"
"v_mfma_f32_16x16x32_bf16 v[112:115], v[176:179], v[44:47], v[112:115]\nv_mfma_f32_16x16x32_bf16 v[116:119], v[180:183], v[44:47], v[116:119]\nv_mfma_f32_16x16x32_bf16 v[120:123], a[48:51], v[44:47], v[120:123]\ns_add_u32 m0, s52, 8192\n"
"v_mfma_f32_16x16x32_bf16 v[124:127], a[52:55], v[44:47], v[124:127]\nds_read_b128 v[44:47], v33 offset:16384\ns_add_u32 s53, s49, 256\ns_waitcnt lgkmcnt(3)\n"
"v_mfma_f32_16x16x32_bf16 v[96:99], v[184:187], v[48:51], v[96:99]\nbuffer_load_dwordx4 v10, s[12:15], s53 offen lds\nv_mfma_f32_16x16x32_bf16 v[100:103], v[188:191], v[48:51], v[100:103]\nv_mfma_f32_16x16x32_bf16 v[104:107], a[56:59], v[48:51], v[104:107]\n"
"v_mfma_f32_16x16x32_bf16 v[108:111], a[60:63], v[48:51], v[108:111]\nds_read_b128 v[48:51], v34 offset:16384\ns_waitcnt lgkmcnt(3)\nv_mfma_f32_16x16x32_bf16 v[112:115], v[184:187], v[52:55], v[112:115]\n"
"v_mfma_f32_16x16x32_bf16 v[116:119], v[188:191], v[52:55], v[116:119]\nv_mfma_f32_16x16x32_bf16 v[120:123], a[56:59], v[52:55], v[120:123]\nv_mfma_f32_16x16x32_bf16 v[124:127], a[60:63], v[52:55], v[124:127]\nds_read_b128 v[52:55], v35 offset:16384\n"
"s_waitcnt lgkmcnt(3)\nv_mfma_f32_16x16x32_bf16 v[96:99], v[192:195], v[40:43], v[96:99]\nv_mfma_f32_16x16x32_bf16 v[100:103], v[196:199], v[40:43], v[100:103]\ns_add_u32 m0, s52, 12288\n"
"v_mfma_f32_16x16x32_bf16 v[104:107], a[64:67], v[40:43], v[104:107]\ns_add_u32 s53, s49, 384\nv_mfma_f32_16x16x32_bf16 v[108:111], a[68:71], v[40:43], v[108:111]\nds_read_b128 v[40:43], v32 offset:20480\n"
"buffer_load_dwordx4 v10, s[12:15], s53 offen lds\ns_waitcnt lgkmcnt(3)\nv_mfma_f32_16x16x32_bf16 v[112:115], v[192:195], v[44:47], v[112:115]\nv_mfma_f32_16x16x32_bf16 v[116:119], v[196:199], v[44:47], v[116:119]\n"
"v_mfma_f32_16x16x32_bf16 v[120:123], a[64:67], v[44:47], v[120:123]\nv_mfma_f32_16x16x32_bf16 v[124:127], a[68:71], v[44:47], v[124:127]\nds_read_b128 v[44:47], v33 offset:20480\ns_waitcnt lgkmcnt(3)\n"
"v_mfma_f32_16x16x32_bf16 v[96:99], v[200:203], v[48:51], v[96:99]\nv_mfma_f32_16x16x32_bf16 v[100:103], v[204:207], v[48:51], v[100:103]\nv_mfma_f32_16x16x32_bf16 v[104:107], a[72:75], v[48:51], v[104:107]\nv_mfma_f32_16x16x32_bf16 v[108:111], a[76:79], v[48:51], v[108:111]\n"
"ds_read_b128 v[48:51], v34 offset:20480\ns_waitcnt lgkmcnt(3)\nv_mfma_f32_16x16x32_bf16 v[112:115], v[200:203], v[52:55], v[112:115]\ns_add_u32 m0, s52, 16384\n"
"v_mfma_f32_16x16x32_bf16 v[116:119], v[204:207], v[52:55], v[116:119]\ns_add_u32 s53, s49, 512\nv_mfma_f32_16x16x32_bf16 v[120:123], a[72:75], v[52:55], v[120:123]\nbuffer_load_dwordx4 v10, s[12:15], s53 offen lds\n"
"v_mfma_f32_16x16x32_bf16 v[124:127], a[76:79], v[52:55], v[124:127]\nds_read_b128 v[52:55], v35 offset:20480\ns_waitcnt lgkmcnt(3)\nv_mfma_f32_16x16x32_bf16 v[96:99], v[208:211], v[40:43], v[96:99]\n"
"v_mfma_f32_16x16x32_bf16 v[100:103], v[212:215], v[40:43], v[100:103]\nv_mfma_f32_16x16x32_bf16 v[104:107], a[80:83], v[40:43], v[104:107]\nv_mfma_f32_16x16x32_bf16 v[108:111], a[84:87], v[40:43], v[108:111]\nds_read_b128 v[40:43], v32 offset:24576\n"
"s_waitcnt lgkmcnt(3)\nv_mfma_f32_16x16x32_bf16 v[112:115], v[208:211], v[44:47], v[112:115]\nv_mfma_f32_16x16x32_bf16 v[116:119], v[212:215], v[44:47], v[116:119]\nv_mfma_f32_16x16x32_bf16 v[120:123], a[80:83], v[44:47], v[120:123]\n"
"v_mfma_f32_16x16x32_bf16 v[124:127], a[84:87], v[44:47], v[124:127]\nds_read_b128 v[44:47], v33 offset:24576\ns_add_u32 m0, s52, 20480\ns_waitcnt lgkmcnt(3)\n"
"v_mfma_f32_16x16x32_bf16 v[96:99], v[216:219], v[48:51], v[96:99]\ns_add_u32 s54, s51, 32768\ns_cmp_eq_u32 s54, 163840\nv_mfma_f32_16x16x32_bf16 v[100:103], v[220:223], v[48:51], v[100:103]\n"
"v_mfma_f32_16x16x32_bf16 v[104:107], a[88:91], v[48:51], v[104:107]\nv_mfma_f32_16x16x32_bf16 v[108:111], a[92:95], v[48:51], v[108:111]\ns_cselect_b32 s54, 0, s54\ns_sub_u32 s54, s54, s51\n"
"ds_read_b128 v[48:51], v34 offset:24576\ns_add_u32 s53, s49, 640\ns_waitcnt lgkmcnt(3)\nv_mfma_f32_16x16x32_bf16 v[112:115], v[216:219], v[52:55], v[112:115]\n"
"buffer_load_dwordx4 v10, s[12:15], s53 offen lds\nv_mfma_f32_16x16x32_bf16 v[116:119], v[220:223], v[52:55], v[116:119]\nv_mfma_f32_16x16x32_bf16 v[120:123], a[88:91], v[52:55], v[120:123]\nv_mfma_f32_16x16x32_bf16 v[124:127], a[92:95], v[52:55], v[124:127]\n"
"ds_read_b128 v[52:55], v35 offset:24576\ns_waitcnt lgkmcnt(3)\nv_mfma_f32_16x16x32_bf16 v[96:99], v[224:227], v[40:43], v[96:99]\nv_add_u32 v32, s54, v32\n"
"v_mfma_f32_16x16x32_bf16 v[100:103], v[228:231], v[40:43], v[100:103]\nv_mfma_f32_16x16x32_bf16 v[104:107], a[96:99], v[40:43], v[104:107]\nv_mfma_f32_16x16x32_bf16 v[108:111], a[100:103], v[40:43], v[108:111]\nds_read_b128 v[40:43], v32 offset:0\n"
"s_waitcnt lgkmcnt(3)\nv_mfma_f32_16x16x32_bf16 v[112:115], v[224:227], v[44:47], v[112:115]\nv_add_u32 v33, s54, v33\nv_mfma_f32_16x16x32_bf16 v[116:119], v[228:231], v[44:47], v[116:119]\n"
"s_add_u32 m0, s52, 24576\nv_mfma_f32_16x16x32_bf16 v[120:123], a[96:99], v[44:47], v[120:123]\ns_add_u32 s53, s49, 768\nv_mfma_f32_16x16x32_bf16 v[124:127], a[100:103], v[44:47], v[124:127]\n"
"ds_read_b128 v[44:47], v33 offset:0\nbuffer_load_dwordx4 v10, s[12:15], s53 offen lds\ns_waitcnt lgkmcnt(3)\nv_mfma_f32_16x16x32_bf16 v[96:99], v[232:235], v[48:51], v[96:99]\n"
"v_add_u32 v34, s54, v34\nv_mfma_f32_16x16x32_bf16 v[100:103], v[236:239], v[48:51], v[100:103]\nv_mfma_f32_16x16x32_bf16 v[104:107], a[104:107], v[48:51], v[104:107]\nv_mfma_f32_16x16x32_bf16 v[108:111], a[108:111], v[48:51], v[108:111]\n"
"ds_read_b128 v[48:51], v34 offset:0\ns_waitcnt lgkmcnt(3)\nv_mfma_f32_16x16x32_bf16 v[112:115], v[232:235], v[52:55], v[112:115]\nv_add_u32 v35, s54, v35\n"
"v_mfma_f32_16x16x32_bf16 v[116:119], v[236:239], v[52:55], v[116:119]\nv_mfma_f32_16x16x32_bf16 v[120:123], a[104:107], v[52:55], v[120:123]\nv_mfma_f32_16x16x32_bf16 v[124:127], a[108:111], v[52:55], v[124:127]\nds_read_b128 v[52:55], v35 offset:0\n"
"s_mov_b32 s52, s47\ns_add_u32 s47, s51, s32\ns_add_u32 s51, s51, s54\ns_add_u32 s49, s49, 28672\n"
"s_cmp_lt_u32 s49, 1921024\ns_cbranch_scc1 fg0_loop\ns_nop 7\nv_cvt_pk_bf16_f32 v56, v96, v97\n"
"v_cvt_pk_bf16_f32 v57, v98, v99\nv_cvt_pk_bf16_f32 v58, v100, v101\nv_cvt_pk_bf16_f32 v59, v102, v103\ns_nop 0\n"
"s_nop 0\nbuffer_store_dwordx4 v[56:59], v15, s[20:23], s50 offen sc1\nv_cvt_pk_bf16_f32 v60, v112, v113\nv_cvt_pk_bf16_f32 v61, v114, v115\n"
"v_cvt_pk_bf16_f32 v62, v116, v117\nv_cvt_pk_bf16_f32 v63, v118, v119\ns_nop 0\ns_nop 0\n"
"buffer_store_dwordx4 v[60:63], v16, s[20:23], s50 offen sc1\nv_cvt_pk_bf16_f32 v56, v104, v105\nv_cvt_pk_bf16_f32 v57, v106, v107\nv_cvt_pk_bf16_f32 v58, v108, v109\n"
"v_cvt_pk_bf16_f32 v59, v110, v111\ns_nop 0\ns_nop 0\nbuffer_store_dwordx4 v[56:59], v17, s[20:23], s50 offen sc1\n"
"v_cvt_pk_bf16_f32 v60, v120, v121\nv_cvt_pk_bf16_f32 v61, v122, v123\nv_cvt_pk_bf16_f32 v62, v124, v125\nv_cvt_pk_bf16_f32 v63, v126, v127\n"
"s_nop 0\ns_nop 0\nbuffer_store_dwordx4 v[60:63], v18, s[20:23], s50 offen sc1\ns_add_u32 s48, s28, 64\n"
"v_mov_b32 v36, s48\ns_waitcnt vmcnt(0) lgkmcnt(0)\ns_barrier\nbuffer_store_dword v36, v19, s[24:27], s33 offen sc1\n"
"fg0_end:\n"
    :  : "{s[8:23]}"(s0), "{s[24:39]}"(s1), "{v[8:23]}"(v0) : "v24", "v25", "v26", "v27", "v28", "v29", "v30", "v31", "v32", "v33", "v34", "v35", "v36", "v37", "v38", "v39", "v40", "v41", "v42", "v43", "v44", "v45", "v46", "v47", "v48", "v49", "v50", "v51", "v52", "v53", "v54", "v55", "v56", "v57", "v58", "v59", "v60", "v61", "v62", "v63", "v64", "v65", "v66", "v67", "v68", "v69", "v70", "v71", "v72", "v73", "v74", "v75", "v76", "v77", "v78", "v79", "v80", "v81", "v82", "v83", "v84", "v85", "v86", "v87", "v88", "v89", "v90", "v91", "v92", "v93", "v94", "v95", "v96", "v97", "v98", "v99", "v100", "v101", "v102", "v103", "v104", "v105", "v106", "v107", "v108", "v109", "v110", "v111", "v112", "v113", "v114", "v115", "v116", "v117", "v118", "v119", "v120", "v121", "v122", "v123", "v124", "v125", "v126", "v127", "v128", "v129", "v130", "v131", "v132", "v133", "v134", "v135", "v136", "v137", "v138", "v139", "v140", "v141", "v142", "v143", "v144", "v145", "v146", "v147", "v148", "v149", "v150", "v151", "v152", "v153", "v154", "v155", "v156", "v157", "v158", "v159", "v160", "v161", "v162", "v163", "v164", "v165", "v166", "v167", "v168", "v169", "v170", "v171", "v172", "v173", "v174", "v175", "v176", "v177", "v178", "v179", "v180", "v181", "v182", "v183", "v184", "v185", "v186", "v187", "v188", "v189", "v190", "v191", "v192", "v193", "v194", "v195", "v196", "v197", "v198", "v199", "v200", "v201", "v202", "v203", "v204", "v205", "v206", "v207", "v208", "v209", "v210", "v211", "v212", "v213", "v214", "v215", "v216", "v217", "v218", "v219", "v220", "v221", "v222", "v223", "v224", "v225", "v226", "v227", "v228", "v229", "v230", "v231", "v232", "v233", "v234", "v235", "v236", "v237", "v238", "v239", "v240", "v241", "v242", "v243", "a0", "a1", "a2", "a3", "a4", "a5", "a6", "a7", "a8", "a9", "a10", "a11", "a12", "a13", "a14", "a15", "a16", "a17", "a18", "a19", "a20", "a21", "a22", "a23", "a24", "a25", "a26", "a27", "a28", "a29", "a30", "a31", "a32", "a33", "a34", "a35", "a36", "a37", "a38", "a39", "a40", "a41", "a42", "a43", "a44", "a45", "a46", "a47", "a48", "a49", "a50", "a51", "a52", "a53", "a54", "a55", "a56", "a57", "a58", "a59", "a60", "a61", "a62", "a63", "a64", "a65", "a66", "a67", "a68", "a69", "a70", "a71", "a72", "a73", "a74", "a75", "a76", "a77", "a78", "a79", "a80", "a81", "a82", "a83", "a84", "a85", "a86", "a87", "a88", "a89", "a90", "a91", "a92", "a93", "a94", "a95", "a96", "a97", "a98", "a99", "a100", "a101", "a102", "a103", "a104", "a105", "a106", "a107", "a108", "a109", "a110", "a111", "a112", "a113", "a114", "a115", "a116", "a117", "a118", "a119", "a120", "a121", "a122", "a123", "a124", "a125", "a126", "a127", "a128", "a129", "a130", "a131", "a132", "a133", "a134", "a135", "a136", "a137", "a138", "a139", "a140", "a141", "a142", "a143", "a144", "a145", "a146", "a147", "a148", "a149", "a150", "a151", "a152", "a153", "a154", "a155", "a156", "a157", "a158", "a159", "a160", "a161", "a162", "a163", "a164", "a165", "a166", "a167", "a168", "a169", "a170", "a171", "a172", "a173", "a174", "a175", "a176", "a177", "a178", "a179", "a180", "a181", "a182", "a183", "a184", "a185", "a186", "a187", "a188", "a189", "a190", "a191", "s40", "s41", "s42", "s43", "s44", "s45", "s46", "s47", "s48", "s49", "s50", "s51", "s52", "s53", "s54", "s55", "s56", "s57", "s58", "s59", "s60", "s61", "vcc", "scc", "memory");
    return o;
}
DI unsigned gemm_asm1(const GCtx& c) {
    const u16v s0 = {RFL(c.rw1[0]), RFL(c.rw1[1]), RFL(c.rw1[2]), RFL(c.rw1[3]), RFL(c.rx[0]), RFL(c.rx[1]), RFL(c.rx[2]), RFL(c.rx[3]), RFL(c.rexp[0]), RFL(c.rexp[1]), RFL(c.rexp[2]), RFL(c.rexp[3]), RFL(c.rout[0]), RFL(c.rout[1]), RFL(c.rout[2]), RFL(c.rout[3])}, s1 = {RFL(c.rhb[0]), RFL(c.rhb[1]), RFL(c.rhb[2]), RFL(c.rhb[3]), RFL(c.tag), RFL(c.rot), RFL(c.sw1), RFL(c.mw), RFL(c.mx), RFL(c.sfg), RFL(c.stay), RFL(c.csc), RFL(c.ostr), RFL(c.kb), RFL(c.p1), RFL(c.p2)}, v0 = {c.vr0, c.vr1, c.vx, c.wa, c.xa, c.vexp, c.vimp, c.vo0, c.vo1, c.vo2, c.vo3, c.vfl, c.vpf, c.vpo, c.vk, c.p5};
    unsigned o = 0;
    asm volatile(
"s_nop 4\ns_add_u32 s40, s29, 0\ns_add_u32 s41, s29, 1\ns_add_u32 s42, s29, 2\n"
"s_add_u32 s43, s29, 3\nv_xor_b32 v24, 0, v11\nv_xor_b32 v25, 64, v11\nv_xor_b32 v26, 128, v11\n"
"v_xor_b32 v27, 192, v11\nv_xor_b32 v28, 2064, v11\nv_xor_b32 v29, 2128, v11\nv_xor_b32 v30, 2192, v11\n"
"v_xor_b32 v31, 2256, v11\nv_xor_b32 v40, 32, v22\nds_read_b64_tr_b16 a[128:129], v22 offset:0\nds_read_b64_tr_b16 a[130:131], v22 offset:512\n"
"ds_read_b64_tr_b16 a[132:133], v22 offset:4096\nds_read_b64_tr_b16 a[134:135], v22 offset:4608\nds_read_b64_tr_b16 a[136:137], v22 offset:8192\nds_read_b64_tr_b16 a[138:139], v22 offset:8704\n"
"ds_read_b64_tr_b16 a[140:141], v22 offset:12288\nds_read_b64_tr_b16 a[142:143], v22 offset:12800\ns_lshl_b32 s53, s41, 19\ns_add_u32 s53, s53, s30\n"
"s_add_u32 m0, s31, 65536\ns_add_u32 s53, s53, 0\nbuffer_load_dwordx4 v8, s[8:11], s53 offen lds\nbuffer_load_dwordx4 v9, s[8:11], s53 offen offset:1024 lds\n"
"s_waitcnt lgkmcnt(0)\nds_read_b64_tr_b16 a[144:145], v22 offset:16384\nds_read_b64_tr_b16 a[146:147], v22 offset:16896\nds_read_b64_tr_b16 a[148:149], v22 offset:20480\n"
"ds_read_b64_tr_b16 a[150:151], v22 offset:20992\nds_read_b64_tr_b16 a[152:153], v22 offset:24576\nds_read_b64_tr_b16 a[154:155], v22 offset:25088\nds_read_b64_tr_b16 a[156:157], v22 offset:28672\n"
"ds_read_b64_tr_b16 a[158:159], v22 offset:29184\ns_lshl_b32 s53, s41, 19\ns_add_u32 s53, s53, s30\ns_add_u32 m0, s31, 73728\n"
"s_add_u32 s53, s53, 131072\nbuffer_load_dwordx4 v8, s[8:11], s53 offen lds\nbuffer_load_dwordx4 v9, s[8:11], s53 offen offset:1024 lds\ns_waitcnt lgkmcnt(0)\n"
"ds_read_b64_tr_b16 a[160:161], v40 offset:0\nds_read_b64_tr_b16 a[162:163], v40 offset:512\nds_read_b64_tr_b16 a[164:165], v40 offset:4096\nds_read_b64_tr_b16 a[166:167], v40 offset:4608\n"
"ds_read_b64_tr_b16 a[168:169], v40 offset:8192\nds_read_b64_tr_b16 a[170:171], v40 offset:8704\nds_read_b64_tr_b16 a[172:173], v40 offset:12288\nds_read_b64_tr_b16 a[174:175], v40 offset:12800\n"
"s_lshl_b32 s53, s41, 19\ns_add_u32 s53, s53, s30\ns_add_u32 m0, s31, 81920\ns_add_u32 s53, s53, 262144\n"
"buffer_load_dwordx4 v8, s[8:11], s53 offen lds\nbuffer_load_dwordx4 v9, s[8:11], s53 offen offset:1024 lds\ns_waitcnt lgkmcnt(0)\nds_read_b64_tr_b16 a[176:177], v40 offset:16384\n"
"ds_read_b64_tr_b16 a[178:179], v40 offset:16896\nds_read_b64_tr_b16 a[180:181], v40 offset:20480\nds_read_b64_tr_b16 a[182:183], v40 offset:20992\nds_read_b64_tr_b16 a[184:185], v40 offset:24576\n"
"ds_read_b64_tr_b16 a[186:187], v40 offset:25088\nds_read_b64_tr_b16 a[188:189], v40 offset:28672\nds_read_b64_tr_b16 a[190:191], v40 offset:29184\ns_lshl_b32 s53, s41, 19\n"
"s_add_u32 s53, s53, s30\ns_add_u32 m0, s31, 90112\ns_add_u32 s53, s53, 393216\nbuffer_load_dwordx4 v8, s[8:11], s53 offen lds\n"
"buffer_load_dwordx4 v9, s[8:11], s53 offen offset:1024 lds\ns_waitcnt lgkmcnt(0)\ns_waitcnt lgkmcnt(0)\nv_add_u32 v40, 65536, v24\n"
"v_add_u32 v41, 65536, v25\nv_add_u32 v42, 65536, v26\nv_add_u32 v43, 65536, v27\nv_add_u32 v44, 65536, v28\n"
"v_add_u32 v45, 65536, v29\nv_add_u32 v46, 65536, v30\nv_add_u32 v47, 65536, v31\nv_xor_b32 v32, 0, v12\n"
"v_xor_b32 v33, 512, v12\nv_xor_b32 v34, 64, v12\nv_xor_b32 v35, 576, v12\ns_mov_b32 s55, 0\n"
"s_waitcnt vmcnt(8)\ns_barrier\nds_read_b128 v[96:99], v24 offset:0\nds_read_b128 v[100:103], v28 offset:0\n"
"ds_read_b128 v[104:107], v25 offset:0\nds_read_b128 v[108:111], v29 offset:0\nds_read_b128 v[112:115], v26 offset:0\nds_read_b128 v[116:119], v30 offset:0\n"
"ds_read_b128 v[120:123], v27 offset:0\nds_read_b128 v[124:127], v31 offset:0\ns_waitcnt lgkmcnt(6)\nv_mfma_f32_16x16x32_bf16 v[64:67], v[96:99], a[128:131], 0\n"
"s_lshl_b32 s53, s42, 19\ns_add_u32 s53, s53, s30\nv_mfma_f32_16x16x32_bf16 v[68:71], v[96:99], a[160:163], 0\nds_read_b128 v[96:99], v24 offset:256\n"
"s_add_u32 m0, s31, 98304\ns_add_u32 s53, s53, 0\nv_mfma_f32_16x16x32_bf16 v[72:75], v[100:103], a[128:131], 0\nbuffer_load_dwordx4 v8, s[8:11], s53 offen lds\n"
"v_mfma_f32_16x16x32_bf16 v[76:79], v[100:103], a[160:163], 0\nds_read_b128 v[100:103], v28 offset:256\nbuffer_load_dwordx4 v9, s[8:11], s53 offen offset:1024 lds\ns_waitcnt lgkmcnt(6)\n"
"v_mfma_f32_16x16x32_bf16 v[64:67], v[104:107], a[132:135], v[64:67]\ns_lshl_b32 s53, s42, 19\ns_add_u32 s53, s53, s30\nv_mfma_f32_16x16x32_bf16 v[68:71], v[104:107], a[164:167], v[68:71]\n"
"ds_read_b128 v[104:107], v25 offset:256\ns_add_u32 m0, s31, 106496\ns_add_u32 s53, s53, 131072\nv_mfma_f32_16x16x32_bf16 v[72:75], v[108:111], a[132:135], v[72:75]\n"
"buffer_load_dwordx4 v8, s[8:11], s53 offen lds\nv_mfma_f32_16x16x32_bf16 v[76:79], v[108:111], a[164:167], v[76:79]\nds_read_b128 v[108:111], v29 offset:256\nbuffer_load_dwordx4 v9, s[8:11], s53 offen offset:1024 lds\n"
"s_waitcnt lgkmcnt(6)\nv_mfma_f32_16x16x32_bf16 v[64:67], v[112:115], a[136:139], v[64:67]\nv_mfma_f32_16x16x32_bf16 v[68:71], v[112:115], a[168:171], v[68:71]\nds_read_b128 v[112:115], v26 offset:256\n"
"v_mfma_f32_16x16x32_bf16 v[72:75], v[116:119], a[136:139], v[72:75]\nv_mfma_f32_16x16x32_bf16 v[76:79], v[116:119], a[168:171], v[76:79]\nds_read_b128 v[116:119], v30 offset:256\ns_waitcnt lgkmcnt(6)\n"
"v_mfma_f32_16x16x32_bf16 v[64:67], v[120:123], a[140:143], v[64:67]\nv_mfma_f32_16x16x32_bf16 v[68:71], v[120:123], a[172:175], v[68:71]\nds_read_b128 v[120:123], v27 offset:256\nv_mfma_f32_16x16x32_bf16 v[72:75], v[124:127], a[140:143], v[72:75]\n"
"v_mfma_f32_16x16x32_bf16 v[76:79], v[124:127], a[172:175], v[76:79]\nds_read_b128 v[124:127], v31 offset:256\ns_waitcnt lgkmcnt(6)\nv_mfma_f32_16x16x32_bf16 v[64:67], v[96:99], a[144:147], v[64:67]\n"
"v_mfma_f32_16x16x32_bf16 v[68:71], v[96:99], a[176:179], v[68:71]\nds_read_b128 v[96:99], v24 offset:16384\nv_mfma_f32_16x16x32_bf16 v[72:75], v[100:103], a[144:147], v[72:75]\nv_mfma_f32_16x16x32_bf16 v[76:79], v[100:103], a[176:179], v[76:79]\n"
"ds_read_b128 v[100:103], v28 offset:16384\ns_waitcnt lgkmcnt(6)\nv_mfma_f32_16x16x32_bf16 v[64:67], v[104:107], a[148:151], v[64:67]\nv_mfma_f32_16x16x32_bf16 v[68:71], v[104:107], a[180:183], v[68:71]\n"
"ds_read_b128 v[104:107], v25 offset:16384\nv_mfma_f32_16x16x32_bf16 v[72:75], v[108:111], a[148:151], v[72:75]\nv_mfma_f32_16x16x32_bf16 v[76:79], v[108:111], a[180:183], v[76:79]\nds_read_b128 v[108:111], v29 offset:16384\n"
"s_waitcnt lgkmcnt(6)\nv_mfma_f32_16x16x32_bf16 v[64:67], v[112:115], a[152:155], v[64:67]\nv_mfma_f32_16x16x32_bf16 v[68:71], v[112:115], a[184:187], v[68:71]\nds_read_b128 v[112:115], v26 offset:16384\n"
"v_mfma_f32_16x16x32_bf16 v[72:75], v[116:119], a[152:155], v[72:75]\nv_mfma_f32_16x16x32_bf16 v[76:79], v[116:119], a[184:187], v[76:79]\nds_read_b128 v[116:119], v30 offset:16384\ns_waitcnt lgkmcnt(6)\n"
"v_mfma_f32_16x16x32_bf16 v[64:67], v[120:123], a[156:159], v[64:67]\nv_mfma_f32_16x16x32_bf16 v[68:71], v[120:123], a[188:191], v[68:71]\nds_read_b128 v[120:123], v27 offset:16384\nv_mfma_f32_16x16x32_bf16 v[72:75], v[124:127], a[156:159], v[72:75]\n"
"v_mfma_f32_16x16x32_bf16 v[76:79], v[124:127], a[188:191], v[76:79]\nds_read_b128 v[124:127], v31 offset:16384\ns_waitcnt lgkmcnt(6)\nv_mfma_f32_16x16x32_bf16 v[80:83], v[96:99], a[128:131], 0\n"
"s_lshl_b32 s53, s42, 19\ns_add_u32 s53, s53, s30\nv_mfma_f32_16x16x32_bf16 v[84:87], v[96:99], a[160:163], 0\nds_read_b128 v[96:99], v24 offset:16640\n"
"s_add_u32 m0, s31, 114688\ns_add_u32 s53, s53, 262144\nv_mfma_f32_16x16x32_bf16 v[88:91], v[100:103], a[128:131], 0\nbuffer_load_dwordx4 v8, s[8:11], s53 offen lds\n"
"v_mfma_f32_16x16x32_bf16 v[92:95], v[100:103], a[160:163], 0\nds_read_b128 v[100:103], v28 offset:16640\nbuffer_load_dwordx4 v9, s[8:11], s53 offen offset:1024 lds\ns_waitcnt lgkmcnt(6)\n"
"v_mfma_f32_16x16x32_bf16 v[80:83], v[104:107], a[132:135], v[80:83]\nv_cvt_pk_bf16_f32 v128, v64, v65\nv_cvt_pk_bf16_f32 v129, v66, v67\nv_mfma_f32_16x16x32_bf16 v[84:87], v[104:107], a[164:167], v[84:87]\n"
"ds_read_b128 v[104:107], v25 offset:16640\nv_cvt_pk_bf16_f32 v130, v72, v73\nv_cvt_pk_bf16_f32 v131, v74, v75\nv_mfma_f32_16x16x32_bf16 v[88:91], v[108:111], a[132:135], v[88:91]\n"
"v_cvt_pk_bf16_f32 v132, v68, v69\nv_cvt_pk_bf16_f32 v133, v70, v71\nv_mfma_f32_16x16x32_bf16 v[92:95], v[108:111], a[164:167], v[92:95]\nds_read_b128 v[108:111], v29 offset:16640\n"
"v_cvt_pk_bf16_f32 v134, v76, v77\nv_cvt_pk_bf16_f32 v135, v78, v79\ns_waitcnt lgkmcnt(6)\nv_mfma_f32_16x16x32_bf16 v[80:83], v[112:115], a[136:139], v[80:83]\n"
"s_lshl_b32 s54, s40, 11\ns_add_u32 s54, s54, 0\nv_mfma_f32_16x16x32_bf16 v[84:87], v[112:115], a[168:171], v[84:87]\nds_read_b128 v[112:115], v26 offset:16640\n"
"buffer_store_dwordx4 v[128:131], v13, s[16:19], s54 offen sc1\nv_mfma_f32_16x16x32_bf16 v[88:91], v[116:119], a[136:139], v[88:91]\ns_add_u32 s54, s54, 229376\nv_mfma_f32_16x16x32_bf16 v[92:95], v[116:119], a[168:171], v[92:95]\n"
"ds_read_b128 v[116:119], v30 offset:16640\nbuffer_store_dwordx4 v[132:135], v13, s[16:19], s54 offen sc1\ns_waitcnt lgkmcnt(6)\nv_mfma_f32_16x16x32_bf16 v[80:83], v[120:123], a[140:143], v[80:83]\n"
"s_lshl_b32 s53, s42, 19\ns_add_u32 s53, s53, s30\nv_mfma_f32_16x16x32_bf16 v[84:87], v[120:123], a[172:175], v[84:87]\nds_read_b128 v[120:123], v27 offset:16640\n"
"s_add_u32 m0, s31, 122880\ns_add_u32 s53, s53, 393216\nv_mfma_f32_16x16x32_bf16 v[88:91], v[124:127], a[140:143], v[88:91]\nbuffer_load_dwordx4 v8, s[8:11], s53 offen lds\n"
"v_mfma_f32_16x16x32_bf16 v[92:95], v[124:127], a[172:175], v[92:95]\nds_read_b128 v[124:127], v31 offset:16640\nbuffer_load_dwordx4 v9, s[8:11], s53 offen offset:1024 lds\ns_waitcnt vmcnt(10)\n"
"s_barrier\ns_waitcnt lgkmcnt(6)\nv_mfma_f32_16x16x32_bf16 v[80:83], v[96:99], a[144:147], v[80:83]\nv_mfma_f32_16x16x32_bf16 v[84:87], v[96:99], a[176:179], v[84:87]\n"
"ds_read_b128 v[96:99], v24 offset:32768\nv_mfma_f32_16x16x32_bf16 v[88:91], v[100:103], a[144:147], v[88:91]\nv_mfma_f32_16x16x32_bf16 v[92:95], v[100:103], a[176:179], v[92:95]\nds_read_b128 v[100:103], v28 offset:32768\n"
"s_waitcnt lgkmcnt(6)\nv_mfma_f32_16x16x32_bf16 v[80:83], v[104:107], a[148:151], v[80:83]\nv_mfma_f32_16x16x32_bf16 v[84:87], v[104:107], a[180:183], v[84:87]\nds_read_b128 v[104:107], v25 offset:32768\n"
"v_mfma_f32_16x16x32_bf16 v[88:91], v[108:111], a[148:151], v[88:91]\nv_mfma_f32_16x16x32_bf16 v[92:95], v[108:111], a[180:183], v[92:95]\nds_read_b128 v[108:111], v29 offset:32768\ns_waitcnt lgkmcnt(6)\n"
"v_mfma_f32_16x16x32_bf16 v[80:83], v[112:115], a[152:155], v[80:83]\nv_mfma_f32_16x16x32_bf16 v[84:87], v[112:115], a[184:187], v[84:87]\nds_read_b128 v[112:115], v26 offset:32768\nv_mfma_f32_16x16x32_bf16 v[88:91], v[116:119], a[152:155], v[88:91]\n"
"v_mfma_f32_16x16x32_bf16 v[92:95], v[116:119], a[184:187], v[92:95]\nds_read_b128 v[116:119], v30 offset:32768\ns_waitcnt lgkmcnt(6)\nv_mfma_f32_16x16x32_bf16 v[80:83], v[120:123], a[156:159], v[80:83]\n"
"v_mfma_f32_16x16x32_bf16 v[84:87], v[120:123], a[188:191], v[84:87]\nds_read_b128 v[120:123], v27 offset:32768\nv_mfma_f32_16x16x32_bf16 v[88:91], v[124:127], a[156:159], v[88:91]\nv_mfma_f32_16x16x32_bf16 v[92:95], v[124:127], a[188:191], v[92:95]\n"
"ds_read_b128 v[124:127], v31 offset:32768\ns_waitcnt lgkmcnt(6)\nv_mfma_f32_16x16x32_bf16 v[64:67], v[96:99], a[128:131], 0\ns_lshl_b32 s53, s43, 19\n"
"s_add_u32 s53, s53, s30\nv_mfma_f32_16x16x32_bf16 v[68:71], v[96:99], a[160:163], 0\nds_read_b128 v[96:99], v24 offset:33024\ns_add_u32 m0, s31, 131072\n"
"s_add_u32 s53, s53, 0\nv_mfma_f32_16x16x32_bf16 v[72:75], v[100:103], a[128:131], 0\nbuffer_load_dwordx4 v8, s[8:11], s53 offen lds\nv_mfma_f32_16x16x32_bf16 v[76:79], v[100:103], a[160:163], 0\n"
"ds_read_b128 v[100:103], v28 offset:33024\nbuffer_load_dwordx4 v9, s[8:11], s53 offen offset:1024 lds\ns_waitcnt lgkmcnt(6)\nv_mfma_f32_16x16x32_bf16 v[64:67], v[104:107], a[132:135], v[64:67]\n"
"v_cvt_pk_bf16_f32 v136, v80, v81\nv_cvt_pk_bf16_f32 v137, v82, v83\nv_mfma_f32_16x16x32_bf16 v[68:71], v[104:107], a[164:167], v[68:71]\nds_read_b128 v[104:107], v25 offset:33024\n"
"v_cvt_pk_bf16_f32 v138, v88, v89\nv_cvt_pk_bf16_f32 v139, v90, v91\nv_mfma_f32_16x16x32_bf16 v[72:75], v[108:111], a[132:135], v[72:75]\nv_cvt_pk_bf16_f32 v140, v84, v85\n"
"v_cvt_pk_bf16_f32 v141, v86, v87\nv_mfma_f32_16x16x32_bf16 v[76:79], v[108:111], a[164:167], v[76:79]\nds_read_b128 v[108:111], v29 offset:33024\nv_cvt_pk_bf16_f32 v142, v92, v93\n"
"v_cvt_pk_bf16_f32 v143, v94, v95\ns_waitcnt lgkmcnt(6)\nv_mfma_f32_16x16x32_bf16 v[64:67], v[112:115], a[136:139], v[64:67]\ns_lshl_b32 s54, s40, 11\n"
"s_add_u32 s54, s54, 1024\nv_mfma_f32_16x16x32_bf16 v[68:71], v[112:115], a[168:171], v[68:71]\nds_read_b128 v[112:115], v26 offset:33024\nbuffer_store_dwordx4 v[136:139], v13, s[16:19], s54 offen sc1\n"
"v_mfma_f32_16x16x32_bf16 v[72:75], v[116:119], a[136:139], v[72:75]\ns_add_u32 s54, s54, 229376\nv_mfma_f32_16x16x32_bf16 v[76:79], v[116:119], a[168:171], v[76:79]\nds_read_b128 v[116:119], v30 offset:33024\n"
"buffer_store_dwordx4 v[140:143], v13, s[16:19], s54 offen sc1\ns_waitcnt lgkmcnt(6)\nv_mfma_f32_16x16x32_bf16 v[64:67], v[120:123], a[140:143], v[64:67]\ns_lshl_b32 s53, s43, 19\n"
"s_add_u32 s53, s53, s30\nv_mfma_f32_16x16x32_bf16 v[68:71], v[120:123], a[172:175], v[68:71]\nds_read_b128 v[120:123], v27 offset:33024\ns_add_u32 m0, s31, 139264\n"
"s_add_u32 s53, s53, 131072\nv_mfma_f32_16x16x32_bf16 v[72:75], v[124:127], a[140:143], v[72:75]\nbuffer_load_dwordx4 v8, s[8:11], s53 offen lds\nv_mfma_f32_16x16x32_bf16 v[76:79], v[124:127], a[172:175], v[76:79]\n"
"ds_read_b128 v[124:127], v31 offset:33024\nbuffer_load_dwordx4 v9, s[8:11], s53 offen offset:1024 lds\ns_waitcnt lgkmcnt(6)\nv_mfma_f32_16x16x32_bf16 v[64:67], v[96:99], a[144:147], v[64:67]\n"
"v_mfma_f32_16x16x32_bf16 v[68:71], v[96:99], a[176:179], v[68:71]\nds_read_b128 v[96:99], v24 offset:49152\nv_mfma_f32_16x16x32_bf16 v[72:75], v[100:103], a[144:147], v[72:75]\nv_mfma_f32_16x16x32_bf16 v[76:79], v[100:103], a[176:179], v[76:79]\n"
"ds_read_b128 v[100:103], v28 offset:49152\ns_waitcnt lgkmcnt(6)\nv_mfma_f32_16x16x32_bf16 v[64:67], v[104:107], a[148:151], v[64:67]\nv_mfma_f32_16x16x32_bf16 v[68:71], v[104:107], a[180:183], v[68:71]\n"
"ds_read_b128 v[104:107], v25 offset:49152\nv_mfma_f32_16x16x32_bf16 v[72:75], v[108:111], a[148:151], v[72:75]\nv_mfma_f32_16x16x32_bf16 v[76:79], v[108:111], a[180:183], v[76:79]\nds_read_b128 v[108:111], v29 offset:49152\n"
"s_waitcnt lgkmcnt(6)\nv_mfma_f32_16x16x32_bf16 v[64:67], v[112:115], a[152:155], v[64:67]\nv_mfma_f32_16x16x32_bf16 v[68:71], v[112:115], a[184:187], v[68:71]\nds_read_b128 v[112:115], v26 offset:49152\n"
"v_mfma_f32_16x16x32_bf16 v[72:75], v[116:119], a[152:155], v[72:75]\nv_mfma_f32_16x16x32_bf16 v[76:79], v[116:119], a[184:187], v[76:79]\nds_read_b128 v[116:119], v30 offset:49152\ns_waitcnt lgkmcnt(6)\n"
"v_mfma_f32_16x16x32_bf16 v[64:67], v[120:123], a[156:159], v[64:67]\nv_mfma_f32_16x16x32_bf16 v[68:71], v[120:123], a[188:191], v[68:71]\nds_read_b128 v[120:123], v27 offset:49152\nv_mfma_f32_16x16x32_bf16 v[72:75], v[124:127], a[156:159], v[72:75]\n"
"v_mfma_f32_16x16x32_bf16 v[76:79], v[124:127], a[188:191], v[76:79]\nds_read_b128 v[124:127], v31 offset:49152\ns_waitcnt lgkmcnt(6)\nv_mfma_f32_16x16x32_bf16 v[80:83], v[96:99], a[128:131], 0\n"
"s_lshl_b32 s53, s43, 19\ns_add_u32 s53, s53, s30\nv_mfma_f32_16x16x32_bf16 v[84:87], v[96:99], a[160:163], 0\nds_read_b128 v[96:99], v24 offset:49408\n"
"s_add_u32 m0, s31, 147456\ns_add_u32 s53, s53, 262144\nv_mfma_f32_16x16x32_bf16 v[88:91], v[100:103], a[128:131], 0\nbuffer_load_dwordx4 v8, s[8:11], s53 offen lds\n"
"v_mfma_f32_16x16x32_bf16 v[92:95], v[100:103], a[160:163], 0\nds_read_b128 v[100:103], v28 offset:49408\nbuffer_load_dwordx4 v9, s[8:11], s53 offen offset:1024 lds\ns_waitcnt lgkmcnt(6)\n"
"v_mfma_f32_16x16x32_bf16 v[80:83], v[104:107], a[132:135], v[80:83]\nv_cvt_pk_bf16_f32 v144, v64, v65\nv_cvt_pk_bf16_f32 v145, v66, v67\nv_mfma_f32_16x16x32_bf16 v[84:87], v[104:107], a[164:167], v[84:87]\n"
"ds_read_b128 v[104:107], v25 offset:49408\nv_cvt_pk_bf16_f32 v146, v72, v73\nv_cvt_pk_bf16_f32 v147, v74, v75\nv_mfma_f32_16x16x32_bf16 v[88:91], v[108:111], a[132:135], v[88:91]\n"
"v_cvt_pk_bf16_f32 v148, v68, v69\nv_cvt_pk_bf16_f32 v149, v70, v71\nv_mfma_f32_16x16x32_bf16 v[92:95], v[108:111], a[164:167], v[92:95]\nds_read_b128 v[108:111], v29 offset:49408\n"
"v_cvt_pk_bf16_f32 v150, v76, v77\nv_cvt_pk_bf16_f32 v151, v78, v79\ns_waitcnt lgkmcnt(6)\nv_mfma_f32_16x16x32_bf16 v[80:83], v[112:115], a[136:139], v[80:83]\n"
"s_lshl_b32 s54, s41, 11\ns_add_u32 s54, s54, 0\nv_mfma_f32_16x16x32_bf16 v[84:87], v[112:115], a[168:171], v[84:87]\nds_read_b128 v[112:115], v26 offset:49408\n"
"buffer_store_dwordx4 v[144:147], v13, s[16:19], s54 offen sc1\nv_mfma_f32_16x16x32_bf16 v[88:91], v[116:119], a[136:139], v[88:91]\ns_add_u32 s54, s54, 229376\nv_mfma_f32_16x16x32_bf16 v[92:95], v[116:119], a[168:171], v[92:95]\n"
"ds_read_b128 v[116:119], v30 offset:49408\nbuffer_store_dwordx4 v[148:151], v13, s[16:19], s54 offen sc1\ns_waitcnt lgkmcnt(6)\nv_mfma_f32_16x16x32_bf16 v[80:83], v[120:123], a[140:143], v[80:83]\n"
"s_lshl_b32 s53, s43, 19\ns_add_u32 s53, s53, s30\nv_mfma_f32_16x16x32_bf16 v[84:87], v[120:123], a[172:175], v[84:87]\nds_read_b128 v[120:123], v27 offset:49408\n"
"s_add_u32 m0, s31, 155648\ns_add_u32 s53, s53, 393216\nv_mfma_f32_16x16x32_bf16 v[88:91], v[124:127], a[140:143], v[88:91]\nbuffer_load_dwordx4 v8, s[8:11], s53 offen lds\n"
"v_mfma_f32_16x16x32_bf16 v[92:95], v[124:127], a[172:175], v[92:95]\nds_read_b128 v[124:127], v31 offset:49408\nbuffer_load_dwordx4 v9, s[8:11], s53 offen offset:1024 lds\ns_waitcnt vmcnt(12)\n"
"s_barrier\ns_waitcnt lgkmcnt(6)\nv_mfma_f32_16x16x32_bf16 v[80:83], v[96:99], a[144:147], v[80:83]\nv_mfma_f32_16x16x32_bf16 v[84:87], v[96:99], a[176:179], v[84:87]\n"
"ds_read_b128 v[96:99], v40 offset:0\nv_mfma_f32_16x16x32_bf16 v[88:91], v[100:103], a[144:147], v[88:91]\nv_mfma_f32_16x16x32_bf16 v[92:95], v[100:103], a[176:179], v[92:95]\nds_read_b128 v[100:103], v44 offset:0\n"
"s_waitcnt lgkmcnt(6)\nv_mfma_f32_16x16x32_bf16 v[80:83], v[104:107], a[148:151], v[80:83]\nv_mfma_f32_16x16x32_bf16 v[84:87], v[104:107], a[180:183], v[84:87]\nds_read_b128 v[104:107], v41 offset:0\n"
"v_mfma_f32_16x16x32_bf16 v[88:91], v[108:111], a[148:151], v[88:91]\nv_mfma_f32_16x16x32_bf16 v[92:95], v[108:111], a[180:183], v[92:95]\nds_read_b128 v[108:111], v45 offset:0\ns_waitcnt lgkmcnt(6)\n"
"v_mfma_f32_16x16x32_bf16 v[80:83], v[112:115], a[152:155], v[80:83]\nv_mfma_f32_16x16x32_bf16 v[84:87], v[112:115], a[184:187], v[84:87]\nds_read_b128 v[112:115], v42 offset:0\nv_mfma_f32_16x16x32_bf16 v[88:91], v[116:119], a[152:155], v[88:91]\n"
"v_mfma_f32_16x16x32_bf16 v[92:95], v[116:119], a[184:187], v[92:95]\nds_read_b128 v[116:119], v46 offset:0\ns_waitcnt lgkmcnt(6)\nv_mfma_f32_16x16x32_bf16 v[80:83], v[120:123], a[156:159], v[80:83]\n"
"v_mfma_f32_16x16x32_bf16 v[84:87], v[120:123], a[188:191], v[84:87]\nds_read_b128 v[120:123], v43 offset:0\nv_mfma_f32_16x16x32_bf16 v[88:91], v[124:127], a[156:159], v[88:91]\nv_mfma_f32_16x16x32_bf16 v[92:95], v[124:127], a[188:191], v[92:95]\n"
"ds_read_b128 v[124:127], v47 offset:0\ns_waitcnt lgkmcnt(6)\nv_mfma_f32_16x16x32_bf16 v[64:67], v[96:99], a[128:131], 0\ns_add_u32 m0, s32, 0\n"
"v_mfma_f32_16x16x32_bf16 v[68:71], v[96:99], a[160:163], 0\nds_read_b128 v[96:99], v40 offset:256\ns_mov_b32 s53, 0\nv_mfma_f32_16x16x32_bf16 v[72:75], v[100:103], a[128:131], 0\n"
"buffer_load_dwordx4 v10, s[12:15], s53 offen lds\nv_mfma_f32_16x16x32_bf16 v[76:79], v[100:103], a[160:163], 0\nds_read_b128 v[100:103], v44 offset:256\nv_cvt_pk_bf16_f32 v152, v80, v81\n"
"v_cvt_pk_bf16_f32 v153, v82, v83\ns_waitcnt lgkmcnt(6)\nv_mfma_f32_16x16x32_bf16 v[64:67], v[104:107], a[132:135], v[64:67]\nv_cvt_pk_bf16_f32 v154, v88, v89\n"
"v_cvt_pk_bf16_f32 v155, v90, v91\nv_mfma_f32_16x16x32_bf16 v[68:71], v[104:107], a[164:167], v[68:71]\nds_read_b128 v[104:107], v41 offset:256\nv_cvt_pk_bf16_f32 v156, v84, v85\n"
"v_cvt_pk_bf16_f32 v157, v86, v87\nv_mfma_f32_16x16x32_bf16 v[72:75], v[108:111], a[132:135], v[72:75]\nv_cvt_pk_bf16_f32 v158, v92, v93\nv_cvt_pk_bf16_f32 v159, v94, v95\n"
"v_mfma_f32_16x16x32_bf16 v[76:79], v[108:111], a[164:167], v[76:79]\nds_read_b128 v[108:111], v45 offset:256\ns_lshl_b32 s54, s41, 11\ns_add_u32 s54, s54, 1024\n"
"s_waitcnt lgkmcnt(6)\nv_mfma_f32_16x16x32_bf16 v[64:67], v[112:115], a[136:139], v[64:67]\nbuffer_store_dwordx4 v[152:155], v13, s[16:19], s54 offen sc1\nv_mfma_f32_16x16x32_bf16 v[68:71], v[112:115], a[168:171], v[68:71]\n"
"ds_read_b128 v[112:115], v42 offset:256\ns_add_u32 s54, s54, 229376\nv_mfma_f32_16x16x32_bf16 v[72:75], v[116:119], a[136:139], v[72:75]\nbuffer_store_dwordx4 v[156:159], v13, s[16:19], s54 offen sc1\n"
"v_mfma_f32_16x16x32_bf16 v[76:79], v[116:119], a[168:171], v[76:79]\nds_read_b128 v[116:119], v46 offset:256\ns_add_u32 m0, s32, 4096\ns_waitcnt lgkmcnt(6)\n"
"v_mfma_f32_16x16x32_bf16 v[64:67], v[120:123], a[140:143], v[64:67]\ns_mov_b32 s53, 128\nv_mfma_f32_16x16x32_bf16 v[68:71], v[120:123], a[172:175], v[68:71]\nds_read_b128 v[120:123], v43 offset:256\n"
"buffer_load_dwordx4 v10, s[12:15], s53 offen lds\nv_mfma_f32_16x16x32_bf16 v[72:75], v[124:127], a[140:143], v[72:75]\ns_add_u32 m0, s32, 8192\nv_mfma_f32_16x16x32_bf16 v[76:79], v[124:127], a[172:175], v[76:79]\n"
"ds_read_b128 v[124:127], v47 offset:256\ns_mov_b32 s53, 256\ns_waitcnt lgkmcnt(6)\nv_mfma_f32_16x16x32_bf16 v[64:67], v[96:99], a[144:147], v[64:67]\n"
"buffer_load_dwordx4 v10, s[12:15], s53 offen lds\nv_mfma_f32_16x16x32_bf16 v[68:71], v[96:99], a[176:179], v[68:71]\nds_read_b128 v[96:99], v40 offset:16384\ns_add_u32 m0, s32, 12288\n"
"v_mfma_f32_16x16x32_bf16 v[72:75], v[100:103], a[144:147], v[72:75]\ns_mov_b32 s53, 384\nv_mfma_f32_16x16x32_bf16 v[76:79], v[100:103], a[176:179], v[76:79]\nds_read_b128 v[100:103], v44 offset:16384\n"
"buffer_load_dwordx4 v10, s[12:15], s53 offen lds\ns_waitcnt lgkmcnt(6)\nv_mfma_f32_16x16x32_bf16 v[64:67], v[104:107], a[148:151], v[64:67]\nv_mfma_f32_16x16x32_bf16 v[68:71], v[104:107], a[180:183], v[68:71]\n"
"ds_read_b128 v[104:107], v41 offset:16384\nv_mfma_f32_16x16x32_bf16 v[72:75], v[108:111], a[148:151], v[72:75]\nv_mfma_f32_16x16x32_bf16 v[76:79], v[108:111], a[180:183], v[76:79]\nds_read_b128 v[108:111], v45 offset:16384\n"
"s_waitcnt lgkmcnt(6)\nv_mfma_f32_16x16x32_bf16 v[64:67], v[112:115], a[152:155], v[64:67]\nv_mfma_f32_16x16x32_bf16 v[68:71], v[112:115], a[184:187], v[68:71]\nds_read_b128 v[112:115], v42 offset:16384\n"
"v_mfma_f32_16x16x32_bf16 v[72:75], v[116:119], a[152:155], v[72:75]\nv_mfma_f32_16x16x32_bf16 v[76:79], v[116:119], a[184:187], v[76:79]\nds_read_b128 v[116:119], v46 offset:16384\ns_waitcnt lgkmcnt(6)\n"
"v_mfma_f32_16x16x32_bf16 v[64:67], v[120:123], a[156:159], v[64:67]\nv_mfma_f32_16x16x32_bf16 v[68:71], v[120:123], a[188:191], v[68:71]\nds_read_b128 v[120:123], v43 offset:16384\nv_mfma_f32_16x16x32_bf16 v[72:75], v[124:127], a[156:159], v[72:75]\n"
"v_mfma_f32_16x16x32_bf16 v[76:79], v[124:127], a[188:191], v[76:79]\nds_read_b128 v[124:127], v47 offset:16384\ns_waitcnt lgkmcnt(6)\nv_mfma_f32_16x16x32_bf16 v[80:83], v[96:99], a[128:131], 0\n"
"s_add_u32 m0, s32, 16384\nv_mfma_f32_16x16x32_bf16 v[84:87], v[96:99], a[160:163], 0\nds_read_b128 v[96:99], v40 offset:16640\ns_mov_b32 s53, 512\n"
"v_mfma_f32_16x16x32_bf16 v[88:91], v[100:103], a[128:131], 0\nbuffer_load_dwordx4 v10, s[12:15], s53 offen lds\nv_mfma_f32_16x16x32_bf16 v[92:95], v[100:103], a[160:163], 0\nds_read_b128 v[100:103], v44 offset:16640\n"
"v_cvt_pk_bf16_f32 v160, v64, v65\nv_cvt_pk_bf16_f32 v161, v66, v67\ns_waitcnt lgkmcnt(6)\nv_mfma_f32_16x16x32_bf16 v[80:83], v[104:107], a[132:135], v[80:83]\n"
"v_cvt_pk_bf16_f32 v162, v72, v73\nv_cvt_pk_bf16_f32 v163, v74, v75\nv_mfma_f32_16x16x32_bf16 v[84:87], v[104:107], a[164:167], v[84:87]\nds_read_b128 v[104:107], v41 offset:16640\n"
"v_cvt_pk_bf16_f32 v164, v68, v69\nv_cvt_pk_bf16_f32 v165, v70, v71\nv_mfma_f32_16x16x32_bf16 v[88:91], v[108:111], a[132:135], v[88:91]\nv_cvt_pk_bf16_f32 v166, v76, v77\n"
"v_cvt_pk_bf16_f32 v167, v78, v79\nv_mfma_f32_16x16x32_bf16 v[92:95], v[108:111], a[164:167], v[92:95]\nds_read_b128 v[108:111], v45 offset:16640\ns_lshl_b32 s54, s42, 11\n"
"s_add_u32 s54, s54, 0\ns_waitcnt lgkmcnt(6)\nv_mfma_f32_16x16x32_bf16 v[80:83], v[112:115], a[136:139], v[80:83]\nbuffer_store_dwordx4 v[160:163], v13, s[16:19], s54 offen sc1\n"
"v_mfma_f32_16x16x32_bf16 v[84:87], v[112:115], a[168:171], v[84:87]\nds_read_b128 v[112:115], v42 offset:16640\ns_add_u32 s54, s54, 229376\nv_mfma_f32_16x16x32_bf16 v[88:91], v[116:119], a[136:139], v[88:91]\n"
"buffer_store_dwordx4 v[164:167], v13, s[16:19], s54 offen sc1\nv_mfma_f32_16x16x32_bf16 v[92:95], v[116:119], a[168:171], v[92:95]\nds_read_b128 v[116:119], v46 offset:16640\ns_add_u32 m0, s32, 20480\n"
"s_waitcnt lgkmcnt(6)\nv_mfma_f32_16x16x32_bf16 v[80:83], v[120:123], a[140:143], v[80:83]\ns_mov_b32 s53, 640\nv_mfma_f32_16x16x32_bf16 v[84:87], v[120:123], a[172:175], v[84:87]\n"
"ds_read_b128 v[120:123], v43 offset:16640\nbuffer_load_dwordx4 v10, s[12:15], s53 offen lds\nv_mfma_f32_16x16x32_bf16 v[88:91], v[124:127], a[140:143], v[88:91]\ns_add_u32 m0, s32, 24576\n"
"v_mfma_f32_16x16x32_bf16 v[92:95], v[124:127], a[172:175], v[92:95]\nds_read_b128 v[124:127], v47 offset:16640\ns_mov_b32 s53, 768\ns_waitcnt vmcnt(10)\n"
"s_barrier\ns_waitcnt lgkmcnt(6)\nv_mfma_f32_16x16x32_bf16 v[80:83], v[96:99], a[144:147], v[80:83]\nbuffer_load_dwordx4 v10, s[12:15], s53 offen lds\n"
"v_mfma_f32_16x16x32_bf16 v[84:87], v[96:99], a[176:179], v[84:87]\nds_read_b128 v[96:99], v40 offset:32768\nv_mfma_f32_16x16x32_bf16 v[88:91], v[100:103], a[144:147], v[88:91]\nv_mfma_f32_16x16x32_bf16 v[92:95], v[100:103], a[176:179], v[92:95]\n"
"ds_read_b128 v[100:103], v44 offset:32768\ns_waitcnt lgkmcnt(6)\nv_mfma_f32_16x16x32_bf16 v[80:83], v[104:107], a[148:151], v[80:83]\nv_mfma_f32_16x16x32_bf16 v[84:87], v[104:107], a[180:183], v[84:87]\n"
"ds_read_b128 v[104:107], v41 offset:32768\nv_mfma_f32_16x16x32_bf16 v[88:91], v[108:111], a[148:151], v[88:91]\nv_mfma_f32_16x16x32_bf16 v[92:95], v[108:111], a[180:183], v[92:95]\nds_read_b128 v[108:111], v45 offset:32768\n"
"s_waitcnt lgkmcnt(6)\nv_mfma_f32_16x16x32_bf16 v[80:83], v[112:115], a[152:155], v[80:83]\nv_mfma_f32_16x16x32_bf16 v[84:87], v[112:115], a[184:187], v[84:87]\nds_read_b128 v[112:115], v42 offset:32768\n"
"v_mfma_f32_16x16x32_bf16 v[88:91], v[116:119], a[152:155], v[88:91]\nv_mfma_f32_16x16x32_bf16 v[92:95], v[116:119], a[184:187], v[92:95]\nds_read_b128 v[116:119], v46 offset:32768\ns_waitcnt lgkmcnt(6)\n"
"v_mfma_f32_16x16x32_bf16 v[80:83], v[120:123], a[156:159], v[80:83]\nv_mfma_f32_16x16x32_bf16 v[84:87], v[120:123], a[188:191], v[84:87]\nds_read_b128 v[120:123], v43 offset:32768\nv_mfma_f32_16x16x32_bf16 v[88:91], v[124:127], a[156:159], v[88:91]\n"
"v_mfma_f32_16x16x32_bf16 v[92:95], v[124:127], a[188:191], v[92:95]\nds_read_b128 v[124:127], v47 offset:32768\ns_cmp_lg_u32 s34, 2\ns_cbranch_scc1 fg1_main\n"
"s_nop 7\nv_cvt_pk_bf16_f32 v168, v80, v81\nv_cvt_pk_bf16_f32 v169, v82, v83\nv_cvt_pk_bf16_f32 v170, v88, v89\n"
"v_cvt_pk_bf16_f32 v171, v90, v91\nv_cvt_pk_bf16_f32 v172, v84, v85\nv_cvt_pk_bf16_f32 v173, v86, v87\nv_cvt_pk_bf16_f32 v174, v92, v93\n"
"v_cvt_pk_bf16_f32 v175, v94, v95\ns_lshl_b32 s54, s42, 11\ns_add_u32 s54, s54, 1024\nbuffer_store_dwordx4 v[168:171], v13, s[16:19], s54 offen sc1\n"
"s_add_u32 s54, s54, 229376\nbuffer_store_dwordx4 v[172:175], v13, s[16:19], s54 offen sc1\nv_mov_b32 v36, s28\ns_waitcnt vmcnt(0) lgkmcnt(0)\n"
"s_barrier\nbuffer_store_dword v36, v19, s[24:27], 0 offen sc1\ns_branch fg1_end\nfg1_main:\n"
"s_waitcnt lgkmcnt(6)\nv_mfma_f32_16x16x32_bf16 v[64:67], v[96:99], a[128:131], 0\ns_add_u32 m0, s32, 32768\nv_mfma_f32_16x16x32_bf16 v[68:71], v[96:99], a[160:163], 0\n"
"ds_read_b128 v[96:99], v40 offset:33024\ns_mov_b32 s53, 28672\nv_mfma_f32_16x16x32_bf16 v[72:75], v[100:103], a[128:131], 0\nbuffer_load_dwordx4 v10, s[12:15], s53 offen lds\n"
"v_mfma_f32_16x16x32_bf16 v[76:79], v[100:103], a[160:163], 0\nds_read_b128 v[100:103], v44 offset:33024\nv_cvt_pk_bf16_f32 v168, v80, v81\nv_cvt_pk_bf16_f32 v169, v82, v83\n"
"s_waitcnt lgkmcnt(6)\nv_mfma_f32_16x16x32_bf16 v[64:67], v[104:107], a[132:135], v[64:67]\nv_cvt_pk_bf16_f32 v170, v88, v89\nv_cvt_pk_bf16_f32 v171, v90, v91\n"
"v_mfma_f32_16x16x32_bf16 v[68:71], v[104:107], a[164:167], v[68:71]\nds_read_b128 v[104:107], v41 offset:33024\nv_cvt_pk_bf16_f32 v172, v84, v85\nv_cvt_pk_bf16_f32 v173, v86, v87\n"
"v_mfma_f32_16x16x32_bf16 v[72:75], v[108:111], a[132:135], v[72:75]\nv_cvt_pk_bf16_f32 v174, v92, v93\nv_cvt_pk_bf16_f32 v175, v94, v95\nv_mfma_f32_16x16x32_bf16 v[76:79], v[108:111], a[164:167], v[76:79]\n"
"ds_read_b128 v[108:111], v45 offset:33024\ns_lshl_b32 s54, s42, 11\ns_add_u32 s54, s54, 1024\ns_waitcnt lgkmcnt(6)\n"
"v_mfma_f32_16x16x32_bf16 v[64:67], v[112:115], a[136:139], v[64:67]\nbuffer_store_dwordx4 v[168:171], v13, s[16:19], s54 offen sc1\nv_mfma_f32_16x16x32_bf16 v[68:71], v[112:115], a[168:171], v[68:71]\nds_read_b128 v[112:115], v42 offset:33024\n"
"s_add_u32 s54, s54, 229376\nv_mfma_f32_16x16x32_bf16 v[72:75], v[116:119], a[136:139], v[72:75]\nbuffer_store_dwordx4 v[172:175], v13, s[16:19], s54 offen sc1\nv_mfma_f32_16x16x32_bf16 v[76:79], v[116:119], a[168:171], v[76:79]\n"
"ds_read_b128 v[116:119], v46 offset:33024\ns_add_u32 m0, s32, 36864\ns_waitcnt lgkmcnt(6)\nv_mfma_f32_16x16x32_bf16 v[64:67], v[120:123], a[140:143], v[64:67]\n"
"s_mov_b32 s53, 28800\nv_mfma_f32_16x16x32_bf16 v[68:71], v[120:123], a[172:175], v[68:71]\nds_read_b128 v[120:123], v43 offset:33024\nbuffer_load_dwordx4 v10, s[12:15], s53 offen lds\n"
"v_mfma_f32_16x16x32_bf16 v[72:75], v[124:127], a[140:143], v[72:75]\ns_add_u32 m0, s32, 40960\nv_mfma_f32_16x16x32_bf16 v[76:79], v[124:127], a[172:175], v[76:79]\nds_read_b128 v[124:127], v47 offset:33024\n"
"s_mov_b32 s53, 28928\ns_waitcnt lgkmcnt(6)\nv_mfma_f32_16x16x32_bf16 v[64:67], v[96:99], a[144:147], v[64:67]\nbuffer_load_dwordx4 v10, s[12:15], s53 offen lds\n"
"v_mfma_f32_16x16x32_bf16 v[68:71], v[96:99], a[176:179], v[68:71]\nds_read_b128 v[96:99], v40 offset:49152\ns_add_u32 m0, s32, 45056\nv_mfma_f32_16x16x32_bf16 v[72:75], v[100:103], a[144:147], v[72:75]\n"
"s_mov_b32 s53, 29056\nv_mfma_f32_16x16x32_bf16 v[76:79], v[100:103], a[176:179], v[76:79]\nds_read_b128 v[100:103], v44 offset:49152\nbuffer_load_dwordx4 v10, s[12:15], s53 offen lds\n"
"s_waitcnt lgkmcnt(6)\nv_mfma_f32_16x16x32_bf16 v[64:67], v[104:107], a[148:151], v[64:67]\nv_mfma_f32_16x16x32_bf16 v[68:71], v[104:107], a[180:183], v[68:71]\nds_read_b128 v[104:107], v41 offset:49152\n"
"v_mfma_f32_16x16x32_bf16 v[72:75], v[108:111], a[148:151], v[72:75]\nv_mfma_f32_16x16x32_bf16 v[76:79], v[108:111], a[180:183], v[76:79]\nds_read_b128 v[108:111], v45 offset:49152\ns_waitcnt lgkmcnt(6)\n"
"v_mfma_f32_16x16x32_bf16 v[64:67], v[112:115], a[152:155], v[64:67]\nv_mfma_f32_16x16x32_bf16 v[68:71], v[112:115], a[184:187], v[68:71]\nds_read_b128 v[112:115], v42 offset:49152\nv_mfma_f32_16x16x32_bf16 v[72:75], v[116:119], a[152:155], v[72:75]\n"
"v_mfma_f32_16x16x32_bf16 v[76:79], v[116:119], a[184:187], v[76:79]\nds_read_b128 v[116:119], v46 offset:49152\ns_waitcnt lgkmcnt(6)\nv_mfma_f32_16x16x32_bf16 v[64:67], v[120:123], a[156:159], v[64:67]\n"
"v_mfma_f32_16x16x32_bf16 v[68:71], v[120:123], a[188:191], v[68:71]\nds_read_b128 v[120:123], v43 offset:49152\nv_mfma_f32_16x16x32_bf16 v[72:75], v[124:127], a[156:159], v[72:75]\nv_mfma_f32_16x16x32_bf16 v[76:79], v[124:127], a[188:191], v[76:79]\n"
"ds_read_b128 v[124:127], v47 offset:49152\ns_waitcnt lgkmcnt(6)\nv_mfma_f32_16x16x32_bf16 v[80:83], v[96:99], a[128:131], 0\ns_add_u32 m0, s32, 49152\n"
"v_mfma_f32_16x16x32_bf16 v[84:87], v[96:99], a[160:163], 0\nds_read_b128 v[96:99], v40 offset:49408\ns_mov_b32 s53, 29184\nv_mfma_f32_16x16x32_bf16 v[88:91], v[100:103], a[128:131], 0\n"
"buffer_load_dwordx4 v10, s[12:15], s53 offen lds\nv_mfma_f32_16x16x32_bf16 v[92:95], v[100:103], a[160:163], 0\nds_read_b128 v[100:103], v44 offset:49408\nv_cvt_pk_bf16_f32 v176, v64, v65\n"
"v_cvt_pk_bf16_f32 v177, v66, v67\ns_waitcnt lgkmcnt(6)\nv_mfma_f32_16x16x32_bf16 v[80:83], v[104:107], a[132:135], v[80:83]\nv_cvt_pk_bf16_f32 v178, v72, v73\n"
"v_cvt_pk_bf16_f32 v179, v74, v75\nv_mfma_f32_16x16x32_bf16 v[84:87], v[104:107], a[164:167], v[84:87]\nds_read_b128 v[104:107], v41 offset:49408\nv_cvt_pk_bf16_f32 v180, v68, v69\n"
"v_cvt_pk_bf16_f32 v181, v70, v71\nv_mfma_f32_16x16x32_bf16 v[88:91], v[108:111], a[132:135], v[88:91]\nv_cvt_pk_bf16_f32 v182, v76, v77\nv_cvt_pk_bf16_f32 v183, v78, v79\n"
"v_mfma_f32_16x16x32_bf16 v[92:95], v[108:111], a[164:167], v[92:95]\nds_read_b128 v[108:111], v45 offset:49408\ns_lshl_b32 s54, s43, 11\ns_add_u32 s54, s54, 0\n"
"s_waitcnt lgkmcnt(6)\nv_mfma_f32_16x16x32_bf16 v[80:83], v[112:115], a[136:139], v[80:83]\nbuffer_store_dwordx4 v[176:179], v13, s[16:19], s54 offen sc1\nv_mfma_f32_16x16x32_bf16 v[84:87], v[112:115], a[168:171], v[84:87]\n"
"ds_read_b128 v[112:115], v42 offset:49408\ns_add_u32 s54, s54, 229376\nv_mfma_f32_16x16x32_bf16 v[88:91], v[116:119], a[136:139], v[88:91]\nbuffer_store_dwordx4 v[180:183], v13, s[16:19], s54 offen sc1\n"
"v_mfma_f32_16x16x32_bf16 v[92:95], v[116:119], a[168:171], v[92:95]\nds_read_b128 v[116:119], v46 offset:49408\ns_add_u32 m0, s32, 53248\ns_waitcnt lgkmcnt(6)\n"
"v_mfma_f32_16x16x32_bf16 v[80:83], v[120:123], a[140:143], v[80:83]\ns_mov_b32 s53, 29312\nv_mfma_f32_16x16x32_bf16 v[84:87], v[120:123], a[172:175], v[84:87]\nds_read_b128 v[120:123], v43 offset:49408\n"
"buffer_load_dwordx4 v10, s[12:15], s53 offen lds\nv_mfma_f32_16x16x32_bf16 v[88:91], v[124:127], a[140:143], v[88:91]\ns_add_u32 m0, s32, 57344\nv_mfma_f32_16x16x32_bf16 v[92:95], v[124:127], a[172:175], v[92:95]\n"
"ds_read_b128 v[124:127], v47 offset:49408\ns_mov_b32 s53, 29440\ns_waitcnt lgkmcnt(6)\nv_mfma_f32_16x16x32_bf16 v[80:83], v[96:99], a[144:147], v[80:83]\n"
"buffer_load_dwordx4 v10, s[12:15], s53 offen lds\nv_mfma_f32_16x16x32_bf16 v[84:87], v[96:99], a[176:179], v[84:87]\nbuffer_load_dword v37, v21, s[24:27], 0 offen sc1\nv_mfma_f32_16x16x32_bf16 v[88:91], v[100:103], a[144:147], v[88:91]\n"
"v_mfma_f32_16x16x32_bf16 v[92:95], v[100:103], a[176:179], v[92:95]\ns_waitcnt lgkmcnt(4)\nv_mfma_f32_16x16x32_bf16 v[80:83], v[104:107], a[148:151], v[80:83]\nv_mfma_f32_16x16x32_bf16 v[84:87], v[104:107], a[180:183], v[84:87]\n"
"v_mfma_f32_16x16x32_bf16 v[88:91], v[108:111], a[148:151], v[88:91]\nv_mfma_f32_16x16x32_bf16 v[92:95], v[108:111], a[180:183], v[92:95]\ns_waitcnt lgkmcnt(2)\nv_mfma_f32_16x16x32_bf16 v[80:83], v[112:115], a[152:155], v[80:83]\n"
"v_mfma_f32_16x16x32_bf16 v[84:87], v[112:115], a[184:187], v[84:87]\nv_mfma_f32_16x16x32_bf16 v[88:91], v[116:119], a[152:155], v[88:91]\nv_mfma_f32_16x16x32_bf16 v[92:95], v[116:119], a[184:187], v[92:95]\ns_waitcnt lgkmcnt(0)\n"
"v_mfma_f32_16x16x32_bf16 v[80:83], v[120:123], a[156:159], v[80:83]\nv_mfma_f32_16x16x32_bf16 v[84:87], v[120:123], a[188:191], v[84:87]\nv_mfma_f32_16x16x32_bf16 v[88:91], v[124:127], a[156:159], v[88:91]\nv_mfma_f32_16x16x32_bf16 v[92:95], v[124:127], a[188:191], v[92:95]\n"
"s_nop 7\nv_cvt_pk_bf16_f32 v184, v80, v81\nv_cvt_pk_bf16_f32 v185, v82, v83\nv_cvt_pk_bf16_f32 v186, v88, v89\n"
"v_cvt_pk_bf16_f32 v187, v90, v91\nv_cvt_pk_bf16_f32 v188, v84, v85\nv_cvt_pk_bf16_f32 v189, v86, v87\nv_cvt_pk_bf16_f32 v190, v92, v93\n"
"v_cvt_pk_bf16_f32 v191, v94, v95\ns_lshl_b32 s54, s43, 11\ns_add_u32 s54, s54, 1024\nbuffer_store_dwordx4 v[184:187], v13, s[16:19], s54 offen sc1\n"
"s_add_u32 s54, s54, 229376\nbuffer_store_dwordx4 v[188:191], v13, s[16:19], s54 offen sc1\ns_waitcnt vmcnt(2)\nv_mov_b32 v36, s28\n"
"v_cmp_ne_u32 vcc, s28, v37\ns_cbranch_vccz fg1_e1\ns_waitcnt vmcnt(0)\ns_barrier\n"
"buffer_store_dword v36, v19, s[24:27], 0 offen sc1\nfg1_po:\nv_cmp_ne_u32 vcc, s28, v37\ns_cbranch_vccz fg1_pook\n"
"s_add_u32 s55, s55, 1\ns_cmp_gt_u32 s55, 262144\ns_cbranch_scc0 fg1_po2\ns_endpgm\n"
"fg1_po2:\ns_sleep 1\nbuffer_load_dword v37, v21, s[24:27], 0 offen sc1\ns_waitcnt vmcnt(0)\n"
"s_branch fg1_po\nfg1_pook:\ns_mov_b32 s53, 8192\nbuffer_load_dwordx4 v[192:195], v13, s[16:19], s53 offen sc1\n"
"s_mov_b32 s53, 237568\nbuffer_load_dwordx4 v[196:199], v13, s[16:19], s53 offen sc1\ns_mov_b32 s53, 9216\nbuffer_load_dwordx4 v[200:203], v13, s[16:19], s53 offen sc1\n"
"s_mov_b32 s53, 238592\nbuffer_load_dwordx4 v[204:207], v13, s[16:19], s53 offen sc1\ns_mov_b32 s53, 10240\nbuffer_load_dwordx4 v[208:211], v13, s[16:19], s53 offen sc1\n"
"s_mov_b32 s53, 239616\nbuffer_load_dwordx4 v[212:215], v13, s[16:19], s53 offen sc1\ns_mov_b32 s53, 11264\nbuffer_load_dwordx4 v[216:219], v13, s[16:19], s53 offen sc1\n"
"s_mov_b32 s53, 240640\nbuffer_load_dwordx4 v[220:223], v13, s[16:19], s53 offen sc1\ns_mov_b32 s53, 12288\nbuffer_load_dwordx4 v[224:227], v13, s[16:19], s53 offen sc1\n"
"s_mov_b32 s53, 241664\nbuffer_load_dwordx4 v[228:231], v13, s[16:19], s53 offen sc1\ns_mov_b32 s53, 13312\nbuffer_load_dwordx4 v[232:235], v13, s[16:19], s53 offen sc1\n"
"s_mov_b32 s53, 242688\nbuffer_load_dwordx4 v[236:239], v13, s[16:19], s53 offen sc1\nbuffer_store_dword v36, v19, s[24:27], 0 offen sc1\ns_branch fg1_e2\n"
"fg1_e1:\ns_mov_b32 s53, 8192\nbuffer_load_dwordx4 v[192:195], v13, s[16:19], s53 offen sc1\ns_mov_b32 s53, 237568\n"
"buffer_load_dwordx4 v[196:199], v13, s[16:19], s53 offen sc1\ns_mov_b32 s53, 9216\nbuffer_load_dwordx4 v[200:203], v13, s[16:19], s53 offen sc1\ns_mov_b32 s53, 238592\n"
"buffer_load_dwordx4 v[204:207], v13, s[16:19], s53 offen sc1\ns_mov_b32 s53, 10240\nbuffer_load_dwordx4 v[208:211], v13, s[16:19], s53 offen sc1\ns_mov_b32 s53, 239616\n"
"buffer_load_dwordx4 v[212:215], v13, s[16:19], s53 offen sc1\ns_mov_b32 s53, 11264\nbuffer_load_dwordx4 v[216:219], v13, s[16:19], s53 offen sc1\ns_mov_b32 s53, 240640\n"
"buffer_load_dwordx4 v[220:223], v13, s[16:19], s53 offen sc1\ns_mov_b32 s53, 12288\nbuffer_load_dwordx4 v[224:227], v13, s[16:19], s53 offen sc1\ns_mov_b32 s53, 241664\n"
"buffer_load_dwordx4 v[228:231], v13, s[16:19], s53 offen sc1\ns_mov_b32 s53, 13312\nbuffer_load_dwordx4 v[232:235], v13, s[16:19], s53 offen sc1\ns_mov_b32 s53, 242688\n"
"buffer_load_dwordx4 v[236:239], v13, s[16:19], s53 offen sc1\ns_waitcnt vmcnt(12)\ns_barrier\nbuffer_store_dword v36, v19, s[24:27], 0 offen sc1\n"
"fg1_e2:\ns_add_u32 m0, s32, 65536\ns_mov_b32 s53, 57344\nbuffer_load_dwordx4 v10, s[12:15], s53 offen lds\n"
"s_add_u32 m0, s32, 69632\ns_mov_b32 s53, 57472\nbuffer_load_dwordx4 v10, s[12:15], s53 offen lds\ns_add_u32 m0, s32, 73728\n"
"s_mov_b32 s53, 57600\nbuffer_load_dwordx4 v10, s[12:15], s53 offen lds\ns_add_u32 m0, s32, 77824\ns_mov_b32 s53, 57728\n"
"buffer_load_dwordx4 v10, s[12:15], s53 offen lds\ns_add_u32 m0, s32, 81920\ns_mov_b32 s53, 57856\nbuffer_load_dwordx4 v10, s[12:15], s53 offen lds\n"
"s_add_u32 m0, s32, 86016\ns_mov_b32 s53, 57984\nbuffer_load_dwordx4 v10, s[12:15], s53 offen lds\ns_add_u32 m0, s32, 90112\n"
"s_mov_b32 s53, 58112\nbuffer_load_dwordx4 v10, s[12:15], s53 offen lds\ns_sub_u32 s48, s28, 1\ns_mov_b32 s49, 86016\n"
"s_mov_b32 s50, 0\ns_mov_b32 s51, 0\ns_add_u32 s52, s32, 98304\ns_add_u32 s47, s32, 131072\n"
"ds_read_b128 v[40:43], v32 offset:0\nds_read_b128 v[44:47], v33 offset:0\nds_read_b128 v[48:51], v34 offset:0\nds_read_b128 v[52:55], v35 offset:0\n"
"s_barrier\ns_waitcnt lgkmcnt(3)\nv_mfma_f32_16x16x32_bf16 v[64:67], v[40:43], v[128:131], 0\nv_mov_b32 v36, s48\n"
"s_add_u32 s48, s48, 1\ns_nop 0\nbuffer_store_dword v36, v19, s[24:27], s33 offen sc1\nv_mfma_f32_16x16x32_bf16 v[68:71], v[40:43], v[132:135], 0\n"
"ds_read_b128 v[40:43], v32 offset:4096\ns_waitcnt lgkmcnt(3)\nv_mfma_f32_16x16x32_bf16 v[72:75], v[44:47], v[128:131], 0\nv_mfma_f32_16x16x32_bf16 v[76:79], v[44:47], v[132:135], 0\n"
"ds_read_b128 v[44:47], v33 offset:4096\ns_waitcnt lgkmcnt(3)\nv_mfma_f32_16x16x32_bf16 v[64:67], v[48:51], v[136:139], v[64:67]\ns_add_u32 m0, s52, 0\n"
"v_mfma_f32_16x16x32_bf16 v[68:71], v[48:51], v[140:143], v[68:71]\nds_read_b128 v[48:51], v34 offset:4096\ns_add_u32 s53, s49, 0\ns_waitcnt lgkmcnt(3)\n"
"v_mfma_f32_16x16x32_bf16 v[72:75], v[52:55], v[136:139], v[72:75]\nbuffer_load_dwordx4 v10, s[12:15], s53 offen lds\nv_mfma_f32_16x16x32_bf16 v[76:79], v[52:55], v[140:143], v[76:79]\nds_read_b128 v[52:55], v35 offset:4096\n"
"s_add_u32 m0, s52, 4096\ns_waitcnt lgkmcnt(3)\nv_mfma_f32_16x16x32_bf16 v[64:67], v[40:43], v[144:147], v[64:67]\ns_add_u32 s53, s49, 128\n"
"v_mfma_f32_16x16x32_bf16 v[68:71], v[40:43], v[148:151], v[68:71]\nds_read_b128 v[40:43], v32 offset:8192\nbuffer_load_dwordx4 v10, s[12:15], s53 offen lds\ns_waitcnt lgkmcnt(3)\n"
"v_mfma_f32_16x16x32_bf16 v[72:75], v[44:47], v[144:147], v[72:75]\ns_add_u32 m0, s52, 8192\nv_mfma_f32_16x16x32_bf16 v[76:79], v[44:47], v[148:151], v[76:79]\nds_read_b128 v[44:47], v33 offset:8192\n"
"s_add_u32 s53, s49, 256\ns_waitcnt lgkmcnt(3)\nv_mfma_f32_16x16x32_bf16 v[64:67], v[48:51], v[152:155], v[64:67]\nbuffer_load_dwordx4 v10, s[12:15], s53 offen lds\n"
"v_mfma_f32_16x16x32_bf16 v[68:71], v[48:51], v[156:159], v[68:71]\nds_read_b128 v[48:51], v34 offset:8192\ns_add_u32 m0, s52, 12288\ns_waitcnt lgkmcnt(3)\n"
"v_mfma_f32_16x16x32_bf16 v[72:75], v[52:55], v[152:155], v[72:75]\ns_add_u32 s53, s49, 384\nv_mfma_f32_16x16x32_bf16 v[76:79], v[52:55], v[156:159], v[76:79]\nds_read_b128 v[52:55], v35 offset:8192\n"
"buffer_load_dwordx4 v10, s[12:15], s53 offen lds\ns_waitcnt lgkmcnt(3)\nv_mfma_f32_16x16x32_bf16 v[64:67], v[40:43], v[160:163], v[64:67]\ns_add_u32 m0, s52, 16384\n"
"v_mfma_f32_16x16x32_bf16 v[68:71], v[40:43], v[164:167], v[68:71]\nds_read_b128 v[40:43], v32 offset:12288\ns_add_u32 s53, s49, 512\ns_waitcnt lgkmcnt(3)\n"
"v_mfma_f32_16x16x32_bf16 v[72:75], v[44:47], v[160:163], v[72:75]\nbuffer_load_dwordx4 v10, s[12:15], s53 offen lds\nv_mfma_f32_16x16x32_bf16 v[76:79], v[44:47], v[164:167], v[76:79]\nds_read_b128 v[44:47], v33 offset:12288\n"
"s_add_u32 m0, s52, 20480\ns_waitcnt lgkmcnt(3)\nv_mfma_f32_16x16x32_bf16 v[64:67], v[48:51], v[168:171], v[64:67]\ns_add_u32 s53, s49, 640\n"
"v_mfma_f32_16x16x32_bf16 v[68:71], v[48:51], v[172:175], v[68:71]\nds_read_b128 v[48:51], v34 offset:12288\nbuffer_load_dwordx4 v10, s[12:15], s53 offen lds\ns_waitcnt lgkmcnt(3)\n"
"v_mfma_f32_16x16x32_bf16 v[72:75], v[52:55], v[168:171], v[72:75]\ns_add_u32 m0, s52, 24576\nv_mfma_f32_16x16x32_bf16 v[76:79], v[52:55], v[172:175], v[76:79]\nds_read_b128 v[52:55], v35 offset:12288\n"
"s_add_u32 s53, s49, 768\ns_waitcnt lgkmcnt(3)\nv_mfma_f32_16x16x32_bf16 v[64:67], v[40:43], v[176:179], v[64:67]\nbuffer_load_dwordx4 v10, s[12:15], s53 offen lds\n"
"v_mfma_f32_16x16x32_bf16 v[68:71], v[40:43], v[180:183], v[68:71]\nds_read_b128 v[40:43], v32 offset:16384\ns_waitcnt lgkmcnt(3)\nv_mfma_f32_16x16x32_bf16 v[72:75], v[44:47], v[176:179], v[72:75]\n"
"v_mfma_f32_16x16x32_bf16 v[76:79], v[44:47], v[180:183], v[76:79]\nds_read_b128 v[44:47], v33 offset:16384\ns_waitcnt lgkmcnt(3)\nv_mfma_f32_16x16x32_bf16 v[64:67], v[48:51], v[184:187], v[64:67]\n"
"v_mfma_f32_16x16x32_bf16 v[68:71], v[48:51], v[188:191], v[68:71]\nds_read_b128 v[48:51], v34 offset:16384\ns_waitcnt lgkmcnt(3)\nv_mfma_f32_16x16x32_bf16 v[72:75], v[52:55], v[184:187], v[72:75]\n"
"v_mfma_f32_16x16x32_bf16 v[76:79], v[52:55], v[188:191], v[76:79]\nds_read_b128 v[52:55], v35 offset:16384\ns_waitcnt vmcnt(16)\ns_waitcnt lgkmcnt(3)\n"
"v_mfma_f32_16x16x32_bf16 v[64:67], v[40:43], v[192:195], v[64:67]\nv_mfma_f32_16x16x32_bf16 v[68:71], v[40:43], v[196:199], v[68:71]\nds_read_b128 v[40:43], v32 offset:20480\ns_waitcnt lgkmcnt(3)\n"
"v_mfma_f32_16x16x32_bf16 v[72:75], v[44:47], v[192:195], v[72:75]\nv_mfma_f32_16x16x32_bf16 v[76:79], v[44:47], v[196:199], v[76:79]\nds_read_b128 v[44:47], v33 offset:20480\ns_waitcnt lgkmcnt(3)\n"
"v_mfma_f32_16x16x32_bf16 v[64:67], v[48:51], v[200:203], v[64:67]\nv_mfma_f32_16x16x32_bf16 v[68:71], v[48:51], v[204:207], v[68:71]\nds_read_b128 v[48:51], v34 offset:20480\ns_waitcnt lgkmcnt(3)\n"
"v_mfma_f32_16x16x32_bf16 v[72:75], v[52:55], v[200:203], v[72:75]\nv_mfma_f32_16x16x32_bf16 v[76:79], v[52:55], v[204:207], v[76:79]\nds_read_b128 v[52:55], v35 offset:20480\ns_waitcnt lgkmcnt(3)\n"
"v_mfma_f32_16x16x32_bf16 v[64:67], v[40:43], v[208:211], v[64:67]\nv_mfma_f32_16x16x32_bf16 v[68:71], v[40:43], v[212:215], v[68:71]\nds_read_b128 v[40:43], v32 offset:24576\ns_waitcnt lgkmcnt(3)\n"
"v_mfma_f32_16x16x32_bf16 v[72:75], v[44:47], v[208:211], v[72:75]\nv_mfma_f32_16x16x32_bf16 v[76:79], v[44:47], v[212:215], v[76:79]\nds_read_b128 v[44:47], v33 offset:24576\ns_waitcnt lgkmcnt(3)\n"
"v_mfma_f32_16x16x32_bf16 v[64:67], v[48:51], v[216:219], v[64:67]\ns_add_u32 s54, s51, 32768\ns_cmp_eq_u32 s54, 163840\nv_mfma_f32_16x16x32_bf16 v[68:71], v[48:51], v[220:223], v[68:71]\n"
"s_cselect_b32 s54, 0, s54\ns_sub_u32 s54, s54, s51\nds_read_b128 v[48:51], v34 offset:24576\ns_waitcnt lgkmcnt(3)\n"
"v_mfma_f32_16x16x32_bf16 v[72:75], v[52:55], v[216:219], v[72:75]\nv_mfma_f32_16x16x32_bf16 v[76:79], v[52:55], v[220:223], v[76:79]\nds_read_b128 v[52:55], v35 offset:24576\ns_waitcnt lgkmcnt(3)\n"
"v_mfma_f32_16x16x32_bf16 v[64:67], v[40:43], v[224:227], v[64:67]\nv_add_u32 v32, s54, v32\nv_mfma_f32_16x16x32_bf16 v[68:71], v[40:43], v[228:231], v[68:71]\nds_read_b128 v[40:43], v32 offset:0\n"
"s_waitcnt lgkmcnt(3)\nv_mfma_f32_16x16x32_bf16 v[72:75], v[44:47], v[224:227], v[72:75]\nv_add_u32 v33, s54, v33\nv_mfma_f32_16x16x32_bf16 v[76:79], v[44:47], v[228:231], v[76:79]\n"
"ds_read_b128 v[44:47], v33 offset:0\ns_waitcnt lgkmcnt(3)\nv_mfma_f32_16x16x32_bf16 v[64:67], v[48:51], v[232:235], v[64:67]\nv_add_u32 v34, s54, v34\n"
"v_mfma_f32_16x16x32_bf16 v[68:71], v[48:51], v[236:239], v[68:71]\nds_read_b128 v[48:51], v34 offset:0\ns_waitcnt lgkmcnt(3)\nv_mfma_f32_16x16x32_bf16 v[72:75], v[52:55], v[232:235], v[72:75]\n"
"v_add_u32 v35, s54, v35\nv_mfma_f32_16x16x32_bf16 v[76:79], v[52:55], v[236:239], v[76:79]\nds_read_b128 v[52:55], v35 offset:0\ns_mov_b32 s52, s47\n"
"s_add_u32 s47, s51, s32\ns_add_u32 s51, s51, s54\ns_add_u32 s49, s49, 28672\ns_waitcnt vmcnt(8)\n"
"s_barrier\ns_waitcnt lgkmcnt(3)\nv_mfma_f32_16x16x32_bf16 v[96:99], v[40:43], v[128:131], 0\nv_mov_b32 v36, s48\n"
"s_add_u32 s48, s48, 1\ns_nop 0\nbuffer_store_dword v36, v19, s[24:27], s33 offen sc1\nv_mfma_f32_16x16x32_bf16 v[100:103], v[40:43], v[132:135], 0\n"
"ds_read_b128 v[40:43], v32 offset:4096\ns_waitcnt lgkmcnt(3)\nv_mfma_f32_16x16x32_bf16 v[104:107], v[44:47], v[128:131], 0\nv_mfma_f32_16x16x32_bf16 v[108:111], v[44:47], v[132:135], 0\n"
"ds_read_b128 v[44:47], v33 offset:4096\nv_cvt_pk_bf16_f32 v56, v64, v65\ns_waitcnt lgkmcnt(3)\nv_mfma_f32_16x16x32_bf16 v[96:99], v[48:51], v[136:139], v[96:99]\n"
"v_cvt_pk_bf16_f32 v57, v66, v67\nv_mfma_f32_16x16x32_bf16 v[100:103], v[48:51], v[140:143], v[100:103]\nds_read_b128 v[48:51], v34 offset:4096\nv_cvt_pk_bf16_f32 v58, v72, v73\n"
"s_waitcnt lgkmcnt(3)\nv_mfma_f32_16x16x32_bf16 v[104:107], v[52:55], v[136:139], v[104:107]\nv_cvt_pk_bf16_f32 v59, v74, v75\nv_mfma_f32_16x16x32_bf16 v[108:111], v[52:55], v[140:143], v[108:111]\n"
"ds_read_b128 v[52:55], v35 offset:4096\ns_waitcnt lgkmcnt(3)\nv_mfma_f32_16x16x32_bf16 v[96:99], v[40:43], v[144:147], v[96:99]\nv_mfma_f32_16x16x32_bf16 v[100:103], v[40:43], v[148:151], v[100:103]\n"
"ds_read_b128 v[40:43], v32 offset:8192\nbuffer_store_dwordx4 v[56:59], v15, s[20:23], s50 offen sc1\ns_waitcnt lgkmcnt(3)\nv_mfma_f32_16x16x32_bf16 v[104:107], v[44:47], v[144:147], v[104:107]\n"
"v_cvt_pk_bf16_f32 v60, v68, v69\nv_mfma_f32_16x16x32_bf16 v[108:111], v[44:47], v[148:151], v[108:111]\nds_read_b128 v[44:47], v33 offset:8192\nv_cvt_pk_bf16_f32 v61, v70, v71\n"
"s_waitcnt lgkmcnt(3)\nv_mfma_f32_16x16x32_bf16 v[96:99], v[48:51], v[152:155], v[96:99]\nv_cvt_pk_bf16_f32 v62, v76, v77\nv_mfma_f32_16x16x32_bf16 v[100:103], v[48:51], v[156:159], v[100:103]\n"
"ds_read_b128 v[48:51], v34 offset:8192\nv_cvt_pk_bf16_f32 v63, v78, v79\ns_waitcnt lgkmcnt(3)\nv_mfma_f32_16x16x32_bf16 v[104:107], v[52:55], v[152:155], v[104:107]\n"
"v_mfma_f32_16x16x32_bf16 v[108:111], v[52:55], v[156:159], v[108:111]\nds_read_b128 v[52:55], v35 offset:8192\ns_waitcnt lgkmcnt(3)\nv_mfma_f32_16x16x32_bf16 v[96:99], v[40:43], v[160:163], v[96:99]\n"
"buffer_store_dwordx4 v[60:63], v16, s[20:23], s50 offen sc1\nv_mfma_f32_16x16x32_bf16 v[100:103], v[40:43], v[164:167], v[100:103]\nds_read_b128 v[40:43], v32 offset:12288\ns_add_u32 s50, s50, s36\n"
"s_waitcnt lgkmcnt(3)\nv_mfma_f32_16x16x32_bf16 v[104:107], v[44:47], v[160:163], v[104:107]\ns_add_u32 m0, s52, 0\nv_mfma_f32_16x16x32_bf16 v[108:111], v[44:47], v[164:167], v[108:111]\n"
"ds_read_b128 v[44:47], v33 offset:12288\ns_add_u32 s53, s49, 0\ns_waitcnt lgkmcnt(3)\nv_mfma_f32_16x16x32_bf16 v[96:99], v[48:51], v[168:171], v[96:99]\n"
"buffer_load_dwordx4 v10, s[12:15], s53 offen lds\nv_mfma_f32_16x16x32_bf16 v[100:103], v[48:51], v[172:175], v[100:103]\nds_read_b128 v[48:51], v34 offset:12288\ns_add_u32 m0, s52, 4096\n"
"s_waitcnt lgkmcnt(3)\nv_mfma_f32_16x16x32_bf16 v[104:107], v[52:55], v[168:171], v[104:107]\ns_add_u32 s53, s49, 128\nv_mfma_f32_16x16x32_bf16 v[108:111], v[52:55], v[172:175], v[108:111]\n"
"ds_read_b128 v[52:55], v35 offset:12288\nbuffer_load_dwordx4 v10, s[12:15], s53 offen lds\ns_waitcnt lgkmcnt(3)\nv_mfma_f32_16x16x32_bf16 v[96:99], v[40:43], v[176:179], v[96:99]\n"
"s_add_u32 m0, s52, 8192\nv_mfma_f32_16x16x32_bf16 v[100:103], v[40:43], v[180:183], v[100:103]\nds_read_b128 v[40:43], v32 offset:16384\ns_add_u32 s53, s49, 256\n"
"s_waitcnt lgkmcnt(3)\nv_mfma_f32_16x16x32_bf16 v[104:107], v[44:47], v[176:179], v[104:107]\nbuffer_load_dwordx4 v10, s[12:15], s53 offen lds\nv_mfma_f32_16x16x32_bf16 v[108:111], v[44:47], v[180:183], v[108:111]\n"
"ds_read_b128 v[44:47], v33 offset:16384\ns_add_u32 m0, s52, 12288\ns_waitcnt lgkmcnt(3)\nv_mfma_f32_16x16x32_bf16 v[96:99], v[48:51], v[184:187], v[96:99]\n"
"s_add_u32 s53, s49, 384\nv_mfma_f32_16x16x32_bf16 v[100:103], v[48:51], v[188:191], v[100:103]\nds_read_b128 v[48:51], v34 offset:16384\nbuffer_load_dwordx4 v10, s[12:15], s53 offen lds\n"
"s_waitcnt lgkmcnt(3)\nv_mfma_f32_16x16x32_bf16 v[104:107], v[52:55], v[184:187], v[104:107]\ns_add_u32 m0, s52, 16384\nv_mfma_f32_16x16x32_bf16 v[108:111], v[52:55], v[188:191], v[108:111]\n"
"ds_read_b128 v[52:55], v35 offset:16384\ns_add_u32 s53, s49, 512\ns_waitcnt lgkmcnt(3)\nv_mfma_f32_16x16x32_bf16 v[96:99], v[40:43], v[192:195], v[96:99]\n"
"buffer_load_dwordx4 v10, s[12:15], s53 offen lds\nv_mfma_f32_16x16x32_bf16 v[100:103], v[40:43], v[196:199], v[100:103]\nds_read_b128 v[40:43], v32 offset:20480\ns_add_u32 m0, s52, 20480\n"
"s_waitcnt lgkmcnt(3)\nv_mfma_f32_16x16x32_bf16 v[104:107], v[44:47], v[192:195], v[104:107]\ns_add_u32 s53, s49, 640\nv_mfma_f32_16x16x32_bf16 v[108:111], v[44:47], v[196:199], v[108:111]\n"
"ds_read_b128 v[44:47], v33 offset:20480\nbuffer_load_dwordx4 v10, s[12:15], s53 offen lds\ns_waitcnt lgkmcnt(3)\nv_mfma_f32_16x16x32_bf16 v[96:99], v[48:51], v[200:203], v[96:99]\n"
"s_add_u32 m0, s52, 24576\nv_mfma_f32_16x16x32_bf16 v[100:103], v[48:51], v[204:207], v[100:103]\nds_read_b128 v[48:51], v34 offset:20480\ns_add_u32 s53, s49, 768\n"
"s_waitcnt lgkmcnt(3)\nv_mfma_f32_16x16x32_bf16 v[104:107], v[52:55], v[200:203], v[104:107]\nbuffer_load_dwordx4 v10, s[12:15], s53 offen lds\nv_mfma_f32_16x16x32_bf16 v[108:111], v[52:55], v[204:207], v[108:111]\n"
"ds_read_b128 v[52:55], v35 offset:20480\ns_waitcnt lgkmcnt(3)\nv_mfma_f32_16x16x32_bf16 v[96:99], v[40:43], v[208:211], v[96:99]\nv_mfma_f32_16x16x32_bf16 v[100:103], v[40:43], v[212:215], v[100:103]\n"
"ds_read_b128 v[40:43], v32 offset:24576\ns_waitcnt lgkmcnt(3)\nv_mfma_f32_16x16x32_bf16 v[104:107], v[44:47], v[208:211], v[104:107]\nv_mfma_f32_16x16x32_bf16 v[108:111], v[44:47], v[212:215], v[108:111]\n"
"ds_read_b128 v[44:47], v33 offset:24576\ns_waitcnt lgkmcnt(3)\nv_mfma_f32_16x16x32_bf16 v[96:99], v[48:51], v[216:219], v[96:99]\ns_add_u32 s54, s51, 32768\n"
"s_cmp_eq_u32 s54, 163840\nv_mfma_f32_16x16x32_bf16 v[100:103], v[48:51], v[220:223], v[100:103]\ns_cselect_b32 s54, 0, s54\ns_sub_u32 s54, s54, s51\n"
"ds_read_b128 v[48:51], v34 offset:24576\ns_waitcnt lgkmcnt(3)\nv_mfma_f32_16x16x32_bf16 v[104:107], v[52:55], v[216:219], v[104:107]\nv_mfma_f32_16x16x32_bf16 v[108:111], v[52:55], v[220:223], v[108:111]\n"
"ds_read_b128 v[52:55], v35 offset:24576\ns_waitcnt lgkmcnt(3)\nv_mfma_f32_16x16x32_bf16 v[96:99], v[40:43], v[224:227], v[96:99]\nv_add_u32 v32, s54, v32\n"
"v_mfma_f32_16x16x32_bf16 v[100:103], v[40:43], v[228:231], v[100:103]\nds_read_b128 v[40:43], v32 offset:0\ns_waitcnt lgkmcnt(3)\nv_mfma_f32_16x16x32_bf16 v[104:107], v[44:47], v[224:227], v[104:107]\n"
"v_add_u32 v33, s54, v33\nv_mfma_f32_16x16x32_bf16 v[108:111], v[44:47], v[228:231], v[108:111]\nds_read_b128 v[44:47], v33 offset:0\ns_waitcnt lgkmcnt(3)\n"
"v_mfma_f32_16x16x32_bf16 v[96:99], v[48:51], v[232:235], v[96:99]\nv_add_u32 v34, s54, v34\nv_mfma_f32_16x16x32_bf16 v[100:103], v[48:51], v[236:239], v[100:103]\nds_read_b128 v[48:51], v34 offset:0\n"
"s_waitcnt lgkmcnt(3)\nv_mfma_f32_16x16x32_bf16 v[104:107], v[52:55], v[232:235], v[104:107]\nv_add_u32 v35, s54, v35\nv_mfma_f32_16x16x32_bf16 v[108:111], v[52:55], v[236:239], v[108:111]\n"
"ds_read_b128 v[52:55], v35 offset:0\ns_mov_b32 s52, s47\ns_add_u32 s47, s51, s32\ns_add_u32 s51, s51, s54\n"
"s_add_u32 s49, s49, 28672\ns_waitcnt vmcnt(7)\ns_barrier\ns_waitcnt lgkmcnt(3)\n"
"v_mfma_f32_16x16x32_bf16 v[64:67], v[40:43], v[128:131], 0\nv_mov_b32 v36, s48\ns_add_u32 s48, s48, 1\ns_nop 0\n"
"buffer_store_dword v36, v19, s[24:27], s33 offen sc1\nv_mfma_f32_16x16x32_bf16 v[68:71], v[40:43], v[132:135], 0\nds_read_b128 v[40:43], v32 offset:4096\ns_waitcnt lgkmcnt(3)\n"
"v_mfma_f32_16x16x32_bf16 v[72:75], v[44:47], v[128:131], 0\nv_mfma_f32_16x16x32_bf16 v[76:79], v[44:47], v[132:135], 0\nds_read_b128 v[44:47], v33 offset:4096\nv_cvt_pk_bf16_f32 v56, v96, v97\n"
"s_waitcnt lgkmcnt(3)\nv_mfma_f32_16x16x32_bf16 v[64:67], v[48:51], v[136:139], v[64:67]\nv_cvt_pk_bf16_f32 v57, v98, v99\nv_mfma_f32_16x16x32_bf16 v[68:71], v[48:51], v[140:143], v[68:71]\n"
"ds_read_b128 v[48:51], v34 offset:4096\nv_cvt_pk_bf16_f32 v58, v104, v105\ns_waitcnt lgkmcnt(3)\nv_mfma_f32_16x16x32_bf16 v[72:75], v[52:55], v[136:139], v[72:75]\n"
"v_cvt_pk_bf16_f32 v59, v106, v107\nv_mfma_f32_16x16x32_bf16 v[76:79], v[52:55], v[140:143], v[76:79]\nds_read_b128 v[52:55], v35 offset:4096\ns_waitcnt lgkmcnt(3)\n"
"v_mfma_f32_16x16x32_bf16 v[64:67], v[40:43], v[144:147], v[64:67]\nv_mfma_f32_16x16x32_bf16 v[68:71], v[40:43], v[148:151], v[68:71]\nds_read_b128 v[40:43], v32 offset:8192\nbuffer_store_dwordx4 v[56:59], v15, s[20:23], s50 offen sc1\n"
"s_waitcnt lgkmcnt(3)\nv_mfma_f32_16x16x32_bf16 v[72:75], v[44:47], v[144:147], v[72:75]\nv_cvt_pk_bf16_f32 v60, v100, v101\nv_mfma_f32_16x16x32_bf16 v[76:79], v[44:47], v[148:151], v[76:79]\n"
"ds_read_b128 v[44:47], v33 offset:8192\nv_cvt_pk_bf16_f32 v61, v102, v103\ns_waitcnt lgkmcnt(3)\nv_mfma_f32_16x16x32_bf16 v[64:67], v[48:51], v[152:155], v[64:67]\n"
"v_cvt_pk_bf16_f32 v62, v108, v109\nv_mfma_f32_16x16x32_bf16 v[68:71], v[48:51], v[156:159], v[68:71]\nds_read_b128 v[48:51], v34 offset:8192\nv_cvt_pk_bf16_f32 v63, v110, v111\n"
"s_waitcnt lgkmcnt(3)\nv_mfma_f32_16x16x32_bf16 v[72:75], v[52:55], v[152:155], v[72:75]\nv_mfma_f32_16x16x32_bf16 v[76:79], v[52:55], v[156:159], v[76:79]\nds_read_b128 v[52:55], v35 offset:8192\n"
"s_waitcnt lgkmcnt(3)\nv_mfma_f32_16x16x32_bf16 v[64:67], v[40:43], v[160:163], v[64:67]\nbuffer_store_dwordx4 v[60:63], v16, s[20:23], s50 offen sc1\nv_mfma_f32_16x16x32_bf16 v[68:71], v[40:43], v[164:167], v[68:71]\n"
"ds_read_b128 v[40:43], v32 offset:12288\ns_add_u32 s50, s50, s36\ns_waitcnt lgkmcnt(3)\nv_mfma_f32_16x16x32_bf16 v[72:75], v[44:47], v[160:163], v[72:75]\n"
"s_add_u32 m0, s52, 0\nv_mfma_f32_16x16x32_bf16 v[76:79], v[44:47], v[164:167], v[76:79]\nds_read_b128 v[44:47], v33 offset:12288\ns_add_u32 s53, s49, 0\n"
"s_waitcnt lgkmcnt(3)\nv_mfma_f32_16x16x32_bf16 v[64:67], v[48:51], v[168:171], v[64:67]\nbuffer_load_dwordx4 v10, s[12:15], s53 offen lds\nv_mfma_f32_16x16x32_bf16 v[68:71], v[48:51], v[172:175], v[68:71]\n"
"ds_read_b128 v[48:51], v34 offset:12288\ns_add_u32 m0, s52, 4096\ns_waitcnt lgkmcnt(3)\nv_mfma_f32_16x16x32_bf16 v[72:75], v[52:55], v[168:171], v[72:75]\n"
"s_add_u32 s53, s49, 128\nv_mfma_f32_16x16x32_bf16 v[76:79], v[52:55], v[172:175], v[76:79]\nds_read_b128 v[52:55], v35 offset:12288\nbuffer_load_dwordx4 v10, s[12:15], s53 offen lds\n"
"s_waitcnt lgkmcnt(3)\nv_mfma_f32_16x16x32_bf16 v[64:67], v[40:43], v[176:179], v[64:67]\ns_add_u32 m0, s52, 8192\nv_mfma_f32_16x16x32_bf16 v[68:71], v[40:43], v[180:183], v[68:71]\n"
"ds_read_b128 v[40:43], v32 offset:16384\ns_add_u32 s53, s49, 256\ns_waitcnt lgkmcnt(3)\nv_mfma_f32_16x16x32_bf16 v[72:75], v[44:47], v[176:179], v[72:75]\n"
"buffer_load_dwordx4 v10, s[12:15], s53 offen lds\nv_mfma_f32_16x16x32_bf16 v[76:79], v[44:47], v[180:183], v[76:79]\nds_read_b128 v[44:47], v33 offset:16384\ns_add_u32 m0, s52, 12288\n"
"s_waitcnt lgkmcnt(3)\nv_mfma_f32_16x16x32_bf16 v[64:67], v[48:51], v[184:187], v[64:67]\ns_add_u32 s53, s49, 384\nv_mfma_f32_16x16x32_bf16 v[68:71], v[48:51], v[188:191], v[68:71]\n"
"ds_read_b128 v[48:51], v34 offset:16384\nbuffer_load_dwordx4 v10, s[12:15], s53 offen lds\ns_waitcnt lgkmcnt(3)\nv_mfma_f32_16x16x32_bf16 v[72:75], v[52:55], v[184:187], v[72:75]\n"
"s_add_u32 m0, s52, 16384\nv_mfma_f32_16x16x32_bf16 v[76:79], v[52:55], v[188:191], v[76:79]\nds_read_b128 v[52:55], v35 offset:16384\ns_add_u32 s53, s49, 512\n"
"s_waitcnt lgkmcnt(3)\nv_mfma_f32_16x16x32_bf16 v[64:67], v[40:43], v[192:195], v[64:67]\nbuffer_load_dwordx4 v10, s[12:15], s53 offen lds\nv_mfma_f32_16x16x32_bf16 v[68:71], v[40:43], v[196:199], v[68:71]\n"
"ds_read_b128 v[40:43], v32 offset:20480\ns_add_u32 m0, s52, 20480\ns_waitcnt lgkmcnt(3)\nv_mfma_f32_16x16x32_bf16 v[72:75], v[44:47], v[192:195], v[72:75]\n"
"s_add_u32 s53, s49, 640\nv_mfma_f32_16x16x32_bf16 v[76:79], v[44:47], v[196:199], v[76:79]\nds_read_b128 v[44:47], v33 offset:20480\nbuffer_load_dwordx4 v10, s[12:15], s53 offen lds\n"
"s_waitcnt lgkmcnt(3)\nv_mfma_f32_16x16x32_bf16 v[64:67], v[48:51], v[200:203], v[64:67]\ns_add_u32 m0, s52, 24576\nv_mfma_f32_16x16x32_bf16 v[68:71], v[48:51], v[204:207], v[68:71]\n"
"ds_read_b128 v[48:51], v34 offset:20480\ns_add_u32 s53, s49, 768\ns_waitcnt lgkmcnt(3)\nv_mfma_f32_16x16x32_bf16 v[72:75], v[52:55], v[200:203], v[72:75]\n"
"buffer_load_dwordx4 v10, s[12:15], s53 offen lds\nv_mfma_f32_16x16x32_bf16 v[76:79], v[52:55], v[204:207], v[76:79]\nds_read_b128 v[52:55], v35 offset:20480\ns_waitcnt lgkmcnt(3)\n"
"v_mfma_f32_16x16x32_bf16 v[64:67], v[40:43], v[208:211], v[64:67]\nv_mfma_f32_16x16x32_bf16 v[68:71], v[40:43], v[212:215], v[68:71]\nds_read_b128 v[40:43], v32 offset:24576\ns_waitcnt lgkmcnt(3)\n"
"v_mfma_f32_16x16x32_bf16 v[72:75], v[44:47], v[208:211], v[72:75]\nv_mfma_f32_16x16x32_bf16 v[76:79], v[44:47], v[212:215], v[76:79]\nds_read_b128 v[44:47], v33 offset:24576\ns_waitcnt lgkmcnt(3)\n"
"v_mfma_f32_16x16x32_bf16 v[64:67], v[48:51], v[216:219], v[64:67]\ns_add_u32 s54, s51, 32768\ns_cmp_eq_u32 s54, 163840\nv_mfma_f32_16x16x32_bf16 v[68:71], v[48:51], v[220:223], v[68:71]\n"
"s_cselect_b32 s54, 0, s54\ns_sub_u32 s54, s54, s51\nds_read_b128 v[48:51], v34 offset:24576\ns_waitcnt lgkmcnt(3)\n"
"v_mfma_f32_16x16x32_bf16 v[72:75], v[52:55], v[216:219], v[72:75]\nv_mfma_f32_16x16x32_bf16 v[76:79], v[52:55], v[220:223], v[76:79]\nds_read_b128 v[52:55], v35 offset:24576\ns_waitcnt lgkmcnt(3)\n"
"v_mfma_f32_16x16x32_bf16 v[64:67], v[40:43], v[224:227], v[64:67]\nv_add_u32 v32, s54, v32\nv_mfma_f32_16x16x32_bf16 v[68:71], v[40:43], v[228:231], v[68:71]\nds_read_b128 v[40:43], v32 offset:0\n"
"s_waitcnt lgkmcnt(3)\nv_mfma_f32_16x16x32_bf16 v[72:75], v[44:47], v[224:227], v[72:75]\nv_add_u32 v33, s54, v33\nv_mfma_f32_16x16x32_bf16 v[76:79], v[44:47], v[228:231], v[76:79]\n"
"ds_read_b128 v[44:47], v33 offset:0\ns_waitcnt lgkmcnt(3)\nv_mfma_f32_16x16x32_bf16 v[64:67], v[48:51], v[232:235], v[64:67]\nv_add_u32 v34, s54, v34\n"
"v_mfma_f32_16x16x32_bf16 v[68:71], v[48:51], v[236:239], v[68:71]\nds_read_b128 v[48:51], v34 offset:0\ns_waitcnt lgkmcnt(3)\nv_mfma_f32_16x16x32_bf16 v[72:75], v[52:55], v[232:235], v[72:75]\n"
"v_add_u32 v35, s54, v35\nv_mfma_f32_16x16x32_bf16 v[76:79], v[52:55], v[236:239], v[76:79]\nds_read_b128 v[52:55], v35 offset:0\ns_mov_b32 s52, s47\n"
"s_add_u32 s47, s51, s32\ns_add_u32 s51, s51, s54\ns_add_u32 s49, s49, 28672\ns_waitcnt vmcnt(7)\n"
"s_barrier\ns_waitcnt lgkmcnt(3)\nv_mfma_f32_16x16x32_bf16 v[96:99], v[40:43], v[128:131], 0\nv_mov_b32 v36, s48\n"
"s_add_u32 s48, s48, 1\ns_nop 0\nbuffer_store_dword v36, v19, s[24:27], s33 offen sc1\nv_mfma_f32_16x16x32_bf16 v[100:103], v[40:43], v[132:135], 0\n"
"ds_read_b128 v[40:43], v32 offset:4096\ns_waitcnt lgkmcnt(3)\nv_mfma_f32_16x16x32_bf16 v[104:107], v[44:47], v[128:131], 0\nv_mfma_f32_16x16x32_bf16 v[108:111], v[44:47], v[132:135], 0\n"
"ds_read_b128 v[44:47], v33 offset:4096\nv_cvt_pk_bf16_f32 v56, v64, v65\ns_waitcnt lgkmcnt(3)\nv_mfma_f32_16x16x32_bf16 v[96:99], v[48:51], v[136:139], v[96:99]\n"
"v_cvt_pk_bf16_f32 v57, v66, v67\nv_mfma_f32_16x16x32_bf16 v[100:103], v[48:51], v[140:143], v[100:103]\nds_read_b128 v[48:51], v34 offset:4096\nv_cvt_pk_bf16_f32 v58, v72, v73\n"
"s_waitcnt lgkmcnt(3)\nv_mfma_f32_16x16x32_bf16 v[104:107], v[52:55], v[136:139], v[104:107]\nv_cvt_pk_bf16_f32 v59, v74, v75\nv_mfma_f32_16x16x32_bf16 v[108:111], v[52:55], v[140:143], v[108:111]\n"
"ds_read_b128 v[52:55], v35 offset:4096\ns_waitcnt lgkmcnt(3)\nv_mfma_f32_16x16x32_bf16 v[96:99], v[40:43], v[144:147], v[96:99]\nv_mfma_f32_16x16x32_bf16 v[100:103], v[40:43], v[148:151], v[100:103]\n"
"ds_read_b128 v[40:43], v32 offset:8192\nbuffer_store_dwordx4 v[56:59], v15, s[20:23], s50 offen sc1\ns_waitcnt lgkmcnt(3)\nv_mfma_f32_16x16x32_bf16 v[104:107], v[44:47], v[144:147], v[104:107]\n"
"v_cvt_pk_bf16_f32 v60, v68, v69\nv_mfma_f32_16x16x32_bf16 v[108:111], v[44:47], v[148:151], v[108:111]\nds_read_b128 v[44:47], v33 offset:8192\nv_cvt_pk_bf16_f32 v61, v70, v71\n"
"s_waitcnt lgkmcnt(3)\nv_mfma_f32_16x16x32_bf16 v[96:99], v[48:51], v[152:155], v[96:99]\nv_cvt_pk_bf16_f32 v62, v76, v77\nv_mfma_f32_16x16x32_bf16 v[100:103], v[48:51], v[156:159], v[100:103]\n"
"ds_read_b128 v[48:51], v34 offset:8192\nv_cvt_pk_bf16_f32 v63, v78, v79\ns_waitcnt lgkmcnt(3)\nv_mfma_f32_16x16x32_bf16 v[104:107], v[52:55], v[152:155], v[104:107]\n"
"v_mfma_f32_16x16x32_bf16 v[108:111], v[52:55], v[156:159], v[108:111]\nds_read_b128 v[52:55], v35 offset:8192\ns_waitcnt lgkmcnt(3)\nv_mfma_f32_16x16x32_bf16 v[96:99], v[40:43], v[160:163], v[96:99]\n"
"buffer_store_dwordx4 v[60:63], v16, s[20:23], s50 offen sc1\nv_mfma_f32_16x16x32_bf16 v[100:103], v[40:43], v[164:167], v[100:103]\nds_read_b128 v[40:43], v32 offset:12288\ns_add_u32 s50, s50, s36\n"
"s_waitcnt lgkmcnt(3)\nv_mfma_f32_16x16x32_bf16 v[104:107], v[44:47], v[160:163], v[104:107]\ns_add_u32 m0, s52, 0\nv_mfma_f32_16x16x32_bf16 v[108:111], v[44:47], v[164:167], v[108:111]\n"
"ds_read_b128 v[44:47], v33 offset:12288\ns_add_u32 s53, s49, 0\ns_waitcnt lgkmcnt(3)\nv_mfma_f32_16x16x32_bf16 v[96:99], v[48:51], v[168:171], v[96:99]\n"
"buffer_load_dwordx4 v10, s[12:15], s53 offen lds\nv_mfma_f32_16x16x32_bf16 v[100:103], v[48:51], v[172:175], v[100:103]\nds_read_b128 v[48:51], v34 offset:12288\ns_add_u32 m0, s52, 4096\n"
"s_waitcnt lgkmcnt(3)\nv_mfma_f32_16x16x32_bf16 v[104:107], v[52:55], v[168:171], v[104:107]\ns_add_u32 s53, s49, 128\nv_mfma_f32_16x16x32_bf16 v[108:111], v[52:55], v[172:175], v[108:111]\n"
"ds_read_b128 v[52:55], v35 offset:12288\nbuffer_load_dwordx4 v10, s[12:15], s53 offen lds\ns_waitcnt lgkmcnt(3)\nv_mfma_f32_16x16x32_bf16 v[96:99], v[40:43], v[176:179], v[96:99]\n"
"s_add_u32 m0, s52, 8192\nv_mfma_f32_16x16x32_bf16 v[100:103], v[40:43], v[180:183], v[100:103]\nds_read_b128 v[40:43], v32 offset:16384\ns_add_u32 s53, s49, 256\n"
"s_waitcnt lgkmcnt(3)\nv_mfma_f32_16x16x32_bf16 v[104:107], v[44:47], v[176:179], v[104:107]\nbuffer_load_dwordx4 v10, s[12:15], s53 offen lds\nv_mfma_f32_16x16x32_bf16 v[108:111], v[44:47], v[180:183], v[108:111]\n"
"ds_read_b128 v[44:47], v33 offset:16384\ns_add_u32 m0, s52, 12288\ns_waitcnt lgkmcnt(3)\nv_mfma_f32_16x16x32_bf16 v[96:99], v[48:51], v[184:187], v[96:99]\n"
"s_add_u32 s53, s49, 384\nv_mfma_f32_16x16x32_bf16 v[100:103], v[48:51], v[188:191], v[100:103]\nds_read_b128 v[48:51], v34 offset:16384\nbuffer_load_dwordx4 v10, s[12:15], s53 offen lds\n"
"s_waitcnt lgkmcnt(3)\nv_mfma_f32_16x16x32_bf16 v[104:107], v[52:55], v[184:187], v[104:107]\ns_add_u32 m0, s52, 16384\nv_mfma_f32_16x16x32_bf16 v[108:111], v[52:55], v[188:191], v[108:111]\n"
"ds_read_b128 v[52:55], v35 offset:16384\ns_add_u32 s53, s49, 512\ns_waitcnt lgkmcnt(3)\nv_mfma_f32_16x16x32_bf16 v[96:99], v[40:43], v[192:195], v[96:99]\n"
"buffer_load_dwordx4 v10, s[12:15], s53 offen lds\nv_mfma_f32_16x16x32_bf16 v[100:103], v[40:43], v[196:199], v[100:103]\nds_read_b128 v[40:43], v32 offset:20480\ns_add_u32 m0, s52, 20480\n"
"s_waitcnt lgkmcnt(3)\nv_mfma_f32_16x16x32_bf16 v[104:107], v[44:47], v[192:195], v[104:107]\ns_add_u32 s53, s49, 640\nv_mfma_f32_16x16x32_bf16 v[108:111], v[44:47], v[196:199], v[108:111]\n"
"ds_read_b128 v[44:47], v33 offset:20480\nbuffer_load_dwordx4 v10, s[12:15], s53 offen lds\ns_waitcnt lgkmcnt(3)\nv_mfma_f32_16x16x32_bf16 v[96:99], v[48:51], v[200:203], v[96:99]\n"
"s_add_u32 m0, s52, 24576\nv_mfma_f32_16x16x32_bf16 v[100:103], v[48:51], v[204:207], v[100:103]\nds_read_b128 v[48:51], v34 offset:20480\ns_add_u32 s53, s49, 768\n"
"s_waitcnt lgkmcnt(3)\nv_mfma_f32_16x16x32_bf16 v[104:107], v[52:55], v[200:203], v[104:107]\nbuffer_load_dwordx4 v10, s[12:15], s53 offen lds\nv_mfma_f32_16x16x32_bf16 v[108:111], v[52:55], v[204:207], v[108:111]\n"
"ds_read_b128 v[52:55], v35 offset:20480\ns_waitcnt lgkmcnt(3)\nv_mfma_f32_16x16x32_bf16 v[96:99], v[40:43], v[208:211], v[96:99]\nv_mfma_f32_16x16x32_bf16 v[100:103], v[40:43], v[212:215], v[100:103]\n"
"ds_read_b128 v[40:43], v32 offset:24576\ns_waitcnt lgkmcnt(3)\nv_mfma_f32_16x16x32_bf16 v[104:107], v[44:47], v[208:211], v[104:107]\nv_mfma_f32_16x16x32_bf16 v[108:111], v[44:47], v[212:215], v[108:111]\n"
"ds_read_b128 v[44:47], v33 offset:24576\ns_waitcnt lgkmcnt(3)\nv_mfma_f32_16x16x32_bf16 v[96:99], v[48:51], v[216:219], v[96:99]\ns_add_u32 s54, s51, 32768\n"
"s_cmp_eq_u32 s54, 163840\nv_mfma_f32_16x16x32_bf16 v[100:103], v[48:51], v[220:223], v[100:103]\ns_cselect_b32 s54, 0, s54\ns_sub_u32 s54, s54, s51\n"
"ds_read_b128 v[48:51], v34 offset:24576\ns_waitcnt lgkmcnt(3)\nv_mfma_f32_16x16x32_bf16 v[104:107], v[52:55], v[216:219], v[104:107]\nv_mfma_f32_16x16x32_bf16 v[108:111], v[52:55], v[220:223], v[108:111]\n"
"ds_read_b128 v[52:55], v35 offset:24576\ns_waitcnt lgkmcnt(3)\nv_mfma_f32_16x16x32_bf16 v[96:99], v[40:43], v[224:227], v[96:99]\nv_add_u32 v32, s54, v32\n"
"v_mfma_f32_16x16x32_bf16 v[100:103], v[40:43], v[228:231], v[100:103]\nds_read_b128 v[40:43], v32 offset:0\ns_waitcnt lgkmcnt(3)\nv_mfma_f32_16x16x32_bf16 v[104:107], v[44:47], v[224:227], v[104:107]\n"
"v_add_u32 v33, s54, v33\nv_mfma_f32_16x16x32_bf16 v[108:111], v[44:47], v[228:231], v[108:111]\nds_read_b128 v[44:47], v33 offset:0\ns_waitcnt lgkmcnt(3)\n"
"v_mfma_f32_16x16x32_bf16 v[96:99], v[48:51], v[232:235], v[96:99]\nv_add_u32 v34, s54, v34\nv_mfma_f32_16x16x32_bf16 v[100:103], v[48:51], v[236:239], v[100:103]\nds_read_b128 v[48:51], v34 offset:0\n"
"s_waitcnt lgkmcnt(3)\nv_mfma_f32_16x16x32_bf16 v[104:107], v[52:55], v[232:235], v[104:107]\nv_add_u32 v35, s54, v35\nv_mfma_f32_16x16x32_bf16 v[108:111], v[52:55], v[236:239], v[108:111]\n"
"ds_read_b128 v[52:55], v35 offset:0\ns_mov_b32 s52, s47\ns_add_u32 s47, s51, s32\ns_add_u32 s51, s51, s54\n"
"s_add_u32 s49, s49, 28672\ns_waitcnt vmcnt(7)\ns_barrier\ns_waitcnt lgkmcnt(3)\n"
"v_mfma_f32_16x16x32_bf16 v[64:67], v[40:43], v[128:131], 0\nv_mov_b32 v36, s48\ns_add_u32 s48, s48, 1\ns_nop 0\n"
"buffer_store_dword v36, v19, s[24:27], s33 offen sc1\nv_mfma_f32_16x16x32_bf16 v[68:71], v[40:43], v[132:135], 0\nds_read_b128 v[40:43], v32 offset:4096\ns_waitcnt lgkmcnt(3)\n"
"v_mfma_f32_16x16x32_bf16 v[72:75], v[44:47], v[128:131], 0\nv_mfma_f32_16x16x32_bf16 v[76:79], v[44:47], v[132:135], 0\nds_read_b128 v[44:47], v33 offset:4096\nv_cvt_pk_bf16_f32 v56, v96, v97\n"
"s_waitcnt lgkmcnt(3)\nv_mfma_f32_16x16x32_bf16 v[64:67], v[48:51], v[136:139], v[64:67]\nv_cvt_pk_bf16_f32 v57, v98, v99\nv_mfma_f32_16x16x32_bf16 v[68:71], v[48:51], v[140:143], v[68:71]\n"
"ds_read_b128 v[48:51], v34 offset:4096\nv_cvt_pk_bf16_f32 v58, v104, v105\ns_waitcnt lgkmcnt(3)\nv_mfma_f32_16x16x32_bf16 v[72:75], v[52:55], v[136:139], v[72:75]\n"
"v_cvt_pk_bf16_f32 v59, v106, v107\nv_mfma_f32_16x16x32_bf16 v[76:79], v[52:55], v[140:143], v[76:79]\nds_read_b128 v[52:55], v35 offset:4096\ns_waitcnt lgkmcnt(3)\n"
"v_mfma_f32_16x16x32_bf16 v[64:67], v[40:43], v[144:147], v[64:67]\nv_mfma_f32_16x16x32_bf16 v[68:71], v[40:43], v[148:151], v[68:71]\nds_read_b128 v[40:43], v32 offset:8192\nbuffer_store_dwordx4 v[56:59], v15, s[20:23], s50 offen sc1\n"
"s_waitcnt lgkmcnt(3)\nv_mfma_f32_16x16x32_bf16 v[72:75], v[44:47], v[144:147], v[72:75]\nv_cvt_pk_bf16_f32 v60, v100, v101\nv_mfma_f32_16x16x32_bf16 v[76:79], v[44:47], v[148:151], v[76:79]\n"
"ds_read_b128 v[44:47], v33 offset:8192\nv_cvt_pk_bf16_f32 v61, v102, v103\ns_waitcnt lgkmcnt(3)\nv_mfma_f32_16x16x32_bf16 v[64:67], v[48:51], v[152:155], v[64:67]\n"
"v_cvt_pk_bf16_f32 v62, v108, v109\nv_mfma_f32_16x16x32_bf16 v[68:71], v[48:51], v[156:159], v[68:71]\nds_read_b128 v[48:51], v34 offset:8192\nv_cvt_pk_bf16_f32 v63, v110, v111\n"
"s_waitcnt lgkmcnt(3)\nv_mfma_f32_16x16x32_bf16 v[72:75], v[52:55], v[152:155], v[72:75]\nv_mfma_f32_16x16x32_bf16 v[76:79], v[52:55], v[156:159], v[76:79]\nds_read_b128 v[52:55], v35 offset:8192\n"
"s_waitcnt lgkmcnt(3)\nv_mfma_f32_16x16x32_bf16 v[64:67], v[40:43], v[160:163], v[64:67]\nbuffer_store_dwordx4 v[60:63], v16, s[20:23], s50 offen sc1\nv_mfma_f32_16x16x32_bf16 v[68:71], v[40:43], v[164:167], v[68:71]\n"
"ds_read_b128 v[40:43], v32 offset:12288\ns_add_u32 s50, s50, s36\ns_waitcnt lgkmcnt(3)\nv_mfma_f32_16x16x32_bf16 v[72:75], v[44:47], v[160:163], v[72:75]\n"
"s_add_u32 m0, s52, 0\nv_mfma_f32_16x16x32_bf16 v[76:79], v[44:47], v[164:167], v[76:79]\nds_read_b128 v[44:47], v33 offset:12288\ns_add_u32 s53, s49, 0\n"
"s_waitcnt lgkmcnt(3)\nv_mfma_f32_16x16x32_bf16 v[64:67], v[48:51], v[168:171], v[64:67]\nbuffer_load_dwordx4 v10, s[12:15], s53 offen lds\nv_mfma_f32_16x16x32_bf16 v[68:71], v[48:51], v[172:175], v[68:71]\n"
"ds_read_b128 v[48:51], v34 offset:12288\ns_add_u32 m0, s52, 4096\ns_waitcnt lgkmcnt(3)\nv_mfma_f32_16x16x32_bf16 v[72:75], v[52:55], v[168:171], v[72:75]\n"
"s_add_u32 s53, s49, 128\nv_mfma_f32_16x16x32_bf16 v[76:79], v[52:55], v[172:175], v[76:79]\nds_read_b128 v[52:55], v35 offset:12288\nbuffer_load_dwordx4 v10, s[12:15], s53 offen lds\n"
"s_waitcnt lgkmcnt(3)\nv_mfma_f32_16x16x32_bf16 v[64:67], v[40:43], v[176:179], v[64:67]\ns_add_u32 m0, s52, 8192\nv_mfma_f32_16x16x32_bf16 v[68:71], v[40:43], v[180:183], v[68:71]\n"
"ds_read_b128 v[40:43], v32 offset:16384\ns_add_u32 s53, s49, 256\ns_waitcnt lgkmcnt(3)\nv_mfma_f32_16x16x32_bf16 v[72:75], v[44:47], v[176:179], v[72:75]\n"
"buffer_load_dwordx4 v10, s[12:15], s53 offen lds\nv_mfma_f32_16x16x32_bf16 v[76:79], v[44:47], v[180:183], v[76:79]\nds_read_b128 v[44:47], v33 offset:16384\ns_add_u32 m0, s52, 12288\n"
"s_waitcnt lgkmcnt(3)\nv_mfma_f32_16x16x32_bf16 v[64:67], v[48:51], v[184:187], v[64:67]\ns_add_u32 s53, s49, 384\nv_mfma_f32_16x16x32_bf16 v[68:71], v[48:51], v[188:191], v[68:71]\n"
"ds_read_b128 v[48:51], v34 offset:16384\nbuffer_load_dwordx4 v10, s[12:15], s53 offen lds\ns_waitcnt lgkmcnt(3)\nv_mfma_f32_16x16x32_bf16 v[72:75], v[52:55], v[184:187], v[72:75]\n"
"s_add_u32 m0, s52, 16384\nv_mfma_f32_16x16x32_bf16 v[76:79], v[52:55], v[188:191], v[76:79]\nds_read_b128 v[52:55], v35 offset:16384\ns_add_u32 s53, s49, 512\n"
"s_waitcnt lgkmcnt(3)\nv_mfma_f32_16x16x32_bf16 v[64:67], v[40:43], v[192:195], v[64:67]\nbuffer_load_dwordx4 v10, s[12:15], s53 offen lds\nv_mfma_f32_16x16x32_bf16 v[68:71], v[40:43], v[196:199], v[68:71]\n"
"ds_read_b128 v[40:43], v32 offset:20480\ns_add_u32 m0, s52, 20480\ns_waitcnt lgkmcnt(3)\nv_mfma_f32_16x16x32_bf16 v[72:75], v[44:47], v[192:195], v[72:75]\n"
"s_add_u32 s53, s49, 640\nv_mfma_f32_16x16x32_bf16 v[76:79], v[44:47], v[196:199], v[76:79]\nds_read_b128 v[44:47], v33 offset:20480\nbuffer_load_dwordx4 v10, s[12:15], s53 offen lds\n"
"s_waitcnt lgkmcnt(3)\nv_mfma_f32_16x16x32_bf16 v[64:67], v[48:51], v[200:203], v[64:67]\ns_add_u32 m0, s52, 24576\nv_mfma_f32_16x16x32_bf16 v[68:71], v[48:51], v[204:207], v[68:71]\n"
"ds_read_b128 v[48:51], v34 offset:20480\ns_add_u32 s53, s49, 768\ns_waitcnt lgkmcnt(3)\nv_mfma_f32_16x16x32_bf16 v[72:75], v[52:55], v[200:203], v[72:75]\n"
"buffer_load_dwordx4 v10, s[12:15], s53 offen lds\nv_mfma_f32_16x16x32_bf16 v[76:79], v[52:55], v[204:207], v[76:79]\nds_read_b128 v[52:55], v35 offset:20480\ns_waitcnt lgkmcnt(3)\n"
"v_mfma_f32_16x16x32_bf16 v[64:67], v[40:43], v[208:211], v[64:67]\nv_mfma_f32_16x16x32_bf16 v[68:71], v[40:43], v[212:215], v[68:71]\nds_read_b128 v[40:43], v32 offset:24576\ns_waitcnt lgkmcnt(3)\n"
"v_mfma_f32_16x16x32_bf16 v[72:75], v[44:47], v[208:211], v[72:75]\nv_mfma_f32_16x16x32_bf16 v[76:79], v[44:47], v[212:215], v[76:79]\nds_read_b128 v[44:47], v33 offset:24576\ns_waitcnt lgkmcnt(3)\n"
"v_mfma_f32_16x16x32_bf16 v[64:67], v[48:51], v[216:219], v[64:67]\ns_add_u32 s54, s51, 32768\ns_cmp_eq_u32 s54, 163840\nv_mfma_f32_16x16x32_bf16 v[68:71], v[48:51], v[220:223], v[68:71]\n"
"s_cselect_b32 s54, 0, s54\ns_sub_u32 s54, s54, s51\nds_read_b128 v[48:51], v34 offset:24576\ns_waitcnt lgkmcnt(3)\n"
"v_mfma_f32_16x16x32_bf16 v[72:75], v[52:55], v[216:219], v[72:75]\nv_mfma_f32_16x16x32_bf16 v[76:79], v[52:55], v[220:223], v[76:79]\nds_read_b128 v[52:55], v35 offset:24576\ns_waitcnt lgkmcnt(3)\n"
"v_mfma_f32_16x16x32_bf16 v[64:67], v[40:43], v[224:227], v[64:67]\nv_add_u32 v32, s54, v32\nv_mfma_f32_16x16x32_bf16 v[68:71], v[40:43], v[228:231], v[68:71]\nds_read_b128 v[40:43], v32 offset:0\n"
"s_waitcnt lgkmcnt(3)\nv_mfma_f32_16x16x32_bf16 v[72:75], v[44:47], v[224:227], v[72:75]\nv_add_u32 v33, s54, v33\nv_mfma_f32_16x16x32_bf16 v[76:79], v[44:47], v[228:231], v[76:79]\n"
"ds_read_b128 v[44:47], v33 offset:0\ns_waitcnt lgkmcnt(3)\nv_mfma_f32_16x16x32_bf16 v[64:67], v[48:51], v[232:235], v[64:67]\nv_add_u32 v34, s54, v34\n"
"v_mfma_f32_16x16x32_bf16 v[68:71], v[48:51], v[236:239], v[68:71]\nds_read_b128 v[48:51], v34 offset:0\ns_waitcnt lgkmcnt(3)\nv_mfma_f32_16x16x32_bf16 v[72:75], v[52:55], v[232:235], v[72:75]\n"
"v_add_u32 v35, s54, v35\nv_mfma_f32_16x16x32_bf16 v[76:79], v[52:55], v[236:239], v[76:79]\nds_read_b128 v[52:55], v35 offset:0\ns_mov_b32 s52, s47\n"
"s_add_u32 s47, s51, s32\ns_add_u32 s51, s51, s54\ns_add_u32 s49, s49, 28672\ns_waitcnt vmcnt(7)\n"
"s_barrier\ns_waitcnt lgkmcnt(3)\nv_mfma_f32_16x16x32_bf16 v[96:99], v[40:43], v[128:131], 0\nv_mov_b32 v36, s48\n"
"s_add_u32 s48, s48, 1\ns_nop 0\nbuffer_store_dword v36, v19, s[24:27], s33 offen sc1\nv_mfma_f32_16x16x32_bf16 v[100:103], v[40:43], v[132:135], 0\n"
"ds_read_b128 v[40:43], v32 offset:4096\ns_waitcnt lgkmcnt(3)\nv_mfma_f32_16x16x32_bf16 v[104:107], v[44:47], v[128:131], 0\nv_mfma_f32_16x16x32_bf16 v[108:111], v[44:47], v[132:135], 0\n"
"ds_read_b128 v[44:47], v33 offset:4096\nv_cvt_pk_bf16_f32 v56, v64, v65\ns_waitcnt lgkmcnt(3)\nv_mfma_f32_16x16x32_bf16 v[96:99], v[48:51], v[136:139], v[96:99]\n"
"v_cvt_pk_bf16_f32 v57, v66, v67\nv_mfma_f32_16x16x32_bf16 v[100:103], v[48:51], v[140:143], v[100:103]\nds_read_b128 v[48:51], v34 offset:4096\nv_cvt_pk_bf16_f32 v58, v72, v73\n"
"s_waitcnt lgkmcnt(3)\nv_mfma_f32_16x16x32_bf16 v[104:107], v[52:55], v[136:139], v[104:107]\nv_cvt_pk_bf16_f32 v59, v74, v75\nv_mfma_f32_16x16x32_bf16 v[108:111], v[52:55], v[140:143], v[108:111]\n"
"ds_read_b128 v[52:55], v35 offset:4096\ns_waitcnt lgkmcnt(3)\nv_mfma_f32_16x16x32_bf16 v[96:99], v[40:43], v[144:147], v[96:99]\nv_mfma_f32_16x16x32_bf16 v[100:103], v[40:43], v[148:151], v[100:103]\n"
"ds_read_b128 v[40:43], v32 offset:8192\nbuffer_store_dwordx4 v[56:59], v15, s[20:23], s50 offen sc1\ns_waitcnt lgkmcnt(3)\nv_mfma_f32_16x16x32_bf16 v[104:107], v[44:47], v[144:147], v[104:107]\n"
"v_cvt_pk_bf16_f32 v60, v68, v69\nv_mfma_f32_16x16x32_bf16 v[108:111], v[44:47], v[148:151], v[108:111]\nds_read_b128 v[44:47], v33 offset:8192\nv_cvt_pk_bf16_f32 v61, v70, v71\n"
"s_waitcnt lgkmcnt(3)\nv_mfma_f32_16x16x32_bf16 v[96:99], v[48:51], v[152:155], v[96:99]\nv_cvt_pk_bf16_f32 v62, v76, v77\nv_mfma_f32_16x16x32_bf16 v[100:103], v[48:51], v[156:159], v[100:103]\n"
"ds_read_b128 v[48:51], v34 offset:8192\nv_cvt_pk_bf16_f32 v63, v78, v79\ns_waitcnt lgkmcnt(3)\nv_mfma_f32_16x16x32_bf16 v[104:107], v[52:55], v[152:155], v[104:107]\n"
"v_mfma_f32_16x16x32_bf16 v[108:111], v[52:55], v[156:159], v[108:111]\nds_read_b128 v[52:55], v35 offset:8192\ns_waitcnt lgkmcnt(3)\nv_mfma_f32_16x16x32_bf16 v[96:99], v[40:43], v[160:163], v[96:99]\n"
"buffer_store_dwordx4 v[60:63], v16, s[20:23], s50 offen sc1\nv_mfma_f32_16x16x32_bf16 v[100:103], v[40:43], v[164:167], v[100:103]\nds_read_b128 v[40:43], v32 offset:12288\ns_add_u32 s50, s50, s36\n"
"s_waitcnt lgkmcnt(3)\nv_mfma_f32_16x16x32_bf16 v[104:107], v[44:47], v[160:163], v[104:107]\ns_add_u32 m0, s52, 0\nv_mfma_f32_16x16x32_bf16 v[108:111], v[44:47], v[164:167], v[108:111]\n"
"ds_read_b128 v[44:47], v33 offset:12288\ns_add_u32 s53, s49, 0\ns_waitcnt lgkmcnt(3)\nv_mfma_f32_16x16x32_bf16 v[96:99], v[48:51], v[168:171], v[96:99]\n"
"buffer_load_dwordx4 v10, s[12:15], s53 offen lds\nv_mfma_f32_16x16x32_bf16 v[100:103], v[48:51], v[172:175], v[100:103]\nds_read_b128 v[48:51], v34 offset:12288\ns_add_u32 m0, s52, 4096\n"
"s_waitcnt lgkmcnt(3)\nv_mfma_f32_16x16x32_bf16 v[104:107], v[52:55], v[168:171], v[104:107]\ns_add_u32 s53, s49, 128\nv_mfma_f32_16x16x32_bf16 v[108:111], v[52:55], v[172:175], v[108:111]\n"
"ds_read_b128 v[52:55], v35 offset:12288\nbuffer_load_dwordx4 v10, s[12:15], s53 offen lds\ns_waitcnt lgkmcnt(3)\nv_mfma_f32_16x16x32_bf16 v[96:99], v[40:43], v[176:179], v[96:99]\n"
"s_add_u32 m0, s52, 8192\nv_mfma_f32_16x16x32_bf16 v[100:103], v[40:43], v[180:183], v[100:103]\nds_read_b128 v[40:43], v32 offset:16384\ns_add_u32 s53, s49, 256\n"
"s_waitcnt lgkmcnt(3)\nv_mfma_f32_16x16x32_bf16 v[104:107], v[44:47], v[176:179], v[104:107]\nbuffer_load_dwordx4 v10, s[12:15], s53 offen lds\nv_mfma_f32_16x16x32_bf16 v[108:111], v[44:47], v[180:183], v[108:111]\n"
"ds_read_b128 v[44:47], v33 offset:16384\ns_add_u32 m0, s52, 12288\ns_waitcnt lgkmcnt(3)\nv_mfma_f32_16x16x32_bf16 v[96:99], v[48:51], v[184:187], v[96:99]\n"
"s_add_u32 s53, s49, 384\nv_mfma_f32_16x16x32_bf16 v[100:103], v[48:51], v[188:191], v[100:103]\nds_read_b128 v[48:51], v34 offset:16384\nbuffer_load_dwordx4 v10, s[12:15], s53 offen lds\n"
"s_waitcnt lgkmcnt(3)\nv_mfma_f32_16x16x32_bf16 v[104:107], v[52:55], v[184:187], v[104:107]\ns_add_u32 m0, s52, 16384\nv_mfma_f32_16x16x32_bf16 v[108:111], v[52:55], v[188:191], v[108:111]\n"
"ds_read_b128 v[52:55], v35 offset:16384\ns_add_u32 s53, s49, 512\ns_waitcnt lgkmcnt(3)\nv_mfma_f32_16x16x32_bf16 v[96:99], v[40:43], v[192:195], v[96:99]\n"
"buffer_load_dwordx4 v10, s[12:15], s53 offen lds\nv_mfma_f32_16x16x32_bf16 v[100:103], v[40:43], v[196:199], v[100:103]\nds_read_b128 v[40:43], v32 offset:20480\ns_add_u32 m0, s52, 20480\n"
"s_waitcnt lgkmcnt(3)\nv_mfma_f32_16x16x32_bf16 v[104:107], v[44:47], v[192:195], v[104:107]\ns_add_u32 s53, s49, 640\nv_mfma_f32_16x16x32_bf16 v[108:111], v[44:47], v[196:199], v[108:111]\n"
"ds_read_b128 v[44:47], v33 offset:20480\nbuffer_load_dwordx4 v10, s[12:15], s53 offen lds\ns_waitcnt lgkmcnt(3)\nv_mfma_f32_16x16x32_bf16 v[96:99], v[48:51], v[200:203], v[96:99]\n"
"s_add_u32 m0, s52, 24576\nv_mfma_f32_16x16x32_bf16 v[100:103], v[48:51], v[204:207], v[100:103]\nds_read_b128 v[48:51], v34 offset:20480\ns_add_u32 s53, s49, 768\n"
"s_waitcnt lgkmcnt(3)\nv_mfma_f32_16x16x32_bf16 v[104:107], v[52:55], v[200:203], v[104:107]\nbuffer_load_dwordx4 v10, s[12:15], s53 offen lds\nv_mfma_f32_16x16x32_bf16 v[108:111], v[52:55], v[204:207], v[108:111]\n"
"ds_read_b128 v[52:55], v35 offset:20480\ns_waitcnt lgkmcnt(3)\nv_mfma_f32_16x16x32_bf16 v[96:99], v[40:43], v[208:211], v[96:99]\nv_mfma_f32_16x16x32_bf16 v[100:103], v[40:43], v[212:215], v[100:103]\n"
"ds_read_b128 v[40:43], v32 offset:24576\ns_waitcnt lgkmcnt(3)\nv_mfma_f32_16x16x32_bf16 v[104:107], v[44:47], v[208:211], v[104:107]\nv_mfma_f32_16x16x32_bf16 v[108:111], v[44:47], v[212:215], v[108:111]\n"
"ds_read_b128 v[44:47], v33 offset:24576\ns_waitcnt lgkmcnt(3)\nv_mfma_f32_16x16x32_bf16 v[96:99], v[48:51], v[216:219], v[96:99]\ns_add_u32 s54, s51, 32768\n"
"s_cmp_eq_u32 s54, 163840\nv_mfma_f32_16x16x32_bf16 v[100:103], v[48:51], v[220:223], v[100:103]\ns_cselect_b32 s54, 0, s54\ns_sub_u32 s54, s54, s51\n"
"ds_read_b128 v[48:51], v34 offset:24576\ns_waitcnt lgkmcnt(3)\nv_mfma_f32_16x16x32_bf16 v[104:107], v[52:55], v[216:219], v[104:107]\nv_mfma_f32_16x16x32_bf16 v[108:111], v[52:55], v[220:223], v[108:111]\n"
"ds_read_b128 v[52:55], v35 offset:24576\ns_waitcnt lgkmcnt(3)\nv_mfma_f32_16x16x32_bf16 v[96:99], v[40:43], v[224:227], v[96:99]\nv_add_u32 v32, s54, v32\n"
"v_mfma_f32_16x16x32_bf16 v[100:103], v[40:43], v[228:231], v[100:103]\nds_read_b128 v[40:43], v32 offset:0\ns_waitcnt lgkmcnt(3)\nv_mfma_f32_16x16x32_bf16 v[104:107], v[44:47], v[224:227], v[104:107]\n"
"v_add_u32 v33, s54, v33\nv_mfma_f32_16x16x32_bf16 v[108:111], v[44:47], v[228:231], v[108:111]\nds_read_b128 v[44:47], v33 offset:0\ns_waitcnt lgkmcnt(3)\n"
"v_mfma_f32_16x16x32_bf16 v[96:99], v[48:51], v[232:235], v[96:99]\nv_add_u32 v34, s54, v34\nv_mfma_f32_16x16x32_bf16 v[100:103], v[48:51], v[236:239], v[100:103]\nds_read_b128 v[48:51], v34 offset:0\n"
"s_waitcnt lgkmcnt(3)\nv_mfma_f32_16x16x32_bf16 v[104:107], v[52:55], v[232:235], v[104:107]\nv_add_u32 v35, s54, v35\nv_mfma_f32_16x16x32_bf16 v[108:111], v[52:55], v[236:239], v[108:111]\n"
"ds_read_b128 v[52:55], v35 offset:0\ns_mov_b32 s52, s47\ns_add_u32 s47, s51, s32\ns_add_u32 s51, s51, s54\n"
"s_add_u32 s49, s49, 28672\ns_waitcnt vmcnt(7)\ns_barrier\ns_waitcnt lgkmcnt(3)\n"
"v_mfma_f32_16x16x32_bf16 v[64:67], v[40:43], v[128:131], 0\nv_mov_b32 v36, s48\ns_add_u32 s48, s48, 1\ns_nop 0\n"
"buffer_store_dword v36, v19, s[24:27], s33 offen sc1\nv_mfma_f32_16x16x32_bf16 v[68:71], v[40:43], v[132:135], 0\nds_read_b128 v[40:43], v32 offset:4096\ns_waitcnt lgkmcnt(3)\n"
"v_mfma_f32_16x16x32_bf16 v[72:75], v[44:47], v[128:131], 0\nv_mfma_f32_16x16x32_bf16 v[76:79], v[44:47], v[132:135], 0\nds_read_b128 v[44:47], v33 offset:4096\nv_cvt_pk_bf16_f32 v56, v96, v97\n"
"s_waitcnt lgkmcnt(3)\nv_mfma_f32_16x16x32_bf16 v[64:67], v[48:51], v[136:139], v[64:67]\nv_cvt_pk_bf16_f32 v57, v98, v99\nv_mfma_f32_16x16x32_bf16 v[68:71], v[48:51], v[140:143], v[68:71]\n"
"ds_read_b128 v[48:51], v34 offset:4096\nv_cvt_pk_bf16_f32 v58, v104, v105\ns_waitcnt lgkmcnt(3)\nv_mfma_f32_16x16x32_bf16 v[72:75], v[52:55], v[136:139], v[72:75]\n"
"v_cvt_pk_bf16_f32 v59, v106, v107\nv_mfma_f32_16x16x32_bf16 v[76:79], v[52:55], v[140:143], v[76:79]\nds_read_b128 v[52:55], v35 offset:4096\ns_waitcnt lgkmcnt(3)\n"
"v_mfma_f32_16x16x32_bf16 v[64:67], v[40:43], v[144:147], v[64:67]\nv_mfma_f32_16x16x32_bf16 v[68:71], v[40:43], v[148:151], v[68:71]\nds_read_b128 v[40:43], v32 offset:8192\nbuffer_store_dwordx4 v[56:59], v15, s[20:23], s50 offen sc1\n"
"s_waitcnt lgkmcnt(3)\nv_mfma_f32_16x16x32_bf16 v[72:75], v[44:47], v[144:147], v[72:75]\nv_cvt_pk_bf16_f32 v60, v100, v101\nv_mfma_f32_16x16x32_bf16 v[76:79], v[44:47], v[148:151], v[76:79]\n"
"ds_read_b128 v[44:47], v33 offset:8192\nv_cvt_pk_bf16_f32 v61, v102, v103\ns_waitcnt lgkmcnt(3)\nv_mfma_f32_16x16x32_bf16 v[64:67], v[48:51], v[152:155], v[64:67]\n"
"v_cvt_pk_bf16_f32 v62, v108, v109\nv_mfma_f32_16x16x32_bf16 v[68:71], v[48:51], v[156:159], v[68:71]\nds_read_b128 v[48:51], v34 offset:8192\nv_cvt_pk_bf16_f32 v63, v110, v111\n"
"s_waitcnt lgkmcnt(3)\nv_mfma_f32_16x16x32_bf16 v[72:75], v[52:55], v[152:155], v[72:75]\nv_mfma_f32_16x16x32_bf16 v[76:79], v[52:55], v[156:159], v[76:79]\nds_read_b128 v[52:55], v35 offset:8192\n"
"s_waitcnt lgkmcnt(3)\nv_mfma_f32_16x16x32_bf16 v[64:67], v[40:43], v[160:163], v[64:67]\nbuffer_store_dwordx4 v[60:63], v16, s[20:23], s50 offen sc1\nv_mfma_f32_16x16x32_bf16 v[68:71], v[40:43], v[164:167], v[68:71]\n"
"ds_read_b128 v[40:43], v32 offset:12288\ns_add_u32 s50, s50, s36\ns_waitcnt lgkmcnt(3)\nv_mfma_f32_16x16x32_bf16 v[72:75], v[44:47], v[160:163], v[72:75]\n"
"s_add_u32 m0, s52, 0\nv_mfma_f32_16x16x32_bf16 v[76:79], v[44:47], v[164:167], v[76:79]\nds_read_b128 v[44:47], v33 offset:12288\ns_add_u32 s53, s49, 0\n"
"s_waitcnt lgkmcnt(3)\nv_mfma_f32_16x16x32_bf16 v[64:67], v[48:51], v[168:171], v[64:67]\nbuffer_load_dwordx4 v10, s[12:15], s53 offen lds\nv_mfma_f32_16x16x32_bf16 v[68:71], v[48:51], v[172:175], v[68:71]\n"
"ds_read_b128 v[48:51], v34 offset:12288\ns_add_u32 m0, s52, 4096\ns_waitcnt lgkmcnt(3)\nv_mfma_f32_16x16x32_bf16 v[72:75], v[52:55], v[168:171], v[72:75]\n"
"s_add_u32 s53, s49, 128\nv_mfma_f32_16x16x32_bf16 v[76:79], v[52:55], v[172:175], v[76:79]\nds_read_b128 v[52:55], v35 offset:12288\nbuffer_load_dwordx4 v10, s[12:15], s53 offen lds\n"
"s_waitcnt lgkmcnt(3)\nv_mfma_f32_16x16x32_bf16 v[64:67], v[40:43], v[176:179], v[64:67]\ns_add_u32 m0, s52, 8192\nv_mfma_f32_16x16x32_bf16 v[68:71], v[40:43], v[180:183], v[68:71]\n"
"ds_read_b128 v[40:43], v32 offset:16384\ns_add_u32 s53, s49, 256\ns_waitcnt lgkmcnt(3)\nv_mfma_f32_16x16x32_bf16 v[72:75], v[44:47], v[176:179], v[72:75]\n"
"buffer_load_dwordx4 v10, s[12:15], s53 offen lds\nv_mfma_f32_16x16x32_bf16 v[76:79], v[44:47], v[180:183], v[76:79]\nds_read_b128 v[44:47], v33 offset:16384\ns_add_u32 m0, s52, 12288\n"
"s_waitcnt lgkmcnt(3)\nv_mfma_f32_16x16x32_bf16 v[64:67], v[48:51], v[184:187], v[64:67]\ns_add_u32 s53, s49, 384\nv_mfma_f32_16x16x32_bf16 v[68:71], v[48:51], v[188:191], v[68:71]\n"
"ds_read_b128 v[48:51], v34 offset:16384\nbuffer_load_dwordx4 v10, s[12:15], s53 offen lds\ns_waitcnt lgkmcnt(3)\nv_mfma_f32_16x16x32_bf16 v[72:75], v[52:55], v[184:187], v[72:75]\n"
"s_add_u32 m0, s52, 16384\nv_mfma_f32_16x16x32_bf16 v[76:79], v[52:55], v[188:191], v[76:79]\nds_read_b128 v[52:55], v35 offset:16384\ns_add_u32 s53, s49, 512\n"
"s_waitcnt lgkmcnt(3)\nv_mfma_f32_16x16x32_bf16 v[64:67], v[40:43], v[192:195], v[64:67]\nbuffer_load_dwordx4 v10, s[12:15], s53 offen lds\nv_mfma_f32_16x16x32_bf16 v[68:71], v[40:43], v[196:199], v[68:71]\n"
"ds_read_b128 v[40:43], v32 offset:20480\ns_add_u32 m0, s52, 20480\ns_waitcnt lgkmcnt(3)\nv_mfma_f32_16x16x32_bf16 v[72:75], v[44:47], v[192:195], v[72:75]\n"
"s_add_u32 s53, s49, 640\nv_mfma_f32_16x16x32_bf16 v[76:79], v[44:47], v[196:199], v[76:79]\nds_read_b128 v[44:47], v33 offset:20480\nbuffer_load_dwordx4 v10, s[12:15], s53 offen lds\n"
"s_waitcnt lgkmcnt(3)\nv_mfma_f32_16x16x32_bf16 v[64:67], v[48:51], v[200:203], v[64:67]\ns_add_u32 m0, s52, 24576\nv_mfma_f32_16x16x32_bf16 v[68:71], v[48:51], v[204:207], v[68:71]\n"
"ds_read_b128 v[48:51], v34 offset:20480\ns_add_u32 s53, s49, 768\ns_waitcnt lgkmcnt(3)\nv_mfma_f32_16x16x32_bf16 v[72:75], v[52:55], v[200:203], v[72:75]\n"
"buffer_load_dwordx4 v10, s[12:15], s53 offen lds\nv_mfma_f32_16x16x32_bf16 v[76:79], v[52:55], v[204:207], v[76:79]\nds_read_b128 v[52:55], v35 offset:20480\nglobal_load_dwordx4 v[240:243], v23, s[38:39] offset:0\n"
"s_waitcnt lgkmcnt(3)\nv_mfma_f32_16x16x32_bf16 v[64:67], v[40:43], v[208:211], v[64:67]\nv_mfma_f32_16x16x32_bf16 v[68:71], v[40:43], v[212:215], v[68:71]\nds_read_b128 v[40:43], v32 offset:24576\n"
"s_waitcnt lgkmcnt(3)\nv_mfma_f32_16x16x32_bf16 v[72:75], v[44:47], v[208:211], v[72:75]\nv_mfma_f32_16x16x32_bf16 v[76:79], v[44:47], v[212:215], v[76:79]\nds_read_b128 v[44:47], v33 offset:24576\n"
"s_waitcnt lgkmcnt(3)\nv_mfma_f32_16x16x32_bf16 v[64:67], v[48:51], v[216:219], v[64:67]\ns_add_u32 s54, s51, 32768\ns_cmp_eq_u32 s54, 163840\n"
"v_mfma_f32_16x16x32_bf16 v[68:71], v[48:51], v[220:223], v[68:71]\ns_cselect_b32 s54, 0, s54\ns_sub_u32 s54, s54, s51\nds_read_b128 v[48:51], v34 offset:24576\n"
"s_waitcnt lgkmcnt(3)\nv_mfma_f32_16x16x32_bf16 v[72:75], v[52:55], v[216:219], v[72:75]\nv_mfma_f32_16x16x32_bf16 v[76:79], v[52:55], v[220:223], v[76:79]\nds_read_b128 v[52:55], v35 offset:24576\n"
"s_waitcnt lgkmcnt(3)\nv_mfma_f32_16x16x32_bf16 v[64:67], v[40:43], v[224:227], v[64:67]\nv_add_u32 v32, s54, v32\nv_mfma_f32_16x16x32_bf16 v[68:71], v[40:43], v[228:231], v[68:71]\n"
"ds_read_b128 v[40:43], v32 offset:0\ns_waitcnt lgkmcnt(3)\nv_mfma_f32_16x16x32_bf16 v[72:75], v[44:47], v[224:227], v[72:75]\nv_add_u32 v33, s54, v33\n"
"v_mfma_f32_16x16x32_bf16 v[76:79], v[44:47], v[228:231], v[76:79]\nds_read_b128 v[44:47], v33 offset:0\ns_waitcnt lgkmcnt(3)\nv_mfma_f32_16x16x32_bf16 v[64:67], v[48:51], v[232:235], v[64:67]\n"
"v_add_u32 v34, s54, v34\nv_mfma_f32_16x16x32_bf16 v[68:71], v[48:51], v[236:239], v[68:71]\nds_read_b128 v[48:51], v34 offset:0\ns_waitcnt lgkmcnt(3)\n"
"v_mfma_f32_16x16x32_bf16 v[72:75], v[52:55], v[232:235], v[72:75]\nv_add_u32 v35, s54, v35\nv_mfma_f32_16x16x32_bf16 v[76:79], v[52:55], v[236:239], v[76:79]\nds_read_b128 v[52:55], v35 offset:0\n"
"s_mov_b32 s52, s47\ns_add_u32 s47, s51, s32\ns_add_u32 s51, s51, s54\ns_add_u32 s49, s49, 28672\n"
"s_waitcnt vmcnt(8)\ns_barrier\ns_waitcnt lgkmcnt(3)\nv_mfma_f32_16x16x32_bf16 v[96:99], v[40:43], v[128:131], 0\n"
"v_mov_b32 v36, s48\ns_add_u32 s48, s48, 1\ns_nop 0\nbuffer_store_dword v36, v19, s[24:27], s33 offen sc1\n"
"buffer_load_dword v37, v20, s[24:27], 0 offen sc1\nv_mfma_f32_16x16x32_bf16 v[100:103], v[40:43], v[132:135], 0\nds_read_b128 v[40:43], v32 offset:4096\ns_waitcnt lgkmcnt(3)\n"
"v_mfma_f32_16x16x32_bf16 v[104:107], v[44:47], v[128:131], 0\nv_mfma_f32_16x16x32_bf16 v[108:111], v[44:47], v[132:135], 0\nds_read_b128 v[44:47], v33 offset:4096\nv_cvt_pk_bf16_f32 v56, v64, v65\n"
"s_waitcnt lgkmcnt(3)\nv_mfma_f32_16x16x32_bf16 v[96:99], v[48:51], v[136:139], v[96:99]\nv_cvt_pk_bf16_f32 v57, v66, v67\nv_mfma_f32_16x16x32_bf16 v[100:103], v[48:51], v[140:143], v[100:103]\n"
"ds_read_b128 v[48:51], v34 offset:4096\nv_cvt_pk_bf16_f32 v58, v72, v73\ns_waitcnt lgkmcnt(3)\nv_mfma_f32_16x16x32_bf16 v[104:107], v[52:55], v[136:139], v[104:107]\n"
"v_cvt_pk_bf16_f32 v59, v74, v75\nv_mfma_f32_16x16x32_bf16 v[108:111], v[52:55], v[140:143], v[108:111]\nds_read_b128 v[52:55], v35 offset:4096\ns_waitcnt lgkmcnt(3)\n"
"v_mfma_f32_16x16x32_bf16 v[96:99], v[40:43], v[144:147], v[96:99]\nv_mfma_f32_16x16x32_bf16 v[100:103], v[40:43], v[148:151], v[100:103]\nds_read_b128 v[40:43], v32 offset:8192\nbuffer_store_dwordx4 v[56:59], v15, s[20:23], s50 offen sc1\n"
"s_waitcnt lgkmcnt(3)\nv_mfma_f32_16x16x32_bf16 v[104:107], v[44:47], v[144:147], v[104:107]\nv_cvt_pk_bf16_f32 v60, v68, v69\nv_mfma_f32_16x16x32_bf16 v[108:111], v[44:47], v[148:151], v[108:111]\n"
"ds_read_b128 v[44:47], v33 offset:8192\nv_cvt_pk_bf16_f32 v61, v70, v71\ns_waitcnt lgkmcnt(3)\nv_mfma_f32_16x16x32_bf16 v[96:99], v[48:51], v[152:155], v[96:99]\n"
"v_cvt_pk_bf16_f32 v62, v76, v77\nv_mfma_f32_16x16x32_bf16 v[100:103], v[48:51], v[156:159], v[100:103]\nds_read_b128 v[48:51], v34 offset:8192\nv_cvt_pk_bf16_f32 v63, v78, v79\n"
"s_waitcnt lgkmcnt(3)\nv_mfma_f32_16x16x32_bf16 v[104:107], v[52:55], v[152:155], v[104:107]\nv_mfma_f32_16x16x32_bf16 v[108:111], v[52:55], v[156:159], v[108:111]\nds_read_b128 v[52:55], v35 offset:8192\n"
"s_waitcnt lgkmcnt(3)\nv_mfma_f32_16x16x32_bf16 v[96:99], v[40:43], v[160:163], v[96:99]\nbuffer_store_dwordx4 v[60:63], v16, s[20:23], s50 offen sc1\nv_mfma_f32_16x16x32_bf16 v[100:103], v[40:43], v[164:167], v[100:103]\n"
"ds_read_b128 v[40:43], v32 offset:12288\ns_add_u32 s50, s50, s36\ns_waitcnt lgkmcnt(3)\nv_mfma_f32_16x16x32_bf16 v[104:107], v[44:47], v[160:163], v[104:107]\n"
"s_add_u32 m0, s52, 0\nv_mfma_f32_16x16x32_bf16 v[108:111], v[44:47], v[164:167], v[108:111]\nds_read_b128 v[44:47], v33 offset:12288\ns_add_u32 s53, s49, 0\n"
"s_waitcnt lgkmcnt(3)\nv_mfma_f32_16x16x32_bf16 v[96:99], v[48:51], v[168:171], v[96:99]\nbuffer_load_dwordx4 v10, s[12:15], s53 offen lds\nv_mfma_f32_16x16x32_bf16 v[100:103], v[48:51], v[172:175], v[100:103]\n"
"ds_read_b128 v[48:51], v34 offset:12288\ns_add_u32 m0, s52, 4096\ns_waitcnt lgkmcnt(3)\nv_mfma_f32_16x16x32_bf16 v[104:107], v[52:55], v[168:171], v[104:107]\n"
"s_add_u32 s53, s49, 128\nv_mfma_f32_16x16x32_bf16 v[108:111], v[52:55], v[172:175], v[108:111]\nds_read_b128 v[52:55], v35 offset:12288\nbuffer_load_dwordx4 v10, s[12:15], s53 offen lds\n"
"s_waitcnt lgkmcnt(3)\nv_mfma_f32_16x16x32_bf16 v[96:99], v[40:43], v[176:179], v[96:99]\ns_add_u32 m0, s52, 8192\nv_mfma_f32_16x16x32_bf16 v[100:103], v[40:43], v[180:183], v[100:103]\n"
"ds_read_b128 v[40:43], v32 offset:16384\ns_add_u32 s53, s49, 256\ns_waitcnt lgkmcnt(3)\nv_mfma_f32_16x16x32_bf16 v[104:107], v[44:47], v[176:179], v[104:107]\n"
"buffer_load_dwordx4 v10, s[12:15], s53 offen lds\nv_mfma_f32_16x16x32_bf16 v[108:111], v[44:47], v[180:183], v[108:111]\nds_read_b128 v[44:47], v33 offset:16384\ns_add_u32 m0, s52, 12288\n"
"s_waitcnt lgkmcnt(3)\nv_mfma_f32_16x16x32_bf16 v[96:99], v[48:51], v[184:187], v[96:99]\ns_add_u32 s53, s49, 384\nv_mfma_f32_16x16x32_bf16 v[100:103], v[48:51], v[188:191], v[100:103]\n"
"ds_read_b128 v[48:51], v34 offset:16384\nbuffer_load_dwordx4 v10, s[12:15], s53 offen lds\ns_waitcnt lgkmcnt(3)\nv_mfma_f32_16x16x32_bf16 v[104:107], v[52:55], v[184:187], v[104:107]\n"
"s_add_u32 m0, s52, 16384\nv_mfma_f32_16x16x32_bf16 v[108:111], v[52:55], v[188:191], v[108:111]\nds_read_b128 v[52:55], v35 offset:16384\ns_add_u32 s53, s49, 512\n"
"s_waitcnt lgkmcnt(3)\nv_mfma_f32_16x16x32_bf16 v[96:99], v[40:43], v[192:195], v[96:99]\nbuffer_load_dwordx4 v10, s[12:15], s53 offen lds\nv_mfma_f32_16x16x32_bf16 v[100:103], v[40:43], v[196:199], v[100:103]\n"
"ds_read_b128 v[40:43], v32 offset:20480\ns_add_u32 m0, s52, 20480\ns_waitcnt lgkmcnt(3)\nv_mfma_f32_16x16x32_bf16 v[104:107], v[44:47], v[192:195], v[104:107]\n"
"s_add_u32 s53, s49, 640\nv_mfma_f32_16x16x32_bf16 v[108:111], v[44:47], v[196:199], v[108:111]\nds_read_b128 v[44:47], v33 offset:20480\nbuffer_load_dwordx4 v10, s[12:15], s53 offen lds\n"
"s_waitcnt lgkmcnt(3)\nv_mfma_f32_16x16x32_bf16 v[96:99], v[48:51], v[200:203], v[96:99]\ns_add_u32 m0, s52, 24576\nv_mfma_f32_16x16x32_bf16 v[100:103], v[48:51], v[204:207], v[100:103]\n"
"ds_read_b128 v[48:51], v34 offset:20480\ns_add_u32 s53, s49, 768\ns_waitcnt lgkmcnt(3)\nv_mfma_f32_16x16x32_bf16 v[104:107], v[52:55], v[200:203], v[104:107]\n"
"buffer_load_dwordx4 v10, s[12:15], s53 offen lds\nv_mfma_f32_16x16x32_bf16 v[108:111], v[52:55], v[204:207], v[108:111]\nds_read_b128 v[52:55], v35 offset:20480\nglobal_load_dwordx4 v[240:243], v23, s[38:39] offset:1024\n"
"s_waitcnt lgkmcnt(3)\nv_mfma_f32_16x16x32_bf16 v[96:99], v[40:43], v[208:211], v[96:99]\nv_mfma_f32_16x16x32_bf16 v[100:103], v[40:43], v[212:215], v[100:103]\nds_read_b128 v[40:43], v32 offset:24576\n"
"s_waitcnt lgkmcnt(3)\nv_mfma_f32_16x16x32_bf16 v[104:107], v[44:47], v[208:211], v[104:107]\nv_mfma_f32_16x16x32_bf16 v[108:111], v[44:47], v[212:215], v[108:111]\nds_read_b128 v[44:47], v33 offset:24576\n"
"s_waitcnt lgkmcnt(3)\nv_mfma_f32_16x16x32_bf16 v[96:99], v[48:51], v[216:219], v[96:99]\ns_add_u32 s54, s51, 32768\ns_cmp_eq_u32 s54, 163840\n"
"v_mfma_f32_16x16x32_bf16 v[100:103], v[48:51], v[220:223], v[100:103]\ns_cselect_b32 s54, 0, s54\ns_sub_u32 s54, s54, s51\nds_read_b128 v[48:51], v34 offset:24576\n"
"s_waitcnt lgkmcnt(3)\nv_mfma_f32_16x16x32_bf16 v[104:107], v[52:55], v[216:219], v[104:107]\nv_mfma_f32_16x16x32_bf16 v[108:111], v[52:55], v[220:223], v[108:111]\nds_read_b128 v[52:55], v35 offset:24576\n"
"s_waitcnt lgkmcnt(3)\nv_mfma_f32_16x16x32_bf16 v[96:99], v[40:43], v[224:227], v[96:99]\nv_add_u32 v32, s54, v32\nv_mfma_f32_16x16x32_bf16 v[100:103], v[40:43], v[228:231], v[100:103]\n"
"ds_read_b128 v[40:43], v32 offset:0\ns_waitcnt lgkmcnt(3)\nv_mfma_f32_16x16x32_bf16 v[104:107], v[44:47], v[224:227], v[104:107]\nv_add_u32 v33, s54, v33\n"
"v_mfma_f32_16x16x32_bf16 v[108:111], v[44:47], v[228:231], v[108:111]\nds_read_b128 v[44:47], v33 offset:0\ns_waitcnt lgkmcnt(3)\nv_mfma_f32_16x16x32_bf16 v[96:99], v[48:51], v[232:235], v[96:99]\n"
"v_add_u32 v34, s54, v34\nv_mfma_f32_16x16x32_bf16 v[100:103], v[48:51], v[236:239], v[100:103]\nds_read_b128 v[48:51], v34 offset:0\ns_waitcnt lgkmcnt(3)\n"
"v_mfma_f32_16x16x32_bf16 v[104:107], v[52:55], v[232:235], v[104:107]\nv_add_u32 v35, s54, v35\nv_mfma_f32_16x16x32_bf16 v[108:111], v[52:55], v[236:239], v[108:111]\nds_read_b128 v[52:55], v35 offset:0\n"
"s_mov_b32 s52, s47\ns_add_u32 s47, s51, s32\ns_add_u32 s51, s51, s54\ns_add_u32 s49, s49, 28672\n"
"s_waitcnt vmcnt(8)\nfg1_pf:\nv_cmp_ne_u32 vcc, s28, v37\ns_cbranch_vccz fg1_pfok\n"
"s_add_u32 s55, s55, 1\ns_cmp_gt_u32 s55, 262144\ns_cbranch_scc0 fg1_pf2\ns_endpgm\n"
"fg1_pf2:\ns_sleep 1\nbuffer_load_dword v37, v20, s[24:27], 0 offen sc1\ns_waitcnt vmcnt(0)\n"
"s_branch fg1_pf\nfg1_pfok:\ns_barrier\ns_waitcnt lgkmcnt(3)\n"
"v_mfma_f32_16x16x32_bf16 v[64:67], v[40:43], v[128:131], 0\nv_mov_b32 v36, s48\ns_add_u32 s48, s48, 1\ns_nop 0\n"
"buffer_store_dword v36, v19, s[24:27], s33 offen sc1\nv_mfma_f32_16x16x32_bf16 v[68:71], v[40:43], v[132:135], 0\nds_read_b128 v[40:43], v32 offset:4096\ns_waitcnt lgkmcnt(3)\n"
"v_mfma_f32_16x16x32_bf16 v[72:75], v[44:47], v[128:131], 0\nv_mfma_f32_16x16x32_bf16 v[76:79], v[44:47], v[132:135], 0\nds_read_b128 v[44:47], v33 offset:4096\nv_cvt_pk_bf16_f32 v56, v96, v97\n"
"s_waitcnt lgkmcnt(3)\nv_mfma_f32_16x16x32_bf16 v[64:67], v[48:51], v[136:139], v[64:67]\nv_cvt_pk_bf16_f32 v57, v98, v99\nv_mfma_f32_16x16x32_bf16 v[68:71], v[48:51], v[140:143], v[68:71]\n"
"ds_read_b128 v[48:51], v34 offset:4096\nv_cvt_pk_bf16_f32 v58, v104, v105\ns_waitcnt lgkmcnt(3)\nv_mfma_f32_16x16x32_bf16 v[72:75], v[52:55], v[136:139], v[72:75]\n"
"v_cvt_pk_bf16_f32 v59, v106, v107\nv_mfma_f32_16x16x32_bf16 v[76:79], v[52:55], v[140:143], v[76:79]\nds_read_b128 v[52:55], v35 offset:4096\ns_waitcnt lgkmcnt(3)\n"
"v_mfma_f32_16x16x32_bf16 v[64:67], v[40:43], v[144:147], v[64:67]\nv_mfma_f32_16x16x32_bf16 v[68:71], v[40:43], v[148:151], v[68:71]\nds_read_b128 v[40:43], v32 offset:8192\nbuffer_store_dwordx4 v[56:59], v15, s[20:23], s50 offen sc1\n"
"s_waitcnt lgkmcnt(3)\nv_mfma_f32_16x16x32_bf16 v[72:75], v[44:47], v[144:147], v[72:75]\nv_cvt_pk_bf16_f32 v60, v100, v101\nv_mfma_f32_16x16x32_bf16 v[76:79], v[44:47], v[148:151], v[76:79]\n"
"ds_read_b128 v[44:47], v33 offset:8192\nv_cvt_pk_bf16_f32 v61, v102, v103\ns_waitcnt lgkmcnt(3)\nv_mfma_f32_16x16x32_bf16 v[64:67], v[48:51], v[152:155], v[64:67]\n"
"v_cvt_pk_bf16_f32 v62, v108, v109\nv_mfma_f32_16x16x32_bf16 v[68:71], v[48:51], v[156:159], v[68:71]\nds_read_b128 v[48:51], v34 offset:8192\nv_cvt_pk_bf16_f32 v63, v110, v111\n"
"s_waitcnt lgkmcnt(3)\nv_mfma_f32_16x16x32_bf16 v[72:75], v[52:55], v[152:155], v[72:75]\nv_mfma_f32_16x16x32_bf16 v[76:79], v[52:55], v[156:159], v[76:79]\nds_read_b128 v[52:55], v35 offset:8192\n"
"s_waitcnt lgkmcnt(3)\nv_mfma_f32_16x16x32_bf16 v[64:67], v[40:43], v[160:163], v[64:67]\nbuffer_store_dwordx4 v[60:63], v16, s[20:23], s50 offen sc1\nv_mfma_f32_16x16x32_bf16 v[68:71], v[40:43], v[164:167], v[68:71]\n"
"ds_read_b128 v[40:43], v32 offset:12288\ns_add_u32 s50, s50, s36\ns_waitcnt lgkmcnt(3)\nv_mfma_f32_16x16x32_bf16 v[72:75], v[44:47], v[160:163], v[72:75]\n"
"s_add_u32 m0, s52, 0\nv_mfma_f32_16x16x32_bf16 v[76:79], v[44:47], v[164:167], v[76:79]\nds_read_b128 v[44:47], v33 offset:12288\ns_add_u32 s53, s49, 0\n"
"s_waitcnt lgkmcnt(3)\nv_mfma_f32_16x16x32_bf16 v[64:67], v[48:51], v[168:171], v[64:67]\nbuffer_load_dwordx4 v10, s[12:15], s53 offen lds\nv_mfma_f32_16x16x32_bf16 v[68:71], v[48:51], v[172:175], v[68:71]\n"
"ds_read_b128 v[48:51], v34 offset:12288\ns_add_u32 m0, s52, 4096\ns_waitcnt lgkmcnt(3)\nv_mfma_f32_16x16x32_bf16 v[72:75], v[52:55], v[168:171], v[72:75]\n"
"s_add_u32 s53, s49, 128\nv_mfma_f32_16x16x32_bf16 v[76:79], v[52:55], v[172:175], v[76:79]\nds_read_b128 v[52:55], v35 offset:12288\nbuffer_load_dwordx4 v10, s[12:15], s53 offen lds\n"
"s_waitcnt lgkmcnt(3)\nv_mfma_f32_16x16x32_bf16 v[64:67], v[40:43], v[176:179], v[64:67]\ns_add_u32 m0, s52, 8192\nv_mfma_f32_16x16x32_bf16 v[68:71], v[40:43], v[180:183], v[68:71]\n"
"ds_read_b128 v[40:43], v32 offset:16384\ns_add_u32 s53, s49, 256\ns_waitcnt lgkmcnt(3)\nv_mfma_f32_16x16x32_bf16 v[72:75], v[44:47], v[176:179], v[72:75]\n"
"buffer_load_dwordx4 v10, s[12:15], s53 offen lds\nv_mfma_f32_16x16x32_bf16 v[76:79], v[44:47], v[180:183], v[76:79]\nds_read_b128 v[44:47], v33 offset:16384\ns_add_u32 m0, s52, 12288\n"
"s_waitcnt lgkmcnt(3)\nv_mfma_f32_16x16x32_bf16 v[64:67], v[48:51], v[184:187], v[64:67]\ns_add_u32 s53, s49, 384\nv_mfma_f32_16x16x32_bf16 v[68:71], v[48:51], v[188:191], v[68:71]\n"
"ds_read_b128 v[48:51], v34 offset:16384\nbuffer_load_dwordx4 v10, s[12:15], s53 offen lds\ns_waitcnt lgkmcnt(3)\nv_mfma_f32_16x16x32_bf16 v[72:75], v[52:55], v[184:187], v[72:75]\n"
"s_add_u32 m0, s52, 16384\nv_mfma_f32_16x16x32_bf16 v[76:79], v[52:55], v[188:191], v[76:79]\nds_read_b128 v[52:55], v35 offset:16384\ns_add_u32 s53, s49, 512\n"
"s_waitcnt lgkmcnt(3)\nv_mfma_f32_16x16x32_bf16 v[64:67], v[40:43], v[192:195], v[64:67]\nbuffer_load_dwordx4 v10, s[12:15], s53 offen lds\nv_mfma_f32_16x16x32_bf16 v[68:71], v[40:43], v[196:199], v[68:71]\n"
"ds_read_b128 v[40:43], v32 offset:20480\ns_add_u32 m0, s52, 20480\ns_waitcnt lgkmcnt(3)\nv_mfma_f32_16x16x32_bf16 v[72:75], v[44:47], v[192:195], v[72:75]\n"
"s_add_u32 s53, s49, 640\nv_mfma_f32_16x16x32_bf16 v[76:79], v[44:47], v[196:199], v[76:79]\nds_read_b128 v[44:47], v33 offset:20480\nbuffer_load_dwordx4 v10, s[12:15], s53 offen lds\n"
"s_waitcnt lgkmcnt(3)\nv_mfma_f32_16x16x32_bf16 v[64:67], v[48:51], v[200:203], v[64:67]\ns_add_u32 m0, s52, 24576\nv_mfma_f32_16x16x32_bf16 v[68:71], v[48:51], v[204:207], v[68:71]\n"
"ds_read_b128 v[48:51], v34 offset:20480\ns_add_u32 s53, s49, 768\ns_waitcnt lgkmcnt(3)\nv_mfma_f32_16x16x32_bf16 v[72:75], v[52:55], v[200:203], v[72:75]\n"
"buffer_load_dwordx4 v10, s[12:15], s53 offen lds\nv_mfma_f32_16x16x32_bf16 v[76:79], v[52:55], v[204:207], v[76:79]\nds_read_b128 v[52:55], v35 offset:20480\nglobal_load_dwordx4 v[240:243], v23, s[38:39] offset:2048\n"
"s_waitcnt lgkmcnt(3)\nv_mfma_f32_16x16x32_bf16 v[64:67], v[40:43], v[208:211], v[64:67]\ns_mov_b32 s53, 0\nbuffer_load_dwordx4 a[0:3], v14, s[16:19], s53 offen sc1\n"
"v_mfma_f32_16x16x32_bf16 v[68:71], v[40:43], v[212:215], v[68:71]\nds_read_b128 v[40:43], v32 offset:24576\ns_mov_b32 s53, 229376\nbuffer_load_dwordx4 a[4:7], v14, s[16:19], s53 offen sc1\n"
"s_waitcnt lgkmcnt(3)\nv_mfma_f32_16x16x32_bf16 v[72:75], v[44:47], v[208:211], v[72:75]\ns_mov_b32 s53, 1024\nbuffer_load_dwordx4 a[8:11], v14, s[16:19], s53 offen sc1\n"
"v_mfma_f32_16x16x32_bf16 v[76:79], v[44:47], v[212:215], v[76:79]\nds_read_b128 v[44:47], v33 offset:24576\ns_mov_b32 s53, 230400\nbuffer_load_dwordx4 a[12:15], v14, s[16:19], s53 offen sc1\n"
"s_waitcnt lgkmcnt(3)\nv_mfma_f32_16x16x32_bf16 v[64:67], v[48:51], v[216:219], v[64:67]\ns_add_u32 s54, s51, 32768\ns_cmp_eq_u32 s54, 163840\n"
"v_mfma_f32_16x16x32_bf16 v[68:71], v[48:51], v[220:223], v[68:71]\ns_cselect_b32 s54, 0, s54\ns_sub_u32 s54, s54, s51\nds_read_b128 v[48:51], v34 offset:24576\n"
"s_mov_b32 s53, 2048\nbuffer_load_dwordx4 a[16:19], v14, s[16:19], s53 offen sc1\ns_waitcnt lgkmcnt(3)\nv_mfma_f32_16x16x32_bf16 v[72:75], v[52:55], v[216:219], v[72:75]\n"
"v_mfma_f32_16x16x32_bf16 v[76:79], v[52:55], v[220:223], v[76:79]\nds_read_b128 v[52:55], v35 offset:24576\ns_waitcnt lgkmcnt(3)\nv_mfma_f32_16x16x32_bf16 v[64:67], v[40:43], v[224:227], v[64:67]\n"
"v_add_u32 v32, s54, v32\nv_mfma_f32_16x16x32_bf16 v[68:71], v[40:43], v[228:231], v[68:71]\nds_read_b128 v[40:43], v32 offset:0\ns_waitcnt lgkmcnt(3)\n"
"v_mfma_f32_16x16x32_bf16 v[72:75], v[44:47], v[224:227], v[72:75]\nv_add_u32 v33, s54, v33\nv_mfma_f32_16x16x32_bf16 v[76:79], v[44:47], v[228:231], v[76:79]\nds_read_b128 v[44:47], v33 offset:0\n"
"s_waitcnt lgkmcnt(3)\nv_mfma_f32_16x16x32_bf16 v[64:67], v[48:51], v[232:235], v[64:67]\nv_add_u32 v34, s54, v34\nv_mfma_f32_16x16x32_bf16 v[68:71], v[48:51], v[236:239], v[68:71]\n"
"ds_read_b128 v[48:51], v34 offset:0\ns_waitcnt lgkmcnt(3)\nv_mfma_f32_16x16x32_bf16 v[72:75], v[52:55], v[232:235], v[72:75]\nv_add_u32 v35, s54, v35\n"
"v_mfma_f32_16x16x32_bf16 v[76:79], v[52:55], v[236:239], v[76:79]\nds_read_b128 v[52:55], v35 offset:0\ns_mov_b32 s52, s47\ns_add_u32 s47, s51, s32\n"
"s_add_u32 s51, s51, s54\ns_add_u32 s49, s49, 28672\ns_waitcnt vmcnt(13)\ns_barrier\n"
"s_waitcnt lgkmcnt(3)\nv_mfma_f32_16x16x32_bf16 v[96:99], v[40:43], v[128:131], 0\nv_mov_b32 v36, s48\ns_add_u32 s48, s48, 1\n"
"s_nop 0\nbuffer_store_dword v36, v19, s[24:27], s33 offen sc1\nv_mfma_f32_16x16x32_bf16 v[100:103], v[40:43], v[132:135], 0\nds_read_b128 v[40:43], v32 offset:4096\n"
"s_waitcnt lgkmcnt(3)\nv_mfma_f32_16x16x32_bf16 v[104:107], v[44:47], v[128:131], 0\nv_mfma_f32_16x16x32_bf16 v[108:111], v[44:47], v[132:135], 0\nds_read_b128 v[44:47], v33 offset:4096\n"
"v_cvt_pk_bf16_f32 v56, v64, v65\ns_waitcnt lgkmcnt(3)\nv_mfma_f32_16x16x32_bf16 v[96:99], v[48:51], v[136:139], v[96:99]\nv_cvt_pk_bf16_f32 v57, v66, v67\n"
"v_mfma_f32_16x16x32_bf16 v[100:103], v[48:51], v[140:143], v[100:103]\nds_read_b128 v[48:51], v34 offset:4096\nv_cvt_pk_bf16_f32 v58, v72, v73\ns_waitcnt lgkmcnt(3)\n"
"v_mfma_f32_16x16x32_bf16 v[104:107], v[52:55], v[136:139], v[104:107]\nv_cvt_pk_bf16_f32 v59, v74, v75\nv_mfma_f32_16x16x32_bf16 v[108:111], v[52:55], v[140:143], v[108:111]\nds_read_b128 v[52:55], v35 offset:4096\n"
"s_waitcnt lgkmcnt(3)\nv_mfma_f32_16x16x32_bf16 v[96:99], v[40:43], v[144:147], v[96:99]\nv_mfma_f32_16x16x32_bf16 v[100:103], v[40:43], v[148:151], v[100:103]\nds_read_b128 v[40:43], v32 offset:8192\n"
"buffer_store_dwordx4 v[56:59], v15, s[20:23], s50 offen sc1\ns_waitcnt lgkmcnt(3)\nv_mfma_f32_16x16x32_bf16 v[104:107], v[44:47], v[144:147], v[104:107]\nv_cvt_pk_bf16_f32 v60, v68, v69\n"
"v_mfma_f32_16x16x32_bf16 v[108:111], v[44:47], v[148:151], v[108:111]\nds_read_b128 v[44:47], v33 offset:8192\nv_cvt_pk_bf16_f32 v61, v70, v71\ns_waitcnt lgkmcnt(3)\n"
"v_mfma_f32_16x16x32_bf16 v[96:99], v[48:51], v[152:155], v[96:99]\nv_cvt_pk_bf16_f32 v62, v76, v77\nv_mfma_f32_16x16x32_bf16 v[100:103], v[48:51], v[156:159], v[100:103]\nds_read_b128 v[48:51], v34 offset:8192\n"
"v_cvt_pk_bf16_f32 v63, v78, v79\ns_waitcnt lgkmcnt(3)\nv_mfma_f32_16x16x32_bf16 v[104:107], v[52:55], v[152:155], v[104:107]\nv_mfma_f32_16x16x32_bf16 v[108:111], v[52:55], v[156:159], v[108:111]\n"
"ds_read_b128 v[52:55], v35 offset:8192\ns_waitcnt lgkmcnt(3)\nv_mfma_f32_16x16x32_bf16 v[96:99], v[40:43], v[160:163], v[96:99]\nbuffer_store_dwordx4 v[60:63], v16, s[20:23], s50 offen sc1\n"
"v_mfma_f32_16x16x32_bf16 v[100:103], v[40:43], v[164:167], v[100:103]\nds_read_b128 v[40:43], v32 offset:12288\ns_add_u32 s50, s50, s36\ns_waitcnt lgkmcnt(3)\n"
"v_mfma_f32_16x16x32_bf16 v[104:107], v[44:47], v[160:163], v[104:107]\ns_add_u32 m0, s52, 0\nv_mfma_f32_16x16x32_bf16 v[108:111], v[44:47], v[164:167], v[108:111]\nds_read_b128 v[44:47], v33 offset:12288\n"
"s_add_u32 s53, s49, 0\ns_waitcnt lgkmcnt(3)\nv_mfma_f32_16x16x32_bf16 v[96:99], v[48:51], v[168:171], v[96:99]\nbuffer_load_dwordx4 v10, s[12:15], s53 offen lds\n"
"v_mfma_f32_16x16x32_bf16 v[100:103], v[48:51], v[172:175], v[100:103]\nds_read_b128 v[48:51], v34 offset:12288\ns_add_u32 m0, s52, 4096\ns_waitcnt lgkmcnt(3)\n"
"v_mfma_f32_16x16x32_bf16 v[104:107], v[52:55], v[168:171], v[104:107]\ns_add_u32 s53, s49, 128\nv_mfma_f32_16x16x32_bf16 v[108:111], v[52:55], v[172:175], v[108:111]\nds_read_b128 v[52:55], v35 offset:12288\n"
"buffer_load_dwordx4 v10, s[12:15], s53 offen lds\ns_waitcnt lgkmcnt(3)\nv_mfma_f32_16x16x32_bf16 v[96:99], v[40:43], v[176:179], v[96:99]\ns_add_u32 m0, s52, 8192\n"
"v_mfma_f32_16x16x32_bf16 v[100:103], v[40:43], v[180:183], v[100:103]\nds_read_b128 v[40:43], v32 offset:16384\ns_add_u32 s53, s49, 256\ns_waitcnt lgkmcnt(3)\n"
"v_mfma_f32_16x16x32_bf16 v[104:107], v[44:47], v[176:179], v[104:107]\nbuffer_load_dwordx4 v10, s[12:15], s53 offen lds\nv_mfma_f32_16x16x32_bf16 v[108:111], v[44:47], v[180:183], v[108:111]\nds_read_b128 v[44:47], v33 offset:16384\n"
"s_add_u32 m0, s52, 12288\ns_waitcnt lgkmcnt(3)\nv_mfma_f32_16x16x32_bf16 v[96:99], v[48:51], v[184:187], v[96:99]\ns_add_u32 s53, s49, 384\n"
"v_mfma_f32_16x16x32_bf16 v[100:103], v[48:51], v[188:191], v[100:103]\nds_read_b128 v[48:51], v34 offset:16384\nbuffer_load_dwordx4 v10, s[12:15], s53 offen lds\ns_waitcnt lgkmcnt(3)\n"
"v_mfma_f32_16x16x32_bf16 v[104:107], v[52:55], v[184:187], v[104:107]\ns_add_u32 m0, s52, 16384\nv_mfma_f32_16x16x32_bf16 v[108:111], v[52:55], v[188:191], v[108:111]\nds_read_b128 v[52:55], v35 offset:16384\n"
"s_add_u32 s53, s49, 512\ns_waitcnt lgkmcnt(3)\nv_mfma_f32_16x16x32_bf16 v[96:99], v[40:43], v[192:195], v[96:99]\nbuffer_load_dwordx4 v10, s[12:15], s53 offen lds\n"
"v_mfma_f32_16x16x32_bf16 v[100:103], v[40:43], v[196:199], v[100:103]\nds_read_b128 v[40:43], v32 offset:20480\ns_add_u32 m0, s52, 20480\ns_waitcnt lgkmcnt(3)\n"
"v_mfma_f32_16x16x32_bf16 v[104:107], v[44:47], v[192:195], v[104:107]\ns_add_u32 s53, s49, 640\nv_mfma_f32_16x16x32_bf16 v[108:111], v[44:47], v[196:199], v[108:111]\nds_read_b128 v[44:47], v33 offset:20480\n"
"buffer_load_dwordx4 v10, s[12:15], s53 offen lds\ns_waitcnt lgkmcnt(3)\nv_mfma_f32_16x16x32_bf16 v[96:99], v[48:51], v[200:203], v[96:99]\ns_add_u32 m0, s52, 24576\n"
"v_mfma_f32_16x16x32_bf16 v[100:103], v[48:51], v[204:207], v[100:103]\nds_read_b128 v[48:51], v34 offset:20480\ns_add_u32 s53, s49, 768\ns_waitcnt lgkmcnt(3)\n"
"v_mfma_f32_16x16x32_bf16 v[104:107], v[52:55], v[200:203], v[104:107]\nbuffer_load_dwordx4 v10, s[12:15], s53 offen lds\nv_mfma_f32_16x16x32_bf16 v[108:111], v[52:55], v[204:207], v[108:111]\nds_read_b128 v[52:55], v35 offset:20480\n"
"global_load_dwordx4 v[240:243], v23, s[38:39] offset:3072\ns_waitcnt lgkmcnt(3)\nv_mfma_f32_16x16x32_bf16 v[96:99], v[40:43], v[208:211], v[96:99]\ns_mov_b32 s53, 231424\n"
"buffer_load_dwordx4 a[20:23], v14, s[16:19], s53 offen sc1\nv_mfma_f32_16x16x32_bf16 v[100:103], v[40:43], v[212:215], v[100:103]\nds_read_b128 v[40:43], v32 offset:24576\ns_mov_b32 s53, 3072\n"
"buffer_load_dwordx4 a[24:27], v14, s[16:19], s53 offen sc1\ns_waitcnt lgkmcnt(3)\nv_mfma_f32_16x16x32_bf16 v[104:107], v[44:47], v[208:211], v[104:107]\ns_mov_b32 s53, 232448\n"
"buffer_load_dwordx4 a[28:31], v14, s[16:19], s53 offen sc1\nv_mfma_f32_16x16x32_bf16 v[108:111], v[44:47], v[212:215], v[108:111]\nds_read_b128 v[44:47], v33 offset:24576\ns_mov_b32 s53, 4096\n"
"buffer_load_dwordx4 a[32:35], v14, s[16:19], s53 offen sc1\ns_waitcnt lgkmcnt(3)\nv_mfma_f32_16x16x32_bf16 v[96:99], v[48:51], v[216:219], v[96:99]\ns_add_u32 s54, s51, 32768\n"
"s_cmp_eq_u32 s54, 163840\nv_mfma_f32_16x16x32_bf16 v[100:103], v[48:51], v[220:223], v[100:103]\ns_cselect_b32 s54, 0, s54\ns_sub_u32 s54, s54, s51\n"
"ds_read_b128 v[48:51], v34 offset:24576\ns_mov_b32 s53, 233472\nbuffer_load_dwordx4 a[36:39], v14, s[16:19], s53 offen sc1\ns_waitcnt lgkmcnt(3)\n"
"v_mfma_f32_16x16x32_bf16 v[104:107], v[52:55], v[216:219], v[104:107]\nv_mfma_f32_16x16x32_bf16 v[108:111], v[52:55], v[220:223], v[108:111]\nds_read_b128 v[52:55], v35 offset:24576\ns_waitcnt lgkmcnt(3)\n"
"v_mfma_f32_16x16x32_bf16 v[96:99], v[40:43], v[224:227], v[96:99]\nv_add_u32 v32, s54, v32\nv_mfma_f32_16x16x32_bf16 v[100:103], v[40:43], v[228:231], v[100:103]\nds_read_b128 v[40:43], v32 offset:0\n"
"s_waitcnt lgkmcnt(3)\nv_mfma_f32_16x16x32_bf16 v[104:107], v[44:47], v[224:227], v[104:107]\nv_add_u32 v33, s54, v33\nv_mfma_f32_16x16x32_bf16 v[108:111], v[44:47], v[228:231], v[108:111]\n"
"ds_read_b128 v[44:47], v33 offset:0\ns_waitcnt lgkmcnt(3)\nv_mfma_f32_16x16x32_bf16 v[96:99], v[48:51], v[232:235], v[96:99]\nv_add_u32 v34, s54, v34\n"
"v_mfma_f32_16x16x32_bf16 v[100:103], v[48:51], v[236:239], v[100:103]\nds_read_b128 v[48:51], v34 offset:0\ns_waitcnt lgkmcnt(3)\nv_mfma_f32_16x16x32_bf16 v[104:107], v[52:55], v[232:235], v[104:107]\n"
"v_add_u32 v35, s54, v35\nv_mfma_f32_16x16x32_bf16 v[108:111], v[52:55], v[236:239], v[108:111]\nds_read_b128 v[52:55], v35 offset:0\ns_mov_b32 s52, s47\n"
"s_add_u32 s47, s51, s32\ns_add_u32 s51, s51, s54\ns_add_u32 s49, s49, 28672\ns_waitcnt vmcnt(13)\n"
"s_barrier\ns_waitcnt lgkmcnt(3)\nv_mfma_f32_16x16x32_bf16 v[64:67], v[40:43], v[128:131], 0\nv_mov_b32 v36, s48\n"
"s_add_u32 s48, s48, 1\ns_nop 0\nbuffer_store_dword v36, v19, s[24:27], s33 offen sc1\nv_mfma_f32_16x16x32_bf16 v[68:71], v[40:43], v[132:135], 0\n"
"ds_read_b128 v[40:43], v32 offset:4096\ns_waitcnt lgkmcnt(3)\nv_mfma_f32_16x16x32_bf16 v[72:75], v[44:47], v[128:131], 0\nv_mfma_f32_16x16x32_bf16 v[76:79], v[44:47], v[132:135], 0\n"
"ds_read_b128 v[44:47], v33 offset:4096\nv_cvt_pk_bf16_f32 v56, v96, v97\ns_waitcnt lgkmcnt(3)\nv_mfma_f32_16x16x32_bf16 v[64:67], v[48:51], v[136:139], v[64:67]\n"
"v_cvt_pk_bf16_f32 v57, v98, v99\nv_mfma_f32_16x16x32_bf16 v[68:71], v[48:51], v[140:143], v[68:71]\nds_read_b128 v[48:51], v34 offset:4096\nv_cvt_pk_bf16_f32 v58, v104, v105\n"
"s_waitcnt lgkmcnt(3)\nv_mfma_f32_16x16x32_bf16 v[72:75], v[52:55], v[136:139], v[72:75]\nv_cvt_pk_bf16_f32 v59, v106, v107\nv_mfma_f32_16x16x32_bf16 v[76:79], v[52:55], v[140:143], v[76:79]\n"
"ds_read_b128 v[52:55], v35 offset:4096\ns_waitcnt lgkmcnt(3)\nv_mfma_f32_16x16x32_bf16 v[64:67], v[40:43], v[144:147], v[64:67]\nv_mfma_f32_16x16x32_bf16 v[68:71], v[40:43], v[148:151], v[68:71]\n"
"ds_read_b128 v[40:43], v32 offset:8192\nbuffer_store_dwordx4 v[56:59], v15, s[20:23], s50 offen sc1\ns_waitcnt lgkmcnt(3)\nv_mfma_f32_16x16x32_bf16 v[72:75], v[44:47], v[144:147], v[72:75]\n"
"v_cvt_pk_bf16_f32 v60, v100, v101\nv_mfma_f32_16x16x32_bf16 v[76:79], v[44:47], v[148:151], v[76:79]\nds_read_b128 v[44:47], v33 offset:8192\nv_cvt_pk_bf16_f32 v61, v102, v103\n"
"s_waitcnt lgkmcnt(3)\nv_mfma_f32_16x16x32_bf16 v[64:67], v[48:51], v[152:155], v[64:67]\nv_cvt_pk_bf16_f32 v62, v108, v109\nv_mfma_f32_16x16x32_bf16 v[68:71], v[48:51], v[156:159], v[68:71]\n"
"ds_read_b128 v[48:51], v34 offset:8192\nv_cvt_pk_bf16_f32 v63, v110, v111\ns_waitcnt lgkmcnt(3)\nv_mfma_f32_16x16x32_bf16 v[72:75], v[52:55], v[152:155], v[72:75]\n"
"v_mfma_f32_16x16x32_bf16 v[76:79], v[52:55], v[156:159], v[76:79]\nds_read_b128 v[52:55], v35 offset:8192\ns_waitcnt lgkmcnt(3)\nv_mfma_f32_16x16x32_bf16 v[64:67], v[40:43], v[160:163], v[64:67]\n"
"buffer_store_dwordx4 v[60:63], v16, s[20:23], s50 offen sc1\nv_mfma_f32_16x16x32_bf16 v[68:71], v[40:43], v[164:167], v[68:71]\nds_read_b128 v[40:43], v32 offset:12288\ns_add_u32 s50, s50, s36\n"
"s_waitcnt lgkmcnt(3)\nv_mfma_f32_16x16x32_bf16 v[72:75], v[44:47], v[160:163], v[72:75]\ns_add_u32 m0, s52, 0\nv_mfma_f32_16x16x32_bf16 v[76:79], v[44:47], v[164:167], v[76:79]\n"
"ds_read_b128 v[44:47], v33 offset:12288\ns_add_u32 s53, s49, 0\ns_waitcnt lgkmcnt(3)\nv_mfma_f32_16x16x32_bf16 v[64:67], v[48:51], v[168:171], v[64:67]\n"
"buffer_load_dwordx4 v10, s[12:15], s53 offen lds\nv_mfma_f32_16x16x32_bf16 v[68:71], v[48:51], v[172:175], v[68:71]\nds_read_b128 v[48:51], v34 offset:12288\ns_add_u32 m0, s52, 4096\n"
"s_waitcnt lgkmcnt(3)\nv_mfma_f32_16x16x32_bf16 v[72:75], v[52:55], v[168:171], v[72:75]\ns_add_u32 s53, s49, 128\nv_mfma_f32_16x16x32_bf16 v[76:79], v[52:55], v[172:175], v[76:79]\n"
"ds_read_b128 v[52:55], v35 offset:12288\nbuffer_load_dwordx4 v10, s[12:15], s53 offen lds\ns_waitcnt lgkmcnt(3)\nv_mfma_f32_16x16x32_bf16 v[64:67], v[40:43], v[176:179], v[64:67]\n"
"s_add_u32 m0, s52, 8192\nv_mfma_f32_16x16x32_bf16 v[68:71], v[40:43], v[180:183], v[68:71]\nds_read_b128 v[40:43], v32 offset:16384\ns_add_u32 s53, s49, 256\n"
"s_waitcnt lgkmcnt(3)\nv_mfma_f32_16x16x32_bf16 v[72:75], v[44:47], v[176:179], v[72:75]\nbuffer_load_dwordx4 v10, s[12:15], s53 offen lds\nv_mfma_f32_16x16x32_bf16 v[76:79], v[44:47], v[180:183], v[76:79]\n"
"ds_read_b128 v[44:47], v33 offset:16384\ns_add_u32 m0, s52, 12288\ns_waitcnt lgkmcnt(3)\nv_mfma_f32_16x16x32_bf16 v[64:67], v[48:51], v[184:187], v[64:67]\n"
"s_add_u32 s53, s49, 384\nv_mfma_f32_16x16x32_bf16 v[68:71], v[48:51], v[188:191], v[68:71]\nds_read_b128 v[48:51], v34 offset:16384\nbuffer_load_dwordx4 v10, s[12:15], s53 offen lds\n"
"s_waitcnt lgkmcnt(3)\nv_mfma_f32_16x16x32_bf16 v[72:75], v[52:55], v[184:187], v[72:75]\ns_add_u32 m0, s52, 16384\nv_mfma_f32_16x16x32_bf16 v[76:79], v[52:55], v[188:191], v[76:79]\n"
"ds_read_b128 v[52:55], v35 offset:16384\ns_add_u32 s53, s49, 512\ns_waitcnt lgkmcnt(3)\nv_mfma_f32_16x16x32_bf16 v[64:67], v[40:43], v[192:195], v[64:67]\n"
"buffer_load_dwordx4 v10, s[12:15], s53 offen lds\nv_mfma_f32_16x16x32_bf16 v[68:71], v[40:43], v[196:199], v[68:71]\nds_read_b128 v[40:43], v32 offset:20480\ns_add_u32 m0, s52, 20480\n"
"s_waitcnt lgkmcnt(3)\nv_mfma_f32_16x16x32_bf16 v[72:75], v[44:47], v[192:195], v[72:75]\ns_add_u32 s53, s49, 640\nv_mfma_f32_16x16x32_bf16 v[76:79], v[44:47], v[196:199], v[76:79]\n"
"ds_read_b128 v[44:47], v33 offset:20480\nbuffer_load_dwordx4 v10, s[12:15], s53 offen lds\ns_waitcnt lgkmcnt(3)\nv_mfma_f32_16x16x32_bf16 v[64:67], v[48:51], v[200:203], v[64:67]\n"
"s_add_u32 m0, s52, 24576\nv_mfma_f32_16x16x32_bf16 v[68:71], v[48:51], v[204:207], v[68:71]\nds_read_b128 v[48:51], v34 offset:20480\ns_add_u32 s53, s49, 768\n"
"s_waitcnt lgkmcnt(3)\nv_mfma_f32_16x16x32_bf16 v[72:75], v[52:55], v[200:203], v[72:75]\nbuffer_load_dwordx4 v10, s[12:15], s53 offen lds\nv_mfma_f32_16x16x32_bf16 v[76:79], v[52:55], v[204:207], v[76:79]\n"
"ds_read_b128 v[52:55], v35 offset:20480\ns_mov_b32 s53, 5120\nbuffer_load_dwordx4 a[40:43], v14, s[16:19], s53 offen sc1\ns_waitcnt lgkmcnt(3)\n"
"v_mfma_f32_16x16x32_bf16 v[64:67], v[40:43], v[208:211], v[64:67]\ns_mov_b32 s53, 234496\nbuffer_load_dwordx4 a[44:47], v14, s[16:19], s53 offen sc1\nv_mfma_f32_16x16x32_bf16 v[68:71], v[40:43], v[212:215], v[68:71]\n"
"ds_read_b128 v[40:43], v32 offset:24576\ns_mov_b32 s53, 6144\nbuffer_load_dwordx4 a[48:51], v14, s[16:19], s53 offen sc1\ns_waitcnt lgkmcnt(3)\n"
"v_mfma_f32_16x16x32_bf16 v[72:75], v[44:47], v[208:211], v[72:75]\ns_mov_b32 s53, 235520\nbuffer_load_dwordx4 a[52:55], v14, s[16:19], s53 offen sc1\nv_mfma_f32_16x16x32_bf16 v[76:79], v[44:47], v[212:215], v[76:79]\n"
"ds_read_b128 v[44:47], v33 offset:24576\ns_mov_b32 s53, 7168\nbuffer_load_dwordx4 a[56:59], v14, s[16:19], s53 offen sc1\ns_waitcnt lgkmcnt(3)\n"
"v_mfma_f32_16x16x32_bf16 v[64:67], v[48:51], v[216:219], v[64:67]\ns_add_u32 s54, s51, 32768\ns_cmp_eq_u32 s54, 163840\nv_mfma_f32_16x16x32_bf16 v[68:71], v[48:51], v[220:223], v[68:71]\n"
"s_cselect_b32 s54, 0, s54\ns_sub_u32 s54, s54, s51\nds_read_b128 v[48:51], v34 offset:24576\ns_waitcnt lgkmcnt(3)\n"
"v_mfma_f32_16x16x32_bf16 v[72:75], v[52:55], v[216:219], v[72:75]\nv_mfma_f32_16x16x32_bf16 v[76:79], v[52:55], v[220:223], v[76:79]\nds_read_b128 v[52:55], v35 offset:24576\ns_waitcnt lgkmcnt(3)\n"
"v_mfma_f32_16x16x32_bf16 v[64:67], v[40:43], v[224:227], v[64:67]\nv_add_u32 v32, s54, v32\nv_mfma_f32_16x16x32_bf16 v[68:71], v[40:43], v[228:231], v[68:71]\nds_read_b128 v[40:43], v32 offset:0\n"
"s_waitcnt lgkmcnt(3)\nv_mfma_f32_16x16x32_bf16 v[72:75], v[44:47], v[224:227], v[72:75]\nv_add_u32 v33, s54, v33\nv_mfma_f32_16x16x32_bf16 v[76:79], v[44:47], v[228:231], v[76:79]\n"
"ds_read_b128 v[44:47], v33 offset:0\ns_waitcnt lgkmcnt(3)\nv_mfma_f32_16x16x32_bf16 v[64:67], v[48:51], v[232:235], v[64:67]\nv_add_u32 v34, s54, v34\n"
"v_mfma_f32_16x16x32_bf16 v[68:71], v[48:51], v[236:239], v[68:71]\nds_read_b128 v[48:51], v34 offset:0\ns_waitcnt lgkmcnt(3)\nv_mfma_f32_16x16x32_bf16 v[72:75], v[52:55], v[232:235], v[72:75]\n"
"v_add_u32 v35, s54, v35\nv_mfma_f32_16x16x32_bf16 v[76:79], v[52:55], v[236:239], v[76:79]\nds_read_b128 v[52:55], v35 offset:0\ns_mov_b32 s52, s47\n"
"s_add_u32 s47, s51, s32\ns_add_u32 s51, s51, s54\ns_add_u32 s49, s49, 28672\ns_waitcnt vmcnt(12)\n"
"s_barrier\ns_waitcnt lgkmcnt(3)\nv_mfma_f32_16x16x32_bf16 v[96:99], v[40:43], v[128:131], 0\nv_mov_b32 v36, s48\n"
"s_add_u32 s48, s48, 1\ns_nop 0\nbuffer_store_dword v36, v19, s[24:27], s33 offen sc1\nv_mfma_f32_16x16x32_bf16 v[100:103], v[40:43], v[132:135], 0\n"
"ds_read_b128 v[40:43], v32 offset:4096\ns_waitcnt lgkmcnt(3)\nv_mfma_f32_16x16x32_bf16 v[104:107], v[44:47], v[128:131], 0\nv_mfma_f32_16x16x32_bf16 v[108:111], v[44:47], v[132:135], 0\n"
"ds_read_b128 v[44:47], v33 offset:4096\nv_cvt_pk_bf16_f32 v56, v64, v65\ns_waitcnt lgkmcnt(3)\nv_mfma_f32_16x16x32_bf16 v[96:99], v[48:51], v[136:139], v[96:99]\n"
"v_cvt_pk_bf16_f32 v57, v66, v67\nv_mfma_f32_16x16x32_bf16 v[100:103], v[48:51], v[140:143], v[100:103]\nds_read_b128 v[48:51], v34 offset:4096\nv_cvt_pk_bf16_f32 v58, v72, v73\n"
"s_waitcnt lgkmcnt(3)\nv_mfma_f32_16x16x32_bf16 v[104:107], v[52:55], v[136:139], v[104:107]\nv_cvt_pk_bf16_f32 v59, v74, v75\nv_mfma_f32_16x16x32_bf16 v[108:111], v[52:55], v[140:143], v[108:111]\n"
"ds_read_b128 v[52:55], v35 offset:4096\ns_waitcnt lgkmcnt(3)\nv_mfma_f32_16x16x32_bf16 v[96:99], v[40:43], v[144:147], v[96:99]\nv_mfma_f32_16x16x32_bf16 v[100:103], v[40:43], v[148:151], v[100:103]\n"
"ds_read_b128 v[40:43], v32 offset:8192\nbuffer_store_dwordx4 v[56:59], v15, s[20:23], s50 offen sc1\ns_waitcnt lgkmcnt(3)\nv_mfma_f32_16x16x32_bf16 v[104:107], v[44:47], v[144:147], v[104:107]\n"
"v_cvt_pk_bf16_f32 v60, v68, v69\nv_mfma_f32_16x16x32_bf16 v[108:111], v[44:47], v[148:151], v[108:111]\nds_read_b128 v[44:47], v33 offset:8192\nv_cvt_pk_bf16_f32 v61, v70, v71\n"
"s_waitcnt lgkmcnt(3)\nv_mfma_f32_16x16x32_bf16 v[96:99], v[48:51], v[152:155], v[96:99]\nv_cvt_pk_bf16_f32 v62, v76, v77\nv_mfma_f32_16x16x32_bf16 v[100:103], v[48:51], v[156:159], v[100:103]\n"
"ds_read_b128 v[48:51], v34 offset:8192\nv_cvt_pk_bf16_f32 v63, v78, v79\ns_waitcnt lgkmcnt(3)\nv_mfma_f32_16x16x32_bf16 v[104:107], v[52:55], v[152:155], v[104:107]\n"
"v_mfma_f32_16x16x32_bf16 v[108:111], v[52:55], v[156:159], v[108:111]\nds_read_b128 v[52:55], v35 offset:8192\ns_waitcnt lgkmcnt(3)\nv_mfma_f32_16x16x32_bf16 v[96:99], v[40:43], v[160:163], v[96:99]\n"
"buffer_store_dwordx4 v[60:63], v16, s[20:23], s50 offen sc1\nv_mfma_f32_16x16x32_bf16 v[100:103], v[40:43], v[164:167], v[100:103]\nds_read_b128 v[40:43], v32 offset:12288\ns_add_u32 s50, s50, s36\n"
"s_waitcnt lgkmcnt(3)\nv_mfma_f32_16x16x32_bf16 v[104:107], v[44:47], v[160:163], v[104:107]\ns_add_u32 m0, s52, 0\nv_mfma_f32_16x16x32_bf16 v[108:111], v[44:47], v[164:167], v[108:111]\n"
"ds_read_b128 v[44:47], v33 offset:12288\ns_add_u32 s53, s49, 0\ns_waitcnt lgkmcnt(3)\nv_mfma_f32_16x16x32_bf16 v[96:99], v[48:51], v[168:171], v[96:99]\n"
"buffer_load_dwordx4 v10, s[12:15], s53 offen lds\nv_mfma_f32_16x16x32_bf16 v[100:103], v[48:51], v[172:175], v[100:103]\nds_read_b128 v[48:51], v34 offset:12288\ns_add_u32 m0, s52, 4096\n"
"s_waitcnt lgkmcnt(3)\nv_mfma_f32_16x16x32_bf16 v[104:107], v[52:55], v[168:171], v[104:107]\ns_add_u32 s53, s49, 128\nv_mfma_f32_16x16x32_bf16 v[108:111], v[52:55], v[172:175], v[108:111]\n"
"ds_read_b128 v[52:55], v35 offset:12288\nbuffer_load_dwordx4 v10, s[12:15], s53 offen lds\ns_waitcnt lgkmcnt(3)\nv_mfma_f32_16x16x32_bf16 v[96:99], v[40:43], v[176:179], v[96:99]\n"
"s_add_u32 m0, s52, 8192\nv_mfma_f32_16x16x32_bf16 v[100:103], v[40:43], v[180:183], v[100:103]\nds_read_b128 v[40:43], v32 offset:16384\ns_add_u32 s53, s49, 256\n"
"s_waitcnt lgkmcnt(3)\nv_mfma_f32_16x16x32_bf16 v[104:107], v[44:47], v[176:179], v[104:107]\nbuffer_load_dwordx4 v10, s[12:15], s53 offen lds\nv_mfma_f32_16x16x32_bf16 v[108:111], v[44:47], v[180:183], v[108:111]\n"
"ds_read_b128 v[44:47], v33 offset:16384\ns_add_u32 m0, s52, 12288\ns_waitcnt lgkmcnt(3)\nv_mfma_f32_16x16x32_bf16 v[96:99], v[48:51], v[184:187], v[96:99]\n"
"s_add_u32 s53, s49, 384\nv_mfma_f32_16x16x32_bf16 v[100:103], v[48:51], v[188:191], v[100:103]\nds_read_b128 v[48:51], v34 offset:16384\nbuffer_load_dwordx4 v10, s[12:15], s53 offen lds\n"
"s_waitcnt lgkmcnt(3)\nv_mfma_f32_16x16x32_bf16 v[104:107], v[52:55], v[184:187], v[104:107]\ns_add_u32 m0, s52, 16384\nv_mfma_f32_16x16x32_bf16 v[108:111], v[52:55], v[188:191], v[108:111]\n"
"ds_read_b128 v[52:55], v35 offset:16384\ns_add_u32 s53, s49, 512\ns_waitcnt lgkmcnt(3)\nv_mfma_f32_16x16x32_bf16 v[96:99], v[40:43], v[192:195], v[96:99]\n"
"buffer_load_dwordx4 v10, s[12:15], s53 offen lds\nv_mfma_f32_16x16x32_bf16 v[100:103], v[40:43], v[196:199], v[100:103]\nds_read_b128 v[40:43], v32 offset:20480\ns_add_u32 m0, s52, 20480\n"
"s_waitcnt lgkmcnt(3)\nv_mfma_f32_16x16x32_bf16 v[104:107], v[44:47], v[192:195], v[104:107]\ns_add_u32 s53, s49, 640\nv_mfma_f32_16x16x32_bf16 v[108:111], v[44:47], v[196:199], v[108:111]\n"
"ds_read_b128 v[44:47], v33 offset:20480\nbuffer_load_dwordx4 v10, s[12:15], s53 offen lds\ns_waitcnt lgkmcnt(3)\nv_mfma_f32_16x16x32_bf16 v[96:99], v[48:51], v[200:203], v[96:99]\n"
"s_add_u32 m0, s52, 24576\nv_mfma_f32_16x16x32_bf16 v[100:103], v[48:51], v[204:207], v[100:103]\nds_read_b128 v[48:51], v34 offset:20480\ns_add_u32 s53, s49, 768\n"
"s_waitcnt lgkmcnt(3)\nv_mfma_f32_16x16x32_bf16 v[104:107], v[52:55], v[200:203], v[104:107]\nbuffer_load_dwordx4 v10, s[12:15], s53 offen lds\nv_mfma_f32_16x16x32_bf16 v[108:111], v[52:55], v[204:207], v[108:111]\n"
"ds_read_b128 v[52:55], v35 offset:20480\ns_mov_b32 s53, 236544\nbuffer_load_dwordx4 a[60:63], v14, s[16:19], s53 offen sc1\ns_waitcnt lgkmcnt(3)\n"
"v_mfma_f32_16x16x32_bf16 v[96:99], v[40:43], v[208:211], v[96:99]\ns_mov_b32 s53, 8192\nbuffer_load_dwordx4 a[64:67], v14, s[16:19], s53 offen sc1\nv_mfma_f32_16x16x32_bf16 v[100:103], v[40:43], v[212:215], v[100:103]\n"
"ds_read_b128 v[40:43], v32 offset:24576\ns_mov_b32 s53, 237568\nbuffer_load_dwordx4 a[68:71], v14, s[16:19], s53 offen sc1\ns_waitcnt lgkmcnt(3)\n"
"v_mfma_f32_16x16x32_bf16 v[104:107], v[44:47], v[208:211], v[104:107]\ns_mov_b32 s53, 9216\nbuffer_load_dwordx4 a[72:75], v14, s[16:19], s53 offen sc1\nv_mfma_f32_16x16x32_bf16 v[108:111], v[44:47], v[212:215], v[108:111]\n"
"ds_read_b128 v[44:47], v33 offset:24576\ns_mov_b32 s53, 238592\nbuffer_load_dwordx4 a[76:79], v14, s[16:19], s53 offen sc1\ns_waitcnt lgkmcnt(3)\n"
"v_mfma_f32_16x16x32_bf16 v[96:99], v[48:51], v[216:219], v[96:99]\ns_add_u32 s54, s51, 32768\ns_cmp_eq_u32 s54, 163840\nv_mfma_f32_16x16x32_bf16 v[100:103], v[48:51], v[220:223], v[100:103]\n"
"s_cselect_b32 s54, 0, s54\ns_sub_u32 s54, s54, s51\nds_read_b128 v[48:51], v34 offset:24576\ns_waitcnt lgkmcnt(3)\n"
"v_mfma_f32_16x16x32_bf16 v[104:107], v[52:55], v[216:219], v[104:107]\nv_mfma_f32_16x16x32_bf16 v[108:111], v[52:55], v[220:223], v[108:111]\nds_read_b128 v[52:55], v35 offset:24576\ns_waitcnt lgkmcnt(3)\n"
"v_mfma_f32_16x16x32_bf16 v[96:99], v[40:43], v[224:227], v[96:99]\nv_add_u32 v32, s54, v32\nv_mfma_f32_16x16x32_bf16 v[100:103], v[40:43], v[228:231], v[100:103]\nds_read_b128 v[40:43], v32 offset:0\n"
"s_waitcnt lgkmcnt(3)\nv_mfma_f32_16x16x32_bf16 v[104:107], v[44:47], v[224:227], v[104:107]\nv_add_u32 v33, s54, v33\nv_mfma_f32_16x16x32_bf16 v[108:111], v[44:47], v[228:231], v[108:111]\n"
"ds_read_b128 v[44:47], v33 offset:0\ns_waitcnt lgkmcnt(3)\nv_mfma_f32_16x16x32_bf16 v[96:99], v[48:51], v[232:235], v[96:99]\nv_add_u32 v34, s54, v34\n"
"v_mfma_f32_16x16x32_bf16 v[100:103], v[48:51], v[236:239], v[100:103]\nds_read_b128 v[48:51], v34 offset:0\ns_waitcnt lgkmcnt(3)\nv_mfma_f32_16x16x32_bf16 v[104:107], v[52:55], v[232:235], v[104:107]\n"
"v_add_u32 v35, s54, v35\nv_mfma_f32_16x16x32_bf16 v[108:111], v[52:55], v[236:239], v[108:111]\nds_read_b128 v[52:55], v35 offset:0\ns_mov_b32 s52, s47\n"
"s_add_u32 s47, s51, s32\ns_add_u32 s51, s51, s54\ns_add_u32 s49, s49, 28672\ns_waitcnt vmcnt(12)\n"
"s_barrier\ns_waitcnt lgkmcnt(3)\nv_mfma_f32_16x16x32_bf16 v[64:67], v[40:43], v[128:131], 0\nv_mov_b32 v36, s48\n"
"s_add_u32 s48, s48, 1\ns_nop 0\nbuffer_store_dword v36, v19, s[24:27], s33 offen sc1\nv_mfma_f32_16x16x32_bf16 v[68:71], v[40:43], v[132:135], 0\n"
"ds_read_b128 v[40:43], v32 offset:4096\ns_waitcnt lgkmcnt(3)\nv_mfma_f32_16x16x32_bf16 v[72:75], v[44:47], v[128:131], 0\nv_mfma_f32_16x16x32_bf16 v[76:79], v[44:47], v[132:135], 0\n"
"ds_read_b128 v[44:47], v33 offset:4096\nv_cvt_pk_bf16_f32 v56, v96, v97\ns_waitcnt lgkmcnt(3)\nv_mfma_f32_16x16x32_bf16 v[64:67], v[48:51], v[136:139], v[64:67]\n"
"v_cvt_pk_bf16_f32 v57, v98, v99\nv_mfma_f32_16x16x32_bf16 v[68:71], v[48:51], v[140:143], v[68:71]\nds_read_b128 v[48:51], v34 offset:4096\nv_cvt_pk_bf16_f32 v58, v104, v105\n"
"s_waitcnt lgkmcnt(3)\nv_mfma_f32_16x16x32_bf16 v[72:75], v[52:55], v[136:139], v[72:75]\nv_cvt_pk_bf16_f32 v59, v106, v107\nv_mfma_f32_16x16x32_bf16 v[76:79], v[52:55], v[140:143], v[76:79]\n"
"ds_read_b128 v[52:55], v35 offset:4096\ns_waitcnt lgkmcnt(3)\nv_mfma_f32_16x16x32_bf16 v[64:67], v[40:43], v[144:147], v[64:67]\nv_mfma_f32_16x16x32_bf16 v[68:71], v[40:43], v[148:151], v[68:71]\n"
"ds_read_b128 v[40:43], v32 offset:8192\nbuffer_store_dwordx4 v[56:59], v15, s[20:23], s50 offen sc1\ns_waitcnt lgkmcnt(3)\nv_mfma_f32_16x16x32_bf16 v[72:75], v[44:47], v[144:147], v[72:75]\n"
"v_cvt_pk_bf16_f32 v60, v100, v101\nv_mfma_f32_16x16x32_bf16 v[76:79], v[44:47], v[148:151], v[76:79]\nds_read_b128 v[44:47], v33 offset:8192\nv_cvt_pk_bf16_f32 v61, v102, v103\n"
"s_waitcnt lgkmcnt(3)\nv_mfma_f32_16x16x32_bf16 v[64:67], v[48:51], v[152:155], v[64:67]\nv_cvt_pk_bf16_f32 v62, v108, v109\nv_mfma_f32_16x16x32_bf16 v[68:71], v[48:51], v[156:159], v[68:71]\n"
"ds_read_b128 v[48:51], v34 offset:8192\nv_cvt_pk_bf16_f32 v63, v110, v111\ns_waitcnt lgkmcnt(3)\nv_mfma_f32_16x16x32_bf16 v[72:75], v[52:55], v[152:155], v[72:75]\n"
"v_mfma_f32_16x16x32_bf16 v[76:79], v[52:55], v[156:159], v[76:79]\nds_read_b128 v[52:55], v35 offset:8192\ns_waitcnt lgkmcnt(3)\nv_mfma_f32_16x16x32_bf16 v[64:67], v[40:43], v[160:163], v[64:67]\n"
"buffer_store_dwordx4 v[60:63], v16, s[20:23], s50 offen sc1\nv_mfma_f32_16x16x32_bf16 v[68:71], v[40:43], v[164:167], v[68:71]\nds_read_b128 v[40:43], v32 offset:12288\ns_add_u32 s50, s50, s36\n"
"s_waitcnt lgkmcnt(3)\nv_mfma_f32_16x16x32_bf16 v[72:75], v[44:47], v[160:163], v[72:75]\ns_add_u32 m0, s52, 0\nv_mfma_f32_16x16x32_bf16 v[76:79], v[44:47], v[164:167], v[76:79]\n"
"ds_read_b128 v[44:47], v33 offset:12288\ns_add_u32 s53, s49, 0\ns_waitcnt lgkmcnt(3)\nv_mfma_f32_16x16x32_bf16 v[64:67], v[48:51], v[168:171], v[64:67]\n"
"buffer_load_dwordx4 v10, s[12:15], s53 offen lds\nv_mfma_f32_16x16x32_bf16 v[68:71], v[48:51], v[172:175], v[68:71]\nds_read_b128 v[48:51], v34 offset:12288\ns_add_u32 m0, s52, 4096\n"
"s_waitcnt lgkmcnt(3)\nv_mfma_f32_16x16x32_bf16 v[72:75], v[52:55], v[168:171], v[72:75]\ns_add_u32 s53, s49, 128\nv_mfma_f32_16x16x32_bf16 v[76:79], v[52:55], v[172:175], v[76:79]\n"
"ds_read_b128 v[52:55], v35 offset:12288\nbuffer_load_dwordx4 v10, s[12:15], s53 offen lds\ns_waitcnt lgkmcnt(3)\nv_mfma_f32_16x16x32_bf16 v[64:67], v[40:43], v[176:179], v[64:67]\n"
"s_add_u32 m0, s52, 8192\nv_mfma_f32_16x16x32_bf16 v[68:71], v[40:43], v[180:183], v[68:71]\nds_read_b128 v[40:43], v32 offset:16384\ns_add_u32 s53, s49, 256\n"
"s_waitcnt lgkmcnt(3)\nv_mfma_f32_16x16x32_bf16 v[72:75], v[44:47], v[176:179], v[72:75]\nbuffer_load_dwordx4 v10, s[12:15], s53 offen lds\nv_mfma_f32_16x16x32_bf16 v[76:79], v[44:47], v[180:183], v[76:79]\n"
"ds_read_b128 v[44:47], v33 offset:16384\ns_add_u32 m0, s52, 12288\ns_waitcnt lgkmcnt(3)\nv_mfma_f32_16x16x32_bf16 v[64:67], v[48:51], v[184:187], v[64:67]\n"
"s_add_u32 s53, s49, 384\nv_mfma_f32_16x16x32_bf16 v[68:71], v[48:51], v[188:191], v[68:71]\nds_read_b128 v[48:51], v34 offset:16384\nbuffer_load_dwordx4 v10, s[12:15], s53 offen lds\n"
"s_waitcnt lgkmcnt(3)\nv_mfma_f32_16x16x32_bf16 v[72:75], v[52:55], v[184:187], v[72:75]\ns_add_u32 m0, s52, 16384\nv_mfma_f32_16x16x32_bf16 v[76:79], v[52:55], v[188:191], v[76:79]\n"
"ds_read_b128 v[52:55], v35 offset:16384\ns_add_u32 s53, s49, 512\ns_waitcnt lgkmcnt(3)\nv_mfma_f32_16x16x32_bf16 v[64:67], v[40:43], v[192:195], v[64:67]\n"
"buffer_load_dwordx4 v10, s[12:15], s53 offen lds\nv_mfma_f32_16x16x32_bf16 v[68:71], v[40:43], v[196:199], v[68:71]\nds_read_b128 v[40:43], v32 offset:20480\ns_add_u32 m0, s52, 20480\n"
"s_waitcnt lgkmcnt(3)\nv_mfma_f32_16x16x32_bf16 v[72:75], v[44:47], v[192:195], v[72:75]\ns_add_u32 s53, s49, 640\nv_mfma_f32_16x16x32_bf16 v[76:79], v[44:47], v[196:199], v[76:79]\n"
"ds_read_b128 v[44:47], v33 offset:20480\nbuffer_load_dwordx4 v10, s[12:15], s53 offen lds\ns_waitcnt lgkmcnt(3)\nv_mfma_f32_16x16x32_bf16 v[64:67], v[48:51], v[200:203], v[64:67]\n"
"s_add_u32 m0, s52, 24576\nv_mfma_f32_16x16x32_bf16 v[68:71], v[48:51], v[204:207], v[68:71]\nds_read_b128 v[48:51], v34 offset:20480\ns_add_u32 s53, s49, 768\n"
"s_waitcnt lgkmcnt(3)\nv_mfma_f32_16x16x32_bf16 v[72:75], v[52:55], v[200:203], v[72:75]\nbuffer_load_dwordx4 v10, s[12:15], s53 offen lds\nv_mfma_f32_16x16x32_bf16 v[76:79], v[52:55], v[204:207], v[76:79]\n"
"ds_read_b128 v[52:55], v35 offset:20480\ns_mov_b32 s53, 10240\nbuffer_load_dwordx4 a[80:83], v14, s[16:19], s53 offen sc1\ns_waitcnt lgkmcnt(3)\n"
"v_mfma_f32_16x16x32_bf16 v[64:67], v[40:43], v[208:211], v[64:67]\ns_mov_b32 s53, 239616\nbuffer_load_dwordx4 a[84:87], v14, s[16:19], s53 offen sc1\nv_mfma_f32_16x16x32_bf16 v[68:71], v[40:43], v[212:215], v[68:71]\n"
"ds_read_b128 v[40:43], v32 offset:24576\ns_mov_b32 s53, 11264\nbuffer_load_dwordx4 a[88:91], v14, s[16:19], s53 offen sc1\ns_waitcnt lgkmcnt(3)\n"
"v_mfma_f32_16x16x32_bf16 v[72:75], v[44:47], v[208:211], v[72:75]\ns_mov_b32 s53, 240640\nbuffer_load_dwordx4 a[92:95], v14, s[16:19], s53 offen sc1\nv_mfma_f32_16x16x32_bf16 v[76:79], v[44:47], v[212:215], v[76:79]\n"
"ds_read_b128 v[44:47], v33 offset:24576\ns_mov_b32 s53, 12288\nbuffer_load_dwordx4 a[96:99], v14, s[16:19], s53 offen sc1\ns_waitcnt lgkmcnt(3)\n"
"v_mfma_f32_16x16x32_bf16 v[64:67], v[48:51], v[216:219], v[64:67]\ns_add_u32 s54, s51, 32768\ns_cmp_eq_u32 s54, 163840\nv_mfma_f32_16x16x32_bf16 v[68:71], v[48:51], v[220:223], v[68:71]\n"
"s_cselect_b32 s54, 0, s54\ns_sub_u32 s54, s54, s51\nds_read_b128 v[48:51], v34 offset:24576\ns_waitcnt lgkmcnt(3)\n"
"v_mfma_f32_16x16x32_bf16 v[72:75], v[52:55], v[216:219], v[72:75]\nv_mfma_f32_16x16x32_bf16 v[76:79], v[52:55], v[220:223], v[76:79]\nds_read_b128 v[52:55], v35 offset:24576\ns_waitcnt lgkmcnt(3)\n"
"v_mfma_f32_16x16x32_bf16 v[64:67], v[40:43], v[224:227], v[64:67]\nv_add_u32 v32, s54, v32\nv_mfma_f32_16x16x32_bf16 v[68:71], v[40:43], v[228:231], v[68:71]\nds_read_b128 v[40:43], v32 offset:0\n"
"s_waitcnt lgkmcnt(3)\nv_mfma_f32_16x16x32_bf16 v[72:75], v[44:47], v[224:227], v[72:75]\nv_add_u32 v33, s54, v33\nv_mfma_f32_16x16x32_bf16 v[76:79], v[44:47], v[228:231], v[76:79]\n"
"ds_read_b128 v[44:47], v33 offset:0\ns_waitcnt lgkmcnt(3)\nv_mfma_f32_16x16x32_bf16 v[64:67], v[48:51], v[232:235], v[64:67]\nv_add_u32 v34, s54, v34\n"
"v_mfma_f32_16x16x32_bf16 v[68:71], v[48:51], v[236:239], v[68:71]\nds_read_b128 v[48:51], v34 offset:0\ns_waitcnt lgkmcnt(3)\nv_mfma_f32_16x16x32_bf16 v[72:75], v[52:55], v[232:235], v[72:75]\n"
"v_add_u32 v35, s54, v35\nv_mfma_f32_16x16x32_bf16 v[76:79], v[52:55], v[236:239], v[76:79]\nds_read_b128 v[52:55], v35 offset:0\ns_mov_b32 s52, s47\n"
"s_add_u32 s47, s51, s32\ns_add_u32 s51, s51, s54\ns_add_u32 s49, s49, 28672\ns_waitcnt vmcnt(12)\n"
"s_barrier\ns_waitcnt lgkmcnt(3)\nv_mfma_f32_16x16x32_bf16 v[96:99], v[40:43], v[128:131], 0\nv_mov_b32 v36, s48\n"
"s_add_u32 s48, s48, 1\ns_nop 0\nbuffer_store_dword v36, v19, s[24:27], s33 offen sc1\nv_mfma_f32_16x16x32_bf16 v[100:103], v[40:43], v[132:135], 0\n"
"ds_read_b128 v[40:43], v32 offset:4096\ns_waitcnt lgkmcnt(3)\nv_mfma_f32_16x16x32_bf16 v[104:107], v[44:47], v[128:131], 0\nv_mfma_f32_16x16x32_bf16 v[108:111], v[44:47], v[132:135], 0\n"
"ds_read_b128 v[44:47], v33 offset:4096\nv_cvt_pk_bf16_f32 v56, v64, v65\ns_waitcnt lgkmcnt(3)\nv_mfma_f32_16x16x32_bf16 v[96:99], v[48:51], v[136:139], v[96:99]\n"
"v_cvt_pk_bf16_f32 v57, v66, v67\nv_mfma_f32_16x16x32_bf16 v[100:103], v[48:51], v[140:143], v[100:103]\nds_read_b128 v[48:51], v34 offset:4096\nv_cvt_pk_bf16_f32 v58, v72, v73\n"
"s_waitcnt lgkmcnt(3)\nv_mfma_f32_16x16x32_bf16 v[104:107], v[52:55], v[136:139], v[104:107]\nv_cvt_pk_bf16_f32 v59, v74, v75\nv_mfma_f32_16x16x32_bf16 v[108:111], v[52:55], v[140:143], v[108:111]\n"
"ds_read_b128 v[52:55], v35 offset:4096\ns_waitcnt lgkmcnt(3)\nv_mfma_f32_16x16x32_bf16 v[96:99], v[40:43], v[144:147], v[96:99]\nv_mfma_f32_16x16x32_bf16 v[100:103], v[40:43], v[148:151], v[100:103]\n"
"ds_read_b128 v[40:43], v32 offset:8192\nbuffer_store_dwordx4 v[56:59], v15, s[20:23], s50 offen sc1\ns_waitcnt lgkmcnt(3)\nv_mfma_f32_16x16x32_bf16 v[104:107], v[44:47], v[144:147], v[104:107]\n"
"v_cvt_pk_bf16_f32 v60, v68, v69\nv_mfma_f32_16x16x32_bf16 v[108:111], v[44:47], v[148:151], v[108:111]\nds_read_b128 v[44:47], v33 offset:8192\nv_cvt_pk_bf16_f32 v61, v70, v71\n"
"s_waitcnt lgkmcnt(3)\nv_mfma_f32_16x16x32_bf16 v[96:99], v[48:51], v[152:155], v[96:99]\nv_cvt_pk_bf16_f32 v62, v76, v77\nv_mfma_f32_16x16x32_bf16 v[100:103], v[48:51], v[156:159], v[100:103]\n"
"ds_read_b128 v[48:51], v34 offset:8192\nv_cvt_pk_bf16_f32 v63, v78, v79\ns_waitcnt lgkmcnt(3)\nv_mfma_f32_16x16x32_bf16 v[104:107], v[52:55], v[152:155], v[104:107]\n"
"v_mfma_f32_16x16x32_bf16 v[108:111], v[52:55], v[156:159], v[108:111]\nds_read_b128 v[52:55], v35 offset:8192\ns_waitcnt lgkmcnt(3)\nv_mfma_f32_16x16x32_bf16 v[96:99], v[40:43], v[160:163], v[96:99]\n"
"buffer_store_dwordx4 v[60:63], v16, s[20:23], s50 offen sc1\nv_mfma_f32_16x16x32_bf16 v[100:103], v[40:43], v[164:167], v[100:103]\nds_read_b128 v[40:43], v32 offset:12288\ns_add_u32 s50, s50, s36\n"
"s_waitcnt lgkmcnt(3)\nv_mfma_f32_16x16x32_bf16 v[104:107], v[44:47], v[160:163], v[104:107]\ns_add_u32 m0, s52, 0\nv_mfma_f32_16x16x32_bf16 v[108:111], v[44:47], v[164:167], v[108:111]\n"
"ds_read_b128 v[44:47], v33 offset:12288\ns_add_u32 s53, s49, 0\ns_waitcnt lgkmcnt(3)\nv_mfma_f32_16x16x32_bf16 v[96:99], v[48:51], v[168:171], v[96:99]\n"
"buffer_load_dwordx4 v10, s[12:15], s53 offen lds\nv_mfma_f32_16x16x32_bf16 v[100:103], v[48:51], v[172:175], v[100:103]\nds_read_b128 v[48:51], v34 offset:12288\ns_add_u32 m0, s52, 4096\n"
"s_waitcnt lgkmcnt(3)\nv_mfma_f32_16x16x32_bf16 v[104:107], v[52:55], v[168:171], v[104:107]\ns_add_u32 s53, s49, 128\nv_mfma_f32_16x16x32_bf16 v[108:111], v[52:55], v[172:175], v[108:111]\n"
"ds_read_b128 v[52:55], v35 offset:12288\nbuffer_load_dwordx4 v10, s[12:15], s53 offen lds\ns_waitcnt lgkmcnt(3)\nv_mfma_f32_16x16x32_bf16 v[96:99], v[40:43], v[176:179], v[96:99]\n"
"s_add_u32 m0, s52, 8192\nv_mfma_f32_16x16x32_bf16 v[100:103], v[40:43], v[180:183], v[100:103]\nds_read_b128 v[40:43], v32 offset:16384\ns_add_u32 s53, s49, 256\n"
"s_waitcnt lgkmcnt(3)\nv_mfma_f32_16x16x32_bf16 v[104:107], v[44:47], v[176:179], v[104:107]\nbuffer_load_dwordx4 v10, s[12:15], s53 offen lds\nv_mfma_f32_16x16x32_bf16 v[108:111], v[44:47], v[180:183], v[108:111]\n"
"ds_read_b128 v[44:47], v33 offset:16384\ns_add_u32 m0, s52, 12288\ns_waitcnt lgkmcnt(3)\nv_mfma_f32_16x16x32_bf16 v[96:99], v[48:51], v[184:187], v[96:99]\n"
"s_add_u32 s53, s49, 384\nv_mfma_f32_16x16x32_bf16 v[100:103], v[48:51], v[188:191], v[100:103]\nds_read_b128 v[48:51], v34 offset:16384\nbuffer_load_dwordx4 v10, s[12:15], s53 offen lds\n"
"s_waitcnt lgkmcnt(3)\nv_mfma_f32_16x16x32_bf16 v[104:107], v[52:55], v[184:187], v[104:107]\ns_add_u32 m0, s52, 16384\nv_mfma_f32_16x16x32_bf16 v[108:111], v[52:55], v[188:191], v[108:111]\n"
"ds_read_b128 v[52:55], v35 offset:16384\ns_add_u32 s53, s49, 512\ns_waitcnt lgkmcnt(3)\nv_mfma_f32_16x16x32_bf16 v[96:99], v[40:43], v[192:195], v[96:99]\n"
"buffer_load_dwordx4 v10, s[12:15], s53 offen lds\nv_mfma_f32_16x16x32_bf16 v[100:103], v[40:43], v[196:199], v[100:103]\nds_read_b128 v[40:43], v32 offset:20480\ns_add_u32 m0, s52, 20480\n"
"s_waitcnt lgkmcnt(3)\nv_mfma_f32_16x16x32_bf16 v[104:107], v[44:47], v[192:195], v[104:107]\ns_add_u32 s53, s49, 640\nv_mfma_f32_16x16x32_bf16 v[108:111], v[44:47], v[196:199], v[108:111]\n"
"ds_read_b128 v[44:47], v33 offset:20480\nbuffer_load_dwordx4 v10, s[12:15], s53 offen lds\ns_waitcnt lgkmcnt(3)\nv_mfma_f32_16x16x32_bf16 v[96:99], v[48:51], v[200:203], v[96:99]\n"
"s_add_u32 m0, s52, 24576\nv_mfma_f32_16x16x32_bf16 v[100:103], v[48:51], v[204:207], v[100:103]\nds_read_b128 v[48:51], v34 offset:20480\ns_add_u32 s53, s49, 768\n"
"s_waitcnt lgkmcnt(3)\nv_mfma_f32_16x16x32_bf16 v[104:107], v[52:55], v[200:203], v[104:107]\nbuffer_load_dwordx4 v10, s[12:15], s53 offen lds\nv_mfma_f32_16x16x32_bf16 v[108:111], v[52:55], v[204:207], v[108:111]\n"
"ds_read_b128 v[52:55], v35 offset:20480\ns_mov_b32 s53, 241664\nbuffer_load_dwordx4 a[100:103], v14, s[16:19], s53 offen sc1\ns_waitcnt lgkmcnt(3)\n"
"v_mfma_f32_16x16x32_bf16 v[96:99], v[40:43], v[208:211], v[96:99]\ns_mov_b32 s53, 13312\nbuffer_load_dwordx4 a[104:107], v14, s[16:19], s53 offen sc1\nv_mfma_f32_16x16x32_bf16 v[100:103], v[40:43], v[212:215], v[100:103]\n"
"ds_read_b128 v[40:43], v32 offset:24576\ns_mov_b32 s53, 242688\nbuffer_load_dwordx4 a[108:111], v14, s[16:19], s53 offen sc1\ns_waitcnt lgkmcnt(3)\n"
"v_mfma_f32_16x16x32_bf16 v[104:107], v[44:47], v[208:211], v[104:107]\nv_mfma_f32_16x16x32_bf16 v[108:111], v[44:47], v[212:215], v[108:111]\nds_read_b128 v[44:47], v33 offset:24576\ns_waitcnt lgkmcnt(3)\n"
"v_mfma_f32_16x16x32_bf16 v[96:99], v[48:51], v[216:219], v[96:99]\ns_add_u32 s54, s51, 32768\ns_cmp_eq_u32 s54, 163840\nv_mfma_f32_16x16x32_bf16 v[100:103], v[48:51], v[220:223], v[100:103]\n"
"s_cselect_b32 s54, 0, s54\ns_sub_u32 s54, s54, s51\nds_read_b128 v[48:51], v34 offset:24576\ns_waitcnt lgkmcnt(3)\n"
"v_mfma_f32_16x16x32_bf16 v[104:107], v[52:55], v[216:219], v[104:107]\nv_mfma_f32_16x16x32_bf16 v[108:111], v[52:55], v[220:223], v[108:111]\nds_read_b128 v[52:55], v35 offset:24576\ns_waitcnt lgkmcnt(3)\n"
"v_mfma_f32_16x16x32_bf16 v[96:99], v[40:43], v[224:227], v[96:99]\nv_add_u32 v32, s54, v32\nv_mfma_f32_16x16x32_bf16 v[100:103], v[40:43], v[228:231], v[100:103]\nds_read_b128 v[40:43], v32 offset:0\n"
"s_waitcnt lgkmcnt(3)\nv_mfma_f32_16x16x32_bf16 v[104:107], v[44:47], v[224:227], v[104:107]\nv_add_u32 v33, s54, v33\nv_mfma_f32_16x16x32_bf16 v[108:111], v[44:47], v[228:231], v[108:111]\n"
"ds_read_b128 v[44:47], v33 offset:0\ns_waitcnt lgkmcnt(3)\nv_mfma_f32_16x16x32_bf16 v[96:99], v[48:51], v[232:235], v[96:99]\nv_add_u32 v34, s54, v34\n"
"v_mfma_f32_16x16x32_bf16 v[100:103], v[48:51], v[236:239], v[100:103]\nds_read_b128 v[48:51], v34 offset:0\ns_waitcnt lgkmcnt(3)\nv_mfma_f32_16x16x32_bf16 v[104:107], v[52:55], v[232:235], v[104:107]\n"
"v_add_u32 v35, s54, v35\nv_mfma_f32_16x16x32_bf16 v[108:111], v[52:55], v[236:239], v[108:111]\nds_read_b128 v[52:55], v35 offset:0\ns_mov_b32 s52, s47\n"
"s_add_u32 s47, s51, s32\ns_add_u32 s51, s51, s54\ns_add_u32 s49, s49, 28672\ns_cmp_lg_u32 s34, 0\n"
"s_cbranch_scc1 fg1_stay\ns_nop 7\nv_cvt_pk_bf16_f32 v56, v96, v97\nv_cvt_pk_bf16_f32 v57, v98, v99\n"
"v_cvt_pk_bf16_f32 v58, v104, v105\nv_cvt_pk_bf16_f32 v59, v106, v107\ns_nop 0\ns_nop 0\n"
"buffer_store_dwordx4 v[56:59], v15, s[20:23], s50 offen sc1\nv_cvt_pk_bf16_f32 v60, v100, v101\nv_cvt_pk_bf16_f32 v61, v102, v103\nv_cvt_pk_bf16_f32 v62, v108, v109\n"
"v_cvt_pk_bf16_f32 v63, v110, v111\ns_nop 0\ns_nop 0\nbuffer_store_dwordx4 v[60:63], v16, s[20:23], s50 offen sc1\n"
"s_add_u32 s48, s28, 64\nv_mov_b32 v36, s48\ns_waitcnt vmcnt(0) lgkmcnt(0)\ns_barrier\n"
"buffer_store_dword v36, v19, s[24:27], s33 offen sc1\ns_branch fg1_end\nfg1_stay:\ns_waitcnt vmcnt(0)\n"
"s_barrier\ns_waitcnt lgkmcnt(3)\nv_mfma_f32_16x16x32_bf16 v[64:67], v[40:43], v[128:131], 0\nv_mov_b32 v36, s48\n"
"s_add_u32 s48, s48, 1\ns_nop 0\nbuffer_store_dword v36, v19, s[24:27], s33 offen sc1\nv_mfma_f32_16x16x32_bf16 v[68:71], v[40:43], v[132:135], 0\n"
"v_mfma_f32_16x16x32_bf16 v[72:75], v[40:43], a[0:3], 0\nv_mfma_f32_16x16x32_bf16 v[76:79], v[40:43], a[4:7], 0\nds_read_b128 v[40:43], v32 offset:4096\nv_cvt_pk_bf16_f32 v56, v96, v97\n"
"s_waitcnt lgkmcnt(3)\nv_mfma_f32_16x16x32_bf16 v[80:83], v[44:47], v[128:131], 0\nv_cvt_pk_bf16_f32 v57, v98, v99\nv_mfma_f32_16x16x32_bf16 v[84:87], v[44:47], v[132:135], 0\n"
"v_cvt_pk_bf16_f32 v58, v104, v105\nv_mfma_f32_16x16x32_bf16 v[88:91], v[44:47], a[0:3], 0\nv_cvt_pk_bf16_f32 v59, v106, v107\nv_mfma_f32_16x16x32_bf16 v[92:95], v[44:47], a[4:7], 0\n"
"ds_read_b128 v[44:47], v33 offset:4096\ns_waitcnt lgkmcnt(3)\nv_mfma_f32_16x16x32_bf16 v[64:67], v[48:51], v[136:139], v[64:67]\nv_mfma_f32_16x16x32_bf16 v[68:71], v[48:51], v[140:143], v[68:71]\n"
"buffer_store_dwordx4 v[56:59], v15, s[20:23], s50 offen sc1\nv_mfma_f32_16x16x32_bf16 v[72:75], v[48:51], a[8:11], v[72:75]\nv_cvt_pk_bf16_f32 v60, v100, v101\nv_mfma_f32_16x16x32_bf16 v[76:79], v[48:51], a[12:15], v[76:79]\n"
"ds_read_b128 v[48:51], v34 offset:4096\nv_cvt_pk_bf16_f32 v61, v102, v103\ns_waitcnt lgkmcnt(3)\nv_mfma_f32_16x16x32_bf16 v[80:83], v[52:55], v[136:139], v[80:83]\n"
"v_cvt_pk_bf16_f32 v62, v108, v109\nv_mfma_f32_16x16x32_bf16 v[84:87], v[52:55], v[140:143], v[84:87]\nv_cvt_pk_bf16_f32 v63, v110, v111\nv_mfma_f32_16x16x32_bf16 v[88:91], v[52:55], a[8:11], v[88:91]\n"
"v_mfma_f32_16x16x32_bf16 v[92:95], v[52:55], a[12:15], v[92:95]\nds_read_b128 v[52:55], v35 offset:4096\ns_waitcnt lgkmcnt(3)\nv_mfma_f32_16x16x32_bf16 v[64:67], v[40:43], v[144:147], v[64:67]\n"
"buffer_store_dwordx4 v[60:63], v16, s[20:23], s50 offen sc1\nv_mfma_f32_16x16x32_bf16 v[68:71], v[40:43], v[148:151], v[68:71]\ns_add_u32 s50, s50, s36\nv_mfma_f32_16x16x32_bf16 v[72:75], v[40:43], a[16:19], v[72:75]\n"
"s_add_u32 m0, s52, 0\nv_mfma_f32_16x16x32_bf16 v[76:79], v[40:43], a[20:23], v[76:79]\nds_read_b128 v[40:43], v32 offset:8192\ns_add_u32 s53, s49, 0\n"
"s_waitcnt lgkmcnt(3)\nv_mfma_f32_16x16x32_bf16 v[80:83], v[44:47], v[144:147], v[80:83]\nbuffer_load_dwordx4 v10, s[12:15], s53 offen lds\nv_mfma_f32_16x16x32_bf16 v[84:87], v[44:47], v[148:151], v[84:87]\n"
"v_mfma_f32_16x16x32_bf16 v[88:91], v[44:47], a[16:19], v[88:91]\nv_mfma_f32_16x16x32_bf16 v[92:95], v[44:47], a[20:23], v[92:95]\nds_read_b128 v[44:47], v33 offset:8192\ns_waitcnt lgkmcnt(3)\n"
"v_mfma_f32_16x16x32_bf16 v[64:67], v[48:51], v[152:155], v[64:67]\nv_mfma_f32_16x16x32_bf16 v[68:71], v[48:51], v[156:159], v[68:71]\nv_mfma_f32_16x16x32_bf16 v[72:75], v[48:51], a[24:27], v[72:75]\nv_mfma_f32_16x16x32_bf16 v[76:79], v[48:51], a[28:31], v[76:79]\n"
"ds_read_b128 v[48:51], v34 offset:8192\ns_waitcnt lgkmcnt(3)\nv_mfma_f32_16x16x32_bf16 v[80:83], v[52:55], v[152:155], v[80:83]\nv_mfma_f32_16x16x32_bf16 v[84:87], v[52:55], v[156:159], v[84:87]\n"
"s_add_u32 m0, s52, 4096\nv_mfma_f32_16x16x32_bf16 v[88:91], v[52:55], a[24:27], v[88:91]\ns_add_u32 s53, s49, 128\nv_mfma_f32_16x16x32_bf16 v[92:95], v[52:55], a[28:31], v[92:95]\n"
"ds_read_b128 v[52:55], v35 offset:8192\nbuffer_load_dwordx4 v10, s[12:15], s53 offen lds\ns_waitcnt lgkmcnt(3)\nv_mfma_f32_16x16x32_bf16 v[64:67], v[40:43], v[160:163], v[64:67]\n"
"v_mfma_f32_16x16x32_bf16 v[68:71], v[40:43], v[164:167], v[68:71]\nv_mfma_f32_16x16x32_bf16 v[72:75], v[40:43], a[32:35], v[72:75]\nv_mfma_f32_16x16x32_bf16 v[76:79], v[40:43], a[36:39], v[76:79]\nds_read_b128 v[40:43], v32 offset:12288\n"
"s_waitcnt lgkmcnt(3)\nv_mfma_f32_16x16x32_bf16 v[80:83], v[44:47], v[160:163], v[80:83]\nv_mfma_f32_16x16x32_bf16 v[84:87], v[44:47], v[164:167], v[84:87]\nv_mfma_f32_16x16x32_bf16 v[88:91], v[44:47], a[32:35], v[88:91]\n"
"v_mfma_f32_16x16x32_bf16 v[92:95], v[44:47], a[36:39], v[92:95]\nds_read_b128 v[44:47], v33 offset:12288\ns_waitcnt lgkmcnt(3)\nv_mfma_f32_16x16x32_bf16 v[64:67], v[48:51], v[168:171], v[64:67]\n"
"s_add_u32 m0, s52, 8192\nv_mfma_f32_16x16x32_bf16 v[68:71], v[48:51], v[172:175], v[68:71]\ns_add_u32 s53, s49, 256\nv_mfma_f32_16x16x32_bf16 v[72:75], v[48:51], a[40:43], v[72:75]\n"
"buffer_load_dwordx4 v10, s[12:15], s53 offen lds\nv_mfma_f32_16x16x32_bf16 v[76:79], v[48:51], a[44:47], v[76:79]\nds_read_b128 v[48:51], v34 offset:12288\ns_waitcnt lgkmcnt(3)\n"
"v_mfma_f32_16x16x32_bf16 v[80:83], v[52:55], v[168:171], v[80:83]\nv_mfma_f32_16x16x32_bf16 v[84:87], v[52:55], v[172:175], v[84:87]\nv_mfma_f32_16x16x32_bf16 v[88:91], v[52:55], a[40:43], v[88:91]\nv_mfma_f32_16x16x32_bf16 v[92:95], v[52:55], a[44:47], v[92:95]\n"
"ds_read_b128 v[52:55], v35 offset:12288\ns_waitcnt lgkmcnt(3)\nv_mfma_f32_16x16x32_bf16 v[64:67], v[40:43], v[176:179], v[64:67]\nv_mfma_f32_16x16x32_bf16 v[68:71], v[40:43], v[180:183], v[68:71]\n"
"v_mfma_f32_16x16x32_bf16 v[72:75], v[40:43], a[48:51], v[72:75]\nv_mfma_f32_16x16x32_bf16 v[76:79], v[40:43], a[52:55], v[76:79]\nds_read_b128 v[40:43], v32 offset:16384\ns_add_u32 m0, s52, 12288\n"
"s_waitcnt lgkmcnt(3)\nv_mfma_f32_16x16x32_bf16 v[80:83], v[44:47], v[176:179], v[80:83]\ns_add_u32 s53, s49, 384\nv_mfma_f32_16x16x32_bf16 v[84:87], v[44:47], v[180:183], v[84:87]\n"
"buffer_load_dwordx4 v10, s[12:15], s53 offen lds\nv_mfma_f32_16x16x32_bf16 v[88:91], v[44:47], a[48:51], v[88:91]\nv_mfma_f32_16x16x32_bf16 v[92:95], v[44:47], a[52:55], v[92:95]\nds_read_b128 v[44:47], v33 offset:16384\n"
"s_waitcnt lgkmcnt(3)\nv_mfma_f32_16x16x32_bf16 v[64:67], v[48:51], v[184:187], v[64:67]\nv_mfma_f32_16x16x32_bf16 v[68:71], v[48:51], v[188:191], v[68:71]\nv_mfma_f32_16x16x32_bf16 v[72:75], v[48:51], a[56:59], v[72:75]\n"
"v_mfma_f32_16x16x32_bf16 v[76:79], v[48:51], a[60:63], v[76:79]\nds_read_b128 v[48:51], v34 offset:16384\ns_waitcnt lgkmcnt(3)\nv_mfma_f32_16x16x32_bf16 v[80:83], v[52:55], v[184:187], v[80:83]\n"
"v_mfma_f32_16x16x32_bf16 v[84:87], v[52:55], v[188:191], v[84:87]\nv_mfma_f32_16x16x32_bf16 v[88:91], v[52:55], a[56:59], v[88:91]\ns_add_u32 m0, s52, 16384\nv_mfma_f32_16x16x32_bf16 v[92:95], v[52:55], a[60:63], v[92:95]\n"
"ds_read_b128 v[52:55], v35 offset:16384\ns_add_u32 s53, s49, 512\ns_waitcnt lgkmcnt(3)\nv_mfma_f32_16x16x32_bf16 v[64:67], v[40:43], v[192:195], v[64:67]\n"
"buffer_load_dwordx4 v10, s[12:15], s53 offen lds\nv_mfma_f32_16x16x32_bf16 v[68:71], v[40:43], v[196:199], v[68:71]\nv_mfma_f32_16x16x32_bf16 v[72:75], v[40:43], a[64:67], v[72:75]\nv_mfma_f32_16x16x32_bf16 v[76:79], v[40:43], a[68:71], v[76:79]\n"
"ds_read_b128 v[40:43], v32 offset:20480\ns_waitcnt lgkmcnt(3)\nv_mfma_f32_16x16x32_bf16 v[80:83], v[44:47], v[192:195], v[80:83]\nv_mfma_f32_16x16x32_bf16 v[84:87], v[44:47], v[196:199], v[84:87]\n"
"v_mfma_f32_16x16x32_bf16 v[88:91], v[44:47], a[64:67], v[88:91]\nv_mfma_f32_16x16x32_bf16 v[92:95], v[44:47], a[68:71], v[92:95]\nds_read_b128 v[44:47], v33 offset:20480\ns_waitcnt lgkmcnt(3)\n"
"v_mfma_f32_16x16x32_bf16 v[64:67], v[48:51], v[200:203], v[64:67]\nv_mfma_f32_16x16x32_bf16 v[68:71], v[48:51], v[204:207], v[68:71]\ns_add_u32 m0, s52, 20480\nv_mfma_f32_16x16x32_bf16 v[72:75], v[48:51], a[72:75], v[72:75]\n"
"s_add_u32 s53, s49, 640\nv_mfma_f32_16x16x32_bf16 v[76:79], v[48:51], a[76:79], v[76:79]\nds_read_b128 v[48:51], v34 offset:20480\nbuffer_load_dwordx4 v10, s[12:15], s53 offen lds\n"
"s_waitcnt lgkmcnt(3)\nv_mfma_f32_16x16x32_bf16 v[80:83], v[52:55], v[200:203], v[80:83]\nv_mfma_f32_16x16x32_bf16 v[84:87], v[52:55], v[204:207], v[84:87]\nv_mfma_f32_16x16x32_bf16 v[88:91], v[52:55], a[72:75], v[88:91]\n"
"v_mfma_f32_16x16x32_bf16 v[92:95], v[52:55], a[76:79], v[92:95]\nds_read_b128 v[52:55], v35 offset:20480\ns_waitcnt lgkmcnt(3)\nv_mfma_f32_16x16x32_bf16 v[64:67], v[40:43], v[208:211], v[64:67]\n"
"v_mfma_f32_16x16x32_bf16 v[68:71], v[40:43], v[212:215], v[68:71]\nv_mfma_f32_16x16x32_bf16 v[72:75], v[40:43], a[80:83], v[72:75]\nv_mfma_f32_16x16x32_bf16 v[76:79], v[40:43], a[84:87], v[76:79]\nds_read_b128 v[40:43], v32 offset:24576\n"
"s_waitcnt lgkmcnt(3)\nv_mfma_f32_16x16x32_bf16 v[80:83], v[44:47], v[208:211], v[80:83]\ns_add_u32 m0, s52, 24576\nv_mfma_f32_16x16x32_bf16 v[84:87], v[44:47], v[212:215], v[84:87]\n"
"s_add_u32 s53, s49, 768\nv_mfma_f32_16x16x32_bf16 v[88:91], v[44:47], a[80:83], v[88:91]\nbuffer_load_dwordx4 v10, s[12:15], s53 offen lds\nv_mfma_f32_16x16x32_bf16 v[92:95], v[44:47], a[84:87], v[92:95]\n"
"ds_read_b128 v[44:47], v33 offset:24576\ns_waitcnt lgkmcnt(3)\nv_mfma_f32_16x16x32_bf16 v[64:67], v[48:51], v[216:219], v[64:67]\ns_add_u32 s54, s51, 32768\n"
"s_cmp_eq_u32 s54, 163840\nv_mfma_f32_16x16x32_bf16 v[68:71], v[48:51], v[220:223], v[68:71]\nv_mfma_f32_16x16x32_bf16 v[72:75], v[48:51], a[88:91], v[72:75]\nv_mfma_f32_16x16x32_bf16 v[76:79], v[48:51], a[92:95], v[76:79]\n"
"s_cselect_b32 s54, 0, s54\ns_sub_u32 s54, s54, s51\nds_read_b128 v[48:51], v34 offset:24576\ns_waitcnt lgkmcnt(3)\n"
"v_mfma_f32_16x16x32_bf16 v[80:83], v[52:55], v[216:219], v[80:83]\nv_mfma_f32_16x16x32_bf16 v[84:87], v[52:55], v[220:223], v[84:87]\nv_mfma_f32_16x16x32_bf16 v[88:91], v[52:55], a[88:91], v[88:91]\nv_mfma_f32_16x16x32_bf16 v[92:95], v[52:55], a[92:95], v[92:95]\n"
"ds_read_b128 v[52:55], v35 offset:24576\ns_waitcnt lgkmcnt(3)\nv_mfma_f32_16x16x32_bf16 v[64:67], v[40:43], v[224:227], v[64:67]\nv_add_u32 v32, s54, v32\n"
"v_mfma_f32_16x16x32_bf16 v[68:71], v[40:43], v[228:231], v[68:71]\nv_mfma_f32_16x16x32_bf16 v[72:75], v[40:43], a[96:99], v[72:75]\nv_mfma_f32_16x16x32_bf16 v[76:79], v[40:43], a[100:103], v[76:79]\nds_read_b128 v[40:43], v32 offset:0\n"
"s_waitcnt lgkmcnt(3)\nv_mfma_f32_16x16x32_bf16 v[80:83], v[44:47], v[224:227], v[80:83]\nv_add_u32 v33, s54, v33\nv_mfma_f32_16x16x32_bf16 v[84:87], v[44:47], v[228:231], v[84:87]\n"
"v_mfma_f32_16x16x32_bf16 v[88:91], v[44:47], a[96:99], v[88:91]\nv_mfma_f32_16x16x32_bf16 v[92:95], v[44:47], a[100:103], v[92:95]\nds_read_b128 v[44:47], v33 offset:0\ns_waitcnt lgkmcnt(3)\n"
"v_mfma_f32_16x16x32_bf16 v[64:67], v[48:51], v[232:235], v[64:67]\nv_add_u32 v34, s54, v34\nv_mfma_f32_16x16x32_bf16 v[68:71], v[48:51], v[236:239], v[68:71]\nv_mfma_f32_16x16x32_bf16 v[72:75], v[48:51], a[104:107], v[72:75]\n"
"v_mfma_f32_16x16x32_bf16 v[76:79], v[48:51], a[108:111], v[76:79]\nds_read_b128 v[48:51], v34 offset:0\ns_waitcnt lgkmcnt(3)\nv_mfma_f32_16x16x32_bf16 v[80:83], v[52:55], v[232:235], v[80:83]\n"
"v_add_u32 v35, s54, v35\nv_mfma_f32_16x16x32_bf16 v[84:87], v[52:55], v[236:239], v[84:87]\nv_mfma_f32_16x16x32_bf16 v[88:91], v[52:55], a[104:107], v[88:91]\nv_mfma_f32_16x16x32_bf16 v[92:95], v[52:55], a[108:111], v[92:95]\n"
"ds_read_b128 v[52:55], v35 offset:0\ns_mov_b32 s52, s47\ns_add_u32 s47, s51, s32\ns_add_u32 s51, s51, s54\n"
"s_add_u32 s49, s49, 28672\ns_waitcnt vmcnt(7)\ns_barrier\ns_waitcnt lgkmcnt(3)\n"
"v_mfma_f32_16x16x32_bf16 v[96:99], v[40:43], v[128:131], 0\nv_mov_b32 v36, s48\ns_add_u32 s48, s48, 1\ns_nop 0\n"
"buffer_store_dword v36, v19, s[24:27], s33 offen sc1\nv_mfma_f32_16x16x32_bf16 v[100:103], v[40:43], v[132:135], 0\nv_mfma_f32_16x16x32_bf16 v[104:107], v[40:43], a[0:3], 0\nv_mfma_f32_16x16x32_bf16 v[108:111], v[40:43], a[4:7], 0\n"
"ds_read_b128 v[40:43], v32 offset:4096\nv_cvt_pk_bf16_f32 v56, v64, v65\ns_waitcnt lgkmcnt(3)\nv_mfma_f32_16x16x32_bf16 v[112:115], v[44:47], v[128:131], 0\n"
"v_cvt_pk_bf16_f32 v57, v66, v67\nv_mfma_f32_16x16x32_bf16 v[116:119], v[44:47], v[132:135], 0\nv_cvt_pk_bf16_f32 v58, v80, v81\nv_mfma_f32_16x16x32_bf16 v[120:123], v[44:47], a[0:3], 0\n"
"v_cvt_pk_bf16_f32 v59, v82, v83\nv_mfma_f32_16x16x32_bf16 v[124:127], v[44:47], a[4:7], 0\nds_read_b128 v[44:47], v33 offset:4096\ns_waitcnt lgkmcnt(3)\n"
"v_mfma_f32_16x16x32_bf16 v[96:99], v[48:51], v[136:139], v[96:99]\nv_mfma_f32_16x16x32_bf16 v[100:103], v[48:51], v[140:143], v[100:103]\nbuffer_store_dwordx4 v[56:59], v15, s[20:23], s50 offen sc1\nv_mfma_f32_16x16x32_bf16 v[104:107], v[48:51], a[8:11], v[104:107]\n"
"v_cvt_pk_bf16_f32 v60, v68, v69\nv_mfma_f32_16x16x32_bf16 v[108:111], v[48:51], a[12:15], v[108:111]\nds_read_b128 v[48:51], v34 offset:4096\nv_cvt_pk_bf16_f32 v61, v70, v71\n"
"s_waitcnt lgkmcnt(3)\nv_mfma_f32_16x16x32_bf16 v[112:115], v[52:55], v[136:139], v[112:115]\nv_cvt_pk_bf16_f32 v62, v84, v85\nv_mfma_f32_16x16x32_bf16 v[116:119], v[52:55], v[140:143], v[116:119]\n"
"v_cvt_pk_bf16_f32 v63, v86, v87\nv_mfma_f32_16x16x32_bf16 v[120:123], v[52:55], a[8:11], v[120:123]\nv_mfma_f32_16x16x32_bf16 v[124:127], v[52:55], a[12:15], v[124:127]\nds_read_b128 v[52:55], v35 offset:4096\n"
"s_waitcnt lgkmcnt(3)\nv_mfma_f32_16x16x32_bf16 v[96:99], v[40:43], v[144:147], v[96:99]\nbuffer_store_dwordx4 v[60:63], v16, s[20:23], s50 offen sc1\nv_mfma_f32_16x16x32_bf16 v[100:103], v[40:43], v[148:151], v[100:103]\n"
"v_cvt_pk_bf16_f32 v56, v72, v73\nv_mfma_f32_16x16x32_bf16 v[104:107], v[40:43], a[16:19], v[104:107]\nv_cvt_pk_bf16_f32 v57, v74, v75\nv_mfma_f32_16x16x32_bf16 v[108:111], v[40:43], a[20:23], v[108:111]\n"
"ds_read_b128 v[40:43], v32 offset:8192\nv_cvt_pk_bf16_f32 v58, v88, v89\ns_waitcnt lgkmcnt(3)\nv_mfma_f32_16x16x32_bf16 v[112:115], v[44:47], v[144:147], v[112:115]\n"
"v_cvt_pk_bf16_f32 v59, v90, v91\nv_mfma_f32_16x16x32_bf16 v[116:119], v[44:47], v[148:151], v[116:119]\nv_mfma_f32_16x16x32_bf16 v[120:123], v[44:47], a[16:19], v[120:123]\nv_mfma_f32_16x16x32_bf16 v[124:127], v[44:47], a[20:23], v[124:127]\n"
"ds_read_b128 v[44:47], v33 offset:8192\nbuffer_store_dwordx4 v[56:59], v17, s[20:23], s50 offen sc1\ns_waitcnt lgkmcnt(3)\nv_mfma_f32_16x16x32_bf16 v[96:99], v[48:51], v[152:155], v[96:99]\n"
"v_cvt_pk_bf16_f32 v60, v76, v77\nv_mfma_f32_16x16x32_bf16 v[100:103], v[48:51], v[156:159], v[100:103]\nv_cvt_pk_bf16_f32 v61, v78, v79\nv_mfma_f32_16x16x32_bf16 v[104:107], v[48:51], a[24:27], v[104:107]\n"
"v_cvt_pk_bf16_f32 v62, v92, v93\nv_mfma_f32_16x16x32_bf16 v[108:111], v[48:51], a[28:31], v[108:111]\nds_read_b128 v[48:51], v34 offset:8192\nv_cvt_pk_bf16_f32 v63, v94, v95\n"
"s_waitcnt lgkmcnt(3)\nv_mfma_f32_16x16x32_bf16 v[112:115], v[52:55], v[152:155], v[112:115]\nv_mfma_f32_16x16x32_bf16 v[116:119], v[52:55], v[156:159], v[116:119]\nv_mfma_f32_16x16x32_bf16 v[120:123], v[52:55], a[24:27], v[120:123]\n"
"buffer_store_dwordx4 v[60:63], v18, s[20:23], s50 offen sc1\nv_mfma_f32_16x16x32_bf16 v[124:127], v[52:55], a[28:31], v[124:127]\nds_read_b128 v[52:55], v35 offset:8192\ns_add_u32 s50, s50, s36\n"
"s_waitcnt lgkmcnt(3)\nv_mfma_f32_16x16x32_bf16 v[96:99], v[40:43], v[160:163], v[96:99]\ns_add_u32 m0, s52, 0\nv_mfma_f32_16x16x32_bf16 v[100:103], v[40:43], v[164:167], v[100:103]\n"
"s_add_u32 s53, s49, 0\nv_mfma_f32_16x16x32_bf16 v[104:107], v[40:43], a[32:35], v[104:107]\nbuffer_load_dwordx4 v10, s[12:15], s53 offen lds\nv_mfma_f32_16x16x32_bf16 v[108:111], v[40:43], a[36:39], v[108:111]\n"
"ds_read_b128 v[40:43], v32 offset:12288\ns_waitcnt lgkmcnt(3)\nv_mfma_f32_16x16x32_bf16 v[112:115], v[44:47], v[160:163], v[112:115]\nv_mfma_f32_16x16x32_bf16 v[116:119], v[44:47], v[164:167], v[116:119]\n"
"v_mfma_f32_16x16x32_bf16 v[120:123], v[44:47], a[32:35], v[120:123]\nv_mfma_f32_16x16x32_bf16 v[124:127], v[44:47], a[36:39], v[124:127]\nds_read_b128 v[44:47], v33 offset:12288\ns_waitcnt lgkmcnt(3)\n"
"v_mfma_f32_16x16x32_bf16 v[96:99], v[48:51], v[168:171], v[96:99]\nv_mfma_f32_16x16x32_bf16 v[100:103], v[48:51], v[172:175], v[100:103]\nv_mfma_f32_16x16x32_bf16 v[104:107], v[48:51], a[40:43], v[104:107]\nv_mfma_f32_16x16x32_bf16 v[108:111], v[48:51], a[44:47], v[108:111]\n"
"ds_read_b128 v[48:51], v34 offset:12288\ns_add_u32 m0, s52, 4096\ns_waitcnt lgkmcnt(3)\nv_mfma_f32_16x16x32_bf16 v[112:115], v[52:55], v[168:171], v[112:115]\n"
"s_add_u32 s53, s49, 128\nv_mfma_f32_16x16x32_bf16 v[116:119], v[52:55], v[172:175], v[116:119]\nbuffer_load_dwordx4 v10, s[12:15], s53 offen lds\nv_mfma_f32_16x16x32_bf16 v[120:123], v[52:55], a[40:43], v[120:123]\n"
"v_mfma_f32_16x16x32_bf16 v[124:127], v[52:55], a[44:47], v[124:127]\nds_read_b128 v[52:55], v35 offset:12288\ns_waitcnt lgkmcnt(3)\nv_mfma_f32_16x16x32_bf16 v[96:99], v[40:43], v[176:179], v[96:99]\n"
"v_mfma_f32_16x16x32_bf16 v[100:103], v[40:43], v[180:183], v[100:103]\nv_mfma_f32_16x16x32_bf16 v[104:107], v[40:43], a[48:51], v[104:107]\nv_mfma_f32_16x16x32_bf16 v[108:111], v[40:43], a[52:55], v[108:111]\nds_read_b128 v[40:43], v32 offset:16384\n"
"s_waitcnt lgkmcnt(3)\nv_mfma_f32_16x16x32_bf16 v[112:115], v[44:47], v[176:179], v[112:115]\nv_mfma_f32_16x16x32_bf16 v[116:119], v[44:47], v[180:183], v[116:119]\nv_mfma_f32_16x16x32_bf16 v[120:123], v[44:47], a[48:51], v[120:123]\n"
"s_add_u32 m0, s52, 8192\nv_mfma_f32_16x16x32_bf16 v[124:127], v[44:47], a[52:55], v[124:127]\nds_read_b128 v[44:47], v33 offset:16384\ns_add_u32 s53, s49, 256\n"
"s_waitcnt lgkmcnt(3)\nv_mfma_f32_16x16x32_bf16 v[96:99], v[48:51], v[184:187], v[96:99]\nbuffer_load_dwordx4 v10, s[12:15], s53 offen lds\nv_mfma_f32_16x16x32_bf16 v[100:103], v[48:51], v[188:191], v[100:103]\n"
"v_mfma_f32_16x16x32_bf16 v[104:107], v[48:51], a[56:59], v[104:107]\nv_mfma_f32_16x16x32_bf16 v[108:111], v[48:51], a[60:63], v[108:111]\nds_read_b128 v[48:51], v34 offset:16384\ns_waitcnt lgkmcnt(3)\n"
"v_mfma_f32_16x16x32_bf16 v[112:115], v[52:55], v[184:187], v[112:115]\nv_mfma_f32_16x16x32_bf16 v[116:119], v[52:55], v[188:191], v[116:119]\nv_mfma_f32_16x16x32_bf16 v[120:123], v[52:55], a[56:59], v[120:123]\nv_mfma_f32_16x16x32_bf16 v[124:127], v[52:55], a[60:63], v[124:127]\n"
"ds_read_b128 v[52:55], v35 offset:16384\ns_waitcnt lgkmcnt(3)\nv_mfma_f32_16x16x32_bf16 v[96:99], v[40:43], v[192:195], v[96:99]\nv_mfma_f32_16x16x32_bf16 v[100:103], v[40:43], v[196:199], v[100:103]\n"
"s_add_u32 m0, s52, 12288\nv_mfma_f32_16x16x32_bf16 v[104:107], v[40:43], a[64:67], v[104:107]\ns_add_u32 s53, s49, 384\nv_mfma_f32_16x16x32_bf16 v[108:111], v[40:43], a[68:71], v[108:111]\n"
"ds_read_b128 v[40:43], v32 offset:20480\nbuffer_load_dwordx4 v10, s[12:15], s53 offen lds\ns_waitcnt lgkmcnt(3)\nv_mfma_f32_16x16x32_bf16 v[112:115], v[44:47], v[192:195], v[112:115]\n"
"v_mfma_f32_16x16x32_bf16 v[116:119], v[44:47], v[196:199], v[116:119]\nv_mfma_f32_16x16x32_bf16 v[120:123], v[44:47], a[64:67], v[120:123]\nv_mfma_f32_16x16x32_bf16 v[124:127], v[44:47], a[68:71], v[124:127]\nds_read_b128 v[44:47], v33 offset:20480\n"
"s_waitcnt lgkmcnt(3)\nv_mfma_f32_16x16x32_bf16 v[96:99], v[48:51], v[200:203], v[96:99]\nv_mfma_f32_16x16x32_bf16 v[100:103], v[48:51], v[204:207], v[100:103]\nv_mfma_f32_16x16x32_bf16 v[104:107], v[48:51], a[72:75], v[104:107]\n"
"v_mfma_f32_16x16x32_bf16 v[108:111], v[48:51], a[76:79], v[108:111]\nds_read_b128 v[48:51], v34 offset:20480\ns_waitcnt lgkmcnt(3)\nv_mfma_f32_16x16x32_bf16 v[112:115], v[52:55], v[200:203], v[112:115]\n"
"s_add_u32 m0, s52, 16384\nv_mfma_f32_16x16x32_bf16 v[116:119], v[52:55], v[204:207], v[116:119]\ns_add_u32 s53, s49, 512\nv_mfma_f32_16x16x32_bf16 v[120:123], v[52:55], a[72:75], v[120:123]\n"
"buffer_load_dwordx4 v10, s[12:15], s53 offen lds\nv_mfma_f32_16x16x32_bf16 v[124:127], v[52:55], a[76:79], v[124:127]\nds_read_b128 v[52:55], v35 offset:20480\ns_waitcnt lgkmcnt(3)\n"
"v_mfma_f32_16x16x32_bf16 v[96:99], v[40:43], v[208:211], v[96:99]\nv_mfma_f32_16x16x32_bf16 v[100:103], v[40:43], v[212:215], v[100:103]\nv_mfma_f32_16x16x32_bf16 v[104:107], v[40:43], a[80:83], v[104:107]\nv_mfma_f32_16x16x32_bf16 v[108:111], v[40:43], a[84:87], v[108:111]\n"
"ds_read_b128 v[40:43], v32 offset:24576\ns_waitcnt lgkmcnt(3)\nv_mfma_f32_16x16x32_bf16 v[112:115], v[44:47], v[208:211], v[112:115]\nv_mfma_f32_16x16x32_bf16 v[116:119], v[44:47], v[212:215], v[116:119]\n"
"v_mfma_f32_16x16x32_bf16 v[120:123], v[44:47], a[80:83], v[120:123]\nv_mfma_f32_16x16x32_bf16 v[124:127], v[44:47], a[84:87], v[124:127]\nds_read_b128 v[44:47], v33 offset:24576\ns_add_u32 m0, s52, 20480\n"
"s_waitcnt lgkmcnt(3)\nv_mfma_f32_16x16x32_bf16 v[96:99], v[48:51], v[216:219], v[96:99]\ns_add_u32 s54, s51, 32768\ns_cmp_eq_u32 s54, 163840\n"
"v_mfma_f32_16x16x32_bf16 v[100:103], v[48:51], v[220:223], v[100:103]\nv_mfma_f32_16x16x32_bf16 v[104:107], v[48:51], a[88:91], v[104:107]\nv_mfma_f32_16x16x32_bf16 v[108:111], v[48:51], a[92:95], v[108:111]\ns_cselect_b32 s54, 0, s54\n"
"s_sub_u32 s54, s54, s51\nds_read_b128 v[48:51], v34 offset:24576\ns_add_u32 s53, s49, 640\ns_waitcnt lgkmcnt(3)\n"
"v_mfma_f32_16x16x32_bf16 v[112:115], v[52:55], v[216:219], v[112:115]\nbuffer_load_dwordx4 v10, s[12:15], s53 offen lds\nv_mfma_f32_16x16x32_bf16 v[116:119], v[52:55], v[220:223], v[116:119]\nv_mfma_f32_16x16x32_bf16 v[120:123], v[52:55], a[88:91], v[120:123]\n"
"v_mfma_f32_16x16x32_bf16 v[124:127], v[52:55], a[92:95], v[124:127]\nds_read_b128 v[52:55], v35 offset:24576\ns_waitcnt lgkmcnt(3)\nv_mfma_f32_16x16x32_bf16 v[96:99], v[40:43], v[224:227], v[96:99]\n"
"v_add_u32 v32, s54, v32\nv_mfma_f32_16x16x32_bf16 v[100:103], v[40:43], v[228:231], v[100:103]\nv_mfma_f32_16x16x32_bf16 v[104:107], v[40:43], a[96:99], v[104:107]\nv_mfma_f32_16x16x32_bf16 v[108:111], v[40:43], a[100:103], v[108:111]\n"
"ds_read_b128 v[40:43], v32 offset:0\ns_waitcnt lgkmcnt(3)\nv_mfma_f32_16x16x32_bf16 v[112:115], v[44:47], v[224:227], v[112:115]\nv_add_u32 v33, s54, v33\n"
"v_mfma_f32_16x16x32_bf16 v[116:119], v[44:47], v[228:231], v[116:119]\ns_add_u32 m0, s52, 24576\nv_mfma_f32_16x16x32_bf16 v[120:123], v[44:47], a[96:99], v[120:123]\ns_add_u32 s53, s49, 768\n"
"v_mfma_f32_16x16x32_bf16 v[124:127], v[44:47], a[100:103], v[124:127]\nds_read_b128 v[44:47], v33 offset:0\nbuffer_load_dwordx4 v10, s[12:15], s53 offen lds\ns_waitcnt lgkmcnt(3)\n"
"v_mfma_f32_16x16x32_bf16 v[96:99], v[48:51], v[232:235], v[96:99]\nv_add_u32 v34, s54, v34\nv_mfma_f32_16x16x32_bf16 v[100:103], v[48:51], v[236:239], v[100:103]\nv_mfma_f32_16x16x32_bf16 v[104:107], v[48:51], a[104:107], v[104:107]\n"
"v_mfma_f32_16x16x32_bf16 v[108:111], v[48:51], a[108:111], v[108:111]\nds_read_b128 v[48:51], v34 offset:0\ns_waitcnt lgkmcnt(3)\nv_mfma_f32_16x16x32_bf16 v[112:115], v[52:55], v[232:235], v[112:115]\n"
"v_add_u32 v35, s54, v35\nv_mfma_f32_16x16x32_bf16 v[116:119], v[52:55], v[236:239], v[116:119]\nv_mfma_f32_16x16x32_bf16 v[120:123], v[52:55], a[104:107], v[120:123]\nv_mfma_f32_16x16x32_bf16 v[124:127], v[52:55], a[108:111], v[124:127]\n"
"ds_read_b128 v[52:55], v35 offset:0\ns_mov_b32 s52, s47\ns_add_u32 s47, s51, s32\ns_add_u32 s51, s51, s54\n"
"s_add_u32 s49, s49, 28672\n.p2align 6\nfg1_loop:\ns_waitcnt vmcnt(7)\n"
"s_barrier\ns_waitcnt lgkmcnt(3)\nv_mfma_f32_16x16x32_bf16 v[64:67], v[40:43], v[128:131], 0\nv_mov_b32 v36, s48\n"
"s_add_u32 s48, s48, 1\ns_nop 0\nbuffer_store_dword v36, v19, s[24:27], s33 offen sc1\nv_mfma_f32_16x16x32_bf16 v[68:71], v[40:43], v[132:135], 0\n"
"v_mfma_f32_16x16x32_bf16 v[72:75], v[40:43], a[0:3], 0\nv_mfma_f32_16x16x32_bf16 v[76:79], v[40:43], a[4:7], 0\nds_read_b128 v[40:43], v32 offset:4096\nv_cvt_pk_bf16_f32 v56, v96, v97\n"
"s_waitcnt lgkmcnt(3)\nv_mfma_f32_16x16x32_bf16 v[80:83], v[44:47], v[128:131], 0\nv_cvt_pk_bf16_f32 v57, v98, v99\nv_mfma_f32_16x16x32_bf16 v[84:87], v[44:47], v[132:135], 0\n"
"v_cvt_pk_bf16_f32 v58, v112, v113\nv_mfma_f32_16x16x32_bf16 v[88:91], v[44:47], a[0:3], 0\nv_cvt_pk_bf16_f32 v59, v114, v115\nv_mfma_f32_16x16x32_bf16 v[92:95], v[44:47], a[4:7], 0\n"
"ds_read_b128 v[44:47], v33 offset:4096\ns_waitcnt lgkmcnt(3)\nv_mfma_f32_16x16x32_bf16 v[64:67], v[48:51], v[136:139], v[64:67]\nv_mfma_f32_16x16x32_bf16 v[68:71], v[48:51], v[140:143], v[68:71]\n"
"buffer_store_dwordx4 v[56:59], v15, s[20:23], s50 offen sc1\nv_mfma_f32_16x16x32_bf16 v[72:75], v[48:51], a[8:11], v[72:75]\nv_cvt_pk_bf16_f32 v60, v100, v101\nv_mfma_f32_16x16x32_bf16 v[76:79], v[48:51], a[12:15], v[76:79]\n"
"ds_read_b128 v[48:51], v34 offset:4096\nv_cvt_pk_bf16_f32 v61, v102, v103\ns_waitcnt lgkmcnt(3)\nv_mfma_f32_16x16x32_bf16 v[80:83], v[52:55], v[136:139], v[80:83]\n"
"v_cvt_pk_bf16_f32 v62, v116, v117\nv_mfma_f32_16x16x32_bf16 v[84:87], v[52:55], v[140:143], v[84:87]\nv_cvt_pk_bf16_f32 v63, v118, v119\nv_mfma_f32_16x16x32_bf16 v[88:91], v[52:55], a[8:11], v[88:91]\n"
"v_mfma_f32_16x16x32_bf16 v[92:95], v[52:55], a[12:15], v[92:95]\nds_read_b128 v[52:55], v35 offset:4096\ns_waitcnt lgkmcnt(3)\nv_mfma_f32_16x16x32_bf16 v[64:67], v[40:43], v[144:147], v[64:67]\n"
"buffer_store_dwordx4 v[60:63], v16, s[20:23], s50 offen sc1\nv_mfma_f32_16x16x32_bf16 v[68:71], v[40:43], v[148:151], v[68:71]\nv_cvt_pk_bf16_f32 v56, v104, v105\nv_mfma_f32_16x16x32_bf16 v[72:75], v[40:43], a[16:19], v[72:75]\n"
"v_cvt_pk_bf16_f32 v57, v106, v107\nv_mfma_f32_16x16x32_bf16 v[76:79], v[40:43], a[20:23], v[76:79]\nds_read_b128 v[40:43], v32 offset:8192\nv_cvt_pk_bf16_f32 v58, v120, v121\n"
"s_waitcnt lgkmcnt(3)\nv_mfma_f32_16x16x32_bf16 v[80:83], v[44:47], v[144:147], v[80:83]\nv_cvt_pk_bf16_f32 v59, v122, v123\nv_mfma_f32_16x16x32_bf16 v[84:87], v[44:47], v[148:151], v[84:87]\n"
"v_mfma_f32_16x16x32_bf16 v[88:91], v[44:47], a[16:19], v[88:91]\nv_mfma_f32_16x16x32_bf16 v[92:95], v[44:47], a[20:23], v[92:95]\nds_read_b128 v[44:47], v33 offset:8192\nbuffer_store_dwordx4 v[56:59], v17, s[20:23], s50 offen sc1\n"
"s_waitcnt lgkmcnt(3)\nv_mfma_f32_16x16x32_bf16 v[64:67], v[48:51], v[152:155], v[64:67]\nv_cvt_pk_bf16_f32 v60, v108, v109\nv_mfma_f32_16x16x32_bf16 v[68:71], v[48:51], v[156:159], v[68:71]\n"
"v_cvt_pk_bf16_f32 v61, v110, v111\nv_mfma_f32_16x16x32_bf16 v[72:75], v[48:51], a[24:27], v[72:75]\nv_cvt_pk_bf16_f32 v62, v124, v125\nv_mfma_f32_16x16x32_bf16 v[76:79], v[48:51], a[28:31], v[76:79]\n"
"ds_read_b128 v[48:51], v34 offset:8192\nv_cvt_pk_bf16_f32 v63, v126, v127\ns_waitcnt lgkmcnt(3)\nv_mfma_f32_16x16x32_bf16 v[80:83], v[52:55], v[152:155], v[80:83]\n"
"v_mfma_f32_16x16x32_bf16 v[84:87], v[52:55], v[156:159], v[84:87]\nv_mfma_f32_16x16x32_bf16 v[88:91], v[52:55], a[24:27], v[88:91]\nbuffer_store_dwordx4 v[60:63], v18, s[20:23], s50 offen sc1\nv_mfma_f32_16x16x32_bf16 v[92:95], v[52:55], a[28:31], v[92:95]\n"
"ds_read_b128 v[52:55], v35 offset:8192\ns_add_u32 s50, s50, s36\ns_waitcnt lgkmcnt(3)\nv_mfma_f32_16x16x32_bf16 v[64:67], v[40:43], v[160:163], v[64:67]\n"
"s_add_u32 m0, s52, 0\nv_mfma_f32_16x16x32_bf16 v[68:71], v[40:43], v[164:167], v[68:71]\ns_add_u32 s53, s49, 0\nv_mfma_f32_16x16x32_bf16 v[72:75], v[40:43], a[32:35], v[72:75]\n"
"buffer_load_dwordx4 v10, s[12:15], s53 offen lds\nv_mfma_f32_16x16x32_bf16 v[76:79], v[40:43], a[36:39], v[76:79]\nds_read_b128 v[40:43], v32 offset:12288\ns_waitcnt lgkmcnt(3)\n"
"v_mfma_f32_16x16x32_bf16 v[80:83], v[44:47], v[160:163], v[80:83]\nv_mfma_f32_16x16x32_bf16 v[84:87], v[44:47], v[164:167], v[84:87]\nv_mfma_f32_16x16x32_bf16 v[88:91], v[44:47], a[32:35], v[88:91]\nv_mfma_f32_16x16x32_bf16 v[92:95], v[44:47], a[36:39], v[92:95]\n"
"ds_read_b128 v[44:47], v33 offset:12288\ns_waitcnt lgkmcnt(3)\nv_mfma_f32_16x16x32_bf16 v[64:67], v[48:51], v[168:171], v[64:67]\nv_mfma_f32_16x16x32_bf16 v[68:71], v[48:51], v[172:175], v[68:71]\n"
"v_mfma_f32_16x16x32_bf16 v[72:75], v[48:51], a[40:43], v[72:75]\nv_mfma_f32_16x16x32_bf16 v[76:79], v[48:51], a[44:47], v[76:79]\nds_read_b128 v[48:51], v34 offset:12288\ns_add_u32 m0, s52, 4096\n"
"s_waitcnt lgkmcnt(3)\nv_mfma_f32_16x16x32_bf16 v[80:83], v[52:55], v[168:171], v[80:83]\ns_add_u32 s53, s49, 128\nv_mfma_f32_16x16x32_bf16 v[84:87], v[52:55], v[172:175], v[84:87]\n"
"buffer_load_dwordx4 v10, s[12:15], s53 offen lds\nv_mfma_f32_16x16x32_bf16 v[88:91], v[52:55], a[40:43], v[88:91]\nv_mfma_f32_16x16x32_bf16 v[92:95], v[52:55], a[44:47], v[92:95]\nds_read_b128 v[52:55], v35 offset:12288\n"
"s_waitcnt lgkmcnt(3)\nv_mfma_f32_16x16x32_bf16 v[64:67], v[40:43], v[176:179], v[64:67]\nv_mfma_f32_16x16x32_bf16 v[68:71], v[40:43], v[180:183], v[68:71]\nv_mfma_f32_16x16x32_bf16 v[72:75], v[40:43], a[48:51], v[72:75]\n"
"v_mfma_f32_16x16x32_bf16 v[76:79], v[40:43], a[52:55], v[76:79]\nds_read_b128 v[40:43], v32 offset:16384\ns_waitcnt lgkmcnt(3)\nv_mfma_f32_16x16x32_bf16 v[80:83], v[44:47], v[176:179], v[80:83]\n"
"v_mfma_f32_16x16x32_bf16 v[84:87], v[44:47], v[180:183], v[84:87]\nv_mfma_f32_16x16x32_bf16 v[88:91], v[44:47], a[48:51], v[88:91]\ns_add_u32 m0, s52, 8192\nv_mfma_f32_16x16x32_bf16 v[92:95], v[44:47], a[52:55], v[92:95]\n"
"ds_read_b128 v[44:47], v33 offset:16384\ns_add_u32 s53, s49, 256\ns_waitcnt lgkmcnt(3)\nv_mfma_f32_16x16x32_bf16 v[64:67], v[48:51], v[184:187], v[64:67]\n"
"buffer_load_dwordx4 v10, s[12:15], s53 offen lds\nv_mfma_f32_16x16x32_bf16 v[68:71], v[48:51], v[188:191], v[68:71]\nv_mfma_f32_16x16x32_bf16 v[72:75], v[48:51], a[56:59], v[72:75]\nv_mfma_f32_16x16x32_bf16 v[76:79], v[48:51], a[60:63], v[76:79]\n"
"ds_read_b128 v[48:51], v34 offset:16384\ns_waitcnt lgkmcnt(3)\nv_mfma_f32_16x16x32_bf16 v[80:83], v[52:55], v[184:187], v[80:83]\nv_mfma_f32_16x16x32_bf16 v[84:87], v[52:55], v[188:191], v[84:87]\n"
"v_mfma_f32_16x16x32_bf16 v[88:91], v[52:55], a[56:59], v[88:91]\nv_mfma_f32_16x16x32_bf16 v[92:95], v[52:55], a[60:63], v[92:95]\nds_read_b128 v[52:55], v35 offset:16384\ns_waitcnt lgkmcnt(3)\n"
"v_mfma_f32_16x16x32_bf16 v[64:67], v[40:43], v[192:195], v[64:67]\nv_mfma_f32_16x16x32_bf16 v[68:71], v[40:43], v[196:199], v[68:71]\ns_add_u32 m0, s52, 12288\nv_mfma_f32_16x16x32_bf16 v[72:75], v[40:43], a[64:67], v[72:75]\n"
"s_add_u32 s53, s49, 384\nv_mfma_f32_16x16x32_bf16 v[76:79], v[40:43], a[68:71], v[76:79]\nds_read_b128 v[40:43], v32 offset:20480\nbuffer_load_dwordx4 v10, s[12:15], s53 offen lds\n"
"s_waitcnt lgkmcnt(3)\nv_mfma_f32_16x16x32_bf16 v[80:83], v[44:47], v[192:195], v[80:83]\nv_mfma_f32_16x16x32_bf16 v[84:87], v[44:47], v[196:199], v[84:87]\nv_mfma_f32_16x16x32_bf16 v[88:91], v[44:47], a[64:67], v[88:91]\n"
"v_mfma_f32_16x16x32_bf16 v[92:95], v[44:47], a[68:71], v[92:95]\nds_read_b128 v[44:47], v33 offset:20480\ns_waitcnt lgkmcnt(3)\nv_mfma_f32_16x16x32_bf16 v[64:67], v[48:51], v[200:203], v[64:67]\n"
"v_mfma_f32_16x16x32_bf16 v[68:71], v[48:51], v[204:207], v[68:71]\nv_mfma_f32_16x16x32_bf16 v[72:75], v[48:51], a[72:75], v[72:75]\nv_mfma_f32_16x16x32_bf16 v[76:79], v[48:51], a[76:79], v[76:79]\nds_read_b128 v[48:51], v34 offset:20480\n"
"s_waitcnt lgkmcnt(3)\nv_mfma_f32_16x16x32_bf16 v[80:83], v[52:55], v[200:203], v[80:83]\ns_add_u32 m0, s52, 16384\nv_mfma_f32_16x16x32_bf16 v[84:87], v[52:55], v[204:207], v[84:87]\n"
"s_add_u32 s53, s49, 512\nv_mfma_f32_16x16x32_bf16 v[88:91], v[52:55], a[72:75], v[88:91]\nbuffer_load_dwordx4 v10, s[12:15], s53 offen lds\nv_mfma_f32_16x16x32_bf16 v[92:95], v[52:55], a[76:79], v[92:95]\n"
"ds_read_b128 v[52:55], v35 offset:20480\ns_waitcnt lgkmcnt(3)\nv_mfma_f32_16x16x32_bf16 v[64:67], v[40:43], v[208:211], v[64:67]\nv_mfma_f32_16x16x32_bf16 v[68:71], v[40:43], v[212:215], v[68:71]\n"
"v_mfma_f32_16x16x32_bf16 v[72:75], v[40:43], a[80:83], v[72:75]\nv_mfma_f32_16x16x32_bf16 v[76:79], v[40:43], a[84:87], v[76:79]\nds_read_b128 v[40:43], v32 offset:24576\ns_waitcnt lgkmcnt(3)\n"
"v_mfma_f32_16x16x32_bf16 v[80:83], v[44:47], v[208:211], v[80:83]\nv_mfma_f32_16x16x32_bf16 v[84:87], v[44:47], v[212:215], v[84:87]\nv_mfma_f32_16x16x32_bf16 v[88:91], v[44:47], a[80:83], v[88:91]\nv_mfma_f32_16x16x32_bf16 v[92:95], v[44:47], a[84:87], v[92:95]\n"
"ds_read_b128 v[44:47], v33 offset:24576\ns_add_u32 m0, s52, 20480\ns_waitcnt lgkmcnt(3)\nv_mfma_f32_16x16x32_bf16 v[64:67], v[48:51], v[216:219], v[64:67]\n"
"s_add_u32 s54, s51, 32768\ns_cmp_eq_u32 s54, 163840\nv_mfma_f32_16x16x32_bf16 v[68:71], v[48:51], v[220:223], v[68:71]\nv_mfma_f32_16x16x32_bf16 v[72:75], v[48:51], a[88:91], v[72:75]\n"
"v_mfma_f32_16x16x32_bf16 v[76:79], v[48:51], a[92:95], v[76:79]\ns_cselect_b32 s54, 0, s54\ns_sub_u32 s54, s54, s51\nds_read_b128 v[48:51], v34 offset:24576\n"
"s_add_u32 s53, s49, 640\ns_waitcnt lgkmcnt(3)\nv_mfma_f32_16x16x32_bf16 v[80:83], v[52:55], v[216:219], v[80:83]\nbuffer_load_dwordx4 v10, s[12:15], s53 offen lds\n"
"v_mfma_f32_16x16x32_bf16 v[84:87], v[52:55], v[220:223], v[84:87]\nv_mfma_f32_16x16x32_bf16 v[88:91], v[52:55], a[88:91], v[88:91]\nv_mfma_f32_16x16x32_bf16 v[92:95], v[52:55], a[92:95], v[92:95]\nds_read_b128 v[52:55], v35 offset:24576\n"
"s_waitcnt lgkmcnt(3)\nv_mfma_f32_16x16x32_bf16 v[64:67], v[40:43], v[224:227], v[64:67]\nv_add_u32 v32, s54, v32\nv_mfma_f32_16x16x32_bf16 v[68:71], v[40:43], v[228:231], v[68:71]\n"
"v_mfma_f32_16x16x32_bf16 v[72:75], v[40:43], a[96:99], v[72:75]\nv_mfma_f32_16x16x32_bf16 v[76:79], v[40:43], a[100:103], v[76:79]\nds_read_b128 v[40:43], v32 offset:0\ns_waitcnt lgkmcnt(3)\n"
"v_mfma_f32_16x16x32_bf16 v[80:83], v[44:47], v[224:227], v[80:83]\nv_add_u32 v33, s54, v33\nv_mfma_f32_16x16x32_bf16 v[84:87], v[44:47], v[228:231], v[84:87]\ns_add_u32 m0, s52, 24576\n"
"v_mfma_f32_16x16x32_bf16 v[88:91], v[44:47], a[96:99], v[88:91]\ns_add_u32 s53, s49, 768\nv_mfma_f32_16x16x32_bf16 v[92:95], v[44:47], a[100:103], v[92:95]\nds_read_b128 v[44:47], v33 offset:0\n"
"buffer_load_dwordx4 v10, s[12:15], s53 offen lds\ns_waitcnt lgkmcnt(3)\nv_mfma_f32_16x16x32_bf16 v[64:67], v[48:51], v[232:235], v[64:67]\nv_add_u32 v34, s54, v34\n"
"v_mfma_f32_16x16x32_bf16 v[68:71], v[48:51], v[236:239], v[68:71]\nv_mfma_f32_16x16x32_bf16 v[72:75], v[48:51], a[104:107], v[72:75]\nv_mfma_f32_16x16x32_bf16 v[76:79], v[48:51], a[108:111], v[76:79]\nds_read_b128 v[48:51], v34 offset:0\n"
"s_waitcnt lgkmcnt(3)\nv_mfma_f32_16x16x32_bf16 v[80:83], v[52:55], v[232:235], v[80:83]\nv_add_u32 v35, s54, v35\nv_mfma_f32_16x16x32_bf16 v[84:87], v[52:55], v[236:239], v[84:87]\n"
"v_mfma_f32_16x16x32_bf16 v[88:91], v[52:55], a[104:107], v[88:91]\nv_mfma_f32_16x16x32_bf16 v[92:95], v[52:55], a[108:111], v[92:95]\nds_read_b128 v[52:55], v35 offset:0\ns_mov_b32 s52, s47\n"
"s_add_u32 s47, s51, s32\ns_add_u32 s51, s51, s54\ns_add_u32 s49, s49, 28672\ns_waitcnt vmcnt(7)\n"
"s_barrier\ns_waitcnt lgkmcnt(3)\nv_mfma_f32_16x16x32_bf16 v[96:99], v[40:43], v[128:131], 0\ns_add_u32 s48, s48, 1\n"
"v_mfma_f32_16x16x32_bf16 v[100:103], v[40:43], v[132:135], 0\nv_mfma_f32_16x16x32_bf16 v[104:107], v[40:43], a[0:3], 0\nv_mfma_f32_16x16x32_bf16 v[108:111], v[40:43], a[4:7], 0\nds_read_b128 v[40:43], v32 offset:4096\n"
"v_cvt_pk_bf16_f32 v56, v64, v65\ns_waitcnt lgkmcnt(3)\nv_mfma_f32_16x16x32_bf16 v[112:115], v[44:47], v[128:131], 0\nv_cvt_pk_bf16_f32 v57, v66, v67\n"
"v_mfma_f32_16x16x32_bf16 v[116:119], v[44:47], v[132:135], 0\nv_cvt_pk_bf16_f32 v58, v80, v81\nv_mfma_f32_16x16x32_bf16 v[120:123], v[44:47], a[0:3], 0\nv_cvt_pk_bf16_f32 v59, v82, v83\n"
"v_mfma_f32_16x16x32_bf16 v[124:127], v[44:47], a[4:7], 0\nds_read_b128 v[44:47], v33 offset:4096\ns_waitcnt lgkmcnt(3)\nv_mfma_f32_16x16x32_bf16 v[96:99], v[48:51], v[136:139], v[96:99]\n"
"v_mfma_f32_16x16x32_bf16 v[100:103], v[48:51], v[140:143], v[100:103]\nbuffer_store_dwordx4 v[56:59], v15, s[20:23], s50 offen sc1\nv_mfma_f32_16x16x32_bf16 v[104:107], v[48:51], a[8:11], v[104:107]\nv_cvt_pk_bf16_f32 v60, v68, v69\n"
"v_mfma_f32_16x16x32_bf16 v[108:111], v[48:51], a[12:15], v[108:111]\nds_read_b128 v[48:51], v34 offset:4096\nv_cvt_pk_bf16_f32 v61, v70, v71\ns_waitcnt lgkmcnt(3)\n"
"v_mfma_f32_16x16x32_bf16 v[112:115], v[52:55], v[136:139], v[112:115]\nv_cvt_pk_bf16_f32 v62, v84, v85\nv_mfma_f32_16x16x32_bf16 v[116:119], v[52:55], v[140:143], v[116:119]\nv_cvt_pk_bf16_f32 v63, v86, v87\n"
"v_mfma_f32_16x16x32_bf16 v[120:123], v[52:55], a[8:11], v[120:123]\nv_mfma_f32_16x16x32_bf16 v[124:127], v[52:55], a[12:15], v[124:127]\nds_read_b128 v[52:55], v35 offset:4096\ns_waitcnt lgkmcnt(3)\n"
"v_mfma_f32_16x16x32_bf16 v[96:99], v[40:43], v[144:147], v[96:99]\nbuffer_store_dwordx4 v[60:63], v16, s[20:23], s50 offen sc1\nv_mfma_f32_16x16x32_bf16 v[100:103], v[40:43], v[148:151], v[100:103]\nv_cvt_pk_bf16_f32 v56, v72, v73\n"
"v_mfma_f32_16x16x32_bf16 v[104:107], v[40:43], a[16:19], v[104:107]\nv_cvt_pk_bf16_f32 v57, v74, v75\nv_mfma_f32_16x16x32_bf16 v[108:111], v[40:43], a[20:23], v[108:111]\nds_read_b128 v[40:43], v32 offset:8192\n"
"v_cvt_pk_bf16_f32 v58, v88, v89\ns_waitcnt lgkmcnt(3)\nv_mfma_f32_16x16x32_bf16 v[112:115], v[44:47], v[144:147], v[112:115]\nv_cvt_pk_bf16_f32 v59, v90, v91\n"
"v_mfma_f32_16x16x32_bf16 v[116:119], v[44:47], v[148:151], v[116:119]\nv_mfma_f32_16x16x32_bf16 v[120:123], v[44:47], a[16:19], v[120:123]\nv_mfma_f32_16x16x32_bf16 v[124:127], v[44:47], a[20:23], v[124:127]\nds_read_b128 v[44:47], v33 offset:8192\n"
"buffer_store_dwordx4 v[56:59], v17, s[20:23], s50 offen sc1\ns_waitcnt lgkmcnt(3)\nv_mfma_f32_16x16x32_bf16 v[96:99], v[48:51], v[152:155], v[96:99]\nv_cvt_pk_bf16_f32 v60, v76, v77\n"
"v_mfma_f32_16x16x32_bf16 v[100:103], v[48:51], v[156:159], v[100:103]\nv_cvt_pk_bf16_f32 v61, v78, v79\nv_mfma_f32_16x16x32_bf16 v[104:107], v[48:51], a[24:27], v[104:107]\nv_cvt_pk_bf16_f32 v62, v92, v93\n"
"v_mfma_f32_16x16x32_bf16 v[108:111], v[48:51], a[28:31], v[108:111]\nds_read_b128 v[48:51], v34 offset:8192\nv_cvt_pk_bf16_f32 v63, v94, v95\ns_waitcnt lgkmcnt(3)\n"
"v_mfma_f32_16x16x32_bf16 v[112:115], v[52:55], v[152:155], v[112:115]\nv_mfma_f32_16x16x32_bf16 v[116:119], v[52:55], v[156:159], v[116:119]\nv_mfma_f32_16x16x32_bf16 v[120:123], v[52:55], a[24:27], v[120:123]\nbuffer_store_dwordx4 v[60:63], v18, s[20:23], s50 offen sc1\n"
"v_mfma_f32_16x16x32_bf16 v[124:127], v[52:55], a[28:31], v[124:127]\nds_read_b128 v[52:55], v35 offset:8192\ns_add_u32 s50, s50, s36\ns_waitcnt lgkmcnt(3)\n"
"v_mfma_f32_16x16x32_bf16 v[96:99], v[40:43], v[160:163], v[96:99]\ns_add_u32 m0, s52, 0\nv_mfma_f32_16x16x32_bf16 v[100:103], v[40:43], v[164:167], v[100:103]\ns_add_u32 s53, s49, 0\n"
"v_mfma_f32_16x16x32_bf16 v[104:107], v[40:43], a[32:35], v[104:107]\nbuffer_load_dwordx4 v10, s[12:15], s53 offen lds\nv_mfma_f32_16x16x32_bf16 v[108:111], v[40:43], a[36:39], v[108:111]\nds_read_b128 v[40:43], v32 offset:12288\n"
"s_waitcnt lgkmcnt(3)\nv_mfma_f32_16x16x32_bf16 v[112:115], v[44:47], v[160:163], v[112:115]\nv_mfma_f32_16x16x32_bf16 v[116:119], v[44:47], v[164:167], v[116:119]\nv_mfma_f32_16x16x32_bf16 v[120:123], v[44:47], a[32:35], v[120:123]\n"
"v_mfma_f32_16x16x32_bf16 v[124:127], v[44:47], a[36:39], v[124:127]\nds_read_b128 v[44:47], v33 offset:12288\ns_waitcnt lgkmcnt(3)\nv_mfma_f32_16x16x32_bf16 v[96:99], v[48:51], v[168:171], v[96:99]\n"
"v_mfma_f32_16x16x32_bf16 v[100:103], v[48:51], v[172:175], v[100:103]\nv_mfma_f32_16x16x32_bf16 v[104:107], v[48:51], a[40:43], v[104:107]\nv_mfma_f32_16x16x32_bf16 v[108:111], v[48:51], a[44:47], v[108:111]\nds_read_b128 v[48:51], v34 offset:12288\n"
"s_add_u32 m0, s52, 4096\ns_waitcnt lgkmcnt(3)\nv_mfma_f32_16x16x32_bf16 v[112:115], v[52:55], v[168:171], v[112:115]\ns_add_u32 s53, s49, 128\n"
"v_mfma_f32_16x16x32_bf16 v[116:119], v[52:55], v[172:175], v[116:119]\nbuffer_load_dwordx4 v10, s[12:15], s53 offen lds\nv_mfma_f32_16x16x32_bf16 v[120:123], v[52:55], a[40:43], v[120:123]\nv_mfma_f32_16x16x32_bf16 v[124:127], v[52:55], a[44:47], v[124:127]\n"
"ds_read_b128 v[52:55], v35 offset:12288\ns_waitcnt lgkmcnt(3)\nv_mfma_f32_16x16x32_bf16 v[96:99], v[40:43], v[176:179], v[96:99]\nv_mfma_f32_16x16x32_bf16 v[100:103], v[40:43], v[180:183], v[100:103]\n"
"v_mfma_f32_16x16x32_bf16 v[104:107], v[40:43], a[48:51], v[104:107]\nv_mfma_f32_16x16x32_bf16 v[108:111], v[40:43], a[52:55], v[108:111]\nds_read_b128 v[40:43], v32 offset:16384\ns_waitcnt lgkmcnt(3)\n"
"v_mfma_f32_16x16x32_bf16 v[112:115], v[44:47], v[176:179], v[112:115]\nv_mfma_f32_16x16x32_bf16 v[116:119], v[44:47], v[180:183], v[116:119]\nv_mfma_f32_16x16x32_bf16 v[120:123], v[44:47], a[48:51], v[120:123]\ns_add_u32 m0, s52, 8192\n"
"v_mfma_f32_16x16x32_bf16 v[124:127], v[44:47], a[52:55], v[124:127]\nds_read_b128 v[44:47], v33 offset:16384\ns_add_u32 s53, s49, 256\ns_waitcnt lgkmcnt(3)\n"
"v_mfma_f32_16x16x32_bf16 v[96:99], v[48:51], v[184:187], v[96:99]\nbuffer_load_dwordx4 v10, s[12:15], s53 offen lds\nv_mfma_f32_16x16x32_bf16 v[100:103], v[48:51], v[188:191], v[100:103]\nv_mfma_f32_16x16x32_bf16 v[104:107], v[48:51], a[56:59], v[104:107]\n"
"v_mfma_f32_16x16x32_bf16 v[108:111], v[48:51], a[60:63], v[108:111]\nds_read_b128 v[48:51], v34 offset:16384\ns_waitcnt lgkmcnt(3)\nv_mfma_f32_16x16x32_bf16 v[112:115], v[52:55], v[184:187], v[112:115]\n"
"v_mfma_f32_16x16x32_bf16 v[116:119], v[52:55], v[188:191], v[116:119]\nv_mfma_f32_16x16x32_bf16 v[120:123], v[52:55], a[56:59], v[120:123]\nv_mfma_f32_16x16x32_bf16 v[124:127], v[52:55], a[60:63], v[124:127]\nds_read_b128 v[52:55], v35 offset:16384\n"
"s_waitcnt lgkmcnt(3)\nv_mfma_f32_16x16x32_bf16 v[96:99], v[40:43], v[192:195], v[96:99]\nv_mfma_f32_16x16x32_bf16 v[100:103], v[40:43], v[196:199], v[100:103]\ns_add_u32 m0, s52, 12288\n"
"v_mfma_f32_16x16x32_bf16 v[104:107], v[40:43], a[64:67], v[104:107]\ns_add_u32 s53, s49, 384\nv_mfma_f32_16x16x32_bf16 v[108:111], v[40:43], a[68:71], v[108:111]\nds_read_b128 v[40:43], v32 offset:20480\n"
"buffer_load_dwordx4 v10, s[12:15], s53 offen lds\ns_waitcnt lgkmcnt(3)\nv_mfma_f32_16x16x32_bf16 v[112:115], v[44:47], v[192:195], v[112:115]\nv_mfma_f32_16x16x32_bf16 v[116:119], v[44:47], v[196:199], v[116:119]\n"
"v_mfma_f32_16x16x32_bf16 v[120:123], v[44:47], a[64:67], v[120:123]\nv_mfma_f32_16x16x32_bf16 v[124:127], v[44:47], a[68:71], v[124:127]\nds_read_b128 v[44:47], v33 offset:20480\ns_waitcnt lgkmcnt(3)\n"
"v_mfma_f32_16x16x32_bf16 v[96:99], v[48:51], v[200:203], v[96:99]\nv_mfma_f32_16x16x32_bf16 v[100:103], v[48:51], v[204:207], v[100:103]\nv_mfma_f32_16x16x32_bf16 v[104:107], v[48:51], a[72:75], v[104:107]\nv_mfma_f32_16x16x32_bf16 v[108:111], v[48:51], a[76:79], v[108:111]\n"
"ds_read_b128 v[48:51], v34 offset:20480\ns_waitcnt lgkmcnt(3)\nv_mfma_f32_16x16x32_bf16 v[112:115], v[52:55], v[200:203], v[112:115]\ns_add_u32 m0, s52, 16384\n"
"v_mfma_f32_16x16x32_bf16 v[116:119], v[52:55], v[204:207], v[116:119]\ns_add_u32 s53, s49, 512\nv_mfma_f32_16x16x32_bf16 v[120:123], v[52:55], a[72:75], v[120:123]\nbuffer_load_dwordx4 v10, s[12:15], s53 offen lds\n"
"v_mfma_f32_16x16x32_bf16 v[124:127], v[52:55], a[76:79], v[124:127]\nds_read_b128 v[52:55], v35 offset:20480\ns_waitcnt lgkmcnt(3)\nv_mfma_f32_16x16x32_bf16 v[96:99], v[40:43], v[208:211], v[96:99]\n"
"v_mfma_f32_16x16x32_bf16 v[100:103], v[40:43], v[212:215], v[100:103]\nv_mfma_f32_16x16x32_bf16 v[104:107], v[40:43], a[80:83], v[104:107]\nv_mfma_f32_16x16x32_bf16 v[108:111], v[40:43], a[84:87], v[108:111]\nds_read_b128 v[40:43], v32 offset:24576\n"
"s_waitcnt lgkmcnt(3)\nv_mfma_f32_16x16x32_bf16 v[112:115], v[44:47], v[208:211], v[112:115]\nv_mfma_f32_16x16x32_bf16 v[116:119], v[44:47], v[212:215], v[116:119]\nv_mfma_f32_16x16x32_bf16 v[120:123], v[44:47], a[80:83], v[120:123]\n"
"v_mfma_f32_16x16x32_bf16 v[124:127], v[44:47], a[84:87], v[124:127]\nds_read_b128 v[44:47], v33 offset:24576\ns_add_u32 m0, s52, 20480\ns_waitcnt lgkmcnt(3)\n"
"v_mfma_f32_16x16x32_bf16 v[96:99], v[48:51], v[216:219], v[96:99]\ns_add_u32 s54, s51, 32768\ns_cmp_eq_u32 s54, 163840\nv_mfma_f32_16x16x32_bf16 v[100:103], v[48:51], v[220:223], v[100:103]\n"
"v_mfma_f32_16x16x32_bf16 v[104:107], v[48:51], a[88:91], v[104:107]\nv_mfma_f32_16x16x32_bf16 v[108:111], v[48:51], a[92:95], v[108:111]\ns_cselect_b32 s54, 0, s54\ns_sub_u32 s54, s54, s51\n"
"ds_read_b128 v[48:51], v34 offset:24576\ns_add_u32 s53, s49, 640\ns_waitcnt lgkmcnt(3)\nv_mfma_f32_16x16x32_bf16 v[112:115], v[52:55], v[216:219], v[112:115]\n"
"buffer_load_dwordx4 v10, s[12:15], s53 offen lds\nv_mfma_f32_16x16x32_bf16 v[116:119], v[52:55], v[220:223], v[116:119]\nv_mfma_f32_16x16x32_bf16 v[120:123], v[52:55], a[88:91], v[120:123]\nv_mfma_f32_16x16x32_bf16 v[124:127], v[52:55], a[92:95], v[124:127]\n"
"ds_read_b128 v[52:55], v35 offset:24576\ns_waitcnt lgkmcnt(3)\nv_mfma_f32_16x16x32_bf16 v[96:99], v[40:43], v[224:227], v[96:99]\nv_add_u32 v32, s54, v32\n"
"v_mfma_f32_16x16x32_bf16 v[100:103], v[40:43], v[228:231], v[100:103]\nv_mfma_f32_16x16x32_bf16 v[104:107], v[40:43], a[96:99], v[104:107]\nv_mfma_f32_16x16x32_bf16 v[108:111], v[40:43], a[100:103], v[108:111]\nds_read_b128 v[40:43], v32 offset:0\n"
"s_waitcnt lgkmcnt(3)\nv_mfma_f32_16x16x32_bf16 v[112:115], v[44:47], v[224:227], v[112:115]\nv_add_u32 v33, s54, v33\nv_mfma_f32_16x16x32_bf16 v[116:119], v[44:47], v[228:231], v[116:119]\n"
"s_add_u32 m0, s52, 24576\nv_mfma_f32_16x16x32_bf16 v[120:123], v[44:47], a[96:99], v[120:123]\ns_add_u32 s53, s49, 768\nv_mfma_f32_16x16x32_bf16 v[124:127], v[44:47], a[100:103], v[124:127]\n"
"ds_read_b128 v[44:47], v33 offset:0\nbuffer_load_dwordx4 v10, s[12:15], s53 offen lds\ns_waitcnt lgkmcnt(3)\nv_mfma_f32_16x16x32_bf16 v[96:99], v[48:51], v[232:235], v[96:99]\n"
"v_add_u32 v34, s54, v34\nv_mfma_f32_16x16x32_bf16 v[100:103], v[48:51], v[236:239], v[100:103]\nv_mfma_f32_16x16x32_bf16 v[104:107], v[48:51], a[104:107], v[104:107]\nv_mfma_f32_16x16x32_bf16 v[108:111], v[48:51], a[108:111], v[108:111]\n"
"ds_read_b128 v[48:51], v34 offset:0\ns_waitcnt lgkmcnt(3)\nv_mfma_f32_16x16x32_bf16 v[112:115], v[52:55], v[232:235], v[112:115]\nv_add_u32 v35, s54, v35\n"
"v_mfma_f32_16x16x32_bf16 v[116:119], v[52:55], v[236:239], v[116:119]\nv_mfma_f32_16x16x32_bf16 v[120:123], v[52:55], a[104:107], v[120:123]\nv_mfma_f32_16x16x32_bf16 v[124:127], v[52:55], a[108:111], v[124:127]\nds_read_b128 v[52:55], v35 offset:0\n"
"s_mov_b32 s52, s47\ns_add_u32 s47, s51, s32\ns_add_u32 s51, s51, s54\ns_add_u32 s49, s49, 28672\n"
"s_cmp_lt_u32 s49, 1921024\ns_cbranch_scc1 fg1_loop\ns_nop 7\nv_cvt_pk_bf16_f32 v56, v96, v97\n"
"v_cvt_pk_bf16_f32 v57, v98, v99\nv_cvt_pk_bf16_f32 v58, v112, v113\nv_cvt_pk_bf16_f32 v59, v114, v115\ns_nop 0\n"
"s_nop 0\nbuffer_store_dwordx4 v[56:59], v15, s[20:23], s50 offen sc1\nv_cvt_pk_bf16_f32 v60, v100, v101\nv_cvt_pk_bf16_f32 v61, v102, v103\n"
"v_cvt_pk_bf16_f32 v62, v116, v117\nv_cvt_pk_bf16_f32 v63, v118, v119\ns_nop 0\ns_nop 0\n"
"buffer_store_dwordx4 v[60:63], v16, s[20:23], s50 offen sc1\nv_cvt_pk_bf16_f32 v56, v104, v105\nv_cvt_pk_bf16_f32 v57, v106, v107\nv_cvt_pk_bf16_f32 v58, v120, v121\n"
"v_cvt_pk_bf16_f32 v59, v122, v123\ns_nop 0\ns_nop 0\nbuffer_store_dwordx4 v[56:59], v17, s[20:23], s50 offen sc1\n"
"v_cvt_pk_bf16_f32 v60, v108, v109\nv_cvt_pk_bf16_f32 v61, v110, v111\nv_cvt_pk_bf16_f32 v62, v124, v125\nv_cvt_pk_bf16_f32 v63, v126, v127\n"
"s_nop 0\ns_nop 0\nbuffer_store_dwordx4 v[60:63], v18, s[20:23], s50 offen sc1\ns_add_u32 s48, s28, 64\n"
"v_mov_b32 v36, s48\ns_waitcnt vmcnt(0) lgkmcnt(0)\ns_barrier\nbuffer_store_dword v36, v19, s[24:27], s33 offen sc1\n"
"fg1_end:\n"
    :  : "{s[8:23]}"(s0), "{s[24:39]}"(s1), "{v[8:23]}"(v0) : "v24", "v25", "v26", "v27", "v28", "v29", "v30", "v31", "v32", "v33", "v34", "v35", "v36", "v37", "v38", "v39", "v40", "v41", "v42", "v43", "v44", "v45", "v46", "v47", "v48", "v49", "v50", "v51", "v52", "v53", "v54", "v55", "v56", "v57", "v58", "v59", "v60", "v61", "v62", "v63", "v64", "v65", "v66", "v67", "v68", "v69", "v70", "v71", "v72", "v73", "v74", "v75", "v76", "v77", "v78", "v79", "v80", "v81", "v82", "v83", "v84", "v85", "v86", "v87", "v88", "v89", "v90", "v91", "v92", "v93", "v94", "v95", "v96", "v97", "v98", "v99", "v100", "v101", "v102", "v103", "v104", "v105", "v106", "v107", "v108", "v109", "v110", "v111", "v112", "v113", "v114", "v115", "v116", "v117", "v118", "v119", "v120", "v121", "v122", "v123", "v124", "v125", "v126", "v127", "v128", "v129", "v130", "v131", "v132", "v133", "v134", "v135", "v136", "v137", "v138", "v139", "v140", "v141", "v142", "v143", "v144", "v145", "v146", "v147", "v148", "v149", "v150", "v151", "v152", "v153", "v154", "v155", "v156", "v157", "v158", "v159", "v160", "v161", "v162", "v163", "v164", "v165", "v166", "v167", "v168", "v169", "v170", "v171", "v172", "v173", "v174", "v175", "v176", "v177", "v178", "v179", "v180", "v181", "v182", "v183", "v184", "v185", "v186", "v187", "v188", "v189", "v190", "v191", "v192", "v193", "v194", "v195", "v196", "v197", "v198", "v199", "v200", "v201", "v202", "v203", "v204", "v205", "v206", "v207", "v208", "v209", "v210", "v211", "v212", "v213", "v214", "v215", "v216", "v217", "v218", "v219", "v220", "v221", "v222", "v223", "v224", "v225", "v226", "v227", "v228", "v229", "v230", "v231", "v232", "v233", "v234", "v235", "v236", "v237", "v238", "v239", "v240", "v241", "v242", "v243", "a0", "a1", "a2", "a3", "a4", "a5", "a6", "a7", "a8", "a9", "a10", "a11", "a12", "a13", "a14", "a15", "a16", "a17", "a18", "a19", "a20", "a21", "a22", "a23", "a24", "a25", "a26", "a27", "a28", "a29", "a30", "a31", "a32", "a33", "a34", "a35", "a36", "a37", "a38", "a39", "a40", "a41", "a42", "a43", "a44", "a45", "a46", "a47", "a48", "a49", "a50", "a51", "a52", "a53", "a54", "a55", "a56", "a57", "a58", "a59", "a60", "a61", "a62", "a63", "a64", "a65", "a66", "a67", "a68", "a69", "a70", "a71", "a72", "a73", "a74", "a75", "a76", "a77", "a78", "a79", "a80", "a81", "a82", "a83", "a84", "a85", "a86", "a87", "a88", "a89", "a90", "a91", "a92", "a93", "a94", "a95", "a96", "a97", "a98", "a99", "a100", "a101", "a102", "a103", "a104", "a105", "a106", "a107", "a108", "a109", "a110", "a111", "a112", "a113", "a114", "a115", "a116", "a117", "a118", "a119", "a120", "a121", "a122", "a123", "a124", "a125", "a126", "a127", "a128", "a129", "a130", "a131", "a132", "a133", "a134", "a135", "a136", "a137", "a138", "a139", "a140", "a141", "a142", "a143", "a144", "a145", "a146", "a147", "a148", "a149", "a150", "a151", "a152", "a153", "a154", "a155", "a156", "a157", "a158", "a159", "a160", "a161", "a162", "a163", "a164", "a165", "a166", "a167", "a168", "a169", "a170", "a171", "a172", "a173", "a174", "a175", "a176", "a177", "a178", "a179", "a180", "a181", "a182", "a183", "a184", "a185", "a186", "a187", "a188", "a189", "a190", "a191", "s40", "s41", "s42", "s43", "s44", "s45", "s46", "s47", "s48", "s49", "s50", "s51", "s52", "s53", "s54", "s55", "s56", "s57", "s58", "s59", "s60", "s61", "vcc", "scc", "memory");
    return o;
}
struct OCtx { u4 r; unsigned sg, ssx, sr, su, sum, nt, hx, p0, p1, p2, p3, p4; unsigned vg, vsx, vu, vum, vx, vt, vpx, vpm, vpr, vo, p5, p6, p7, p8, p9, pa; };
DI void o_asm(const OCtx& c) {
    const u16v s0 = {RFL(c.r[0]), RFL(c.r[1]), RFL(c.r[2]), RFL(c.r[3]), RFL(c.sg), RFL(c.ssx), RFL(c.sr), RFL(c.su), RFL(c.sum), RFL(c.nt), RFL(c.hx), RFL(c.p0), RFL(c.p1), RFL(c.p2), RFL(c.p3), RFL(c.p4)}, v0 = {c.vg, c.vsx, c.vu, c.vum, c.vx, c.vt, c.vpx, c.vpm, c.vpr, c.vo, c.p5, c.p6, c.p7, c.p8, c.p9, c.pa};
    asm volatile(
"s_nop 4\ns_xor_b32 s72, s18, 0\ns_mul_i32 s72, s72, 1835008\ns_add_u32 s72, s72, s12\n"
"buffer_load_dwordx4 a[0:3], v8, s[8:11], s72 offen offset:0 sc1\nbuffer_load_dwordx4 a[4:7], v8, s[8:11], s72 offen offset:512 sc1\nbuffer_load_dwordx4 a[8:11], v8, s[8:11], s72 offen offset:1024 sc1\nbuffer_load_dwordx4 a[12:15], v8, s[8:11], s72 offen offset:1536 sc1\n"
"buffer_load_dwordx4 a[16:19], v8, s[8:11], s72 offen offset:2048 sc1\nbuffer_load_dwordx4 a[20:23], v8, s[8:11], s72 offen offset:2560 sc1\nbuffer_load_dwordx4 a[24:27], v8, s[8:11], s72 offen offset:3072 sc1\nbuffer_load_dwordx4 a[28:31], v8, s[8:11], s72 offen offset:3584 sc1\n"
"s_add_u32 s72, s72, 4096\nbuffer_load_dwordx4 a[32:35], v8, s[8:11], s72 offen offset:0 sc1\nbuffer_load_dwordx4 a[36:39], v8, s[8:11], s72 offen offset:512 sc1\nbuffer_load_dwordx4 a[40:43], v8, s[8:11], s72 offen offset:1024 sc1\n"
"buffer_load_dwordx4 a[44:47], v8, s[8:11], s72 offen offset:1536 sc1\nbuffer_load_dwordx4 a[48:51], v8, s[8:11], s72 offen offset:2048 sc1\nbuffer_load_dwordx4 a[52:55], v8, s[8:11], s72 offen offset:2560 sc1\nbuffer_load_dwordx4 a[56:59], v8, s[8:11], s72 offen offset:3072 sc1\n"
"buffer_load_dwordx4 a[60:63], v8, s[8:11], s72 offen offset:3584 sc1\ns_add_u32 s72, s72, 4096\nbuffer_load_dwordx4 a[64:67], v8, s[8:11], s72 offen offset:0 sc1\nbuffer_load_dwordx4 a[68:71], v8, s[8:11], s72 offen offset:512 sc1\n"
"buffer_load_dwordx4 a[72:75], v8, s[8:11], s72 offen offset:1024 sc1\nbuffer_load_dwordx4 a[76:79], v8, s[8:11], s72 offen offset:1536 sc1\nbuffer_load_dwordx4 a[80:83], v8, s[8:11], s72 offen offset:2048 sc1\nbuffer_load_dwordx4 a[84:87], v8, s[8:11], s72 offen offset:2560 sc1\n"
"buffer_load_dwordx4 a[88:91], v8, s[8:11], s72 offen offset:3072 sc1\nbuffer_load_dwordx4 a[92:95], v8, s[8:11], s72 offen offset:3584 sc1\ns_add_u32 s72, s72, 4096\nbuffer_load_dwordx4 a[96:99], v8, s[8:11], s72 offen offset:0 sc1\n"
"buffer_load_dwordx4 a[100:103], v8, s[8:11], s72 offen offset:512 sc1\nbuffer_load_dwordx4 a[104:107], v8, s[8:11], s72 offen offset:1024 sc1\nbuffer_load_dwordx4 a[108:111], v8, s[8:11], s72 offen offset:1536 sc1\ns_xor_b32 s73, s18, 0\n"
"s_lshl_b32 s73, s73, 18\ns_add_u32 s73, s73, s13\nbuffer_load_dwordx4 v[64:67], v9, s[8:11], s73 offen sc1\ns_add_u32 s73, s73, 65536\n"
"buffer_load_dwordx4 v[68:71], v9, s[8:11], s73 offen sc1\ns_add_u32 s73, s73, 65536\nbuffer_load_dwordx4 v[72:75], v9, s[8:11], s73 offen sc1\ns_add_u32 s73, s73, 65536\n"
"buffer_load_dwordx4 v[76:79], v9, s[8:11], s73 offen sc1\ns_xor_b32 s72, s18, 1\ns_mul_i32 s72, s72, 1835008\ns_add_u32 s72, s72, s12\n"
"buffer_load_dwordx4 a[112:115], v8, s[8:11], s72 offen offset:0 sc1\nbuffer_load_dwordx4 a[116:119], v8, s[8:11], s72 offen offset:512 sc1\nbuffer_load_dwordx4 a[120:123], v8, s[8:11], s72 offen offset:1024 sc1\nbuffer_load_dwordx4 a[124:127], v8, s[8:11], s72 offen offset:1536 sc1\n"
"buffer_load_dwordx4 a[128:131], v8, s[8:11], s72 offen offset:2048 sc1\nbuffer_load_dwordx4 a[132:135], v8, s[8:11], s72 offen offset:2560 sc1\nbuffer_load_dwordx4 a[136:139], v8, s[8:11], s72 offen offset:3072 sc1\nbuffer_load_dwordx4 a[140:143], v8, s[8:11], s72 offen offset:3584 sc1\n"
"s_add_u32 s72, s72, 4096\nbuffer_load_dwordx4 a[144:147], v8, s[8:11], s72 offen offset:0 sc1\nbuffer_load_dwordx4 a[148:151], v8, s[8:11], s72 offen offset:512 sc1\nbuffer_load_dwordx4 a[152:155], v8, s[8:11], s72 offen offset:1024 sc1\n"
"buffer_load_dwordx4 a[156:159], v8, s[8:11], s72 offen offset:1536 sc1\nbuffer_load_dwordx4 a[160:163], v8, s[8:11], s72 offen offset:2048 sc1\nbuffer_load_dwordx4 a[164:167], v8, s[8:11], s72 offen offset:2560 sc1\nbuffer_load_dwordx4 a[168:171], v8, s[8:11], s72 offen offset:3072 sc1\n"
"buffer_load_dwordx4 a[172:175], v8, s[8:11], s72 offen offset:3584 sc1\ns_add_u32 s72, s72, 4096\nbuffer_load_dwordx4 a[176:179], v8, s[8:11], s72 offen offset:0 sc1\nbuffer_load_dwordx4 a[180:183], v8, s[8:11], s72 offen offset:512 sc1\n"
"buffer_load_dwordx4 a[184:187], v8, s[8:11], s72 offen offset:1024 sc1\nbuffer_load_dwordx4 a[188:191], v8, s[8:11], s72 offen offset:1536 sc1\nbuffer_load_dwordx4 a[192:195], v8, s[8:11], s72 offen offset:2048 sc1\nbuffer_load_dwordx4 a[196:199], v8, s[8:11], s72 offen offset:2560 sc1\n"
"buffer_load_dwordx4 a[200:203], v8, s[8:11], s72 offen offset:3072 sc1\nbuffer_load_dwordx4 a[204:207], v8, s[8:11], s72 offen offset:3584 sc1\ns_add_u32 s72, s72, 4096\nbuffer_load_dwordx4 a[208:211], v8, s[8:11], s72 offen offset:0 sc1\n"
"buffer_load_dwordx4 a[212:215], v8, s[8:11], s72 offen offset:512 sc1\nbuffer_load_dwordx4 a[216:219], v8, s[8:11], s72 offen offset:1024 sc1\nbuffer_load_dwordx4 a[220:223], v8, s[8:11], s72 offen offset:1536 sc1\ns_add_u32 s76, s15, 0\n"
"v_xor_b32 v24, 0, v12\ns_add_u32 s77, s15, 4096\nv_xor_b32 v25, 32, v12\ns_add_u32 s78, s15, 8192\n"
"v_xor_b32 v26, 64, v12\ns_add_u32 s79, s15, 12288\nv_xor_b32 v27, 96, v12\ns_mov_b32 s80, s16\n"
"v_cmp_gt_i32 s[40:41], 0, v13\nv_cmp_gt_i32 s[42:43], 1, v13\nv_cmp_gt_i32 s[44:45], 2, v13\nv_cmp_gt_i32 s[46:47], 3, v13\n"
"v_cmp_gt_i32 s[48:49], 4, v13\nv_cmp_gt_i32 s[50:51], 5, v13\nv_cmp_gt_i32 s[52:53], 6, v13\nv_cmp_gt_i32 s[54:55], 7, v13\n"
"v_cmp_gt_i32 s[56:57], 16, v13\nv_cmp_gt_i32 s[58:59], 17, v13\nv_cmp_gt_i32 s[60:61], 18, v13\nv_cmp_gt_i32 s[62:63], 19, v13\n"
"v_cmp_gt_i32 s[64:65], 20, v13\nv_cmp_gt_i32 s[66:67], 21, v13\nv_cmp_gt_i32 s[68:69], 22, v13\nv_cmp_gt_i32 s[70:71], 23, v13\n"
"s_barrier\nds_read_b128 v[128:131], v24 offset:0\nds_read_b128 v[132:135], v25 offset:0\nds_read_b128 v[136:139], v26 offset:0\n"
"ds_read_b128 v[140:143], v27 offset:0\nds_read_b128 v[144:147], v24 offset:4096\nds_read_b128 v[148:151], v25 offset:4096\nds_read_b128 v[152:155], v26 offset:4096\n"
"ds_read_b128 v[156:159], v27 offset:4096\nds_read_b128 v[160:163], v24 offset:8192\nds_read_b128 v[164:167], v25 offset:8192\nds_read_b128 v[168:171], v26 offset:8192\n"
"ds_read_b128 v[172:175], v27 offset:8192\ns_waitcnt lgkmcnt(0)\nds_read_b128 v[176:179], v24 offset:12288\nds_read_b128 v[180:183], v25 offset:12288\n"
"ds_read_b128 v[184:187], v26 offset:12288\nds_read_b128 v[188:191], v27 offset:12288\nds_read_b128 v[192:195], v24 offset:16384\nds_read_b128 v[196:199], v25 offset:16384\n"
"ds_read_b128 v[200:203], v26 offset:16384\nds_read_b128 v[204:207], v27 offset:16384\nds_read_b128 v[208:211], v24 offset:20480\nds_read_b128 v[212:215], v25 offset:20480\n"
"ds_read_b128 v[216:219], v26 offset:20480\nds_read_b128 v[220:223], v27 offset:20480\ns_waitcnt lgkmcnt(0)\nds_read_b128 v[224:227], v24 offset:24576\n"
"ds_read_b128 v[228:231], v25 offset:24576\nds_read_b128 v[232:235], v26 offset:24576\nds_read_b128 v[236:239], v27 offset:24576\nv_mov_b32 v31, 0xff800000\n"
"s_waitcnt lgkmcnt(0)\ns_waitcnt vmcnt(59)\nv_mfma_f32_32x32x16_bf16 v[40:55], a[0:3], v[128:131], 0\ns_xor_b32 s73, s18, 1\n"
"s_lshl_b32 s73, s73, 18\ns_add_u32 s73, s73, s13\nbuffer_load_dwordx4 v[80:83], v9, s[8:11], s73 offen sc1\ns_add_u32 s73, s73, 65536\n"
"buffer_load_dwordx4 v[84:87], v9, s[8:11], s73 offen sc1\ns_add_u32 s73, s73, 65536\nbuffer_load_dwordx4 v[88:91], v9, s[8:11], s73 offen sc1\ns_add_u32 s73, s73, 65536\n"
"buffer_load_dwordx4 v[92:95], v9, s[8:11], s73 offen sc1\ns_xor_b32 s72, s18, 2\ns_mul_i32 s72, s72, 1835008\ns_add_u32 s72, s72, s12\n"
"buffer_load_dwordx4 a[0:3], v8, s[8:11], s72 offen offset:0 sc1\ns_waitcnt vmcnt(63)\nv_mfma_f32_32x32x16_bf16 v[40:55], a[4:7], v[132:135], v[40:55]\nbuffer_load_dwordx4 a[4:7], v8, s[8:11], s72 offen offset:512 sc1\n"
"s_waitcnt vmcnt(63)\nv_mfma_f32_32x32x16_bf16 v[40:55], a[8:11], v[136:139], v[40:55]\nbuffer_load_dwordx4 a[8:11], v8, s[8:11], s72 offen offset:1024 sc1\ns_waitcnt vmcnt(63)\n"
"v_mfma_f32_32x32x16_bf16 v[40:55], a[12:15], v[140:143], v[40:55]\nbuffer_load_dwordx4 a[12:15], v8, s[8:11], s72 offen offset:1536 sc1\ns_waitcnt vmcnt(63)\nv_mfma_f32_32x32x16_bf16 v[40:55], a[16:19], v[144:147], v[40:55]\n"
"buffer_load_dwordx4 a[16:19], v8, s[8:11], s72 offen offset:2048 sc1\ns_waitcnt vmcnt(63)\nv_mfma_f32_32x32x16_bf16 v[40:55], a[20:23], v[148:151], v[40:55]\nbuffer_load_dwordx4 a[20:23], v8, s[8:11], s72 offen offset:2560 sc1\n"
"s_waitcnt vmcnt(63)\nv_mfma_f32_32x32x16_bf16 v[40:55], a[24:27], v[152:155], v[40:55]\nbuffer_load_dwordx4 a[24:27], v8, s[8:11], s72 offen offset:3072 sc1\ns_waitcnt vmcnt(63)\n"
"v_mfma_f32_32x32x16_bf16 v[40:55], a[28:31], v[156:159], v[40:55]\nbuffer_load_dwordx4 a[28:31], v8, s[8:11], s72 offen offset:3584 sc1\ns_waitcnt vmcnt(63)\nv_mfma_f32_32x32x16_bf16 v[40:55], a[32:35], v[160:163], v[40:55]\n"
"s_add_u32 s72, s72, 4096\nbuffer_load_dwordx4 a[32:35], v8, s[8:11], s72 offen offset:0 sc1\ns_waitcnt vmcnt(63)\nv_mfma_f32_32x32x16_bf16 v[40:55], a[36:39], v[164:167], v[40:55]\n"
"buffer_load_dwordx4 a[36:39], v8, s[8:11], s72 offen offset:512 sc1\ns_waitcnt vmcnt(63)\nv_mfma_f32_32x32x16_bf16 v[40:55], a[40:43], v[168:171], v[40:55]\nbuffer_load_dwordx4 a[40:43], v8, s[8:11], s72 offen offset:1024 sc1\n"
"s_waitcnt vmcnt(63)\nv_mfma_f32_32x32x16_bf16 v[40:55], a[44:47], v[172:175], v[40:55]\nbuffer_load_dwordx4 a[44:47], v8, s[8:11], s72 offen offset:1536 sc1\ns_waitcnt vmcnt(63)\n"
"v_mfma_f32_32x32x16_bf16 v[40:55], a[48:51], v[176:179], v[40:55]\nbuffer_load_dwordx4 a[48:51], v8, s[8:11], s72 offen offset:2048 sc1\ns_waitcnt vmcnt(63)\nv_mfma_f32_32x32x16_bf16 v[40:55], a[52:55], v[180:183], v[40:55]\n"
"buffer_load_dwordx4 a[52:55], v8, s[8:11], s72 offen offset:2560 sc1\ns_waitcnt vmcnt(63)\nv_mfma_f32_32x32x16_bf16 v[40:55], a[56:59], v[184:187], v[40:55]\nbuffer_load_dwordx4 a[56:59], v8, s[8:11], s72 offen offset:3072 sc1\n"
"s_waitcnt vmcnt(63)\nv_mfma_f32_32x32x16_bf16 v[40:55], a[60:63], v[188:191], v[40:55]\nbuffer_load_dwordx4 a[60:63], v8, s[8:11], s72 offen offset:3584 sc1\ns_waitcnt vmcnt(63)\n"
"v_mfma_f32_32x32x16_bf16 v[40:55], a[64:67], v[192:195], v[40:55]\ns_add_u32 s72, s72, 4096\nbuffer_load_dwordx4 a[64:67], v8, s[8:11], s72 offen offset:0 sc1\ns_waitcnt vmcnt(63)\n"
"v_mfma_f32_32x32x16_bf16 v[40:55], a[68:71], v[196:199], v[40:55]\nbuffer_load_dwordx4 a[68:71], v8, s[8:11], s72 offen offset:512 sc1\ns_waitcnt vmcnt(63)\nv_mfma_f32_32x32x16_bf16 v[40:55], a[72:75], v[200:203], v[40:55]\n"
"buffer_load_dwordx4 a[72:75], v8, s[8:11], s72 offen offset:1024 sc1\ns_waitcnt vmcnt(63)\nv_mfma_f32_32x32x16_bf16 v[40:55], a[76:79], v[204:207], v[40:55]\nbuffer_load_dwordx4 a[76:79], v8, s[8:11], s72 offen offset:1536 sc1\n"
"s_waitcnt vmcnt(63)\nv_mfma_f32_32x32x16_bf16 v[40:55], a[80:83], v[208:211], v[40:55]\nbuffer_load_dwordx4 a[80:83], v8, s[8:11], s72 offen offset:2048 sc1\ns_waitcnt vmcnt(63)\n"
"v_mfma_f32_32x32x16_bf16 v[40:55], a[84:87], v[212:215], v[40:55]\nbuffer_load_dwordx4 a[84:87], v8, s[8:11], s72 offen offset:2560 sc1\ns_waitcnt vmcnt(63)\nv_mfma_f32_32x32x16_bf16 v[40:55], a[88:91], v[216:219], v[40:55]\n"
"buffer_load_dwordx4 a[88:91], v8, s[8:11], s72 offen offset:3072 sc1\ns_waitcnt vmcnt(63)\nv_mfma_f32_32x32x16_bf16 v[40:55], a[92:95], v[220:223], v[40:55]\nbuffer_load_dwordx4 a[92:95], v8, s[8:11], s72 offen offset:3584 sc1\n"
"s_waitcnt vmcnt(63)\nv_mfma_f32_32x32x16_bf16 v[40:55], a[96:99], v[224:227], v[40:55]\ns_add_u32 s72, s72, 4096\nbuffer_load_dwordx4 a[96:99], v8, s[8:11], s72 offen offset:0 sc1\n"
"s_waitcnt vmcnt(63)\nv_mfma_f32_32x32x16_bf16 v[40:55], a[100:103], v[228:231], v[40:55]\nbuffer_load_dwordx4 a[100:103], v8, s[8:11], s72 offen offset:512 sc1\ns_waitcnt vmcnt(63)\n"
"v_mfma_f32_32x32x16_bf16 v[40:55], a[104:107], v[232:235], v[40:55]\nbuffer_load_dwordx4 a[104:107], v8, s[8:11], s72 offen offset:1024 sc1\ns_waitcnt vmcnt(63)\nv_mfma_f32_32x32x16_bf16 v[40:55], a[108:111], v[236:239], v[40:55]\n"
"buffer_load_dwordx4 a[108:111], v8, s[8:11], s72 offen offset:1536 sc1\ns_waitcnt vmcnt(60)\nv_add_f32 v64, v64, v68\nv_add_f32 v72, v72, v76\n"
"v_add_f32 v65, v65, v69\nv_add_f32 v73, v73, v77\nv_add_f32 v66, v66, v70\nv_add_f32 v74, v74, v78\n"
"v_add_f32 v67, v67, v71\nv_add_f32 v75, v75, v79\nv_add_f32 v64, v64, v72\nv_add_f32 v65, v65, v73\n"
"v_add_f32 v66, v66, v74\nv_add_f32 v67, v67, v75\nv_max3_f32 v28, v64, v65, v66\nv_max_f32 v28, v28, v67\n"
"v_cndmask_b32 v40, v40, v31, s[40:41]\nv_cndmask_b32 v41, v41, v31, s[42:43]\nv_cndmask_b32 v42, v42, v31, s[44:45]\nv_cndmask_b32 v43, v43, v31, s[46:47]\n"
"v_cndmask_b32 v44, v44, v31, s[48:49]\nv_cndmask_b32 v45, v45, v31, s[50:51]\nv_cndmask_b32 v46, v46, v31, s[52:53]\nv_cndmask_b32 v47, v47, v31, s[54:55]\n"
"v_cndmask_b32 v48, v48, v31, s[56:57]\nv_cndmask_b32 v49, v49, v31, s[58:59]\nv_cndmask_b32 v50, v50, v31, s[60:61]\nv_cndmask_b32 v51, v51, v31, s[62:63]\n"
"v_cndmask_b32 v52, v52, v31, s[64:65]\nv_cndmask_b32 v53, v53, v31, s[66:67]\nv_cndmask_b32 v54, v54, v31, s[68:69]\nv_cndmask_b32 v55, v55, v31, s[70:71]\n"
"v_max3_f32 v28, v28, v40, v41\nv_max3_f32 v28, v28, v42, v43\nv_max3_f32 v28, v28, v44, v45\nv_max3_f32 v28, v28, v46, v47\n"
"v_max3_f32 v28, v28, v48, v49\nv_max3_f32 v28, v28, v50, v51\nv_max3_f32 v28, v28, v52, v53\nv_max3_f32 v28, v28, v54, v55\n"
"v_mov_b32 v30, v28\ns_nop 1\nv_permlane32_swap_b32 v28, v30\nv_max_f32 v28, v28, v30\n"
"v_sub_f32 v40, v40, v28\nv_sub_f32 v41, v41, v28\nv_sub_f32 v42, v42, v28\nv_sub_f32 v43, v43, v28\n"
"v_sub_f32 v44, v44, v28\nv_sub_f32 v45, v45, v28\nv_sub_f32 v46, v46, v28\nv_sub_f32 v47, v47, v28\n"
"v_sub_f32 v48, v48, v28\nv_sub_f32 v49, v49, v28\nv_sub_f32 v50, v50, v28\nv_sub_f32 v51, v51, v28\n"
"v_sub_f32 v52, v52, v28\nv_sub_f32 v53, v53, v28\nv_sub_f32 v54, v54, v28\nv_sub_f32 v55, v55, v28\n"
"v_sub_f32 v64, v64, v28\nv_sub_f32 v65, v65, v28\nv_sub_f32 v66, v66, v28\nv_sub_f32 v67, v67, v28\n"
"v_exp_f32 v40, v40\nv_exp_f32 v41, v41\nv_exp_f32 v42, v42\nv_exp_f32 v43, v43\n"
"v_exp_f32 v44, v44\nv_exp_f32 v45, v45\nv_exp_f32 v46, v46\nv_exp_f32 v47, v47\n"
"v_exp_f32 v48, v48\nv_exp_f32 v49, v49\nv_exp_f32 v50, v50\nv_exp_f32 v51, v51\n"
"v_exp_f32 v52, v52\nv_exp_f32 v53, v53\nv_exp_f32 v54, v54\nv_exp_f32 v55, v55\n"
"v_exp_f32 v64, v64\nv_exp_f32 v65, v65\nv_exp_f32 v66, v66\nv_exp_f32 v67, v67\n"
"v_add_f32 v29, v40, v41\nv_add_f32 v30, v42, v43\nv_add_f32 v29, v29, v44\nv_add_f32 v30, v30, v45\n"
"v_add_f32 v29, v29, v46\nv_add_f32 v30, v30, v47\nv_add_f32 v29, v29, v48\nv_add_f32 v30, v30, v49\n"
"v_add_f32 v29, v29, v50\nv_add_f32 v30, v30, v51\nv_add_f32 v29, v29, v52\nv_add_f32 v30, v30, v53\n"
"v_add_f32 v29, v29, v54\nv_add_f32 v30, v30, v55\nv_add_f32 v29, v29, v64\nv_add_f32 v30, v30, v65\n"
"v_add_f32 v29, v29, v66\nv_add_f32 v30, v30, v67\nv_add_f32 v29, v29, v30\nv_mov_b32 v30, v29\n"
"s_nop 1\nv_permlane32_swap_b32 v29, v30\nv_add_f32 v29, v29, v30\nv_rcp_f32 v29, v29\n"
"s_nop 0\nv_mul_f32 v40, v40, v29\nv_mul_f32 v41, v41, v29\nv_mul_f32 v42, v42, v29\n"
"v_mul_f32 v43, v43, v29\nv_mul_f32 v44, v44, v29\nv_mul_f32 v45, v45, v29\nv_mul_f32 v46, v46, v29\n"
"v_mul_f32 v47, v47, v29\nv_mul_f32 v48, v48, v29\nv_mul_f32 v49, v49, v29\nv_mul_f32 v50, v50, v29\n"
"v_mul_f32 v51, v51, v29\nv_mul_f32 v52, v52, v29\nv_mul_f32 v53, v53, v29\nv_mul_f32 v54, v54, v29\n"
"v_mul_f32 v55, v55, v29\nv_mul_f32 v64, v64, v29\nv_mul_f32 v65, v65, v29\nv_mul_f32 v66, v66, v29\n"
"v_mul_f32 v67, v67, v29\nv_cvt_pk_bf16_f32 v56, v40, v41\nv_cvt_pk_bf16_f32 v57, v42, v43\nv_cvt_pk_bf16_f32 v58, v44, v45\n"
"v_cvt_pk_bf16_f32 v59, v46, v47\nv_cvt_pk_bf16_f32 v60, v48, v49\nv_cvt_pk_bf16_f32 v61, v50, v51\nv_cvt_pk_bf16_f32 v62, v52, v53\n"
"v_cvt_pk_bf16_f32 v63, v54, v55\nv_cvt_pk_bf16_f32 v32, v64, v65\nv_cvt_pk_bf16_f32 v36, v66, v67\nv_xor_b32 v30, 0, v14\n"
"ds_write_b128 v30, v[56:59]\nds_write_b128 v30, v[60:63] offset:1024\ns_waitcnt vmcnt(59)\nv_mfma_f32_32x32x16_bf16 v[40:55], a[112:115], v[128:131], 0\n"
"s_xor_b32 s73, s18, 2\ns_lshl_b32 s73, s73, 18\ns_add_u32 s73, s73, s13\nbuffer_load_dwordx4 v[96:99], v9, s[8:11], s73 offen sc1\n"
"s_add_u32 s73, s73, 65536\nbuffer_load_dwordx4 v[100:103], v9, s[8:11], s73 offen sc1\ns_add_u32 s73, s73, 65536\nbuffer_load_dwordx4 v[104:107], v9, s[8:11], s73 offen sc1\n"
"s_add_u32 s73, s73, 65536\nbuffer_load_dwordx4 v[108:111], v9, s[8:11], s73 offen sc1\ns_xor_b32 s72, s18, 3\ns_mul_i32 s72, s72, 1835008\n"
"s_add_u32 s72, s72, s12\nbuffer_load_dwordx4 a[112:115], v8, s[8:11], s72 offen offset:0 sc1\ns_waitcnt vmcnt(63)\nv_mfma_f32_32x32x16_bf16 v[40:55], a[116:119], v[132:135], v[40:55]\n"
"buffer_load_dwordx4 a[116:119], v8, s[8:11], s72 offen offset:512 sc1\ns_waitcnt vmcnt(63)\nv_mfma_f32_32x32x16_bf16 v[40:55], a[120:123], v[136:139], v[40:55]\nbuffer_load_dwordx4 a[120:123], v8, s[8:11], s72 offen offset:1024 sc1\n"
"s_waitcnt vmcnt(63)\nv_mfma_f32_32x32x16_bf16 v[40:55], a[124:127], v[140:143], v[40:55]\nbuffer_load_dwordx4 a[124:127], v8, s[8:11], s72 offen offset:1536 sc1\ns_waitcnt vmcnt(63)\n"
"v_mfma_f32_32x32x16_bf16 v[40:55], a[128:131], v[144:147], v[40:55]\nbuffer_load_dwordx4 a[128:131], v8, s[8:11], s72 offen offset:2048 sc1\ns_waitcnt vmcnt(63)\nv_mfma_f32_32x32x16_bf16 v[40:55], a[132:135], v[148:151], v[40:55]\n"
"buffer_load_dwordx4 a[132:135], v8, s[8:11], s72 offen offset:2560 sc1\ns_waitcnt vmcnt(63)\nv_mfma_f32_32x32x16_bf16 v[40:55], a[136:139], v[152:155], v[40:55]\nbuffer_load_dwordx4 a[136:139], v8, s[8:11], s72 offen offset:3072 sc1\n"
"s_waitcnt vmcnt(63)\nv_mfma_f32_32x32x16_bf16 v[40:55], a[140:143], v[156:159], v[40:55]\nbuffer_load_dwordx4 a[140:143], v8, s[8:11], s72 offen offset:3584 sc1\ns_waitcnt vmcnt(63)\n"
"v_mfma_f32_32x32x16_bf16 v[40:55], a[144:147], v[160:163], v[40:55]\ns_add_u32 s72, s72, 4096\nbuffer_load_dwordx4 a[144:147], v8, s[8:11], s72 offen offset:0 sc1\ns_waitcnt vmcnt(63)\n"
"v_mfma_f32_32x32x16_bf16 v[40:55], a[148:151], v[164:167], v[40:55]\nbuffer_load_dwordx4 a[148:151], v8, s[8:11], s72 offen offset:512 sc1\ns_waitcnt vmcnt(63)\nv_mfma_f32_32x32x16_bf16 v[40:55], a[152:155], v[168:171], v[40:55]\n"
"buffer_load_dwordx4 a[152:155], v8, s[8:11], s72 offen offset:1024 sc1\ns_waitcnt vmcnt(63)\nv_mfma_f32_32x32x16_bf16 v[40:55], a[156:159], v[172:175], v[40:55]\nbuffer_load_dwordx4 a[156:159], v8, s[8:11], s72 offen offset:1536 sc1\n"
"s_waitcnt vmcnt(63)\nv_mfma_f32_32x32x16_bf16 v[40:55], a[160:163], v[176:179], v[40:55]\nbuffer_load_dwordx4 a[160:163], v8, s[8:11], s72 offen offset:2048 sc1\ns_waitcnt vmcnt(63)\n"
"v_mfma_f32_32x32x16_bf16 v[40:55], a[164:167], v[180:183], v[40:55]\nbuffer_load_dwordx4 a[164:167], v8, s[8:11], s72 offen offset:2560 sc1\ns_waitcnt vmcnt(63)\nv_mfma_f32_32x32x16_bf16 v[40:55], a[168:171], v[184:187], v[40:55]\n"
"buffer_load_dwordx4 a[168:171], v8, s[8:11], s72 offen offset:3072 sc1\ns_waitcnt vmcnt(63)\nv_mfma_f32_32x32x16_bf16 v[40:55], a[172:175], v[188:191], v[40:55]\nbuffer_load_dwordx4 a[172:175], v8, s[8:11], s72 offen offset:3584 sc1\n"
"s_waitcnt vmcnt(63)\nv_mfma_f32_32x32x16_bf16 v[40:55], a[176:179], v[192:195], v[40:55]\ns_add_u32 s72, s72, 4096\nbuffer_load_dwordx4 a[176:179], v8, s[8:11], s72 offen offset:0 sc1\n"
"s_waitcnt vmcnt(63)\nv_mfma_f32_32x32x16_bf16 v[40:55], a[180:183], v[196:199], v[40:55]\nbuffer_load_dwordx4 a[180:183], v8, s[8:11], s72 offen offset:512 sc1\ns_waitcnt vmcnt(63)\n"
"v_mfma_f32_32x32x16_bf16 v[40:55], a[184:187], v[200:203], v[40:55]\nbuffer_load_dwordx4 a[184:187], v8, s[8:11], s72 offen offset:1024 sc1\ns_waitcnt vmcnt(63)\nv_mfma_f32_32x32x16_bf16 v[40:55], a[188:191], v[204:207], v[40:55]\n"
"buffer_load_dwordx4 a[188:191], v8, s[8:11], s72 offen offset:1536 sc1\ns_waitcnt vmcnt(63)\nv_mfma_f32_32x32x16_bf16 v[40:55], a[192:195], v[208:211], v[40:55]\nbuffer_load_dwordx4 a[192:195], v8, s[8:11], s72 offen offset:2048 sc1\n"
"s_waitcnt vmcnt(63)\nv_mfma_f32_32x32x16_bf16 v[40:55], a[196:199], v[212:215], v[40:55]\nbuffer_load_dwordx4 a[196:199], v8, s[8:11], s72 offen offset:2560 sc1\ns_waitcnt vmcnt(63)\n"
"v_mfma_f32_32x32x16_bf16 v[40:55], a[200:203], v[216:219], v[40:55]\nbuffer_load_dwordx4 a[200:203], v8, s[8:11], s72 offen offset:3072 sc1\ns_waitcnt vmcnt(63)\nv_mfma_f32_32x32x16_bf16 v[40:55], a[204:207], v[220:223], v[40:55]\n"
"buffer_load_dwordx4 a[204:207], v8, s[8:11], s72 offen offset:3584 sc1\ns_waitcnt vmcnt(63)\nv_mfma_f32_32x32x16_bf16 v[40:55], a[208:211], v[224:227], v[40:55]\ns_add_u32 s72, s72, 4096\n"
"buffer_load_dwordx4 a[208:211], v8, s[8:11], s72 offen offset:0 sc1\ns_waitcnt vmcnt(63)\nv_mfma_f32_32x32x16_bf16 v[40:55], a[212:215], v[228:231], v[40:55]\nbuffer_load_dwordx4 a[212:215], v8, s[8:11], s72 offen offset:512 sc1\n"
"s_waitcnt vmcnt(63)\nv_mfma_f32_32x32x16_bf16 v[40:55], a[216:219], v[232:235], v[40:55]\nbuffer_load_dwordx4 a[216:219], v8, s[8:11], s72 offen offset:1024 sc1\ns_waitcnt vmcnt(63)\n"
"v_mfma_f32_32x32x16_bf16 v[40:55], a[220:223], v[236:239], v[40:55]\nbuffer_load_dwordx4 a[220:223], v8, s[8:11], s72 offen offset:1536 sc1\ns_waitcnt vmcnt(60)\nv_add_f32 v80, v80, v84\n"
"v_add_f32 v88, v88, v92\nv_add_f32 v81, v81, v85\nv_add_f32 v89, v89, v93\nv_add_f32 v82, v82, v86\n"
"v_add_f32 v90, v90, v94\nv_add_f32 v83, v83, v87\nv_add_f32 v91, v91, v95\nv_add_f32 v80, v80, v88\n"
"v_add_f32 v81, v81, v89\nv_add_f32 v82, v82, v90\nv_add_f32 v83, v83, v91\nv_max3_f32 v28, v80, v81, v82\n"
"v_max_f32 v28, v28, v83\nv_cndmask_b32 v40, v40, v31, s[40:41]\nv_cndmask_b32 v41, v41, v31, s[42:43]\nv_cndmask_b32 v42, v42, v31, s[44:45]\n"
"v_cndmask_b32 v43, v43, v31, s[46:47]\nv_cndmask_b32 v44, v44, v31, s[48:49]\nv_cndmask_b32 v45, v45, v31, s[50:51]\nv_cndmask_b32 v46, v46, v31, s[52:53]\n"
"v_cndmask_b32 v47, v47, v31, s[54:55]\nv_cndmask_b32 v48, v48, v31, s[56:57]\nv_cndmask_b32 v49, v49, v31, s[58:59]\nv_cndmask_b32 v50, v50, v31, s[60:61]\n"
"v_cndmask_b32 v51, v51, v31, s[62:63]\nv_cndmask_b32 v52, v52, v31, s[64:65]\nv_cndmask_b32 v53, v53, v31, s[66:67]\nv_cndmask_b32 v54, v54, v31, s[68:69]\n"
"v_cndmask_b32 v55, v55, v31, s[70:71]\nv_max3_f32 v28, v28, v40, v41\nv_max3_f32 v28, v28, v42, v43\nv_max3_f32 v28, v28, v44, v45\n"
"v_max3_f32 v28, v28, v46, v47\nv_max3_f32 v28, v28, v48, v49\nv_max3_f32 v28, v28, v50, v51\nv_max3_f32 v28, v28, v52, v53\n"
"v_max3_f32 v28, v28, v54, v55\nv_mov_b32 v30, v28\ns_nop 1\nv_permlane32_swap_b32 v28, v30\n"
"v_max_f32 v28, v28, v30\nv_sub_f32 v40, v40, v28\nv_sub_f32 v41, v41, v28\nv_sub_f32 v42, v42, v28\n"
"v_sub_f32 v43, v43, v28\nv_sub_f32 v44, v44, v28\nv_sub_f32 v45, v45, v28\nv_sub_f32 v46, v46, v28\n"
"v_sub_f32 v47, v47, v28\nv_sub_f32 v48, v48, v28\nv_sub_f32 v49, v49, v28\nv_sub_f32 v50, v50, v28\n"
"v_sub_f32 v51, v51, v28\nv_sub_f32 v52, v52, v28\nv_sub_f32 v53, v53, v28\nv_sub_f32 v54, v54, v28\n"
"v_sub_f32 v55, v55, v28\nv_sub_f32 v80, v80, v28\nv_sub_f32 v81, v81, v28\nv_sub_f32 v82, v82, v28\n"
"v_sub_f32 v83, v83, v28\nv_exp_f32 v40, v40\nv_exp_f32 v41, v41\nv_exp_f32 v42, v42\n"
"v_exp_f32 v43, v43\nv_exp_f32 v44, v44\nv_exp_f32 v45, v45\nv_exp_f32 v46, v46\n"
"v_exp_f32 v47, v47\nv_exp_f32 v48, v48\nv_exp_f32 v49, v49\nv_exp_f32 v50, v50\n"
"v_exp_f32 v51, v51\nv_exp_f32 v52, v52\nv_exp_f32 v53, v53\nv_exp_f32 v54, v54\n"
"v_exp_f32 v55, v55\nv_exp_f32 v80, v80\nv_exp_f32 v81, v81\nv_exp_f32 v82, v82\n"
"v_exp_f32 v83, v83\nv_add_f32 v29, v40, v41\nv_add_f32 v30, v42, v43\nv_add_f32 v29, v29, v44\n"
"v_add_f32 v30, v30, v45\nv_add_f32 v29, v29, v46\nv_add_f32 v30, v30, v47\nv_add_f32 v29, v29, v48\n"
"v_add_f32 v30, v30, v49\nv_add_f32 v29, v29, v50\nv_add_f32 v30, v30, v51\nv_add_f32 v29, v29, v52\n"
"v_add_f32 v30, v30, v53\nv_add_f32 v29, v29, v54\nv_add_f32 v30, v30, v55\nv_add_f32 v29, v29, v80\n"
"v_add_f32 v30, v30, v81\nv_add_f32 v29, v29, v82\nv_add_f32 v30, v30, v83\nv_add_f32 v29, v29, v30\n"
"v_mov_b32 v30, v29\ns_nop 1\nv_permlane32_swap_b32 v29, v30\nv_add_f32 v29, v29, v30\n"
"v_rcp_f32 v29, v29\ns_nop 0\nv_mul_f32 v40, v40, v29\nv_mul_f32 v41, v41, v29\n"
"v_mul_f32 v42, v42, v29\nv_mul_f32 v43, v43, v29\nv_mul_f32 v44, v44, v29\nv_mul_f32 v45, v45, v29\n"
"v_mul_f32 v46, v46, v29\nv_mul_f32 v47, v47, v29\nv_mul_f32 v48, v48, v29\nv_mul_f32 v49, v49, v29\n"
"v_mul_f32 v50, v50, v29\nv_mul_f32 v51, v51, v29\nv_mul_f32 v52, v52, v29\nv_mul_f32 v53, v53, v29\n"
"v_mul_f32 v54, v54, v29\nv_mul_f32 v55, v55, v29\nv_mul_f32 v80, v80, v29\nv_mul_f32 v81, v81, v29\n"
"v_mul_f32 v82, v82, v29\nv_mul_f32 v83, v83, v29\nv_cvt_pk_bf16_f32 v56, v40, v41\nv_cvt_pk_bf16_f32 v57, v42, v43\n"
"v_cvt_pk_bf16_f32 v58, v44, v45\nv_cvt_pk_bf16_f32 v59, v46, v47\nv_cvt_pk_bf16_f32 v60, v48, v49\nv_cvt_pk_bf16_f32 v61, v50, v51\n"
"v_cvt_pk_bf16_f32 v62, v52, v53\nv_cvt_pk_bf16_f32 v63, v54, v55\nv_cvt_pk_bf16_f32 v33, v80, v81\nv_cvt_pk_bf16_f32 v37, v82, v83\n"
"v_xor_b32 v30, 2048, v14\nds_write_b128 v30, v[56:59]\nds_write_b128 v30, v[60:63] offset:1024\ns_waitcnt vmcnt(59)\n"
"v_mfma_f32_32x32x16_bf16 v[40:55], a[0:3], v[128:131], 0\ns_xor_b32 s73, s18, 3\ns_lshl_b32 s73, s73, 18\ns_add_u32 s73, s73, s13\n"
"buffer_load_dwordx4 v[112:115], v9, s[8:11], s73 offen sc1\ns_add_u32 s73, s73, 65536\nbuffer_load_dwordx4 v[116:119], v9, s[8:11], s73 offen sc1\ns_add_u32 s73, s73, 65536\n"
"buffer_load_dwordx4 v[120:123], v9, s[8:11], s73 offen sc1\ns_add_u32 s73, s73, 65536\nbuffer_load_dwordx4 v[124:127], v9, s[8:11], s73 offen sc1\nbuffer_load_dwordx4 a[0:3], v10, s[8:11], s76 offen offset:0 sc1\n"
"s_waitcnt vmcnt(63)\nv_mfma_f32_32x32x16_bf16 v[40:55], a[4:7], v[132:135], v[40:55]\nbuffer_load_dwordx4 a[4:7], v10, s[8:11], s76 offen offset:512 sc1\ns_waitcnt vmcnt(63)\n"
"v_mfma_f32_32x32x16_bf16 v[40:55], a[8:11], v[136:139], v[40:55]\nbuffer_load_dwordx4 a[8:11], v10, s[8:11], s76 offen offset:1024 sc1\ns_waitcnt vmcnt(63)\nv_mfma_f32_32x32x16_bf16 v[40:55], a[12:15], v[140:143], v[40:55]\n"
"buffer_load_dwordx4 a[12:15], v10, s[8:11], s76 offen offset:1536 sc1\ns_waitcnt vmcnt(63)\nv_mfma_f32_32x32x16_bf16 v[40:55], a[16:19], v[144:147], v[40:55]\nbuffer_load_dwordx4 a[16:19], v10, s[8:11], s76 offen offset:2048 sc1\n"
"s_waitcnt vmcnt(63)\nv_mfma_f32_32x32x16_bf16 v[40:55], a[20:23], v[148:151], v[40:55]\nbuffer_load_dwordx4 a[20:23], v10, s[8:11], s76 offen offset:2560 sc1\ns_waitcnt vmcnt(63)\n"
"v_mfma_f32_32x32x16_bf16 v[40:55], a[24:27], v[152:155], v[40:55]\nbuffer_load_dwordx4 a[24:27], v10, s[8:11], s76 offen offset:3072 sc1\ns_waitcnt vmcnt(63)\nv_mfma_f32_32x32x16_bf16 v[40:55], a[28:31], v[156:159], v[40:55]\n"
"buffer_load_dwordx4 a[28:31], v10, s[8:11], s76 offen offset:3584 sc1\ns_waitcnt vmcnt(63)\nv_mfma_f32_32x32x16_bf16 v[40:55], a[32:35], v[160:163], v[40:55]\nbuffer_load_dwordx4 a[32:35], v10, s[8:11], s77 offen offset:0 sc1\n"
"s_waitcnt vmcnt(63)\nv_mfma_f32_32x32x16_bf16 v[40:55], a[36:39], v[164:167], v[40:55]\nbuffer_load_dwordx4 a[36:39], v10, s[8:11], s77 offen offset:512 sc1\ns_waitcnt vmcnt(63)\n"
"v_mfma_f32_32x32x16_bf16 v[40:55], a[40:43], v[168:171], v[40:55]\nbuffer_load_dwordx4 a[40:43], v10, s[8:11], s77 offen offset:1024 sc1\ns_waitcnt vmcnt(63)\nv_mfma_f32_32x32x16_bf16 v[40:55], a[44:47], v[172:175], v[40:55]\n"
"buffer_load_dwordx4 a[44:47], v10, s[8:11], s77 offen offset:1536 sc1\ns_waitcnt vmcnt(63)\nv_mfma_f32_32x32x16_bf16 v[40:55], a[48:51], v[176:179], v[40:55]\nbuffer_load_dwordx4 a[48:51], v10, s[8:11], s77 offen offset:2048 sc1\n"
"s_waitcnt vmcnt(63)\nv_mfma_f32_32x32x16_bf16 v[40:55], a[52:55], v[180:183], v[40:55]\nbuffer_load_dwordx4 a[52:55], v10, s[8:11], s77 offen offset:2560 sc1\ns_waitcnt vmcnt(63)\n"
"v_mfma_f32_32x32x16_bf16 v[40:55], a[56:59], v[184:187], v[40:55]\nbuffer_load_dwordx4 a[56:59], v10, s[8:11], s77 offen offset:3072 sc1\ns_waitcnt vmcnt(63)\nv_mfma_f32_32x32x16_bf16 v[40:55], a[60:63], v[188:191], v[40:55]\n"
"buffer_load_dwordx4 a[60:63], v10, s[8:11], s77 offen offset:3584 sc1\ns_waitcnt vmcnt(63)\nv_mfma_f32_32x32x16_bf16 v[40:55], a[64:67], v[192:195], v[40:55]\nbuffer_load_dwordx4 a[64:67], v10, s[8:11], s78 offen offset:0 sc1\n"
"s_waitcnt vmcnt(63)\nv_mfma_f32_32x32x16_bf16 v[40:55], a[68:71], v[196:199], v[40:55]\nbuffer_load_dwordx4 a[68:71], v10, s[8:11], s78 offen offset:512 sc1\ns_waitcnt vmcnt(63)\n"
"v_mfma_f32_32x32x16_bf16 v[40:55], a[72:75], v[200:203], v[40:55]\nbuffer_load_dwordx4 a[72:75], v10, s[8:11], s78 offen offset:1024 sc1\ns_waitcnt vmcnt(63)\nv_mfma_f32_32x32x16_bf16 v[40:55], a[76:79], v[204:207], v[40:55]\n"
"buffer_load_dwordx4 a[76:79], v10, s[8:11], s78 offen offset:1536 sc1\ns_waitcnt vmcnt(63)\nv_mfma_f32_32x32x16_bf16 v[40:55], a[80:83], v[208:211], v[40:55]\nbuffer_load_dwordx4 a[80:83], v10, s[8:11], s78 offen offset:2048 sc1\n"
"s_waitcnt vmcnt(63)\nv_mfma_f32_32x32x16_bf16 v[40:55], a[84:87], v[212:215], v[40:55]\nbuffer_load_dwordx4 a[84:87], v10, s[8:11], s78 offen offset:2560 sc1\ns_waitcnt vmcnt(63)\n"
"v_mfma_f32_32x32x16_bf16 v[40:55], a[88:91], v[216:219], v[40:55]\nbuffer_load_dwordx4 a[88:91], v10, s[8:11], s78 offen offset:3072 sc1\ns_waitcnt vmcnt(63)\nv_mfma_f32_32x32x16_bf16 v[40:55], a[92:95], v[220:223], v[40:55]\n"
"buffer_load_dwordx4 a[92:95], v10, s[8:11], s78 offen offset:3584 sc1\ns_waitcnt vmcnt(63)\nv_mfma_f32_32x32x16_bf16 v[40:55], a[96:99], v[224:227], v[40:55]\nbuffer_load_dwordx4 a[96:99], v10, s[8:11], s79 offen offset:0 sc1\n"
"s_waitcnt vmcnt(63)\nv_mfma_f32_32x32x16_bf16 v[40:55], a[100:103], v[228:231], v[40:55]\nbuffer_load_dwordx4 a[100:103], v10, s[8:11], s79 offen offset:512 sc1\ns_waitcnt vmcnt(63)\n"
"v_mfma_f32_32x32x16_bf16 v[40:55], a[104:107], v[232:235], v[40:55]\nbuffer_load_dwordx4 a[104:107], v10, s[8:11], s79 offen offset:1024 sc1\ns_waitcnt vmcnt(63)\nv_mfma_f32_32x32x16_bf16 v[40:55], a[108:111], v[236:239], v[40:55]\n"
"buffer_load_dwordx4 a[108:111], v10, s[8:11], s79 offen offset:1536 sc1\ns_waitcnt vmcnt(60)\nv_add_f32 v96, v96, v100\nv_add_f32 v104, v104, v108\n"
"v_add_f32 v97, v97, v101\nv_add_f32 v105, v105, v109\nv_add_f32 v98, v98, v102\nv_add_f32 v106, v106, v110\n"
"v_add_f32 v99, v99, v103\nv_add_f32 v107, v107, v111\nv_add_f32 v96, v96, v104\nv_add_f32 v97, v97, v105\n"
"v_add_f32 v98, v98, v106\nv_add_f32 v99, v99, v107\nv_max3_f32 v28, v96, v97, v98\nv_max_f32 v28, v28, v99\n"
"v_cndmask_b32 v40, v40, v31, s[40:41]\nv_cndmask_b32 v41, v41, v31, s[42:43]\nv_cndmask_b32 v42, v42, v31, s[44:45]\nv_cndmask_b32 v43, v43, v31, s[46:47]\n"
"v_cndmask_b32 v44, v44, v31, s[48:49]\nv_cndmask_b32 v45, v45, v31, s[50:51]\nv_cndmask_b32 v46, v46, v31, s[52:53]\nv_cndmask_b32 v47, v47, v31, s[54:55]\n"
"v_cndmask_b32 v48, v48, v31, s[56:57]\nv_cndmask_b32 v49, v49, v31, s[58:59]\nv_cndmask_b32 v50, v50, v31, s[60:61]\nv_cndmask_b32 v51, v51, v31, s[62:63]\n"
"v_cndmask_b32 v52, v52, v31, s[64:65]\nv_cndmask_b32 v53, v53, v31, s[66:67]\nv_cndmask_b32 v54, v54, v31, s[68:69]\nv_cndmask_b32 v55, v55, v31, s[70:71]\n"
"v_max3_f32 v28, v28, v40, v41\nv_max3_f32 v28, v28, v42, v43\nv_max3_f32 v28, v28, v44, v45\nv_max3_f32 v28, v28, v46, v47\n"
"v_max3_f32 v28, v28, v48, v49\nv_max3_f32 v28, v28, v50, v51\nv_max3_f32 v28, v28, v52, v53\nv_max3_f32 v28, v28, v54, v55\n"
"v_mov_b32 v30, v28\ns_nop 1\nv_permlane32_swap_b32 v28, v30\nv_max_f32 v28, v28, v30\n"
"v_sub_f32 v40, v40, v28\nv_sub_f32 v41, v41, v28\nv_sub_f32 v42, v42, v28\nv_sub_f32 v43, v43, v28\n"
"v_sub_f32 v44, v44, v28\nv_sub_f32 v45, v45, v28\nv_sub_f32 v46, v46, v28\nv_sub_f32 v47, v47, v28\n"
"v_sub_f32 v48, v48, v28\nv_sub_f32 v49, v49, v28\nv_sub_f32 v50, v50, v28\nv_sub_f32 v51, v51, v28\n"
"v_sub_f32 v52, v52, v28\nv_sub_f32 v53, v53, v28\nv_sub_f32 v54, v54, v28\nv_sub_f32 v55, v55, v28\n"
"v_sub_f32 v96, v96, v28\nv_sub_f32 v97, v97, v28\nv_sub_f32 v98, v98, v28\nv_sub_f32 v99, v99, v28\n"
"v_exp_f32 v40, v40\nv_exp_f32 v41, v41\nv_exp_f32 v42, v42\nv_exp_f32 v43, v43\n"
"v_exp_f32 v44, v44\nv_exp_f32 v45, v45\nv_exp_f32 v46, v46\nv_exp_f32 v47, v47\n"
"v_exp_f32 v48, v48\nv_exp_f32 v49, v49\nv_exp_f32 v50, v50\nv_exp_f32 v51, v51\n"
"v_exp_f32 v52, v52\nv_exp_f32 v53, v53\nv_exp_f32 v54, v54\nv_exp_f32 v55, v55\n"
"v_exp_f32 v96, v96\nv_exp_f32 v97, v97\nv_exp_f32 v98, v98\nv_exp_f32 v99, v99\n"
"v_add_f32 v29, v40, v41\nv_add_f32 v30, v42, v43\nv_add_f32 v29, v29, v44\nv_add_f32 v30, v30, v45\n"
"v_add_f32 v29, v29, v46\nv_add_f32 v30, v30, v47\nv_add_f32 v29, v29, v48\nv_add_f32 v30, v30, v49\n"
"v_add_f32 v29, v29, v50\nv_add_f32 v30, v30, v51\nv_add_f32 v29, v29, v52\nv_add_f32 v30, v30, v53\n"
"v_add_f32 v29, v29, v54\nv_add_f32 v30, v30, v55\nv_add_f32 v29, v29, v96\nv_add_f32 v30, v30, v97\n"
"v_add_f32 v29, v29, v98\nv_add_f32 v30, v30, v99\nv_add_f32 v29, v29, v30\nv_mov_b32 v30, v29\n"
"s_nop 1\nv_permlane32_swap_b32 v29, v30\nv_add_f32 v29, v29, v30\nv_rcp_f32 v29, v29\n"
"s_nop 0\nv_mul_f32 v40, v40, v29\nv_mul_f32 v41, v41, v29\nv_mul_f32 v42, v42, v29\n"
"v_mul_f32 v43, v43, v29\nv_mul_f32 v44, v44, v29\nv_mul_f32 v45, v45, v29\nv_mul_f32 v46, v46, v29\n"
"v_mul_f32 v47, v47, v29\nv_mul_f32 v48, v48, v29\nv_mul_f32 v49, v49, v29\nv_mul_f32 v50, v50, v29\n"
"v_mul_f32 v51, v51, v29\nv_mul_f32 v52, v52, v29\nv_mul_f32 v53, v53, v29\nv_mul_f32 v54, v54, v29\n"
"v_mul_f32 v55, v55, v29\nv_mul_f32 v96, v96, v29\nv_mul_f32 v97, v97, v29\nv_mul_f32 v98, v98, v29\n"
"v_mul_f32 v99, v99, v29\nv_cvt_pk_bf16_f32 v56, v40, v41\nv_cvt_pk_bf16_f32 v57, v42, v43\nv_cvt_pk_bf16_f32 v58, v44, v45\n"
"v_cvt_pk_bf16_f32 v59, v46, v47\nv_cvt_pk_bf16_f32 v60, v48, v49\nv_cvt_pk_bf16_f32 v61, v50, v51\nv_cvt_pk_bf16_f32 v62, v52, v53\n"
"v_cvt_pk_bf16_f32 v63, v54, v55\nv_cvt_pk_bf16_f32 v34, v96, v97\nv_cvt_pk_bf16_f32 v38, v98, v99\nv_xor_b32 v30, 4096, v14\n"
"ds_write_b128 v30, v[56:59]\nds_write_b128 v30, v[60:63] offset:1024\ns_waitcnt vmcnt(59)\nv_mfma_f32_32x32x16_bf16 v[40:55], a[112:115], v[128:131], 0\n"
"buffer_load_dwordx4 a[112:115], v10, s[8:11], s79 offen offset:2048 sc1\ns_waitcnt vmcnt(59)\nv_mfma_f32_32x32x16_bf16 v[40:55], a[116:119], v[132:135], v[40:55]\nbuffer_load_dwordx4 a[116:119], v10, s[8:11], s79 offen offset:2560 sc1\n"
"s_waitcnt vmcnt(59)\nv_mfma_f32_32x32x16_bf16 v[40:55], a[120:123], v[136:139], v[40:55]\nbuffer_load_dwordx4 a[120:123], v10, s[8:11], s79 offen offset:3072 sc1\ns_waitcnt vmcnt(59)\n"
"v_mfma_f32_32x32x16_bf16 v[40:55], a[124:127], v[140:143], v[40:55]\nbuffer_load_dwordx4 a[124:127], v10, s[8:11], s79 offen offset:3584 sc1\ns_waitcnt vmcnt(59)\nv_mfma_f32_32x32x16_bf16 v[40:55], a[128:131], v[144:147], v[40:55]\n"
"buffer_load_dwordx4 a[128:131], v11, s[8:11], s80 offen offset:0 sc1\ns_waitcnt vmcnt(59)\nv_mfma_f32_32x32x16_bf16 v[40:55], a[132:135], v[148:151], v[40:55]\nbuffer_load_dwordx4 a[132:135], v11, s[8:11], s80 offen offset:256 sc1\n"
"s_waitcnt vmcnt(59)\nv_mfma_f32_32x32x16_bf16 v[40:55], a[136:139], v[152:155], v[40:55]\nbuffer_load_dwordx4 a[136:139], v11, s[8:11], s80 offen offset:1024 sc1\ns_waitcnt vmcnt(59)\n"
"v_mfma_f32_32x32x16_bf16 v[40:55], a[140:143], v[156:159], v[40:55]\nbuffer_load_dwordx4 a[140:143], v11, s[8:11], s80 offen offset:1280 sc1\ns_waitcnt vmcnt(59)\nv_mfma_f32_32x32x16_bf16 v[40:55], a[144:147], v[160:163], v[40:55]\n"
"buffer_load_dwordx4 a[144:147], v11, s[8:11], s80 offen offset:2048 sc1\ns_waitcnt vmcnt(59)\nv_mfma_f32_32x32x16_bf16 v[40:55], a[148:151], v[164:167], v[40:55]\nbuffer_load_dwordx4 a[148:151], v11, s[8:11], s80 offen offset:2304 sc1\n"
"s_waitcnt vmcnt(59)\nv_mfma_f32_32x32x16_bf16 v[40:55], a[152:155], v[168:171], v[40:55]\nbuffer_load_dwordx4 a[152:155], v11, s[8:11], s80 offen offset:3072 sc1\ns_waitcnt vmcnt(59)\n"
"v_mfma_f32_32x32x16_bf16 v[40:55], a[156:159], v[172:175], v[40:55]\nbuffer_load_dwordx4 a[156:159], v11, s[8:11], s80 offen offset:3328 sc1\ns_waitcnt vmcnt(59)\nv_mfma_f32_32x32x16_bf16 v[40:55], a[160:163], v[176:179], v[40:55]\n"
"s_waitcnt vmcnt(58)\nv_mfma_f32_32x32x16_bf16 v[40:55], a[164:167], v[180:183], v[40:55]\ns_waitcnt vmcnt(57)\nv_mfma_f32_32x32x16_bf16 v[40:55], a[168:171], v[184:187], v[40:55]\n"
"s_waitcnt vmcnt(56)\nv_mfma_f32_32x32x16_bf16 v[40:55], a[172:175], v[188:191], v[40:55]\ns_waitcnt vmcnt(55)\nv_mfma_f32_32x32x16_bf16 v[40:55], a[176:179], v[192:195], v[40:55]\n"
"s_waitcnt vmcnt(54)\nv_mfma_f32_32x32x16_bf16 v[40:55], a[180:183], v[196:199], v[40:55]\ns_waitcnt vmcnt(53)\nv_mfma_f32_32x32x16_bf16 v[40:55], a[184:187], v[200:203], v[40:55]\n"
"s_waitcnt vmcnt(52)\nv_mfma_f32_32x32x16_bf16 v[40:55], a[188:191], v[204:207], v[40:55]\ns_waitcnt vmcnt(51)\nv_mfma_f32_32x32x16_bf16 v[40:55], a[192:195], v[208:211], v[40:55]\n"
"s_waitcnt vmcnt(50)\nv_mfma_f32_32x32x16_bf16 v[40:55], a[196:199], v[212:215], v[40:55]\ns_waitcnt vmcnt(49)\nv_mfma_f32_32x32x16_bf16 v[40:55], a[200:203], v[216:219], v[40:55]\n"
"s_waitcnt vmcnt(48)\nv_mfma_f32_32x32x16_bf16 v[40:55], a[204:207], v[220:223], v[40:55]\ns_waitcnt vmcnt(47)\nv_mfma_f32_32x32x16_bf16 v[40:55], a[208:211], v[224:227], v[40:55]\n"
"s_waitcnt vmcnt(46)\nv_mfma_f32_32x32x16_bf16 v[40:55], a[212:215], v[228:231], v[40:55]\ns_waitcnt vmcnt(45)\nv_mfma_f32_32x32x16_bf16 v[40:55], a[216:219], v[232:235], v[40:55]\n"
"s_waitcnt vmcnt(44)\nv_mfma_f32_32x32x16_bf16 v[40:55], a[220:223], v[236:239], v[40:55]\ns_waitcnt vmcnt(40)\nv_add_f32 v112, v112, v116\n"
"v_add_f32 v120, v120, v124\nv_add_f32 v113, v113, v117\nv_add_f32 v121, v121, v125\nv_add_f32 v114, v114, v118\n"
"v_add_f32 v122, v122, v126\nv_add_f32 v115, v115, v119\nv_add_f32 v123, v123, v127\nv_add_f32 v112, v112, v120\n"
"v_add_f32 v113, v113, v121\nv_add_f32 v114, v114, v122\nv_add_f32 v115, v115, v123\nv_max3_f32 v28, v112, v113, v114\n"
"v_max_f32 v28, v28, v115\nv_cndmask_b32 v40, v40, v31, s[40:41]\nv_cndmask_b32 v41, v41, v31, s[42:43]\nv_cndmask_b32 v42, v42, v31, s[44:45]\n"
"v_cndmask_b32 v43, v43, v31, s[46:47]\nv_cndmask_b32 v44, v44, v31, s[48:49]\nv_cndmask_b32 v45, v45, v31, s[50:51]\nv_cndmask_b32 v46, v46, v31, s[52:53]\n"
"v_cndmask_b32 v47, v47, v31, s[54:55]\nv_cndmask_b32 v48, v48, v31, s[56:57]\nv_cndmask_b32 v49, v49, v31, s[58:59]\nv_cndmask_b32 v50, v50, v31, s[60:61]\n"
"v_cndmask_b32 v51, v51, v31, s[62:63]\nv_cndmask_b32 v52, v52, v31, s[64:65]\nv_cndmask_b32 v53, v53, v31, s[66:67]\nv_cndmask_b32 v54, v54, v31, s[68:69]\n"
"v_cndmask_b32 v55, v55, v31, s[70:71]\nv_max3_f32 v28, v28, v40, v41\nv_max3_f32 v28, v28, v42, v43\nv_max3_f32 v28, v28, v44, v45\n"
"v_max3_f32 v28, v28, v46, v47\nv_max3_f32 v28, v28, v48, v49\nv_max3_f32 v28, v28, v50, v51\nv_max3_f32 v28, v28, v52, v53\n"
"v_max3_f32 v28, v28, v54, v55\nv_mov_b32 v30, v28\ns_nop 1\nv_permlane32_swap_b32 v28, v30\n"
"v_max_f32 v28, v28, v30\nv_sub_f32 v40, v40, v28\nv_sub_f32 v41, v41, v28\nv_sub_f32 v42, v42, v28\n"
"v_sub_f32 v43, v43, v28\nv_sub_f32 v44, v44, v28\nv_sub_f32 v45, v45, v28\nv_sub_f32 v46, v46, v28\n"
"v_sub_f32 v47, v47, v28\nv_sub_f32 v48, v48, v28\nv_sub_f32 v49, v49, v28\nv_sub_f32 v50, v50, v28\n"
"v_sub_f32 v51, v51, v28\nv_sub_f32 v52, v52, v28\nv_sub_f32 v53, v53, v28\nv_sub_f32 v54, v54, v28\n"
"v_sub_f32 v55, v55, v28\nv_sub_f32 v112, v112, v28\nv_sub_f32 v113, v113, v28\nv_sub_f32 v114, v114, v28\n"
"v_sub_f32 v115, v115, v28\nv_exp_f32 v40, v40\nv_exp_f32 v41, v41\nv_exp_f32 v42, v42\n"
"v_exp_f32 v43, v43\nv_exp_f32 v44, v44\nv_exp_f32 v45, v45\nv_exp_f32 v46, v46\n"
"v_exp_f32 v47, v47\nv_exp_f32 v48, v48\nv_exp_f32 v49, v49\nv_exp_f32 v50, v50\n"
"v_exp_f32 v51, v51\nv_exp_f32 v52, v52\nv_exp_f32 v53, v53\nv_exp_f32 v54, v54\n"
"v_exp_f32 v55, v55\nv_exp_f32 v112, v112\nv_exp_f32 v113, v113\nv_exp_f32 v114, v114\n"
"v_exp_f32 v115, v115\nv_add_f32 v29, v40, v41\nv_add_f32 v30, v42, v43\nv_add_f32 v29, v29, v44\n"
"v_add_f32 v30, v30, v45\nv_add_f32 v29, v29, v46\nv_add_f32 v30, v30, v47\nv_add_f32 v29, v29, v48\n"
"v_add_f32 v30, v30, v49\nv_add_f32 v29, v29, v50\nv_add_f32 v30, v30, v51\nv_add_f32 v29, v29, v52\n"
"v_add_f32 v30, v30, v53\nv_add_f32 v29, v29, v54\nv_add_f32 v30, v30, v55\nv_add_f32 v29, v29, v112\n"
"v_add_f32 v30, v30, v113\nv_add_f32 v29, v29, v114\nv_add_f32 v30, v30, v115\nv_add_f32 v29, v29, v30\n"
"v_mov_b32 v30, v29\ns_nop 1\nv_permlane32_swap_b32 v29, v30\nv_add_f32 v29, v29, v30\n"
"v_rcp_f32 v29, v29\ns_nop 0\nv_mul_f32 v40, v40, v29\nv_mul_f32 v41, v41, v29\n"
"v_mul_f32 v42, v42, v29\nv_mul_f32 v43, v43, v29\nv_mul_f32 v44, v44, v29\nv_mul_f32 v45, v45, v29\n"
"v_mul_f32 v46, v46, v29\nv_mul_f32 v47, v47, v29\nv_mul_f32 v48, v48, v29\nv_mul_f32 v49, v49, v29\n"
"v_mul_f32 v50, v50, v29\nv_mul_f32 v51, v51, v29\nv_mul_f32 v52, v52, v29\nv_mul_f32 v53, v53, v29\n"
"v_mul_f32 v54, v54, v29\nv_mul_f32 v55, v55, v29\nv_mul_f32 v112, v112, v29\nv_mul_f32 v113, v113, v29\n"
"v_mul_f32 v114, v114, v29\nv_mul_f32 v115, v115, v29\nv_cvt_pk_bf16_f32 v56, v40, v41\nv_cvt_pk_bf16_f32 v57, v42, v43\n"
"v_cvt_pk_bf16_f32 v58, v44, v45\nv_cvt_pk_bf16_f32 v59, v46, v47\nv_cvt_pk_bf16_f32 v60, v48, v49\nv_cvt_pk_bf16_f32 v61, v50, v51\n"
"v_cvt_pk_bf16_f32 v62, v52, v53\nv_cvt_pk_bf16_f32 v63, v54, v55\nv_cvt_pk_bf16_f32 v35, v112, v113\nv_cvt_pk_bf16_f32 v39, v114, v115\n"
"v_xor_b32 v30, 6144, v14\nds_write_b128 v30, v[56:59]\nds_write_b128 v30, v[60:63] offset:1024\nds_write_b128 v15, v[32:35]\n"
"ds_write_b128 v15, v[36:39] offset:1024\ns_waitcnt lgkmcnt(0)\ns_barrier\nds_read_b128 v[96:99], v16 offset:0\n"
"ds_read_b128 v[100:103], v16 offset:1024\nds_read_b128 v[104:107], v16 offset:2048\nds_read_b128 v[108:111], v16 offset:3072\nds_read_b128 v[112:115], v16 offset:4096\n"
"ds_read_b128 v[116:119], v16 offset:5120\nds_read_b128 v[120:123], v16 offset:6144\nds_read_b128 v[124:127], v16 offset:7168\nds_read_b128 v[128:131], v16 offset:8192\n"
"ds_read_b128 v[132:135], v16 offset:9216\nds_read_b128 v[136:139], v16 offset:10240\nds_read_b128 v[140:143], v16 offset:11264\ns_waitcnt lgkmcnt(0)\n"
"ds_read_b128 v[144:147], v16 offset:12288\nds_read_b128 v[148:151], v16 offset:13312\nds_read_b128 v[152:155], v16 offset:14336\nds_read_b128 v[156:159], v16 offset:15360\n"
"ds_read_b128 v[160:163], v16 offset:16384\nds_read_b128 v[164:167], v16 offset:17408\nds_read_b128 v[168:171], v16 offset:18432\nds_read_b128 v[172:175], v16 offset:19456\n"
"ds_read_b128 v[176:179], v16 offset:20480\nds_read_b128 v[180:183], v16 offset:21504\nds_read_b128 v[184:187], v16 offset:22528\nds_read_b128 v[188:191], v16 offset:23552\n"
"s_waitcnt lgkmcnt(0)\nds_read_b128 v[192:195], v16 offset:24576\nds_read_b128 v[196:199], v16 offset:25600\nds_read_b128 v[200:203], v16 offset:26624\n"
"ds_read_b128 v[204:207], v16 offset:27648\nds_read_b128 v[208:211], v16 offset:28672\nds_read_b128 v[212:215], v16 offset:29696\nds_read_b128 v[216:219], v16 offset:30720\n"
"ds_read_b128 v[220:223], v16 offset:31744\nds_read_b128 v[224:227], v16 offset:32768\nds_read_b128 v[228:231], v16 offset:33792\nds_read_b128 v[232:235], v16 offset:34816\n"
"ds_read_b128 v[236:239], v16 offset:35840\ns_waitcnt lgkmcnt(0)\nds_read_b128 v[240:243], v16 offset:36864\nds_read_b128 v[244:247], v16 offset:37888\n"
"ds_read_b128 v[248:251], v16 offset:38912\nds_read_b128 v[252:255], v16 offset:39936\nv_mov_b32 v24, v17\ns_sub_u32 s72, s17, 1\n"
"s_waitcnt lgkmcnt(0)\nfo_loop:\ns_add_u32 s76, s76, 8388608\ns_add_u32 s77, s77, 8388608\n"
"s_add_u32 s78, s78, 8388608\ns_add_u32 s79, s79, 8388608\ns_add_u32 s80, s80, 2097152\ns_waitcnt vmcnt(39)\n"
"v_mfma_f32_32x32x16_bf16 v[40:55], a[0:3], v[96:99], 0\nbuffer_load_dwordx4 a[0:3], v10, s[8:11], s76 offen offset:0 sc1\ns_waitcnt vmcnt(39)\nv_mfma_f32_32x32x16_bf16 v[40:55], a[4:7], v[100:103], v[40:55]\n"
"buffer_load_dwordx4 a[4:7], v10, s[8:11], s76 offen offset:512 sc1\ns_waitcnt vmcnt(39)\nv_mfma_f32_32x32x16_bf16 v[40:55], a[8:11], v[104:107], v[40:55]\nbuffer_load_dwordx4 a[8:11], v10, s[8:11], s76 offen offset:1024 sc1\n"
"s_waitcnt vmcnt(39)\nv_mfma_f32_32x32x16_bf16 v[40:55], a[12:15], v[108:111], v[40:55]\nbuffer_load_dwordx4 a[12:15], v10, s[8:11], s76 offen offset:1536 sc1\ns_waitcnt vmcnt(39)\n"
"v_mfma_f32_32x32x16_bf16 v[40:55], a[16:19], v[112:115], v[40:55]\nbuffer_load_dwordx4 a[16:19], v10, s[8:11], s76 offen offset:2048 sc1\ns_waitcnt vmcnt(39)\nv_mfma_f32_32x32x16_bf16 v[40:55], a[20:23], v[116:119], v[40:55]\n"
"buffer_load_dwordx4 a[20:23], v10, s[8:11], s76 offen offset:2560 sc1\ns_waitcnt vmcnt(39)\nv_mfma_f32_32x32x16_bf16 v[40:55], a[24:27], v[120:123], v[40:55]\nbuffer_load_dwordx4 a[24:27], v10, s[8:11], s76 offen offset:3072 sc1\n"
"s_waitcnt vmcnt(39)\nv_mfma_f32_32x32x16_bf16 v[40:55], a[28:31], v[124:127], v[40:55]\nbuffer_load_dwordx4 a[28:31], v10, s[8:11], s76 offen offset:3584 sc1\ns_waitcnt vmcnt(39)\n"
"v_mfma_f32_32x32x16_bf16 v[40:55], a[32:35], v[128:131], v[40:55]\nbuffer_load_dwordx4 a[32:35], v10, s[8:11], s77 offen offset:0 sc1\ns_waitcnt vmcnt(39)\nv_mfma_f32_32x32x16_bf16 v[40:55], a[36:39], v[132:135], v[40:55]\n"
"buffer_load_dwordx4 a[36:39], v10, s[8:11], s77 offen offset:512 sc1\ns_waitcnt vmcnt(39)\nv_mfma_f32_32x32x16_bf16 v[40:55], a[40:43], v[136:139], v[40:55]\nbuffer_load_dwordx4 a[40:43], v10, s[8:11], s77 offen offset:1024 sc1\n"
"s_waitcnt vmcnt(39)\nv_mfma_f32_32x32x16_bf16 v[40:55], a[44:47], v[140:143], v[40:55]\nbuffer_load_dwordx4 a[44:47], v10, s[8:11], s77 offen offset:1536 sc1\ns_waitcnt vmcnt(39)\n"
"v_mfma_f32_32x32x16_bf16 v[40:55], a[48:51], v[144:147], v[40:55]\nbuffer_load_dwordx4 a[48:51], v10, s[8:11], s77 offen offset:2048 sc1\ns_waitcnt vmcnt(39)\nv_mfma_f32_32x32x16_bf16 v[40:55], a[52:55], v[148:151], v[40:55]\n"
"buffer_load_dwordx4 a[52:55], v10, s[8:11], s77 offen offset:2560 sc1\ns_waitcnt vmcnt(39)\nv_mfma_f32_32x32x16_bf16 v[40:55], a[56:59], v[152:155], v[40:55]\nbuffer_load_dwordx4 a[56:59], v10, s[8:11], s77 offen offset:3072 sc1\n"
"s_waitcnt vmcnt(39)\nv_mfma_f32_32x32x16_bf16 v[40:55], a[60:63], v[156:159], v[40:55]\nbuffer_load_dwordx4 a[60:63], v10, s[8:11], s77 offen offset:3584 sc1\ns_waitcnt vmcnt(39)\n"
"v_mfma_f32_32x32x16_bf16 v[40:55], a[64:67], v[160:163], v[40:55]\nbuffer_load_dwordx4 a[64:67], v10, s[8:11], s78 offen offset:0 sc1\ns_waitcnt vmcnt(39)\nv_mfma_f32_32x32x16_bf16 v[40:55], a[68:71], v[164:167], v[40:55]\n"
"buffer_load_dwordx4 a[68:71], v10, s[8:11], s78 offen offset:512 sc1\ns_waitcnt vmcnt(39)\nv_mfma_f32_32x32x16_bf16 v[40:55], a[72:75], v[168:171], v[40:55]\nbuffer_load_dwordx4 a[72:75], v10, s[8:11], s78 offen offset:1024 sc1\n"
"s_waitcnt vmcnt(39)\nv_mfma_f32_32x32x16_bf16 v[40:55], a[76:79], v[172:175], v[40:55]\nbuffer_load_dwordx4 a[76:79], v10, s[8:11], s78 offen offset:1536 sc1\ns_waitcnt vmcnt(39)\n"
"v_mfma_f32_32x32x16_bf16 v[40:55], a[80:83], v[176:179], v[40:55]\nbuffer_load_dwordx4 a[80:83], v10, s[8:11], s78 offen offset:2048 sc1\ns_waitcnt vmcnt(39)\nv_mfma_f32_32x32x16_bf16 v[40:55], a[84:87], v[180:183], v[40:55]\n"
"buffer_load_dwordx4 a[84:87], v10, s[8:11], s78 offen offset:2560 sc1\ns_waitcnt vmcnt(39)\nv_mfma_f32_32x32x16_bf16 v[40:55], a[88:91], v[184:187], v[40:55]\nbuffer_load_dwordx4 a[88:91], v10, s[8:11], s78 offen offset:3072 sc1\n"
"s_waitcnt vmcnt(39)\nv_mfma_f32_32x32x16_bf16 v[40:55], a[92:95], v[188:191], v[40:55]\nbuffer_load_dwordx4 a[92:95], v10, s[8:11], s78 offen offset:3584 sc1\ns_waitcnt vmcnt(39)\n"
"v_mfma_f32_32x32x16_bf16 v[40:55], a[96:99], v[192:195], v[40:55]\nbuffer_load_dwordx4 a[96:99], v10, s[8:11], s79 offen offset:0 sc1\ns_waitcnt vmcnt(39)\nv_mfma_f32_32x32x16_bf16 v[40:55], a[100:103], v[196:199], v[40:55]\n"
"buffer_load_dwordx4 a[100:103], v10, s[8:11], s79 offen offset:512 sc1\ns_waitcnt vmcnt(39)\nv_mfma_f32_32x32x16_bf16 v[40:55], a[104:107], v[200:203], v[40:55]\nbuffer_load_dwordx4 a[104:107], v10, s[8:11], s79 offen offset:1024 sc1\n"
"s_waitcnt vmcnt(39)\nv_mfma_f32_32x32x16_bf16 v[40:55], a[108:111], v[204:207], v[40:55]\nbuffer_load_dwordx4 a[108:111], v10, s[8:11], s79 offen offset:1536 sc1\ns_waitcnt vmcnt(39)\n"
"v_mfma_f32_32x32x16_bf16 v[40:55], a[112:115], v[208:211], v[40:55]\nbuffer_load_dwordx4 a[112:115], v10, s[8:11], s79 offen offset:2048 sc1\ns_waitcnt vmcnt(39)\nv_mfma_f32_32x32x16_bf16 v[40:55], a[116:119], v[212:215], v[40:55]\n"
"buffer_load_dwordx4 a[116:119], v10, s[8:11], s79 offen offset:2560 sc1\ns_waitcnt vmcnt(39)\nv_mfma_f32_32x32x16_bf16 v[40:55], a[120:123], v[216:219], v[40:55]\nbuffer_load_dwordx4 a[120:123], v10, s[8:11], s79 offen offset:3072 sc1\n"
"s_waitcnt vmcnt(39)\nv_mfma_f32_32x32x16_bf16 v[40:55], a[124:127], v[220:223], v[40:55]\nbuffer_load_dwordx4 a[124:127], v10, s[8:11], s79 offen offset:3584 sc1\ns_waitcnt vmcnt(39)\n"
"v_mfma_f32_32x32x16_bf16 v[40:55], a[128:131], v[224:227], v[40:55]\nbuffer_load_dwordx4 a[128:131], v11, s[8:11], s80 offen offset:0 sc1\ns_waitcnt vmcnt(39)\nv_mfma_f32_32x32x16_bf16 v[40:55], a[132:135], v[228:231], v[40:55]\n"
"buffer_load_dwordx4 a[132:135], v11, s[8:11], s80 offen offset:256 sc1\ns_waitcnt vmcnt(39)\nv_mfma_f32_32x32x16_bf16 v[40:55], a[136:139], v[232:235], v[40:55]\nbuffer_load_dwordx4 a[136:139], v11, s[8:11], s80 offen offset:1024 sc1\n"
"s_waitcnt vmcnt(39)\nv_mfma_f32_32x32x16_bf16 v[40:55], a[140:143], v[236:239], v[40:55]\nbuffer_load_dwordx4 a[140:143], v11, s[8:11], s80 offen offset:1280 sc1\ns_waitcnt vmcnt(39)\n"
"v_mfma_f32_32x32x16_bf16 v[40:55], a[144:147], v[240:243], v[40:55]\nbuffer_load_dwordx4 a[144:147], v11, s[8:11], s80 offen offset:2048 sc1\ns_waitcnt vmcnt(39)\nv_mfma_f32_32x32x16_bf16 v[40:55], a[148:151], v[244:247], v[40:55]\n"
"buffer_load_dwordx4 a[148:151], v11, s[8:11], s80 offen offset:2304 sc1\ns_waitcnt vmcnt(39)\nv_mfma_f32_32x32x16_bf16 v[40:55], a[152:155], v[248:251], v[40:55]\nbuffer_load_dwordx4 a[152:155], v11, s[8:11], s80 offen offset:3072 sc1\n"
"s_waitcnt vmcnt(39)\nv_mfma_f32_32x32x16_bf16 v[40:55], a[156:159], v[252:255], v[40:55]\nbuffer_load_dwordx4 a[156:159], v11, s[8:11], s80 offen offset:3328 sc1\ns_nop 11\n"
"v_cvt_pk_bf16_f32 v56, v40, v41\nv_cvt_pk_bf16_f32 v57, v42, v43\nv_cvt_pk_bf16_f32 v58, v44, v45\nv_cvt_pk_bf16_f32 v59, v46, v47\n"
"v_cvt_pk_bf16_f32 v60, v48, v49\nv_cvt_pk_bf16_f32 v61, v50, v51\nv_cvt_pk_bf16_f32 v62, v52, v53\nv_cvt_pk_bf16_f32 v63, v54, v55\n"
"ds_write_b128 v24, v[56:59]\nds_write_b128 v24, v[60:63] offset:32\nv_add_u32 v24, 256, v24\ns_sub_u32 s72, s72, 1\n"
"s_cmp_lg_u32 s72, 0\ns_cbranch_scc1 fo_loop\ns_waitcnt vmcnt(39)\nv_mfma_f32_32x32x16_bf16 v[40:55], a[0:3], v[96:99], 0\n"
"s_waitcnt vmcnt(38)\nv_mfma_f32_32x32x16_bf16 v[40:55], a[4:7], v[100:103], v[40:55]\ns_waitcnt vmcnt(37)\nv_mfma_f32_32x32x16_bf16 v[40:55], a[8:11], v[104:107], v[40:55]\n"
"s_waitcnt vmcnt(36)\nv_mfma_f32_32x32x16_bf16 v[40:55], a[12:15], v[108:111], v[40:55]\ns_waitcnt vmcnt(35)\nv_mfma_f32_32x32x16_bf16 v[40:55], a[16:19], v[112:115], v[40:55]\n"
"s_waitcnt vmcnt(34)\nv_mfma_f32_32x32x16_bf16 v[40:55], a[20:23], v[116:119], v[40:55]\ns_waitcnt vmcnt(33)\nv_mfma_f32_32x32x16_bf16 v[40:55], a[24:27], v[120:123], v[40:55]\n"
"s_waitcnt vmcnt(32)\nv_mfma_f32_32x32x16_bf16 v[40:55], a[28:31], v[124:127], v[40:55]\ns_waitcnt vmcnt(31)\nv_mfma_f32_32x32x16_bf16 v[40:55], a[32:35], v[128:131], v[40:55]\n"
"s_waitcnt vmcnt(30)\nv_mfma_f32_32x32x16_bf16 v[40:55], a[36:39], v[132:135], v[40:55]\ns_waitcnt vmcnt(29)\nv_mfma_f32_32x32x16_bf16 v[40:55], a[40:43], v[136:139], v[40:55]\n"
"s_waitcnt vmcnt(28)\nv_mfma_f32_32x32x16_bf16 v[40:55], a[44:47], v[140:143], v[40:55]\ns_waitcnt vmcnt(27)\nv_mfma_f32_32x32x16_bf16 v[40:55], a[48:51], v[144:147], v[40:55]\n"
"s_waitcnt vmcnt(26)\nv_mfma_f32_32x32x16_bf16 v[40:55], a[52:55], v[148:151], v[40:55]\ns_waitcnt vmcnt(25)\nv_mfma_f32_32x32x16_bf16 v[40:55], a[56:59], v[152:155], v[40:55]\n"
"s_waitcnt vmcnt(24)\nv_mfma_f32_32x32x16_bf16 v[40:55], a[60:63], v[156:159], v[40:55]\ns_waitcnt vmcnt(23)\nv_mfma_f32_32x32x16_bf16 v[40:55], a[64:67], v[160:163], v[40:55]\n"
"s_waitcnt vmcnt(22)\nv_mfma_f32_32x32x16_bf16 v[40:55], a[68:71], v[164:167], v[40:55]\ns_waitcnt vmcnt(21)\nv_mfma_f32_32x32x16_bf16 v[40:55], a[72:75], v[168:171], v[40:55]\n"
"s_waitcnt vmcnt(20)\nv_mfma_f32_32x32x16_bf16 v[40:55], a[76:79], v[172:175], v[40:55]\ns_waitcnt vmcnt(19)\nv_mfma_f32_32x32x16_bf16 v[40:55], a[80:83], v[176:179], v[40:55]\n"
"s_waitcnt vmcnt(18)\nv_mfma_f32_32x32x16_bf16 v[40:55], a[84:87], v[180:183], v[40:55]\ns_waitcnt vmcnt(17)\nv_mfma_f32_32x32x16_bf16 v[40:55], a[88:91], v[184:187], v[40:55]\n"
"s_waitcnt vmcnt(16)\nv_mfma_f32_32x32x16_bf16 v[40:55], a[92:95], v[188:191], v[40:55]\ns_waitcnt vmcnt(15)\nv_mfma_f32_32x32x16_bf16 v[40:55], a[96:99], v[192:195], v[40:55]\n"
"s_waitcnt vmcnt(14)\nv_mfma_f32_32x32x16_bf16 v[40:55], a[100:103], v[196:199], v[40:55]\ns_waitcnt vmcnt(13)\nv_mfma_f32_32x32x16_bf16 v[40:55], a[104:107], v[200:203], v[40:55]\n"
"s_waitcnt vmcnt(12)\nv_mfma_f32_32x32x16_bf16 v[40:55], a[108:111], v[204:207], v[40:55]\ns_waitcnt vmcnt(11)\nv_mfma_f32_32x32x16_bf16 v[40:55], a[112:115], v[208:211], v[40:55]\n"
"s_waitcnt vmcnt(10)\nv_mfma_f32_32x32x16_bf16 v[40:55], a[116:119], v[212:215], v[40:55]\ns_waitcnt vmcnt(9)\nv_mfma_f32_32x32x16_bf16 v[40:55], a[120:123], v[216:219], v[40:55]\n"
"s_waitcnt vmcnt(8)\nv_mfma_f32_32x32x16_bf16 v[40:55], a[124:127], v[220:223], v[40:55]\ns_waitcnt vmcnt(7)\nv_mfma_f32_32x32x16_bf16 v[40:55], a[128:131], v[224:227], v[40:55]\n"
"s_waitcnt vmcnt(6)\nv_mfma_f32_32x32x16_bf16 v[40:55], a[132:135], v[228:231], v[40:55]\ns_waitcnt vmcnt(5)\nv_mfma_f32_32x32x16_bf16 v[40:55], a[136:139], v[232:235], v[40:55]\n"
"s_waitcnt vmcnt(4)\nv_mfma_f32_32x32x16_bf16 v[40:55], a[140:143], v[236:239], v[40:55]\ns_waitcnt vmcnt(3)\nv_mfma_f32_32x32x16_bf16 v[40:55], a[144:147], v[240:243], v[40:55]\n"
"s_waitcnt vmcnt(2)\nv_mfma_f32_32x32x16_bf16 v[40:55], a[148:151], v[244:247], v[40:55]\ns_waitcnt vmcnt(1)\nv_mfma_f32_32x32x16_bf16 v[40:55], a[152:155], v[248:251], v[40:55]\n"
"s_waitcnt vmcnt(0)\nv_mfma_f32_32x32x16_bf16 v[40:55], a[156:159], v[252:255], v[40:55]\ns_nop 11\nv_cvt_pk_bf16_f32 v56, v40, v41\n"
"v_cvt_pk_bf16_f32 v57, v42, v43\nv_cvt_pk_bf16_f32 v58, v44, v45\nv_cvt_pk_bf16_f32 v59, v46, v47\nv_cvt_pk_bf16_f32 v60, v48, v49\n"
"v_cvt_pk_bf16_f32 v61, v50, v51\nv_cvt_pk_bf16_f32 v62, v52, v53\nv_cvt_pk_bf16_f32 v63, v54, v55\nds_write_b128 v24, v[56:59]\n"
"ds_write_b128 v24, v[60:63] offset:32\nv_add_u32 v24, 256, v24\n"
    :: "{s[8:23]}"(s0), "{v[8:23]}"(v0) : "v24", "v25", "v26", "v27", "v28", "v29", "v30", "v31", "v32", "v33", "v34", "v35", "v36", "v37", "v38", "v39", "v40", "v41", "v42", "v43", "v44", "v45", "v46", "v47", "v48", "v49", "v50", "v51", "v52", "v53", "v54", "v55", "v56", "v57", "v58", "v59", "v60", "v61", "v62", "v63", "v64", "v65", "v66", "v67", "v68", "v69", "v70", "v71", "v72", "v73", "v74", "v75", "v76", "v77", "v78", "v79", "v80", "v81", "v82", "v83", "v84", "v85", "v86", "v87", "v88", "v89", "v90", "v91", "v92", "v93", "v94", "v95", "v96", "v97", "v98", "v99", "v100", "v101", "v102", "v103", "v104", "v105", "v106", "v107", "v108", "v109", "v110", "v111", "v112", "v113", "v114", "v115", "v116", "v117", "v118", "v119", "v120", "v121", "v122", "v123", "v124", "v125", "v126", "v127", "v128", "v129", "v130", "v131", "v132", "v133", "v134", "v135", "v136", "v137", "v138", "v139", "v140", "v141", "v142", "v143", "v144", "v145", "v146", "v147", "v148", "v149", "v150", "v151", "v152", "v153", "v154", "v155", "v156", "v157", "v158", "v159", "v160", "v161", "v162", "v163", "v164", "v165", "v166", "v167", "v168", "v169", "v170", "v171", "v172", "v173", "v174", "v175", "v176", "v177", "v178", "v179", "v180", "v181", "v182", "v183", "v184", "v185", "v186", "v187", "v188", "v189", "v190", "v191", "v192", "v193", "v194", "v195", "v196", "v197", "v198", "v199", "v200", "v201", "v202", "v203", "v204", "v205", "v206", "v207", "v208", "v209", "v210", "v211", "v212", "v213", "v214", "v215", "v216", "v217", "v218", "v219", "v220", "v221", "v222", "v223", "v224", "v225", "v226", "v227", "v228", "v229", "v230", "v231", "v232", "v233", "v234", "v235", "v236", "v237", "v238", "v239", "v240", "v241", "v242", "v243", "v244", "v245", "v246", "v247", "v248", "v249", "v250", "v251", "v252", "v253", "v254", "v255", "a0", "a1", "a2", "a3", "a4", "a5", "a6", "a7", "a8", "a9", "a10", "a11", "a12", "a13", "a14", "a15", "a16", "a17", "a18", "a19", "a20", "a21", "a22", "a23", "a24", "a25", "a26", "a27", "a28", "a29", "a30", "a31", "a32", "a33", "a34", "a35", "a36", "a37", "a38", "a39", "a40", "a41", "a42", "a43", "a44", "a45", "a46", "a47", "a48", "a49", "a50", "a51", "a52", "a53", "a54", "a55", "a56", "a57", "a58", "a59", "a60", "a61", "a62", "a63", "a64", "a65", "a66", "a67", "a68", "a69", "a70", "a71", "a72", "a73", "a74", "a75", "a76", "a77", "a78", "a79", "a80", "a81", "a82", "a83", "a84", "a85", "a86", "a87", "a88", "a89", "a90", "a91", "a92", "a93", "a94", "a95", "a96", "a97", "a98", "a99", "a100", "a101", "a102", "a103", "a104", "a105", "a106", "a107", "a108", "a109", "a110", "a111", "a112", "a113", "a114", "a115", "a116", "a117", "a118", "a119", "a120", "a121", "a122", "a123", "a124", "a125", "a126", "a127", "a128", "a129", "a130", "a131", "a132", "a133", "a134", "a135", "a136", "a137", "a138", "a139", "a140", "a141", "a142", "a143", "a144", "a145", "a146", "a147", "a148", "a149", "a150", "a151", "a152", "a153", "a154", "a155", "a156", "a157", "a158", "a159", "a160", "a161", "a162", "a163", "a164", "a165", "a166", "a167", "a168", "a169", "a170", "a171", "a172", "a173", "a174", "a175", "a176", "a177", "a178", "a179", "a180", "a181", "a182", "a183", "a184", "a185", "a186", "a187", "a188", "a189", "a190", "a191", "a192", "a193", "a194", "a195", "a196", "a197", "a198", "a199", "a200", "a201", "a202", "a203", "a204", "a205", "a206", "a207", "a208", "a209", "a210", "a211", "a212", "a213", "a214", "a215", "a216", "a217", "a218", "a219", "a220", "a221", "a222", "a223", "s40", "s41", "s42", "s43", "s44", "s45", "s46", "s47", "s48", "s49", "s50", "s51", "s52", "s53", "s54", "s55", "s56", "s57", "s58", "s59", "s60", "s61", "s62", "s63", "s64", "s65", "s66", "s67", "s68", "s69", "s70", "s71", "s72", "s73", "s74", "s75", "s76", "s77", "s78", "s79", "s80", "s81", "vcc", "scc", "memory");
}
DI u4 pack8(const f16v& x, int s, float sc) { return (u4){pk2(x[8 * s] * sc, x[8 * s + 1] * sc), pk2(x[8 * s + 2] * sc, x[8 * s + 3] * sc), pk2(x[8 * s + 4] * sc, x[8 * s + 5] * sc), pk2(x[8 * s + 6] * sc, x[8 * s + 7] * sc)}; }
DI u4 pack8(const f16v& x, int s) { return (u4){pk2(x[8 * s], x[8 * s + 1]), pk2(x[8 * s + 2], x[8 * s + 3]), pk2(x[8 * s + 4], x[8 * s + 5]), pk2(x[8 * s + 6], x[8 * s + 7])}; }
DI void dma_pair(u4 srd, int v0, int v1, int soff, unsigned m) {
    asm volatile("s_mov_b32 m0, %4\n\ts_nop 0\n\tbuffer_load_dwordx4 %0, %2, %3 offen lds\n\tbuffer_load_dwordx4 %1, %2, %3 offen offset:1024 lds" :: "v"(v0), "v"(v1), "s"(srd), "s"(soff), "s"(m) : "memory");
}
DI void dma_one(u4 srd, int v0, int soff, unsigned m) {
    asm volatile("s_mov_b32 m0, %3\n\ts_nop 0\n\tbuffer_load_dwordx4 %0, %1, %2 offen lds" :: "v"(v0), "s"(srd), "s"(soff), "s"(m) : "memory");
}
DI void mV_va(f16v& c, u4 x, u4 y) { asm volatile("v_mfma_f32_32x32x16_bf16 %0, %1, %2, %0" : "+v"(c) : "v"(x), "a"(y)); }
DI void mV_va0(f16v& c, u4 x, u4 y) { asm volatile("v_mfma_f32_32x32x16_bf16 %0, %1, %2, 0" : "=&v"(c) : "v"(x), "a"(y)); }
DI void mV_av(f16v& c, u4 x, u4 y) { asm volatile("v_mfma_f32_32x32x16_bf16 %0, %1, %2, %0" : "+v"(c) : "a"(x), "v"(y)); }
DI void mV_av0(f16v& c, u4 x, u4 y) { asm volatile("v_mfma_f32_32x32x16_bf16 %0, %1, %2, 0" : "=&v"(c) : "a"(x), "v"(y)); }
DI void mV_vv(f16v& c, u4 x, u4 y) { asm volatile("v_mfma_f32_32x32x16_bf16 %0, %1, %2, %0" : "+v"(c) : "v"(x), "v"(y)); }
DI void mV_vv0(f16v& c, u4 x, u4 y) { asm volatile("v_mfma_f32_32x32x16_bf16 %0, %1, %2, 0" : "=&v"(c) : "v"(x), "v"(y)); }
template <int N, int PD, class RD, class MM, class EX> DI void pipe(RD rd, MM mm, EX ex) {
    u4 f[PD];
#pragma unroll
    for (int j = 0; j < PD; ++j) f[j] = rd(j);
    SB();
#pragma unroll
    for (int j = 0; j < N; ++j) { mm(j, f[j % PD]); if (j + PD < N) f[j % PD] = rd(j + PD); ex(j); SB(); }
}
DI int swzr(int row) { return ((row >> 2) & 1) | ((row & 3) << 1) | (row & 8); }
DI int swap23(int x) { return (x & ~12) | ((x & 4) << 1) | ((x & 8) >> 1); }
#define SLICE 32768
#define MFW32(x, y) asm volatile("s_nop 11" : "+v"(x), "+v"(y))
struct SideCtx {
    char LAS* sm; unsigned smb; int lane, wave;
    int vr0, vr1, vc0, vc1;
};
DI SideCtx make_ctx(int wave) {
    SideCtx c; c.sm = (char LAS*)smem_; c.smb = (unsigned)(size_t)c.sm; c.lane = fresh_lane(); c.wave = wave;
    asm volatile("" : "+s"(c.wave), "+s"(c.smb));
    const int l = c.lane;
    { int row = 4 * c.wave + (l >> 5); c.vr0 = row * 8192 + (((l & 31) ^ swzr(row)) * 16); row += 2; c.vr1 = row * 8192 + (((l & 31) ^ swzr(row)) * 16) - 1024; }
    { int row = l >> 3; c.vc0 = row * 896 + (((l & 7) ^ (((row >> 1) & 1) << 2)) * 16); c.vc1 = c.vc0 + 8 * 896 - 1024; }
    return c;
}
DI void dma_rows_q(const SideCtx& c, u4 srd, int h, int r0, int slot, int g) { dma_pair(srd, c.vr0, c.vr1, (r0 + 16 * g) * 8192 + h * 512, c.smb + slot * SLICE + (8 * g + 2 * c.wave) * 1024); }
DI void dma_cols_q(const SideCtx& c, u4 srd, int h, int c0, int slot, int g) { const int k = 8 * c.wave + 2 * g; dma_pair(srd, c.vc0, c.vc1, (h * 256 + 8 * k) * 896 + c0 * 2, c.smb + slot * SLICE + k * 1024); }
DI void dma_rows(const SideCtx& c, u4 srd, int h, int r0, int slot) {
#pragma unroll
    for (int g = 0; g < 4; ++g) dma_rows_q(c, srd, h, r0, slot, g);
}
DI void dma_cols(const SideCtx& c, u4 srd, int h, int c0, int slot) {
#pragma unroll
    for (int g = 0; g < 4; ++g) dma_cols_q(c, srd, h, c0, slot, g);
}
DI void set_flag(const SideCtx& c, u4 rhb, int off, unsigned tag) {
    waitvm<0>(); bar();
    if (c.wave == 0) sst32(tag, rhb, c.lane == 0 ? off : OOB, 0);
}

DI void slp(int n) {
#pragma nounroll
    for (int i = 0; i < n; ++i) asm volatile("s_sleep 1");
}
DI int xsrc(int wave, int lane, int k) { const int n = 64 * (7 * (wave & 1) + k) + lane, row = (n * 1171) >> 16, pos = n - 56 * row; return (row + 16 * (wave >> 1)) * 896 + ((pos ^ ((row >> 1) & 7)) * 16); }
DI int xrd(int row, int hi, int k) { return row * 896 + (((2 * k + hi) ^ ((row >> 1) & 7)) * 16); }
#define WSL(u) ((u) + 1)

DI int xswz(int type, int row) { return type ? ((row >> 1) & 1) | (((row >> 3) & 3) << 1) : (row >> 1) & 7; }
DI int xsrc8(int type, int wave, int lane) { const int row = 8 * wave + (lane >> 3); return row * 896 + (((lane & 7) ^ xswz(type, row)) * 16); }
template <int PART>
DI void gemm_first(int wave, int half, int type, int h, int sub) {
    CArgs& a = get_args();
    const SideCtx c = make_ctx(wave);
    h = __builtin_amdgcn_readfirstlane(h); sub = __builtin_amdgcn_readfirstlane(sub); half = __builtin_amdgcn_readfirstlane(half); asm volatile("" : "+s"(h), "+s"(sub), "+s"(half));
    if (PART == 0) {
        const u4 rw2 = mksrd(type ? a.Wo : a.Wq, 448 * 8192);
        const int c1 = sub == 3 ? 384 : sub * 128 + 64;
        if (type) { dma_cols(c, rw2, h, sub * 128, WSL(2)); dma_cols(c, rw2, h, c1, WSL(3)); } else { dma_rows(c, rw2, h, sub * 128, WSL(2)); dma_rows(c, rw2, h, c1, WSL(3)); }
    } else dma_rows(c, mksrd(type ? a.Wv : a.Wk, 448 * 8192), h, 256 * half, WSL(0));
}
template <int TYPE>
DI void gemm_role(unsigned tag, int vb, int wave, int half, int h, int sub) {
    CArgs& a = get_args();
    const SideCtx c = make_ctx(wave);
    const int lane = c.lane, g = lane >> 4, n = lane & 15;
    h = __builtin_amdgcn_readfirstlane(h); sub = __builtin_amdgcn_readfirstlane(sub); half = __builtin_amdgcn_readfirstlane(half); asm volatile("" : "+s"(h), "+s"(sub), "+s"(half));
    const int B = 4 * sub + c.wave, prow = 8 * (n >> 2) + (n & 3);
    GCtx x;
    x.rw1 = mksrd(TYPE ? a.Wv : a.Wk, 448 * 8192); x.rx = mksrd(a.X, half ? 0 : 2048 * 896); x.rhb = mksrd(a.HB, HB_SZ);
    x.rexp = mksrd(TYPE ? a.WvoF : a.WqkF, 28 * 16 * 14336); x.rout = mksrd(TYPE ? a.UF : a.GF, 16 * 64 * 28672);
    x.tag = tag; x.rot = 4 * half; x.sw1 = h * 512; x.mw = c.smb + c.wave * 2048; x.mx = c.smb + c.wave * 1024;
    x.sfg = O_FG - O_FW + (h * 2 + TYPE - TYPE * 16 - h) * 256; x.stay = half ? 2 : sub < 2; x.csc = __float_as_uint(CSCALE); x.ostr = TYPE ? 16384 : 28672;
    x.p1 = (unsigned)(u64)a.Au; x.p2 = (unsigned)((u64)a.Au >> 32); x.p5 = min(((TYPE * 16 + h) * 16 + B) * 3584, 2048 * 896 - 4096) + lane * 16; x.kb = 65536 + c.wave * 16384;
    x.vr0 = c.vr0; x.vr1 = c.vr1; x.vx = xsrc8(TYPE, c.wave, lane);
    x.wa = c.smb + SLICE + prow * 512 + ((g ^ swzr(prow)) * 16);
    { const int row = TYPE ? prow : n; x.xa = c.smb + row * 128 + ((g ^ xswz(TYPE, row)) * 16); }
    auto wof = [&](int blk) { return (TYPE ? 2 * blk * 16 + h : h * 28 + 2 * blk) * 14336 + lane * 16; };
    auto oof = [&](int blk, int i) { return blk > 13 ? OOB : (TYPE ? (2 * blk + i) * 1048576 + h * 1024 : h * 64 * 28672 + i * 14336 + blk * 1024) + lane * 16; };
    x.vexp = B > 13 ? OOB : wof(B); x.vimp = B > 5 ? OOB : wof(B + 8);
    x.vk = c.smb + (3 + (c.wave >> 1)) * SLICE + (8 * g + (n >> 2)) * 128 + (((4 * (c.wave & 1) + ((n & 3) >> 1)) ^ ((n >> 3) << 2)) * 16) + 8 * (n & 1);
    x.vo0 = oof(B, 0); x.vo1 = oof(B, 1); x.vo2 = oof(B + 8, 0); x.vo3 = oof(B + 8, 1);
    x.vfl = (c.wave == 0 && lane == 0) ? O_FW + half * 8192 + ((TYPE * 16 + h) * 4 + sub) * 64 : OOB;
    x.vpf = O_FW + (lane & 1) * 8192 + ((TYPE * 16 + h) * 4 + (sub | 2)) * 64; x.vpo = O_FW + 8192 + ((TYPE * 16 + h) * 4 + sub) * 64;
    PSTAMP(1);
    const unsigned tw = TYPE ? gemm_asm1(x) : gemm_asm0(x);
}

#define O_LAG 2
#define O_LAGN 24
DI void o_task(unsigned tag, int wave, int s) {
    {
        CArgs& a = get_args();
        const SideCtx c = make_ctx(wave);
        const int lane = c.lane, hi = lane >> 5, l31 = lane & 31, w = c.wave, pc = swap23(l31);
        s = __builtin_amdgcn_readfirstlane(s); asm volatile("" : "+s"(s));
        const u4 rhb = mksrd(a.HB, HB_SZ), rx = mksrd(a.X, 2048 * 896);
        bar();
        const int vx = xsrc8(0, w, lane);
#pragma unroll
        for (int k = 0; k < 7; ++k) dma_one(rx, vx, s * 28672 + k * 128, c.smb + k * 4096 + w * 1024);
        wait_flags(rhb, lane < 16 ? O_FH + (16 * w + lane) * 128 : OOB, tag + (s < O_LAGN ? s + O_LAG - (O_LAG - 2) * s / O_LAGN : s + 2));
        OCtx x;
        x.r = mksrd(a.HB, 0x7000000);
        x.sg = (unsigned)((char*)a.GF - a.HB) + s * 28672; x.ssx = (unsigned)((char*)a.SXM - a.HB) + s * 1024; x.hx = 4 * w;
        x.su = (unsigned)((char*)a.UF - a.HB) + s * 16384 + w * 2097152; x.sum = (unsigned)((char*)a.UM - a.HB) + s * 4096 + w * 524288; x.nt = w < 2 ? 4 : 3;
        x.sr = x.p0 = x.p1 = x.p2 = x.p3 = x.p4 = x.p5 = x.p6 = x.p7 = x.p8 = x.p9 = x.pa = 0;
        x.vg = (pc >> 4) * 14336 + hi * 256 + (pc & 15) * 16;
        x.vsx = (l31 >> 4) * 512 + hi * 256 + (l31 & 15) * 16;
        x.vu = (pc >> 4) * 1048576 + hi * 256 + (pc & 15) * 16; x.vum = (pc >> 4) * 262144 + hi * 512 + (pc & 15) * 16;
        x.vx = c.smb + l31 * 128 + ((hi ^ xswz(0, l31)) * 16); x.vt = l31 - 8 * hi;
        x.vpx = c.smb + 32768 + x.hx * 2048 + lane * 16; x.vpm = c.smb + 65536 + w * 2048 + lane * 16; x.vpr = c.smb + 32768 + lane * 16;
        x.vo = c.smb + 73728 + l31 * 912 + w * 64 + hi * 16;
        o_asm(x);
    }
    bar();
    const int tid = wave * 64 + fresh_lane();
    const u4 ro = mksrd(get_args().out, 2048 * 1792);
#pragma unroll
    for (int k = 0; k < 7; ++k) sst128(*(const u4 LAS*)(smem_ + 73728 + (tid >> 3) * 912 + (tid & 7) * 16 + k * 128), ro, (tid >> 3) * 1792 + (tid & 7) * 16, s * 32 * 1792 + 896 + k * 128);
}

DI void e_task(unsigned tag, int wave, int s, int hg) {
    CArgs& a = get_args();
    const SideCtx c = make_ctx(wave);
    const int lane = c.lane, hi = lane >> 5, l31 = lane & 31;
    s = __builtin_amdgcn_readfirstlane(s); hg = __builtin_amdgcn_readfirstlane(hg); asm volatile("" : "+s"(s), "+s"(hg));
    const int h = 4 * hg + c.wave;
    const u4 rhb = mksrd(a.HB, HB_SZ), rg = mksrd(a.GF, 16 * 64 * 28672), rx = mksrd(a.X, 2048 * 896);
    bar();
#pragma unroll
    for (int k = 0; k < 7; ++k) dma_one(rx, xsrc(c.wave, lane, k), s * 28672, c.smb + (7 * c.wave + k) * 1024);
    wait_flags(rhb, lane < 4 ? O_FG + (h * 8 + lane) * 64 : OOB, tag + s + 1);
    u4 gf[28];
    const int key = swap23(l31), o_g = (key >> 4) * 14336 + hi * 256 + (key & 15) * 16;
#pragma unroll
    for (int k = 0; k < 28; ++k) asm volatile("buffer_load_dwordx4 %0, %1, %2, %3 offen sc1" : "=a"(gf[k]) : "v"(o_g), "s"(rg), "s"((h * 64 + s) * 28672 + k * 512) : "memory");
    bar();
    f16v sx;
    pipe<28, 4>([&](int k) { return *(const u4 LAS*)(c.sm + xrd(l31, hi, k & 3) + (k >> 2) * 128); },
                [&](int k, u4 f) { if (k == 0) WAITV(21, "+a"(gf[0]), "+a"(gf[1]), "+a"(gf[2]), "+a"(gf[3]), "+a"(gf[4]), "+a"(gf[5]), "+a"(gf[6]));
                                   if (k == 7) WAITV(14, "+a"(gf[7]), "+a"(gf[8]), "+a"(gf[9]), "+a"(gf[10]), "+a"(gf[11]), "+a"(gf[12]), "+a"(gf[13]));
                                   if (k == 14) WAITV(7, "+a"(gf[14]), "+a"(gf[15]), "+a"(gf[16]), "+a"(gf[17]), "+a"(gf[18]), "+a"(gf[19]), "+a"(gf[20]));
                                   if (k == 21) WAITV(0, "+a"(gf[21]), "+a"(gf[22]), "+a"(gf[23]), "+a"(gf[24]), "+a"(gf[25]), "+a"(gf[26]), "+a"(gf[27]));
                                   if (k == 0) mV_av0(sx, gf[k], f); else mV_av(sx, gf[k], f); },
                [&](int) {});
    asm volatile("s_nop 11" : "+v"(sx));
    float m = -3.0e38f;
#pragma unroll
    for (int j = 0; j < 16; ++j) { if (16 * (j >> 3) + 8 * hi + (j & 7) > l31) sx[j] = -INFINITY; m = fmaxf(m, sx[j]); }
    m = x32max(m);
    float z = 0.f;
#pragma unroll
    for (int j = 0; j < 16; ++j) { sx[j] = __builtin_amdgcn_exp2f(sx[j] - m); z += sx[j]; }
    z = x32add(z);
    const u4 rex = mksrd(a.EX, 64 * 16 * 2048), rmz = mksrd(a.MZX, 64 * 16 * 256);
    sst128(pack8(sx, 0), rex, lane * 16, (s * 16 + h) * 2048); sst128(pack8(sx, 1), rex, lane * 16, (s * 16 + h) * 2048 + 1024);
    sst64((u2){__float_as_uint(m), __float_as_uint(z)}, rmz, lane < 32 ? lane * 8 : OOB, (s * 16 + h) * 256);
    set_flag(c, rhb, O_FE + (s * 4 + hg) * 64, tag);
}

#define A_SLP 80
#define A_LATE 1
#define X_NOU 0
#define P_XSLP 24
#define P_USLP 4
DI void x_task(unsigned tag, int wave, int s, int cb) {
    CArgs& a = get_args();
    const SideCtx c = make_ctx(wave);
    const int lane = c.lane, hi = lane >> 5, l31 = lane & 31, w = c.wave;
    s = __builtin_amdgcn_readfirstlane(s); cb = __builtin_amdgcn_readfirstlane(cb); asm volatile("" : "+s"(s), "+s"(cb));
    const u4 rhb = mksrd(a.HB, HB_SZ);
    const u4 ru = mksrd(a.UF, 16 * 64 * 28672), rex = mksrd(a.EX, 64 * 16 * 2048), rmz = mksrd(a.MZX, 64 * 16 * 256);
    const u4 rum = mksrd(a.UM, 28 * 64 * 4096), rsx = mksrd(a.SXM, 64 * 65536);
    wait_flags(rhb, lane < 8 ? O_FG + (((4 * w + (lane & 3)) * 2 + 1) * 4 + ((cb >> 1) & (lane < 4 ? 3 : 1))) * 64 : lane == 8 ? O_FE + (s * 4 + w) * 64 : OOB, lane < 8 ? tag + s + 1 : tag);
    u4 uf[4][2][2], ex[4][2]; u2 mz[4];
    const int o_u = (l31 >> 4) * 1048576 + hi * 256 + (l31 & 15) * 16;
#pragma unroll
    for (int k = 0; k < 4; ++k) {
        const int h = 4 * w + k, sh = s * 16 + h;
#pragma unroll
        for (int sg = 0; sg < 2; ++sg) {
            ex[k][sg] = sld128(rex, lane * 16, sh * 2048 + sg * 1024);
#pragma unroll
            for (int nt = 0; nt < 2; ++nt) uf[k][sg][nt] = sld128(ru, o_u, (4 * cb + 2 * nt) * 1048576 + sh * 1024 + sg * 512);
        }
        mz[k] = sld64(rmz, l31 * 8, sh * 256);
        asm volatile("s_sleep %0" :: "i"(P_XSLP));
    }
    waitvm<0>();
    f16v rr[4][2];
#pragma unroll
    for (int k = 0; k < 4; ++k) {
        asm volatile("" : "+v"(mz[k]), "+v"(ex[k][0]), "+v"(ex[k][1]), "+v"(uf[k][0][0]), "+v"(uf[k][0][1]), "+v"(uf[k][1][0]), "+v"(uf[k][1][1]));
        mV_vv0(rr[k][0], uf[k][0][0], ex[k][0]); mV_vv0(rr[k][1], uf[k][0][1], ex[k][0]);
        mV_vv(rr[k][0], uf[k][1][0], ex[k][1]); mV_vv(rr[k][1], uf[k][1][1], ex[k][1]);
        MFW32(rr[k][0], rr[k][1]);
    }
    wait_flags<2>(rhb, lane < 16 ? O_FH + (16 * w + lane) * 128 : s == 63 && lane < 32 ? O_FH + (16 * w + lane - 16) * 128 + 64 : OOB, tag + (s < 62 ? s + 2 : s + 1));
    u4 sp[4][4], um[2][2];
    const int o_sx = (l31 >> 4) * 512 + hi * 256 + (l31 & 15) * 16, o_um = (l31 >> 4) * 262144 + hi * 512 + (l31 & 15) * 16;
#pragma unroll
    for (int k = 0; k < 16; ++k) sp[k >> 2][k & 3] = sld128(rsx, o_sx, ((4 * w + (k >> 2)) * 4 + (k & 3)) * 65536 + s * 1024);
    if (s >= 62) wait_flags<2>(rhb, lane < 4 ? O_FP + ((4 * cb + lane) * 4 + w) * 64 : OOB, tag + 64);
#pragma unroll
    for (int k = 0; k < 4; ++k) um[k >> 1][k & 1] = sld128(rum, o_um, (4 * cb + 2 * (k & 1)) * 262144 + s * 4096 + w * 1024 + (k >> 1) * 256);
    u2 pm[4]; float scx[4];
#pragma unroll
    for (int k = 0; k < 4; ++k) {
        if (k == 0) WAITV(16, "+v"(sp[0][0]), "+v"(sp[0][1]), "+v"(sp[0][2]), "+v"(sp[0][3]));
        if (k == 1) WAITV(12, "+v"(sp[1][0]), "+v"(sp[1][1]), "+v"(sp[1][2]), "+v"(sp[1][3]));
        if (k == 2) WAITV(8, "+v"(sp[2][0]), "+v"(sp[2][1]), "+v"(sp[2][2]), "+v"(sp[2][3]));
        if (k == 3) WAITV(4, "+v"(sp[3][0]), "+v"(sp[3][1]), "+v"(sp[3][2]), "+v"(sp[3][3]));
        const f4 sc = (__builtin_bit_cast(f4, sp[k][0]) + __builtin_bit_cast(f4, sp[k][1])) + (__builtin_bit_cast(f4, sp[k][2]) + __builtin_bit_cast(f4, sp[k][3]));
        const float mx = __uint_as_float(mz[k][0]);
        float m = fmaxf(x32max(fmaxf(fmaxf(sc[0], sc[1]), fmaxf(sc[2], sc[3]))), mx);
        float e[4];
#pragma unroll
        for (int j = 0; j < 4; ++j) e[j] = __builtin_amdgcn_exp2f(sc[j] - m);
        const float cx = __builtin_amdgcn_exp2f(mx - m);
        const float z = x32add((e[0] + e[1]) + (e[2] + e[3])) + cx * __uint_as_float(mz[k][1]);
        const float inv = __builtin_amdgcn_rcpf(z);
        scx[k] = cx * inv;
        pm[k] = (u2){pk2(e[0] * inv, e[1] * inv), pk2(e[2] * inv, e[3] * inv)};
    }
    WAITV(0, "+v"(um[0][0]), "+v"(um[0][1]), "+v"(um[1][0]), "+v"(um[1][1]));
    f16v d0, d1;
    {
        u4 p0 = {pm[0][0], pm[1][0], pm[2][0], pm[3][0]}, p1 = {pm[0][1], pm[1][1], pm[2][1], pm[3][1]};
        asm volatile("s_nop 1\n\tv_mfma_f32_32x32x16_bf16 %0, %2, %6, 0\n\tv_mfma_f32_32x32x16_bf16 %1, %3, %6, 0\n\tv_mfma_f32_32x32x16_bf16 %0, %4, %7, %0\n\tv_mfma_f32_32x32x16_bf16 %1, %5, %7, %1"
                     : "=&v"(d0), "=&v"(d1) : "v"(um[0][0]), "v"(um[0][1]), "v"(um[1][0]), "v"(um[1][1]), "v"(p0), "v"(p1));
    }
    f16v a0 = (rr[0][0] * scx[0] + rr[1][0] * scx[1]) + (rr[2][0] * scx[2] + rr[3][0] * scx[3]);
    f16v a1 = (rr[0][1] * scx[0] + rr[1][1] * scx[1]) + (rr[2][1] * scx[2] + rr[3][1] * scx[3]);
    asm volatile("s_nop 11" : "+v"(d0), "+v"(d1), "+v"(a0), "+v"(a1));
    a0 += d0; a1 += d1;
    bar();
#pragma unroll
    for (int g = 0; g < 4; ++g) {
        *(f4 LAS*)(c.sm + w * 8704 + l31 * 272 + (8 * g + 4 * hi) * 4) = (f4){a0[4 * g], a0[4 * g + 1], a0[4 * g + 2], a0[4 * g + 3]};
        *(f4 LAS*)(c.sm + w * 8704 + l31 * 272 + 128 + (8 * g + 4 * hi) * 4) = (f4){a1[4 * g], a1[4 * g + 1], a1[4 * g + 2], a1[4 * g + 3]};
    }
    bar();
    {
        const int t = w * 8 + (lane >> 3), c8 = lane & 7;
        f4 s0 = {0.f, 0.f, 0.f, 0.f}, s1 = s0;
#pragma unroll
        for (int p = 0; p < 4; ++p) { s0 += *(const f4 LAS*)(c.sm + p * 8704 + t * 272 + c8 * 32); s1 += *(const f4 LAS*)(c.sm + p * 8704 + t * 272 + c8 * 32 + 16); }
        sst128((u4){pk2(s0[0], s0[1]), pk2(s0[2], s0[3]), pk2(s1[0], s1[1]), pk2(s1[2], s1[3])}, mksrd(a.out, 2048 * 1792), t * 1792 + c8 * 16, s * 32 * 1792 + 896 + cb * 128);
    }
}

#define PA_UT 0
#define PA_TU 32896
#define PA_KH (PA_TU + 33280)
#define PA_SC (PA_KH + 7680)
#define PA_KT (PA_SC + 128)
#define PA_RED (PA_KT + 24576)
typedef __attribute__((ext_vector_type(2))) _Float16 h2;
typedef __attribute__((ext_vector_type(8))) _Float16 h8;
DI unsigned pkh(float a, float b) { f2 v = {a, b}; return __builtin_bit_cast(unsigned, __builtin_convertvector(v, h2)); }
DI void parta_task(unsigned tag, int wave, int task) {
    CArgs& a = get_args();
    const SideCtx c = make_ctx(wave);
    char LAS* sm = c.sm;
    const int lane = c.lane, w = c.wave, tid = w * 64 + lane, hi = lane >> 5, l31 = lane & 31, l15 = lane & 15, kk = lane >> 4;
    task = __builtin_amdgcn_readfirstlane(task); asm volatile("" : "+s"(task));
    const int d0 = task * 8;
    const u4 rpa = mksrd(a.PA, 56 * 32768);
    bar();
#pragma unroll
    for (int g = 0; g < 2; ++g) {
        const int d = d0 + 4 * g + w;
        const float dt = bfr(expf(bf2f(a.Alogdt[d])));
        const u2 x0 = *(const u2*)(a.AlogA + d * 256 + lane * 4), x1 = *(const u2*)(a.Aim + d * 256 + lane * 4), x2 = *(const u2*)(a.ACr + d * 256 + lane * 4), x3 = *(const u2*)(a.ACi + d * 256 + lane * 4);
        f4 z[4]; unsigned mx = 0;
#pragma unroll
        for (int i = 0; i < 4; ++i) {
            auto el = [&](u2 x) { return bf2f((u16)(x[i >> 1] >> (16 * (i & 1)))); };
            z[i] = (f4){-bfr(expf(el(x0))) * dt * LOG2E, el(x1) * dt * 0.15915494309189535f, 2.f * el(x2) * dt, 2.f * el(x3) * dt};
            mx = max(mx, max(__float_as_uint(z[i][2]) & 0x7f800000u, __float_as_uint(z[i][3]) & 0x7f800000u));
        }
#pragma unroll
        for (int o = 1; o < 64; o <<= 1) mx = max(mx, (unsigned)__shfl_xor((int)mx, o));
        mx = min(max(mx, 0x00800000u), 0x7e800000u);
        *(unsigned LAS*)(sm + PA_SC + (4 * g + w) * 4) = mx;
        const float inv = __uint_as_float(0x7f000000u - mx);
#pragma unroll
        for (int i = 0; i < 4; ++i) { z[i][2] *= inv; z[i][3] *= inv; *(f4 LAS*)(sm + PA_RED + w * 9216 + g * 4096 + (i * 64 + lane) * 16) = z[i]; }
    }
    const u4 dsk4 = *(const u4*)(a.ADsk + d0);
    if (tid < 8) *(u4 LAS*)(sm + PA_UT + tid * 4112 + 4096) = (u4){0u, 0u, 0u, 0u};
    if (tid < 155) *(u4 LAS*)(sm + PA_KH + tid * 16) = (u4){0u, 0u, 0u, 0u};
    float qpos[4];
    qpos[0] = (float)l15;
    qpos[1] = l15 < 8 ? (float)(256 + 2 * l15) : (float)(512 + ((l15 - 8) >> 2) * 256 + (l15 & 3) * 4);
    qpos[2] = l15 < 8 ? (float)(1024 + (l15 >> 1) * 256 + (l15 & 1) * 8) : 2048.f;
    qpos[3] = (float)(16 * l15);
    auto kgen = [&](int g) __attribute__((always_inline)) {
        const char LAS* pt = sm + PA_RED + w * 9216 + g * 4096 + kk * 16;
        f4 acc[3] = {{0.f, 0.f, 0.f, 0.f}, {0.f, 0.f, 0.f, 0.f}, {0.f, 0.f, 0.f, 0.f}};
#pragma nounroll
        for (int st = 0; st < 16; ++st) {
            u4 op[4];
#pragma unroll
            for (int i = 0; i < 4; ++i) {
                const f4 z = *(const f4 LAS*)(pt + st * 256 + i * 64);
#pragma unroll
                for (int t = 0; t < 4; ++t) {
                    const float ex = __builtin_amdgcn_exp2f(z[0] * qpos[t]), rv = __builtin_amdgcn_fractf(z[1] * qpos[t]);
                    const float er = ex * __builtin_amdgcn_cosf(rv), ei = ex * __builtin_amdgcn_sinf(rv);
                    op[t][i] = t < 3 ? pkh(er, ei) : pkh(z[2] * er - z[3] * ei, -(z[2] * ei + z[3] * er));
                }
            }
#pragma unroll
            for (int t = 0; t < 3; ++t) acc[t] = __builtin_amdgcn_mfma_f32_16x16x32_f16(__builtin_bit_cast(h8, op[3]), __builtin_bit_cast(h8, op[t]), acc[t], 0, 0, 0);
        }
#pragma unroll
        for (int t = 0; t < 3; ++t) *(f4 LAS*)(sm + PA_KT + (4 * g + w) * 3072 + ((t * 16 + l15) * 16 + 4 * kk) * 4) = acc[t];
    };
    kgen(0);
    if (A_LATE && task < 20) kgen(1);
    {
        const u4 ru = mksrd(a.Au, (X_NOU == 1 || (X_NOU == 2 && task < 20) || (X_NOU == 3 && task >= 20)) ? 0 : 2048 * 896);
#pragma unroll
        for (int i = 0; i < 8; ++i) { dma_one(ru, tid * 896, i * 256 * 896 + d0 * 2, c.smb + PA_TU + i * 4096 + w * 1024); asm volatile("s_sleep %0" :: "i"(P_USLP)); }
    }
    if (!(A_LATE && task < 20)) kgen(1);
    waitvm<0>(); bar();
    u4 uv[8];
#pragma unroll
    for (int i = 0; i < 8; ++i) uv[i] = *(const u4 LAS*)(sm + PA_TU + tid * 128 + i * 16);
#pragma unroll
    for (int ch = 0; ch < 8; ++ch) {
        u4 o;
#pragma unroll
        for (int j = 0; j < 4; ++j) o[j] = __builtin_amdgcn_perm(uv[6 - 2 * j][ch >> 1], uv[7 - 2 * j][ch >> 1], (ch & 1) ? 0x07060302u : 0x05040100u);
        *(u4 LAS*)(sm + PA_UT + ch * 4112 + (255 - tid) * 16) = o;
    }
    {
        typedef __attribute__((ext_vector_type(2))) unsigned short us2;
        u4 mm;
#pragma unroll
        for (int j = 0; j < 4; ++j) {
            us2 m = {0, 0};
#pragma unroll
            for (int i = 0; i < 8; ++i) m = __builtin_elementwise_max(m, __builtin_bit_cast(us2, uv[i][j] & 0x7f807f80u));
#pragma unroll
            for (int o = 1; o < 64; o <<= 1) m = __builtin_elementwise_max(m, __builtin_bit_cast(us2, __shfl_xor(__builtin_bit_cast(int, m), o)));
            mm[j] = __builtin_bit_cast(unsigned, m);
        }
        *(u4 LAS*)(sm + PA_SC + 64 + w * 16) = mm;
    }
    int toff[8];
#pragma unroll
    for (int j = 0; j < 8; ++j) {
        const int p = (int)bfr((float)(tid * 8 + j));
        int col, ar = (p & 255) >> 4;
        if (p < 256) col = p & 15; else if (p < 512) col = 16 + ((p & 15) >> 1); else if (p < 1024) col = 24 + ((p - 512) >> 8) * 4 + ((p & 15) >> 2);
        else if (p < 2048) col = 32 + ((p - 1024) >> 8) * 2 + ((p & 15) >> 3); else col = 40;
        toff[j] = (col * 16 + ar) * 4;
    }
    bar();
    if (tid < 32) *(u4 LAS*)(sm + PA_TU + ((tid >> 2) * 260 + 256 + (tid & 3)) * 16) = (u4){0u, 0u, 0u, 0u};
    {
#pragma nounroll
        for (int ch = 0; ch < 8; ++ch) {
            if (task < 20) slp(ch ? A_SLP : (task % 10) * (A_SLP + 70) / 10);
            const u16 LAS* mp = (const u16 LAS*)(sm + PA_SC + 64) + ch;
            const unsigned ue = min(max((unsigned)max(max(mp[0], mp[8]), max(mp[16], mp[24])) << 16, 0x07800000u), 0x7e800000u) - 0x07000000u;
            {
                const u4 x0 = *(const u4 LAS*)(sm + PA_UT + ch * 4112 + tid * 16), x1 = *(const u4 LAS*)(sm + PA_UT + ch * 4112 + tid * 16 + 16);
                float kv[8];
#pragma unroll
                for (int j = 0; j < 8; ++j) kv[j] = *(const float LAS*)(sm + PA_KT + ch * 3072 + toff[j]);
                const float us = __uint_as_float(0x7f000000u - ue);
                unsigned r[8];
#pragma unroll
                for (int j = 0; j < 8; ++j) { const unsigned x = j < 4 ? x0[j & 3] : x1[j & 3]; r[j] = pkh(__uint_as_float(x << 16) * us, __uint_as_float(x & 0xffff0000u) * us); }
#pragma unroll
                for (int cp = 0; cp < 8; ++cp) {
                    u4 o;
#pragma unroll
                    for (int j = 0; j < 4; ++j) o[j] = (cp & 1) ? __builtin_amdgcn_alignbit(r[(cp >> 1) + j + 1], r[(cp >> 1) + j], 16) : r[(cp >> 1) + j];
                    *(u4 LAS*)(sm + PA_TU + (cp * 260 + tid) * 16) = o;
                }
                *(u4 LAS*)(sm + PA_KH + (31 + (tid >> 2)) * 80 + (tid & 3) * 16) = (u4){pkh(kv[0], kv[1]), pkh(kv[2], kv[3]), pkh(kv[4], kv[5]), pkh(kv[6], kv[7])};
            }
            bar();
            {
                f16v y0, y1;
                const int m00 = 2047 - l31 + 8 * hi, cp = m00 & 7;
                const char LAS* ab = sm + PA_TU + (cp * 260 + (m00 >> 3)) * 16 - 63 * 64;
                const char LAS* bb = sm + PA_KH + l31 * 80 + hi * 16;
                const char LAS* a1w = ab - w * 64; const char LAS* b1w = bb - w * 80;
                const char LAS* a2w = ab + w * 64; const char LAS* b2w = bb + w * 80;
                u4 fr[4][3];
                auto rdg = [&](int g) __attribute__((always_inline)) {
                    const int i = (g & 15) >> 1, ks = g & 1, sl = g & 3;
                    if (g < 16) { fr[sl][0] = *(const u4 LAS*)(b1w + (31 - 4 * i) * 80 + ks * 32); fr[sl][1] = *(const u4 LAS*)(a1w + (63 - 4 * i) * 64 + ks * 32);
                                  fr[sl][2] = *(const u4 LAS*)(a1w + (31 - 4 * i) * 64 + ks * 32); }
                    else { fr[sl][0] = *(const u4 LAS*)(b2w + (32 + 4 * i) * 80 + ks * 32); fr[sl][2] = *(const u4 LAS*)(a2w + (32 + 4 * i) * 64 + ks * 32); }
                };
                rdg(0); rdg(1); rdg(2); SB();
#pragma unroll
                for (int g = 0; g < 32; ++g) {
                    const int sl = g & 3;
                    if (g + 3 < 32) rdg(g + 3);
                    SB();
                    if (g == 0) asm volatile("v_mfma_f32_32x32x16_f16 %0, %2, %4, 0\n\tv_mfma_f32_32x32x16_f16 %1, %3, %4, 0" : "=&v"(y0), "=&v"(y1) : "v"(fr[0][1]), "v"(fr[0][2]), "v"(fr[0][0]));
                    else { if (g < 16) asm volatile("v_mfma_f32_32x32x16_f16 %0, %1, %2, %0" : "+v"(y0) : "v"(fr[sl][1]), "v"(fr[sl][0]));
                           asm volatile("v_mfma_f32_32x32x16_f16 %0, %1, %2, %0" : "+v"(y1) : "v"(fr[sl][2]), "v"(fr[sl][0])); }
                    SB();
                }
                MFW32(y0, y1);
#pragma unroll
                for (int g = 0; g < 4; ++g) {
                    *(f4 LAS*)(sm + PA_RED + w * 9216 + l31 * 144 + (8 * g + 4 * hi) * 4) = (f4){y0[4 * g], y0[4 * g + 1], y0[4 * g + 2], y0[4 * g + 3]};
                    *(f4 LAS*)(sm + PA_RED + w * 9216 + (32 + l31) * 144 + (8 * g + 4 * hi) * 4) = (f4){y1[4 * g], y1[4 * g + 1], y1[4 * g + 2], y1[4 * g + 3]};
                }
            }
            bar();
            {
                f4 s0 = {0.f, 0.f, 0.f, 0.f}, s1 = s0;
#pragma unroll
                for (int p = 0; p < 4; ++p) { const char LAS* q_ = sm + PA_RED + p * 9216 + (tid >> 2) * 144 + (tid & 3) * 32; s0 += *(const f4 LAS*)q_; s1 += *(const f4 LAS*)(q_ + 16); }
                const u4 uu = *(const u4 LAS*)(sm + PA_UT + ch * 4112 + (255 - tid) * 16);
                const unsigned dw = (ch & 4) ? ((ch & 2) ? dsk4[3] : dsk4[2]) : ((ch & 2) ? dsk4[1] : dsk4[0]);
                const float dsk = __uint_as_float((ch & 1) ? dw & 0xffff0000u : dw << 16);
                const float ks_ = *(const float LAS*)(sm + PA_SC + ch * 4), us_ = __uint_as_float(ue);
                float yv[8];
#pragma unroll
                for (int j = 0; j < 8; ++j) yv[j] = bfr((j < 4 ? s0[j & 3] : s1[j & 3]) * us_ * ks_) + bfr(__uint_as_float((j & 1) ? uu[3 - (j >> 1)] << 16 : uu[3 - (j >> 1)] & 0xffff0000u) * dsk);
                sst128((u4){pk2(yv[0], yv[1]), pk2(yv[2], yv[3]), pk2(yv[4], yv[5]), pk2(yv[6], yv[7])}, rpa, tid * 16, (d0 + ch) * 4096);
            }
        }
    }
    set_flag(c, mksrd(a.HB, HB_SZ), O_FA + task * 64, tag);
}
#define P_ASLP 8
DI void pa_out(unsigned tag, int wave, int t) {
    CArgs& a = get_args();
    const SideCtx c = make_ctx(wave);
    const int lane = c.lane, tid = c.wave * 64 + lane;
    t = __builtin_amdgcn_readfirstlane(t); asm volatile("" : "+s"(t));
    const u4 rpa = mksrd(a.PA, 56 * 32768), ro = mksrd(a.out, 2048 * 1792);
    wait_flags(mksrd(a.HB, HB_SZ), lane < 8 ? O_FA + ((t & ~7) + lane) * 64 : OOB, tag);
    u4 v[8];
#pragma unroll
    for (int i = 0; i < 8; ++i) v[i] = sld128(rpa, (tid >> 5) * 4096 + (tid & 31) * 16, ((t >> 3) * 64 + i * 8) * 4096 + (t & 7) * 512);
    WAITV(0, "+v"(v[0]), "+v"(v[1]), "+v"(v[2]), "+v"(v[3]), "+v"(v[4]), "+v"(v[5]), "+v"(v[6]), "+v"(v[7]));
    bar();
#pragma unroll
    for (int i = 0; i < 8; ++i) *(u4 LAS*)(c.sm + (i * 8 + (tid >> 5)) * 528 + (tid & 31) * 16) = v[i];
    bar();
#pragma unroll
    for (int k = 0; k < 8; ++k) {
        const char LAS* p = c.sm + (tid & 7) * 8 * 528 + ((tid >> 3) + 32 * k) * 2;
        u4 o;
#pragma unroll
        for (int j = 0; j < 4; ++j) o[j] = (unsigned)*(const u16 LAS*)(p + (2 * j) * 528) | ((unsigned)*(const u16 LAS*)(p + (2 * j + 1) * 528) << 16);
        sst128(o, ro, (tid >> 3) * 1792 + (tid & 7) * 16, ((t & 7) * 256 + 32 * k) * 1792 + (t >> 3) * 128);
        asm volatile("s_sleep %0" :: "i"(P_ASLP));
    }
}
#define P_M 0ull
#define X_NOA 0
#define P_TOUCH 1
#define P_OWN 1
#define P_S1 1
#define P_S3 5
#define P_XOR 4
#define L_SCR 28672
#define V_M P_M
#define V_A P_A
#define V_B P_B

DI void chain_role(unsigned tag, int vb, int wave) {
    CArgs& a = get_args();
    const int nx = vb & 3, id = vb >> 3, W = wave;
    const int lane = fresh_lane(), q = lane >> 4, n = lane & 15;
    const u4 r = mksrd(a.HB, HB_SZ);
    for (int i = W * 64 + lane; i < 1792; i += 256) *(u4 LAS*)(smem_ + i * 16) = (u4){0u, 0u, 0u, 0u};
    {
        const char* b = id < 16 ? (W & 1 ? (const char*)a.X : (const char*)a.SXM + (nx * 16 + id) * 65536) : id < 20 ? (const char*)a.GF + (size_t)(nx * 4 + id - 16) * 64 * 28672
                      : W & 1 ? (const char*)a.UF + (size_t)(nx * 7 + id - 20) * 1048576 : (const char*)a.UM + (nx * 7 + id - 20) * 262144;
        const int st = id < 16 ? (W & 1 ? 28672 : 1024) : id < 20 ? 28672 : W & 1 ? 16384 : 4096;
        if (W < 2 && id < 27) { unsigned t = sld32(mksrd(b, 64 * st), lane * st, 0); WAITV(0, "+v"(t)); }
    }
    const int o_need = id < 16 ? O_FW + (lane & 4) * 2048 + ((nx * 4 + (id >> 2)) * 4 + (lane & 3)) * 64 : id < 20 ? O_FG + ((nx * 4 + id - 16) * 8 + (lane & 3)) * 64
                     : id < 27 ? O_FW + (lane & 4) * 2048 + ((16 + 4 * W + (lane & 3)) * 4 + ((nx * 7 + id - 20) >> 3)) * 64 : O_FW + 8192 + (W & 1) * 4096 + lane * 64;
    const unsigned want = tag + (id >= 16 && id < 20);
    unsigned win, t0;
    {
        unsigned sp = 0;
#pragma nounroll
        for (;;) {
            u4 v = sld128(r, O_PAR, 0); unsigned f = sld32(r, o_need, 0); WAITV(0, "+v"(v), "+v"(f));
            win = __builtin_amdgcn_readfirstlane(v[0]) ^ tag; t0 = __builtin_amdgcn_readfirstlane(v[1]);
            if (win <= 3u && (t0 ^ tag) == (unsigned)__builtin_amdgcn_readfirstlane(v[2]) && __all(f - want <= 72u)) break;
            asm volatile("s_sleep 2" ::: "memory");
            if (++sp > SPIN_MAX) __builtin_amdgcn_endpgm();
        }
    }
    const int wA = win * 8192, wB = wA ^ 16384, wF = 524288 + wA;
    auto copy_off = [&](int g) { return (2 + (g >> 1)) * 32768 + ((g & 1) ? wA : wB); };
    if (id == 27) {
        unsigned m[7];
#pragma unroll
        for (int k = 0; k < 7; ++k) m[k] = san(*(const unsigned*)(a.mem0 + (lane >> 3) * 448 + (4 * k + W) * 16 + 4 * (lane & 3) + 2 * ((lane >> 2) & 1)));
        bar();
#pragma unroll
        for (int k = 0; k < 7; ++k) sst32(m[k], r, copy_off(nx) + lane * 4, 3 * 131072 + (4 * k + W) * 256);
        return;
    }
    Ctx c;
    c.r = r; c.rts = r; c.t0 = t0; c.per = V_A + V_B; c.R = P_R; c.tag = tag; c.lv0 = (unsigned)(V_M); c.lv1 = (unsigned)((V_M) >> 32); c.des = P_EPS; c.role = (id < 16 ? 0 : id < 20 ? 4 : 8) + W; c.tf0 = 0;
    c.idx = (lane & 1) ? 0xEEEE : 0x4444; c.vl16 = lane * 16;
    c.vg = c.vg3 = c.vtl = c.vtr = c.vsel = c.vraw = c.vfo = c.ve = c.vs = c.vrw = c.vrr = c.vr1 = c.vr2 = c.vdbg = 0;
    c.vtb = lane < 28 ? O_ANN + lane * 128 : OOB;
    c.vts = 32768 + W * 4096 + (STAMPS ? lane * 4 + (lane < 16 ? 0 : 32768) : 0);
    if (id < 16) {
        const int hb = nx * 16 + id, h = hb >> 2, part = hb & 3;
        c.rw = mksrd(a.WqkF, 16 * 28 * 14336); c.sw = h * 28 + part * 7; c.vsel = ((n >> 2) & 1) * 14336 + q * 256 + (4 * (n >> 3) + (n & 3)) * 16;
        c.rx = mksrd(a.SXM, 64 * 65536); c.sx = hb * 65536 + W * 512;
        c.rg = mksrd(a.X, 2048 * 896); c.sg = 0;
        c.vm = copy_off(nx) + (W >> 1) * 4096 + lane * 16;
        c.vtl = q * 1088 + n * 8 + (W >> 1) * 512;
        const int r_ = (lane >> 2) & 3, c_ = lane & 3;
        const int rec = (h >> 1) * 1024 + part * 256 + (h & 1) * 128;
        const int raw = (h >> 2) * 32768 + wF + (h & 3) * 2048 + part * 512;
        c.vfl = lane == 0 ? O_FH + hb * 128 + W * 64 : OOB; c.vpub = OOB;
        if (W < 2) {
            c.vg = (16 * W + (n & 7)) * 896 + (112 * part + 32 * (q >> 1) + 16 * (n >> 3) + 8 * (q & 1)) * 2; c.vg3 = (q >= 2 && n >= 8) ? OOB : c.vg + 128;
            c.vtr = c_ * 1088 + (8 * q + r_) * 8;
            c.vpub = (q >> 1) * 256 + n * 16 + (q & 1) * 8;
        } else if (W == 2) {
            c.vraw = lane < 16 ? (lane < 8 ? wA : wB) + rec + (lane & 7) * 16 : lane < 32 ? raw + (lane - 16) * 16 : id < 14 ? copy_off(nx) + id * 512 + (lane - 32) * 16 : OOB;
        } else {
            c.vg = copy_off(nx) + (7 * part + 2 * (q >> 1) + (n >> 3)) * 256 + (2 * (n & 7) + (q & 1)) * 16; c.vg3 = (q >= 2 && n >= 8) ? OOB : c.vg + 1024;
            c.vtr = c_ * 1088 + (16 * (q >> 1) + 4 * (r_ >> 1) + 2 * (q & 1) + (r_ & 1)) * 8;
            c.vpub = (n < 8 ? wA : wB) + rec + (n & 7) * 16 + q * 4;
            c.vraw = n < 8 ? raw + n * 32 + q * 8 : OOB;
        }
    } else if (id < 20) {
        const int h = nx * 4 + id - 16;
        c.rw = r; c.sw = 0; c.rx = r; c.sx = 0;
        c.rg = mksrd(a.GF, 16 * 64 * 28672); c.sg = h * 64 * 28672 + W * 4096;
        c.vm = copy_off(nx) + W * 2048 + lane * 16;
        c.vtl = W * 1280 + 2 * q * 160 + n * 4;
        c.vtr = ((lane >> 2) & 7) * 160 + (8 * W + 2 * (lane & 3)) * 4;
        const int rec = 32768 + (h >> 1) * 1024 + W * 256 + (h & 1) * 128;
        const int raw = (h >> 2) * 32768 + wF + (h & 3) * 2048 + W * 512 + 256;
        c.vpub = (lane < 32 ? wA : wB) + rec + (lane & 31) * 4;
        c.vraw = lane < 32 ? raw + lane * 8 : OOB;
        c.vsel = lane < 16 ? (lane < 8 ? wA : wB) + rec + (lane & 7) * 16 : lane < 32 ? raw + (lane - 16) * 16 : OOB;
        c.vfl = O_FG + (h * 8 + (lane & 7)) * 64;
    } else {
        const int pid = nx * 7 + id - 20;
        c.rw = mksrd(a.WvoF, 28 * 16 * 14336); c.sw = (pid * 16 + 4 * W) * 14336;
        c.rg = mksrd(a.UF, 16 * 64 * 28672); c.sg = pid * 1048576 + W * 4096;
        c.rx = mksrd(a.UM, 28 * 64 * 4096); c.sx = pid * 262144 + W * 1024;
        c.tf0 = V_A;
        c.vm = copy_off(nx) + W * 2048 + lane * 16;
        c.vpub = copy_off(W) + pid * 256 + lane * 4;
        c.vs = ((nx & 1) ? wA : wB) + W * 2048 + lane * 16; c.ve = c.vs + 32768;
        auto qb = [](int x) { return x * 256 + (x & 1) * 32 + (x >> 1) * 128; };
        c.vrw = 8192 + W * 1280 + qb(q) + n * 8;
        c.vrr = 8192 + qb(lane & 3) + ((lane >> 3) + 8 * ((lane >> 2) & 1)) * 8;
        c.vr1 = W * 32768 + wF + (n >> 3) * 2048 + (n & 7) * 32 + q * 8;
        c.vr2 = W * 32768 + wF + (n >> 3) * 2048 + q * 512 + 256 + (n & 7) * 32;
        c.vg = O_FG + (((4 * W + (lane & 3)) * 2 + 1) * 4 + (pid >> 3)) * 64;
        c.vfl = lane == 0 ? O_FP + (pid * 4 + W) * 64 : OOB;
        { const int o = P_OWN ? (W - nx - 1) & 3 : W; c.vtl = 8 + 16 * (o == 3 ? P_S3 : o * P_S1); }
        c.vg3 = W < 3 ? c.vm + 1024 : c.vm - 6144;
        c.vsel = (pid == 0 && W == 0 && lane == 0) ? O_EPOCH : OOB;
        c.vfo = (lane == 0 && W == 0) ? O_ANN + pid * 128 : OOB;
    }
    PSTAMP(5);
    asm volatile("s_waitcnt lgkmcnt(0)" ::: "memory");
    chain_asm(c);
}

__global__ __launch_bounds__(256, 1) void fused_kernel(Args a_) {
    CArgs& a = get_args();
    const int wave = __builtin_amdgcn_readfirstlane(threadIdx.x >> 6);
#define PT(k)
    const u4 rhb = mksrd(a.HB, HB_SZ);
    unsigned tag;
    asm volatile("s_load_dword %0, %1, %2" : "=s"(tag) : "s"(a.HB), "s"(O_EPOCH) : "memory");
    const int xcc = (__builtin_amdgcn_s_getreg((31 << 11) | 20) & 7) ^ P_XOR;
    int vb = xcc + (blockIdx.x & ~7);
    const bool prober = vb == 249 && wave;
    if (prober) { const int l = fresh_lane(); sst32(0u, rhb, l < 4 ? O_PROBE + l * 8192 : OOB, wave * 32768); }
    if (wave) {
        u64 pc; unsigned len;
        asm volatile("s_getpc_b64 %0\nfk_code_lo:\n\ts_max_i32 %1, fk_code_hi-fk_code_lo, 0" : "=s"(pc), "=s"(len) :: "scc");
        const u4 rc = mksrd((const void*)pc, len & ~15u);
        const int vo = (((blockIdx.x >> 3) * 3 + wave - 1) * 2) * 1024 + (threadIdx.x & 63) * 16;
#pragma unroll
        for (int i = 0; i < 2; ++i) dma_one(rc, vo + i * 1024, 0, (unsigned)(size_t)(char LAS*)smem_ + L_SCR + wave * 1024);
    }
    PT(4);
    unsigned win = 0;
#pragma nounroll
    for (int pass = 0;; ++pass) {
        gemm_first<0>(wave, (vb & 4) == 0, vb >> 7, 4 * (vb & 3) + ((vb >> 5) & 3), (vb >> 3) & 3);
        unsigned old = 0;
        PT(0);
        if (pass == 0) {
            asm volatile("s_waitcnt lgkmcnt(0)" : "+s"(tag) :: "memory");
            tag += 0x9E3779B4u; old = tag;
            if (wave == 0) asm volatile("s_nop 4\n\tbuffer_atomic_swap %0, %1, %2, %3 offen sc0 sc1" : "+v"(old) : "v"(threadIdx.x == 0 ? O_CNT : OOB), "s"(rhb), "s"((xcc * 32 + (int)(blockIdx.x >> 3)) * 128) : "memory");
        }
        if (pass == 0 && prober) {
            waitvm<18>();
            unsigned best = 1u << 20;
#pragma nounroll
            for (int k = 0; k < 4; ++k) {
                u64 c0, c1; unsigned d;
                asm volatile("s_memtime %0\n\ts_waitcnt lgkmcnt(0)\n\ts_load_dword %2, %3, %4\n\ts_waitcnt lgkmcnt(0)\n\ts_memtime %1\n\ts_waitcnt lgkmcnt(0)" : "=&s"(c0), "=&s"(c1), "=&s"(d) : "s"(a.HB), "s"(O_PROBE + wave * 32768 + ((k + wave) & 3) * 8192) : "memory");
                best = min(best, ((unsigned)(c1 - c0)) * 4 + ((k + wave) & 3));
            }
            *(unsigned LAS*)(smem_ + L_SCR + wave * 4) = best;
        }
        PT(1);
        gemm_first<1>(wave, (vb & 4) == 0, vb >> 7, 4 * (vb & 3) + ((vb >> 5) & 3), (vb >> 3) & 3);
        PT(2);
        WAITV(8, "+v"(old));
        PT(3);
        if (pass) { bar(); break; }
        if (threadIdx.x == 0) {
            unsigned slot = (unsigned)xcc * 32u + (blockIdx.x >> 3);
            if (old == tag)
                for (int k = 0; k < 256; ++k) { slot = (slot + 1u) & 255u; if (__hip_atomic_exchange((unsigned*)(a.HB + O_CNT) + slot * 32u, tag, __ATOMIC_RELAXED, __HIP_MEMORY_SCOPE_AGENT) != tag) break; }
            *(unsigned LAS*)(smem_ + L_SCR) = (slot >> 5) + 8u * (slot & 31u);
        }
        bar();
        const int got = __builtin_amdgcn_readfirstlane(*(const unsigned LAS*)(smem_ + L_SCR));
        if (got == vb) { if (prober) win = min(min(*(const unsigned LAS*)(smem_ + L_SCR + 4), *(const unsigned LAS*)(smem_ + L_SCR + 8)), *(const unsigned LAS*)(smem_ + L_SCR + 12)) & 3u; break; }
        vb = got; waitvm<0>(); bar();
    }
    vb = __builtin_amdgcn_readfirstlane(vb); tag = __builtin_amdgcn_readfirstlane(tag);
    const int res = vb & 7, idx = vb >> 3;
    if (res < 4) {
        const int lane = fresh_lane(), nb = res * 32 + idx;
        if (vb == 249 && wave == 3) { const unsigned t0 = rt_now(); sst128((u4){win ^ tag, t0, t0 ^ tag, 0u}, rhb, lane == 0 ? O_PAR : OOB, 0); }
        sst128((u4){FF, FF, FF, FF}, rhb, lane * 16, nb * 8192 + wave * 2048); sst128((u4){FF, FF, FF, FF}, rhb, lane * 16, nb * 8192 + wave * 2048 + 1024);
        if (nb == 0) sst128((u4){0u, 0u, 0u, 0u}, rhb, lane * 16, O_ANN + wave * 1024);
    }
    if (idx >> 4) gemm_role<1>(tag, vb, wave, res < 4, 4 * (res & 3) + ((idx >> 2) & 3), idx & 3); else gemm_role<0>(tag, vb, wave, res < 4, 4 * (res & 3) + ((idx >> 2) & 3), idx & 3);
    PSTAMP(3);
    if (res < 4 && idx < 28) chain_role(tag, vb, wave);
    const int f = res < 4 ? res * 5 + idx - 27 : ((idx & 2) ? 20 : 84) + (idx >> 2) * 8 + (idx & 1) * 4 + (res - 4);
    if (X_NOA && f < 20) { sst32(tag, mksrd(get_args().HB, HB_SZ), O_FA + f * 64, 0); return; }
    if (f < 56) { parta_task(tag, wave, f < 20 ? f : 20 + ((f - 20) & 3) * 9 + ((f - 20) >> 2)); PSTAMP(5); }
    if (f < 20) return;
#pragma nounroll
    for (int it = 0; it < 6; ++it) {
        int t = 0;
        if (f >= 84) t = it == 0 ? 0x3000 + 20 + f - 84 : 0;
        else if (f >= 56 && (it == 0 || f < 80)) t = it < 2 ? 0x1000 + f - 56 + 28 * it : 0;
        else {
            const int i = f < 56 ? f - 16 : f - 80;
            t = it == 1 ? 0x2000 + i : it == 2 ? (i < 8 ? 0x2000 + 40 + i : 0x4000 + i - 8) : it == 3 ? (i < 20 ? 0x3000 + i : 0x4000 + 12 + i) : it == 4 ? (i >= 36 ? 0x4000 + 16 + i : 0) : 0;
        }
        const int k = t >> 12, j = t & 4095;
        if (k == 1) o_task(tag, wave, j); else if (k == 2) e_task(tag, wave, 52 + (j >> 2), j & 3); else if (k == 3) x_task(tag, wave, 52 + j / 7, j % 7); else if (k == 4) pa_out(tag, wave, j);
    }
    PSTAMP(6);
}
__device__ __attribute__((used, noinline)) void fk_tail() { asm volatile("fk_code_hi:"); }

extern "C" void kernel_launch(void* const* d_in, const int* in_sizes, int n_in, void* d_out, int out_size, void* d_ws, size_t ws_size, hipStream_t stream) {
    Args a;
    a.Au = (const u16*)d_in[0]; a.AlogA = (const u16*)d_in[1]; a.Aim = (const u16*)d_in[2]; a.ACr = (const u16*)d_in[3]; a.ACi = (const u16*)d_in[4];
    a.Alogdt = (const u16*)d_in[5]; a.ADsk = (const u16*)d_in[6]; a.X = (const u16*)d_in[7]; a.mem0 = (const u16*)d_in[8];
    a.Wq = (const u16*)d_in[9]; a.Wk = (const u16*)d_in[10]; a.Wv = (const u16*)d_in[11]; a.Wo = (const u16*)d_in[12];
    a.out = (u16*)d_out;
    char* base = (char*)(((size_t)d_ws + (2u << 20) - 1) & ~(size_t)((2u << 20) - 1));
    a.HB = base;
    size_t off = HB_SZ;
    auto alloc = [&](size_t n) { char* p = base + off; off += (n + 4095) & ~(size_t)4095; return p; };
    a.TS = (unsigned*)alloc(8u << 20);
    a.WvoF = (u16*)alloc(28 * 16 * 14336); a.WqkF = (u16*)alloc(28 * 16 * 14336); a.GF = (u16*)alloc((size_t)16 * 64 * 28672); a.UF = (u16*)alloc((size_t)16 * 64 * 28672);
    a.EX = (u16*)alloc(64 * 16 * 2048); a.UM = (u16*)alloc(28 * 64 * 4096); a.MZX = (float*)alloc(64 * 16 * 256); a.SXM = (float*)alloc(64 * 65536); a.PA = (u16*)alloc(56 * 32768);
    (void)hipFuncSetAttribute((const void*)fused_kernel, hipFuncAttributeMaxDynamicSharedMemorySize, 160 * 1024);
    fused_kernel<<<256, 256, 160 * 1024, stream>>>(a);
}
```

```cpp
#include <hip/hip_runtime.h>
#include <stdint.h>
typedef unsigned short u16;
typedef unsigned long long u64;
typedef __attribute__((ext_vector_type(2))) __bf16 bf2;
typedef __attribute__((ext_vector_type(2))) float f2;
typedef __attribute__((ext_vector_type(4))) float f4;
typedef __attribute__((ext_vector_type(16))) float f16v;
typedef __attribute__((ext_vector_type(2))) unsigned u2;
typedef __attribute__((ext_vector_type(4))) unsigned u4;
typedef __attribute__((ext_vector_type(16))) unsigned u16v;
typedef __attribute__((ext_vector_type(4))) short s4;
#define LAS __attribute__((address_space(3)))
#define DI __device__ __forceinline__
#define SB() __builtin_amdgcn_sched_barrier(0)
#define PIN(x) asm volatile("" : "+v"(x))
#define WAITV(n, ...) asm volatile("s_waitcnt vmcnt(" #n ")" : __VA_ARGS__ :: "memory")
#define LOG2E 1.4426950408889634f
#define CSCALE (LOG2E * 0.0625f)
#define FF 0xFFFFFFFFu
#define OOB 0x7f000000
#define SPIN_MAX (1u << 18)

#define HB_SZ (2 << 20)
#define O_CTL (1 << 20)
#define O_ANN O_CTL
#define O_PAR (O_CTL + 4096)
#define O_FW (O_CTL + 8192)
#define O_FG (O_CTL + 24576)
#define O_FE (O_CTL + 32768)
#define O_FH (O_CTL + 49152)
#define O_FP (O_CTL + 57344)
#define O_FA (O_CTL + 65536)
#define O_CNT (O_CTL + 73728)
#define O_EPOCH (O_CNT + 32768)
#define O_PROBE (O_CTL + 131072)

#define GH 1835008
#define UH 1048576
#define NC 8
#define MH (NC * 4096 + 16 * 14336)
#define SH (NC * 1024 + 8 * 14336)
#define GSZ (34 << 20)
struct Args {
    const u16 *Au, *AlogA, *Aim, *ACr, *ACi, *Alogdt, *ADsk, *X, *mem0, *Wq, *Wk, *Wv, *Wo;
    u16* out;
    char* HB;
    u16 *GF, *UF, *EX, *UM, *PA; float *MZX, *SXM;
    unsigned* TS;
};
typedef const Args __attribute__((address_space(4))) CArgs;
DI CArgs& get_args() { CArgs* p = (CArgs*)__builtin_amdgcn_kernarg_segment_ptr(); asm volatile("" : "+s"(p)); return *p; }
extern __shared__ __attribute__((aligned(16))) char smem_[];

DI unsigned pk2(float a, float b) { f2 v = {a, b}; return __builtin_bit_cast(unsigned, __builtin_convertvector(v, bf2)); }
DI float bfr(float a) { return __uint_as_float(pk2(a, a) & 0xffff0000u); }
DI float bf2f(u16 x) { return __uint_as_float((unsigned)x << 16); }
DI unsigned san(unsigned x) { return x < 0xFFFFFFFEu ? x : 0xFFFFFFFEu; }
DI void bar() { asm volatile("s_waitcnt lgkmcnt(0)\n\ts_barrier" ::: "memory"); SB(); }
DI float x32max(float a) { auto r = __builtin_amdgcn_permlane32_swap(__float_as_uint(a), __float_as_uint(a), false, false); return fmaxf(__uint_as_float(r[0]), __uint_as_float(r[1])); }
DI float x32add(float a) { auto r = __builtin_amdgcn_permlane32_swap(__float_as_uint(a), __float_as_uint(a), false, false); return __uint_as_float(r[0]) + __uint_as_float(r[1]); }
DI u2 trread(const char LAS* p) { return __builtin_bit_cast(u2, __builtin_amdgcn_ds_read_tr16_b64_v4i16((LAS s4*)p)); }
DI u4 mksrd(const void* p, unsigned bytes) { u64 x = (u64)p; return (u4){(unsigned)x, (unsigned)(x >> 32) & 0xffffu, bytes, 0x00020000u}; }
DI unsigned now32() { u64 t; asm volatile("s_memtime %0\n\ts_waitcnt lgkmcnt(0)" : "=s"(t) :: "memory"); return (unsigned)t; }
DI unsigned rt_now() { u64 t; asm volatile("s_memrealtime %0\n\ts_waitcnt lgkmcnt(0)" : "=s"(t) :: "memory"); return (unsigned)t; }
DI u4 ld128(u4 srd, int voff, int soff) { u4 v; asm volatile("buffer_load_dwordx4 %0, %1, %2, %3 offen sc1" : "=v"(v) : "v"(voff), "s"(srd), "s"(soff) : "memory"); return v; }
DI void st128(u4 v, u4 srd, int voff, int soff) { asm volatile("buffer_store_dwordx4 %0, %1, %2, %3 offen sc1\n\ts_nop 1" :: "v"(v), "v"(voff), "s"(srd), "s"(soff) : "memory"); }
DI void st32(unsigned v, u4 srd, int voff, int soff) { asm volatile("buffer_store_dword %0, %1, %2, %3 offen sc1" :: "v"(v), "v"(voff), "s"(srd), "s"(soff) : "memory"); }
DI u4 sld128(u4 srd, int voff, int soff) { u4 v; asm volatile("s_nop 4\n\tbuffer_load_dwordx4 %0, %1, %2, %3 offen sc1" : "=v"(v) : "v"(voff), "s"(srd), "s"(soff) : "memory"); return v; }
DI u2 sld64(u4 srd, int voff, int soff) { u2 v; asm volatile("s_nop 4\n\tbuffer_load_dwordx2 %0, %1, %2, %3 offen sc1" : "=v"(v) : "v"(voff), "s"(srd), "s"(soff) : "memory"); return v; }
DI unsigned sld32(u4 srd, int voff, int soff) { unsigned v; asm volatile("s_nop 4\n\tbuffer_load_dword %0, %1, %2, %3 offen sc1" : "=v"(v) : "v"(voff), "s"(srd), "s"(soff) : "memory"); return v; }
DI void sst128(u4 v, u4 srd, int voff, int soff) { asm volatile("s_nop 4\n\tbuffer_store_dwordx4 %0, %1, %2, %3 offen sc1\n\ts_nop 1" :: "v"(v), "v"(voff), "s"(srd), "s"(soff) : "memory"); }
DI void sst64(u2 v, u4 srd, int voff, int soff) { asm volatile("s_nop 4\n\tbuffer_store_dwordx2 %0, %1, %2, %3 offen sc1" :: "v"(v), "v"(voff), "s"(srd), "s"(soff) : "memory"); }
DI void sst32(unsigned v, u4 srd, int voff, int soff) { asm volatile("s_nop 4\n\tbuffer_store_dword %0, %1, %2, %3 offen sc1" :: "v"(v), "v"(voff), "s"(srd), "s"(soff) : "memory"); }
template <int N> DI void waitvm() { asm volatile("s_waitcnt vmcnt(%0)" :: "i"(N) : "memory"); }
DI int fresh_lane() { int l; asm volatile("v_mbcnt_lo_u32_b32 %0, -1, 0\n\tv_mbcnt_hi_u32_b32 %0, -1, %0" : "=v"(l)); __builtin_assume(l >= 0 && l < 64); return l; }
#define W_SLP 8
template <int SLP = W_SLP>
DI void wait_flags(u4 rhb, int voff_or_oob, unsigned want) {
    unsigned sp = 0;
#pragma nounroll
    for (;;) {
        unsigned v = sld32(rhb, voff_or_oob, 0); WAITV(0, "+v"(v));
        if (__all((v - want <= 72u) | (voff_or_oob >= OOB))) break;
        unsigned k = __builtin_amdgcn_readfirstlane(want - v);
        k = k > 64u ? (SLP < W_SLP ? 1u : 2u) : k;
        asm volatile("s_sleep %0" :: "i"(SLP) : "memory");
#pragma nounroll
        for (unsigned i = 1; i < k; ++i) asm volatile("s_sleep 30" ::: "memory");
        if (++sp > SPIN_MAX / 16) __builtin_amdgcn_endpgm();
    }
}
#define PSTAMP(k) do { } while (0)
#define RFL(x) (unsigned)__builtin_amdgcn_readfirstlane(x)
#define STAMPS 0
struct Ctx { u4 r, rw, rg, rx, rts; unsigned lv0, lv1, t0, tf0, per, R, tag, sw, sg, sx, des, role; unsigned idx, vl16, vm, vg, vg3, vtl, vtr, vsel, vpub, vraw, vfl, vfo, ve, vs, vrw, vrr, vr1, vr2, vts, vdbg, vtb; };
DI void chain_asm(const Ctx& c) {
    const u16v s0 = {RFL(c.r[0]), RFL(c.r[1]), RFL(c.r[2]), RFL(c.r[3]), RFL(c.rw[0]), RFL(c.rw[1]), RFL(c.rw[2]), RFL(c.rw[3]), RFL(c.rg[0]), RFL(c.rg[1]), RFL(c.rg[2]), RFL(c.rg[3]), RFL(c.rx[0]), RFL(c.rx[1]), RFL(c.rx[2]), RFL(c.rx[3])}, s1 = {RFL(c.rts[0]), RFL(c.rts[1]), RFL(c.rts[2]), RFL(c.rts[3]), RFL(c.lv0), RFL(c.lv1), RFL(c.t0), RFL(c.tf0), RFL(c.per), RFL(c.R), RFL(c.tag), RFL(c.sw), RFL(c.sg), RFL(c.sx), RFL(c.des), RFL(c.role)}, v0 = {c.idx, c.vl16, c.vm, c.vg, c.vg3, c.vtl, c.vtr, c.vsel, c.vpub, c.vraw, c.vfl, c.vfo, c.ve, c.vs, c.vrw, c.vrr}, v1 = {c.vr1, c.vr2, c.vts, c.vdbg, c.vtb, 0u, 0u, 0u, 0u, 0u, 0u, 0u, 0u, 0u, 0u, 0u};
    asm volatile(
"s_nop 4\ns_cmp_eq_u32 s39, 0\ns_cbranch_scc1 fk_jh0\ns_cmp_eq_u32 s39, 1\n"
"s_cbranch_scc1 fk_jh1\ns_cmp_eq_u32 s39, 2\ns_cbranch_scc1 fk_jh2\ns_cmp_eq_u32 s39, 3\n"
"s_cbranch_scc1 fk_jh3\ns_cmp_eq_u32 s39, 4\ns_cbranch_scc1 fk_jx0\ns_cmp_eq_u32 s39, 5\n"
"s_cbranch_scc1 fk_jx1\ns_cmp_eq_u32 s39, 6\ns_cbranch_scc1 fk_jx2\ns_cmp_eq_u32 s39, 7\n"
"s_cbranch_scc1 fk_jx3\ns_cmp_ge_u32 s39, 8\ns_cbranch_scc1 fk_jp0\ns_endpgm\n"
"fk_jh0:\ns_branch fk_h0\nfk_jh1:\ns_branch fk_h1\n"
"fk_jh2:\ns_branch fk_h2\nfk_jh3:\ns_branch fk_h3\n"
"fk_jx0:\ns_branch fk_x0\nfk_jx1:\ns_branch fk_x1\n"
"fk_jx2:\ns_branch fk_x2\nfk_jx3:\ns_branch fk_x3\n"
"fk_jp0:\ns_branch fk_p0\nfk_h0:\ns_mov_b32 s40, 0\n"
"s_mov_b32 s41, 0\ns_mov_b32 s42, 262144\ns_mov_b32 s43, 393216\ns_mov_b32 s44, s30\n"
"s_mov_b32 s45, 0\ns_bcnt1_i32_b64 s46, s[28:29]\ns_mov_b32 s92, 99\ns_mov_b32 s49, 0\n"
"s_mov_b32 s53, 0\ns_mov_b32 s50, s36\ns_mov_b32 s51, s37\nv_mov_b32 v131, v13\n"
"v_mov_b32 v132, v14\nv_mov_b32 v134, 24576\nv_mov_b32 v115, 0\nv_mov_b32 v120, -1\n"
"v_mov_b32 v121, -1\nv_mov_b32 v122, -1\nv_mov_b32 v123, -1\ns_add_u32 s47, s35, 0\n"
"s_and_b32 s52, s47, 1\ns_mul_i32 s52, s52, -14208\ns_mul_i32 s48, s47, 14336\ns_add_u32 s48, s48, s52\n"
"s_lshr_b32 s52, s47, 3\ns_mul_i32 s52, s52, 8192\ns_add_u32 s48, s48, s52\ns_add_u32 s47, s48, 0\n"
"buffer_load_dwordx4 a[0:3], v15, s[12:15], s47 offen sc1\nbuffer_load_dwordx4 a[4:7], v15, s[12:15], s47 offen offset:1024 sc1\ns_add_u32 s47, s48, 2048\nbuffer_load_dwordx4 a[8:11], v15, s[12:15], s47 offen sc1\n"
"buffer_load_dwordx4 a[12:15], v15, s[12:15], s47 offen offset:1024 sc1\ns_add_u32 s47, s48, 4096\nbuffer_load_dwordx4 a[16:19], v15, s[12:15], s47 offen sc1\nbuffer_load_dwordx4 a[20:23], v15, s[12:15], s47 offen offset:1024 sc1\n"
"s_add_u32 s47, s48, 6144\nbuffer_load_dwordx4 a[24:27], v15, s[12:15], s47 offen sc1\nbuffer_load_dwordx4 a[28:31], v15, s[12:15], s47 offen offset:1024 sc1\ns_add_u32 s47, s35, 1\n"
"s_and_b32 s52, s47, 1\ns_mul_i32 s52, s52, -14208\ns_mul_i32 s48, s47, 14336\ns_add_u32 s48, s48, s52\n"
"s_lshr_b32 s52, s47, 3\ns_mul_i32 s52, s52, 8192\ns_add_u32 s48, s48, s52\ns_add_u32 s47, s48, 0\n"
"buffer_load_dwordx4 a[32:35], v15, s[12:15], s47 offen sc1\nbuffer_load_dwordx4 a[36:39], v15, s[12:15], s47 offen offset:1024 sc1\ns_add_u32 s47, s48, 2048\nbuffer_load_dwordx4 a[40:43], v15, s[12:15], s47 offen sc1\n"
"buffer_load_dwordx4 a[44:47], v15, s[12:15], s47 offen offset:1024 sc1\ns_add_u32 s47, s48, 4096\nbuffer_load_dwordx4 a[48:51], v15, s[12:15], s47 offen sc1\nbuffer_load_dwordx4 a[52:55], v15, s[12:15], s47 offen offset:1024 sc1\n"
"s_add_u32 s47, s48, 6144\nbuffer_load_dwordx4 a[56:59], v15, s[12:15], s47 offen sc1\nbuffer_load_dwordx4 a[60:63], v15, s[12:15], s47 offen offset:1024 sc1\ns_add_u32 s47, s35, 2\n"
"s_and_b32 s52, s47, 1\ns_mul_i32 s52, s52, -14208\ns_mul_i32 s48, s47, 14336\ns_add_u32 s48, s48, s52\n"
"s_lshr_b32 s52, s47, 3\ns_mul_i32 s52, s52, 8192\ns_add_u32 s48, s48, s52\ns_add_u32 s47, s48, 0\n"
"buffer_load_dwordx4 a[64:67], v15, s[12:15], s47 offen sc1\nbuffer_load_dwordx4 a[68:71], v15, s[12:15], s47 offen offset:1024 sc1\ns_add_u32 s47, s48, 2048\nbuffer_load_dwordx4 a[72:75], v15, s[12:15], s47 offen sc1\n"
"buffer_load_dwordx4 a[76:79], v15, s[12:15], s47 offen offset:1024 sc1\ns_add_u32 s47, s48, 4096\nbuffer_load_dwordx4 a[80:83], v15, s[12:15], s47 offen sc1\nbuffer_load_dwordx4 a[84:87], v15, s[12:15], s47 offen offset:1024 sc1\n"
"s_add_u32 s47, s48, 6144\nbuffer_load_dwordx4 a[88:91], v15, s[12:15], s47 offen sc1\nbuffer_load_dwordx4 a[92:95], v15, s[12:15], s47 offen offset:1024 sc1\ns_add_u32 s47, s35, 3\n"
"s_and_b32 s52, s47, 1\ns_mul_i32 s52, s52, -14208\ns_mul_i32 s48, s47, 14336\ns_add_u32 s48, s48, s52\n"
"s_lshr_b32 s52, s47, 3\ns_mul_i32 s52, s52, 8192\ns_add_u32 s48, s48, s52\ns_add_u32 s47, s48, 0\n"
"buffer_load_dwordx4 a[96:99], v15, s[12:15], s47 offen sc1\nbuffer_load_dwordx4 a[100:103], v15, s[12:15], s47 offen offset:1024 sc1\ns_add_u32 s47, s48, 2048\nbuffer_load_dwordx4 a[104:107], v15, s[12:15], s47 offen sc1\n"
"buffer_load_dwordx4 a[108:111], v15, s[12:15], s47 offen offset:1024 sc1\ns_add_u32 s47, s48, 4096\nbuffer_load_dwordx4 a[112:115], v15, s[12:15], s47 offen sc1\nbuffer_load_dwordx4 a[116:119], v15, s[12:15], s47 offen offset:1024 sc1\n"
"s_add_u32 s47, s48, 6144\nbuffer_load_dwordx4 a[120:123], v15, s[12:15], s47 offen sc1\nbuffer_load_dwordx4 a[124:127], v15, s[12:15], s47 offen offset:1024 sc1\ns_add_u32 s47, s50, 7168\n"
"buffer_load_dwordx4 v[72:75], v11, s[16:19], s50 offen\nbuffer_load_dwordx4 v[76:79], v12, s[16:19], s50 offen\nbuffer_load_dwordx4 v[80:83], v11, s[16:19], s47 offen\nbuffer_load_dwordx4 v[84:87], v12, s[16:19], s47 offen\n"
"s_add_u32 s50, s50, 28672\ns_waitcnt vmcnt(0)\ns_barrier\ns_memrealtime s[54:55]\n"
"s_waitcnt lgkmcnt(0)\ns_add_u32 s44, s54, 16\ns_mov_b32 s45, 8\n.p2align 6\n"
"top_h0:\nv_mov_b32 v56, 0\nv_mov_b32 v57, 0\nv_mov_b32 v58, 0\n"
"v_mov_b32 v59, 0\nv_mov_b32 v60, 0\nv_mov_b32 v61, 0\nv_mov_b32 v62, 0\n"
"v_mov_b32 v63, 0\nv_mov_b32 v64, 0\nv_mov_b32 v65, 0\nv_mov_b32 v66, 0\n"
"v_mov_b32 v67, 0\nv_mov_b32 v68, 0\nv_mov_b32 v69, 0\nv_mov_b32 v70, 0\n"
"v_mov_b32 v71, 0\nv_mov_b32 v128, 0\ns_mul_i32 s48, s45, 6\ns_and_b32 s48, s48, -4\n"
"s_getpc_b64 s[60:61]\nspc_h0:\ns_sub_u32 s48, sgo_h0-spc_h0, s48\ns_add_u32 s60, s60, s48\n"
"s_addc_u32 s61, s61, 0\nsf_h0:\ns_memrealtime s[54:55]\ns_nop 2\n"
"s_memrealtime s[56:57]\ns_nop 2\ns_memrealtime s[58:59]\nsg_h0:\n"
"s_waitcnt lgkmcnt(4)\ns_sub_u32 s47, s54, s44\ns_add_u32 s48, s47, 12\ns_cmp_ge_u32 s48, 12\n"
"s_cbranch_scc1 so_h0\ns_memrealtime s[54:55]\ns_waitcnt lgkmcnt(4)\ns_cmp_ge_u32 s56, s44\n"
"s_cbranch_scc1 sx_h0\ns_memrealtime s[56:57]\ns_waitcnt lgkmcnt(4)\ns_cmp_ge_u32 s58, s44\n"
"s_cbranch_scc1 sx_h0\ns_memrealtime s[58:59]\ns_waitcnt lgkmcnt(4)\ns_cmp_ge_u32 s54, s44\n"
"s_cbranch_scc1 sx_h0\ns_memrealtime s[54:55]\ns_waitcnt lgkmcnt(4)\ns_cmp_ge_u32 s56, s44\n"
"s_cbranch_scc1 sx_h0\ns_memrealtime s[56:57]\ns_waitcnt lgkmcnt(4)\ns_cmp_ge_u32 s58, s44\n"
"s_cbranch_scc1 sx_h0\ns_memrealtime s[58:59]\ns_waitcnt lgkmcnt(4)\ns_cmp_ge_u32 s54, s44\n"
"s_cbranch_scc1 sx_h0\ns_memrealtime s[54:55]\ns_waitcnt lgkmcnt(4)\ns_cmp_ge_u32 s56, s44\n"
"s_cbranch_scc1 sx_h0\ns_memrealtime s[56:57]\ns_waitcnt lgkmcnt(4)\ns_cmp_ge_u32 s58, s44\n"
"s_cbranch_scc1 sx_h0\ns_memrealtime s[58:59]\ns_waitcnt lgkmcnt(4)\ns_cmp_ge_u32 s54, s44\n"
"s_cbranch_scc1 sx_h0\ns_memrealtime s[54:55]\ns_waitcnt lgkmcnt(4)\ns_cmp_ge_u32 s56, s44\n"
"s_cbranch_scc1 sx_h0\ns_memrealtime s[56:57]\ns_waitcnt lgkmcnt(4)\ns_cmp_ge_u32 s58, s44\n"
"s_cbranch_scc1 sx_h0\ns_memrealtime s[58:59]\ns_waitcnt lgkmcnt(4)\ns_cmp_ge_u32 s54, s44\n"
"s_cbranch_scc1 sx_h0\ns_memrealtime s[54:55]\ns_waitcnt lgkmcnt(4)\ns_cmp_ge_u32 s56, s44\n"
"s_cbranch_scc1 sx_h0\ns_memrealtime s[56:57]\ns_waitcnt lgkmcnt(4)\ns_cmp_ge_u32 s58, s44\n"
"s_cbranch_scc1 sx_h0\ns_memrealtime s[58:59]\ns_waitcnt lgkmcnt(4)\ns_cmp_ge_u32 s54, s44\n"
"s_cbranch_scc1 sx_h0\ns_memrealtime s[54:55]\ns_waitcnt lgkmcnt(4)\ns_cmp_ge_u32 s56, s44\n"
"s_cbranch_scc1 sx_h0\ns_memrealtime s[56:57]\ns_branch sx_h0\nso_h0:\n"
"s_waitcnt lgkmcnt(0)\ns_cmp_lt_i32 s47, 0\ns_cbranch_scc1 sf_h0\ns_branch sgo_h0\n"
"sx_h0:\ns_setpc_b64 s[60:61]\ns_nop 0\ns_nop 0\n"
"s_nop 0\ns_nop 0\ns_nop 0\ns_nop 0\n"
"s_nop 0\ns_nop 0\ns_nop 0\ns_nop 0\n"
"s_nop 0\ns_nop 0\ns_nop 0\ns_nop 0\n"
"s_nop 0\ns_nop 0\ns_nop 0\ns_nop 0\n"
"s_nop 0\ns_nop 0\ns_nop 0\ns_nop 0\n"
"s_nop 0\ns_nop 0\nsgo_h0:\nbuffer_load_dwordx4 v[40:43], v10, s[8:11], s43 offen offset:0 sc1\n"
"buffer_load_dwordx4 v[44:47], v10, s[8:11], s43 offen offset:1024 sc1\ns_nop 15\ns_nop 3\nbuffer_load_dwordx4 v[48:51], v10, s[8:11], s43 offen offset:2048 sc1\n"
"s_nop 13\nbuffer_load_dwordx4 v[52:55], v10, s[8:11], s43 offen offset:3072 sc1\nlanded_h0:\ns_waitcnt vmcnt(3)\n"
"v_smfmac_f32_16x16x64_bf16 v[56:59], v[40:43], a[0:7], v8\nv_max3_u32 v128, v128, v40, v41\nv_smfmac_f32_16x16x64_bf16 v[60:63], v[40:43], a[32:39], v8\nv_max3_u32 v128, v128, v42, v43\n"
"v_smfmac_f32_16x16x64_bf16 v[64:67], v[40:43], a[64:71], v8\nv_smfmac_f32_16x16x64_bf16 v[68:71], v[40:43], a[96:103], v8\ns_waitcnt vmcnt(2)\nv_smfmac_f32_16x16x64_bf16 v[56:59], v[44:47], a[8:15], v8\n"
"v_max3_u32 v128, v128, v44, v45\nv_smfmac_f32_16x16x64_bf16 v[60:63], v[44:47], a[40:47], v8\nv_max3_u32 v128, v128, v46, v47\nv_smfmac_f32_16x16x64_bf16 v[64:67], v[44:47], a[72:79], v8\n"
"v_smfmac_f32_16x16x64_bf16 v[68:71], v[44:47], a[104:111], v8\ns_waitcnt vmcnt(1)\nv_smfmac_f32_16x16x64_bf16 v[56:59], v[48:51], a[16:23], v8\nv_max3_u32 v128, v128, v48, v49\n"
"v_smfmac_f32_16x16x64_bf16 v[60:63], v[48:51], a[48:55], v8\nv_max3_u32 v128, v128, v50, v51\nv_smfmac_f32_16x16x64_bf16 v[64:67], v[48:51], a[80:87], v8\nv_smfmac_f32_16x16x64_bf16 v[68:71], v[48:51], a[112:119], v8\n"
"s_waitcnt vmcnt(0)\nv_smfmac_f32_16x16x64_bf16 v[56:59], v[52:55], a[24:31], v8\nv_max3_u32 v128, v128, v52, v53\nv_smfmac_f32_16x16x64_bf16 v[60:63], v[52:55], a[56:63], v8\n"
"v_max3_u32 v128, v128, v54, v55\nv_smfmac_f32_16x16x64_bf16 v[64:67], v[52:55], a[88:95], v8\nv_cmp_eq_u32 vcc, -1, v128\nv_smfmac_f32_16x16x64_bf16 v[68:71], v[52:55], a[120:127], v8\n"
"v_add_f32 v129, v56, v57\nv_add_f32 v130, v58, v59\nv_cvt_pk_bf16_f32 v112, v129, v130\nv_add_f32 v129, v60, v61\n"
"v_add_f32 v130, v62, v63\nv_cvt_pk_bf16_f32 v113, v129, v130\nv_add_f32 v129, v64, v65\nv_add_f32 v130, v66, v67\n"
"v_cvt_pk_bf16_f32 v114, v129, v130\nv_add_f32 v129, v68, v69\nv_add_f32 v130, v70, v71\nv_cvt_pk_bf16_f32 v115, v129, v130\n"
"ds_write2_b64 v131, v[112:113], v[114:115] offset0:0 offset1:16\ns_cbranch_vccnz retry_h0\nv_mov_b32 v129, 0\ns_nop 1\n"
"v_mov_b32 v129, 0\ns_nop 1\ns_waitcnt lgkmcnt(0)\ns_barrier\n"
"s_nop 9\nds_read_b64_tr_b16 v[88:89], v132 offset:0\nds_read_b64_tr_b16 v[90:91], v132 offset:32\nds_read_b64_tr_b16 v[92:93], v132 offset:256\n"
"ds_read_b64_tr_b16 v[94:95], v132 offset:288\nds_read_b64_tr_b16 v[96:97], v132 offset:512\nds_read_b64_tr_b16 v[98:99], v132 offset:544\nds_read_b64_tr_b16 v[100:101], v132 offset:768\n"
"ds_read_b64_tr_b16 v[102:103], v132 offset:800\ns_waitcnt lgkmcnt(6)\nv_mfma_f32_16x16x32_bf16 v[104:107], v[88:91], v[72:75], 0\nv_mfma_f32_16x16x32_bf16 v[108:111], v[88:91], v[80:83], 0\n"
"s_waitcnt lgkmcnt(4)\nv_mfma_f32_16x16x32_bf16 v[104:107], v[92:95], v[76:79], v[104:107]\nv_mfma_f32_16x16x32_bf16 v[108:111], v[92:95], v[84:87], v[108:111]\ns_waitcnt lgkmcnt(2)\n"
"v_mfma_f32_16x16x32_bf16 v[104:107], v[96:99], v[72:75], v[104:107]\nv_mfma_f32_16x16x32_bf16 v[108:111], v[96:99], v[80:83], v[108:111]\ns_waitcnt lgkmcnt(0)\nv_mfma_f32_16x16x32_bf16 v[104:107], v[100:103], v[76:79], v[104:107]\n"
"v_mfma_f32_16x16x32_bf16 v[108:111], v[100:103], v[84:87], v[108:111]\ns_nop 7\nv_add_f32_dpp v124, v106, v104 row_ror:8 row_mask:0xf bank_mask:0x3\nv_add_f32_dpp v124, v108, v110 row_ror:8 row_mask:0xf bank_mask:0xc\n"
"v_add_f32_dpp v125, v107, v105 row_ror:8 row_mask:0xf bank_mask:0x3\nv_add_f32_dpp v125, v109, v111 row_ror:8 row_mask:0xf bank_mask:0xc\ns_add_u32 s47, s50, 7168\nbuffer_load_dwordx4 v[72:75], v11, s[16:19], s50 offen\n"
"buffer_load_dwordx4 v[76:79], v12, s[16:19], s50 offen\nbuffer_load_dwordx4 v[80:83], v11, s[16:19], s47 offen\nbuffer_load_dwordx4 v[84:87], v12, s[16:19], s47 offen\ns_add_u32 s50, s50, 28672\n"
"buffer_store_dwordx2 v[124:125], v16, s[20:23], s51 offen sc1\ns_add_u32 s51, s51, 1024\ns_add_u32 s47, s40, s34\nv_mov_b32 v135, s47\n"
"buffer_store_dword v135, v18, s[8:11], 0 offen sc1\nds_read_b32 v133, v134\nv_xor_b32 v131, 8192, v131\nv_xor_b32 v132, 8192, v132\n"
"s_waitcnt lgkmcnt(0)\nv_readfirstlane_b32 s47, v133\ns_sub_u32 s47, s47, s40\ns_cmp_eq_u32 s47, 1\n"
"s_cbranch_scc1 adopt_h0\nback_h0:\ns_mul_i32 s48, s46, 8\ns_add_u32 s45, s45, s48\n"
"s_add_u32 s45, s45, s32\ns_lshr_b32 s48, s45, 4\ns_lshl_b32 s48, s48, 2\ns_add_u32 s44, s44, s48\n"
"s_and_b32 s45, s45, 15\ns_add_u32 s52, s40, 1\ns_bitcmp1_b64 s[28:29], s52\ns_subb_u32 s46, s46, 0\n"
"s_add_u32 s40, s40, 1\ns_mov_b32 s43, s41\ns_add_u32 s41, s41, 131072\ns_and_b32 s41, s41, 524287\n"
"s_add_u32 s92, s92, 1\ns_xor_b32 s42, s41, 262144\ns_waitcnt lgkmcnt(0)\ns_cmp_lt_u32 s40, 64\n"
"s_cbranch_scc1 top_h0\ns_add_u32 s47, s34, 64\nv_mov_b32 v135, s47\ns_waitcnt vmcnt(0)\n"
"buffer_store_dword v135, v18, s[8:11], 0 offen sc1\ns_branch end_h0\nretry2_h0:\ns_sleep 3\n"
"retry_h0:\ns_add_u32 s53, s53, 1\ns_add_u32 s49, s49, 1\ns_cmp_gt_u32 s49, 65536\n"
"s_cbranch_scc1 end_h0\nbuffer_load_dwordx4 v[40:43], v10, s[8:11], s43 offen offset:0 sc1\nbuffer_load_dwordx4 v[44:47], v10, s[8:11], s43 offen offset:1024 sc1\nbuffer_load_dwordx4 v[48:51], v10, s[8:11], s43 offen offset:2048 sc1\n"
"buffer_load_dwordx4 v[52:55], v10, s[8:11], s43 offen offset:3072 sc1\nv_mov_b32 v128, 0\ns_add_u32 s47, s40, 1\nv_mov_b32 v133, s47\n"
"ds_write_b32 v134, v133\ns_waitcnt vmcnt(0)\nv_max3_u32 v128, v128, v40, v41\nv_max3_u32 v128, v128, v42, v43\n"
"v_max3_u32 v128, v128, v44, v45\nv_max3_u32 v128, v128, v46, v47\nv_max3_u32 v128, v128, v48, v49\nv_max3_u32 v128, v128, v50, v51\n"
"v_max3_u32 v128, v128, v52, v53\nv_max3_u32 v128, v128, v54, v55\nv_cmp_eq_u32 vcc, -1, v128\nv_mov_b32 v56, 0\n"
"v_mov_b32 v57, 0\nv_mov_b32 v58, 0\nv_mov_b32 v59, 0\nv_mov_b32 v60, 0\n"
"v_mov_b32 v61, 0\nv_mov_b32 v62, 0\nv_mov_b32 v63, 0\nv_mov_b32 v64, 0\n"
"v_mov_b32 v65, 0\nv_mov_b32 v66, 0\nv_mov_b32 v67, 0\nv_mov_b32 v68, 0\n"
"v_mov_b32 v69, 0\nv_mov_b32 v70, 0\nv_mov_b32 v71, 0\ns_cbranch_vccnz retry2_h0\n"
"s_branch landed_h0\nadopt_h0:\nbuffer_load_dword v116, v28, s[8:11], 0 offen sc1\ns_lshl_b32 s52, s40, 26\n"
"s_or_b32 s47, s52, 0x3ffffff\ns_waitcnt vmcnt(0)\nv_cmp_ge_u32 vcc, s47, v116\ns_nop 1\n"
"v_cndmask_b32 v133, 0, v116, vcc\ns_nop 1\nv_max_u32_dpp v133, v133, v133 row_ror:8 row_mask:0xf bank_mask:0xf\ns_nop 1\n"
"v_max_u32_dpp v133, v133, v133 row_ror:4 row_mask:0xf bank_mask:0xf\ns_nop 1\nv_max_u32_dpp v133, v133, v133 row_ror:2 row_mask:0xf bank_mask:0xf\ns_nop 1\n"
"v_max_u32_dpp v133, v133, v133 row_ror:1 row_mask:0xf bank_mask:0xf\ns_nop 0\nv_readlane_b32 s47, v133, 0\nv_readlane_b32 s48, v133, 16\n"
"s_max_u32 s48, s48, s47\ns_cmp_le_u32 s48, s52\ns_cbranch_scc1 back_h0\ns_bfe_u32 s52, s48, 1179656\n"
"s_add_u32 s52, s52, s30\ns_sub_u32 s45, s52, s44\ns_and_b32 s45, s45, 3\ns_sub_u32 s44, s52, s45\n"
"s_lshl_b32 s45, s45, 2\ns_and_b32 s46, s48, 255\ns_mov_b32 s92, 0\ns_branch back_h0\n"
"end_h0:\ns_branch fk_done\nfk_h1:\ns_mov_b32 s40, 0\n"
"s_mov_b32 s41, 0\ns_mov_b32 s42, 262144\ns_mov_b32 s43, 393216\ns_mov_b32 s44, s30\n"
"s_mov_b32 s45, 0\ns_bcnt1_i32_b64 s46, s[28:29]\ns_mov_b32 s92, 99\ns_mov_b32 s49, 0\n"
"s_mov_b32 s53, 0\ns_mov_b32 s50, s36\ns_mov_b32 s51, s37\nv_mov_b32 v131, v13\n"
"v_mov_b32 v132, v14\nv_mov_b32 v134, 24576\nv_mov_b32 v115, 0\nv_mov_b32 v120, -1\n"
"v_mov_b32 v121, -1\nv_mov_b32 v122, -1\nv_mov_b32 v123, -1\ns_add_u32 s47, s35, 4\n"
"s_and_b32 s52, s47, 1\ns_mul_i32 s52, s52, -14208\ns_mul_i32 s48, s47, 14336\ns_add_u32 s48, s48, s52\n"
"s_lshr_b32 s52, s47, 3\ns_mul_i32 s52, s52, 8192\ns_add_u32 s48, s48, s52\ns_add_u32 s47, s48, 2048\n"
"buffer_load_dwordx4 a[8:11], v15, s[12:15], s47 offen sc1\nbuffer_load_dwordx4 a[12:15], v15, s[12:15], s47 offen offset:1024 sc1\ns_add_u32 s47, s48, 0\nbuffer_load_dwordx4 a[0:3], v15, s[12:15], s47 offen sc1\n"
"buffer_load_dwordx4 a[4:7], v15, s[12:15], s47 offen offset:1024 sc1\ns_add_u32 s47, s48, 6144\nbuffer_load_dwordx4 a[24:27], v15, s[12:15], s47 offen sc1\nbuffer_load_dwordx4 a[28:31], v15, s[12:15], s47 offen offset:1024 sc1\n"
"s_add_u32 s47, s48, 4096\nbuffer_load_dwordx4 a[16:19], v15, s[12:15], s47 offen sc1\nbuffer_load_dwordx4 a[20:23], v15, s[12:15], s47 offen offset:1024 sc1\ns_add_u32 s47, s35, 5\n"
"s_and_b32 s52, s47, 1\ns_mul_i32 s52, s52, -14208\ns_mul_i32 s48, s47, 14336\ns_add_u32 s48, s48, s52\n"
"s_lshr_b32 s52, s47, 3\ns_mul_i32 s52, s52, 8192\ns_add_u32 s48, s48, s52\ns_add_u32 s47, s48, 2048\n"
"buffer_load_dwordx4 a[40:43], v15, s[12:15], s47 offen sc1\nbuffer_load_dwordx4 a[44:47], v15, s[12:15], s47 offen offset:1024 sc1\ns_add_u32 s47, s48, 0\nbuffer_load_dwordx4 a[32:35], v15, s[12:15], s47 offen sc1\n"
"buffer_load_dwordx4 a[36:39], v15, s[12:15], s47 offen offset:1024 sc1\ns_add_u32 s47, s48, 6144\nbuffer_load_dwordx4 a[56:59], v15, s[12:15], s47 offen sc1\nbuffer_load_dwordx4 a[60:63], v15, s[12:15], s47 offen offset:1024 sc1\n"
"s_add_u32 s47, s48, 4096\nbuffer_load_dwordx4 a[48:51], v15, s[12:15], s47 offen sc1\nbuffer_load_dwordx4 a[52:55], v15, s[12:15], s47 offen offset:1024 sc1\ns_add_u32 s47, s35, 6\n"
"s_and_b32 s52, s47, 1\ns_mul_i32 s52, s52, -14208\ns_mul_i32 s48, s47, 14336\ns_add_u32 s48, s48, s52\n"
"s_lshr_b32 s52, s47, 3\ns_mul_i32 s52, s52, 8192\ns_add_u32 s48, s48, s52\ns_add_u32 s47, s48, 2048\n"
"buffer_load_dwordx4 a[72:75], v15, s[12:15], s47 offen sc1\nbuffer_load_dwordx4 a[76:79], v15, s[12:15], s47 offen offset:1024 sc1\ns_add_u32 s47, s48, 0\nbuffer_load_dwordx4 a[64:67], v15, s[12:15], s47 offen sc1\n"
"buffer_load_dwordx4 a[68:71], v15, s[12:15], s47 offen offset:1024 sc1\ns_add_u32 s47, s48, 6144\nbuffer_load_dwordx4 a[88:91], v15, s[12:15], s47 offen sc1\nbuffer_load_dwordx4 a[92:95], v15, s[12:15], s47 offen offset:1024 sc1\n"
"s_add_u32 s47, s48, 4096\nbuffer_load_dwordx4 a[80:83], v15, s[12:15], s47 offen sc1\nbuffer_load_dwordx4 a[84:87], v15, s[12:15], s47 offen offset:1024 sc1\ns_add_u32 s47, s50, 7168\n"
"buffer_load_dwordx4 v[72:75], v11, s[16:19], s50 offen\nbuffer_load_dwordx4 v[76:79], v12, s[16:19], s50 offen\nbuffer_load_dwordx4 v[80:83], v11, s[16:19], s47 offen\nbuffer_load_dwordx4 v[84:87], v12, s[16:19], s47 offen\n"
"s_add_u32 s50, s50, 28672\ns_waitcnt vmcnt(0)\ns_barrier\ns_memrealtime s[54:55]\n"
"s_waitcnt lgkmcnt(0)\ns_add_u32 s44, s54, 16\ns_mov_b32 s45, 8\n.p2align 6\n"
"top_h1:\nv_mov_b32 v56, 0\nv_mov_b32 v57, 0\nv_mov_b32 v58, 0\n"
"v_mov_b32 v59, 0\nv_mov_b32 v60, 0\nv_mov_b32 v61, 0\nv_mov_b32 v62, 0\n"
"v_mov_b32 v63, 0\nv_mov_b32 v64, 0\nv_mov_b32 v65, 0\nv_mov_b32 v66, 0\n"
"v_mov_b32 v67, 0\nv_mov_b32 v128, 0\ns_mul_i32 s48, s45, 6\ns_and_b32 s48, s48, -4\n"
"s_getpc_b64 s[60:61]\nspc_h1:\ns_sub_u32 s48, sgo_h1-spc_h1, s48\ns_add_u32 s60, s60, s48\n"
"s_addc_u32 s61, s61, 0\nsf_h1:\ns_memrealtime s[54:55]\ns_nop 2\n"
"s_memrealtime s[56:57]\ns_nop 2\ns_memrealtime s[58:59]\nsg_h1:\n"
"s_waitcnt lgkmcnt(4)\ns_sub_u32 s47, s54, s44\ns_add_u32 s48, s47, 12\ns_cmp_ge_u32 s48, 12\n"
"s_cbranch_scc1 so_h1\ns_memrealtime s[54:55]\ns_waitcnt lgkmcnt(4)\ns_cmp_ge_u32 s56, s44\n"
"s_cbranch_scc1 sx_h1\ns_memrealtime s[56:57]\ns_waitcnt lgkmcnt(4)\ns_cmp_ge_u32 s58, s44\n"
"s_cbranch_scc1 sx_h1\ns_memrealtime s[58:59]\ns_waitcnt lgkmcnt(4)\ns_cmp_ge_u32 s54, s44\n"
"s_cbranch_scc1 sx_h1\ns_memrealtime s[54:55]\ns_waitcnt lgkmcnt(4)\ns_cmp_ge_u32 s56, s44\n"
"s_cbranch_scc1 sx_h1\ns_memrealtime s[56:57]\ns_waitcnt lgkmcnt(4)\ns_cmp_ge_u32 s58, s44\n"
"s_cbranch_scc1 sx_h1\ns_memrealtime s[58:59]\ns_waitcnt lgkmcnt(4)\ns_cmp_ge_u32 s54, s44\n"
"s_cbranch_scc1 sx_h1\ns_memrealtime s[54:55]\ns_waitcnt lgkmcnt(4)\ns_cmp_ge_u32 s56, s44\n"
"s_cbranch_scc1 sx_h1\ns_memrealtime s[56:57]\ns_waitcnt lgkmcnt(4)\ns_cmp_ge_u32 s58, s44\n"
"s_cbranch_scc1 sx_h1\ns_memrealtime s[58:59]\ns_waitcnt lgkmcnt(4)\ns_cmp_ge_u32 s54, s44\n"
"s_cbranch_scc1 sx_h1\ns_memrealtime s[54:55]\ns_waitcnt lgkmcnt(4)\ns_cmp_ge_u32 s56, s44\n"
"s_cbranch_scc1 sx_h1\ns_memrealtime s[56:57]\ns_waitcnt lgkmcnt(4)\ns_cmp_ge_u32 s58, s44\n"
"s_cbranch_scc1 sx_h1\ns_memrealtime s[58:59]\ns_waitcnt lgkmcnt(4)\ns_cmp_ge_u32 s54, s44\n"
"s_cbranch_scc1 sx_h1\ns_memrealtime s[54:55]\ns_waitcnt lgkmcnt(4)\ns_cmp_ge_u32 s56, s44\n"
"s_cbranch_scc1 sx_h1\ns_memrealtime s[56:57]\ns_waitcnt lgkmcnt(4)\ns_cmp_ge_u32 s58, s44\n"
"s_cbranch_scc1 sx_h1\ns_memrealtime s[58:59]\ns_waitcnt lgkmcnt(4)\ns_cmp_ge_u32 s54, s44\n"
"s_cbranch_scc1 sx_h1\ns_memrealtime s[54:55]\ns_waitcnt lgkmcnt(4)\ns_cmp_ge_u32 s56, s44\n"
"s_cbranch_scc1 sx_h1\ns_memrealtime s[56:57]\ns_branch sx_h1\nso_h1:\n"
"s_waitcnt lgkmcnt(0)\ns_cmp_lt_i32 s47, 0\ns_cbranch_scc1 sf_h1\ns_branch sgo_h1\n"
"sx_h1:\ns_setpc_b64 s[60:61]\ns_nop 0\ns_nop 0\n"
"s_nop 0\ns_nop 0\ns_nop 0\ns_nop 0\n"
"s_nop 0\ns_nop 0\ns_nop 0\ns_nop 0\n"
"s_nop 0\ns_nop 0\ns_nop 0\ns_nop 0\n"
"s_nop 0\ns_nop 0\ns_nop 0\ns_nop 0\n"
"s_nop 0\ns_nop 0\ns_nop 0\ns_nop 0\n"
"s_nop 0\ns_nop 0\nsgo_h1:\ns_nop 4\n"
"buffer_load_dwordx4 v[40:43], v10, s[8:11], s43 offen offset:1024 sc1\ns_nop 4\nbuffer_load_dwordx4 v[44:47], v10, s[8:11], s43 offen offset:0 sc1\ns_nop 13\n"
"buffer_load_dwordx4 v[48:51], v10, s[8:11], s43 offen offset:3072 sc1\ns_nop 13\nbuffer_load_dwordx4 v[52:55], v10, s[8:11], s43 offen offset:2048 sc1\nlanded_h1:\n"
"s_waitcnt vmcnt(3)\nv_smfmac_f32_16x16x64_bf16 v[56:59], v[40:43], a[8:15], v8\nv_max3_u32 v128, v128, v40, v41\nv_smfmac_f32_16x16x64_bf16 v[60:63], v[40:43], a[40:47], v8\n"
"v_max3_u32 v128, v128, v42, v43\nv_smfmac_f32_16x16x64_bf16 v[64:67], v[40:43], a[72:79], v8\ns_waitcnt vmcnt(2)\nv_smfmac_f32_16x16x64_bf16 v[56:59], v[44:47], a[0:7], v8\n"
"v_max3_u32 v128, v128, v44, v45\nv_smfmac_f32_16x16x64_bf16 v[60:63], v[44:47], a[32:39], v8\nv_max3_u32 v128, v128, v46, v47\nv_smfmac_f32_16x16x64_bf16 v[64:67], v[44:47], a[64:71], v8\n"
"s_waitcnt vmcnt(1)\nv_smfmac_f32_16x16x64_bf16 v[56:59], v[48:51], a[24:31], v8\nv_max3_u32 v128, v128, v48, v49\nv_smfmac_f32_16x16x64_bf16 v[60:63], v[48:51], a[56:63], v8\n"
"v_max3_u32 v128, v128, v50, v51\nv_smfmac_f32_16x16x64_bf16 v[64:67], v[48:51], a[88:95], v8\ns_waitcnt vmcnt(0)\nv_smfmac_f32_16x16x64_bf16 v[56:59], v[52:55], a[16:23], v8\n"
"v_max3_u32 v128, v128, v52, v53\nv_smfmac_f32_16x16x64_bf16 v[60:63], v[52:55], a[48:55], v8\nv_max3_u32 v128, v128, v54, v55\nv_smfmac_f32_16x16x64_bf16 v[64:67], v[52:55], a[80:87], v8\n"
"v_add_f32 v129, v56, v57\nv_add_f32 v130, v58, v59\nv_cvt_pk_bf16_f32 v112, v129, v130\nv_add_f32 v129, v60, v61\n"
"v_add_f32 v130, v62, v63\nv_cvt_pk_bf16_f32 v113, v129, v130\nv_cmp_eq_u32 vcc, -1, v128\nv_add_f32 v129, v64, v65\n"
"v_add_f32 v130, v66, v67\nv_cvt_pk_bf16_f32 v114, v129, v130\nds_write2_b64 v131, v[112:113], v[114:115] offset0:32 offset1:48\ns_cbranch_vccnz retry_h1\n"
"v_mov_b32 v129, 0\ns_nop 1\nv_mov_b32 v129, 0\ns_nop 1\n"
"s_waitcnt lgkmcnt(0)\ns_barrier\ns_nop 9\nds_read_b64_tr_b16 v[88:89], v132 offset:0\n"
"ds_read_b64_tr_b16 v[90:91], v132 offset:32\nds_read_b64_tr_b16 v[92:93], v132 offset:256\nds_read_b64_tr_b16 v[94:95], v132 offset:288\nds_read_b64_tr_b16 v[96:97], v132 offset:512\n"
"ds_read_b64_tr_b16 v[98:99], v132 offset:544\nds_read_b64_tr_b16 v[100:101], v132 offset:768\nds_read_b64_tr_b16 v[102:103], v132 offset:800\ns_waitcnt lgkmcnt(6)\n"
"v_mfma_f32_16x16x32_bf16 v[104:107], v[88:91], v[72:75], 0\nv_mfma_f32_16x16x32_bf16 v[108:111], v[88:91], v[80:83], 0\ns_waitcnt lgkmcnt(4)\nv_mfma_f32_16x16x32_bf16 v[104:107], v[92:95], v[76:79], v[104:107]\n"
"v_mfma_f32_16x16x32_bf16 v[108:111], v[92:95], v[84:87], v[108:111]\ns_waitcnt lgkmcnt(2)\nv_mfma_f32_16x16x32_bf16 v[104:107], v[96:99], v[72:75], v[104:107]\nv_mfma_f32_16x16x32_bf16 v[108:111], v[96:99], v[80:83], v[108:111]\n"
"s_waitcnt lgkmcnt(0)\nv_mfma_f32_16x16x32_bf16 v[104:107], v[100:103], v[76:79], v[104:107]\nv_mfma_f32_16x16x32_bf16 v[108:111], v[100:103], v[84:87], v[108:111]\ns_nop 7\n"
"v_add_f32_dpp v124, v106, v104 row_ror:8 row_mask:0xf bank_mask:0x3\nv_add_f32_dpp v124, v108, v110 row_ror:8 row_mask:0xf bank_mask:0xc\nv_add_f32_dpp v125, v107, v105 row_ror:8 row_mask:0xf bank_mask:0x3\nv_add_f32_dpp v125, v109, v111 row_ror:8 row_mask:0xf bank_mask:0xc\n"
"s_add_u32 s47, s50, 7168\nbuffer_load_dwordx4 v[72:75], v11, s[16:19], s50 offen\nbuffer_load_dwordx4 v[76:79], v12, s[16:19], s50 offen\nbuffer_load_dwordx4 v[80:83], v11, s[16:19], s47 offen\n"
"buffer_load_dwordx4 v[84:87], v12, s[16:19], s47 offen\ns_add_u32 s50, s50, 28672\nbuffer_store_dwordx2 v[124:125], v16, s[20:23], s51 offen sc1\ns_add_u32 s51, s51, 1024\n"
"ds_read_b32 v133, v134\nv_xor_b32 v131, 8192, v131\nv_xor_b32 v132, 8192, v132\ns_waitcnt lgkmcnt(0)\n"
"v_readfirstlane_b32 s47, v133\ns_sub_u32 s47, s47, s40\ns_cmp_eq_u32 s47, 1\ns_cbranch_scc1 adopt_h1\n"
"back_h1:\ns_mul_i32 s48, s46, 8\ns_add_u32 s45, s45, s48\ns_add_u32 s45, s45, s32\n"
"s_lshr_b32 s48, s45, 4\ns_lshl_b32 s48, s48, 2\ns_add_u32 s44, s44, s48\ns_and_b32 s45, s45, 15\n"
"s_add_u32 s52, s40, 1\ns_bitcmp1_b64 s[28:29], s52\ns_subb_u32 s46, s46, 0\ns_add_u32 s40, s40, 1\n"
"s_mov_b32 s43, s41\ns_add_u32 s41, s41, 131072\ns_and_b32 s41, s41, 524287\ns_add_u32 s92, s92, 1\n"
"s_xor_b32 s42, s41, 262144\ns_waitcnt lgkmcnt(0)\ns_cmp_lt_u32 s40, 64\ns_cbranch_scc1 top_h1\n"
"s_add_u32 s47, s34, 64\nv_mov_b32 v135, s47\ns_waitcnt vmcnt(0)\nbuffer_store_dword v135, v18, s[8:11], 0 offen sc1\n"
"s_branch end_h1\nretry2_h1:\ns_sleep 3\nretry_h1:\n"
"s_add_u32 s53, s53, 1\ns_add_u32 s49, s49, 1\ns_cmp_gt_u32 s49, 65536\ns_cbranch_scc1 end_h1\n"
"buffer_load_dwordx4 v[40:43], v10, s[8:11], s43 offen offset:1024 sc1\nbuffer_load_dwordx4 v[44:47], v10, s[8:11], s43 offen offset:0 sc1\nbuffer_load_dwordx4 v[48:51], v10, s[8:11], s43 offen offset:3072 sc1\nbuffer_load_dwordx4 v[52:55], v10, s[8:11], s43 offen offset:2048 sc1\n"
"v_mov_b32 v128, 0\ns_add_u32 s47, s40, 1\nv_mov_b32 v133, s47\nds_write_b32 v134, v133\n"
"s_waitcnt vmcnt(0)\nv_max3_u32 v128, v128, v40, v41\nv_max3_u32 v128, v128, v42, v43\nv_max3_u32 v128, v128, v44, v45\n"
"v_max3_u32 v128, v128, v46, v47\nv_max3_u32 v128, v128, v48, v49\nv_max3_u32 v128, v128, v50, v51\nv_max3_u32 v128, v128, v52, v53\n"
"v_max3_u32 v128, v128, v54, v55\nv_cmp_eq_u32 vcc, -1, v128\nv_mov_b32 v56, 0\nv_mov_b32 v57, 0\n"
"v_mov_b32 v58, 0\nv_mov_b32 v59, 0\nv_mov_b32 v60, 0\nv_mov_b32 v61, 0\n"
"v_mov_b32 v62, 0\nv_mov_b32 v63, 0\nv_mov_b32 v64, 0\nv_mov_b32 v65, 0\n"
"v_mov_b32 v66, 0\nv_mov_b32 v67, 0\ns_cbranch_vccnz retry2_h1\ns_branch landed_h1\n"
"adopt_h1:\nbuffer_load_dword v116, v28, s[8:11], 0 offen sc1\ns_lshl_b32 s52, s40, 26\ns_or_b32 s47, s52, 0x3ffffff\n"
"s_waitcnt vmcnt(0)\nv_cmp_ge_u32 vcc, s47, v116\ns_nop 1\nv_cndmask_b32 v133, 0, v116, vcc\n"
"s_nop 1\nv_max_u32_dpp v133, v133, v133 row_ror:8 row_mask:0xf bank_mask:0xf\ns_nop 1\nv_max_u32_dpp v133, v133, v133 row_ror:4 row_mask:0xf bank_mask:0xf\n"
"s_nop 1\nv_max_u32_dpp v133, v133, v133 row_ror:2 row_mask:0xf bank_mask:0xf\ns_nop 1\nv_max_u32_dpp v133, v133, v133 row_ror:1 row_mask:0xf bank_mask:0xf\n"
"s_nop 0\nv_readlane_b32 s47, v133, 0\nv_readlane_b32 s48, v133, 16\ns_max_u32 s48, s48, s47\n"
"s_cmp_le_u32 s48, s52\ns_cbranch_scc1 back_h1\ns_bfe_u32 s52, s48, 1179656\ns_add_u32 s52, s52, s30\n"
"s_sub_u32 s45, s52, s44\ns_and_b32 s45, s45, 3\ns_sub_u32 s44, s52, s45\ns_lshl_b32 s45, s45, 2\n"
"s_and_b32 s46, s48, 255\ns_mov_b32 s92, 0\ns_branch back_h1\nend_h1:\n"
"s_branch fk_done\nfk_h2:\ns_mov_b32 s40, 0\ns_mov_b32 s41, 0\n"
"s_mov_b32 s42, 262144\ns_mov_b32 s43, 393216\ns_mov_b32 s44, s30\ns_mov_b32 s45, 0\n"
"s_bcnt1_i32_b64 s46, s[28:29]\ns_mov_b32 s92, 99\ns_mov_b32 s49, 0\ns_mov_b32 s53, 0\n"
"s_mov_b32 s50, s36\ns_mov_b32 s51, s37\nv_mov_b32 v127, v13\nv_mov_b32 v128, v14\n"
"v_mov_b32 v130, 24576\nv_mov_b32 v111, 0\nv_mov_b32 v116, -1\nv_mov_b32 v117, -1\n"
"v_mov_b32 v118, -1\nv_mov_b32 v119, -1\ns_add_u32 s47, s35, 0\ns_and_b32 s52, s47, 1\n"
"s_mul_i32 s52, s52, -14208\ns_mul_i32 s48, s47, 14336\ns_add_u32 s48, s48, s52\ns_lshr_b32 s52, s47, 3\n"
"s_mul_i32 s52, s52, 8192\ns_add_u32 s48, s48, s52\ns_add_u32 s47, s48, 8192\nbuffer_load_dwordx4 a[0:3], v15, s[12:15], s47 offen sc1\n"
"buffer_load_dwordx4 a[4:7], v15, s[12:15], s47 offen offset:1024 sc1\ns_add_u32 s47, s48, 10240\nbuffer_load_dwordx4 a[8:11], v15, s[12:15], s47 offen sc1\nbuffer_load_dwordx4 a[12:15], v15, s[12:15], s47 offen offset:1024 sc1\n"
"s_add_u32 s47, s48, 12288\nbuffer_load_dwordx4 a[16:19], v15, s[12:15], s47 offen sc1\nbuffer_load_dwordx4 a[20:23], v15, s[12:15], s47 offen offset:1024 sc1\ns_add_u32 s47, s35, 1\n"
"s_and_b32 s52, s47, 1\ns_mul_i32 s52, s52, -14208\ns_mul_i32 s48, s47, 14336\ns_add_u32 s48, s48, s52\n"
"s_lshr_b32 s52, s47, 3\ns_mul_i32 s52, s52, 8192\ns_add_u32 s48, s48, s52\ns_add_u32 s47, s48, 8192\n"
"buffer_load_dwordx4 a[32:35], v15, s[12:15], s47 offen sc1\nbuffer_load_dwordx4 a[36:39], v15, s[12:15], s47 offen offset:1024 sc1\ns_add_u32 s47, s48, 10240\nbuffer_load_dwordx4 a[40:43], v15, s[12:15], s47 offen sc1\n"
"buffer_load_dwordx4 a[44:47], v15, s[12:15], s47 offen offset:1024 sc1\ns_add_u32 s47, s48, 12288\nbuffer_load_dwordx4 a[48:51], v15, s[12:15], s47 offen sc1\nbuffer_load_dwordx4 a[52:55], v15, s[12:15], s47 offen offset:1024 sc1\n"
"s_add_u32 s47, s35, 2\ns_and_b32 s52, s47, 1\ns_mul_i32 s52, s52, -14208\ns_mul_i32 s48, s47, 14336\n"
"s_add_u32 s48, s48, s52\ns_lshr_b32 s52, s47, 3\ns_mul_i32 s52, s52, 8192\ns_add_u32 s48, s48, s52\n"
"s_add_u32 s47, s48, 8192\nbuffer_load_dwordx4 a[64:67], v15, s[12:15], s47 offen sc1\nbuffer_load_dwordx4 a[68:71], v15, s[12:15], s47 offen offset:1024 sc1\ns_add_u32 s47, s48, 10240\n"
"buffer_load_dwordx4 a[72:75], v15, s[12:15], s47 offen sc1\nbuffer_load_dwordx4 a[76:79], v15, s[12:15], s47 offen offset:1024 sc1\ns_add_u32 s47, s48, 12288\nbuffer_load_dwordx4 a[80:83], v15, s[12:15], s47 offen sc1\n"
"buffer_load_dwordx4 a[84:87], v15, s[12:15], s47 offen offset:1024 sc1\ns_add_u32 s47, s35, 3\ns_and_b32 s52, s47, 1\ns_mul_i32 s52, s52, -14208\n"
"s_mul_i32 s48, s47, 14336\ns_add_u32 s48, s48, s52\ns_lshr_b32 s52, s47, 3\ns_mul_i32 s52, s52, 8192\n"
"s_add_u32 s48, s48, s52\ns_add_u32 s47, s48, 8192\nbuffer_load_dwordx4 a[96:99], v15, s[12:15], s47 offen sc1\nbuffer_load_dwordx4 a[100:103], v15, s[12:15], s47 offen offset:1024 sc1\n"
"s_add_u32 s47, s48, 10240\nbuffer_load_dwordx4 a[104:107], v15, s[12:15], s47 offen sc1\nbuffer_load_dwordx4 a[108:111], v15, s[12:15], s47 offen offset:1024 sc1\ns_add_u32 s47, s48, 12288\n"
"buffer_load_dwordx4 a[112:115], v15, s[12:15], s47 offen sc1\nbuffer_load_dwordx4 a[116:119], v15, s[12:15], s47 offen offset:1024 sc1\ns_waitcnt vmcnt(0)\ns_barrier\n"
"s_memrealtime s[54:55]\ns_waitcnt lgkmcnt(0)\ns_add_u32 s44, s54, 16\ns_mov_b32 s45, 8\n"
".p2align 6\ntop_h2:\nv_mov_b32 v52, 0\nv_mov_b32 v53, 0\n"
"v_mov_b32 v54, 0\nv_mov_b32 v55, 0\nv_mov_b32 v56, 0\nv_mov_b32 v57, 0\n"
"v_mov_b32 v58, 0\nv_mov_b32 v59, 0\nv_mov_b32 v60, 0\nv_mov_b32 v61, 0\n"
"v_mov_b32 v62, 0\nv_mov_b32 v63, 0\nv_mov_b32 v64, 0\nv_mov_b32 v65, 0\n"
"v_mov_b32 v66, 0\nv_mov_b32 v67, 0\nv_mov_b32 v124, 0\ns_mul_i32 s48, s45, 6\n"
"s_and_b32 s48, s48, -4\ns_getpc_b64 s[60:61]\nspc_h2:\ns_sub_u32 s48, sgo_h2-spc_h2, s48\n"
"s_add_u32 s60, s60, s48\ns_addc_u32 s61, s61, 0\nsf_h2:\ns_memrealtime s[54:55]\n"
"s_nop 2\ns_memrealtime s[56:57]\ns_nop 2\ns_memrealtime s[58:59]\n"
"sg_h2:\ns_waitcnt lgkmcnt(4)\ns_sub_u32 s47, s54, s44\ns_add_u32 s48, s47, 12\n"
"s_cmp_ge_u32 s48, 12\ns_cbranch_scc1 so_h2\ns_memrealtime s[54:55]\ns_waitcnt lgkmcnt(4)\n"
"s_cmp_ge_u32 s56, s44\ns_cbranch_scc1 sx_h2\ns_memrealtime s[56:57]\ns_waitcnt lgkmcnt(4)\n"
"s_cmp_ge_u32 s58, s44\ns_cbranch_scc1 sx_h2\ns_memrealtime s[58:59]\ns_waitcnt lgkmcnt(4)\n"
"s_cmp_ge_u32 s54, s44\ns_cbranch_scc1 sx_h2\ns_memrealtime s[54:55]\ns_waitcnt lgkmcnt(4)\n"
"s_cmp_ge_u32 s56, s44\ns_cbranch_scc1 sx_h2\ns_memrealtime s[56:57]\ns_waitcnt lgkmcnt(4)\n"
"s_cmp_ge_u32 s58, s44\ns_cbranch_scc1 sx_h2\ns_memrealtime s[58:59]\ns_waitcnt lgkmcnt(4)\n"
"s_cmp_ge_u32 s54, s44\ns_cbranch_scc1 sx_h2\ns_memrealtime s[54:55]\ns_waitcnt lgkmcnt(4)\n"
"s_cmp_ge_u32 s56, s44\ns_cbranch_scc1 sx_h2\ns_memrealtime s[56:57]\ns_waitcnt lgkmcnt(4)\n"
"s_cmp_ge_u32 s58, s44\ns_cbranch_scc1 sx_h2\ns_memrealtime s[58:59]\ns_waitcnt lgkmcnt(4)\n"
"s_cmp_ge_u32 s54, s44\ns_cbranch_scc1 sx_h2\ns_memrealtime s[54:55]\ns_waitcnt lgkmcnt(4)\n"
"s_cmp_ge_u32 s56, s44\ns_cbranch_scc1 sx_h2\ns_memrealtime s[56:57]\ns_waitcnt lgkmcnt(4)\n"
"s_cmp_ge_u32 s58, s44\ns_cbranch_scc1 sx_h2\ns_memrealtime s[58:59]\ns_waitcnt lgkmcnt(4)\n"
"s_cmp_ge_u32 s54, s44\ns_cbranch_scc1 sx_h2\ns_memrealtime s[54:55]\ns_waitcnt lgkmcnt(4)\n"
"s_cmp_ge_u32 s56, s44\ns_cbranch_scc1 sx_h2\ns_memrealtime s[56:57]\ns_waitcnt lgkmcnt(4)\n"
"s_cmp_ge_u32 s58, s44\ns_cbranch_scc1 sx_h2\ns_memrealtime s[58:59]\ns_waitcnt lgkmcnt(4)\n"
"s_cmp_ge_u32 s54, s44\ns_cbranch_scc1 sx_h2\ns_memrealtime s[54:55]\ns_waitcnt lgkmcnt(4)\n"
"s_cmp_ge_u32 s56, s44\ns_cbranch_scc1 sx_h2\ns_memrealtime s[56:57]\ns_branch sx_h2\n"
"so_h2:\ns_waitcnt lgkmcnt(0)\ns_cmp_lt_i32 s47, 0\ns_cbranch_scc1 sf_h2\n"
"s_branch sgo_h2\nsx_h2:\ns_setpc_b64 s[60:61]\ns_nop 0\n"
"s_nop 0\ns_nop 0\ns_nop 0\ns_nop 0\n"
"s_nop 0\ns_nop 0\ns_nop 0\ns_nop 0\n"
"s_nop 0\ns_nop 0\ns_nop 0\ns_nop 0\n"
"s_nop 0\ns_nop 0\ns_nop 0\ns_nop 0\n"
"s_nop 0\ns_nop 0\ns_nop 0\ns_nop 0\n"
"s_nop 0\ns_nop 0\ns_nop 0\nsgo_h2:\n"
"s_nop 8\nbuffer_load_dwordx4 v[40:43], v10, s[8:11], s43 offen offset:0 sc1\ns_nop 13\nbuffer_load_dwordx4 v[44:47], v10, s[8:11], s43 offen offset:1024 sc1\n"
"s_nop 13\nbuffer_load_dwordx4 v[48:51], v10, s[8:11], s43 offen offset:2048 sc1\nlanded_h2:\ns_waitcnt vmcnt(2)\n"
"v_smfmac_f32_16x16x64_bf16 v[52:55], v[40:43], a[0:7], v8\nv_max3_u32 v124, v124, v40, v41\nv_smfmac_f32_16x16x64_bf16 v[56:59], v[40:43], a[32:39], v8\nv_max3_u32 v124, v124, v42, v43\n"
"v_smfmac_f32_16x16x64_bf16 v[60:63], v[40:43], a[64:71], v8\nv_smfmac_f32_16x16x64_bf16 v[64:67], v[40:43], a[96:103], v8\ns_waitcnt vmcnt(1)\nv_smfmac_f32_16x16x64_bf16 v[52:55], v[44:47], a[8:15], v8\n"
"v_max3_u32 v124, v124, v44, v45\nv_smfmac_f32_16x16x64_bf16 v[56:59], v[44:47], a[40:47], v8\nv_max3_u32 v124, v124, v46, v47\nv_smfmac_f32_16x16x64_bf16 v[60:63], v[44:47], a[72:79], v8\n"
"v_smfmac_f32_16x16x64_bf16 v[64:67], v[44:47], a[104:111], v8\ns_waitcnt vmcnt(0)\nv_smfmac_f32_16x16x64_bf16 v[52:55], v[48:51], a[16:23], v8\nv_max3_u32 v124, v124, v48, v49\n"
"v_smfmac_f32_16x16x64_bf16 v[56:59], v[48:51], a[48:55], v8\nv_max3_u32 v124, v124, v50, v51\nv_smfmac_f32_16x16x64_bf16 v[60:63], v[48:51], a[80:87], v8\nv_cmp_eq_u32 vcc, -1, v124\n"
"v_smfmac_f32_16x16x64_bf16 v[64:67], v[48:51], a[112:119], v8\nv_add_f32 v125, v52, v53\nv_add_f32 v126, v54, v55\nv_cvt_pk_bf16_f32 v108, v125, v126\n"
"v_add_f32 v125, v56, v57\nv_add_f32 v126, v58, v59\nv_cvt_pk_bf16_f32 v109, v125, v126\nv_add_f32 v125, v60, v61\n"
"v_add_f32 v126, v62, v63\nv_cvt_pk_bf16_f32 v110, v125, v126\nv_add_f32 v125, v64, v65\nv_add_f32 v126, v66, v67\n"
"v_cvt_pk_bf16_f32 v111, v125, v126\nds_write2_b64 v127, v[108:109], v[110:111] offset0:0 offset1:16\ns_cbranch_vccnz retry_h2\nv_mov_b32 v125, 0\n"
"s_nop 1\nv_mov_b32 v125, 0\ns_nop 1\ns_waitcnt lgkmcnt(0)\n"
"s_barrier\nv_mov_b32 v125, 0\ns_nop 1\nv_mov_b32 v125, 0\n"
"s_nop 1\nv_mov_b32 v125, 0\ns_nop 1\nv_mov_b32 v125, 0\n"
"s_nop 1\nv_mov_b32 v125, 0\ns_nop 1\nv_mov_b32 v125, 0\n"
"s_nop 1\nv_mov_b32 v125, 0\ns_nop 1\nv_mov_b32 v125, 0\n"
"s_nop 1\nv_mov_b32 v125, 0\ns_nop 1\nv_mov_b32 v125, 0\n"
"s_nop 1\nv_mov_b32 v125, 0\ns_nop 1\nv_mov_b32 v125, 0\n"
"s_nop 1\nv_mov_b32 v125, 0\ns_nop 1\nv_mov_b32 v125, 0\n"
"s_nop 1\nv_mov_b32 v125, 0\ns_nop 1\nv_mov_b32 v125, 0\n"
"s_nop 1\nv_mov_b32 v125, 0\ns_nop 1\nv_mov_b32 v125, 0\n"
"s_nop 1\nv_mov_b32 v125, 0\ns_nop 1\nv_mov_b32 v125, 0\n"
"s_nop 1\nv_mov_b32 v125, 0\ns_nop 1\nv_mov_b32 v125, 0\n"
"buffer_store_dwordx4 v[116:119], v17, s[8:11], s42 offen sc1\nds_read_b32 v129, v130\nv_xor_b32 v127, 8192, v127\nv_xor_b32 v128, 8192, v128\n"
"s_waitcnt lgkmcnt(0)\nv_readfirstlane_b32 s47, v129\ns_sub_u32 s47, s47, s40\ns_cmp_eq_u32 s47, 1\n"
"s_cbranch_scc1 adopt_h2\nback_h2:\ns_mul_i32 s48, s46, 8\ns_add_u32 s45, s45, s48\n"
"s_add_u32 s45, s45, s32\ns_lshr_b32 s48, s45, 4\ns_lshl_b32 s48, s48, 2\ns_add_u32 s44, s44, s48\n"
"s_and_b32 s45, s45, 15\ns_add_u32 s52, s40, 1\ns_bitcmp1_b64 s[28:29], s52\ns_subb_u32 s46, s46, 0\n"
"s_add_u32 s40, s40, 1\ns_mov_b32 s43, s41\ns_add_u32 s41, s41, 131072\ns_and_b32 s41, s41, 524287\n"
"s_add_u32 s92, s92, 1\ns_xor_b32 s42, s41, 262144\ns_waitcnt lgkmcnt(0)\ns_cmp_lt_u32 s40, 64\n"
"s_cbranch_scc1 top_h2\ns_branch end_h2\nretry2_h2:\ns_sleep 3\n"
"retry_h2:\ns_add_u32 s53, s53, 1\ns_add_u32 s49, s49, 1\ns_cmp_gt_u32 s49, 65536\n"
"s_cbranch_scc1 end_h2\nbuffer_load_dwordx4 v[40:43], v10, s[8:11], s43 offen offset:0 sc1\nbuffer_load_dwordx4 v[44:47], v10, s[8:11], s43 offen offset:1024 sc1\nbuffer_load_dwordx4 v[48:51], v10, s[8:11], s43 offen offset:2048 sc1\n"
"v_mov_b32 v124, 0\ns_add_u32 s47, s40, 1\nv_mov_b32 v129, s47\nds_write_b32 v130, v129\n"
"s_waitcnt vmcnt(0)\nv_max3_u32 v124, v124, v40, v41\nv_max3_u32 v124, v124, v42, v43\nv_max3_u32 v124, v124, v44, v45\n"
"v_max3_u32 v124, v124, v46, v47\nv_max3_u32 v124, v124, v48, v49\nv_max3_u32 v124, v124, v50, v51\nv_cmp_eq_u32 vcc, -1, v124\n"
"v_mov_b32 v52, 0\nv_mov_b32 v53, 0\nv_mov_b32 v54, 0\nv_mov_b32 v55, 0\n"
"v_mov_b32 v56, 0\nv_mov_b32 v57, 0\nv_mov_b32 v58, 0\nv_mov_b32 v59, 0\n"
"v_mov_b32 v60, 0\nv_mov_b32 v61, 0\nv_mov_b32 v62, 0\nv_mov_b32 v63, 0\n"
"v_mov_b32 v64, 0\nv_mov_b32 v65, 0\nv_mov_b32 v66, 0\nv_mov_b32 v67, 0\n"
"s_cbranch_vccnz retry2_h2\ns_branch landed_h2\nadopt_h2:\nbuffer_load_dword v112, v28, s[8:11], 0 offen sc1\n"
"s_lshl_b32 s52, s40, 26\ns_or_b32 s47, s52, 0x3ffffff\ns_waitcnt vmcnt(0)\nv_cmp_ge_u32 vcc, s47, v112\n"
"s_nop 1\nv_cndmask_b32 v129, 0, v112, vcc\ns_nop 1\nv_max_u32_dpp v129, v129, v129 row_ror:8 row_mask:0xf bank_mask:0xf\n"
"s_nop 1\nv_max_u32_dpp v129, v129, v129 row_ror:4 row_mask:0xf bank_mask:0xf\ns_nop 1\nv_max_u32_dpp v129, v129, v129 row_ror:2 row_mask:0xf bank_mask:0xf\n"
"s_nop 1\nv_max_u32_dpp v129, v129, v129 row_ror:1 row_mask:0xf bank_mask:0xf\ns_nop 0\nv_readlane_b32 s47, v129, 0\n"
"v_readlane_b32 s48, v129, 16\ns_max_u32 s48, s48, s47\ns_cmp_le_u32 s48, s52\ns_cbranch_scc1 back_h2\n"
"s_bfe_u32 s52, s48, 1179656\ns_add_u32 s52, s52, s30\ns_sub_u32 s45, s52, s44\ns_and_b32 s45, s45, 3\n"
"s_sub_u32 s44, s52, s45\ns_lshl_b32 s45, s45, 2\ns_and_b32 s46, s48, 255\ns_mov_b32 s92, 0\n"
"s_branch back_h2\nend_h2:\ns_branch fk_done\nfk_h3:\n"
"s_mov_b32 s40, 0\ns_mov_b32 s41, 0\ns_mov_b32 s42, 262144\ns_mov_b32 s43, 393216\n"
"s_mov_b32 s44, s30\ns_mov_b32 s45, 0\ns_bcnt1_i32_b64 s46, s[28:29]\ns_mov_b32 s92, 99\n"
"s_mov_b32 s49, 0\ns_mov_b32 s53, 0\ns_mov_b32 s50, s36\ns_mov_b32 s51, s37\n"
"v_mov_b32 v127, v13\nv_mov_b32 v128, v14\nv_mov_b32 v130, 24576\nv_mov_b32 v111, 0\n"
"v_mov_b32 v116, -1\nv_mov_b32 v117, -1\nv_mov_b32 v118, -1\nv_mov_b32 v119, -1\n"
"s_add_u32 s47, s35, 4\ns_and_b32 s52, s47, 1\ns_mul_i32 s52, s52, -14208\ns_mul_i32 s48, s47, 14336\n"
"s_add_u32 s48, s48, s52\ns_lshr_b32 s52, s47, 3\ns_mul_i32 s52, s52, 8192\ns_add_u32 s48, s48, s52\n"
"s_add_u32 s47, s48, 10240\nbuffer_load_dwordx4 a[8:11], v15, s[12:15], s47 offen sc1\nbuffer_load_dwordx4 a[12:15], v15, s[12:15], s47 offen offset:1024 sc1\ns_add_u32 s47, s48, 8192\n"
"buffer_load_dwordx4 a[0:3], v15, s[12:15], s47 offen sc1\nbuffer_load_dwordx4 a[4:7], v15, s[12:15], s47 offen offset:1024 sc1\ns_add_u32 s47, s48, 12288\nbuffer_load_dwordx4 a[16:19], v15, s[12:15], s47 offen sc1\n"
"buffer_load_dwordx4 a[20:23], v15, s[12:15], s47 offen offset:1024 sc1\ns_add_u32 s47, s35, 5\ns_and_b32 s52, s47, 1\ns_mul_i32 s52, s52, -14208\n"
"s_mul_i32 s48, s47, 14336\ns_add_u32 s48, s48, s52\ns_lshr_b32 s52, s47, 3\ns_mul_i32 s52, s52, 8192\n"
"s_add_u32 s48, s48, s52\ns_add_u32 s47, s48, 10240\nbuffer_load_dwordx4 a[40:43], v15, s[12:15], s47 offen sc1\nbuffer_load_dwordx4 a[44:47], v15, s[12:15], s47 offen offset:1024 sc1\n"
"s_add_u32 s47, s48, 8192\nbuffer_load_dwordx4 a[32:35], v15, s[12:15], s47 offen sc1\nbuffer_load_dwordx4 a[36:39], v15, s[12:15], s47 offen offset:1024 sc1\ns_add_u32 s47, s48, 12288\n"
"buffer_load_dwordx4 a[48:51], v15, s[12:15], s47 offen sc1\nbuffer_load_dwordx4 a[52:55], v15, s[12:15], s47 offen offset:1024 sc1\ns_add_u32 s47, s35, 6\ns_and_b32 s52, s47, 1\n"
"s_mul_i32 s52, s52, -14208\ns_mul_i32 s48, s47, 14336\ns_add_u32 s48, s48, s52\ns_lshr_b32 s52, s47, 3\n"
"s_mul_i32 s52, s52, 8192\ns_add_u32 s48, s48, s52\ns_add_u32 s47, s48, 10240\nbuffer_load_dwordx4 a[72:75], v15, s[12:15], s47 offen sc1\n"
"buffer_load_dwordx4 a[76:79], v15, s[12:15], s47 offen offset:1024 sc1\ns_add_u32 s47, s48, 8192\nbuffer_load_dwordx4 a[64:67], v15, s[12:15], s47 offen sc1\nbuffer_load_dwordx4 a[68:71], v15, s[12:15], s47 offen offset:1024 sc1\n"
"s_add_u32 s47, s48, 12288\nbuffer_load_dwordx4 a[80:83], v15, s[12:15], s47 offen sc1\nbuffer_load_dwordx4 a[84:87], v15, s[12:15], s47 offen offset:1024 sc1\nv_lshlrev_b32 v114, 2, v9\n"
"s_mov_b32 s52, 1064960\nbuffer_load_dword v112, v114, s[8:11], s52 offen sc1\ns_add_u32 s52, s52, 4096\nbuffer_load_dword v113, v114, s[8:11], s52 offen sc1\n"
"s_waitcnt vmcnt(0)\nar_h3:\nv_xor_b32 v112, s34, v112\nv_xor_b32 v113, s34, v113\n"
"v_or_b32 v112, v112, v113\nv_cmp_ne_u32 vcc, 0, v112\ns_cbranch_vccz arok_h3\ns_add_u32 s53, s53, 0\n"
"s_add_u32 s49, s49, 1\ns_cmp_gt_u32 s49, 65536\ns_cbranch_scc1 end_h3\ns_sleep 2\n"
"v_lshlrev_b32 v114, 2, v9\ns_mov_b32 s52, 1064960\nbuffer_load_dword v112, v114, s[8:11], s52 offen sc1\ns_add_u32 s52, s52, 4096\n"
"buffer_load_dword v113, v114, s[8:11], s52 offen sc1\ns_waitcnt vmcnt(0)\ns_branch ar_h3\narok_h3:\n"
"s_barrier\ns_memrealtime s[54:55]\ns_waitcnt lgkmcnt(0)\ns_add_u32 s44, s54, 16\n"
"s_mov_b32 s45, 8\n.p2align 6\ntop_h3:\nv_mov_b32 v52, 0\n"
"v_mov_b32 v53, 0\nv_mov_b32 v54, 0\nv_mov_b32 v55, 0\nv_mov_b32 v56, 0\n"
"v_mov_b32 v57, 0\nv_mov_b32 v58, 0\nv_mov_b32 v59, 0\nv_mov_b32 v60, 0\n"
"v_mov_b32 v61, 0\nv_mov_b32 v62, 0\nv_mov_b32 v63, 0\nv_mov_b32 v124, 0\n"
"s_mul_i32 s48, s45, 6\ns_and_b32 s48, s48, -4\ns_getpc_b64 s[60:61]\nspc_h3:\n"
"s_sub_u32 s48, sgo_h3-spc_h3, s48\ns_add_u32 s60, s60, s48\ns_addc_u32 s61, s61, 0\nsf_h3:\n"
"s_memrealtime s[54:55]\ns_nop 2\ns_memrealtime s[56:57]\ns_nop 2\n"
"s_memrealtime s[58:59]\nsg_h3:\ns_waitcnt lgkmcnt(4)\ns_sub_u32 s47, s54, s44\n"
"s_add_u32 s48, s47, 12\ns_cmp_ge_u32 s48, 12\ns_cbranch_scc1 so_h3\ns_memrealtime s[54:55]\n"
"s_waitcnt lgkmcnt(4)\ns_cmp_ge_u32 s56, s44\ns_cbranch_scc1 sx_h3\ns_memrealtime s[56:57]\n"
"s_waitcnt lgkmcnt(4)\ns_cmp_ge_u32 s58, s44\ns_cbranch_scc1 sx_h3\ns_memrealtime s[58:59]\n"
"s_waitcnt lgkmcnt(4)\ns_cmp_ge_u32 s54, s44\ns_cbranch_scc1 sx_h3\ns_memrealtime s[54:55]\n"
"s_waitcnt lgkmcnt(4)\ns_cmp_ge_u32 s56, s44\ns_cbranch_scc1 sx_h3\ns_memrealtime s[56:57]\n"
"s_waitcnt lgkmcnt(4)\ns_cmp_ge_u32 s58, s44\ns_cbranch_scc1 sx_h3\ns_memrealtime s[58:59]\n"
"s_waitcnt lgkmcnt(4)\ns_cmp_ge_u32 s54, s44\ns_cbranch_scc1 sx_h3\ns_memrealtime s[54:55]\n"
"s_waitcnt lgkmcnt(4)\ns_cmp_ge_u32 s56, s44\ns_cbranch_scc1 sx_h3\ns_memrealtime s[56:57]\n"
"s_waitcnt lgkmcnt(4)\ns_cmp_ge_u32 s58, s44\ns_cbranch_scc1 sx_h3\ns_memrealtime s[58:59]\n"
"s_waitcnt lgkmcnt(4)\ns_cmp_ge_u32 s54, s44\ns_cbranch_scc1 sx_h3\ns_memrealtime s[54:55]\n"
"s_waitcnt lgkmcnt(4)\ns_cmp_ge_u32 s56, s44\ns_cbranch_scc1 sx_h3\ns_memrealtime s[56:57]\n"
"s_waitcnt lgkmcnt(4)\ns_cmp_ge_u32 s58, s44\ns_cbranch_scc1 sx_h3\ns_memrealtime s[58:59]\n"
"s_waitcnt lgkmcnt(4)\ns_cmp_ge_u32 s54, s44\ns_cbranch_scc1 sx_h3\ns_memrealtime s[54:55]\n"
"s_waitcnt lgkmcnt(4)\ns_cmp_ge_u32 s56, s44\ns_cbranch_scc1 sx_h3\ns_memrealtime s[56:57]\n"
"s_waitcnt lgkmcnt(4)\ns_cmp_ge_u32 s58, s44\ns_cbranch_scc1 sx_h3\ns_memrealtime s[58:59]\n"
"s_waitcnt lgkmcnt(4)\ns_cmp_ge_u32 s54, s44\ns_cbranch_scc1 sx_h3\ns_memrealtime s[54:55]\n"
"s_waitcnt lgkmcnt(4)\ns_cmp_ge_u32 s56, s44\ns_cbranch_scc1 sx_h3\ns_memrealtime s[56:57]\n"
"s_branch sx_h3\nso_h3:\ns_waitcnt lgkmcnt(0)\ns_cmp_lt_i32 s47, 0\n"
"s_cbranch_scc1 sf_h3\ns_branch sgo_h3\nsx_h3:\ns_setpc_b64 s[60:61]\n"
"s_nop 0\ns_nop 0\ns_nop 0\ns_nop 0\n"
"s_nop 0\ns_nop 0\ns_nop 0\ns_nop 0\n"
"s_nop 0\ns_nop 0\ns_nop 0\ns_nop 0\n"
"s_nop 0\ns_nop 0\ns_nop 0\ns_nop 0\n"
"s_nop 0\ns_nop 0\ns_nop 0\ns_nop 0\n"
"s_nop 0\ns_nop 0\ns_nop 0\ns_nop 0\n"
"sgo_h3:\ns_nop 15\ns_nop 2\nbuffer_load_dwordx4 v[40:43], v10, s[8:11], s43 offen offset:1024 sc1\n"
"s_nop 13\nbuffer_load_dwordx4 v[44:47], v10, s[8:11], s43 offen offset:0 sc1\ns_nop 13\nbuffer_load_dwordx4 v[48:51], v10, s[8:11], s43 offen offset:2048 sc1\n"
"s_nop 3\nbuffer_load_dwordx4 v[68:71], v11, s[8:11], s43 offen sc1\nbuffer_load_dwordx4 v[72:75], v12, s[8:11], s43 offen sc1\nv_mov_b32 v125, 0\n"
"s_nop 1\nv_mov_b32 v125, 0\ns_nop 1\nv_mov_b32 v125, 0\n"
"s_nop 1\nv_mov_b32 v125, 0\ns_nop 1\nv_mov_b32 v125, 0\n"
"s_nop 1\nv_mov_b32 v125, 0\ns_nop 1\nv_mov_b32 v125, 0\n"
"s_nop 1\nv_mov_b32 v125, 0\ns_nop 1\nv_mov_b32 v125, 0\n"
"s_nop 1\nv_mov_b32 v125, 0\ns_nop 1\nv_mov_b32 v125, 0\n"
"s_nop 1\nv_mov_b32 v125, 0\ns_nop 1\nv_mov_b32 v125, 0\n"
"s_nop 1\nv_mov_b32 v125, 0\ns_nop 1\nv_mov_b32 v125, 0\n"
"s_nop 1\nv_mov_b32 v125, 0\ns_nop 1\nv_mov_b32 v125, 0\n"
"s_nop 0\nlanded_h3:\ns_waitcnt vmcnt(4)\nv_smfmac_f32_16x16x64_bf16 v[52:55], v[40:43], a[8:15], v8\n"
"v_max3_u32 v124, v124, v40, v41\nv_smfmac_f32_16x16x64_bf16 v[56:59], v[40:43], a[40:47], v8\nv_max3_u32 v124, v124, v42, v43\nv_smfmac_f32_16x16x64_bf16 v[60:63], v[40:43], a[72:79], v8\n"
"s_waitcnt vmcnt(3)\nv_smfmac_f32_16x16x64_bf16 v[52:55], v[44:47], a[0:7], v8\nv_max3_u32 v124, v124, v44, v45\nv_smfmac_f32_16x16x64_bf16 v[56:59], v[44:47], a[32:39], v8\n"
"v_max3_u32 v124, v124, v46, v47\nv_smfmac_f32_16x16x64_bf16 v[60:63], v[44:47], a[64:71], v8\ns_waitcnt vmcnt(2)\nv_smfmac_f32_16x16x64_bf16 v[52:55], v[48:51], a[16:23], v8\n"
"v_max3_u32 v124, v124, v48, v49\nv_smfmac_f32_16x16x64_bf16 v[56:59], v[48:51], a[48:55], v8\nv_max3_u32 v124, v124, v50, v51\nv_smfmac_f32_16x16x64_bf16 v[60:63], v[48:51], a[80:87], v8\n"
"v_add_f32 v125, v52, v53\nv_add_f32 v126, v54, v55\nv_cvt_pk_bf16_f32 v108, v125, v126\nv_add_f32 v125, v56, v57\n"
"v_add_f32 v126, v58, v59\nv_cvt_pk_bf16_f32 v109, v125, v126\nv_cmp_eq_u32 vcc, -1, v124\nv_add_f32 v125, v60, v61\n"
"v_add_f32 v126, v62, v63\nv_cvt_pk_bf16_f32 v110, v125, v126\nds_write2_b64 v127, v[108:109], v[110:111] offset0:32 offset1:48\ns_cbranch_vccnz retry_h3\n"
"v_mov_b32 v125, 0\ns_nop 1\nv_mov_b32 v125, 0\ns_nop 1\n"
"s_waitcnt lgkmcnt(0)\ns_barrier\nds_read_b64_tr_b16 v[84:85], v128 offset:0\nds_read_b64_tr_b16 v[86:87], v128 offset:64\n"
"ds_read_b64_tr_b16 v[88:89], v128 offset:256\nds_read_b64_tr_b16 v[90:91], v128 offset:320\nds_read_b64_tr_b16 v[92:93], v128 offset:512\nds_read_b64_tr_b16 v[94:95], v128 offset:576\n"
"ds_read_b64_tr_b16 v[96:97], v128 offset:768\nds_read_b64_tr_b16 v[98:99], v128 offset:832\nmb_h3:\ns_waitcnt vmcnt(0)\n"
"v_max3_u32 v124, v124, v68, v69\nv_max3_u32 v124, v124, v70, v71\nv_max3_u32 v124, v124, v72, v73\nv_max3_u32 v124, v124, v74, v75\n"
"v_cmp_eq_u32 vcc, -1, v124\ns_waitcnt lgkmcnt(6)\nv_mfma_f32_16x16x32_bf16 v[100:103], v[84:87], v[68:71], 0\ns_waitcnt lgkmcnt(4)\n"
"v_mfma_f32_16x16x32_bf16 v[100:103], v[88:91], v[72:75], v[100:103]\ns_waitcnt lgkmcnt(2)\nv_mfma_f32_16x16x32_bf16 v[100:103], v[92:95], v[68:71], v[100:103]\ns_waitcnt lgkmcnt(0)\n"
"v_mfma_f32_16x16x32_bf16 v[100:103], v[96:99], v[72:75], v[100:103]\ns_cbranch_vccnz mbr_h3\ns_nop 6\nv_add_f32_dpp v120, v102, v100 row_ror:8 row_mask:0xf bank_mask:0x3\n"
"v_add_f32_dpp v120, v100, v102 row_ror:8 row_mask:0xf bank_mask:0xc\nv_add_f32_dpp v121, v103, v101 row_ror:8 row_mask:0xf bank_mask:0x3\nv_add_f32_dpp v121, v101, v103 row_ror:8 row_mask:0xf bank_mask:0xc\nv_cvt_pk_f16_f32 v125, v120, v121\n"
"buffer_store_dword v125, v16, s[8:11], s41 offen sc1\ns_nop 15\ns_nop 15\ns_nop 15\n"
"buffer_store_dwordx2 v[120:121], v17, s[8:11], s41 offen sc1\nds_read_b32 v129, v130\nv_xor_b32 v127, 8192, v127\nv_xor_b32 v128, 8192, v128\n"
"s_waitcnt lgkmcnt(0)\nv_readfirstlane_b32 s47, v129\ns_sub_u32 s47, s47, s40\ns_cmp_eq_u32 s47, 1\n"
"s_cbranch_scc1 adopt_h3\nback_h3:\ns_mul_i32 s48, s46, 8\ns_add_u32 s45, s45, s48\n"
"s_add_u32 s45, s45, s32\ns_lshr_b32 s48, s45, 4\ns_lshl_b32 s48, s48, 2\ns_add_u32 s44, s44, s48\n"
"s_and_b32 s45, s45, 15\ns_add_u32 s52, s40, 1\ns_bitcmp1_b64 s[28:29], s52\ns_subb_u32 s46, s46, 0\n"
"s_add_u32 s40, s40, 1\ns_mov_b32 s43, s41\ns_add_u32 s41, s41, 131072\ns_and_b32 s41, s41, 524287\n"
"s_add_u32 s92, s92, 1\ns_xor_b32 s42, s41, 262144\ns_waitcnt lgkmcnt(0)\ns_cmp_lt_u32 s40, 64\n"
"s_cbranch_scc1 top_h3\ns_branch end_h3\nretry2_h3:\ns_sleep 3\n"
"retry_h3:\ns_add_u32 s53, s53, 1\ns_add_u32 s49, s49, 1\ns_cmp_gt_u32 s49, 65536\n"
"s_cbranch_scc1 end_h3\nbuffer_load_dwordx4 v[40:43], v10, s[8:11], s43 offen offset:1024 sc1\nbuffer_load_dwordx4 v[44:47], v10, s[8:11], s43 offen offset:0 sc1\nbuffer_load_dwordx4 v[48:51], v10, s[8:11], s43 offen offset:2048 sc1\n"
"buffer_load_dwordx4 v[68:71], v11, s[8:11], s43 offen sc1\nbuffer_load_dwordx4 v[72:75], v12, s[8:11], s43 offen sc1\nv_mov_b32 v124, 0\ns_add_u32 s47, s40, 1\n"
"v_mov_b32 v129, s47\nds_write_b32 v130, v129\ns_waitcnt vmcnt(0)\nv_max3_u32 v124, v124, v40, v41\n"
"v_max3_u32 v124, v124, v42, v43\nv_max3_u32 v124, v124, v44, v45\nv_max3_u32 v124, v124, v46, v47\nv_max3_u32 v124, v124, v48, v49\n"
"v_max3_u32 v124, v124, v50, v51\nv_cmp_eq_u32 vcc, -1, v124\nv_mov_b32 v52, 0\nv_mov_b32 v53, 0\n"
"v_mov_b32 v54, 0\nv_mov_b32 v55, 0\nv_mov_b32 v56, 0\nv_mov_b32 v57, 0\n"
"v_mov_b32 v58, 0\nv_mov_b32 v59, 0\nv_mov_b32 v60, 0\nv_mov_b32 v61, 0\n"
"v_mov_b32 v62, 0\nv_mov_b32 v63, 0\ns_cbranch_vccnz retry2_h3\ns_branch landed_h3\n"
"adopt_h3:\nbuffer_load_dword v112, v28, s[8:11], 0 offen sc1\ns_lshl_b32 s52, s40, 26\ns_or_b32 s47, s52, 0x3ffffff\n"
"s_waitcnt vmcnt(0)\nv_cmp_ge_u32 vcc, s47, v112\ns_nop 1\nv_cndmask_b32 v129, 0, v112, vcc\n"
"s_nop 1\nv_max_u32_dpp v129, v129, v129 row_ror:8 row_mask:0xf bank_mask:0xf\ns_nop 1\nv_max_u32_dpp v129, v129, v129 row_ror:4 row_mask:0xf bank_mask:0xf\n"
"s_nop 1\nv_max_u32_dpp v129, v129, v129 row_ror:2 row_mask:0xf bank_mask:0xf\ns_nop 1\nv_max_u32_dpp v129, v129, v129 row_ror:1 row_mask:0xf bank_mask:0xf\n"
"s_nop 0\nv_readlane_b32 s47, v129, 0\nv_readlane_b32 s48, v129, 16\ns_max_u32 s48, s48, s47\n"
"s_cmp_le_u32 s48, s52\ns_cbranch_scc1 back_h3\ns_bfe_u32 s52, s48, 1179656\ns_add_u32 s52, s52, s30\n"
"s_sub_u32 s45, s52, s44\ns_and_b32 s45, s45, 3\ns_sub_u32 s44, s52, s45\ns_lshl_b32 s45, s45, 2\n"
"s_and_b32 s46, s48, 255\ns_mov_b32 s92, 0\ns_branch back_h3\nmbr_h3:\n"
"s_add_u32 s53, s53, 1\ns_add_u32 s49, s49, 1\ns_cmp_gt_u32 s49, 65536\ns_cbranch_scc1 end_h3\n"
"buffer_load_dwordx4 v[68:71], v11, s[8:11], s43 offen sc1\nbuffer_load_dwordx4 v[72:75], v12, s[8:11], s43 offen sc1\nv_mov_b32 v124, 0\ns_branch mb_h3\n"
"end_h3:\ns_branch fk_done\nfk_x0:\ns_mov_b32 s40, 0\n"
"s_mov_b32 s41, 0\ns_mov_b32 s42, 262144\ns_mov_b32 s43, 393216\ns_mov_b32 s44, s30\n"
"s_mov_b32 s45, 0\ns_bcnt1_i32_b64 s46, s[28:29]\ns_mov_b32 s92, 99\ns_mov_b32 s49, 0\n"
"s_mov_b32 s53, 0\ns_mov_b32 s50, s36\ns_mov_b32 s51, s37\nv_mov_b32 v81, v13\n"
"v_mov_b32 v82, v14\nv_mov_b32 v84, 24576\ns_add_u32 s51, s50, 14336\nv_mov_b32 v64, -1\n"
"v_mov_b32 v65, -1\nv_mov_b32 v66, -1\nv_mov_b32 v67, -1\nbuffer_load_dwordx4 a[0:3], v9, s[16:19], s50 offen offset:0 sc1\n"
"buffer_load_dwordx4 a[4:7], v9, s[16:19], s50 offen offset:1024 sc1\nbuffer_load_dwordx4 a[8:11], v9, s[16:19], s50 offen offset:2048 sc1\nbuffer_load_dwordx4 a[12:15], v9, s[16:19], s50 offen offset:3072 sc1\nbuffer_load_dwordx4 a[16:19], v9, s[16:19], s51 offen offset:0 sc1\n"
"buffer_load_dwordx4 a[20:23], v9, s[16:19], s51 offen offset:1024 sc1\nbuffer_load_dwordx4 a[24:27], v9, s[16:19], s51 offen offset:2048 sc1\nbuffer_load_dwordx4 a[28:31], v9, s[16:19], s51 offen offset:3072 sc1\ns_add_u32 s50, s50, 28672\n"
"s_add_u32 s51, s51, 28672\nbuffer_load_dword v85, v18, s[8:11], 0 offen sc1\ns_waitcnt vmcnt(0)\nf0_x0:\n"
"s_add_u32 s47, s40, 2\ns_min_u32 s47, s47, 64\ns_add_u32 s47, s47, s34\nv_subrev_u32 v85, s47, v85\n"
"v_cmp_lt_u32 vcc, 64, v85\ns_cbranch_vccz f0ok_x0\ns_add_u32 s53, s53, 0\ns_add_u32 s49, s49, 1\n"
"s_cmp_gt_u32 s49, 65536\ns_cbranch_scc1 end_x0\ns_sleep 2\nbuffer_load_dword v85, v18, s[8:11], 0 offen sc1\n"
"s_waitcnt vmcnt(0)\ns_branch f0_x0\nf0ok_x0:\ns_barrier\n"
"s_memrealtime s[54:55]\ns_waitcnt lgkmcnt(0)\ns_add_u32 s44, s54, 16\ns_mov_b32 s45, 8\n"
".p2align 6\ntop_x0:\nv_mov_b32 v48, 0\nv_mov_b32 v49, 0\n"
"v_mov_b32 v50, 0\nv_mov_b32 v51, 0\nv_mov_b32 v52, 0\nv_mov_b32 v53, 0\n"
"v_mov_b32 v54, 0\nv_mov_b32 v55, 0\nv_mov_b32 v78, 0\ns_mul_i32 s48, s45, 6\n"
"s_and_b32 s48, s48, -4\ns_getpc_b64 s[60:61]\nspc_x0:\ns_sub_u32 s48, sgo_x0-spc_x0, s48\n"
"s_add_u32 s60, s60, s48\ns_addc_u32 s61, s61, 0\nsf_x0:\ns_memrealtime s[54:55]\n"
"s_nop 2\ns_memrealtime s[56:57]\ns_nop 2\ns_memrealtime s[58:59]\n"
"sg_x0:\ns_waitcnt lgkmcnt(4)\ns_sub_u32 s47, s54, s44\ns_add_u32 s48, s47, 12\n"
"s_cmp_ge_u32 s48, 12\ns_cbranch_scc1 so_x0\ns_memrealtime s[54:55]\ns_waitcnt lgkmcnt(4)\n"
"s_cmp_ge_u32 s56, s44\ns_cbranch_scc1 sx_x0\ns_memrealtime s[56:57]\ns_waitcnt lgkmcnt(4)\n"
"s_cmp_ge_u32 s58, s44\ns_cbranch_scc1 sx_x0\ns_memrealtime s[58:59]\ns_waitcnt lgkmcnt(4)\n"
"s_cmp_ge_u32 s54, s44\ns_cbranch_scc1 sx_x0\ns_memrealtime s[54:55]\ns_waitcnt lgkmcnt(4)\n"
"s_cmp_ge_u32 s56, s44\ns_cbranch_scc1 sx_x0\ns_memrealtime s[56:57]\ns_waitcnt lgkmcnt(4)\n"
"s_cmp_ge_u32 s58, s44\ns_cbranch_scc1 sx_x0\ns_memrealtime s[58:59]\ns_waitcnt lgkmcnt(4)\n"
"s_cmp_ge_u32 s54, s44\ns_cbranch_scc1 sx_x0\ns_memrealtime s[54:55]\ns_waitcnt lgkmcnt(4)\n"
"s_cmp_ge_u32 s56, s44\ns_cbranch_scc1 sx_x0\ns_memrealtime s[56:57]\ns_waitcnt lgkmcnt(4)\n"
"s_cmp_ge_u32 s58, s44\ns_cbranch_scc1 sx_x0\ns_memrealtime s[58:59]\ns_waitcnt lgkmcnt(4)\n"
"s_cmp_ge_u32 s54, s44\ns_cbranch_scc1 sx_x0\ns_memrealtime s[54:55]\ns_waitcnt lgkmcnt(4)\n"
"s_cmp_ge_u32 s56, s44\ns_cbranch_scc1 sx_x0\ns_memrealtime s[56:57]\ns_waitcnt lgkmcnt(4)\n"
"s_cmp_ge_u32 s58, s44\ns_cbranch_scc1 sx_x0\ns_memrealtime s[58:59]\ns_waitcnt lgkmcnt(4)\n"
"s_cmp_ge_u32 s54, s44\ns_cbranch_scc1 sx_x0\ns_memrealtime s[54:55]\ns_waitcnt lgkmcnt(4)\n"
"s_cmp_ge_u32 s56, s44\ns_cbranch_scc1 sx_x0\ns_memrealtime s[56:57]\ns_waitcnt lgkmcnt(4)\n"
"s_cmp_ge_u32 s58, s44\ns_cbranch_scc1 sx_x0\ns_memrealtime s[58:59]\ns_waitcnt lgkmcnt(4)\n"
"s_cmp_ge_u32 s54, s44\ns_cbranch_scc1 sx_x0\ns_memrealtime s[54:55]\ns_waitcnt lgkmcnt(4)\n"
"s_cmp_ge_u32 s56, s44\ns_cbranch_scc1 sx_x0\ns_memrealtime s[56:57]\ns_branch sx_x0\n"
"so_x0:\ns_waitcnt lgkmcnt(0)\ns_cmp_lt_i32 s47, 0\ns_cbranch_scc1 sf_x0\n"
"s_branch sgo_x0\nsx_x0:\ns_setpc_b64 s[60:61]\ns_nop 0\n"
"s_nop 0\ns_nop 0\ns_nop 0\ns_nop 0\n"
"s_nop 0\ns_nop 0\ns_nop 0\ns_nop 0\n"
"s_nop 0\ns_nop 0\ns_nop 0\ns_nop 0\n"
"s_nop 0\ns_nop 0\ns_nop 0\ns_nop 0\n"
"s_nop 0\ns_nop 0\ns_nop 0\ns_nop 0\n"
"s_nop 0\ns_nop 0\ns_nop 0\nsgo_x0:\n"
"buffer_load_dwordx4 v[40:43], v10, s[8:11], s43 offen offset:0 sc1\nbuffer_load_dwordx4 v[44:47], v10, s[8:11], s43 offen offset:1024 sc1\nlanded_x0:\ns_waitcnt vmcnt(1)\n"
"v_smfmac_f32_16x16x64_bf16 v[48:51], v[40:43], a[0:7], v8\nv_max3_u32 v78, v78, v40, v41\nv_smfmac_f32_16x16x64_bf16 v[52:55], v[40:43], a[16:23], v8\nv_max3_u32 v78, v78, v42, v43\n"
"s_waitcnt vmcnt(0)\nv_smfmac_f32_16x16x64_bf16 v[48:51], v[44:47], a[8:15], v8\nv_max3_u32 v78, v78, v44, v45\nv_smfmac_f32_16x16x64_bf16 v[52:55], v[44:47], a[24:31], v8\n"
"v_max3_u32 v78, v78, v46, v47\nv_cmp_eq_u32 vcc, -1, v78\ns_nop 0\nv_add_f32 v56, v48, v49\n"
"v_add_f32 v57, v50, v51\ns_nop 1\nv_add_f32 v58, v52, v53\nv_add_f32 v59, v54, v55\n"
"ds_write2_b32 v81, v56, v57 offset0:0 offset1:40\nds_write2_b32 v81, v58, v59 offset0:16 offset1:56\ns_cbranch_vccnz retry_x0\nv_mov_b32 v79, 0\n"
"s_nop 1\nv_mov_b32 v79, 0\ns_nop 1\nv_mov_b32 v79, 0\n"
"s_waitcnt lgkmcnt(0)\ns_barrier\nds_read_b64 v[68:69], v82 offset:0\nds_read_b64 v[70:71], v82 offset:1280\n"
"ds_read_b64 v[72:73], v82 offset:2560\nds_read_b64 v[74:75], v82 offset:3840\nv_mov_b32 v79, 0\ns_nop 1\n"
"v_mov_b32 v79, 0\ns_nop 1\nv_mov_b32 v79, 0\ns_nop 1\n"
"s_waitcnt lgkmcnt(2)\nv_pk_add_f32 v[68:69], v[68:69], v[70:71]\ns_waitcnt lgkmcnt(0)\nv_pk_add_f32 v[72:73], v[72:73], v[74:75]\n"
"v_pk_add_f32 v[68:69], v[68:69], v[72:73]\nv_exp_f32 v79, v68\nv_exp_f32 v80, v69\ns_nop 0\n"
"v_cvt_pk_bf16_f32 v79, v79, v80\nbuffer_store_dword v79, v16, s[8:11], s41 offen sc1\ns_barrier\nbuffer_load_dwordx4 a[0:3], v9, s[16:19], s50 offen offset:0 sc1\n"
"s_nop 15\ns_nop 7\nbuffer_load_dwordx4 a[4:7], v9, s[16:19], s50 offen offset:1024 sc1\ns_nop 15\n"
"s_nop 7\nbuffer_load_dwordx4 a[8:11], v9, s[16:19], s50 offen offset:2048 sc1\ns_nop 15\ns_nop 7\n"
"buffer_load_dwordx4 a[12:15], v9, s[16:19], s50 offen offset:3072 sc1\ns_nop 15\ns_nop 7\nbuffer_load_dwordx4 a[16:19], v9, s[16:19], s51 offen offset:0 sc1\n"
"s_nop 15\ns_nop 7\nbuffer_load_dwordx4 a[20:23], v9, s[16:19], s51 offen offset:1024 sc1\ns_nop 15\n"
"s_nop 7\nbuffer_load_dwordx4 a[24:27], v9, s[16:19], s51 offen offset:2048 sc1\ns_nop 15\ns_nop 7\n"
"buffer_load_dwordx4 a[28:31], v9, s[16:19], s51 offen offset:3072 sc1\ns_nop 15\ns_nop 7\ns_add_u32 s50, s50, 28672\n"
"s_add_u32 s51, s51, 28672\nbuffer_store_dwordx2 v[68:69], v17, s[8:11], s41 offen sc1\nbuffer_store_dwordx4 v[64:67], v15, s[8:11], s42 offen sc1\nds_read_b32 v83, v84\n"
"v_xor_b32 v81, 8192, v81\nv_xor_b32 v82, 8192, v82\ns_waitcnt lgkmcnt(0)\nv_readfirstlane_b32 s47, v83\n"
"s_sub_u32 s47, s47, s40\ns_cmp_eq_u32 s47, 1\ns_cbranch_scc1 adopt_x0\nback_x0:\n"
"s_mul_i32 s48, s46, 8\ns_add_u32 s45, s45, s48\ns_add_u32 s45, s45, s32\ns_lshr_b32 s48, s45, 4\n"
"s_lshl_b32 s48, s48, 2\ns_add_u32 s44, s44, s48\ns_and_b32 s45, s45, 15\ns_add_u32 s52, s40, 1\n"
"s_bitcmp1_b64 s[28:29], s52\ns_subb_u32 s46, s46, 0\ns_add_u32 s40, s40, 1\ns_mov_b32 s43, s41\n"
"s_add_u32 s41, s41, 131072\ns_and_b32 s41, s41, 524287\ns_add_u32 s92, s92, 1\ns_xor_b32 s42, s41, 262144\n"
"s_waitcnt lgkmcnt(0)\ns_cmp_lt_u32 s40, 63\ns_cbranch_scc1 top_x0\ns_branch end_x0\n"
"retry2_x0:\ns_sleep 3\nretry_x0:\ns_add_u32 s53, s53, 1\n"
"s_add_u32 s49, s49, 1\ns_cmp_gt_u32 s49, 65536\ns_cbranch_scc1 end_x0\nbuffer_load_dwordx4 v[40:43], v10, s[8:11], s43 offen offset:0 sc1\n"
"buffer_load_dwordx4 v[44:47], v10, s[8:11], s43 offen offset:1024 sc1\nv_mov_b32 v78, 0\ns_add_u32 s47, s40, 1\nv_mov_b32 v83, s47\n"
"ds_write_b32 v84, v83\ns_waitcnt vmcnt(0)\nv_max3_u32 v78, v78, v40, v41\nv_max3_u32 v78, v78, v42, v43\n"
"v_max3_u32 v78, v78, v44, v45\nv_max3_u32 v78, v78, v46, v47\nv_cmp_eq_u32 vcc, -1, v78\nv_mov_b32 v48, 0\n"
"v_mov_b32 v49, 0\nv_mov_b32 v50, 0\nv_mov_b32 v51, 0\nv_mov_b32 v52, 0\n"
"v_mov_b32 v53, 0\nv_mov_b32 v54, 0\nv_mov_b32 v55, 0\ns_cbranch_vccnz retry2_x0\n"
"s_branch landed_x0\nadopt_x0:\nbuffer_load_dword v60, v28, s[8:11], 0 offen sc1\ns_lshl_b32 s52, s40, 26\n"
"s_or_b32 s47, s52, 0x3ffffff\ns_waitcnt vmcnt(0)\nv_cmp_ge_u32 vcc, s47, v60\ns_nop 1\n"
"v_cndmask_b32 v83, 0, v60, vcc\ns_nop 1\nv_max_u32_dpp v83, v83, v83 row_ror:8 row_mask:0xf bank_mask:0xf\ns_nop 1\n"
"v_max_u32_dpp v83, v83, v83 row_ror:4 row_mask:0xf bank_mask:0xf\ns_nop 1\nv_max_u32_dpp v83, v83, v83 row_ror:2 row_mask:0xf bank_mask:0xf\ns_nop 1\n"
"v_max_u32_dpp v83, v83, v83 row_ror:1 row_mask:0xf bank_mask:0xf\ns_nop 0\nv_readlane_b32 s47, v83, 0\nv_readlane_b32 s48, v83, 16\n"
"s_max_u32 s48, s48, s47\ns_cmp_le_u32 s48, s52\ns_cbranch_scc1 back_x0\ns_bfe_u32 s52, s48, 1179656\n"
"s_add_u32 s52, s52, s30\ns_sub_u32 s45, s52, s44\ns_and_b32 s45, s45, 3\ns_sub_u32 s44, s52, s45\n"
"s_lshl_b32 s45, s45, 2\ns_and_b32 s46, s48, 255\ns_mov_b32 s92, 0\ns_branch back_x0\n"
"end_x0:\ns_branch fk_done\nfk_x1:\ns_mov_b32 s40, 0\n"
"s_mov_b32 s41, 0\ns_mov_b32 s42, 262144\ns_mov_b32 s43, 393216\ns_mov_b32 s44, s30\n"
"s_mov_b32 s45, 0\ns_bcnt1_i32_b64 s46, s[28:29]\ns_mov_b32 s92, 99\ns_mov_b32 s49, 0\n"
"s_mov_b32 s53, 0\ns_mov_b32 s50, s36\ns_mov_b32 s51, s37\nv_mov_b32 v81, v13\n"
"v_mov_b32 v82, v14\nv_mov_b32 v84, 24576\ns_add_u32 s51, s50, 14336\nv_mov_b32 v64, -1\n"
"v_mov_b32 v65, -1\nv_mov_b32 v66, -1\nv_mov_b32 v67, -1\nbuffer_load_dwordx4 a[8:11], v9, s[16:19], s50 offen offset:2048 sc1\n"
"buffer_load_dwordx4 a[12:15], v9, s[16:19], s50 offen offset:3072 sc1\nbuffer_load_dwordx4 a[0:3], v9, s[16:19], s50 offen offset:0 sc1\nbuffer_load_dwordx4 a[4:7], v9, s[16:19], s50 offen offset:1024 sc1\nbuffer_load_dwordx4 a[24:27], v9, s[16:19], s51 offen offset:2048 sc1\n"
"buffer_load_dwordx4 a[28:31], v9, s[16:19], s51 offen offset:3072 sc1\nbuffer_load_dwordx4 a[16:19], v9, s[16:19], s51 offen offset:0 sc1\nbuffer_load_dwordx4 a[20:23], v9, s[16:19], s51 offen offset:1024 sc1\ns_add_u32 s50, s50, 28672\n"
"s_add_u32 s51, s51, 28672\nbuffer_load_dword v85, v18, s[8:11], 0 offen sc1\ns_waitcnt vmcnt(0)\nf0_x1:\n"
"s_add_u32 s47, s40, 2\ns_min_u32 s47, s47, 64\ns_add_u32 s47, s47, s34\nv_subrev_u32 v85, s47, v85\n"
"v_cmp_lt_u32 vcc, 64, v85\ns_cbranch_vccz f0ok_x1\ns_add_u32 s53, s53, 0\ns_add_u32 s49, s49, 1\n"
"s_cmp_gt_u32 s49, 65536\ns_cbranch_scc1 end_x1\ns_sleep 2\nbuffer_load_dword v85, v18, s[8:11], 0 offen sc1\n"
"s_waitcnt vmcnt(0)\ns_branch f0_x1\nf0ok_x1:\ns_barrier\n"
"s_memrealtime s[54:55]\ns_waitcnt lgkmcnt(0)\ns_add_u32 s44, s54, 16\ns_mov_b32 s45, 8\n"
".p2align 6\ntop_x1:\nv_mov_b32 v48, 0\nv_mov_b32 v49, 0\n"
"v_mov_b32 v50, 0\nv_mov_b32 v51, 0\nv_mov_b32 v52, 0\nv_mov_b32 v53, 0\n"
"v_mov_b32 v54, 0\nv_mov_b32 v55, 0\nv_mov_b32 v78, 0\ns_mul_i32 s48, s45, 6\n"
"s_and_b32 s48, s48, -4\ns_getpc_b64 s[60:61]\nspc_x1:\ns_sub_u32 s48, sgo_x1-spc_x1, s48\n"
"s_add_u32 s60, s60, s48\ns_addc_u32 s61, s61, 0\nsf_x1:\ns_memrealtime s[54:55]\n"
"s_nop 2\ns_memrealtime s[56:57]\ns_nop 2\ns_memrealtime s[58:59]\n"
"sg_x1:\ns_waitcnt lgkmcnt(4)\ns_sub_u32 s47, s54, s44\ns_add_u32 s48, s47, 12\n"
"s_cmp_ge_u32 s48, 12\ns_cbranch_scc1 so_x1\ns_memrealtime s[54:55]\ns_waitcnt lgkmcnt(4)\n"
"s_cmp_ge_u32 s56, s44\ns_cbranch_scc1 sx_x1\ns_memrealtime s[56:57]\ns_waitcnt lgkmcnt(4)\n"
"s_cmp_ge_u32 s58, s44\ns_cbranch_scc1 sx_x1\ns_memrealtime s[58:59]\ns_waitcnt lgkmcnt(4)\n"
"s_cmp_ge_u32 s54, s44\ns_cbranch_scc1 sx_x1\ns_memrealtime s[54:55]\ns_waitcnt lgkmcnt(4)\n"
"s_cmp_ge_u32 s56, s44\ns_cbranch_scc1 sx_x1\ns_memrealtime s[56:57]\ns_waitcnt lgkmcnt(4)\n"
"s_cmp_ge_u32 s58, s44\ns_cbranch_scc1 sx_x1\ns_memrealtime s[58:59]\ns_waitcnt lgkmcnt(4)\n"
"s_cmp_ge_u32 s54, s44\ns_cbranch_scc1 sx_x1\ns_memrealtime s[54:55]\ns_waitcnt lgkmcnt(4)\n"
"s_cmp_ge_u32 s56, s44\ns_cbranch_scc1 sx_x1\ns_memrealtime s[56:57]\ns_waitcnt lgkmcnt(4)\n"
"s_cmp_ge_u32 s58, s44\ns_cbranch_scc1 sx_x1\ns_memrealtime s[58:59]\ns_waitcnt lgkmcnt(4)\n"
"s_cmp_ge_u32 s54, s44\ns_cbranch_scc1 sx_x1\ns_memrealtime s[54:55]\ns_waitcnt lgkmcnt(4)\n"
"s_cmp_ge_u32 s56, s44\ns_cbranch_scc1 sx_x1\ns_memrealtime s[56:57]\ns_waitcnt lgkmcnt(4)\n"
"s_cmp_ge_u32 s58, s44\ns_cbranch_scc1 sx_x1\ns_memrealtime s[58:59]\ns_waitcnt lgkmcnt(4)\n"
"s_cmp_ge_u32 s54, s44\ns_cbranch_scc1 sx_x1\ns_memrealtime s[54:55]\ns_waitcnt lgkmcnt(4)\n"
"s_cmp_ge_u32 s56, s44\ns_cbranch_scc1 sx_x1\ns_memrealtime s[56:57]\ns_waitcnt lgkmcnt(4)\n"
"s_cmp_ge_u32 s58, s44\ns_cbranch_scc1 sx_x1\ns_memrealtime s[58:59]\ns_waitcnt lgkmcnt(4)\n"
"s_cmp_ge_u32 s54, s44\ns_cbranch_scc1 sx_x1\ns_memrealtime s[54:55]\ns_waitcnt lgkmcnt(4)\n"
"s_cmp_ge_u32 s56, s44\ns_cbranch_scc1 sx_x1\ns_memrealtime s[56:57]\ns_branch sx_x1\n"
"so_x1:\ns_waitcnt lgkmcnt(0)\ns_cmp_lt_i32 s47, 0\ns_cbranch_scc1 sf_x1\n"
"s_branch sgo_x1\nsx_x1:\ns_setpc_b64 s[60:61]\ns_nop 0\n"
"s_nop 0\ns_nop 0\ns_nop 0\ns_nop 0\n"
"s_nop 0\ns_nop 0\ns_nop 0\ns_nop 0\n"
"s_nop 0\ns_nop 0\ns_nop 0\ns_nop 0\n"
"s_nop 0\ns_nop 0\ns_nop 0\ns_nop 0\n"
"s_nop 0\ns_nop 0\ns_nop 0\ns_nop 0\n"
"s_nop 0\ns_nop 0\ns_nop 0\nsgo_x1:\n"
"s_nop 5\nbuffer_load_dwordx4 v[40:43], v10, s[8:11], s43 offen offset:1024 sc1\nbuffer_load_dwordx4 v[44:47], v10, s[8:11], s43 offen offset:0 sc1\nlanded_x1:\n"
"s_waitcnt vmcnt(1)\nv_smfmac_f32_16x16x64_bf16 v[48:51], v[40:43], a[8:15], v8\nv_max3_u32 v78, v78, v40, v41\nv_smfmac_f32_16x16x64_bf16 v[52:55], v[40:43], a[24:31], v8\n"
"v_max3_u32 v78, v78, v42, v43\ns_waitcnt vmcnt(0)\nv_smfmac_f32_16x16x64_bf16 v[48:51], v[44:47], a[0:7], v8\nv_max3_u32 v78, v78, v44, v45\n"
"v_smfmac_f32_16x16x64_bf16 v[52:55], v[44:47], a[16:23], v8\nv_max3_u32 v78, v78, v46, v47\nv_cmp_eq_u32 vcc, -1, v78\ns_nop 0\n"
"v_add_f32 v56, v48, v49\nv_add_f32 v57, v50, v51\ns_nop 1\nv_add_f32 v58, v52, v53\n"
"v_add_f32 v59, v54, v55\nds_write2_b32 v81, v56, v57 offset0:0 offset1:40\nds_write2_b32 v81, v58, v59 offset0:16 offset1:56\ns_cbranch_vccnz retry_x1\n"
"v_mov_b32 v79, 0\ns_nop 1\nv_mov_b32 v79, 0\ns_nop 1\n"
"v_mov_b32 v79, 0\ns_waitcnt lgkmcnt(0)\ns_barrier\nds_read_b64 v[68:69], v82 offset:0\n"
"ds_read_b64 v[70:71], v82 offset:1280\nds_read_b64 v[72:73], v82 offset:2560\nds_read_b64 v[74:75], v82 offset:3840\nv_mov_b32 v79, 0\n"
"s_nop 1\nv_mov_b32 v79, 0\ns_nop 1\nv_mov_b32 v79, 0\n"
"s_nop 1\ns_waitcnt lgkmcnt(2)\nv_pk_add_f32 v[68:69], v[68:69], v[70:71]\ns_waitcnt lgkmcnt(0)\n"
"v_pk_add_f32 v[72:73], v[72:73], v[74:75]\nv_pk_add_f32 v[68:69], v[68:69], v[72:73]\nv_exp_f32 v79, v68\nv_exp_f32 v80, v69\n"
"s_nop 0\nv_cvt_pk_bf16_f32 v79, v79, v80\nbuffer_store_dword v79, v16, s[8:11], s41 offen sc1\ns_barrier\n"
"buffer_load_dwordx4 a[8:11], v9, s[16:19], s50 offen offset:2048 sc1\ns_nop 15\ns_nop 7\nbuffer_load_dwordx4 a[12:15], v9, s[16:19], s50 offen offset:3072 sc1\n"
"s_nop 15\ns_nop 7\nbuffer_load_dwordx4 a[0:3], v9, s[16:19], s50 offen offset:0 sc1\ns_nop 15\n"
"s_nop 7\nbuffer_load_dwordx4 a[4:7], v9, s[16:19], s50 offen offset:1024 sc1\ns_nop 15\ns_nop 7\n"
"buffer_load_dwordx4 a[24:27], v9, s[16:19], s51 offen offset:2048 sc1\ns_nop 15\ns_nop 7\nbuffer_load_dwordx4 a[28:31], v9, s[16:19], s51 offen offset:3072 sc1\n"
"s_nop 15\ns_nop 7\nbuffer_load_dwordx4 a[16:19], v9, s[16:19], s51 offen offset:0 sc1\ns_nop 15\n"
"s_nop 7\nbuffer_load_dwordx4 a[20:23], v9, s[16:19], s51 offen offset:1024 sc1\ns_nop 15\ns_nop 7\n"
"s_add_u32 s50, s50, 28672\ns_add_u32 s51, s51, 28672\nbuffer_store_dwordx2 v[68:69], v17, s[8:11], s41 offen sc1\nbuffer_store_dwordx4 v[64:67], v15, s[8:11], s42 offen sc1\n"
"ds_read_b32 v83, v84\nv_xor_b32 v81, 8192, v81\nv_xor_b32 v82, 8192, v82\ns_waitcnt lgkmcnt(0)\n"
"v_readfirstlane_b32 s47, v83\ns_sub_u32 s47, s47, s40\ns_cmp_eq_u32 s47, 1\ns_cbranch_scc1 adopt_x1\n"
"back_x1:\ns_mul_i32 s48, s46, 8\ns_add_u32 s45, s45, s48\ns_add_u32 s45, s45, s32\n"
"s_lshr_b32 s48, s45, 4\ns_lshl_b32 s48, s48, 2\ns_add_u32 s44, s44, s48\ns_and_b32 s45, s45, 15\n"
"s_add_u32 s52, s40, 1\ns_bitcmp1_b64 s[28:29], s52\ns_subb_u32 s46, s46, 0\ns_add_u32 s40, s40, 1\n"
"s_mov_b32 s43, s41\ns_add_u32 s41, s41, 131072\ns_and_b32 s41, s41, 524287\ns_add_u32 s92, s92, 1\n"
"s_xor_b32 s42, s41, 262144\ns_waitcnt lgkmcnt(0)\ns_cmp_lt_u32 s40, 63\ns_cbranch_scc1 top_x1\n"
"s_branch end_x1\nretry2_x1:\ns_sleep 3\nretry_x1:\n"
"s_add_u32 s53, s53, 1\ns_add_u32 s49, s49, 1\ns_cmp_gt_u32 s49, 65536\ns_cbranch_scc1 end_x1\n"
"buffer_load_dwordx4 v[40:43], v10, s[8:11], s43 offen offset:1024 sc1\nbuffer_load_dwordx4 v[44:47], v10, s[8:11], s43 offen offset:0 sc1\nv_mov_b32 v78, 0\ns_add_u32 s47, s40, 1\n"
"v_mov_b32 v83, s47\nds_write_b32 v84, v83\ns_waitcnt vmcnt(0)\nv_max3_u32 v78, v78, v40, v41\n"
"v_max3_u32 v78, v78, v42, v43\nv_max3_u32 v78, v78, v44, v45\nv_max3_u32 v78, v78, v46, v47\nv_cmp_eq_u32 vcc, -1, v78\n"
"v_mov_b32 v48, 0\nv_mov_b32 v49, 0\nv_mov_b32 v50, 0\nv_mov_b32 v51, 0\n"
"v_mov_b32 v52, 0\nv_mov_b32 v53, 0\nv_mov_b32 v54, 0\nv_mov_b32 v55, 0\n"
"s_cbranch_vccnz retry2_x1\ns_branch landed_x1\nadopt_x1:\nbuffer_load_dword v60, v28, s[8:11], 0 offen sc1\n"
"s_lshl_b32 s52, s40, 26\ns_or_b32 s47, s52, 0x3ffffff\ns_waitcnt vmcnt(0)\nv_cmp_ge_u32 vcc, s47, v60\n"
"s_nop 1\nv_cndmask_b32 v83, 0, v60, vcc\ns_nop 1\nv_max_u32_dpp v83, v83, v83 row_ror:8 row_mask:0xf bank_mask:0xf\n"
"s_nop 1\nv_max_u32_dpp v83, v83, v83 row_ror:4 row_mask:0xf bank_mask:0xf\ns_nop 1\nv_max_u32_dpp v83, v83, v83 row_ror:2 row_mask:0xf bank_mask:0xf\n"
"s_nop 1\nv_max_u32_dpp v83, v83, v83 row_ror:1 row_mask:0xf bank_mask:0xf\ns_nop 0\nv_readlane_b32 s47, v83, 0\n"
"v_readlane_b32 s48, v83, 16\ns_max_u32 s48, s48, s47\ns_cmp_le_u32 s48, s52\ns_cbranch_scc1 back_x1\n"
"s_bfe_u32 s52, s48, 1179656\ns_add_u32 s52, s52, s30\ns_sub_u32 s45, s52, s44\ns_and_b32 s45, s45, 3\n"
"s_sub_u32 s44, s52, s45\ns_lshl_b32 s45, s45, 2\ns_and_b32 s46, s48, 255\ns_mov_b32 s92, 0\n"
"s_branch back_x1\nend_x1:\ns_branch fk_done\nfk_x2:\n"
"s_mov_b32 s40, 0\ns_mov_b32 s41, 0\ns_mov_b32 s42, 262144\ns_mov_b32 s43, 393216\n"
"s_mov_b32 s44, s30\ns_mov_b32 s45, 0\ns_bcnt1_i32_b64 s46, s[28:29]\ns_mov_b32 s92, 99\n"
"s_mov_b32 s49, 0\ns_mov_b32 s53, 0\ns_mov_b32 s50, s36\ns_mov_b32 s51, s37\n"
"v_mov_b32 v81, v13\nv_mov_b32 v82, v14\nv_mov_b32 v84, 24576\ns_add_u32 s51, s50, 14336\n"
"v_mov_b32 v64, -1\nv_mov_b32 v65, -1\nv_mov_b32 v66, -1\nv_mov_b32 v67, -1\n"
"buffer_load_dwordx4 a[0:3], v9, s[16:19], s50 offen offset:0 sc1\nbuffer_load_dwordx4 a[4:7], v9, s[16:19], s50 offen offset:1024 sc1\nbuffer_load_dwordx4 a[8:11], v9, s[16:19], s50 offen offset:2048 sc1\nbuffer_load_dwordx4 a[12:15], v9, s[16:19], s50 offen offset:3072 sc1\n"
"buffer_load_dwordx4 a[16:19], v9, s[16:19], s51 offen offset:0 sc1\nbuffer_load_dwordx4 a[20:23], v9, s[16:19], s51 offen offset:1024 sc1\nbuffer_load_dwordx4 a[24:27], v9, s[16:19], s51 offen offset:2048 sc1\nbuffer_load_dwordx4 a[28:31], v9, s[16:19], s51 offen offset:3072 sc1\n"
"s_add_u32 s50, s50, 28672\ns_add_u32 s51, s51, 28672\nbuffer_load_dword v85, v18, s[8:11], 0 offen sc1\ns_waitcnt vmcnt(0)\n"
"f0_x2:\ns_add_u32 s47, s40, 2\ns_min_u32 s47, s47, 64\ns_add_u32 s47, s47, s34\n"
"v_subrev_u32 v85, s47, v85\nv_cmp_lt_u32 vcc, 64, v85\ns_cbranch_vccz f0ok_x2\ns_add_u32 s53, s53, 0\n"
"s_add_u32 s49, s49, 1\ns_cmp_gt_u32 s49, 65536\ns_cbranch_scc1 end_x2\ns_sleep 2\n"
"buffer_load_dword v85, v18, s[8:11], 0 offen sc1\ns_waitcnt vmcnt(0)\ns_branch f0_x2\nf0ok_x2:\n"
"s_barrier\ns_memrealtime s[54:55]\ns_waitcnt lgkmcnt(0)\ns_add_u32 s44, s54, 16\n"
"s_mov_b32 s45, 8\n.p2align 6\ntop_x2:\nv_mov_b32 v48, 0\n"
"v_mov_b32 v49, 0\nv_mov_b32 v50, 0\nv_mov_b32 v51, 0\nv_mov_b32 v52, 0\n"
"v_mov_b32 v53, 0\nv_mov_b32 v54, 0\nv_mov_b32 v55, 0\nv_mov_b32 v78, 0\n"
"s_mul_i32 s48, s45, 6\ns_and_b32 s48, s48, -4\ns_getpc_b64 s[60:61]\nspc_x2:\n"
"s_sub_u32 s48, sgo_x2-spc_x2, s48\ns_add_u32 s60, s60, s48\ns_addc_u32 s61, s61, 0\nsf_x2:\n"
"s_memrealtime s[54:55]\ns_nop 2\ns_memrealtime s[56:57]\ns_nop 2\n"
"s_memrealtime s[58:59]\nsg_x2:\ns_waitcnt lgkmcnt(4)\ns_sub_u32 s47, s54, s44\n"
"s_add_u32 s48, s47, 12\ns_cmp_ge_u32 s48, 12\ns_cbranch_scc1 so_x2\ns_memrealtime s[54:55]\n"
"s_waitcnt lgkmcnt(4)\ns_cmp_ge_u32 s56, s44\ns_cbranch_scc1 sx_x2\ns_memrealtime s[56:57]\n"
"s_waitcnt lgkmcnt(4)\ns_cmp_ge_u32 s58, s44\ns_cbranch_scc1 sx_x2\ns_memrealtime s[58:59]\n"
"s_waitcnt lgkmcnt(4)\ns_cmp_ge_u32 s54, s44\ns_cbranch_scc1 sx_x2\ns_memrealtime s[54:55]\n"
"s_waitcnt lgkmcnt(4)\ns_cmp_ge_u32 s56, s44\ns_cbranch_scc1 sx_x2\ns_memrealtime s[56:57]\n"
"s_waitcnt lgkmcnt(4)\ns_cmp_ge_u32 s58, s44\ns_cbranch_scc1 sx_x2\ns_memrealtime s[58:59]\n"
"s_waitcnt lgkmcnt(4)\ns_cmp_ge_u32 s54, s44\ns_cbranch_scc1 sx_x2\ns_memrealtime s[54:55]\n"
"s_waitcnt lgkmcnt(4)\ns_cmp_ge_u32 s56, s44\ns_cbranch_scc1 sx_x2\ns_memrealtime s[56:57]\n"
"s_waitcnt lgkmcnt(4)\ns_cmp_ge_u32 s58, s44\ns_cbranch_scc1 sx_x2\ns_memrealtime s[58:59]\n"
"s_waitcnt lgkmcnt(4)\ns_cmp_ge_u32 s54, s44\ns_cbranch_scc1 sx_x2\ns_memrealtime s[54:55]\n"
"s_waitcnt lgkmcnt(4)\ns_cmp_ge_u32 s56, s44\ns_cbranch_scc1 sx_x2\ns_memrealtime s[56:57]\n"
"s_waitcnt lgkmcnt(4)\ns_cmp_ge_u32 s58, s44\ns_cbranch_scc1 sx_x2\ns_memrealtime s[58:59]\n"
"s_waitcnt lgkmcnt(4)\ns_cmp_ge_u32 s54, s44\ns_cbranch_scc1 sx_x2\ns_memrealtime s[54:55]\n"
"s_waitcnt lgkmcnt(4)\ns_cmp_ge_u32 s56, s44\ns_cbranch_scc1 sx_x2\ns_memrealtime s[56:57]\n"
"s_waitcnt lgkmcnt(4)\ns_cmp_ge_u32 s58, s44\ns_cbranch_scc1 sx_x2\ns_memrealtime s[58:59]\n"
"s_waitcnt lgkmcnt(4)\ns_cmp_ge_u32 s54, s44\ns_cbranch_scc1 sx_x2\ns_memrealtime s[54:55]\n"
"s_waitcnt lgkmcnt(4)\ns_cmp_ge_u32 s56, s44\ns_cbranch_scc1 sx_x2\ns_memrealtime s[56:57]\n"
"s_branch sx_x2\nso_x2:\ns_waitcnt lgkmcnt(0)\ns_cmp_lt_i32 s47, 0\n"
"s_cbranch_scc1 sf_x2\ns_branch sgo_x2\nsx_x2:\ns_setpc_b64 s[60:61]\n"
"s_nop 0\ns_nop 0\ns_nop 0\ns_nop 0\n"
"s_nop 0\ns_nop 0\ns_nop 0\ns_nop 0\n"
"s_nop 0\ns_nop 0\ns_nop 0\ns_nop 0\n"
"s_nop 0\ns_nop 0\ns_nop 0\ns_nop 0\n"
"s_nop 0\ns_nop 0\ns_nop 0\ns_nop 0\n"
"s_nop 0\ns_nop 0\ns_nop 0\ns_nop 0\n"
"sgo_x2:\ns_nop 11\nbuffer_load_dwordx4 v[40:43], v10, s[8:11], s43 offen offset:0 sc1\nbuffer_load_dwordx4 v[44:47], v10, s[8:11], s43 offen offset:1024 sc1\n"
"v_mov_b32 v79, 0\ns_nop 1\nv_mov_b32 v79, 0\ns_nop 1\n"
"v_mov_b32 v79, 0\ns_nop 1\nv_mov_b32 v79, 0\ns_nop 1\n"
"v_mov_b32 v79, 0\ns_nop 1\nv_mov_b32 v79, 0\ns_nop 1\n"
"v_mov_b32 v79, 0\ns_nop 1\nv_mov_b32 v79, 0\ns_nop 1\n"
"v_mov_b32 v79, 0\ns_nop 1\nv_mov_b32 v79, 0\ns_nop 1\n"
"v_mov_b32 v79, 0\ns_nop 1\nv_mov_b32 v79, 0\ns_nop 1\n"
"v_mov_b32 v79, 0\ns_nop 1\nv_mov_b32 v79, 0\ns_nop 1\n"
"v_mov_b32 v79, 0\ns_nop 1\nv_mov_b32 v79, 0\ns_nop 1\n"
"v_mov_b32 v79, 0\ns_nop 1\nv_mov_b32 v79, 0\ns_nop 1\n"
"v_mov_b32 v79, 0\ns_nop 1\nv_mov_b32 v79, 0\ns_nop 1\n"
"v_mov_b32 v79, 0\ns_nop 1\nv_mov_b32 v79, 0\ns_nop 1\n"
"v_mov_b32 v79, 0\ns_nop 1\nv_mov_b32 v79, 0\ns_nop 1\n"
"v_mov_b32 v79, 0\ns_nop 1\nv_mov_b32 v79, 0\ns_nop 1\n"
"v_mov_b32 v79, 0\ns_nop 1\nv_mov_b32 v79, 0\ns_nop 1\n"
"v_mov_b32 v79, 0\nlanded_x2:\ns_waitcnt vmcnt(1)\nv_smfmac_f32_16x16x64_bf16 v[48:51], v[40:43], a[0:7], v8\n"
"v_max3_u32 v78, v78, v40, v41\nv_smfmac_f32_16x16x64_bf16 v[52:55], v[40:43], a[16:23], v8\nv_max3_u32 v78, v78, v42, v43\ns_waitcnt vmcnt(0)\n"
"v_smfmac_f32_16x16x64_bf16 v[48:51], v[44:47], a[8:15], v8\nv_max3_u32 v78, v78, v44, v45\nv_smfmac_f32_16x16x64_bf16 v[52:55], v[44:47], a[24:31], v8\nv_max3_u32 v78, v78, v46, v47\n"
"v_cmp_eq_u32 vcc, -1, v78\ns_nop 0\nv_add_f32 v56, v48, v49\nv_add_f32 v57, v50, v51\n"
"s_nop 1\nv_add_f32 v58, v52, v53\nv_add_f32 v59, v54, v55\nds_write2_b32 v81, v56, v57 offset0:0 offset1:40\n"
"ds_write2_b32 v81, v58, v59 offset0:16 offset1:56\ns_cbranch_vccnz retry_x2\nv_mov_b32 v79, 0\ns_nop 1\n"
"v_mov_b32 v79, 0\ns_nop 1\nv_mov_b32 v79, 0\ns_waitcnt lgkmcnt(0)\n"
"s_barrier\nds_read_b64 v[68:69], v82 offset:0\nds_read_b64 v[70:71], v82 offset:1280\nds_read_b64 v[72:73], v82 offset:2560\n"
"ds_read_b64 v[74:75], v82 offset:3840\nv_mov_b32 v79, 0\ns_nop 1\nv_mov_b32 v79, 0\n"
"s_nop 1\nv_mov_b32 v79, 0\ns_nop 1\ns_waitcnt lgkmcnt(2)\n"
"v_pk_add_f32 v[68:69], v[68:69], v[70:71]\ns_waitcnt lgkmcnt(0)\nv_pk_add_f32 v[72:73], v[72:73], v[74:75]\nv_pk_add_f32 v[68:69], v[68:69], v[72:73]\n"
"v_exp_f32 v79, v68\nv_exp_f32 v80, v69\ns_nop 0\nv_cvt_pk_bf16_f32 v79, v79, v80\n"
"buffer_store_dword v79, v16, s[8:11], s41 offen sc1\ns_barrier\nbuffer_load_dwordx4 a[0:3], v9, s[16:19], s50 offen offset:0 sc1\ns_nop 15\n"
"s_nop 7\nbuffer_load_dwordx4 a[4:7], v9, s[16:19], s50 offen offset:1024 sc1\ns_nop 15\ns_nop 7\n"
"buffer_load_dwordx4 a[8:11], v9, s[16:19], s50 offen offset:2048 sc1\ns_nop 15\ns_nop 7\nbuffer_load_dwordx4 a[12:15], v9, s[16:19], s50 offen offset:3072 sc1\n"
"s_nop 15\ns_nop 7\nbuffer_load_dwordx4 a[16:19], v9, s[16:19], s51 offen offset:0 sc1\ns_nop 15\n"
"s_nop 7\nbuffer_load_dwordx4 a[20:23], v9, s[16:19], s51 offen offset:1024 sc1\ns_nop 15\ns_nop 7\n"
"buffer_load_dwordx4 a[24:27], v9, s[16:19], s51 offen offset:2048 sc1\ns_nop 15\ns_nop 7\nbuffer_load_dwordx4 a[28:31], v9, s[16:19], s51 offen offset:3072 sc1\n"
"s_nop 15\ns_nop 7\ns_add_u32 s50, s50, 28672\ns_add_u32 s51, s51, 28672\n"
"buffer_store_dwordx2 v[68:69], v17, s[8:11], s41 offen sc1\nbuffer_store_dwordx4 v[64:67], v15, s[8:11], s42 offen sc1\nds_read_b32 v83, v84\nv_xor_b32 v81, 8192, v81\n"
"v_xor_b32 v82, 8192, v82\ns_waitcnt lgkmcnt(0)\nv_readfirstlane_b32 s47, v83\ns_sub_u32 s47, s47, s40\n"
"s_cmp_eq_u32 s47, 1\ns_cbranch_scc1 adopt_x2\nback_x2:\ns_mul_i32 s48, s46, 8\n"
"s_add_u32 s45, s45, s48\ns_add_u32 s45, s45, s32\ns_lshr_b32 s48, s45, 4\ns_lshl_b32 s48, s48, 2\n"
"s_add_u32 s44, s44, s48\ns_and_b32 s45, s45, 15\ns_add_u32 s52, s40, 1\ns_bitcmp1_b64 s[28:29], s52\n"
"s_subb_u32 s46, s46, 0\ns_add_u32 s40, s40, 1\ns_mov_b32 s43, s41\ns_add_u32 s41, s41, 131072\n"
"s_and_b32 s41, s41, 524287\ns_add_u32 s92, s92, 1\ns_xor_b32 s42, s41, 262144\ns_waitcnt lgkmcnt(0)\n"
"s_cmp_lt_u32 s40, 63\ns_cbranch_scc1 top_x2\ns_branch end_x2\nretry2_x2:\n"
"s_sleep 3\nretry_x2:\ns_add_u32 s53, s53, 1\ns_add_u32 s49, s49, 1\n"
"s_cmp_gt_u32 s49, 65536\ns_cbranch_scc1 end_x2\nbuffer_load_dwordx4 v[40:43], v10, s[8:11], s43 offen offset:0 sc1\nbuffer_load_dwordx4 v[44:47], v10, s[8:11], s43 offen offset:1024 sc1\n"
"v_mov_b32 v78, 0\ns_add_u32 s47, s40, 1\nv_mov_b32 v83, s47\nds_write_b32 v84, v83\n"
"s_waitcnt vmcnt(0)\nv_max3_u32 v78, v78, v40, v41\nv_max3_u32 v78, v78, v42, v43\nv_max3_u32 v78, v78, v44, v45\n"
"v_max3_u32 v78, v78, v46, v47\nv_cmp_eq_u32 vcc, -1, v78\nv_mov_b32 v48, 0\nv_mov_b32 v49, 0\n"
"v_mov_b32 v50, 0\nv_mov_b32 v51, 0\nv_mov_b32 v52, 0\nv_mov_b32 v53, 0\n"
"v_mov_b32 v54, 0\nv_mov_b32 v55, 0\ns_cbranch_vccnz retry2_x2\ns_branch landed_x2\n"
"adopt_x2:\nbuffer_load_dword v60, v28, s[8:11], 0 offen sc1\ns_lshl_b32 s52, s40, 26\ns_or_b32 s47, s52, 0x3ffffff\n"
"s_waitcnt vmcnt(0)\nv_cmp_ge_u32 vcc, s47, v60\ns_nop 1\nv_cndmask_b32 v83, 0, v60, vcc\n"
"s_nop 1\nv_max_u32_dpp v83, v83, v83 row_ror:8 row_mask:0xf bank_mask:0xf\ns_nop 1\nv_max_u32_dpp v83, v83, v83 row_ror:4 row_mask:0xf bank_mask:0xf\n"
"s_nop 1\nv_max_u32_dpp v83, v83, v83 row_ror:2 row_mask:0xf bank_mask:0xf\ns_nop 1\nv_max_u32_dpp v83, v83, v83 row_ror:1 row_mask:0xf bank_mask:0xf\n"
"s_nop 0\nv_readlane_b32 s47, v83, 0\nv_readlane_b32 s48, v83, 16\ns_max_u32 s48, s48, s47\n"
"s_cmp_le_u32 s48, s52\ns_cbranch_scc1 back_x2\ns_bfe_u32 s52, s48, 1179656\ns_add_u32 s52, s52, s30\n"
"s_sub_u32 s45, s52, s44\ns_and_b32 s45, s45, 3\ns_sub_u32 s44, s52, s45\ns_lshl_b32 s45, s45, 2\n"
"s_and_b32 s46, s48, 255\ns_mov_b32 s92, 0\ns_branch back_x2\nend_x2:\n"
"s_branch fk_done\nfk_x3:\ns_mov_b32 s40, 0\ns_mov_b32 s41, 0\n"
"s_mov_b32 s42, 262144\ns_mov_b32 s43, 393216\ns_mov_b32 s44, s30\ns_mov_b32 s45, 0\n"
"s_bcnt1_i32_b64 s46, s[28:29]\ns_mov_b32 s92, 99\ns_mov_b32 s49, 0\ns_mov_b32 s53, 0\n"
"s_mov_b32 s50, s36\ns_mov_b32 s51, s37\nv_mov_b32 v77, v13\nv_mov_b32 v78, v14\n"
"v_mov_b32 v80, 24576\ns_add_u32 s51, s50, 14336\nv_mov_b32 v60, -1\nv_mov_b32 v61, -1\n"
"v_mov_b32 v62, -1\nv_mov_b32 v63, -1\nbuffer_load_dwordx4 a[0:3], v9, s[16:19], s50 offen offset:0 sc1\nbuffer_load_dwordx4 a[4:7], v9, s[16:19], s50 offen offset:1024 sc1\n"
"buffer_load_dwordx4 a[16:19], v9, s[16:19], s51 offen offset:0 sc1\nbuffer_load_dwordx4 a[20:23], v9, s[16:19], s51 offen offset:1024 sc1\ns_add_u32 s50, s50, 28672\ns_add_u32 s51, s51, 28672\n"
"v_lshlrev_b32 v58, 2, v9\ns_mov_b32 s52, 1064960\nbuffer_load_dword v56, v58, s[8:11], s52 offen sc1\ns_add_u32 s52, s52, 4096\n"
"buffer_load_dword v57, v58, s[8:11], s52 offen sc1\nbuffer_load_dword v81, v18, s[8:11], 0 offen sc1\ns_waitcnt vmcnt(0)\nar_x3:\n"
"v_xor_b32 v56, s34, v56\nv_xor_b32 v57, s34, v57\nv_or_b32 v56, v56, v57\nv_cmp_ne_u32 vcc, 0, v56\n"
"s_cbranch_vccz arok_x3\ns_add_u32 s53, s53, 0\ns_add_u32 s49, s49, 1\ns_cmp_gt_u32 s49, 65536\n"
"s_cbranch_scc1 end_x3\ns_sleep 2\nv_lshlrev_b32 v58, 2, v9\ns_mov_b32 s52, 1064960\n"
"buffer_load_dword v56, v58, s[8:11], s52 offen sc1\ns_add_u32 s52, s52, 4096\nbuffer_load_dword v57, v58, s[8:11], s52 offen sc1\ns_waitcnt vmcnt(0)\n"
"s_branch ar_x3\narok_x3:\nf0_x3:\ns_add_u32 s47, s40, 2\n"
"s_min_u32 s47, s47, 64\ns_add_u32 s47, s47, s34\nv_subrev_u32 v81, s47, v81\nv_cmp_lt_u32 vcc, 64, v81\n"
"s_cbranch_vccz f0ok_x3\ns_add_u32 s53, s53, 0\ns_add_u32 s49, s49, 1\ns_cmp_gt_u32 s49, 65536\n"
"s_cbranch_scc1 end_x3\ns_sleep 2\nbuffer_load_dword v81, v18, s[8:11], 0 offen sc1\ns_waitcnt vmcnt(0)\n"
"s_branch f0_x3\nf0ok_x3:\ns_barrier\ns_memrealtime s[54:55]\n"
"s_waitcnt lgkmcnt(0)\ns_add_u32 s44, s54, 16\ns_mov_b32 s45, 8\n.p2align 6\n"
"top_x3:\nv_mov_b32 v44, 0\nv_mov_b32 v45, 0\nv_mov_b32 v46, 0\n"
"v_mov_b32 v47, 0\nv_mov_b32 v48, 0\nv_mov_b32 v49, 0\nv_mov_b32 v50, 0\n"
"v_mov_b32 v51, 0\nv_mov_b32 v74, 0\ns_mul_i32 s48, s45, 6\ns_and_b32 s48, s48, -4\n"
"s_getpc_b64 s[60:61]\nspc_x3:\ns_sub_u32 s48, sgo_x3-spc_x3, s48\ns_add_u32 s60, s60, s48\n"
"s_addc_u32 s61, s61, 0\nsf_x3:\ns_memrealtime s[54:55]\ns_nop 2\n"
"s_memrealtime s[56:57]\ns_nop 2\ns_memrealtime s[58:59]\nsg_x3:\n"
"s_waitcnt lgkmcnt(4)\ns_sub_u32 s47, s54, s44\ns_add_u32 s48, s47, 12\ns_cmp_ge_u32 s48, 12\n"
"s_cbranch_scc1 so_x3\ns_memrealtime s[54:55]\ns_waitcnt lgkmcnt(4)\ns_cmp_ge_u32 s56, s44\n"
"s_cbranch_scc1 sx_x3\ns_memrealtime s[56:57]\ns_waitcnt lgkmcnt(4)\ns_cmp_ge_u32 s58, s44\n"
"s_cbranch_scc1 sx_x3\ns_memrealtime s[58:59]\ns_waitcnt lgkmcnt(4)\ns_cmp_ge_u32 s54, s44\n"
"s_cbranch_scc1 sx_x3\ns_memrealtime s[54:55]\ns_waitcnt lgkmcnt(4)\ns_cmp_ge_u32 s56, s44\n"
"s_cbranch_scc1 sx_x3\ns_memrealtime s[56:57]\ns_waitcnt lgkmcnt(4)\ns_cmp_ge_u32 s58, s44\n"
"s_cbranch_scc1 sx_x3\ns_memrealtime s[58:59]\ns_waitcnt lgkmcnt(4)\ns_cmp_ge_u32 s54, s44\n"
"s_cbranch_scc1 sx_x3\ns_memrealtime s[54:55]\ns_waitcnt lgkmcnt(4)\ns_cmp_ge_u32 s56, s44\n"
"s_cbranch_scc1 sx_x3\ns_memrealtime s[56:57]\ns_waitcnt lgkmcnt(4)\ns_cmp_ge_u32 s58, s44\n"
"s_cbranch_scc1 sx_x3\ns_memrealtime s[58:59]\ns_waitcnt lgkmcnt(4)\ns_cmp_ge_u32 s54, s44\n"
"s_cbranch_scc1 sx_x3\ns_memrealtime s[54:55]\ns_waitcnt lgkmcnt(4)\ns_cmp_ge_u32 s56, s44\n"
"s_cbranch_scc1 sx_x3\ns_memrealtime s[56:57]\ns_waitcnt lgkmcnt(4)\ns_cmp_ge_u32 s58, s44\n"
"s_cbranch_scc1 sx_x3\ns_memrealtime s[58:59]\ns_waitcnt lgkmcnt(4)\ns_cmp_ge_u32 s54, s44\n"
"s_cbranch_scc1 sx_x3\ns_memrealtime s[54:55]\ns_waitcnt lgkmcnt(4)\ns_cmp_ge_u32 s56, s44\n"
"s_cbranch_scc1 sx_x3\ns_memrealtime s[56:57]\ns_waitcnt lgkmcnt(4)\ns_cmp_ge_u32 s58, s44\n"
"s_cbranch_scc1 sx_x3\ns_memrealtime s[58:59]\ns_waitcnt lgkmcnt(4)\ns_cmp_ge_u32 s54, s44\n"
"s_cbranch_scc1 sx_x3\ns_memrealtime s[54:55]\ns_waitcnt lgkmcnt(4)\ns_cmp_ge_u32 s56, s44\n"
"s_cbranch_scc1 sx_x3\ns_memrealtime s[56:57]\ns_branch sx_x3\nso_x3:\n"
"s_waitcnt lgkmcnt(0)\ns_cmp_lt_i32 s47, 0\ns_cbranch_scc1 sf_x3\ns_branch sgo_x3\n"
"sx_x3:\ns_setpc_b64 s[60:61]\ns_nop 0\ns_nop 0\n"
"s_nop 0\ns_nop 0\ns_nop 0\ns_nop 0\n"
"s_nop 0\ns_nop 0\ns_nop 0\ns_nop 0\n"
"s_nop 0\ns_nop 0\ns_nop 0\ns_nop 0\n"
"s_nop 0\ns_nop 0\ns_nop 0\ns_nop 0\n"
"s_nop 0\ns_nop 0\ns_nop 0\ns_nop 0\n"
"s_nop 0\ns_nop 0\nsgo_x3:\ns_nop 15\n"
"s_nop 1\nbuffer_load_dwordx4 v[40:43], v10, s[8:11], s43 offen offset:0 sc1\nv_mov_b32 v75, 0\ns_nop 1\n"
"v_mov_b32 v75, 0\ns_nop 1\nv_mov_b32 v75, 0\ns_nop 1\n"
"v_mov_b32 v75, 0\ns_nop 1\nv_mov_b32 v75, 0\ns_nop 1\n"
"v_mov_b32 v75, 0\ns_nop 1\nv_mov_b32 v75, 0\ns_nop 1\n"
"v_mov_b32 v75, 0\ns_nop 1\nv_mov_b32 v75, 0\ns_nop 1\n"
"v_mov_b32 v75, 0\ns_nop 1\nv_mov_b32 v75, 0\ns_nop 1\n"
"v_mov_b32 v75, 0\ns_nop 1\nv_mov_b32 v75, 0\ns_nop 1\n"
"v_mov_b32 v75, 0\ns_nop 1\nv_mov_b32 v75, 0\ns_nop 1\n"
"v_mov_b32 v75, 0\ns_nop 1\nv_mov_b32 v75, 0\ns_nop 1\n"
"v_mov_b32 v75, 0\ns_nop 1\nv_mov_b32 v75, 0\ns_nop 1\n"
"v_mov_b32 v75, 0\ns_nop 1\nv_mov_b32 v75, 0\ns_nop 1\n"
"v_mov_b32 v75, 0\ns_nop 1\nv_mov_b32 v75, 0\ns_nop 1\n"
"v_mov_b32 v75, 0\ns_nop 1\nv_mov_b32 v75, 0\ns_nop 1\n"
"v_mov_b32 v75, 0\ns_nop 1\nv_mov_b32 v75, 0\ns_nop 1\n"
"v_mov_b32 v75, 0\ns_nop 1\nv_mov_b32 v75, 0\ns_nop 1\n"
"v_mov_b32 v75, 0\ns_nop 1\nv_mov_b32 v75, 0\ns_nop 1\n"
"v_mov_b32 v75, 0\ns_nop 0\nlanded_x3:\ns_waitcnt vmcnt(0)\n"
"v_smfmac_f32_16x16x64_bf16 v[44:47], v[40:43], a[0:7], v8\nv_max3_u32 v74, v74, v40, v41\nv_smfmac_f32_16x16x64_bf16 v[48:51], v[40:43], a[16:23], v8\nv_max3_u32 v74, v74, v42, v43\n"
"v_cmp_eq_u32 vcc, -1, v74\ns_nop 0\nv_add_f32 v52, v44, v45\nv_add_f32 v53, v46, v47\n"
"s_nop 1\nv_add_f32 v54, v48, v49\nv_add_f32 v55, v50, v51\nds_write2_b32 v77, v52, v53 offset0:0 offset1:40\n"
"ds_write2_b32 v77, v54, v55 offset0:16 offset1:56\ns_cbranch_vccnz retry_x3\nv_mov_b32 v75, 0\ns_nop 1\n"
"v_mov_b32 v75, 0\ns_nop 1\nv_mov_b32 v75, 0\ns_waitcnt lgkmcnt(0)\n"
"s_barrier\nds_read_b64 v[64:65], v78 offset:0\nds_read_b64 v[66:67], v78 offset:1280\nds_read_b64 v[68:69], v78 offset:2560\n"
"ds_read_b64 v[70:71], v78 offset:3840\nv_mov_b32 v75, 0\ns_nop 1\nv_mov_b32 v75, 0\n"
"s_nop 1\nv_mov_b32 v75, 0\ns_nop 1\ns_waitcnt lgkmcnt(2)\n"
"v_pk_add_f32 v[64:65], v[64:65], v[66:67]\ns_waitcnt lgkmcnt(0)\nv_pk_add_f32 v[68:69], v[68:69], v[70:71]\nv_pk_add_f32 v[64:65], v[64:65], v[68:69]\n"
"v_exp_f32 v75, v64\nv_exp_f32 v76, v65\ns_nop 0\nv_cvt_pk_bf16_f32 v75, v75, v76\n"
"buffer_store_dword v75, v16, s[8:11], s41 offen sc1\ns_barrier\nbuffer_load_dword v81, v18, s[8:11], 0 offen sc1\nbuffer_load_dwordx4 a[0:3], v9, s[16:19], s50 offen offset:0 sc1\n"
"s_nop 15\ns_nop 7\nbuffer_load_dwordx4 a[4:7], v9, s[16:19], s50 offen offset:1024 sc1\ns_nop 15\n"
"s_nop 7\nbuffer_load_dwordx4 a[16:19], v9, s[16:19], s51 offen offset:0 sc1\ns_nop 15\ns_nop 7\n"
"buffer_load_dwordx4 a[20:23], v9, s[16:19], s51 offen offset:1024 sc1\ns_nop 15\ns_nop 7\ns_add_u32 s50, s50, 28672\n"
"s_add_u32 s51, s51, 28672\nbuffer_store_dwordx2 v[64:65], v17, s[8:11], s41 offen sc1\nbuffer_store_dwordx4 v[60:63], v15, s[8:11], s42 offen sc1\nds_read_b32 v79, v80\n"
"v_xor_b32 v77, 8192, v77\nv_xor_b32 v78, 8192, v78\ns_waitcnt lgkmcnt(0)\nv_readfirstlane_b32 s47, v79\n"
"s_sub_u32 s47, s47, s40\ns_cmp_eq_u32 s47, 1\ns_cbranch_scc1 adopt_x3\nback_x3:\n"
"s_waitcnt vmcnt(6)\nfl_x3:\ns_add_u32 s47, s40, 4\ns_min_u32 s47, s47, 64\n"
"s_add_u32 s47, s47, s34\nv_subrev_u32 v81, s47, v81\nv_cmp_lt_u32 vcc, 64, v81\ns_cbranch_vccz flok_x3\n"
"s_add_u32 s53, s53, 0\ns_add_u32 s49, s49, 1\ns_cmp_gt_u32 s49, 65536\ns_cbranch_scc1 end_x3\n"
"s_sleep 2\nbuffer_load_dword v81, v18, s[8:11], 0 offen sc1\ns_waitcnt vmcnt(0)\ns_branch fl_x3\n"
"flok_x3:\ns_mul_i32 s48, s46, 8\ns_add_u32 s45, s45, s48\ns_add_u32 s45, s45, s32\n"
"s_lshr_b32 s48, s45, 4\ns_lshl_b32 s48, s48, 2\ns_add_u32 s44, s44, s48\ns_and_b32 s45, s45, 15\n"
"s_add_u32 s52, s40, 1\ns_bitcmp1_b64 s[28:29], s52\ns_subb_u32 s46, s46, 0\ns_add_u32 s40, s40, 1\n"
"s_mov_b32 s43, s41\ns_add_u32 s41, s41, 131072\ns_and_b32 s41, s41, 524287\ns_add_u32 s92, s92, 1\n"
"s_xor_b32 s42, s41, 262144\ns_waitcnt lgkmcnt(0)\ns_cmp_lt_u32 s40, 63\ns_cbranch_scc1 top_x3\n"
"s_branch end_x3\nretry2_x3:\ns_sleep 3\nretry_x3:\n"
"s_add_u32 s53, s53, 1\ns_add_u32 s49, s49, 1\ns_cmp_gt_u32 s49, 65536\ns_cbranch_scc1 end_x3\n"
"buffer_load_dwordx4 v[40:43], v10, s[8:11], s43 offen offset:0 sc1\nv_mov_b32 v74, 0\ns_add_u32 s47, s40, 1\nv_mov_b32 v79, s47\n"
"ds_write_b32 v80, v79\ns_waitcnt vmcnt(0)\nv_max3_u32 v74, v74, v40, v41\nv_max3_u32 v74, v74, v42, v43\n"
"v_cmp_eq_u32 vcc, -1, v74\nv_mov_b32 v44, 0\nv_mov_b32 v45, 0\nv_mov_b32 v46, 0\n"
"v_mov_b32 v47, 0\nv_mov_b32 v48, 0\nv_mov_b32 v49, 0\nv_mov_b32 v50, 0\n"
"v_mov_b32 v51, 0\ns_cbranch_vccnz retry2_x3\ns_branch landed_x3\nadopt_x3:\n"
"buffer_load_dword v56, v28, s[8:11], 0 offen sc1\ns_lshl_b32 s52, s40, 26\ns_or_b32 s47, s52, 0x3ffffff\ns_waitcnt vmcnt(0)\n"
"v_cmp_ge_u32 vcc, s47, v56\ns_nop 1\nv_cndmask_b32 v79, 0, v56, vcc\ns_nop 1\n"
"v_max_u32_dpp v79, v79, v79 row_ror:8 row_mask:0xf bank_mask:0xf\ns_nop 1\nv_max_u32_dpp v79, v79, v79 row_ror:4 row_mask:0xf bank_mask:0xf\ns_nop 1\n"
"v_max_u32_dpp v79, v79, v79 row_ror:2 row_mask:0xf bank_mask:0xf\ns_nop 1\nv_max_u32_dpp v79, v79, v79 row_ror:1 row_mask:0xf bank_mask:0xf\ns_nop 0\n"
"v_readlane_b32 s47, v79, 0\nv_readlane_b32 s48, v79, 16\ns_max_u32 s48, s48, s47\ns_cmp_le_u32 s48, s52\n"
"s_cbranch_scc1 back_x3\ns_bfe_u32 s52, s48, 1179656\ns_add_u32 s52, s52, s30\ns_sub_u32 s45, s52, s44\n"
"s_and_b32 s45, s45, 3\ns_sub_u32 s44, s52, s45\ns_lshl_b32 s45, s45, 2\ns_and_b32 s46, s48, 255\n"
"s_mov_b32 s92, 0\ns_branch back_x3\nend_x3:\ns_branch fk_done\n"
"fk_p0:\ns_mov_b32 s40, 0\ns_mov_b32 s41, 0\ns_mov_b32 s42, 262144\n"
"s_mov_b32 s43, 393216\ns_mov_b32 s44, s30\ns_mov_b32 s45, 0\ns_bcnt1_i32_b64 s46, s[28:29]\n"
"s_mov_b32 s92, 99\ns_mov_b32 s49, 0\ns_mov_b32 s53, 0\ns_mov_b32 s50, s36\n"
"s_mov_b32 s51, s37\nv_mov_b32 v222, 24576\ns_mov_b32 s62, 0x00ff00ff\ns_mov_b32 s63, 0x00ff00ff\n"
"v_cndmask_b32 v223, 0, -1, s[62:63]\nv_not_b32 v224, v223\nv_bfe_u32 v219, v9, 4, 1\nv_lshlrev_b32 v220, 4, v219\n"
"v_mov_b32 v218, 0x3c00\nv_lshlrev_b32 v218, v220, v218\nv_bfe_u32 v220, v9, 5, 3\nv_cmp_eq_u32 vcc, 0, v220\n"
"s_nop 1\nv_cndmask_b32 v168, 0, v218, vcc\nv_cmp_eq_u32 vcc, 2, v220\ns_nop 1\n"
"v_cndmask_b32 v169, 0, v218, vcc\nv_cmp_eq_u32 vcc, 4, v220\ns_nop 1\nv_cndmask_b32 v170, 0, v218, vcc\n"
"v_cmp_eq_u32 vcc, 6, v220\ns_nop 1\nv_cndmask_b32 v171, 0, v218, vcc\nv_cmp_eq_u32 vcc, 1, v220\n"
"s_nop 1\nv_cndmask_b32 v172, 0, v218, vcc\nv_cmp_eq_u32 vcc, 3, v220\ns_nop 1\n"
"v_cndmask_b32 v173, 0, v218, vcc\nv_cmp_eq_u32 vcc, 5, v220\ns_nop 1\nv_cndmask_b32 v174, 0, v218, vcc\n"
"v_cmp_eq_u32 vcc, 7, v220\ns_nop 1\nv_cndmask_b32 v175, 0, v218, vcc\nv_mov_b32 v218, 0x3f803f80\n"
"v_cmp_eq_u32 vcc, 0, v219\ns_nop 1\nv_cndmask_b32 v176, 0, v218, vcc\nv_cndmask_b32 v177, 0, v218, vcc\n"
"v_cndmask_b32 v178, 0, v218, vcc\nv_cndmask_b32 v179, 0, v218, vcc\nv_cndmask_b32 v214, 0, v218, vcc\nv_cmp_eq_u32 vcc, 1, v219\n"
"s_nop 1\nv_cndmask_b32 v180, 0, v218, vcc\nv_cndmask_b32 v181, 0, v218, vcc\nv_cndmask_b32 v182, 0, v218, vcc\n"
"v_cndmask_b32 v183, 0, v218, vcc\nv_cndmask_b32 v215, 0, v218, vcc\ns_sub_u32 s93, s39, 8\ns_lshl_b32 s47, s93, 1\n"
"s_add_u32 s47, s47, 0\ns_sub_u32 s48, s47, 7\ns_cmp_ge_u32 s47, 7\ns_cselect_b32 s47, s48, s47\n"
"s_lshl_b32 s47, s47, 10\nv_add_u32 v226, s47, v9\ns_lshl_b32 s47, s47, 1\ns_add_u32 s81, s47, s35\n"
"s_lshl_b32 s47, s93, 1\ns_add_u32 s47, s47, 1\ns_sub_u32 s48, s47, 7\ns_cmp_ge_u32 s47, 7\n"
"s_cselect_b32 s47, s48, s47\ns_lshl_b32 s47, s47, 10\nv_add_u32 v227, s47, v9\ns_lshl_b32 s47, s47, 1\n"
"s_add_u32 s82, s47, s35\ns_lshl_b32 s47, s93, 1\ns_add_u32 s47, s47, 2\ns_sub_u32 s48, s47, 7\n"
"s_cmp_ge_u32 s47, 7\ns_cselect_b32 s47, s48, s47\ns_lshl_b32 s47, s47, 10\nv_add_u32 v228, s47, v9\n"
"s_lshl_b32 s47, s47, 1\ns_add_u32 s83, s47, s35\ns_lshl_b32 s47, s93, 1\ns_add_u32 s47, s47, 3\n"
"s_sub_u32 s48, s47, 7\ns_cmp_ge_u32 s47, 7\ns_cselect_b32 s47, s48, s47\ns_lshl_b32 s47, s47, 10\n"
"v_add_u32 v229, s47, v9\ns_lshl_b32 s47, s47, 1\ns_add_u32 s84, s47, s35\ns_lshl_b32 s47, s93, 1\n"
"s_add_u32 s47, s47, 4\ns_sub_u32 s48, s47, 7\ns_cmp_ge_u32 s47, 7\ns_cselect_b32 s47, s48, s47\n"
"s_lshl_b32 s47, s47, 10\nv_add_u32 v230, s47, v9\ns_lshl_b32 s47, s47, 1\ns_add_u32 s85, s47, s35\n"
"s_lshl_b32 s47, s93, 1\ns_add_u32 s47, s47, 5\ns_sub_u32 s48, s47, 7\ns_cmp_ge_u32 s47, 7\n"
"s_cselect_b32 s47, s48, s47\ns_lshl_b32 s47, s47, 10\nv_add_u32 v231, s47, v9\ns_lshl_b32 s47, s47, 1\n"
"s_add_u32 s86, s47, s35\ns_lshl_b32 s47, s93, 1\ns_add_u32 s47, s47, 6\ns_sub_u32 s48, s47, 7\n"
"s_cmp_ge_u32 s47, 7\ns_cselect_b32 s47, s48, s47\ns_lshl_b32 s47, s47, 10\nv_add_u32 v232, s47, v9\n"
"s_lshl_b32 s47, s47, 1\ns_add_u32 s87, s47, s35\nbuffer_load_dword v218, v14, s[8:11], 0 offen sc1\ns_add_u32 s42, s35, 8192\n"
"s_mov_b32 s91, 0\nim_p0:\ns_waitcnt vmcnt(0) lgkmcnt(0)\nfw_p0:\n"
"s_add_u32 s47, s40, 0\ns_min_u32 s47, s47, 64\ns_add_u32 s47, s47, s34\nv_subrev_u32 v218, s47, v218\n"
"v_cmp_lt_u32 vcc, 64, v218\ns_cbranch_vccz fwok_p0\ns_add_u32 s53, s53, 0\ns_add_u32 s49, s49, 1\n"
"s_cmp_gt_u32 s49, 65536\ns_cbranch_scc1 end_p0\ns_sleep 2\nbuffer_load_dword v218, v14, s[8:11], 0 offen sc1\n"
"s_waitcnt vmcnt(0)\ns_branch fw_p0\nfwok_p0:\ns_cmp_ge_u32 s81, s42\n"
"s_cselect_b32 s47, 1, 0\ns_cmp_lg_u32 s47, s91\ns_cbranch_scc1 is0_p0\ns_add_u32 s47, s81, 0\n"
"buffer_load_dwordx4 a[0:3], v9, s[12:15], s47 offen sc1\nbuffer_load_dwordx4 a[4:7], v9, s[12:15], s47 offen offset:1024 sc1\ns_add_u32 s47, s81, 14336\nbuffer_load_dwordx4 a[56:59], v9, s[12:15], s47 offen sc1\n"
"buffer_load_dwordx4 a[60:63], v9, s[12:15], s47 offen offset:1024 sc1\ns_add_u32 s47, s81, 28672\nbuffer_load_dwordx4 a[112:115], v9, s[12:15], s47 offen sc1\nbuffer_load_dwordx4 a[116:119], v9, s[12:15], s47 offen offset:1024 sc1\n"
"s_add_u32 s47, s81, 43008\nbuffer_load_dwordx4 a[168:171], v9, s[12:15], s47 offen sc1\nbuffer_load_dwordx4 a[172:175], v9, s[12:15], s47 offen offset:1024 sc1\nis0_p0:\n"
"s_cmp_ge_u32 s82, s42\ns_cselect_b32 s47, 1, 0\ns_cmp_lg_u32 s47, s91\ns_cbranch_scc1 is1_p0\n"
"s_add_u32 s47, s82, 0\nbuffer_load_dwordx4 a[8:11], v9, s[12:15], s47 offen sc1\nbuffer_load_dwordx4 a[12:15], v9, s[12:15], s47 offen offset:1024 sc1\ns_add_u32 s47, s82, 14336\n"
"buffer_load_dwordx4 a[64:67], v9, s[12:15], s47 offen sc1\nbuffer_load_dwordx4 a[68:71], v9, s[12:15], s47 offen offset:1024 sc1\ns_add_u32 s47, s82, 28672\nbuffer_load_dwordx4 a[120:123], v9, s[12:15], s47 offen sc1\n"
"buffer_load_dwordx4 a[124:127], v9, s[12:15], s47 offen offset:1024 sc1\ns_add_u32 s47, s82, 43008\nbuffer_load_dwordx4 a[176:179], v9, s[12:15], s47 offen sc1\nbuffer_load_dwordx4 a[180:183], v9, s[12:15], s47 offen offset:1024 sc1\n"
"is1_p0:\ns_cmp_ge_u32 s83, s42\ns_cselect_b32 s47, 1, 0\ns_cmp_lg_u32 s47, s91\n"
"s_cbranch_scc1 is2_p0\ns_add_u32 s47, s83, 0\nbuffer_load_dwordx4 a[16:19], v9, s[12:15], s47 offen sc1\nbuffer_load_dwordx4 a[20:23], v9, s[12:15], s47 offen offset:1024 sc1\n"
"s_add_u32 s47, s83, 14336\nbuffer_load_dwordx4 a[72:75], v9, s[12:15], s47 offen sc1\nbuffer_load_dwordx4 a[76:79], v9, s[12:15], s47 offen offset:1024 sc1\ns_add_u32 s47, s83, 28672\n"
"buffer_load_dwordx4 a[128:131], v9, s[12:15], s47 offen sc1\nbuffer_load_dwordx4 a[132:135], v9, s[12:15], s47 offen offset:1024 sc1\ns_add_u32 s47, s83, 43008\nbuffer_load_dwordx4 a[184:187], v9, s[12:15], s47 offen sc1\n"
"buffer_load_dwordx4 a[188:191], v9, s[12:15], s47 offen offset:1024 sc1\nis2_p0:\ns_cmp_ge_u32 s84, s42\ns_cselect_b32 s47, 1, 0\n"
"s_cmp_lg_u32 s47, s91\ns_cbranch_scc1 is3_p0\ns_add_u32 s47, s84, 0\nbuffer_load_dwordx4 a[24:27], v9, s[12:15], s47 offen sc1\n"
"buffer_load_dwordx4 a[28:31], v9, s[12:15], s47 offen offset:1024 sc1\ns_add_u32 s47, s84, 14336\nbuffer_load_dwordx4 a[80:83], v9, s[12:15], s47 offen sc1\nbuffer_load_dwordx4 a[84:87], v9, s[12:15], s47 offen offset:1024 sc1\n"
"s_add_u32 s47, s84, 28672\nbuffer_load_dwordx4 a[136:139], v9, s[12:15], s47 offen sc1\nbuffer_load_dwordx4 a[140:143], v9, s[12:15], s47 offen offset:1024 sc1\ns_add_u32 s47, s84, 43008\n"
"buffer_load_dwordx4 a[192:195], v9, s[12:15], s47 offen sc1\nbuffer_load_dwordx4 a[196:199], v9, s[12:15], s47 offen offset:1024 sc1\nis3_p0:\ns_cmp_lg_u32 s91, 0\n"
"s_cbranch_scc1 warm_p0\ns_mul_i32 s47, s93, 2484\ns_getpc_b64 s[60:61]\ndp_p0:\n"
"s_add_u32 s47, s47, vb0_p0-dp_p0\ns_add_u32 s60, s60, s47\ns_addc_u32 s61, s61, 0\ns_mov_b64 exec, 0\n"
"s_setpc_b64 s[60:61]\nwarm_p0:\ns_mov_b64 exec, -1\ns_cmp_ge_u32 s85, s42\n"
"s_cselect_b32 s47, 1, 0\ns_cmp_lg_u32 s47, s91\ns_cbranch_scc1 is4_p0\ns_add_u32 s47, s85, 0\n"
"buffer_load_dwordx4 a[32:35], v9, s[12:15], s47 offen sc1\nbuffer_load_dwordx4 a[36:39], v9, s[12:15], s47 offen offset:1024 sc1\ns_add_u32 s47, s85, 14336\nbuffer_load_dwordx4 a[88:91], v9, s[12:15], s47 offen sc1\n"
"buffer_load_dwordx4 a[92:95], v9, s[12:15], s47 offen offset:1024 sc1\ns_add_u32 s47, s85, 28672\nbuffer_load_dwordx4 a[144:147], v9, s[12:15], s47 offen sc1\nbuffer_load_dwordx4 a[148:151], v9, s[12:15], s47 offen offset:1024 sc1\n"
"s_add_u32 s47, s85, 43008\nbuffer_load_dwordx4 a[200:203], v9, s[12:15], s47 offen sc1\nbuffer_load_dwordx4 a[204:207], v9, s[12:15], s47 offen offset:1024 sc1\nis4_p0:\n"
"s_cmp_ge_u32 s86, s42\ns_cselect_b32 s47, 1, 0\ns_cmp_lg_u32 s47, s91\ns_cbranch_scc1 is5_p0\n"
"s_add_u32 s47, s86, 0\nbuffer_load_dwordx4 a[40:43], v9, s[12:15], s47 offen sc1\nbuffer_load_dwordx4 a[44:47], v9, s[12:15], s47 offen offset:1024 sc1\ns_add_u32 s47, s86, 14336\n"
"buffer_load_dwordx4 a[96:99], v9, s[12:15], s47 offen sc1\nbuffer_load_dwordx4 a[100:103], v9, s[12:15], s47 offen offset:1024 sc1\ns_add_u32 s47, s86, 28672\nbuffer_load_dwordx4 a[152:155], v9, s[12:15], s47 offen sc1\n"
"buffer_load_dwordx4 a[156:159], v9, s[12:15], s47 offen offset:1024 sc1\ns_add_u32 s47, s86, 43008\nbuffer_load_dwordx4 a[208:211], v9, s[12:15], s47 offen sc1\nbuffer_load_dwordx4 a[212:215], v9, s[12:15], s47 offen offset:1024 sc1\n"
"is5_p0:\ns_cmp_ge_u32 s87, s42\ns_cselect_b32 s47, 1, 0\ns_cmp_lg_u32 s47, s91\n"
"s_cbranch_scc1 is6_p0\ns_add_u32 s47, s87, 0\nbuffer_load_dwordx4 a[48:51], v9, s[12:15], s47 offen sc1\nbuffer_load_dwordx4 a[52:55], v9, s[12:15], s47 offen offset:1024 sc1\n"
"s_add_u32 s47, s87, 14336\nbuffer_load_dwordx4 a[104:107], v9, s[12:15], s47 offen sc1\nbuffer_load_dwordx4 a[108:111], v9, s[12:15], s47 offen offset:1024 sc1\ns_add_u32 s47, s87, 28672\n"
"buffer_load_dwordx4 a[160:163], v9, s[12:15], s47 offen sc1\nbuffer_load_dwordx4 a[164:167], v9, s[12:15], s47 offen offset:1024 sc1\ns_add_u32 s47, s87, 43008\nbuffer_load_dwordx4 a[216:219], v9, s[12:15], s47 offen sc1\n"
"buffer_load_dwordx4 a[220:223], v9, s[12:15], s47 offen offset:1024 sc1\nis6_p0:\ns_cmp_lg_u32 s91, 0\ns_cbranch_scc1 id_p0\n"
"s_mov_b32 s91, 1\nv_add_u32 v14, 8192, v14\nbuffer_load_dword v218, v14, s[8:11], 0 offen sc1\ns_branch im_p0\n"
"id_p0:\nv_readfirstlane_b32 s93, v13\nv_lshlrev_b32 v166, 2, v9\ns_mov_b32 s52, 1064960\n"
"buffer_load_dword v164, v166, s[8:11], s52 offen sc1\ns_add_u32 s52, s52, 4096\nbuffer_load_dword v165, v166, s[8:11], s52 offen sc1\nbuffer_load_dword v218, v11, s[8:11], 0 offen sc1\n"
"s_mov_b64 s[94:95], 1\ns_barrier\ns_waitcnt vmcnt(0)\nar_p0:\n"
"v_xor_b32 v164, s34, v164\nv_xor_b32 v165, s34, v165\nv_or_b32 v164, v164, v165\nv_cmp_ne_u32 vcc, 0, v164\n"
"s_cbranch_vccz arok_p0\ns_add_u32 s53, s53, 0\ns_add_u32 s49, s49, 1\ns_cmp_gt_u32 s49, 65536\n"
"s_cbranch_scc1 end_p0\ns_sleep 2\nv_lshlrev_b32 v166, 2, v9\ns_mov_b32 s52, 1064960\n"
"buffer_load_dword v164, v166, s[8:11], s52 offen sc1\ns_add_u32 s52, s52, 4096\nbuffer_load_dword v165, v166, s[8:11], s52 offen sc1\ns_waitcnt vmcnt(0)\n"
"s_branch ar_p0\narok_p0:\nfl_p0:\ns_add_u32 s47, s40, 2\n"
"s_min_u32 s47, s47, 64\ns_add_u32 s47, s47, s34\nv_subrev_u32 v218, s47, v218\nv_cmp_lt_u32 vcc, 64, v218\n"
"s_cbranch_vccz flok_p0\ns_add_u32 s53, s53, 0\ns_add_u32 s49, s49, 1\ns_cmp_gt_u32 s49, 65536\n"
"s_cbranch_scc1 end_p0\ns_sleep 2\nbuffer_load_dword v218, v11, s[8:11], 0 offen sc1\ns_waitcnt vmcnt(0)\n"
"s_branch fl_p0\nflok_p0:\nbuffer_load_dwordx4 v[100:103], v9, s[16:19], s50 offen offset:0 sc1\nbuffer_load_dwordx4 v[104:107], v9, s[16:19], s50 offen offset:1024 sc1\n"
"buffer_load_dwordx4 v[108:111], v9, s[16:19], s50 offen offset:2048 sc1\nbuffer_load_dwordx4 v[112:115], v9, s[16:19], s50 offen offset:3072 sc1\ns_add_u32 s50, s50, 16384\ns_memrealtime s[54:55]\n"
"s_waitcnt lgkmcnt(0)\ns_add_u32 s44, s54, 12\ns_mov_b32 s45, 8\ns_mov_b32 s42, 1\n"
"s_mul_i32 s48, s45, 6\ns_and_b32 s48, s48, -4\ns_getpc_b64 s[88:89]\nmpc_p0:\n"
"s_sub_u32 s48, mgo_p0-mpc_p0, s48\ns_add_u32 s88, s88, s48\ns_addc_u32 s89, s89, 0\ns_branch mf_p0\n"
".p2align 6\ntop_p0:\nmf_p0:\ns_memrealtime s[54:55]\n"
"s_nop 2\ns_memrealtime s[56:57]\ns_nop 2\ns_memrealtime s[58:59]\n"
"mg_p0:\ns_waitcnt lgkmcnt(4)\ns_sub_u32 s47, s54, s44\ns_add_u32 s48, s47, 12\n"
"s_cmp_ge_u32 s48, 12\ns_cbranch_scc1 mo_p0\ns_memrealtime s[54:55]\ns_waitcnt lgkmcnt(4)\n"
"s_cmp_ge_u32 s56, s44\ns_cbranch_scc1 mx_p0\ns_memrealtime s[56:57]\ns_waitcnt lgkmcnt(4)\n"
"s_cmp_ge_u32 s58, s44\ns_cbranch_scc1 mx_p0\ns_memrealtime s[58:59]\ns_waitcnt lgkmcnt(4)\n"
"s_cmp_ge_u32 s54, s44\ns_cbranch_scc1 mx_p0\ns_memrealtime s[54:55]\ns_waitcnt lgkmcnt(4)\n"
"s_cmp_ge_u32 s56, s44\ns_cbranch_scc1 mx_p0\ns_memrealtime s[56:57]\ns_waitcnt lgkmcnt(4)\n"
"s_cmp_ge_u32 s58, s44\ns_cbranch_scc1 mx_p0\ns_memrealtime s[58:59]\ns_waitcnt lgkmcnt(4)\n"
"s_cmp_ge_u32 s54, s44\ns_cbranch_scc1 mx_p0\ns_memrealtime s[54:55]\ns_waitcnt lgkmcnt(4)\n"
"s_cmp_ge_u32 s56, s44\ns_cbranch_scc1 mx_p0\ns_memrealtime s[56:57]\ns_waitcnt lgkmcnt(4)\n"
"s_cmp_ge_u32 s58, s44\ns_cbranch_scc1 mx_p0\ns_memrealtime s[58:59]\ns_waitcnt lgkmcnt(4)\n"
"s_cmp_ge_u32 s54, s44\ns_cbranch_scc1 mx_p0\ns_memrealtime s[54:55]\ns_waitcnt lgkmcnt(4)\n"
"s_cmp_ge_u32 s56, s44\ns_cbranch_scc1 mx_p0\ns_memrealtime s[56:57]\ns_waitcnt lgkmcnt(4)\n"
"s_cmp_ge_u32 s58, s44\ns_cbranch_scc1 mx_p0\ns_memrealtime s[58:59]\ns_waitcnt lgkmcnt(4)\n"
"s_cmp_ge_u32 s54, s44\ns_cbranch_scc1 mx_p0\ns_memrealtime s[54:55]\ns_waitcnt lgkmcnt(4)\n"
"s_cmp_ge_u32 s56, s44\ns_cbranch_scc1 mx_p0\ns_memrealtime s[56:57]\ns_waitcnt lgkmcnt(4)\n"
"s_cmp_ge_u32 s58, s44\ns_cbranch_scc1 mx_p0\ns_memrealtime s[58:59]\ns_waitcnt lgkmcnt(4)\n"
"s_cmp_ge_u32 s54, s44\ns_cbranch_scc1 mx_p0\ns_memrealtime s[54:55]\ns_waitcnt lgkmcnt(4)\n"
"s_cmp_ge_u32 s56, s44\ns_cbranch_scc1 mx_p0\ns_memrealtime s[56:57]\ns_branch mx_p0\n"
"mo_p0:\ns_waitcnt lgkmcnt(0)\ns_cmp_lt_i32 s47, 0\ns_cbranch_scc1 mf_p0\n"
"s_branch mgo_p0\nmx_p0:\ns_setpc_b64 s[88:89]\ns_nop 0\n"
"s_nop 0\ns_nop 0\ns_nop 0\ns_nop 0\n"
"s_nop 0\ns_nop 0\ns_nop 0\ns_nop 0\n"
"s_nop 0\ns_nop 0\ns_nop 0\ns_nop 0\n"
"s_nop 0\ns_nop 0\ns_nop 0\ns_nop 0\n"
"s_nop 0\ns_nop 0\ns_nop 0\ns_nop 0\n"
"s_nop 0\ns_nop 0\ns_nop 0\nmgo_p0:\n"
"top2_p0:\nbuffer_load_dwordx4 v[40:43], v10, s[8:11], s43 offen sc1\nbuffer_load_dwordx4 v[44:47], v12, s[8:11], s43 offen sc1\nv_mov_b32 v68, 0\n"
"v_mov_b32 v69, 0\nv_mov_b32 v70, 0\nv_mov_b32 v71, 0\nv_mov_b32 v72, 0\n"
"v_mov_b32 v73, 0\nv_mov_b32 v74, 0\nv_mov_b32 v75, 0\nv_mov_b32 v76, 0\n"
"v_mov_b32 v77, 0\nv_mov_b32 v78, 0\nv_mov_b32 v79, 0\nv_mov_b32 v80, 0\n"
"v_mov_b32 v81, 0\nv_mov_b32 v82, 0\nv_mov_b32 v83, 0\nv_mov_b32 v218, 0\n"
"s_cmp_eq_u32 s42, 0\ns_cbranch_scc1 np_p0\ns_mov_b32 s42, 0\ns_and_b32 s47, s46, 15\n"
"s_mul_i32 s47, s47, 4\ns_add_u32 s47, s47, s31\ns_add_u32 s47, s47, s45\ns_and_b32 s91, s47, 15\n"
"s_lshr_b32 s47, s47, 4\ns_lshl_b32 s47, s47, 2\ns_add_u32 s90, s44, s47\ns_mul_i32 s47, s91, 6\n"
"s_add_u32 s47, s47, s93\ns_lshr_b32 s47, s47, 4\ns_mul_i32 s47, s47, 268\ns_getpc_b64 s[60:61]\n"
"ep1_p0:\ns_add_u32 s47, s47, sgo_p0-ep1_p0\ns_add_u32 s60, s60, s47\ns_addc_u32 s61, s61, 0\n"
"s_mul_i32 s48, s46, 8\ns_add_u32 s45, s45, s48\ns_add_u32 s45, s45, s32\ns_lshr_b32 s48, s45, 4\n"
"s_lshl_b32 s48, s48, 2\ns_add_u32 s44, s44, s48\ns_and_b32 s45, s45, 15\ns_add_u32 s52, s40, 1\n"
"s_bitcmp1_b64 s[28:29], s52\ns_subb_u32 s46, s46, 0\ns_mul_i32 s48, s45, 6\ns_and_b32 s48, s48, -4\n"
"s_getpc_b64 s[88:89]\nmpc1_p0:\ns_sub_u32 s48, mgo_p0-mpc1_p0, s48\ns_add_u32 s88, s88, s48\n"
"s_addc_u32 s89, s89, -1\nnp_p0:\ns_waitcnt vmcnt(1)\nds_write_b128 v226, v[40:43]\n"
"v_max3_u32 v218, v218, v40, v41\nv_max3_u32 v218, v218, v42, v43\ns_waitcnt vmcnt(0)\nds_write_b128 v227, v[44:47]\n"
"v_max3_u32 v218, v218, v44, v45\nv_max3_u32 v218, v218, v46, v47\nv_cmp_eq_u32 vcc, -1, v218\nv_smfmac_f32_16x16x64_bf16 v[68:71], v[40:43], a[0:7], v8\n"
"v_mov_b32 v84, v100\nv_mov_b32 v85, v101\nv_mov_b32 v86, v102\nv_mov_b32 v87, v103\n"
"v_mov_b32 v88, v104\nv_smfmac_f32_16x16x64_bf16 v[72:75], v[40:43], a[56:63], v8\nv_mov_b32 v89, v105\nv_mov_b32 v90, v106\n"
"v_mov_b32 v91, v107\nv_mov_b32 v92, v108\nv_mov_b32 v93, v109\ns_cbranch_vccnz retry1_p0\n"
"s_waitcnt lgkmcnt(0)\ns_barrier\nds_read_b32 v221, v222\nds_read_b128 v[48:51], v228\n"
"ds_read_b128 v[52:55], v229\nds_read_b128 v[56:59], v230\nds_read_b128 v[60:63], v231\nds_read_b128 v[64:67], v232\n"
"v_smfmac_f32_16x16x64_bf16 v[76:79], v[40:43], a[112:119], v8\nv_mov_b32 v94, v110\nv_smfmac_f32_16x16x64_bf16 v[80:83], v[40:43], a[168:175], v8\nv_mov_b32 v95, v111\n"
"v_smfmac_f32_16x16x64_bf16 v[68:71], v[44:47], a[8:15], v8\nv_mov_b32 v96, v112\nv_smfmac_f32_16x16x64_bf16 v[72:75], v[44:47], a[64:71], v8\nv_mov_b32 v97, v113\n"
"s_waitcnt lgkmcnt(0)\nv_readfirstlane_b32 s47, v221\ns_memrealtime s[54:55]\nv_smfmac_f32_16x16x64_bf16 v[76:79], v[44:47], a[120:127], v8\n"
"v_mov_b32 v98, v114\ns_memrealtime s[56:57]\nv_smfmac_f32_16x16x64_bf16 v[80:83], v[44:47], a[176:183], v8\nv_mov_b32 v99, v115\n"
"s_cmp_eq_u32 s40, 63\ns_cbranch_scc1 last_p0\ns_memrealtime s[58:59]\ns_sub_u32 s47, s47, s40\n"
"s_cmp_eq_u32 s47, 1\ns_cbranch_scc1 adopt_p0\ns_waitcnt lgkmcnt(4)\ns_sub_u32 s47, s54, s90\n"
"s_cmp_ge_i32 s47, 0\ns_cbranch_scc1 so_p0\ns_memrealtime s[54:55]\ngr_p0:\n"
"v_smfmac_f32_16x16x64_bf16 v[68:71], v[48:51], a[16:23], v8\ns_waitcnt lgkmcnt(4)\ns_cmp_ge_u32 s56, s90\ns_cbranch_scc1 sx1_p0\n"
"s_memrealtime s[56:57]\nv_smfmac_f32_16x16x64_bf16 v[72:75], v[48:51], a[72:79], v8\ns_waitcnt lgkmcnt(4)\ns_cmp_ge_u32 s58, s90\n"
"s_cbranch_scc1 sx2_p0\ns_memrealtime s[58:59]\nv_smfmac_f32_16x16x64_bf16 v[76:79], v[48:51], a[128:135], v8\ns_waitcnt lgkmcnt(4)\n"
"s_cmp_ge_u32 s54, s90\ns_cbranch_scc1 sx3_p0\ns_memrealtime s[54:55]\nv_smfmac_f32_16x16x64_bf16 v[80:83], v[48:51], a[184:191], v8\n"
"s_waitcnt lgkmcnt(4)\ns_cmp_ge_u32 s56, s90\ns_cbranch_scc1 sx4_p0\ns_memrealtime s[56:57]\n"
"v_smfmac_f32_16x16x64_bf16 v[68:71], v[52:55], a[24:31], v8\ns_waitcnt lgkmcnt(4)\ns_cmp_ge_u32 s58, s90\ns_cbranch_scc1 sx5_p0\n"
"s_memrealtime s[58:59]\nv_smfmac_f32_16x16x64_bf16 v[72:75], v[52:55], a[80:87], v8\ns_waitcnt lgkmcnt(4)\ns_cmp_ge_u32 s54, s90\n"
"s_cbranch_scc1 sx6_p0\ns_memrealtime s[54:55]\nv_smfmac_f32_16x16x64_bf16 v[76:79], v[52:55], a[136:143], v8\ns_waitcnt lgkmcnt(4)\n"
"s_cmp_ge_u32 s56, s90\ns_cbranch_scc1 sx7_p0\ns_memrealtime s[56:57]\nv_smfmac_f32_16x16x64_bf16 v[80:83], v[52:55], a[192:199], v8\n"
"s_waitcnt lgkmcnt(4)\ns_cmp_ge_u32 s58, s90\ns_cbranch_scc1 sx8_p0\ns_memrealtime s[58:59]\n"
"v_smfmac_f32_16x16x64_bf16 v[68:71], v[56:59], a[32:39], v8\ns_waitcnt lgkmcnt(4)\ns_cmp_ge_u32 s54, s90\ns_cbranch_scc1 sx9_p0\n"
"s_memrealtime s[54:55]\nv_smfmac_f32_16x16x64_bf16 v[72:75], v[56:59], a[88:95], v8\ns_waitcnt lgkmcnt(4)\ns_cmp_ge_u32 s56, s90\n"
"s_cbranch_scc1 sx10_p0\ns_memrealtime s[56:57]\nv_smfmac_f32_16x16x64_bf16 v[76:79], v[56:59], a[144:151], v8\ns_waitcnt lgkmcnt(4)\n"
"s_cmp_ge_u32 s58, s90\ns_cbranch_scc1 sx11_p0\ns_memrealtime s[58:59]\nv_smfmac_f32_16x16x64_bf16 v[80:83], v[56:59], a[200:207], v8\n"
"s_waitcnt lgkmcnt(4)\ns_cmp_ge_u32 s54, s90\ns_cbranch_scc1 sx12_p0\ns_memrealtime s[54:55]\n"
"v_smfmac_f32_16x16x64_bf16 v[68:71], v[60:63], a[40:47], v8\ns_waitcnt lgkmcnt(4)\ns_cmp_ge_u32 s56, s90\ns_cbranch_scc1 sx13_p0\n"
"s_memrealtime s[56:57]\nv_smfmac_f32_16x16x64_bf16 v[72:75], v[60:63], a[96:103], v8\ns_waitcnt lgkmcnt(4)\ns_cmp_ge_u32 s58, s90\n"
"s_cbranch_scc1 sx14_p0\ns_memrealtime s[58:59]\nv_smfmac_f32_16x16x64_bf16 v[76:79], v[60:63], a[152:159], v8\ns_waitcnt lgkmcnt(4)\n"
"s_cmp_ge_u32 s54, s90\ns_cbranch_scc1 sx15_p0\ns_memrealtime s[54:55]\nv_smfmac_f32_16x16x64_bf16 v[80:83], v[60:63], a[208:215], v8\n"
"s_waitcnt lgkmcnt(4)\ns_cmp_ge_u32 s56, s90\ns_cbranch_scc1 sx16_p0\ns_memrealtime s[56:57]\n"
"v_smfmac_f32_16x16x64_bf16 v[68:71], v[64:67], a[48:55], v8\ns_waitcnt lgkmcnt(4)\ns_cmp_ge_u32 s58, s90\ns_cbranch_scc1 sx17_p0\n"
"s_memrealtime s[58:59]\nv_smfmac_f32_16x16x64_bf16 v[72:75], v[64:67], a[104:111], v8\ns_waitcnt lgkmcnt(4)\ns_cmp_ge_u32 s54, s90\n"
"s_cbranch_scc1 sx18_p0\ns_memrealtime s[54:55]\nv_smfmac_f32_16x16x64_bf16 v[76:79], v[64:67], a[160:167], v8\ns_waitcnt lgkmcnt(4)\n"
"s_cmp_ge_u32 s56, s90\ns_cbranch_scc1 sx19_p0\ns_memrealtime s[56:57]\nv_smfmac_f32_16x16x64_bf16 v[80:83], v[64:67], a[216:223], v8\n"
"s_waitcnt lgkmcnt(4)\ns_cmp_ge_u32 s58, s90\ns_cbranch_scc1 sx20_p0\ns_memrealtime s[58:59]\n"
"sp_p0:\ns_waitcnt lgkmcnt(4)\ns_sub_u32 s47, s54, s90\ns_cmp_ge_i32 s47, 0\n"
"s_cbranch_scc1 sx20_p0\ns_memrealtime s[54:55]\ns_waitcnt lgkmcnt(4)\ns_sub_u32 s47, s56, s90\n"
"s_cmp_ge_i32 s47, 0\ns_cbranch_scc1 sx20_p0\ns_memrealtime s[56:57]\ns_waitcnt lgkmcnt(4)\n"
"s_sub_u32 s47, s58, s90\ns_cmp_ge_i32 s47, 0\ns_cbranch_scc1 sx20_p0\ns_memrealtime s[58:59]\n"
"s_waitcnt lgkmcnt(4)\ns_sub_u32 s47, s54, s90\ns_cmp_ge_i32 s47, 0\ns_cbranch_scc1 sx20_p0\n"
"s_memrealtime s[54:55]\ns_waitcnt lgkmcnt(4)\ns_sub_u32 s47, s56, s90\ns_cmp_ge_i32 s47, 0\n"
"s_cbranch_scc1 sx20_p0\ns_memrealtime s[56:57]\ns_waitcnt lgkmcnt(4)\ns_sub_u32 s47, s58, s90\n"
"s_cmp_ge_i32 s47, 0\ns_cbranch_scc1 sx20_p0\ns_memrealtime s[58:59]\ns_cbranch_execnz sp_p0\n"
"s_branch tl8_p0\nso_p0:\ns_cmp_gt_i32 s47, 4\ns_cbranch_scc1 late_p0\n"
"s_branch sgo_p0\ntl8_p0:\nv_smfmac_f32_16x16x64_bf16 v[68:71], v[56:59], a[32:39], v8\ntl9_p0:\n"
"v_smfmac_f32_16x16x64_bf16 v[72:75], v[56:59], a[88:95], v8\ntl10_p0:\nv_smfmac_f32_16x16x64_bf16 v[76:79], v[56:59], a[144:151], v8\ntl11_p0:\n"
"v_smfmac_f32_16x16x64_bf16 v[80:83], v[56:59], a[200:207], v8\ntl12_p0:\nv_smfmac_f32_16x16x64_bf16 v[68:71], v[60:63], a[40:47], v8\ntl13_p0:\n"
"v_smfmac_f32_16x16x64_bf16 v[72:75], v[60:63], a[96:103], v8\ntl14_p0:\nv_smfmac_f32_16x16x64_bf16 v[76:79], v[60:63], a[152:159], v8\ntl15_p0:\n"
"v_smfmac_f32_16x16x64_bf16 v[80:83], v[60:63], a[208:215], v8\ntl16_p0:\nv_smfmac_f32_16x16x64_bf16 v[68:71], v[64:67], a[48:55], v8\ntl17_p0:\n"
"v_smfmac_f32_16x16x64_bf16 v[72:75], v[64:67], a[104:111], v8\ntl18_p0:\nv_smfmac_f32_16x16x64_bf16 v[76:79], v[64:67], a[160:167], v8\ntl19_p0:\n"
"v_smfmac_f32_16x16x64_bf16 v[80:83], v[64:67], a[216:223], v8\ntl20_p0:\nv_add_f32 v219, v68, v69\nv_add_f32 v220, v70, v71\n"
"v_cvt_pk_bf16_f32 v156, v219, v220\ntl21_p0:\nv_add_f32 v219, v72, v73\nv_add_f32 v220, v74, v75\n"
"v_cvt_pk_bf16_f32 v157, v219, v220\ntl22_p0:\nv_add_f32 v219, v76, v77\nv_add_f32 v220, v78, v79\n"
"v_cvt_pk_bf16_f32 v158, v219, v220\ntl23_p0:\nv_add_f32 v219, v80, v81\nv_add_f32 v220, v82, v83\n"
"v_cvt_pk_bf16_f32 v159, v219, v220\ntl24_p0:\ns_cbranch_execz warm_p0\nbuffer_load_dwordx4 v[100:103], v9, s[16:19], s50 offen offset:0 sc1\n"
"buffer_load_dwordx4 v[104:107], v9, s[16:19], s50 offen offset:1024 sc1\nbuffer_load_dwordx4 v[108:111], v9, s[16:19], s50 offen offset:2048 sc1\nbuffer_load_dwordx4 v[112:115], v9, s[16:19], s50 offen offset:3072 sc1\ns_add_u32 s50, s50, 16384\n"
"buffer_store_dwordx4 v[156:159], v9, s[20:23], s51 offen sc1\ns_waitcnt vmcnt(8)\nv_mfma_f32_16x16x32_bf16 v[132:135], v[84:87], v[116:119], 0\nv_mfma_f32_16x16x32_bf16 v[136:139], v[88:91], v[116:119], 0\n"
"v_mfma_f32_16x16x32_bf16 v[148:151], v[176:179], v[116:119], 0\ns_waitcnt vmcnt(7)\nv_mfma_f32_16x16x32_bf16 v[140:143], v[92:95], v[120:123], 0\nv_mfma_f32_16x16x32_bf16 v[144:147], v[96:99], v[120:123], 0\n"
"v_mfma_f32_16x16x32_bf16 v[148:151], v[180:183], v[120:123], v[148:151]\nv_cndmask_b32 v132, v136, v132, s[62:63]\nv_cndmask_b32 v133, v137, v133, s[62:63]\nv_cndmask_b32 v134, v138, v134, s[62:63]\n"
"v_cndmask_b32 v135, v139, v135, s[62:63]\ns_waitcnt vmcnt(6)\nv_mfma_f32_16x16x32_f16 v[152:155], v[168:171], v[124:127], 0\ns_waitcnt vmcnt(5)\n"
"v_mfma_f32_16x16x32_f16 v[152:155], v[172:175], v[128:131], v[152:155]\nv_cndmask_b32 v140, v144, v140, s[62:63]\nv_cndmask_b32 v141, v145, v141, s[62:63]\nv_cndmask_b32 v142, v146, v142, s[62:63]\n"
"v_cndmask_b32 v143, v147, v143, s[62:63]\ns_nop 2\nv_exp_f32 v152, v152\nv_exp_f32 v153, v153\n"
"v_exp_f32 v154, v154\nv_exp_f32 v155, v155\nv_cvt_pk_bf16_f32 v200, v152, v153\nv_cvt_pk_bf16_f32 v201, v154, v155\n"
"v_and_b32 v160, v200, v223\nv_and_b32 v161, v200, v224\nv_mfma_f32_16x16x16_bf16 v[148:151], v[214:215], v[200:201], v[148:151]\nv_and_b32 v162, v201, v223\n"
"v_and_b32 v163, v201, v224\nv_mfma_f32_16x16x16_bf16 v[132:135], v[156:157], v[160:161], v[132:135]\ns_nop 0\nv_mfma_f32_16x16x16_bf16 v[140:143], v[158:159], v[162:163], v[140:143]\n"
"v_rcp_f32 v202, v148\nv_rcp_f32 v203, v149\nv_mul_f32 v132, v132, v202\nv_mul_f32 v133, v133, v202\n"
"v_mul_f32 v134, v134, v202\nv_mul_f32 v135, v135, v202\nv_fmac_f32 v132, v140, v203\nv_fmac_f32 v133, v141, v203\n"
"v_fmac_f32 v134, v142, v203\nv_fmac_f32 v135, v143, v203\nv_add_f32_dpp v204, v132, v132 row_ror:8 row_mask:0xf bank_mask:0x3\nv_add_f32_dpp v205, v133, v133 row_ror:8 row_mask:0xf bank_mask:0x3\n"
"v_add_f32_dpp v204, v134, v134 row_ror:8 row_mask:0xf bank_mask:0xc\nv_add_f32_dpp v205, v135, v135 row_ror:8 row_mask:0xf bank_mask:0xc\nv_subrev_u32 v219, 0x23800000, v148\nv_subrev_u32 v220, 0x23800000, v149\n"
"v_max_u32 v219, v219, v220\nv_cmp_le_u32 vcc, 0x38000000, v219\nds_write_b64 v22, v[204:205]\ns_or_b64 s[56:57], vcc, s[94:95]\n"
"s_cbranch_scc1 slow_p0\njoin_p0:\nv_mov_b32 v219, 0\ns_nop 1\n"
"v_mov_b32 v219, 0\ns_waitcnt lgkmcnt(0)\ns_barrier\nds_read_b64 v[206:207], v23 offset:0\n"
"ds_read_b64 v[208:209], v23 offset:1280\nds_read_b64 v[210:211], v23 offset:2560\nds_read_b64 v[212:213], v23 offset:3840\nds_read_b32 v221, v222\n"
"v_mov_b32 v219, 0\ns_add_u32 s40, s40, 1\ns_mov_b32 s43, s41\ns_add_u32 s41, s41, 131072\n"
"v_mov_b32 v219, 0\ns_and_b32 s41, s41, 524287\ns_add_u32 s92, s92, 1\ns_add_u32 s51, s51, 4096\n"
"v_mov_b32 v219, 0\ns_mov_b32 s42, 1\ns_waitcnt lgkmcnt(3)\nv_pk_add_f32 v[206:207], v[206:207], v[208:209]\n"
"s_waitcnt lgkmcnt(1)\nv_pk_add_f32 v[210:211], v[210:211], v[212:213]\nv_pk_add_f32 v[206:207], v[206:207], v[210:211]\nv_cvt_pk_bf16_f32 v220, v206, v207\n"
"buffer_store_dword v220, v16, s[8:11], s43 offen sc1\ns_waitcnt lgkmcnt(0)\ns_memrealtime s[54:55]\nv_readfirstlane_b32 s47, v221\n"
"s_nop 1\ns_memrealtime s[56:57]\ns_sub_u32 s47, s47, s40\ns_cmp_lg_u32 s47, 1\n"
"s_memrealtime s[58:59]\ns_cbranch_scc1 mg_p0\ns_lshl_b32 s52, s40, 26\ns_cmp_le_u32 s92, 2\n"
"s_cselect_b32 s48, s33, s38\ns_waitcnt lgkmcnt(0)\ns_sub_u32 s47, s54, s30\ns_add_u32 s47, s47, s48\n"
"s_min_u32 s47, s47, 0x3ffff\ns_lshl_b32 s47, s47, 8\ns_or_b32 s47, s47, s52\ns_sub_u32 s52, s92, 2\n"
"s_cmp_lt_u32 s52, 6\ns_cselect_b32 s48, 1, 0\ns_cmp_gt_u32 s40, 12\ns_cselect_b32 s48, s48, 0\n"
"s_add_u32 s48, s48, s46\ns_min_u32 s48, s48, 15\ns_or_b32 s47, s47, s48\nv_mov_b32 v225, s47\n"
"buffer_store_dword v225, v19, s[8:11], 0 offen sc1\ns_cmp_lg_u32 s40, 1\ns_cbranch_scc1 rec_p0\nbuffer_load_dword v218, v11, s[8:11], 0 offen sc1\n"
"s_waitcnt vmcnt(0)\nf1_p0:\ns_add_u32 s47, s40, 2\ns_min_u32 s47, s47, 64\n"
"s_add_u32 s47, s47, s34\nv_subrev_u32 v218, s47, v218\nv_cmp_lt_u32 vcc, 64, v218\ns_cbranch_vccz f1ok_p0\n"
"s_add_u32 s53, s53, 0\ns_add_u32 s49, s49, 1\ns_cmp_gt_u32 s49, 65536\ns_cbranch_scc1 end_p0\n"
"s_sleep 2\nbuffer_load_dword v218, v11, s[8:11], 0 offen sc1\ns_waitcnt vmcnt(0)\ns_branch f1_p0\n"
"f1ok_p0:\ns_branch top2_p0\nlast_p0:\nv_smfmac_f32_16x16x64_bf16 v[68:71], v[48:51], a[16:23], v8\n"
"v_smfmac_f32_16x16x64_bf16 v[72:75], v[48:51], a[72:79], v8\nv_smfmac_f32_16x16x64_bf16 v[76:79], v[48:51], a[128:135], v8\nv_smfmac_f32_16x16x64_bf16 v[80:83], v[48:51], a[184:191], v8\nv_smfmac_f32_16x16x64_bf16 v[68:71], v[52:55], a[24:31], v8\n"
"v_smfmac_f32_16x16x64_bf16 v[72:75], v[52:55], a[80:87], v8\nv_smfmac_f32_16x16x64_bf16 v[76:79], v[52:55], a[136:143], v8\nv_smfmac_f32_16x16x64_bf16 v[80:83], v[52:55], a[192:199], v8\nv_smfmac_f32_16x16x64_bf16 v[68:71], v[56:59], a[32:39], v8\n"
"v_smfmac_f32_16x16x64_bf16 v[72:75], v[56:59], a[88:95], v8\nv_smfmac_f32_16x16x64_bf16 v[76:79], v[56:59], a[144:151], v8\nv_smfmac_f32_16x16x64_bf16 v[80:83], v[56:59], a[200:207], v8\nv_smfmac_f32_16x16x64_bf16 v[68:71], v[60:63], a[40:47], v8\n"
"v_smfmac_f32_16x16x64_bf16 v[72:75], v[60:63], a[96:103], v8\nv_smfmac_f32_16x16x64_bf16 v[76:79], v[60:63], a[152:159], v8\nv_smfmac_f32_16x16x64_bf16 v[80:83], v[60:63], a[208:215], v8\nv_smfmac_f32_16x16x64_bf16 v[68:71], v[64:67], a[48:55], v8\n"
"v_smfmac_f32_16x16x64_bf16 v[72:75], v[64:67], a[104:111], v8\nv_smfmac_f32_16x16x64_bf16 v[76:79], v[64:67], a[160:167], v8\nv_smfmac_f32_16x16x64_bf16 v[80:83], v[64:67], a[216:223], v8\nv_add_f32 v219, v68, v69\n"
"v_add_f32 v220, v70, v71\nv_cvt_pk_bf16_f32 v156, v219, v220\nv_add_f32 v219, v72, v73\nv_add_f32 v220, v74, v75\n"
"v_cvt_pk_bf16_f32 v157, v219, v220\nv_add_f32 v219, v76, v77\nv_add_f32 v220, v78, v79\nv_cvt_pk_bf16_f32 v158, v219, v220\n"
"v_add_f32 v219, v80, v81\nv_add_f32 v220, v82, v83\nv_cvt_pk_bf16_f32 v159, v219, v220\nbuffer_store_dwordx4 v[156:159], v9, s[20:23], s51 offen sc1\n"
"s_add_u32 s47, s34, 64\nv_mov_b32 v219, s47\ns_waitcnt vmcnt(0)\nbuffer_store_dword v219, v18, s[8:11], 0 offen sc1\n"
"v_mov_b32 v220, s34\nbuffer_store_dword v220, v15, s[8:11], 0 offen sc1\ns_branch end_p0\nretry1_p0:\n"
"s_add_u32 s53, s53, 256\ns_add_u32 s49, s49, 1\ns_cmp_gt_u32 s49, 65536\ns_cbranch_scc1 end_p0\n"
"s_add_u32 s47, s40, 1\nv_mov_b32 v221, s47\nds_write_b32 v222, v221\ns_sleep 2\n"
"s_branch top2_p0\nrec_p0:\ns_sleep 6\ns_branch top2_p0\n"
"adopt_p0:\nbuffer_load_dword v164, v28, s[8:11], 0 offen sc1\nv_smfmac_f32_16x16x64_bf16 v[68:71], v[48:51], a[16:23], v8\nv_smfmac_f32_16x16x64_bf16 v[72:75], v[48:51], a[72:79], v8\n"
"v_smfmac_f32_16x16x64_bf16 v[76:79], v[48:51], a[128:135], v8\nv_smfmac_f32_16x16x64_bf16 v[80:83], v[48:51], a[184:191], v8\nv_smfmac_f32_16x16x64_bf16 v[68:71], v[52:55], a[24:31], v8\nv_smfmac_f32_16x16x64_bf16 v[72:75], v[52:55], a[80:87], v8\n"
"v_smfmac_f32_16x16x64_bf16 v[76:79], v[52:55], a[136:143], v8\nv_smfmac_f32_16x16x64_bf16 v[80:83], v[52:55], a[192:199], v8\nv_smfmac_f32_16x16x64_bf16 v[68:71], v[56:59], a[32:39], v8\nv_smfmac_f32_16x16x64_bf16 v[72:75], v[56:59], a[88:95], v8\n"
"v_smfmac_f32_16x16x64_bf16 v[76:79], v[56:59], a[144:151], v8\nv_smfmac_f32_16x16x64_bf16 v[80:83], v[56:59], a[200:207], v8\nv_smfmac_f32_16x16x64_bf16 v[68:71], v[60:63], a[40:47], v8\nv_smfmac_f32_16x16x64_bf16 v[72:75], v[60:63], a[96:103], v8\n"
"v_smfmac_f32_16x16x64_bf16 v[76:79], v[60:63], a[152:159], v8\nv_smfmac_f32_16x16x64_bf16 v[80:83], v[60:63], a[208:215], v8\nv_smfmac_f32_16x16x64_bf16 v[68:71], v[64:67], a[48:55], v8\nv_smfmac_f32_16x16x64_bf16 v[72:75], v[64:67], a[104:111], v8\n"
"v_smfmac_f32_16x16x64_bf16 v[76:79], v[64:67], a[160:167], v8\nv_smfmac_f32_16x16x64_bf16 v[80:83], v[64:67], a[216:223], v8\ns_lshl_b32 s52, s40, 26\ns_or_b32 s47, s52, 0x3ffffff\n"
"s_waitcnt vmcnt(0)\nv_cmp_ge_u32 vcc, s47, v164\ns_nop 1\nv_cndmask_b32 v221, 0, v164, vcc\n"
"s_nop 1\nv_max_u32_dpp v221, v221, v221 row_ror:8 row_mask:0xf bank_mask:0xf\ns_nop 1\nv_max_u32_dpp v221, v221, v221 row_ror:4 row_mask:0xf bank_mask:0xf\n"
"s_nop 1\nv_max_u32_dpp v221, v221, v221 row_ror:2 row_mask:0xf bank_mask:0xf\ns_nop 1\nv_max_u32_dpp v221, v221, v221 row_ror:1 row_mask:0xf bank_mask:0xf\n"
"s_nop 0\nv_readlane_b32 s47, v221, 0\nv_readlane_b32 s48, v221, 16\ns_max_u32 s48, s48, s47\n"
"s_cmp_le_u32 s48, s52\ns_cbranch_scc1 back3_p0\ns_bfe_u32 s52, s48, 1179656\ns_add_u32 s52, s52, s30\n"
"s_sub_u32 s45, s52, s44\ns_and_b32 s45, s45, 3\ns_sub_u32 s44, s52, s45\ns_lshl_b32 s45, s45, 2\n"
"s_and_b32 s46, s48, 255\ns_mov_b32 s92, 0\ns_branch back2_p0\nback2_p0:\n"
"s_and_b32 s47, s46, 15\ns_mul_i32 s47, s47, 4\ns_add_u32 s47, s47, s31\ns_add_u32 s47, s47, s45\n"
"s_and_b32 s91, s47, 15\ns_lshr_b32 s47, s47, 4\ns_lshl_b32 s47, s47, 2\ns_add_u32 s90, s44, s47\n"
"s_mul_i32 s47, s91, 6\ns_add_u32 s47, s47, s93\ns_lshr_b32 s47, s47, 4\ns_mul_i32 s47, s47, 268\n"
"s_getpc_b64 s[60:61]\nep2_p0:\ns_add_u32 s47, s47, sgo_p0-ep2_p0\ns_add_u32 s60, s60, s47\n"
"s_addc_u32 s61, s61, 0\ns_mul_i32 s48, s46, 8\ns_add_u32 s45, s45, s48\ns_add_u32 s45, s45, s32\n"
"s_lshr_b32 s48, s45, 4\ns_lshl_b32 s48, s48, 2\ns_add_u32 s44, s44, s48\ns_and_b32 s45, s45, 15\n"
"s_add_u32 s52, s40, 1\ns_bitcmp1_b64 s[28:29], s52\ns_subb_u32 s46, s46, 0\ns_mul_i32 s48, s45, 6\n"
"s_and_b32 s48, s48, -4\ns_getpc_b64 s[88:89]\nmpc2_p0:\ns_sub_u32 s48, mgo_p0-mpc2_p0, s48\n"
"s_add_u32 s88, s88, s48\ns_addc_u32 s89, s89, -1\nback3_p0:\ns_waitcnt lgkmcnt(0)\n"
"s_memrealtime s[54:55]\ns_nop 2\ns_memrealtime s[56:57]\ns_nop 2\n"
"s_memrealtime s[58:59]\ns_waitcnt lgkmcnt(0)\ns_sub_u32 s47, s58, s90\ns_cmp_lt_i32 s47, 0\n"
"s_cbranch_scc1 sp_p0\ns_cmp_le_i32 s47, 0\ns_cbranch_scc1 sgn_p0\ns_add_u32 s53, s53, 16777216\n"
"s_mov_b64 s[94:95], 1\ns_add_u32 s52, s90, 32\nlw2_p0:\ns_memrealtime s[54:55]\n"
"s_waitcnt lgkmcnt(0)\ns_sub_u32 s47, s54, s52\ns_cmp_lt_i32 s47, 0\ns_cbranch_scc1 lw2_p0\n"
"s_branch sgn_p0\nlate_p0:\ns_add_u32 s53, s53, 16777216\ns_mov_b64 s[94:95], 1\n"
"s_add_u32 s52, s90, 32\nlw_p0:\ns_memrealtime s[54:55]\ns_waitcnt lgkmcnt(0)\n"
"s_sub_u32 s47, s54, s52\ns_cmp_lt_i32 s47, 0\ns_cbranch_scc1 lw_p0\ns_branch sgo_p0\n"
"slow_p0:\ns_mov_b64 s[94:95], 0\ns_cbranch_vccnz slow2_p0\ns_add_u32 s47, s40, 2\n"
"v_mov_b32 v221, s47\nds_write_b32 v222, v221\ns_branch join_p0\nslow2_p0:\n"
"v_max3_u32 v218, v124, v125, v126\nv_max_u32 v218, v218, v127\nv_max3_u32 v218, v218, v128, v129\nv_max3_u32 v218, v218, v130, v131\n"
"v_max3_u32 v218, v218, v116, v117\nv_max3_u32 v218, v218, v118, v119\nv_max3_u32 v218, v218, v120, v121\nv_max3_u32 v218, v218, v122, v123\n"
"v_cmp_eq_u32 vcc, -1, v218\ns_cbranch_vccz exact_p0\ns_branch spin_p0\nspin2_p0:\n"
"s_sleep 3\nspin_p0:\ns_add_u32 s53, s53, 1\ns_add_u32 s49, s49, 1\n"
"s_cmp_gt_u32 s49, 65536\ns_cbranch_scc1 end_p0\nbuffer_load_dwordx4 v[116:119], v20, s[8:11], s41 offen offset:0 sc1\nbuffer_load_dwordx4 v[120:123], v20, s[8:11], s41 offen offset:1024 sc1\n"
"buffer_load_dwordx4 v[124:127], v21, s[8:11], s41 offen offset:0 sc1\nbuffer_load_dwordx4 v[128:131], v21, s[8:11], s41 offen offset:1024 sc1\ns_waitcnt vmcnt(0)\nv_max3_u32 v218, v124, v125, v126\n"
"v_max_u32 v218, v218, v127\nv_max3_u32 v218, v218, v128, v129\nv_max3_u32 v218, v218, v130, v131\nv_max3_u32 v218, v218, v116, v117\n"
"v_max3_u32 v218, v218, v118, v119\nv_max3_u32 v218, v218, v120, v121\nv_max3_u32 v218, v218, v122, v123\nv_cmp_eq_u32 vcc, -1, v218\n"
"s_cbranch_vccnz spin2_p0\ns_add_u32 s47, s40, 2\nv_mov_b32 v221, s47\nds_write_b32 v222, v221\n"
"v_mfma_f32_16x16x32_bf16 v[132:135], v[84:87], v[116:119], 0\nv_mfma_f32_16x16x32_bf16 v[136:139], v[88:91], v[116:119], 0\nv_mfma_f32_16x16x32_bf16 v[148:151], v[176:179], v[116:119], 0\nv_mfma_f32_16x16x32_bf16 v[140:143], v[92:95], v[120:123], 0\n"
"v_mfma_f32_16x16x32_bf16 v[144:147], v[96:99], v[120:123], 0\nv_mfma_f32_16x16x32_bf16 v[148:151], v[180:183], v[120:123], v[148:151]\nv_cndmask_b32 v132, v136, v132, s[62:63]\nv_cndmask_b32 v133, v137, v133, s[62:63]\n"
"v_cndmask_b32 v134, v138, v134, s[62:63]\nv_cndmask_b32 v135, v139, v135, s[62:63]\nv_mfma_f32_16x16x32_f16 v[152:155], v[168:171], v[124:127], 0\nv_mfma_f32_16x16x32_f16 v[152:155], v[172:175], v[128:131], v[152:155]\n"
"v_cndmask_b32 v140, v144, v140, s[62:63]\nv_cndmask_b32 v141, v145, v141, s[62:63]\nv_cndmask_b32 v142, v146, v142, s[62:63]\nv_cndmask_b32 v143, v147, v143, s[62:63]\n"
"s_nop 2\nv_exp_f32 v152, v152\nv_exp_f32 v153, v153\nv_exp_f32 v154, v154\n"
"v_exp_f32 v155, v155\nv_cvt_pk_bf16_f32 v200, v152, v153\nv_cvt_pk_bf16_f32 v201, v154, v155\nv_and_b32 v160, v200, v223\n"
"v_and_b32 v161, v200, v224\nv_mfma_f32_16x16x16_bf16 v[148:151], v[214:215], v[200:201], v[148:151]\nv_and_b32 v162, v201, v223\nv_and_b32 v163, v201, v224\n"
"v_mfma_f32_16x16x16_bf16 v[132:135], v[156:157], v[160:161], v[132:135]\ns_nop 0\nv_mfma_f32_16x16x16_bf16 v[140:143], v[158:159], v[162:163], v[140:143]\nv_rcp_f32 v202, v148\n"
"v_rcp_f32 v203, v149\nv_mul_f32 v132, v132, v202\nv_mul_f32 v133, v133, v202\nv_mul_f32 v134, v134, v202\n"
"v_mul_f32 v135, v135, v202\nv_fmac_f32 v132, v140, v203\nv_fmac_f32 v133, v141, v203\nv_fmac_f32 v134, v142, v203\n"
"v_fmac_f32 v135, v143, v203\nv_add_f32_dpp v204, v132, v132 row_ror:8 row_mask:0xf bank_mask:0x3\nv_add_f32_dpp v205, v133, v133 row_ror:8 row_mask:0xf bank_mask:0x3\nv_add_f32_dpp v204, v134, v134 row_ror:8 row_mask:0xf bank_mask:0xc\n"
"v_add_f32_dpp v205, v135, v135 row_ror:8 row_mask:0xf bank_mask:0xc\nv_subrev_u32 v219, 0x23800000, v148\nv_subrev_u32 v220, 0x23800000, v149\nv_max_u32 v219, v219, v220\n"
"v_cmp_le_u32 vcc, 0x38000000, v219\nds_write_b64 v22, v[204:205]\ns_cbranch_vccz join_p0\nexact_p0:\n"
"s_add_u32 s53, s53, 65536\nex0_p0:\ns_add_u32 s47, s41, 0\nbuffer_load_dwordx2 v[184:185], v24, s[8:11], s47 offen offset:0 sc1\n"
"buffer_load_dwordx2 v[186:187], v24, s[8:11], s47 offen offset:512 sc1\nbuffer_load_dwordx2 v[188:189], v24, s[8:11], s47 offen offset:1024 sc1\nbuffer_load_dwordx2 v[190:191], v24, s[8:11], s47 offen offset:1536 sc1\nbuffer_load_dwordx4 v[192:195], v25, s[8:11], s47 offen sc1\n"
"buffer_load_dwordx4 v[196:199], v25, s[8:11], s47 offen offset:16 sc1\nv_mov_b32 v218, 0\ns_waitcnt vmcnt(0)\nv_max3_u32 v218, v218, v184, v185\n"
"v_max3_u32 v218, v218, v186, v187\nv_max3_u32 v218, v218, v188, v189\nv_max3_u32 v218, v218, v190, v191\nv_max3_u32 v218, v218, v192, v193\n"
"v_max3_u32 v218, v218, v194, v195\nv_max3_u32 v218, v218, v196, v197\nv_max3_u32 v218, v218, v198, v199\nv_cmp_eq_u32 vcc, -1, v218\n"
"s_cbranch_vccz exok0_p0\ns_add_u32 s53, s53, 0\ns_add_u32 s49, s49, 1\ns_cmp_gt_u32 s49, 65536\n"
"s_cbranch_scc1 end_p0\ns_branch ex0_p0\nexok0_p0:\nv_add_f32 v184, v184, v186\n"
"v_add_f32 v188, v188, v190\nv_add_f32 v184, v184, v188\nv_add_f32 v185, v185, v187\nv_add_f32 v189, v189, v191\n"
"v_add_f32 v185, v185, v189\nv_max_f32 v225, v184, v185\nv_max3_f32 v225, v225, v192, v193\nv_max3_f32 v225, v225, v194, v195\n"
"v_max3_f32 v225, v225, v196, v197\nv_max3_f32 v225, v225, v198, v199\nv_mov_b32 v219, v225\ns_nop 1\n"
"v_permlane16_swap_b32 v225, v219\nv_max_f32 v225, v225, v219\nv_mov_b32 v219, v225\ns_nop 1\n"
"v_permlane32_swap_b32 v225, v219\nv_max_f32 v225, v225, v219\nv_sub_f32 v184, v184, v225\nv_exp_f32 v152, v184\n"
"v_sub_f32 v185, v185, v225\nv_exp_f32 v153, v185\nv_sub_f32 v192, v192, v225\nv_exp_f32 v192, v192\n"
"v_sub_f32 v193, v193, v225\nv_exp_f32 v193, v193\nv_sub_f32 v194, v194, v225\nv_exp_f32 v194, v194\n"
"v_sub_f32 v195, v195, v225\nv_exp_f32 v195, v195\nv_sub_f32 v196, v196, v225\nv_exp_f32 v196, v196\n"
"v_sub_f32 v197, v197, v225\nv_exp_f32 v197, v197\nv_sub_f32 v198, v198, v225\nv_exp_f32 v198, v198\n"
"v_sub_f32 v199, v199, v225\nv_exp_f32 v199, v199\ns_nop 0\nv_cvt_pk_bf16_f32 v116, v192, v193\n"
"v_cvt_pk_bf16_f32 v117, v194, v195\nv_cvt_pk_bf16_f32 v118, v196, v197\nv_cvt_pk_bf16_f32 v119, v198, v199\nex1_p0:\n"
"s_add_u32 s47, s41, 4096\nbuffer_load_dwordx2 v[184:185], v24, s[8:11], s47 offen offset:0 sc1\nbuffer_load_dwordx2 v[186:187], v24, s[8:11], s47 offen offset:512 sc1\nbuffer_load_dwordx2 v[188:189], v24, s[8:11], s47 offen offset:1024 sc1\n"
"buffer_load_dwordx2 v[190:191], v24, s[8:11], s47 offen offset:1536 sc1\nbuffer_load_dwordx4 v[192:195], v25, s[8:11], s47 offen sc1\nbuffer_load_dwordx4 v[196:199], v25, s[8:11], s47 offen offset:16 sc1\nv_mov_b32 v218, 0\n"
"s_waitcnt vmcnt(0)\nv_max3_u32 v218, v218, v184, v185\nv_max3_u32 v218, v218, v186, v187\nv_max3_u32 v218, v218, v188, v189\n"
"v_max3_u32 v218, v218, v190, v191\nv_max3_u32 v218, v218, v192, v193\nv_max3_u32 v218, v218, v194, v195\nv_max3_u32 v218, v218, v196, v197\n"
"v_max3_u32 v218, v218, v198, v199\nv_cmp_eq_u32 vcc, -1, v218\ns_cbranch_vccz exok1_p0\ns_add_u32 s53, s53, 0\n"
"s_add_u32 s49, s49, 1\ns_cmp_gt_u32 s49, 65536\ns_cbranch_scc1 end_p0\ns_branch ex1_p0\n"
"exok1_p0:\nv_add_f32 v184, v184, v186\nv_add_f32 v188, v188, v190\nv_add_f32 v184, v184, v188\n"
"v_add_f32 v185, v185, v187\nv_add_f32 v189, v189, v191\nv_add_f32 v185, v185, v189\nv_max_f32 v225, v184, v185\n"
"v_max3_f32 v225, v225, v192, v193\nv_max3_f32 v225, v225, v194, v195\nv_max3_f32 v225, v225, v196, v197\nv_max3_f32 v225, v225, v198, v199\n"
"v_mov_b32 v219, v225\ns_nop 1\nv_permlane16_swap_b32 v225, v219\nv_max_f32 v225, v225, v219\n"
"v_mov_b32 v219, v225\ns_nop 1\nv_permlane32_swap_b32 v225, v219\nv_max_f32 v225, v225, v219\n"
"v_sub_f32 v184, v184, v225\nv_exp_f32 v154, v184\nv_sub_f32 v185, v185, v225\nv_exp_f32 v155, v185\n"
"v_sub_f32 v192, v192, v225\nv_exp_f32 v192, v192\nv_sub_f32 v193, v193, v225\nv_exp_f32 v193, v193\n"
"v_sub_f32 v194, v194, v225\nv_exp_f32 v194, v194\nv_sub_f32 v195, v195, v225\nv_exp_f32 v195, v195\n"
"v_sub_f32 v196, v196, v225\nv_exp_f32 v196, v196\nv_sub_f32 v197, v197, v225\nv_exp_f32 v197, v197\n"
"v_sub_f32 v198, v198, v225\nv_exp_f32 v198, v198\nv_sub_f32 v199, v199, v225\nv_exp_f32 v199, v199\n"
"s_nop 0\nv_cvt_pk_bf16_f32 v120, v192, v193\nv_cvt_pk_bf16_f32 v121, v194, v195\nv_cvt_pk_bf16_f32 v122, v196, v197\n"
"v_cvt_pk_bf16_f32 v123, v198, v199\ns_add_u32 s47, s40, 2\nv_mov_b32 v221, s47\nds_write_b32 v222, v221\n"
"v_mfma_f32_16x16x32_bf16 v[132:135], v[84:87], v[116:119], 0\nv_mfma_f32_16x16x32_bf16 v[136:139], v[88:91], v[116:119], 0\nv_mfma_f32_16x16x32_bf16 v[148:151], v[176:179], v[116:119], 0\nv_mfma_f32_16x16x32_bf16 v[140:143], v[92:95], v[120:123], 0\n"
"v_mfma_f32_16x16x32_bf16 v[144:147], v[96:99], v[120:123], 0\nv_mfma_f32_16x16x32_bf16 v[148:151], v[180:183], v[120:123], v[148:151]\nv_cndmask_b32 v132, v136, v132, s[62:63]\nv_cndmask_b32 v133, v137, v133, s[62:63]\n"
"v_cndmask_b32 v134, v138, v134, s[62:63]\nv_cndmask_b32 v135, v139, v135, s[62:63]\ns_nop 0\nv_cndmask_b32 v140, v144, v140, s[62:63]\n"
"v_cndmask_b32 v141, v145, v141, s[62:63]\nv_cndmask_b32 v142, v146, v142, s[62:63]\nv_cndmask_b32 v143, v147, v143, s[62:63]\nv_cvt_pk_bf16_f32 v200, v152, v153\n"
"v_cvt_pk_bf16_f32 v201, v154, v155\nv_and_b32 v160, v200, v223\nv_and_b32 v161, v200, v224\nv_mfma_f32_16x16x16_bf16 v[148:151], v[214:215], v[200:201], v[148:151]\n"
"v_and_b32 v162, v201, v223\nv_and_b32 v163, v201, v224\nv_mfma_f32_16x16x16_bf16 v[132:135], v[156:157], v[160:161], v[132:135]\ns_nop 0\n"
"v_mfma_f32_16x16x16_bf16 v[140:143], v[158:159], v[162:163], v[140:143]\nv_rcp_f32 v202, v148\nv_rcp_f32 v203, v149\nv_mul_f32 v132, v132, v202\n"
"v_mul_f32 v133, v133, v202\nv_mul_f32 v134, v134, v202\nv_mul_f32 v135, v135, v202\nv_fmac_f32 v132, v140, v203\n"
"v_fmac_f32 v133, v141, v203\nv_fmac_f32 v134, v142, v203\nv_fmac_f32 v135, v143, v203\nv_add_f32_dpp v204, v132, v132 row_ror:8 row_mask:0xf bank_mask:0x3\n"
"v_add_f32_dpp v205, v133, v133 row_ror:8 row_mask:0xf bank_mask:0x3\nv_add_f32_dpp v204, v134, v134 row_ror:8 row_mask:0xf bank_mask:0xc\nv_add_f32_dpp v205, v135, v135 row_ror:8 row_mask:0xf bank_mask:0xc\nds_write_b64 v22, v[204:205]\n"
"s_branch join_p0\n.p2align 6\nva0_p0:\ns_branch warm_p0\n"
"sx0_p0:\ns_add_u32 s60, s60, 276\ns_addc_u32 s61, s61, 0\ns_setpc_b64 s[60:61]\n"
"vb0_p0:\ns_nop 1\ns_nop 1\ns_nop 1\n"
"s_nop 1\ns_nop 1\ns_nop 1\ns_nop 1\n"
"s_nop 1\ns_nop 1\ns_nop 1\ns_nop 1\n"
"s_nop 1\ns_nop 1\ns_nop 1\ns_nop 1\n"
"s_nop 1\ns_nop 1\ns_nop 1\ns_nop 1\n"
"s_nop 1\ns_nop 1\ns_nop 1\nsgo_p0:\n"
"s_cbranch_execz vc0_p0\nbuffer_load_dwordx4 v[116:119], v20, s[8:11], s41 offen offset:0 sc1\nbuffer_load_dwordx4 v[120:123], v20, s[8:11], s41 offen offset:1024 sc1\nvc0_p0:\n"
"v_smfmac_f32_16x16x64_bf16 v[68:71], v[48:51], a[16:23], v8\nv_smfmac_f32_16x16x64_bf16 v[72:75], v[48:51], a[72:79], v8\nv_smfmac_f32_16x16x64_bf16 v[76:79], v[48:51], a[128:135], v8\nv_smfmac_f32_16x16x64_bf16 v[80:83], v[48:51], a[184:191], v8\n"
"v_smfmac_f32_16x16x64_bf16 v[68:71], v[52:55], a[24:31], v8\nv_smfmac_f32_16x16x64_bf16 v[72:75], v[52:55], a[80:87], v8\nv_smfmac_f32_16x16x64_bf16 v[76:79], v[52:55], a[136:143], v8\nv_smfmac_f32_16x16x64_bf16 v[80:83], v[52:55], a[192:199], v8\n"
"s_cbranch_execz vd0_p0\nbuffer_load_dwordx4 v[124:127], v21, s[8:11], s41 offen offset:0 sc1\nbuffer_load_dwordx4 v[128:131], v21, s[8:11], s41 offen offset:1024 sc1\nvd0_p0:\n"
"s_cbranch_execnz tl8_p0\ns_nop 0\ns_nop 0\ns_nop 0\n"
"s_nop 0\ns_nop 0\ns_nop 0\ns_nop 0\n"
"s_nop 0\ns_nop 0\ns_nop 0\ns_nop 0\n"
"s_nop 0\ns_nop 0\ns_nop 0\ns_nop 0\n"
"va1_p0:\ns_branch vb1_p0\nsx1_p0:\ns_add_u32 s60, s60, 276\n"
"s_addc_u32 s61, s61, 0\ns_setpc_b64 s[60:61]\nvb1_p0:\ns_nop 1\n"
"s_nop 1\ns_nop 1\ns_nop 1\ns_nop 1\n"
"s_nop 1\ns_nop 1\ns_nop 1\ns_nop 1\n"
"s_nop 1\ns_nop 1\ns_nop 1\ns_nop 1\n"
"s_nop 1\ns_nop 1\ns_nop 1\ns_nop 1\n"
"s_nop 1\ns_nop 1\ns_nop 1\nv_smfmac_f32_16x16x64_bf16 v[68:71], v[48:51], a[16:23], v8\n"
"s_cbranch_execz vc1_p0\nbuffer_load_dwordx4 v[116:119], v20, s[8:11], s41 offen offset:0 sc1\nbuffer_load_dwordx4 v[120:123], v20, s[8:11], s41 offen offset:1024 sc1\nvc1_p0:\n"
"v_smfmac_f32_16x16x64_bf16 v[72:75], v[48:51], a[72:79], v8\nv_smfmac_f32_16x16x64_bf16 v[76:79], v[48:51], a[128:135], v8\nv_smfmac_f32_16x16x64_bf16 v[80:83], v[48:51], a[184:191], v8\nv_smfmac_f32_16x16x64_bf16 v[68:71], v[52:55], a[24:31], v8\n"
"v_smfmac_f32_16x16x64_bf16 v[72:75], v[52:55], a[80:87], v8\nv_smfmac_f32_16x16x64_bf16 v[76:79], v[52:55], a[136:143], v8\nv_smfmac_f32_16x16x64_bf16 v[80:83], v[52:55], a[192:199], v8\nv_smfmac_f32_16x16x64_bf16 v[68:71], v[56:59], a[32:39], v8\n"
"s_cbranch_execz vd1_p0\nbuffer_load_dwordx4 v[124:127], v21, s[8:11], s41 offen offset:0 sc1\nbuffer_load_dwordx4 v[128:131], v21, s[8:11], s41 offen offset:1024 sc1\nvd1_p0:\n"
"s_cbranch_execnz tl9_p0\ns_nop 0\ns_nop 0\ns_nop 0\n"
"s_nop 0\ns_nop 0\ns_nop 0\ns_nop 0\n"
"s_nop 0\ns_nop 0\ns_nop 0\ns_nop 0\n"
"s_nop 0\ns_nop 0\ns_nop 0\ns_nop 0\n"
"va2_p0:\ns_branch vb2_p0\nsx2_p0:\ns_add_u32 s60, s60, 552\n"
"s_addc_u32 s61, s61, 0\ns_setpc_b64 s[60:61]\nvb2_p0:\ns_nop 1\n"
"s_nop 1\ns_nop 1\ns_nop 1\ns_nop 1\n"
"s_nop 1\ns_nop 1\ns_nop 1\ns_nop 1\n"
"s_nop 1\ns_nop 1\ns_nop 1\ns_nop 1\n"
"s_nop 1\ns_nop 1\ns_nop 1\ns_nop 1\n"
"s_nop 1\nv_smfmac_f32_16x16x64_bf16 v[68:71], v[48:51], a[16:23], v8\nv_smfmac_f32_16x16x64_bf16 v[72:75], v[48:51], a[72:79], v8\ns_cbranch_execz vc2_p0\n"
"buffer_load_dwordx4 v[116:119], v20, s[8:11], s41 offen offset:0 sc1\nbuffer_load_dwordx4 v[120:123], v20, s[8:11], s41 offen offset:1024 sc1\nvc2_p0:\nv_smfmac_f32_16x16x64_bf16 v[76:79], v[48:51], a[128:135], v8\n"
"v_smfmac_f32_16x16x64_bf16 v[80:83], v[48:51], a[184:191], v8\nv_smfmac_f32_16x16x64_bf16 v[68:71], v[52:55], a[24:31], v8\nv_smfmac_f32_16x16x64_bf16 v[72:75], v[52:55], a[80:87], v8\nv_smfmac_f32_16x16x64_bf16 v[76:79], v[52:55], a[136:143], v8\n"
"v_smfmac_f32_16x16x64_bf16 v[80:83], v[52:55], a[192:199], v8\nv_smfmac_f32_16x16x64_bf16 v[68:71], v[56:59], a[32:39], v8\nv_smfmac_f32_16x16x64_bf16 v[72:75], v[56:59], a[88:95], v8\ns_cbranch_execz vd2_p0\n"
"buffer_load_dwordx4 v[124:127], v21, s[8:11], s41 offen offset:0 sc1\nbuffer_load_dwordx4 v[128:131], v21, s[8:11], s41 offen offset:1024 sc1\nvd2_p0:\ns_cbranch_execnz tl10_p0\n"
"s_nop 0\ns_nop 0\ns_nop 0\ns_nop 0\n"
"s_nop 0\ns_nop 0\ns_nop 0\ns_nop 0\n"
"s_nop 0\ns_nop 0\ns_nop 0\ns_nop 0\n"
"s_nop 0\ns_nop 0\ns_nop 0\nva3_p0:\n"
"s_branch vb3_p0\nsx3_p0:\ns_add_u32 s60, s60, 828\ns_addc_u32 s61, s61, 0\n"
"s_setpc_b64 s[60:61]\nvb3_p0:\ns_nop 1\ns_nop 1\n"
"s_nop 1\ns_nop 1\ns_nop 1\ns_nop 1\n"
"s_nop 1\ns_nop 1\ns_nop 1\ns_nop 1\n"
"s_nop 1\ns_nop 1\ns_nop 1\ns_nop 1\n"
"s_nop 1\ns_nop 1\nv_smfmac_f32_16x16x64_bf16 v[68:71], v[48:51], a[16:23], v8\nv_smfmac_f32_16x16x64_bf16 v[72:75], v[48:51], a[72:79], v8\n"
"v_smfmac_f32_16x16x64_bf16 v[76:79], v[48:51], a[128:135], v8\ns_cbranch_execz vc3_p0\nbuffer_load_dwordx4 v[116:119], v20, s[8:11], s41 offen offset:0 sc1\nbuffer_load_dwordx4 v[120:123], v20, s[8:11], s41 offen offset:1024 sc1\n"
"vc3_p0:\nv_smfmac_f32_16x16x64_bf16 v[80:83], v[48:51], a[184:191], v8\nv_smfmac_f32_16x16x64_bf16 v[68:71], v[52:55], a[24:31], v8\nv_smfmac_f32_16x16x64_bf16 v[72:75], v[52:55], a[80:87], v8\n"
"v_smfmac_f32_16x16x64_bf16 v[76:79], v[52:55], a[136:143], v8\nv_smfmac_f32_16x16x64_bf16 v[80:83], v[52:55], a[192:199], v8\nv_smfmac_f32_16x16x64_bf16 v[68:71], v[56:59], a[32:39], v8\nv_smfmac_f32_16x16x64_bf16 v[72:75], v[56:59], a[88:95], v8\n"
"v_smfmac_f32_16x16x64_bf16 v[76:79], v[56:59], a[144:151], v8\ns_cbranch_execz vd3_p0\nbuffer_load_dwordx4 v[124:127], v21, s[8:11], s41 offen offset:0 sc1\nbuffer_load_dwordx4 v[128:131], v21, s[8:11], s41 offen offset:1024 sc1\n"
"vd3_p0:\ns_cbranch_execnz tl11_p0\ns_nop 0\ns_nop 0\n"
"s_nop 0\ns_nop 0\ns_nop 0\ns_nop 0\n"
"s_nop 0\ns_nop 0\ns_nop 0\ns_nop 0\n"
"s_nop 0\ns_nop 0\ns_nop 0\ns_nop 0\n"
"s_nop 0\nva4_p0:\ns_branch vb4_p0\nsx4_p0:\n"
"s_add_u32 s60, s60, 1104\ns_addc_u32 s61, s61, 0\ns_setpc_b64 s[60:61]\nvb4_p0:\n"
"s_nop 1\ns_nop 1\ns_nop 1\ns_nop 1\n"
"s_nop 1\ns_nop 1\ns_nop 1\ns_nop 1\n"
"s_nop 1\ns_nop 1\ns_nop 1\ns_nop 1\n"
"s_nop 1\ns_nop 1\nv_smfmac_f32_16x16x64_bf16 v[68:71], v[48:51], a[16:23], v8\nv_smfmac_f32_16x16x64_bf16 v[72:75], v[48:51], a[72:79], v8\n"
"v_smfmac_f32_16x16x64_bf16 v[76:79], v[48:51], a[128:135], v8\nv_smfmac_f32_16x16x64_bf16 v[80:83], v[48:51], a[184:191], v8\ns_cbranch_execz vc4_p0\nbuffer_load_dwordx4 v[116:119], v20, s[8:11], s41 offen offset:0 sc1\n"
"buffer_load_dwordx4 v[120:123], v20, s[8:11], s41 offen offset:1024 sc1\nvc4_p0:\nv_smfmac_f32_16x16x64_bf16 v[68:71], v[52:55], a[24:31], v8\nv_smfmac_f32_16x16x64_bf16 v[72:75], v[52:55], a[80:87], v8\n"
"v_smfmac_f32_16x16x64_bf16 v[76:79], v[52:55], a[136:143], v8\nv_smfmac_f32_16x16x64_bf16 v[80:83], v[52:55], a[192:199], v8\nv_smfmac_f32_16x16x64_bf16 v[68:71], v[56:59], a[32:39], v8\nv_smfmac_f32_16x16x64_bf16 v[72:75], v[56:59], a[88:95], v8\n"
"v_smfmac_f32_16x16x64_bf16 v[76:79], v[56:59], a[144:151], v8\nv_smfmac_f32_16x16x64_bf16 v[80:83], v[56:59], a[200:207], v8\ns_cbranch_execz vd4_p0\nbuffer_load_dwordx4 v[124:127], v21, s[8:11], s41 offen offset:0 sc1\n"
"buffer_load_dwordx4 v[128:131], v21, s[8:11], s41 offen offset:1024 sc1\ns_nop 0\nvd4_p0:\ns_cbranch_execnz tl12_p0\n"
"s_nop 0\ns_nop 0\ns_nop 0\ns_nop 0\n"
"s_nop 0\ns_nop 0\ns_nop 0\ns_nop 0\n"
"s_nop 0\ns_nop 0\ns_nop 0\ns_nop 0\n"
"s_nop 0\ns_nop 0\nva5_p0:\ns_branch vb5_p0\n"
"sx5_p0:\ns_add_u32 s60, s60, 1380\ns_addc_u32 s61, s61, 0\ns_setpc_b64 s[60:61]\n"
"vb5_p0:\ns_nop 1\ns_nop 1\ns_nop 1\n"
"s_nop 1\ns_nop 1\ns_nop 1\ns_nop 1\n"
"s_nop 1\ns_nop 1\ns_nop 1\ns_nop 1\n"
"s_nop 1\nv_smfmac_f32_16x16x64_bf16 v[68:71], v[48:51], a[16:23], v8\nv_smfmac_f32_16x16x64_bf16 v[72:75], v[48:51], a[72:79], v8\nv_smfmac_f32_16x16x64_bf16 v[76:79], v[48:51], a[128:135], v8\n"
"v_smfmac_f32_16x16x64_bf16 v[80:83], v[48:51], a[184:191], v8\nv_smfmac_f32_16x16x64_bf16 v[68:71], v[52:55], a[24:31], v8\ns_cbranch_execz vc5_p0\nbuffer_load_dwordx4 v[116:119], v20, s[8:11], s41 offen offset:0 sc1\n"
"buffer_load_dwordx4 v[120:123], v20, s[8:11], s41 offen offset:1024 sc1\nvc5_p0:\nv_smfmac_f32_16x16x64_bf16 v[72:75], v[52:55], a[80:87], v8\nv_smfmac_f32_16x16x64_bf16 v[76:79], v[52:55], a[136:143], v8\n"
"v_smfmac_f32_16x16x64_bf16 v[80:83], v[52:55], a[192:199], v8\nv_smfmac_f32_16x16x64_bf16 v[68:71], v[56:59], a[32:39], v8\nv_smfmac_f32_16x16x64_bf16 v[72:75], v[56:59], a[88:95], v8\nv_smfmac_f32_16x16x64_bf16 v[76:79], v[56:59], a[144:151], v8\n"
"v_smfmac_f32_16x16x64_bf16 v[80:83], v[56:59], a[200:207], v8\nv_smfmac_f32_16x16x64_bf16 v[68:71], v[60:63], a[40:47], v8\ns_cbranch_execz vd5_p0\nbuffer_load_dwordx4 v[124:127], v21, s[8:11], s41 offen offset:0 sc1\n"
"buffer_load_dwordx4 v[128:131], v21, s[8:11], s41 offen offset:1024 sc1\ns_nop 4\nvd5_p0:\ns_cbranch_execnz tl13_p0\n"
"s_nop 0\ns_nop 0\ns_nop 0\ns_nop 0\n"
"s_nop 0\ns_nop 0\ns_nop 0\ns_nop 0\n"
"s_nop 0\ns_nop 0\ns_nop 0\ns_nop 0\n"
"s_nop 0\ns_nop 0\nva6_p0:\ns_branch vb6_p0\n"
"sx6_p0:\ns_add_u32 s60, s60, 1656\ns_addc_u32 s61, s61, 0\ns_setpc_b64 s[60:61]\n"
"vb6_p0:\ns_nop 1\ns_nop 1\ns_nop 1\n"
"s_nop 1\ns_nop 1\ns_nop 1\ns_nop 1\n"
"s_nop 1\ns_nop 1\ns_nop 1\nv_smfmac_f32_16x16x64_bf16 v[68:71], v[48:51], a[16:23], v8\n"
"v_smfmac_f32_16x16x64_bf16 v[72:75], v[48:51], a[72:79], v8\nv_smfmac_f32_16x16x64_bf16 v[76:79], v[48:51], a[128:135], v8\nv_smfmac_f32_16x16x64_bf16 v[80:83], v[48:51], a[184:191], v8\nv_smfmac_f32_16x16x64_bf16 v[68:71], v[52:55], a[24:31], v8\n"
"v_smfmac_f32_16x16x64_bf16 v[72:75], v[52:55], a[80:87], v8\ns_cbranch_execz vc6_p0\nbuffer_load_dwordx4 v[116:119], v20, s[8:11], s41 offen offset:0 sc1\nbuffer_load_dwordx4 v[120:123], v20, s[8:11], s41 offen offset:1024 sc1\n"
"vc6_p0:\nv_smfmac_f32_16x16x64_bf16 v[76:79], v[52:55], a[136:143], v8\nv_smfmac_f32_16x16x64_bf16 v[80:83], v[52:55], a[192:199], v8\nv_smfmac_f32_16x16x64_bf16 v[68:71], v[56:59], a[32:39], v8\n"
"v_smfmac_f32_16x16x64_bf16 v[72:75], v[56:59], a[88:95], v8\nv_smfmac_f32_16x16x64_bf16 v[76:79], v[56:59], a[144:151], v8\nv_smfmac_f32_16x16x64_bf16 v[80:83], v[56:59], a[200:207], v8\nv_smfmac_f32_16x16x64_bf16 v[68:71], v[60:63], a[40:47], v8\n"
"v_smfmac_f32_16x16x64_bf16 v[72:75], v[60:63], a[96:103], v8\ns_cbranch_execz vd6_p0\nbuffer_load_dwordx4 v[124:127], v21, s[8:11], s41 offen offset:0 sc1\nbuffer_load_dwordx4 v[128:131], v21, s[8:11], s41 offen offset:1024 sc1\n"
"s_nop 8\nvd6_p0:\ns_cbranch_execnz tl14_p0\ns_nop 0\n"
"s_nop 0\ns_nop 0\ns_nop 0\ns_nop 0\n"
"s_nop 0\ns_nop 0\ns_nop 0\ns_nop 0\n"
"s_nop 0\ns_nop 0\ns_nop 0\ns_nop 0\n"
"s_nop 0\nva7_p0:\ns_branch vb7_p0\nsx7_p0:\n"
"s_add_u32 s60, s60, 1932\ns_addc_u32 s61, s61, 0\ns_setpc_b64 s[60:61]\nvb7_p0:\n"
"s_nop 1\ns_nop 1\ns_nop 1\ns_nop 1\n"
"s_nop 1\ns_nop 1\ns_nop 1\ns_nop 1\n"
"v_smfmac_f32_16x16x64_bf16 v[68:71], v[48:51], a[16:23], v8\nv_smfmac_f32_16x16x64_bf16 v[72:75], v[48:51], a[72:79], v8\nv_smfmac_f32_16x16x64_bf16 v[76:79], v[48:51], a[128:135], v8\nv_smfmac_f32_16x16x64_bf16 v[80:83], v[48:51], a[184:191], v8\n"
"v_smfmac_f32_16x16x64_bf16 v[68:71], v[52:55], a[24:31], v8\nv_smfmac_f32_16x16x64_bf16 v[72:75], v[52:55], a[80:87], v8\nv_smfmac_f32_16x16x64_bf16 v[76:79], v[52:55], a[136:143], v8\ns_cbranch_execz vc7_p0\n"
"buffer_load_dwordx4 v[116:119], v20, s[8:11], s41 offen offset:0 sc1\nbuffer_load_dwordx4 v[120:123], v20, s[8:11], s41 offen offset:1024 sc1\nvc7_p0:\nv_smfmac_f32_16x16x64_bf16 v[80:83], v[52:55], a[192:199], v8\n"
"v_smfmac_f32_16x16x64_bf16 v[68:71], v[56:59], a[32:39], v8\nv_smfmac_f32_16x16x64_bf16 v[72:75], v[56:59], a[88:95], v8\nv_smfmac_f32_16x16x64_bf16 v[76:79], v[56:59], a[144:151], v8\nv_smfmac_f32_16x16x64_bf16 v[80:83], v[56:59], a[200:207], v8\n"
"v_smfmac_f32_16x16x64_bf16 v[68:71], v[60:63], a[40:47], v8\nv_smfmac_f32_16x16x64_bf16 v[72:75], v[60:63], a[96:103], v8\nv_smfmac_f32_16x16x64_bf16 v[76:79], v[60:63], a[152:159], v8\ns_cbranch_execz vd7_p0\n"
"buffer_load_dwordx4 v[124:127], v21, s[8:11], s41 offen offset:0 sc1\nbuffer_load_dwordx4 v[128:131], v21, s[8:11], s41 offen offset:1024 sc1\ns_nop 12\nvd7_p0:\n"
"s_cbranch_execnz tl15_p0\ns_nop 0\ns_nop 0\ns_nop 0\n"
"s_nop 0\ns_nop 0\ns_nop 0\ns_nop 0\n"
"s_nop 0\ns_nop 0\ns_nop 0\ns_nop 0\n"
"s_nop 0\ns_nop 0\ns_nop 0\nva8_p0:\n"
"s_branch vb8_p0\nsx8_p0:\ns_add_u32 s60, s60, 2208\ns_addc_u32 s61, s61, 0\n"
"s_setpc_b64 s[60:61]\nvb8_p0:\ns_nop 1\ns_nop 1\n"
"s_nop 1\ns_nop 1\ns_nop 1\ns_nop 1\n"
"v_smfmac_f32_16x16x64_bf16 v[68:71], v[48:51], a[16:23], v8\nv_smfmac_f32_16x16x64_bf16 v[72:75], v[48:51], a[72:79], v8\nv_smfmac_f32_16x16x64_bf16 v[76:79], v[48:51], a[128:135], v8\nv_smfmac_f32_16x16x64_bf16 v[80:83], v[48:51], a[184:191], v8\n"
"v_smfmac_f32_16x16x64_bf16 v[68:71], v[52:55], a[24:31], v8\nv_smfmac_f32_16x16x64_bf16 v[72:75], v[52:55], a[80:87], v8\nv_smfmac_f32_16x16x64_bf16 v[76:79], v[52:55], a[136:143], v8\nv_smfmac_f32_16x16x64_bf16 v[80:83], v[52:55], a[192:199], v8\n"
"s_cbranch_execz vc8_p0\nbuffer_load_dwordx4 v[116:119], v20, s[8:11], s41 offen offset:0 sc1\nbuffer_load_dwordx4 v[120:123], v20, s[8:11], s41 offen offset:1024 sc1\nvc8_p0:\n"
"v_smfmac_f32_16x16x64_bf16 v[68:71], v[56:59], a[32:39], v8\nv_smfmac_f32_16x16x64_bf16 v[72:75], v[56:59], a[88:95], v8\nv_smfmac_f32_16x16x64_bf16 v[76:79], v[56:59], a[144:151], v8\nv_smfmac_f32_16x16x64_bf16 v[80:83], v[56:59], a[200:207], v8\n"
"v_smfmac_f32_16x16x64_bf16 v[68:71], v[60:63], a[40:47], v8\nv_smfmac_f32_16x16x64_bf16 v[72:75], v[60:63], a[96:103], v8\nv_smfmac_f32_16x16x64_bf16 v[76:79], v[60:63], a[152:159], v8\nv_smfmac_f32_16x16x64_bf16 v[80:83], v[60:63], a[208:215], v8\n"
"s_cbranch_execz vd8_p0\nbuffer_load_dwordx4 v[124:127], v21, s[8:11], s41 offen offset:0 sc1\nbuffer_load_dwordx4 v[128:131], v21, s[8:11], s41 offen offset:1024 sc1\ns_nop 15\n"
"s_nop 0\nvd8_p0:\ns_cbranch_execnz tl16_p0\ns_nop 0\n"
"s_nop 0\ns_nop 0\ns_nop 0\ns_nop 0\n"
"s_nop 0\ns_nop 0\ns_nop 0\ns_nop 0\n"
"s_nop 0\ns_nop 0\ns_nop 0\ns_nop 0\n"
"va9_p0:\ns_branch warm_p0\nsx9_p0:\ns_add_u32 s60, s60, 2484\n"
"s_addc_u32 s61, s61, 0\ns_setpc_b64 s[60:61]\nvb9_p0:\ns_nop 1\n"
"s_nop 1\ns_nop 1\ns_nop 1\nv_smfmac_f32_16x16x64_bf16 v[68:71], v[48:51], a[16:23], v8\n"
"v_smfmac_f32_16x16x64_bf16 v[72:75], v[48:51], a[72:79], v8\nv_smfmac_f32_16x16x64_bf16 v[76:79], v[48:51], a[128:135], v8\nv_smfmac_f32_16x16x64_bf16 v[80:83], v[48:51], a[184:191], v8\nv_smfmac_f32_16x16x64_bf16 v[68:71], v[52:55], a[24:31], v8\n"
"v_smfmac_f32_16x16x64_bf16 v[72:75], v[52:55], a[80:87], v8\nv_smfmac_f32_16x16x64_bf16 v[76:79], v[52:55], a[136:143], v8\nv_smfmac_f32_16x16x64_bf16 v[80:83], v[52:55], a[192:199], v8\nv_smfmac_f32_16x16x64_bf16 v[68:71], v[56:59], a[32:39], v8\n"
"s_cbranch_execz vc9_p0\nbuffer_load_dwordx4 v[116:119], v20, s[8:11], s41 offen offset:0 sc1\nbuffer_load_dwordx4 v[120:123], v20, s[8:11], s41 offen offset:1024 sc1\nvc9_p0:\n"
"v_smfmac_f32_16x16x64_bf16 v[72:75], v[56:59], a[88:95], v8\nv_smfmac_f32_16x16x64_bf16 v[76:79], v[56:59], a[144:151], v8\nv_smfmac_f32_16x16x64_bf16 v[80:83], v[56:59], a[200:207], v8\nv_smfmac_f32_16x16x64_bf16 v[68:71], v[60:63], a[40:47], v8\n"
"v_smfmac_f32_16x16x64_bf16 v[72:75], v[60:63], a[96:103], v8\nv_smfmac_f32_16x16x64_bf16 v[76:79], v[60:63], a[152:159], v8\nv_smfmac_f32_16x16x64_bf16 v[80:83], v[60:63], a[208:215], v8\nv_smfmac_f32_16x16x64_bf16 v[68:71], v[64:67], a[48:55], v8\n"
"s_cbranch_execz vd9_p0\nbuffer_load_dwordx4 v[124:127], v21, s[8:11], s41 offen offset:0 sc1\nbuffer_load_dwordx4 v[128:131], v21, s[8:11], s41 offen offset:1024 sc1\ns_nop 15\n"
"s_nop 4\nvd9_p0:\ns_cbranch_execnz tl17_p0\ns_nop 0\n"
"s_nop 0\ns_nop 0\ns_nop 0\ns_nop 0\n"
"s_nop 0\ns_nop 0\ns_nop 0\ns_nop 0\n"
"s_nop 0\ns_nop 0\ns_nop 0\ns_nop 0\n"
"va10_p0:\ns_branch vb10_p0\nsx10_p0:\ns_add_u32 s60, s60, 2760\n"
"s_addc_u32 s61, s61, 0\ns_setpc_b64 s[60:61]\nvb10_p0:\ns_nop 1\n"
"s_nop 1\nv_smfmac_f32_16x16x64_bf16 v[68:71], v[48:51], a[16:23], v8\nv_smfmac_f32_16x16x64_bf16 v[72:75], v[48:51], a[72:79], v8\nv_smfmac_f32_16x16x64_bf16 v[76:79], v[48:51], a[128:135], v8\n"
"v_smfmac_f32_16x16x64_bf16 v[80:83], v[48:51], a[184:191], v8\nv_smfmac_f32_16x16x64_bf16 v[68:71], v[52:55], a[24:31], v8\nv_smfmac_f32_16x16x64_bf16 v[72:75], v[52:55], a[80:87], v8\nv_smfmac_f32_16x16x64_bf16 v[76:79], v[52:55], a[136:143], v8\n"
"v_smfmac_f32_16x16x64_bf16 v[80:83], v[52:55], a[192:199], v8\nv_smfmac_f32_16x16x64_bf16 v[68:71], v[56:59], a[32:39], v8\nv_smfmac_f32_16x16x64_bf16 v[72:75], v[56:59], a[88:95], v8\ns_cbranch_execz vc10_p0\n"
"buffer_load_dwordx4 v[116:119], v20, s[8:11], s41 offen offset:0 sc1\nbuffer_load_dwordx4 v[120:123], v20, s[8:11], s41 offen offset:1024 sc1\nvc10_p0:\nv_smfmac_f32_16x16x64_bf16 v[76:79], v[56:59], a[144:151], v8\n"
"v_smfmac_f32_16x16x64_bf16 v[80:83], v[56:59], a[200:207], v8\nv_smfmac_f32_16x16x64_bf16 v[68:71], v[60:63], a[40:47], v8\nv_smfmac_f32_16x16x64_bf16 v[72:75], v[60:63], a[96:103], v8\nv_smfmac_f32_16x16x64_bf16 v[76:79], v[60:63], a[152:159], v8\n"
"v_smfmac_f32_16x16x64_bf16 v[80:83], v[60:63], a[208:215], v8\nv_smfmac_f32_16x16x64_bf16 v[68:71], v[64:67], a[48:55], v8\nv_smfmac_f32_16x16x64_bf16 v[72:75], v[64:67], a[104:111], v8\ns_cbranch_execz vd10_p0\n"
"buffer_load_dwordx4 v[124:127], v21, s[8:11], s41 offen offset:0 sc1\nbuffer_load_dwordx4 v[128:131], v21, s[8:11], s41 offen offset:1024 sc1\ns_nop 15\ns_nop 8\n"
"vd10_p0:\ns_cbranch_execnz tl18_p0\ns_nop 0\ns_nop 0\n"
"s_nop 0\ns_nop 0\ns_nop 0\ns_nop 0\n"
"s_nop 0\ns_nop 0\ns_nop 0\ns_nop 0\n"
"s_nop 0\ns_nop 0\ns_nop 0\nva11_p0:\n"
"s_branch vb11_p0\nsx11_p0:\ns_add_u32 s60, s60, 3036\ns_addc_u32 s61, s61, 0\n"
"s_setpc_b64 s[60:61]\nvb11_p0:\nv_smfmac_f32_16x16x64_bf16 v[68:71], v[48:51], a[16:23], v8\nv_smfmac_f32_16x16x64_bf16 v[72:75], v[48:51], a[72:79], v8\n"
"v_smfmac_f32_16x16x64_bf16 v[76:79], v[48:51], a[128:135], v8\nv_smfmac_f32_16x16x64_bf16 v[80:83], v[48:51], a[184:191], v8\nv_smfmac_f32_16x16x64_bf16 v[68:71], v[52:55], a[24:31], v8\nv_smfmac_f32_16x16x64_bf16 v[72:75], v[52:55], a[80:87], v8\n"
"v_smfmac_f32_16x16x64_bf16 v[76:79], v[52:55], a[136:143], v8\nv_smfmac_f32_16x16x64_bf16 v[80:83], v[52:55], a[192:199], v8\nv_smfmac_f32_16x16x64_bf16 v[68:71], v[56:59], a[32:39], v8\nv_smfmac_f32_16x16x64_bf16 v[72:75], v[56:59], a[88:95], v8\n"
"v_smfmac_f32_16x16x64_bf16 v[76:79], v[56:59], a[144:151], v8\ns_cbranch_execz vc11_p0\nbuffer_load_dwordx4 v[116:119], v20, s[8:11], s41 offen offset:0 sc1\nbuffer_load_dwordx4 v[120:123], v20, s[8:11], s41 offen offset:1024 sc1\n"
"vc11_p0:\nv_smfmac_f32_16x16x64_bf16 v[80:83], v[56:59], a[200:207], v8\nv_smfmac_f32_16x16x64_bf16 v[68:71], v[60:63], a[40:47], v8\nv_smfmac_f32_16x16x64_bf16 v[72:75], v[60:63], a[96:103], v8\n"
"v_smfmac_f32_16x16x64_bf16 v[76:79], v[60:63], a[152:159], v8\nv_smfmac_f32_16x16x64_bf16 v[80:83], v[60:63], a[208:215], v8\nv_smfmac_f32_16x16x64_bf16 v[68:71], v[64:67], a[48:55], v8\nv_smfmac_f32_16x16x64_bf16 v[72:75], v[64:67], a[104:111], v8\n"
"v_smfmac_f32_16x16x64_bf16 v[76:79], v[64:67], a[160:167], v8\ns_cbranch_execz vd11_p0\nbuffer_load_dwordx4 v[124:127], v21, s[8:11], s41 offen offset:0 sc1\nbuffer_load_dwordx4 v[128:131], v21, s[8:11], s41 offen offset:1024 sc1\n"
"s_nop 15\ns_nop 12\nvd11_p0:\ns_cbranch_execnz tl19_p0\n"
"s_nop 0\ns_nop 0\ns_nop 0\ns_nop 0\n"
"s_nop 0\ns_nop 0\ns_nop 0\ns_nop 0\n"
"s_nop 0\ns_nop 0\ns_nop 0\ns_nop 0\n"
"s_nop 0\nva12_p0:\ns_branch vb12_p0\nsx12_p0:\n"
"s_add_u32 s60, s60, 3312\ns_addc_u32 s61, s61, 0\ns_setpc_b64 s[60:61]\nvb12_p0:\n"
"v_smfmac_f32_16x16x64_bf16 v[72:75], v[48:51], a[72:79], v8\nv_smfmac_f32_16x16x64_bf16 v[76:79], v[48:51], a[128:135], v8\nv_smfmac_f32_16x16x64_bf16 v[80:83], v[48:51], a[184:191], v8\nv_smfmac_f32_16x16x64_bf16 v[68:71], v[52:55], a[24:31], v8\n"
"v_smfmac_f32_16x16x64_bf16 v[72:75], v[52:55], a[80:87], v8\nv_smfmac_f32_16x16x64_bf16 v[76:79], v[52:55], a[136:143], v8\nv_smfmac_f32_16x16x64_bf16 v[80:83], v[52:55], a[192:199], v8\nv_smfmac_f32_16x16x64_bf16 v[68:71], v[56:59], a[32:39], v8\n"
"v_smfmac_f32_16x16x64_bf16 v[72:75], v[56:59], a[88:95], v8\nv_smfmac_f32_16x16x64_bf16 v[76:79], v[56:59], a[144:151], v8\nv_smfmac_f32_16x16x64_bf16 v[80:83], v[56:59], a[200:207], v8\ns_cbranch_execz vc12_p0\n"
"buffer_load_dwordx4 v[116:119], v20, s[8:11], s41 offen offset:0 sc1\nbuffer_load_dwordx4 v[120:123], v20, s[8:11], s41 offen offset:1024 sc1\nvc12_p0:\nv_smfmac_f32_16x16x64_bf16 v[68:71], v[60:63], a[40:47], v8\n"
"v_smfmac_f32_16x16x64_bf16 v[72:75], v[60:63], a[96:103], v8\nv_smfmac_f32_16x16x64_bf16 v[76:79], v[60:63], a[152:159], v8\nv_smfmac_f32_16x16x64_bf16 v[80:83], v[60:63], a[208:215], v8\nv_smfmac_f32_16x16x64_bf16 v[68:71], v[64:67], a[48:55], v8\n"
"v_smfmac_f32_16x16x64_bf16 v[72:75], v[64:67], a[104:111], v8\nv_smfmac_f32_16x16x64_bf16 v[76:79], v[64:67], a[160:167], v8\nv_smfmac_f32_16x16x64_bf16 v[80:83], v[64:67], a[216:223], v8\ns_cbranch_execz vd12_p0\n"
"buffer_load_dwordx4 v[124:127], v21, s[8:11], s41 offen offset:0 sc1\nbuffer_load_dwordx4 v[128:131], v21, s[8:11], s41 offen offset:1024 sc1\ns_nop 15\ns_nop 15\n"
"s_nop 0\nvd12_p0:\ns_cbranch_execnz tl20_p0\ns_nop 0\n"
"s_nop 0\ns_nop 0\ns_nop 0\ns_nop 0\n"
"s_nop 0\ns_nop 0\ns_nop 0\ns_nop 0\n"
"s_nop 0\ns_nop 0\ns_nop 0\nva13_p0:\n"
"s_branch vb13_p0\nsx13_p0:\ns_add_u32 s60, s60, 3588\ns_addc_u32 s61, s61, 0\n"
"s_setpc_b64 s[60:61]\nvb13_p0:\nv_smfmac_f32_16x16x64_bf16 v[76:79], v[48:51], a[128:135], v8\nv_smfmac_f32_16x16x64_bf16 v[80:83], v[48:51], a[184:191], v8\n"
"v_smfmac_f32_16x16x64_bf16 v[68:71], v[52:55], a[24:31], v8\nv_smfmac_f32_16x16x64_bf16 v[72:75], v[52:55], a[80:87], v8\nv_smfmac_f32_16x16x64_bf16 v[76:79], v[52:55], a[136:143], v8\nv_smfmac_f32_16x16x64_bf16 v[80:83], v[52:55], a[192:199], v8\n"
"v_smfmac_f32_16x16x64_bf16 v[68:71], v[56:59], a[32:39], v8\nv_smfmac_f32_16x16x64_bf16 v[72:75], v[56:59], a[88:95], v8\nv_smfmac_f32_16x16x64_bf16 v[76:79], v[56:59], a[144:151], v8\nv_smfmac_f32_16x16x64_bf16 v[80:83], v[56:59], a[200:207], v8\n"
"v_smfmac_f32_16x16x64_bf16 v[68:71], v[60:63], a[40:47], v8\ns_cbranch_execz vc13_p0\nbuffer_load_dwordx4 v[116:119], v20, s[8:11], s41 offen offset:0 sc1\nbuffer_load_dwordx4 v[120:123], v20, s[8:11], s41 offen offset:1024 sc1\n"
"vc13_p0:\nv_smfmac_f32_16x16x64_bf16 v[72:75], v[60:63], a[96:103], v8\nv_smfmac_f32_16x16x64_bf16 v[76:79], v[60:63], a[152:159], v8\nv_smfmac_f32_16x16x64_bf16 v[80:83], v[60:63], a[208:215], v8\n"
"v_smfmac_f32_16x16x64_bf16 v[68:71], v[64:67], a[48:55], v8\nv_smfmac_f32_16x16x64_bf16 v[72:75], v[64:67], a[104:111], v8\nv_smfmac_f32_16x16x64_bf16 v[76:79], v[64:67], a[160:167], v8\nv_smfmac_f32_16x16x64_bf16 v[80:83], v[64:67], a[216:223], v8\n"
"v_add_f32 v219, v68, v69\nv_add_f32 v220, v70, v71\nv_cvt_pk_bf16_f32 v156, v219, v220\ns_nop 0\n"
"s_cbranch_execz vd13_p0\nbuffer_load_dwordx4 v[124:127], v21, s[8:11], s41 offen offset:0 sc1\nbuffer_load_dwordx4 v[128:131], v21, s[8:11], s41 offen offset:1024 sc1\ns_nop 15\n"
"s_nop 15\ns_nop 3\nvd13_p0:\ns_cbranch_execnz tl21_p0\n"
"s_nop 0\ns_nop 0\ns_nop 0\ns_nop 0\n"
"s_nop 0\ns_nop 0\ns_nop 0\ns_nop 0\n"
"s_nop 0\nva14_p0:\ns_branch vb14_p0\nsx14_p0:\n"
"s_add_u32 s60, s60, 3864\ns_addc_u32 s61, s61, 0\ns_setpc_b64 s[60:61]\nvb14_p0:\n"
"v_smfmac_f32_16x16x64_bf16 v[80:83], v[48:51], a[184:191], v8\nv_smfmac_f32_16x16x64_bf16 v[68:71], v[52:55], a[24:31], v8\nv_smfmac_f32_16x16x64_bf16 v[72:75], v[52:55], a[80:87], v8\nv_smfmac_f32_16x16x64_bf16 v[76:79], v[52:55], a[136:143], v8\n"
"v_smfmac_f32_16x16x64_bf16 v[80:83], v[52:55], a[192:199], v8\nv_smfmac_f32_16x16x64_bf16 v[68:71], v[56:59], a[32:39], v8\nv_smfmac_f32_16x16x64_bf16 v[72:75], v[56:59], a[88:95], v8\nv_smfmac_f32_16x16x64_bf16 v[76:79], v[56:59], a[144:151], v8\n"
"v_smfmac_f32_16x16x64_bf16 v[80:83], v[56:59], a[200:207], v8\nv_smfmac_f32_16x16x64_bf16 v[68:71], v[60:63], a[40:47], v8\nv_smfmac_f32_16x16x64_bf16 v[72:75], v[60:63], a[96:103], v8\ns_cbranch_execz vc14_p0\n"
"buffer_load_dwordx4 v[116:119], v20, s[8:11], s41 offen offset:0 sc1\nbuffer_load_dwordx4 v[120:123], v20, s[8:11], s41 offen offset:1024 sc1\nvc14_p0:\nv_smfmac_f32_16x16x64_bf16 v[76:79], v[60:63], a[152:159], v8\n"
"v_smfmac_f32_16x16x64_bf16 v[80:83], v[60:63], a[208:215], v8\nv_smfmac_f32_16x16x64_bf16 v[68:71], v[64:67], a[48:55], v8\nv_smfmac_f32_16x16x64_bf16 v[72:75], v[64:67], a[104:111], v8\nv_smfmac_f32_16x16x64_bf16 v[76:79], v[64:67], a[160:167], v8\n"
"v_smfmac_f32_16x16x64_bf16 v[80:83], v[64:67], a[216:223], v8\nv_add_f32 v219, v68, v69\nv_add_f32 v220, v70, v71\nv_cvt_pk_bf16_f32 v156, v219, v220\n"
"s_nop 0\nv_add_f32 v219, v72, v73\nv_add_f32 v220, v74, v75\nv_cvt_pk_bf16_f32 v157, v219, v220\n"
"s_nop 0\ns_cbranch_execz vd14_p0\nbuffer_load_dwordx4 v[124:127], v21, s[8:11], s41 offen offset:0 sc1\nbuffer_load_dwordx4 v[128:131], v21, s[8:11], s41 offen offset:1024 sc1\n"
"s_nop 15\ns_nop 15\ns_nop 6\nvd14_p0:\n"
"s_cbranch_execnz tl22_p0\ns_nop 0\ns_nop 0\ns_nop 0\n"
"s_nop 0\ns_nop 0\ns_nop 0\nva15_p0:\n"
"s_branch vb15_p0\nsx15_p0:\ns_add_u32 s60, s60, 4140\ns_addc_u32 s61, s61, 0\n"
"s_setpc_b64 s[60:61]\nvb15_p0:\nv_smfmac_f32_16x16x64_bf16 v[68:71], v[52:55], a[24:31], v8\nv_smfmac_f32_16x16x64_bf16 v[72:75], v[52:55], a[80:87], v8\n"
"v_smfmac_f32_16x16x64_bf16 v[76:79], v[52:55], a[136:143], v8\nv_smfmac_f32_16x16x64_bf16 v[80:83], v[52:55], a[192:199], v8\nv_smfmac_f32_16x16x64_bf16 v[68:71], v[56:59], a[32:39], v8\nv_smfmac_f32_16x16x64_bf16 v[72:75], v[56:59], a[88:95], v8\n"
"v_smfmac_f32_16x16x64_bf16 v[76:79], v[56:59], a[144:151], v8\nv_smfmac_f32_16x16x64_bf16 v[80:83], v[56:59], a[200:207], v8\nv_smfmac_f32_16x16x64_bf16 v[68:71], v[60:63], a[40:47], v8\nv_smfmac_f32_16x16x64_bf16 v[72:75], v[60:63], a[96:103], v8\n"
"v_smfmac_f32_16x16x64_bf16 v[76:79], v[60:63], a[152:159], v8\ns_cbranch_execz vc15_p0\nbuffer_load_dwordx4 v[116:119], v20, s[8:11], s41 offen offset:0 sc1\nbuffer_load_dwordx4 v[120:123], v20, s[8:11], s41 offen offset:1024 sc1\n"
"vc15_p0:\nv_smfmac_f32_16x16x64_bf16 v[80:83], v[60:63], a[208:215], v8\nv_smfmac_f32_16x16x64_bf16 v[68:71], v[64:67], a[48:55], v8\nv_smfmac_f32_16x16x64_bf16 v[72:75], v[64:67], a[104:111], v8\n"
"v_smfmac_f32_16x16x64_bf16 v[76:79], v[64:67], a[160:167], v8\nv_smfmac_f32_16x16x64_bf16 v[80:83], v[64:67], a[216:223], v8\nv_add_f32 v219, v68, v69\nv_add_f32 v220, v70, v71\n"
"v_cvt_pk_bf16_f32 v156, v219, v220\ns_nop 0\nv_add_f32 v219, v72, v73\nv_add_f32 v220, v74, v75\n"
"v_cvt_pk_bf16_f32 v157, v219, v220\ns_nop 0\nv_add_f32 v219, v76, v77\nv_add_f32 v220, v78, v79\n"
"v_cvt_pk_bf16_f32 v158, v219, v220\ns_nop 0\ns_cbranch_execz vd15_p0\nbuffer_load_dwordx4 v[124:127], v21, s[8:11], s41 offen offset:0 sc1\n"
"buffer_load_dwordx4 v[128:131], v21, s[8:11], s41 offen offset:1024 sc1\ns_nop 15\ns_nop 15\ns_nop 9\n"
"vd15_p0:\ns_cbranch_execnz tl23_p0\ns_nop 0\ns_nop 0\n"
"s_nop 0\nva16_p0:\ns_branch vb16_p0\nsx16_p0:\n"
"s_add_u32 s60, s60, 4416\ns_addc_u32 s61, s61, 0\ns_setpc_b64 s[60:61]\nvb16_p0:\n"
"v_smfmac_f32_16x16x64_bf16 v[72:75], v[52:55], a[80:87], v8\nv_smfmac_f32_16x16x64_bf16 v[76:79], v[52:55], a[136:143], v8\nv_smfmac_f32_16x16x64_bf16 v[80:83], v[52:55], a[192:199], v8\nv_smfmac_f32_16x16x64_bf16 v[68:71], v[56:59], a[32:39], v8\n"
"v_smfmac_f32_16x16x64_bf16 v[72:75], v[56:59], a[88:95], v8\nv_smfmac_f32_16x16x64_bf16 v[76:79], v[56:59], a[144:151], v8\nv_smfmac_f32_16x16x64_bf16 v[80:83], v[56:59], a[200:207], v8\nv_smfmac_f32_16x16x64_bf16 v[68:71], v[60:63], a[40:47], v8\n"
"v_smfmac_f32_16x16x64_bf16 v[72:75], v[60:63], a[96:103], v8\nv_smfmac_f32_16x16x64_bf16 v[76:79], v[60:63], a[152:159], v8\nv_smfmac_f32_16x16x64_bf16 v[80:83], v[60:63], a[208:215], v8\ns_cbranch_execz vc16_p0\n"
"buffer_load_dwordx4 v[116:119], v20, s[8:11], s41 offen offset:0 sc1\nbuffer_load_dwordx4 v[120:123], v20, s[8:11], s41 offen offset:1024 sc1\nvc16_p0:\nv_smfmac_f32_16x16x64_bf16 v[68:71], v[64:67], a[48:55], v8\n"
"v_smfmac_f32_16x16x64_bf16 v[72:75], v[64:67], a[104:111], v8\nv_smfmac_f32_16x16x64_bf16 v[76:79], v[64:67], a[160:167], v8\nv_smfmac_f32_16x16x64_bf16 v[80:83], v[64:67], a[216:223], v8\nv_add_f32 v219, v68, v69\n"
"v_add_f32 v220, v70, v71\nv_cvt_pk_bf16_f32 v156, v219, v220\ns_nop 0\nv_add_f32 v219, v72, v73\n"
"v_add_f32 v220, v74, v75\nv_cvt_pk_bf16_f32 v157, v219, v220\ns_nop 0\nv_add_f32 v219, v76, v77\n"
"v_add_f32 v220, v78, v79\nv_cvt_pk_bf16_f32 v158, v219, v220\ns_nop 0\nv_add_f32 v219, v80, v81\n"
"v_add_f32 v220, v82, v83\nv_cvt_pk_bf16_f32 v159, v219, v220\ns_nop 0\ns_cbranch_execz vd16_p0\n"
"buffer_load_dwordx4 v[124:127], v21, s[8:11], s41 offen offset:0 sc1\nbuffer_load_dwordx4 v[128:131], v21, s[8:11], s41 offen offset:1024 sc1\ns_nop 15\ns_nop 15\n"
"s_nop 12\nvd16_p0:\ns_cbranch_execnz tl24_p0\nva17_p0:\n"
"s_branch vb17_p0\nsx17_p0:\ns_add_u32 s60, s60, 4692\ns_addc_u32 s61, s61, 0\n"
"s_setpc_b64 s[60:61]\nvb17_p0:\nv_smfmac_f32_16x16x64_bf16 v[76:79], v[52:55], a[136:143], v8\nv_smfmac_f32_16x16x64_bf16 v[80:83], v[52:55], a[192:199], v8\n"
"v_smfmac_f32_16x16x64_bf16 v[68:71], v[56:59], a[32:39], v8\nv_smfmac_f32_16x16x64_bf16 v[72:75], v[56:59], a[88:95], v8\nv_smfmac_f32_16x16x64_bf16 v[76:79], v[56:59], a[144:151], v8\nv_smfmac_f32_16x16x64_bf16 v[80:83], v[56:59], a[200:207], v8\n"
"v_smfmac_f32_16x16x64_bf16 v[68:71], v[60:63], a[40:47], v8\nv_smfmac_f32_16x16x64_bf16 v[72:75], v[60:63], a[96:103], v8\nv_smfmac_f32_16x16x64_bf16 v[76:79], v[60:63], a[152:159], v8\nv_smfmac_f32_16x16x64_bf16 v[80:83], v[60:63], a[208:215], v8\n"
"v_smfmac_f32_16x16x64_bf16 v[68:71], v[64:67], a[48:55], v8\ns_cbranch_execz vc17_p0\nbuffer_load_dwordx4 v[116:119], v20, s[8:11], s41 offen offset:0 sc1\nbuffer_load_dwordx4 v[120:123], v20, s[8:11], s41 offen offset:1024 sc1\n"
"vc17_p0:\nv_smfmac_f32_16x16x64_bf16 v[72:75], v[64:67], a[104:111], v8\nv_smfmac_f32_16x16x64_bf16 v[76:79], v[64:67], a[160:167], v8\nv_smfmac_f32_16x16x64_bf16 v[80:83], v[64:67], a[216:223], v8\n"
"v_add_f32 v219, v68, v69\nv_add_f32 v220, v70, v71\nv_cvt_pk_bf16_f32 v156, v219, v220\ns_nop 0\n"
"v_add_f32 v219, v72, v73\nv_add_f32 v220, v74, v75\nv_cvt_pk_bf16_f32 v157, v219, v220\ns_nop 0\n"
"v_add_f32 v219, v76, v77\nv_add_f32 v220, v78, v79\nv_cvt_pk_bf16_f32 v158, v219, v220\ns_nop 0\n"
"v_add_f32 v219, v80, v81\nv_add_f32 v220, v82, v83\nv_cvt_pk_bf16_f32 v159, v219, v220\ns_nop 0\n"
"s_nop 1\ns_nop 1\ns_cbranch_execz vd17_p0\nbuffer_load_dwordx4 v[124:127], v21, s[8:11], s41 offen offset:0 sc1\n"
"buffer_load_dwordx4 v[128:131], v21, s[8:11], s41 offen offset:1024 sc1\ns_nop 15\ns_nop 15\ns_nop 12\n"
"vd17_p0:\ns_cbranch_execnz tl24_p0\nva18_p0:\ns_branch warm_p0\n"
"sx18_p0:\ns_add_u32 s60, s60, 4968\ns_addc_u32 s61, s61, 0\ns_setpc_b64 s[60:61]\n"
"vb18_p0:\nv_smfmac_f32_16x16x64_bf16 v[80:83], v[52:55], a[192:199], v8\nv_smfmac_f32_16x16x64_bf16 v[68:71], v[56:59], a[32:39], v8\nv_smfmac_f32_16x16x64_bf16 v[72:75], v[56:59], a[88:95], v8\n"
"v_smfmac_f32_16x16x64_bf16 v[76:79], v[56:59], a[144:151], v8\nv_smfmac_f32_16x16x64_bf16 v[80:83], v[56:59], a[200:207], v8\nv_smfmac_f32_16x16x64_bf16 v[68:71], v[60:63], a[40:47], v8\nv_smfmac_f32_16x16x64_bf16 v[72:75], v[60:63], a[96:103], v8\n"
"v_smfmac_f32_16x16x64_bf16 v[76:79], v[60:63], a[152:159], v8\nv_smfmac_f32_16x16x64_bf16 v[80:83], v[60:63], a[208:215], v8\nv_smfmac_f32_16x16x64_bf16 v[68:71], v[64:67], a[48:55], v8\nv_smfmac_f32_16x16x64_bf16 v[72:75], v[64:67], a[104:111], v8\n"
"s_cbranch_execz vc18_p0\nbuffer_load_dwordx4 v[116:119], v20, s[8:11], s41 offen offset:0 sc1\nbuffer_load_dwordx4 v[120:123], v20, s[8:11], s41 offen offset:1024 sc1\nvc18_p0:\n"
"v_smfmac_f32_16x16x64_bf16 v[76:79], v[64:67], a[160:167], v8\nv_smfmac_f32_16x16x64_bf16 v[80:83], v[64:67], a[216:223], v8\nv_add_f32 v219, v68, v69\nv_add_f32 v220, v70, v71\n"
"v_cvt_pk_bf16_f32 v156, v219, v220\ns_nop 0\nv_add_f32 v219, v72, v73\nv_add_f32 v220, v74, v75\n"
"v_cvt_pk_bf16_f32 v157, v219, v220\ns_nop 0\nv_add_f32 v219, v76, v77\nv_add_f32 v220, v78, v79\n"
"v_cvt_pk_bf16_f32 v158, v219, v220\ns_nop 0\nv_add_f32 v219, v80, v81\nv_add_f32 v220, v82, v83\n"
"v_cvt_pk_bf16_f32 v159, v219, v220\ns_nop 0\ns_nop 1\ns_nop 1\n"
"s_nop 1\ns_nop 1\ns_cbranch_execz vd18_p0\nbuffer_load_dwordx4 v[124:127], v21, s[8:11], s41 offen offset:0 sc1\n"
"buffer_load_dwordx4 v[128:131], v21, s[8:11], s41 offen offset:1024 sc1\ns_nop 15\ns_nop 15\ns_nop 12\n"
"vd18_p0:\ns_cbranch_execnz tl24_p0\nva19_p0:\ns_branch vb19_p0\n"
"sx19_p0:\ns_add_u32 s60, s60, 5244\ns_addc_u32 s61, s61, 0\ns_setpc_b64 s[60:61]\n"
"vb19_p0:\nv_smfmac_f32_16x16x64_bf16 v[68:71], v[56:59], a[32:39], v8\nv_smfmac_f32_16x16x64_bf16 v[72:75], v[56:59], a[88:95], v8\nv_smfmac_f32_16x16x64_bf16 v[76:79], v[56:59], a[144:151], v8\n"
"v_smfmac_f32_16x16x64_bf16 v[80:83], v[56:59], a[200:207], v8\nv_smfmac_f32_16x16x64_bf16 v[68:71], v[60:63], a[40:47], v8\nv_smfmac_f32_16x16x64_bf16 v[72:75], v[60:63], a[96:103], v8\nv_smfmac_f32_16x16x64_bf16 v[76:79], v[60:63], a[152:159], v8\n"
"v_smfmac_f32_16x16x64_bf16 v[80:83], v[60:63], a[208:215], v8\nv_smfmac_f32_16x16x64_bf16 v[68:71], v[64:67], a[48:55], v8\nv_smfmac_f32_16x16x64_bf16 v[72:75], v[64:67], a[104:111], v8\nv_smfmac_f32_16x16x64_bf16 v[76:79], v[64:67], a[160:167], v8\n"
"s_cbranch_execz vc19_p0\nbuffer_load_dwordx4 v[116:119], v20, s[8:11], s41 offen offset:0 sc1\nbuffer_load_dwordx4 v[120:123], v20, s[8:11], s41 offen offset:1024 sc1\nvc19_p0:\n"
"v_smfmac_f32_16x16x64_bf16 v[80:83], v[64:67], a[216:223], v8\nv_add_f32 v219, v68, v69\nv_add_f32 v220, v70, v71\nv_cvt_pk_bf16_f32 v156, v219, v220\n"
"s_nop 0\nv_add_f32 v219, v72, v73\nv_add_f32 v220, v74, v75\nv_cvt_pk_bf16_f32 v157, v219, v220\n"
"s_nop 0\nv_add_f32 v219, v76, v77\nv_add_f32 v220, v78, v79\nv_cvt_pk_bf16_f32 v158, v219, v220\n"
"s_nop 0\nv_add_f32 v219, v80, v81\nv_add_f32 v220, v82, v83\nv_cvt_pk_bf16_f32 v159, v219, v220\n"
"s_nop 0\ns_nop 1\ns_nop 1\ns_nop 1\n"
"s_nop 1\ns_nop 1\ns_nop 1\ns_cbranch_execz vd19_p0\n"
"buffer_load_dwordx4 v[124:127], v21, s[8:11], s41 offen offset:0 sc1\nbuffer_load_dwordx4 v[128:131], v21, s[8:11], s41 offen offset:1024 sc1\ns_nop 15\ns_nop 15\n"
"s_nop 12\nvd19_p0:\ns_cbranch_execnz tl24_p0\nva20_p0:\n"
"s_branch vb20_p0\nsx20_p0:\ns_add_u32 s60, s60, 5520\ns_addc_u32 s61, s61, 0\n"
"s_setpc_b64 s[60:61]\nvb20_p0:\nv_smfmac_f32_16x16x64_bf16 v[72:75], v[56:59], a[88:95], v8\nv_smfmac_f32_16x16x64_bf16 v[76:79], v[56:59], a[144:151], v8\n"
"v_smfmac_f32_16x16x64_bf16 v[80:83], v[56:59], a[200:207], v8\nv_smfmac_f32_16x16x64_bf16 v[68:71], v[60:63], a[40:47], v8\nv_smfmac_f32_16x16x64_bf16 v[72:75], v[60:63], a[96:103], v8\nv_smfmac_f32_16x16x64_bf16 v[76:79], v[60:63], a[152:159], v8\n"
"v_smfmac_f32_16x16x64_bf16 v[80:83], v[60:63], a[208:215], v8\nv_smfmac_f32_16x16x64_bf16 v[68:71], v[64:67], a[48:55], v8\nv_smfmac_f32_16x16x64_bf16 v[72:75], v[64:67], a[104:111], v8\nv_smfmac_f32_16x16x64_bf16 v[76:79], v[64:67], a[160:167], v8\n"
"v_smfmac_f32_16x16x64_bf16 v[80:83], v[64:67], a[216:223], v8\nsgn_p0:\ns_cbranch_execz vc20_p0\nbuffer_load_dwordx4 v[116:119], v20, s[8:11], s41 offen offset:0 sc1\n"
"buffer_load_dwordx4 v[120:123], v20, s[8:11], s41 offen offset:1024 sc1\nvc20_p0:\nv_add_f32 v219, v68, v69\nv_add_f32 v220, v70, v71\n"
"v_cvt_pk_bf16_f32 v156, v219, v220\ns_nop 0\nv_add_f32 v219, v72, v73\nv_add_f32 v220, v74, v75\n"
"v_cvt_pk_bf16_f32 v157, v219, v220\ns_nop 0\nv_add_f32 v219, v76, v77\nv_add_f32 v220, v78, v79\n"
"v_cvt_pk_bf16_f32 v158, v219, v220\ns_nop 0\nv_add_f32 v219, v80, v81\nv_add_f32 v220, v82, v83\n"
"v_cvt_pk_bf16_f32 v159, v219, v220\ns_nop 0\ns_nop 1\ns_nop 1\n"
"s_nop 1\ns_nop 1\ns_nop 1\ns_nop 1\n"
"s_nop 1\ns_nop 1\ns_cbranch_execz vd20_p0\nbuffer_load_dwordx4 v[124:127], v21, s[8:11], s41 offen offset:0 sc1\n"
"buffer_load_dwordx4 v[128:131], v21, s[8:11], s41 offen offset:1024 sc1\ns_nop 15\ns_nop 15\ns_nop 12\n"
"vd20_p0:\ns_cbranch_execnz tl24_p0\nva21_p0:\ns_branch vb21_p0\n"
"sx21_p0:\ns_add_u32 s60, s60, 5796\ns_addc_u32 s61, s61, 0\ns_setpc_b64 s[60:61]\n"
"vb21_p0:\nv_smfmac_f32_16x16x64_bf16 v[76:79], v[56:59], a[144:151], v8\nv_smfmac_f32_16x16x64_bf16 v[80:83], v[56:59], a[200:207], v8\nv_smfmac_f32_16x16x64_bf16 v[68:71], v[60:63], a[40:47], v8\n"
"v_smfmac_f32_16x16x64_bf16 v[72:75], v[60:63], a[96:103], v8\nv_smfmac_f32_16x16x64_bf16 v[76:79], v[60:63], a[152:159], v8\nv_smfmac_f32_16x16x64_bf16 v[80:83], v[60:63], a[208:215], v8\nv_smfmac_f32_16x16x64_bf16 v[68:71], v[64:67], a[48:55], v8\n"
"v_smfmac_f32_16x16x64_bf16 v[72:75], v[64:67], a[104:111], v8\nv_smfmac_f32_16x16x64_bf16 v[76:79], v[64:67], a[160:167], v8\nv_smfmac_f32_16x16x64_bf16 v[80:83], v[64:67], a[216:223], v8\ns_nop 1\n"
"s_nop 1\ns_cbranch_execz vc21_p0\nbuffer_load_dwordx4 v[116:119], v20, s[8:11], s41 offen offset:0 sc1\nbuffer_load_dwordx4 v[120:123], v20, s[8:11], s41 offen offset:1024 sc1\n"
"vc21_p0:\nv_add_f32 v219, v68, v69\nv_add_f32 v220, v70, v71\nv_cvt_pk_bf16_f32 v156, v219, v220\n"
"s_nop 0\nv_add_f32 v219, v72, v73\nv_add_f32 v220, v74, v75\nv_cvt_pk_bf16_f32 v157, v219, v220\n"
"s_nop 0\nv_add_f32 v219, v76, v77\nv_add_f32 v220, v78, v79\nv_cvt_pk_bf16_f32 v158, v219, v220\n"
"s_nop 0\nv_add_f32 v219, v80, v81\nv_add_f32 v220, v82, v83\nv_cvt_pk_bf16_f32 v159, v219, v220\n"
"s_nop 0\ns_nop 1\ns_nop 1\ns_nop 1\n"
"s_nop 1\ns_nop 1\ns_nop 1\ns_nop 1\n"
"s_nop 1\ns_cbranch_execz vd21_p0\nbuffer_load_dwordx4 v[124:127], v21, s[8:11], s41 offen offset:0 sc1\nbuffer_load_dwordx4 v[128:131], v21, s[8:11], s41 offen offset:1024 sc1\n"
"s_nop 15\ns_nop 15\ns_nop 12\nvd21_p0:\n"
"s_cbranch_execnz tl24_p0\nva22_p0:\ns_branch vb22_p0\nsx22_p0:\n"
"s_add_u32 s60, s60, 6072\ns_addc_u32 s61, s61, 0\ns_setpc_b64 s[60:61]\nvb22_p0:\n"
"v_smfmac_f32_16x16x64_bf16 v[80:83], v[56:59], a[200:207], v8\nv_smfmac_f32_16x16x64_bf16 v[68:71], v[60:63], a[40:47], v8\nv_smfmac_f32_16x16x64_bf16 v[72:75], v[60:63], a[96:103], v8\nv_smfmac_f32_16x16x64_bf16 v[76:79], v[60:63], a[152:159], v8\n"
"v_smfmac_f32_16x16x64_bf16 v[80:83], v[60:63], a[208:215], v8\nv_smfmac_f32_16x16x64_bf16 v[68:71], v[64:67], a[48:55], v8\nv_smfmac_f32_16x16x64_bf16 v[72:75], v[64:67], a[104:111], v8\nv_smfmac_f32_16x16x64_bf16 v[76:79], v[64:67], a[160:167], v8\n"
"v_smfmac_f32_16x16x64_bf16 v[80:83], v[64:67], a[216:223], v8\ns_nop 1\ns_nop 1\ns_nop 1\n"
"s_nop 1\ns_cbranch_execz vc22_p0\nbuffer_load_dwordx4 v[116:119], v20, s[8:11], s41 offen offset:0 sc1\nbuffer_load_dwordx4 v[120:123], v20, s[8:11], s41 offen offset:1024 sc1\n"
"vc22_p0:\nv_add_f32 v219, v68, v69\nv_add_f32 v220, v70, v71\nv_cvt_pk_bf16_f32 v156, v219, v220\n"
"s_nop 0\nv_add_f32 v219, v72, v73\nv_add_f32 v220, v74, v75\nv_cvt_pk_bf16_f32 v157, v219, v220\n"
"s_nop 0\nv_add_f32 v219, v76, v77\nv_add_f32 v220, v78, v79\nv_cvt_pk_bf16_f32 v158, v219, v220\n"
"s_nop 0\nv_add_f32 v219, v80, v81\nv_add_f32 v220, v82, v83\nv_cvt_pk_bf16_f32 v159, v219, v220\n"
"s_nop 0\ns_nop 1\ns_nop 1\ns_nop 1\n"
"s_nop 1\ns_nop 1\ns_nop 1\ns_nop 1\n"
"s_nop 1\ns_cbranch_execz vd22_p0\nbuffer_load_dwordx4 v[124:127], v21, s[8:11], s41 offen offset:0 sc1\nbuffer_load_dwordx4 v[128:131], v21, s[8:11], s41 offen offset:1024 sc1\n"
"s_nop 15\ns_nop 15\ns_nop 12\nvd22_p0:\n"
"s_cbranch_execnz tl24_p0\nva23_p0:\ns_branch vb23_p0\nsx23_p0:\n"
"s_add_u32 s60, s60, 6348\ns_addc_u32 s61, s61, 0\ns_setpc_b64 s[60:61]\nvb23_p0:\n"
"v_smfmac_f32_16x16x64_bf16 v[68:71], v[60:63], a[40:47], v8\nv_smfmac_f32_16x16x64_bf16 v[72:75], v[60:63], a[96:103], v8\nv_smfmac_f32_16x16x64_bf16 v[76:79], v[60:63], a[152:159], v8\nv_smfmac_f32_16x16x64_bf16 v[80:83], v[60:63], a[208:215], v8\n"
"v_smfmac_f32_16x16x64_bf16 v[68:71], v[64:67], a[48:55], v8\nv_smfmac_f32_16x16x64_bf16 v[72:75], v[64:67], a[104:111], v8\nv_smfmac_f32_16x16x64_bf16 v[76:79], v[64:67], a[160:167], v8\nv_smfmac_f32_16x16x64_bf16 v[80:83], v[64:67], a[216:223], v8\n"
"s_nop 1\ns_nop 1\ns_nop 1\ns_nop 1\n"
"s_nop 1\ns_nop 1\ns_cbranch_execz vc23_p0\nbuffer_load_dwordx4 v[116:119], v20, s[8:11], s41 offen offset:0 sc1\n"
"buffer_load_dwordx4 v[120:123], v20, s[8:11], s41 offen offset:1024 sc1\nvc23_p0:\nv_add_f32 v219, v68, v69\nv_add_f32 v220, v70, v71\n"
"v_cvt_pk_bf16_f32 v156, v219, v220\ns_nop 0\nv_add_f32 v219, v72, v73\nv_add_f32 v220, v74, v75\n"
"v_cvt_pk_bf16_f32 v157, v219, v220\ns_nop 0\nv_add_f32 v219, v76, v77\nv_add_f32 v220, v78, v79\n"
"v_cvt_pk_bf16_f32 v158, v219, v220\ns_nop 0\nv_add_f32 v219, v80, v81\nv_add_f32 v220, v82, v83\n"
"v_cvt_pk_bf16_f32 v159, v219, v220\ns_nop 0\ns_nop 1\ns_nop 1\n"
"s_nop 1\ns_nop 1\ns_nop 1\ns_nop 1\n"
"s_nop 1\ns_nop 1\ns_cbranch_execz vd23_p0\nbuffer_load_dwordx4 v[124:127], v21, s[8:11], s41 offen offset:0 sc1\n"
"buffer_load_dwordx4 v[128:131], v21, s[8:11], s41 offen offset:1024 sc1\ns_nop 15\ns_nop 15\ns_nop 12\n"
"vd23_p0:\ns_cbranch_execnz tl24_p0\nva24_p0:\ns_branch vb24_p0\n"
"sx24_p0:\ns_add_u32 s60, s60, 6624\ns_addc_u32 s61, s61, 0\ns_setpc_b64 s[60:61]\n"
"vb24_p0:\nv_smfmac_f32_16x16x64_bf16 v[72:75], v[60:63], a[96:103], v8\nv_smfmac_f32_16x16x64_bf16 v[76:79], v[60:63], a[152:159], v8\nv_smfmac_f32_16x16x64_bf16 v[80:83], v[60:63], a[208:215], v8\n"
"v_smfmac_f32_16x16x64_bf16 v[68:71], v[64:67], a[48:55], v8\nv_smfmac_f32_16x16x64_bf16 v[72:75], v[64:67], a[104:111], v8\nv_smfmac_f32_16x16x64_bf16 v[76:79], v[64:67], a[160:167], v8\nv_smfmac_f32_16x16x64_bf16 v[80:83], v[64:67], a[216:223], v8\n"
"s_nop 1\ns_nop 1\ns_nop 1\ns_nop 1\n"
"s_nop 1\ns_nop 1\ns_nop 1\ns_nop 1\n"
"s_cbranch_execz vc24_p0\nbuffer_load_dwordx4 v[116:119], v20, s[8:11], s41 offen offset:0 sc1\nbuffer_load_dwordx4 v[120:123], v20, s[8:11], s41 offen offset:1024 sc1\nvc24_p0:\n"
"v_add_f32 v219, v68, v69\nv_add_f32 v220, v70, v71\nv_cvt_pk_bf16_f32 v156, v219, v220\ns_nop 0\n"
"v_add_f32 v219, v72, v73\nv_add_f32 v220, v74, v75\nv_cvt_pk_bf16_f32 v157, v219, v220\ns_nop 0\n"
"v_add_f32 v219, v76, v77\nv_add_f32 v220, v78, v79\nv_cvt_pk_bf16_f32 v158, v219, v220\ns_nop 0\n"
"v_add_f32 v219, v80, v81\nv_add_f32 v220, v82, v83\nv_cvt_pk_bf16_f32 v159, v219, v220\ns_nop 0\n"
"s_nop 1\ns_nop 1\ns_nop 1\ns_nop 1\n"
"s_nop 1\ns_nop 1\ns_nop 1\ns_nop 1\n"
"s_cbranch_execz vd24_p0\nbuffer_load_dwordx4 v[124:127], v21, s[8:11], s41 offen offset:0 sc1\nbuffer_load_dwordx4 v[128:131], v21, s[8:11], s41 offen offset:1024 sc1\ns_nop 15\n"
"s_nop 15\ns_nop 12\nvd24_p0:\ns_cbranch_execnz tl24_p0\n"
"va25_p0:\ns_branch vb25_p0\nsx25_p0:\ns_add_u32 s60, s60, 6900\n"
"s_addc_u32 s61, s61, 0\ns_setpc_b64 s[60:61]\nvb25_p0:\nv_smfmac_f32_16x16x64_bf16 v[76:79], v[60:63], a[152:159], v8\n"
"v_smfmac_f32_16x16x64_bf16 v[80:83], v[60:63], a[208:215], v8\nv_smfmac_f32_16x16x64_bf16 v[68:71], v[64:67], a[48:55], v8\nv_smfmac_f32_16x16x64_bf16 v[72:75], v[64:67], a[104:111], v8\nv_smfmac_f32_16x16x64_bf16 v[76:79], v[64:67], a[160:167], v8\n"
"v_smfmac_f32_16x16x64_bf16 v[80:83], v[64:67], a[216:223], v8\ns_nop 1\ns_nop 1\ns_nop 1\n"
"s_nop 1\ns_nop 1\ns_nop 1\ns_nop 1\n"
"s_nop 1\ns_nop 1\ns_nop 1\ns_cbranch_execz vc25_p0\n"
"buffer_load_dwordx4 v[116:119], v20, s[8:11], s41 offen offset:0 sc1\nbuffer_load_dwordx4 v[120:123], v20, s[8:11], s41 offen offset:1024 sc1\nvc25_p0:\nv_add_f32 v219, v68, v69\n"
"v_add_f32 v220, v70, v71\nv_cvt_pk_bf16_f32 v156, v219, v220\ns_nop 0\nv_add_f32 v219, v72, v73\n"
"v_add_f32 v220, v74, v75\nv_cvt_pk_bf16_f32 v157, v219, v220\ns_nop 0\nv_add_f32 v219, v76, v77\n"
"v_add_f32 v220, v78, v79\nv_cvt_pk_bf16_f32 v158, v219, v220\ns_nop 0\nv_add_f32 v219, v80, v81\n"
"v_add_f32 v220, v82, v83\nv_cvt_pk_bf16_f32 v159, v219, v220\ns_nop 0\ns_nop 1\n"
"s_nop 1\ns_nop 1\ns_nop 1\ns_nop 1\n"
"s_nop 1\ns_nop 1\ns_nop 1\ns_cbranch_execz vd25_p0\n"
"buffer_load_dwordx4 v[124:127], v21, s[8:11], s41 offen offset:0 sc1\nbuffer_load_dwordx4 v[128:131], v21, s[8:11], s41 offen offset:1024 sc1\ns_nop 15\ns_nop 15\n"
"s_nop 12\nvd25_p0:\ns_cbranch_execnz tl24_p0\nva26_p0:\n"
"s_branch vb26_p0\nsx26_p0:\ns_add_u32 s60, s60, 7176\ns_addc_u32 s61, s61, 0\n"
"s_setpc_b64 s[60:61]\nvb26_p0:\nv_smfmac_f32_16x16x64_bf16 v[80:83], v[60:63], a[208:215], v8\nv_smfmac_f32_16x16x64_bf16 v[68:71], v[64:67], a[48:55], v8\n"
"v_smfmac_f32_16x16x64_bf16 v[72:75], v[64:67], a[104:111], v8\nv_smfmac_f32_16x16x64_bf16 v[76:79], v[64:67], a[160:167], v8\nv_smfmac_f32_16x16x64_bf16 v[80:83], v[64:67], a[216:223], v8\ns_nop 1\n"
"s_nop 1\ns_nop 1\ns_nop 1\ns_nop 1\n"
"s_nop 1\ns_nop 1\ns_nop 1\ns_nop 1\n"
"s_nop 1\ns_nop 1\ns_nop 1\ns_cbranch_execz vc26_p0\n"
"buffer_load_dwordx4 v[116:119], v20, s[8:11], s41 offen offset:0 sc1\nbuffer_load_dwordx4 v[120:123], v20, s[8:11], s41 offen offset:1024 sc1\nvc26_p0:\nv_add_f32 v219, v68, v69\n"
"v_add_f32 v220, v70, v71\nv_cvt_pk_bf16_f32 v156, v219, v220\ns_nop 0\nv_add_f32 v219, v72, v73\n"
"v_add_f32 v220, v74, v75\nv_cvt_pk_bf16_f32 v157, v219, v220\ns_nop 0\nv_add_f32 v219, v76, v77\n"
"v_add_f32 v220, v78, v79\nv_cvt_pk_bf16_f32 v158, v219, v220\ns_nop 0\nv_add_f32 v219, v80, v81\n"
"v_add_f32 v220, v82, v83\nv_cvt_pk_bf16_f32 v159, v219, v220\ns_nop 0\ns_nop 1\n"
"s_nop 1\ns_nop 1\ns_nop 1\ns_nop 1\n"
"s_nop 1\ns_nop 1\ns_nop 1\ns_cbranch_execz vd26_p0\n"
"buffer_load_dwordx4 v[124:127], v21, s[8:11], s41 offen offset:0 sc1\nbuffer_load_dwordx4 v[128:131], v21, s[8:11], s41 offen offset:1024 sc1\ns_nop 15\ns_nop 15\n"
"s_nop 12\nvd26_p0:\ns_cbranch_execnz tl24_p0\nva27_p0:\n"
"s_branch warm_p0\nsx27_p0:\ns_add_u32 s60, s60, 7452\ns_addc_u32 s61, s61, 0\n"
"s_setpc_b64 s[60:61]\nvb27_p0:\nv_smfmac_f32_16x16x64_bf16 v[68:71], v[64:67], a[48:55], v8\nv_smfmac_f32_16x16x64_bf16 v[72:75], v[64:67], a[104:111], v8\n"
"v_smfmac_f32_16x16x64_bf16 v[76:79], v[64:67], a[160:167], v8\nv_smfmac_f32_16x16x64_bf16 v[80:83], v[64:67], a[216:223], v8\ns_nop 1\ns_nop 1\n"
"s_nop 1\ns_nop 1\ns_nop 1\ns_nop 1\n"
"s_nop 1\ns_nop 1\ns_nop 1\ns_nop 1\n"
"s_nop 1\ns_nop 1\ns_nop 1\ns_nop 1\n"
"s_cbranch_execz vc27_p0\nbuffer_load_dwordx4 v[116:119], v20, s[8:11], s41 offen offset:0 sc1\nbuffer_load_dwordx4 v[120:123], v20, s[8:11], s41 offen offset:1024 sc1\nvc27_p0:\n"
"v_add_f32 v219, v68, v69\nv_add_f32 v220, v70, v71\nv_cvt_pk_bf16_f32 v156, v219, v220\ns_nop 0\n"
"v_add_f32 v219, v72, v73\nv_add_f32 v220, v74, v75\nv_cvt_pk_bf16_f32 v157, v219, v220\ns_nop 0\n"
"v_add_f32 v219, v76, v77\nv_add_f32 v220, v78, v79\nv_cvt_pk_bf16_f32 v158, v219, v220\ns_nop 0\n"
"v_add_f32 v219, v80, v81\nv_add_f32 v220, v82, v83\nv_cvt_pk_bf16_f32 v159, v219, v220\ns_nop 0\n"
"s_nop 1\ns_nop 1\ns_nop 1\ns_nop 1\n"
"s_nop 1\ns_nop 1\ns_nop 1\ns_nop 1\n"
"s_cbranch_execz vd27_p0\nbuffer_load_dwordx4 v[124:127], v21, s[8:11], s41 offen offset:0 sc1\nbuffer_load_dwordx4 v[128:131], v21, s[8:11], s41 offen offset:1024 sc1\ns_nop 15\n"
"s_nop 15\ns_nop 12\nvd27_p0:\ns_cbranch_execnz tl24_p0\n"
"va28_p0:\ns_branch vb28_p0\nsx28_p0:\ns_add_u32 s60, s60, 7728\n"
"s_addc_u32 s61, s61, 0\ns_setpc_b64 s[60:61]\nvb28_p0:\nv_smfmac_f32_16x16x64_bf16 v[72:75], v[64:67], a[104:111], v8\n"
"v_smfmac_f32_16x16x64_bf16 v[76:79], v[64:67], a[160:167], v8\nv_smfmac_f32_16x16x64_bf16 v[80:83], v[64:67], a[216:223], v8\ns_nop 1\ns_nop 1\n"
"s_nop 1\ns_nop 1\ns_nop 1\ns_nop 1\n"
"s_nop 1\ns_nop 1\ns_nop 1\ns_nop 1\n"
"s_nop 1\ns_nop 1\ns_nop 1\ns_nop 1\n"
"s_nop 1\ns_nop 1\ns_cbranch_execz vc28_p0\nbuffer_load_dwordx4 v[116:119], v20, s[8:11], s41 offen offset:0 sc1\n"
"buffer_load_dwordx4 v[120:123], v20, s[8:11], s41 offen offset:1024 sc1\nvc28_p0:\nv_add_f32 v219, v68, v69\nv_add_f32 v220, v70, v71\n"
"v_cvt_pk_bf16_f32 v156, v219, v220\ns_nop 0\nv_add_f32 v219, v72, v73\nv_add_f32 v220, v74, v75\n"
"v_cvt_pk_bf16_f32 v157, v219, v220\ns_nop 0\nv_add_f32 v219, v76, v77\nv_add_f32 v220, v78, v79\n"
"v_cvt_pk_bf16_f32 v158, v219, v220\ns_nop 0\nv_add_f32 v219, v80, v81\nv_add_f32 v220, v82, v83\n"
"v_cvt_pk_bf16_f32 v159, v219, v220\ns_nop 0\ns_nop 1\ns_nop 1\n"
"s_nop 1\ns_nop 1\ns_nop 1\ns_nop 1\n"
"s_nop 1\ns_nop 1\ns_cbranch_execz vd28_p0\nbuffer_load_dwordx4 v[124:127], v21, s[8:11], s41 offen offset:0 sc1\n"
"buffer_load_dwordx4 v[128:131], v21, s[8:11], s41 offen offset:1024 sc1\ns_nop 15\ns_nop 15\ns_nop 12\n"
"vd28_p0:\ns_cbranch_execnz tl24_p0\nva29_p0:\ns_branch vb29_p0\n"
"sx29_p0:\ns_add_u32 s60, s60, 8004\ns_addc_u32 s61, s61, 0\ns_setpc_b64 s[60:61]\n"
"vb29_p0:\nv_smfmac_f32_16x16x64_bf16 v[76:79], v[64:67], a[160:167], v8\nv_smfmac_f32_16x16x64_bf16 v[80:83], v[64:67], a[216:223], v8\ns_nop 1\n"
"s_nop 1\ns_nop 1\ns_nop 1\ns_nop 1\n"
"s_nop 1\ns_nop 1\ns_nop 1\ns_nop 1\n"
"s_nop 1\ns_nop 1\ns_nop 1\ns_nop 1\n"
"s_nop 1\ns_nop 1\ns_nop 1\ns_nop 1\n"
"s_nop 1\ns_cbranch_execz vc29_p0\nbuffer_load_dwordx4 v[116:119], v20, s[8:11], s41 offen offset:0 sc1\nbuffer_load_dwordx4 v[120:123], v20, s[8:11], s41 offen offset:1024 sc1\n"
"vc29_p0:\nv_add_f32 v219, v68, v69\nv_add_f32 v220, v70, v71\nv_cvt_pk_bf16_f32 v156, v219, v220\n"
"s_nop 0\nv_add_f32 v219, v72, v73\nv_add_f32 v220, v74, v75\nv_cvt_pk_bf16_f32 v157, v219, v220\n"
"s_nop 0\nv_add_f32 v219, v76, v77\nv_add_f32 v220, v78, v79\nv_cvt_pk_bf16_f32 v158, v219, v220\n"
"s_nop 0\nv_add_f32 v219, v80, v81\nv_add_f32 v220, v82, v83\nv_cvt_pk_bf16_f32 v159, v219, v220\n"
"s_nop 0\ns_nop 1\ns_nop 1\ns_nop 1\n"
"s_nop 1\ns_nop 1\ns_nop 1\ns_nop 1\n"
"s_nop 1\ns_cbranch_execz vd29_p0\nbuffer_load_dwordx4 v[124:127], v21, s[8:11], s41 offen offset:0 sc1\nbuffer_load_dwordx4 v[128:131], v21, s[8:11], s41 offen offset:1024 sc1\n"
"s_nop 15\ns_nop 15\ns_nop 12\nvd29_p0:\n"
"s_cbranch_execnz tl24_p0\nva30_p0:\ns_branch vb30_p0\nsx30_p0:\n"
"s_add_u32 s60, s60, 8280\ns_addc_u32 s61, s61, 0\ns_setpc_b64 s[60:61]\nvb30_p0:\n"
"v_smfmac_f32_16x16x64_bf16 v[80:83], v[64:67], a[216:223], v8\ns_nop 1\ns_nop 1\ns_nop 1\n"
"s_nop 1\ns_nop 1\ns_nop 1\ns_nop 1\n"
"s_nop 1\ns_nop 1\ns_nop 1\ns_nop 1\n"
"s_nop 1\ns_nop 1\ns_nop 1\ns_nop 1\n"
"s_nop 1\ns_nop 1\ns_nop 1\ns_nop 1\n"
"s_nop 1\ns_cbranch_execz vc30_p0\nbuffer_load_dwordx4 v[116:119], v20, s[8:11], s41 offen offset:0 sc1\nbuffer_load_dwordx4 v[120:123], v20, s[8:11], s41 offen offset:1024 sc1\n"
"vc30_p0:\nv_add_f32 v219, v68, v69\nv_add_f32 v220, v70, v71\nv_cvt_pk_bf16_f32 v156, v219, v220\n"
"s_nop 0\nv_add_f32 v219, v72, v73\nv_add_f32 v220, v74, v75\nv_cvt_pk_bf16_f32 v157, v219, v220\n"
"s_nop 0\nv_add_f32 v219, v76, v77\nv_add_f32 v220, v78, v79\nv_cvt_pk_bf16_f32 v158, v219, v220\n"
"s_nop 0\nv_add_f32 v219, v80, v81\nv_add_f32 v220, v82, v83\nv_cvt_pk_bf16_f32 v159, v219, v220\n"
"s_nop 0\ns_nop 1\ns_nop 1\ns_nop 1\n"
"s_nop 1\ns_nop 1\ns_nop 1\ns_nop 1\n"
"s_nop 1\ns_cbranch_execz vd30_p0\nbuffer_load_dwordx4 v[124:127], v21, s[8:11], s41 offen offset:0 sc1\nbuffer_load_dwordx4 v[128:131], v21, s[8:11], s41 offen offset:1024 sc1\n"
"s_nop 15\ns_nop 15\ns_nop 12\nvd30_p0:\n"
"s_cbranch_execnz tl24_p0\nva31_p0:\ns_branch vb31_p0\nsx31_p0:\n"
"s_add_u32 s60, s60, 8556\ns_addc_u32 s61, s61, 0\ns_setpc_b64 s[60:61]\nvb31_p0:\n"
"s_nop 1\ns_nop 1\ns_nop 1\ns_nop 1\n"
"s_nop 1\ns_nop 1\ns_nop 1\ns_nop 1\n"
"s_nop 1\ns_nop 1\ns_nop 1\ns_nop 1\n"
"s_nop 1\ns_nop 1\ns_nop 1\ns_nop 1\n"
"s_nop 1\ns_nop 1\ns_nop 1\ns_nop 1\n"
"s_nop 1\ns_nop 1\ns_cbranch_execz vc31_p0\nbuffer_load_dwordx4 v[116:119], v20, s[8:11], s41 offen offset:0 sc1\n"
"buffer_load_dwordx4 v[120:123], v20, s[8:11], s41 offen offset:1024 sc1\nvc31_p0:\nv_add_f32 v219, v68, v69\nv_add_f32 v220, v70, v71\n"
"v_cvt_pk_bf16_f32 v156, v219, v220\ns_nop 0\nv_add_f32 v219, v72, v73\nv_add_f32 v220, v74, v75\n"
"v_cvt_pk_bf16_f32 v157, v219, v220\ns_nop 0\nv_add_f32 v219, v76, v77\nv_add_f32 v220, v78, v79\n"
"v_cvt_pk_bf16_f32 v158, v219, v220\ns_nop 0\nv_add_f32 v219, v80, v81\nv_add_f32 v220, v82, v83\n"
"v_cvt_pk_bf16_f32 v159, v219, v220\ns_nop 0\ns_nop 1\ns_nop 1\n"
"s_nop 1\ns_nop 1\ns_nop 1\ns_nop 1\n"
"s_nop 1\ns_nop 1\ns_cbranch_execz vd31_p0\nbuffer_load_dwordx4 v[124:127], v21, s[8:11], s41 offen offset:0 sc1\n"
"buffer_load_dwordx4 v[128:131], v21, s[8:11], s41 offen offset:1024 sc1\ns_nop 15\ns_nop 15\ns_nop 12\n"
"vd31_p0:\ns_cbranch_execnz tl24_p0\nva32_p0:\ns_memrealtime s[54:55]\n"
"s_waitcnt lgkmcnt(0)\ns_add_u32 s90, s54, 0x1000\ns_cselect_b32 s90, -1, s90\ns_mov_b32 s56, s54\n"
"s_mov_b32 s58, s54\ns_getpc_b64 s[60:61]\ndq_p0:\ns_sub_u32 s60, s60, dq_p0-vb0_p0\n"
"s_subb_u32 s61, s61, 0\ns_branch gr_p0\nend_p0:\ns_branch fk_done\n"
"fk_done:\ns_endpgm\n"
    :: "{s[8:23]}"(s0), "{s[24:39]}"(s1), "{v[8:23]}"(v0), "{v[24:39]}"(v1) : "v40", "v41", "v42", "v43", "v44", "v45", "v46", "v47", "v48", "v49", "v50", "v51", "v52", "v53", "v54", "v55", "v56", "v57", "v58", "v59", "v60", "v61", "v62", "v63", "v64", "v65", "v66", "v67", "v68", "v69", "v70", "v71", "v72", "v73", "v74", "v75", "v76", "v77", "v78", "v79", "v80", "v81", "v82", "v83", "v84", "v85", "v86", "v87", "v88", "v89", "v90", "v91", "v92", "v93", "v94", "v95", "v96", "v97", "v98", "v99", "v100", "v101", "v102", "v103", "v104", "v105", "v106", "v107", "v108", "v109", "v110", "v111", "v112", "v113", "v114", "v115", "v116", "v117", "v118", "v119", "v120", "v121", "v122", "v123", "v124", "v125", "v126", "v127", "v128", "v129", "v130", "v131", "v132", "v133", "v134", "v135", "v136", "v137", "v138", "v139", "v140", "v141", "v142", "v143", "v144", "v145", "v146", "v147", "v148", "v149", "v150", "v151", "v152", "v153", "v154", "v155", "v156", "v157", "v158", "v159", "v160", "v161", "v162", "v163", "v164", "v165", "v166", "v167", "v168", "v169", "v170", "v171", "v172", "v173", "v174", "v175", "v176", "v177", "v178", "v179", "v180", "v181", "v182", "v183", "v184", "v185", "v186", "v187", "v188", "v189", "v190", "v191", "v192", "v193", "v194", "v195", "v196", "v197", "v198", "v199", "v200", "v201", "v202", "v203", "v204", "v205", "v206", "v207", "v208", "v209", "v210", "v211", "v212", "v213", "v214", "v215", "v216", "v217", "v218", "v219", "v220", "v221", "v222", "v223", "v224", "v225", "v226", "v227", "v228", "v229", "v230", "v231", "v232", "v233", "v234", "v235", "v236", "v237", "v238", "v239", "v240", "v241", "v242", "v243", "v244", "v245", "v246", "v247", "v248", "v249", "v250", "v251", "v252", "v253", "v254", "v255", "a0", "a1", "a2", "a3", "a4", "a5", "a6", "a7", "a8", "a9", "a10", "a11", "a12", "a13", "a14", "a15", "a16", "a17", "a18", "a19", "a20", "a21", "a22", "a23", "a24", "a25", "a26", "a27", "a28", "a29", "a30", "a31", "a32", "a33", "a34", "a35", "a36", "a37", "a38", "a39", "a40", "a41", "a42", "a43", "a44", "a45", "a46", "a47", "a48", "a49", "a50", "a51", "a52", "a53", "a54", "a55", "a56", "a57", "a58", "a59", "a60", "a61", "a62", "a63", "a64", "a65", "a66", "a67", "a68", "a69", "a70", "a71", "a72", "a73", "a74", "a75", "a76", "a77", "a78", "a79", "a80", "a81", "a82", "a83", "a84", "a85", "a86", "a87", "a88", "a89", "a90", "a91", "a92", "a93", "a94", "a95", "a96", "a97", "a98", "a99", "a100", "a101", "a102", "a103", "a104", "a105", "a106", "a107", "a108", "a109", "a110", "a111", "a112", "a113", "a114", "a115", "a116", "a117", "a118", "a119", "a120", "a121", "a122", "a123", "a124", "a125", "a126", "a127", "a128", "a129", "a130", "a131", "a132", "a133", "a134", "a135", "a136", "a137", "a138", "a139", "a140", "a141", "a142", "a143", "a144", "a145", "a146", "a147", "a148", "a149", "a150", "a151", "a152", "a153", "a154", "a155", "a156", "a157", "a158", "a159", "a160", "a161", "a162", "a163", "a164", "a165", "a166", "a167", "a168", "a169", "a170", "a171", "a172", "a173", "a174", "a175", "a176", "a177", "a178", "a179", "a180", "a181", "a182", "a183", "a184", "a185", "a186", "a187", "a188", "a189", "a190", "a191", "a192", "a193", "a194", "a195", "a196", "a197", "a198", "a199", "a200", "a201", "a202", "a203", "a204", "a205", "a206", "a207", "a208", "a209", "a210", "a211", "a212", "a213", "a214", "a215", "a216", "a217", "a218", "a219", "a220", "a221", "a222", "a223", "a224", "a225", "a226", "a227", "a228", "a229", "a230", "a231", "a232", "a233", "a234", "a235", "a236", "a237", "a238", "a239", "a240", "a241", "a242", "a243", "a244", "a245", "a246", "a247", "a248", "a249", "a250", "a251", "a252", "a253", "a254", "a255", "s40", "s41", "s42", "s43", "s44", "s45", "s46", "s47", "s48", "s49", "s50", "s51", "s52", "s53", "s54", "s55", "s56", "s57", "s58", "s59", "s60", "s61", "s62", "s63", "s64", "s65", "s66", "s67", "s68", "s69", "s70", "s71", "s72", "s73", "s74", "s75", "s76", "s77", "s78", "s79", "s80", "s81", "s82", "s83", "s84", "s85", "s86", "s87", "s88", "s89", "s90", "s91", "s92", "s93", "s94", "s95", "vcc", "scc", "memory");
    __builtin_unreachable();
}
struct GCtx { u4 rw1, rx, rexp, rout, rhb; unsigned tag, rot, sw1, mw, mx, sfg, stay, csc, ostr, kb, p1, p2; unsigned vr0, vr1, vx, wa, xa, vexp, vimp, vo0, vo1, vo2, vo3, vfl, vpf, vpo, vk, p5; };
DI unsigned gemm_asm0(const GCtx& c) {
    const u16v s0 = {RFL(c.rw1[0]), RFL(c.rw1[1]), RFL(c.rw1[2]), RFL(c.rw1[3]), RFL(c.rx[0]), RFL(c.rx[1]), RFL(c.rx[2]), RFL(c.rx[3]), RFL(c.rexp[0]), RFL(c.rexp[1]), RFL(c.rexp[2]), RFL(c.rexp[3]), RFL(c.rout[0]), RFL(c.rout[1]), RFL(c.rout[2]), RFL(c.rout[3])}, s1 = {RFL(c.rhb[0]), RFL(c.rhb[1]), RFL(c.rhb[2]), RFL(c.rhb[3]), RFL(c.tag), RFL(c.rot), RFL(c.sw1), RFL(c.mw), RFL(c.mx), RFL(c.sfg), RFL(c.stay), RFL(c.csc), RFL(c.ostr), RFL(c.kb), RFL(c.p1), RFL(c.p2)}, v0 = {c.vr0, c.vr1, c.vx, c.wa, c.xa, c.vexp, c.vimp, c.vo0, c.vo1, c.vo2, c.vo3, c.vfl, c.vpf, c.vpo, c.vk, c.p5};
    unsigned o = 0;
    asm volatile(
"s_nop 4\ns_add_u32 s40, s29, 0\ns_add_u32 s41, s29, 1\ns_add_u32 s42, s29, 2\n"
"s_add_u32 s43, s29, 3\nv_xor_b32 v24, 0, v11\nv_xor_b32 v25, 64, v11\nv_xor_b32 v26, 128, v11\n"
"v_xor_b32 v27, 192, v11\nv_xor_b32 v28, 2064, v11\nv_xor_b32 v29, 2128, v11\nv_xor_b32 v30, 2192, v11\n"
"v_xor_b32 v31, 2256, v11\nv_add_u32 v40, s37, v24\nv_add_u32 v41, s37, v25\nv_add_u32 v42, s37, v26\n"
"v_add_u32 v43, s37, v27\nv_add_u32 v44, s37, v28\nv_add_u32 v45, s37, v29\nv_add_u32 v46, s37, v30\n"
"v_add_u32 v47, s37, v31\nds_read_b128 a[128:131], v40 offset:0\nds_read_b128 a[132:135], v41 offset:0\nds_read_b128 a[136:139], v42 offset:0\n"
"ds_read_b128 a[140:143], v43 offset:0\ns_lshl_b32 s53, s41, 19\ns_add_u32 s53, s53, s30\ns_add_u32 m0, s31, 65536\n"
"s_add_u32 s53, s53, 0\nbuffer_load_dwordx4 v8, s[8:11], s53 offen lds\nbuffer_load_dwordx4 v9, s[8:11], s53 offen offset:1024 lds\nds_read_b128 a[144:147], v40 offset:256\n"
"ds_read_b128 a[148:151], v41 offset:256\nds_read_b128 a[152:155], v42 offset:256\nds_read_b128 a[156:159], v43 offset:256\ns_lshl_b32 s53, s41, 19\n"
"s_add_u32 s53, s53, s30\ns_add_u32 m0, s31, 73728\ns_add_u32 s53, s53, 131072\nbuffer_load_dwordx4 v8, s[8:11], s53 offen lds\n"
"buffer_load_dwordx4 v9, s[8:11], s53 offen offset:1024 lds\ns_waitcnt lgkmcnt(0)\nds_read_b128 a[160:163], v44 offset:0\nds_read_b128 a[164:167], v45 offset:0\n"
"ds_read_b128 a[168:171], v46 offset:0\nds_read_b128 a[172:175], v47 offset:0\ns_lshl_b32 s53, s41, 19\ns_add_u32 s53, s53, s30\n"
"s_add_u32 m0, s31, 81920\ns_add_u32 s53, s53, 262144\nbuffer_load_dwordx4 v8, s[8:11], s53 offen lds\nbuffer_load_dwordx4 v9, s[8:11], s53 offen offset:1024 lds\n"
"ds_read_b128 a[176:179], v44 offset:256\nds_read_b128 a[180:183], v45 offset:256\nds_read_b128 a[184:187], v46 offset:256\nds_read_b128 a[188:191], v47 offset:256\n"
"s_lshl_b32 s53, s41, 19\ns_add_u32 s53, s53, s30\ns_add_u32 m0, s31, 90112\ns_add_u32 s53, s53, 393216\n"
"buffer_load_dwordx4 v8, s[8:11], s53 offen lds\nbuffer_load_dwordx4 v9, s[8:11], s53 offen offset:1024 lds\ns_waitcnt lgkmcnt(0)\nv_add_u32 v40, 65536, v24\n"
"v_add_u32 v41, 65536, v25\nv_add_u32 v42, 65536, v26\nv_add_u32 v43, 65536, v27\nv_add_u32 v44, 65536, v28\n"
"v_add_u32 v45, 65536, v29\nv_add_u32 v46, 65536, v30\nv_add_u32 v47, 65536, v31\nv_xor_b32 v32, 0, v12\n"
"v_xor_b32 v33, 2048, v12\nv_xor_b32 v34, 64, v12\nv_xor_b32 v35, 2112, v12\ns_mov_b32 s55, 0\n"
"s_waitcnt vmcnt(10)\ns_barrier\nds_read_b128 v[96:99], v24 offset:0\nds_read_b128 v[100:103], v28 offset:0\n"
"ds_read_b128 v[104:107], v25 offset:0\nds_read_b128 v[108:111], v29 offset:0\nds_read_b128 v[112:115], v26 offset:0\nds_read_b128 v[116:119], v30 offset:0\n"
"ds_read_b128 v[120:123], v27 offset:0\nds_read_b128 v[124:127], v31 offset:0\ns_waitcnt lgkmcnt(6)\nv_mfma_f32_16x16x32_bf16 v[64:67], v[96:99], a[128:131], 0\n"
"s_lshl_b32 s53, s42, 19\ns_add_u32 s53, s53, s30\nv_mfma_f32_16x16x32_bf16 v[68:71], v[96:99], a[160:163], 0\nds_read_b128 v[96:99], v24 offset:256\n"
"s_add_u32 m0, s31, 98304\ns_add_u32 s53, s53, 0\nv_mfma_f32_16x16x32_bf16 v[72:75], v[100:103], a[128:131], 0\nbuffer_load_dwordx4 v8, s[8:11], s53 offen lds\n"
"v_mfma_f32_16x16x32_bf16 v[76:79], v[100:103], a[160:163], 0\nds_read_b128 v[100:103], v28 offset:256\nbuffer_load_dwordx4 v9, s[8:11], s53 offen offset:1024 lds\ns_waitcnt lgkmcnt(6)\n"
"v_mfma_f32_16x16x32_bf16 v[64:67], v[104:107], a[132:135], v[64:67]\ns_lshl_b32 s53, s42, 19\ns_add_u32 s53, s53, s30\nv_mfma_f32_16x16x32_bf16 v[68:71], v[104:107], a[164:167], v[68:71]\n"
"ds_read_b128 v[104:107], v25 offset:256\ns_add_u32 m0, s31, 106496\ns_add_u32 s53, s53, 131072\nv_mfma_f32_16x16x32_bf16 v[72:75], v[108:111], a[132:135], v[72:75]\n"
"buffer_load_dwordx4 v8, s[8:11], s53 offen lds\nv_mfma_f32_16x16x32_bf16 v[76:79], v[108:111], a[164:167], v[76:79]\nds_read_b128 v[108:111], v29 offset:256\nbuffer_load_dwordx4 v9, s[8:11], s53 offen offset:1024 lds\n"
"s_waitcnt lgkmcnt(6)\nv_mfma_f32_16x16x32_bf16 v[64:67], v[112:115], a[136:139], v[64:67]\nv_mfma_f32_16x16x32_bf16 v[68:71], v[112:115], a[168:171], v[68:71]\nds_read_b128 v[112:115], v26 offset:256\n"
"v_mfma_f32_16x16x32_bf16 v[72:75], v[116:119], a[136:139], v[72:75]\nv_mfma_f32_16x16x32_bf16 v[76:79], v[116:119], a[168:171], v[76:79]\nds_read_b128 v[116:119], v30 offset:256\ns_waitcnt lgkmcnt(6)\n"
"v_mfma_f32_16x16x32_bf16 v[64:67], v[120:123], a[140:143], v[64:67]\nv_mfma_f32_16x16x32_bf16 v[68:71], v[120:123], a[172:175], v[68:71]\nds_read_b128 v[120:123], v27 offset:256\nv_mfma_f32_16x16x32_bf16 v[72:75], v[124:127], a[140:143], v[72:75]\n"
"v_mfma_f32_16x16x32_bf16 v[76:79], v[124:127], a[172:175], v[76:79]\nds_read_b128 v[124:127], v31 offset:256\ns_waitcnt lgkmcnt(6)\nv_mfma_f32_16x16x32_bf16 v[64:67], v[96:99], a[144:147], v[64:67]\n"
"v_mfma_f32_16x16x32_bf16 v[68:71], v[96:99], a[176:179], v[68:71]\nds_read_b128 v[96:99], v24 offset:16384\nv_mfma_f32_16x16x32_bf16 v[72:75], v[100:103], a[144:147], v[72:75]\nv_mfma_f32_16x16x32_bf16 v[76:79], v[100:103], a[176:179], v[76:79]\n"
"ds_read_b128 v[100:103], v28 offset:16384\ns_waitcnt lgkmcnt(6)\nv_mfma_f32_16x16x32_bf16 v[64:67], v[104:107], a[148:151], v[64:67]\nv_mfma_f32_16x16x32_bf16 v[68:71], v[104:107], a[180:183], v[68:71]\n"
"ds_read_b128 v[104:107], v25 offset:16384\nv_mfma_f32_16x16x32_bf16 v[72:75], v[108:111], a[148:151], v[72:75]\nv_mfma_f32_16x16x32_bf16 v[76:79], v[108:111], a[180:183], v[76:79]\nds_read_b128 v[108:111], v29 offset:16384\n"
"s_waitcnt lgkmcnt(6)\nv_mfma_f32_16x16x32_bf16 v[64:67], v[112:115], a[152:155], v[64:67]\nv_mfma_f32_16x16x32_bf16 v[68:71], v[112:115], a[184:187], v[68:71]\nds_read_b128 v[112:115], v26 offset:16384\n"
"v_mfma_f32_16x16x32_bf16 v[72:75], v[116:119], a[152:155], v[72:75]\nv_mfma_f32_16x16x32_bf16 v[76:79], v[116:119], a[184:187], v[76:79]\nds_read_b128 v[116:119], v30 offset:16384\ns_waitcnt lgkmcnt(6)\n"
"v_mfma_f32_16x16x32_bf16 v[64:67], v[120:123], a[156:159], v[64:67]\nv_mfma_f32_16x16x32_bf16 v[68:71], v[120:123], a[188:191], v[68:71]\nds_read_b128 v[120:123], v27 offset:16384\nv_mfma_f32_16x16x32_bf16 v[72:75], v[124:127], a[156:159], v[72:75]\n"
"v_mfma_f32_16x16x32_bf16 v[76:79], v[124:127], a[188:191], v[76:79]\nds_read_b128 v[124:127], v31 offset:16384\ns_waitcnt lgkmcnt(6)\nv_mfma_f32_16x16x32_bf16 v[80:83], v[96:99], a[128:131], 0\n"
"s_lshl_b32 s53, s42, 19\ns_add_u32 s53, s53, s30\nv_mfma_f32_16x16x32_bf16 v[84:87], v[96:99], a[160:163], 0\nds_read_b128 v[96:99], v24 offset:16640\n"
"s_add_u32 m0, s31, 114688\ns_add_u32 s53, s53, 262144\nv_mfma_f32_16x16x32_bf16 v[88:91], v[100:103], a[128:131], 0\nbuffer_load_dwordx4 v8, s[8:11], s53 offen lds\n"
"v_mfma_f32_16x16x32_bf16 v[92:95], v[100:103], a[160:163], 0\nds_read_b128 v[100:103], v28 offset:16640\nbuffer_load_dwordx4 v9, s[8:11], s53 offen offset:1024 lds\ns_waitcnt lgkmcnt(6)\n"
"v_mfma_f32_16x16x32_bf16 v[80:83], v[104:107], a[132:135], v[80:83]\nv_mul_f32 v64, s35, v64\nv_mul_f32 v65, s35, v65\nv_mfma_f32_16x16x32_bf16 v[84:87], v[104:107], a[164:167], v[84:87]\n"
"ds_read_b128 v[104:107], v25 offset:16640\nv_mul_f32 v66, s35, v66\nv_mul_f32 v67, s35, v67\nv_mfma_f32_16x16x32_bf16 v[88:91], v[108:111], a[132:135], v[88:91]\n"
"v_cvt_pk_bf16_f32 v128, v64, v65\nv_cvt_pk_bf16_f32 v129, v66, v67\nv_mfma_f32_16x16x32_bf16 v[92:95], v[108:111], a[164:167], v[92:95]\nds_read_b128 v[108:111], v29 offset:16640\n"
"v_mul_f32 v72, s35, v72\nv_mul_f32 v73, s35, v73\ns_waitcnt lgkmcnt(6)\nv_mfma_f32_16x16x32_bf16 v[80:83], v[112:115], a[136:139], v[80:83]\n"
"v_mul_f32 v74, s35, v74\nv_mul_f32 v75, s35, v75\nv_mfma_f32_16x16x32_bf16 v[84:87], v[112:115], a[168:171], v[84:87]\nds_read_b128 v[112:115], v26 offset:16640\n"
"v_cvt_pk_bf16_f32 v130, v72, v73\nv_cvt_pk_bf16_f32 v131, v74, v75\nv_mfma_f32_16x16x32_bf16 v[88:91], v[116:119], a[136:139], v[88:91]\nv_mul_f32 v68, s35, v68\n"
"v_mul_f32 v69, s35, v69\nv_mfma_f32_16x16x32_bf16 v[92:95], v[116:119], a[168:171], v[92:95]\nds_read_b128 v[116:119], v30 offset:16640\nv_mul_f32 v70, s35, v70\n"
"v_mul_f32 v71, s35, v71\ns_waitcnt lgkmcnt(6)\nv_mfma_f32_16x16x32_bf16 v[80:83], v[120:123], a[140:143], v[80:83]\nv_cvt_pk_bf16_f32 v132, v68, v69\n"
"v_cvt_pk_bf16_f32 v133, v70, v71\nv_mfma_f32_16x16x32_bf16 v[84:87], v[120:123], a[172:175], v[84:87]\nds_read_b128 v[120:123], v27 offset:16640\nv_mul_f32 v76, s35, v76\n"
"v_mul_f32 v77, s35, v77\nv_mfma_f32_16x16x32_bf16 v[88:91], v[124:127], a[140:143], v[88:91]\nv_mul_f32 v78, s35, v78\nv_mul_f32 v79, s35, v79\n"
"v_mfma_f32_16x16x32_bf16 v[92:95], v[124:127], a[172:175], v[92:95]\nds_read_b128 v[124:127], v31 offset:16640\nv_cvt_pk_bf16_f32 v134, v76, v77\nv_cvt_pk_bf16_f32 v135, v78, v79\n"
"s_waitcnt vmcnt(6)\ns_barrier\ns_waitcnt lgkmcnt(6)\nv_mfma_f32_16x16x32_bf16 v[80:83], v[96:99], a[144:147], v[80:83]\n"
"s_lshl_b32 s54, s40, 11\ns_add_u32 s54, s54, 0\nv_mfma_f32_16x16x32_bf16 v[84:87], v[96:99], a[176:179], v[84:87]\nds_read_b128 v[96:99], v24 offset:32768\n"
"buffer_store_dwordx4 v[128:131], v13, s[16:19], s54 offen sc1\nv_mfma_f32_16x16x32_bf16 v[88:91], v[100:103], a[144:147], v[88:91]\ns_add_u32 s54, s54, 14336\nv_mfma_f32_16x16x32_bf16 v[92:95], v[100:103], a[176:179], v[92:95]\n"
"ds_read_b128 v[100:103], v28 offset:32768\nbuffer_store_dwordx4 v[132:135], v13, s[16:19], s54 offen sc1\ns_waitcnt lgkmcnt(6)\nv_mfma_f32_16x16x32_bf16 v[80:83], v[104:107], a[148:151], v[80:83]\n"
"s_lshl_b32 s53, s42, 19\ns_add_u32 s53, s53, s30\nv_mfma_f32_16x16x32_bf16 v[84:87], v[104:107], a[180:183], v[84:87]\nds_read_b128 v[104:107], v25 offset:32768\n"
"s_add_u32 m0, s31, 122880\ns_add_u32 s53, s53, 393216\nv_mfma_f32_16x16x32_bf16 v[88:91], v[108:111], a[148:151], v[88:91]\nbuffer_load_dwordx4 v8, s[8:11], s53 offen lds\n"
"v_mfma_f32_16x16x32_bf16 v[92:95], v[108:111], a[180:183], v[92:95]\nds_read_b128 v[108:111], v29 offset:32768\nbuffer_load_dwordx4 v9, s[8:11], s53 offen offset:1024 lds\ns_waitcnt lgkmcnt(6)\n"
"v_mfma_f32_16x16x32_bf16 v[80:83], v[112:115], a[152:155], v[80:83]\nv_mfma_f32_16x16x32_bf16 v[84:87], v[112:115], a[184:187], v[84:87]\nds_read_b128 v[112:115], v26 offset:32768\nv_mfma_f32_16x16x32_bf16 v[88:91], v[116:119], a[152:155], v[88:91]\n"
"v_mfma_f32_16x16x32_bf16 v[92:95], v[116:119], a[184:187], v[92:95]\nds_read_b128 v[116:119], v30 offset:32768\ns_waitcnt lgkmcnt(6)\nv_mfma_f32_16x16x32_bf16 v[80:83], v[120:123], a[156:159], v[80:83]\n"
"v_mfma_f32_16x16x32_bf16 v[84:87], v[120:123], a[188:191], v[84:87]\nds_read_b128 v[120:123], v27 offset:32768\nv_mfma_f32_16x16x32_bf16 v[88:91], v[124:127], a[156:159], v[88:91]\nv_mfma_f32_16x16x32_bf16 v[92:95], v[124:127], a[188:191], v[92:95]\n"
"ds_read_b128 v[124:127], v31 offset:32768\ns_waitcnt lgkmcnt(6)\nv_mfma_f32_16x16x32_bf16 v[64:67], v[96:99], a[128:131], 0\ns_lshl_b32 s53, s43, 19\n"
"s_add_u32 s53, s53, s30\nv_mfma_f32_16x16x32_bf16 v[68:71], v[96:99], a[160:163], 0\nds_read_b128 v[96:99], v24 offset:33024\ns_add_u32 m0, s31, 131072\n"
"s_add_u32 s53, s53, 0\nv_mfma_f32_16x16x32_bf16 v[72:75], v[100:103], a[128:131], 0\nbuffer_load_dwordx4 v8, s[8:11], s53 offen lds\nv_mfma_f32_16x16x32_bf16 v[76:79], v[100:103], a[160:163], 0\n"
"ds_read_b128 v[100:103], v28 offset:33024\nbuffer_load_dwordx4 v9, s[8:11], s53 offen offset:1024 lds\ns_waitcnt lgkmcnt(6)\nv_mfma_f32_16x16x32_bf16 v[64:67], v[104:107], a[132:135], v[64:67]\n"
"v_mul_f32 v80, s35, v80\nv_mul_f32 v81, s35, v81\nv_mfma_f32_16x16x32_bf16 v[68:71], v[104:107], a[164:167], v[68:71]\nds_read_b128 v[104:107], v25 offset:33024\n"
"v_mul_f32 v82, s35, v82\nv_mul_f32 v83, s35, v83\nv_mfma_f32_16x16x32_bf16 v[72:75], v[108:111], a[132:135], v[72:75]\nv_cvt_pk_bf16_f32 v136, v80, v81\n"
"v_cvt_pk_bf16_f32 v137, v82, v83\nv_mfma_f32_16x16x32_bf16 v[76:79], v[108:111], a[164:167], v[76:79]\nds_read_b128 v[108:111], v29 offset:33024\nv_mul_f32 v88, s35, v88\n"
"v_mul_f32 v89, s35, v89\ns_waitcnt lgkmcnt(6)\nv_mfma_f32_16x16x32_bf16 v[64:67], v[112:115], a[136:139], v[64:67]\nv_mul_f32 v90, s35, v90\n"
"v_mul_f32 v91, s35, v91\nv_mfma_f32_16x16x32_bf16 v[68:71], v[112:115], a[168:171], v[68:71]\nds_read_b128 v[112:115], v26 offset:33024\nv_cvt_pk_bf16_f32 v138, v88, v89\n"
"v_cvt_pk_bf16_f32 v139, v90, v91\nv_mfma_f32_16x16x32_bf16 v[72:75], v[116:119], a[136:139], v[72:75]\nv_mul_f32 v84, s35, v84\nv_mul_f32 v85, s35, v85\n"
"v_mfma_f32_16x16x32_bf16 v[76:79], v[116:119], a[168:171], v[76:79]\nds_read_b128 v[116:119], v30 offset:33024\nv_mul_f32 v86, s35, v86\nv_mul_f32 v87, s35, v87\n"
"s_waitcnt lgkmcnt(6)\nv_mfma_f32_16x16x32_bf16 v[64:67], v[120:123], a[140:143], v[64:67]\nv_cvt_pk_bf16_f32 v140, v84, v85\nv_cvt_pk_bf16_f32 v141, v86, v87\n"
"v_mfma_f32_16x16x32_bf16 v[68:71], v[120:123], a[172:175], v[68:71]\nds_read_b128 v[120:123], v27 offset:33024\nv_mul_f32 v92, s35, v92\nv_mul_f32 v93, s35, v93\n"
"v_mfma_f32_16x16x32_bf16 v[72:75], v[124:127], a[140:143], v[72:75]\nv_mul_f32 v94, s35, v94\nv_mul_f32 v95, s35, v95\nv_mfma_f32_16x16x32_bf16 v[76:79], v[124:127], a[172:175], v[76:79]\n"
"ds_read_b128 v[124:127], v31 offset:33024\nv_cvt_pk_bf16_f32 v142, v92, v93\nv_cvt_pk_bf16_f32 v143, v94, v95\ns_waitcnt lgkmcnt(6)\n"
"v_mfma_f32_16x16x32_bf16 v[64:67], v[96:99], a[144:147], v[64:67]\ns_lshl_b32 s54, s40, 11\ns_add_u32 s54, s54, 1024\nv_mfma_f32_16x16x32_bf16 v[68:71], v[96:99], a[176:179], v[68:71]\n"
"ds_read_b128 v[96:99], v24 offset:49152\nbuffer_store_dwordx4 v[136:139], v13, s[16:19], s54 offen sc1\nv_mfma_f32_16x16x32_bf16 v[72:75], v[100:103], a[144:147], v[72:75]\ns_add_u32 s54, s54, 14336\n"
"v_mfma_f32_16x16x32_bf16 v[76:79], v[100:103], a[176:179], v[76:79]\nds_read_b128 v[100:103], v28 offset:49152\nbuffer_store_dwordx4 v[140:143], v13, s[16:19], s54 offen sc1\ns_waitcnt lgkmcnt(6)\n"
"v_mfma_f32_16x16x32_bf16 v[64:67], v[104:107], a[148:151], v[64:67]\ns_lshl_b32 s53, s43, 19\ns_add_u32 s53, s53, s30\nv_mfma_f32_16x16x32_bf16 v[68:71], v[104:107], a[180:183], v[68:71]\n"
"ds_read_b128 v[104:107], v25 offset:49152\ns_add_u32 m0, s31, 139264\ns_add_u32 s53, s53, 131072\nv_mfma_f32_16x16x32_bf16 v[72:75], v[108:111], a[148:151], v[72:75]\n"
"buffer_load_dwordx4 v8, s[8:11], s53 offen lds\nv_mfma_f32_16x16x32_bf16 v[76:79], v[108:111], a[180:183], v[76:79]\nds_read_b128 v[108:111], v29 offset:49152\nbuffer_load_dwordx4 v9, s[8:11], s53 offen offset:1024 lds\n"
"s_waitcnt lgkmcnt(6)\nv_mfma_f32_16x16x32_bf16 v[64:67], v[112:115], a[152:155], v[64:67]\nv_mfma_f32_16x16x32_bf16 v[68:71], v[112:115], a[184:187], v[68:71]\nds_read_b128 v[112:115], v26 offset:49152\n"
"v_mfma_f32_16x16x32_bf16 v[72:75], v[116:119], a[152:155], v[72:75]\nv_mfma_f32_16x16x32_bf16 v[76:79], v[116:119], a[184:187], v[76:79]\nds_read_b128 v[116:119], v30 offset:49152\ns_waitcnt lgkmcnt(6)\n"
"v_mfma_f32_16x16x32_bf16 v[64:67], v[120:123], a[156:159], v[64:67]\nv_mfma_f32_16x16x32_bf16 v[68:71], v[120:123], a[188:191], v[68:71]\nds_read_b128 v[120:123], v27 offset:49152\nv_mfma_f32_16x16x32_bf16 v[72:75], v[124:127], a[156:159], v[72:75]\n"
"v_mfma_f32_16x16x32_bf16 v[76:79], v[124:127], a[188:191], v[76:79]\nds_read_b128 v[124:127], v31 offset:49152\ns_waitcnt lgkmcnt(6)\nv_mfma_f32_16x16x32_bf16 v[80:83], v[96:99], a[128:131], 0\n"
"s_lshl_b32 s53, s43, 19\ns_add_u32 s53, s53, s30\nv_mfma_f32_16x16x32_bf16 v[84:87], v[96:99], a[160:163], 0\nds_read_b128 v[96:99], v24 offset:49408\n"
"s_add_u32 m0, s31, 147456\ns_add_u32 s53, s53, 262144\nv_mfma_f32_16x16x32_bf16 v[88:91], v[100:103], a[128:131], 0\nbuffer_load_dwordx4 v8, s[8:11], s53 offen lds\n"
"v_mfma_f32_16x16x32_bf16 v[92:95], v[100:103], a[160:163], 0\nds_read_b128 v[100:103], v28 offset:49408\nbuffer_load_dwordx4 v9, s[8:11], s53 offen offset:1024 lds\ns_waitcnt lgkmcnt(6)\n"
"v_mfma_f32_16x16x32_bf16 v[80:83], v[104:107], a[132:135], v[80:83]\nv_mul_f32 v64, s35, v64\nv_mul_f32 v65, s35, v65\nv_mfma_f32_16x16x32_bf16 v[84:87], v[104:107], a[164:167], v[84:87]\n"
"ds_read_b128 v[104:107], v25 offset:49408\nv_mul_f32 v66, s35, v66\nv_mul_f32 v67, s35, v67\nv_mfma_f32_16x16x32_bf16 v[88:91], v[108:111], a[132:135], v[88:91]\n"
"v_cvt_pk_bf16_f32 v144, v64, v65\nv_cvt_pk_bf16_f32 v145, v66, v67\nv_mfma_f32_16x16x32_bf16 v[92:95], v[108:111], a[164:167], v[92:95]\nds_read_b128 v[108:111], v29 offset:49408\n"
"v_mul_f32 v72, s35, v72\nv_mul_f32 v73, s35, v73\ns_waitcnt lgkmcnt(6)\nv_mfma_f32_16x16x32_bf16 v[80:83], v[112:115], a[136:139], v[80:83]\n"
"v_mul_f32 v74, s35, v74\nv_mul_f32 v75, s35, v75\nv_mfma_f32_16x16x32_bf16 v[84:87], v[112:115], a[168:171], v[84:87]\nds_read_b128 v[112:115], v26 offset:49408\n"
"v_cvt_pk_bf16_f32 v146, v72, v73\nv_cvt_pk_bf16_f32 v147, v74, v75\nv_mfma_f32_16x16x32_bf16 v[88:91], v[116:119], a[136:139], v[88:91]\nv_mul_f32 v68, s35, v68\n"
"v_mul_f32 v69, s35, v69\nv_mfma_f32_16x16x32_bf16 v[92:95], v[116:119], a[168:171], v[92:95]\nds_read_b128 v[116:119], v30 offset:49408\nv_mul_f32 v70, s35, v70\n"
"v_mul_f32 v71, s35, v71\ns_waitcnt lgkmcnt(6)\nv_mfma_f32_16x16x32_bf16 v[80:83], v[120:123], a[140:143], v[80:83]\nv_cvt_pk_bf16_f32 v148, v68, v69\n"
"v_cvt_pk_bf16_f32 v149, v70, v71\nv_mfma_f32_16x16x32_bf16 v[84:87], v[120:123], a[172:175], v[84:87]\nds_read_b128 v[120:123], v27 offset:49408\nv_mul_f32 v76, s35, v76\n"
"v_mul_f32 v77, s35, v77\nv_mfma_f32_16x16x32_bf16 v[88:91], v[124:127], a[140:143], v[88:91]\nv_mul_f32 v78, s35, v78\nv_mul_f32 v79, s35, v79\n"
"v_mfma_f32_16x16x32_bf16 v[92:95], v[124:127], a[172:175], v[92:95]\nds_read_b128 v[124:127], v31 offset:49408\nv_cvt_pk_bf16_f32 v150, v76, v77\nv_cvt_pk_bf16_f32 v151, v78, v79\n"
"s_waitcnt vmcnt(8)\ns_barrier\ns_waitcnt lgkmcnt(6)\nv_mfma_f32_16x16x32_bf16 v[80:83], v[96:99], a[144:147], v[80:83]\n"
"s_lshl_b32 s54, s41, 11\ns_add_u32 s54, s54, 0\nv_mfma_f32_16x16x32_bf16 v[84:87], v[96:99], a[176:179], v[84:87]\nds_read_b128 v[96:99], v40 offset:0\n"
"buffer_store_dwordx4 v[144:147], v13, s[16:19], s54 offen sc1\nv_mfma_f32_16x16x32_bf16 v[88:91], v[100:103], a[144:147], v[88:91]\ns_add_u32 s54, s54, 14336\nv_mfma_f32_16x16x32_bf16 v[92:95], v[100:103], a[176:179], v[92:95]\n"
"ds_read_b128 v[100:103], v44 offset:0\nbuffer_store_dwordx4 v[148:151], v13, s[16:19], s54 offen sc1\ns_waitcnt lgkmcnt(6)\nv_mfma_f32_16x16x32_bf16 v[80:83], v[104:107], a[148:151], v[80:83]\n"
"s_lshl_b32 s53, s43, 19\ns_add_u32 s53, s53, s30\nv_mfma_f32_16x16x32_bf16 v[84:87], v[104:107], a[180:183], v[84:87]\nds_read_b128 v[104:107], v41 offset:0\n"
"s_add_u32 m0, s31, 155648\ns_add_u32 s53, s53, 393216\nv_mfma_f32_16x16x32_bf16 v[88:91], v[108:111], a[148:151], v[88:91]\nbuffer_load_dwordx4 v8, s[8:11], s53 offen lds\n"
"v_mfma_f32_16x16x32_bf16 v[92:95], v[108:111], a[180:183], v[92:95]\nds_read_b128 v[108:111], v45 offset:0\nbuffer_load_dwordx4 v9, s[8:11], s53 offen offset:1024 lds\ns_waitcnt lgkmcnt(6)\n"
"v_mfma_f32_16x16x32_bf16 v[80:83], v[112:115], a[152:155], v[80:83]\nv_mfma_f32_16x16x32_bf16 v[84:87], v[112:115], a[184:187], v[84:87]\nds_read_b128 v[112:115], v42 offset:0\nv_mfma_f32_16x16x32_bf16 v[88:91], v[116:119], a[152:155], v[88:91]\n"
"v_mfma_f32_16x16x32_bf16 v[92:95], v[116:119], a[184:187], v[92:95]\nds_read_b128 v[116:119], v46 offset:0\ns_waitcnt lgkmcnt(6)\nv_mfma_f32_16x16x32_bf16 v[80:83], v[120:123], a[156:159], v[80:83]\n"
"v_mfma_f32_16x16x32_bf16 v[84:87], v[120:123], a[188:191], v[84:87]\nds_read_b128 v[120:123], v43 offset:0\nv_mfma_f32_16x16x32_bf16 v[88:91], v[124:127], a[156:159], v[88:91]\nv_mfma_f32_16x16x32_bf16 v[92:95], v[124:127], a[188:191], v[92:95]\n"
"ds_read_b128 v[124:127], v47 offset:0\ns_waitcnt lgkmcnt(6)\nv_mfma_f32_16x16x32_bf16 v[64:67], v[96:99], a[128:131], 0\ns_add_u32 m0, s32, 0\n"
"v_mfma_f32_16x16x32_bf16 v[68:71], v[96:99], a[160:163], 0\nds_read_b128 v[96:99], v40 offset:256\ns_mov_b32 s53, 0\nv_mfma_f32_16x16x32_bf16 v[72:75], v[100:103], a[128:131], 0\n"
"buffer_load_dwordx4 v10, s[12:15], s53 offen lds\nv_mfma_f32_16x16x32_bf16 v[76:79], v[100:103], a[160:163], 0\nds_read_b128 v[100:103], v44 offset:256\nv_mul_f32 v80, s35, v80\n"
"v_mul_f32 v81, s35, v81\ns_waitcnt lgkmcnt(6)\nv_mfma_f32_16x16x32_bf16 v[64:67], v[104:107], a[132:135], v[64:67]\nv_mul_f32 v82, s35, v82\n"
"v_mul_f32 v83, s35, v83\nv_mfma_f32_16x16x32_bf16 v[68:71], v[104:107], a[164:167], v[68:71]\nds_read_b128 v[104:107], v41 offset:256\nv_cvt_pk_bf16_f32 v152, v80, v81\n"
"v_cvt_pk_bf16_f32 v153, v82, v83\nv_mfma_f32_16x16x32_bf16 v[72:75], v[108:111], a[132:135], v[72:75]\nv_mul_f32 v88, s35, v88\nv_mul_f32 v89, s35, v89\n"
"v_mfma_f32_16x16x32_bf16 v[76:79], v[108:111], a[164:167], v[76:79]\nds_read_b128 v[108:111], v45 offset:256\nv_mul_f32 v90, s35, v90\nv_mul_f32 v91, s35, v91\n"
"s_waitcnt lgkmcnt(6)\nv_mfma_f32_16x16x32_bf16 v[64:67], v[112:115], a[136:139], v[64:67]\nv_cvt_pk_bf16_f32 v154, v88, v89\nv_cvt_pk_bf16_f32 v155, v90, v91\n"
"v_mfma_f32_16x16x32_bf16 v[68:71], v[112:115], a[168:171], v[68:71]\nds_read_b128 v[112:115], v42 offset:256\nv_mul_f32 v84, s35, v84\nv_mul_f32 v85, s35, v85\n"
"v_mfma_f32_16x16x32_bf16 v[72:75], v[116:119], a[136:139], v[72:75]\nv_mul_f32 v86, s35, v86\nv_mul_f32 v87, s35, v87\nv_mfma_f32_16x16x32_bf16 v[76:79], v[116:119], a[168:171], v[76:79]\n"
"ds_read_b128 v[116:119], v46 offset:256\nv_cvt_pk_bf16_f32 v156, v84, v85\nv_cvt_pk_bf16_f32 v157, v86, v87\ns_waitcnt lgkmcnt(6)\n"
"v_mfma_f32_16x16x32_bf16 v[64:67], v[120:123], a[140:143], v[64:67]\nv_mul_f32 v92, s35, v92\nv_mul_f32 v93, s35, v93\nv_mfma_f32_16x16x32_bf16 v[68:71], v[120:123], a[172:175], v[68:71]\n"
"ds_read_b128 v[120:123], v43 offset:256\nv_mul_f32 v94, s35, v94\nv_mul_f32 v95, s35, v95\nv_mfma_f32_16x16x32_bf16 v[72:75], v[124:127], a[140:143], v[72:75]\n"
"v_cvt_pk_bf16_f32 v158, v92, v93\nv_cvt_pk_bf16_f32 v159, v94, v95\nv_mfma_f32_16x16x32_bf16 v[76:79], v[124:127], a[172:175], v[76:79]\nds_read_b128 v[124:127], v47 offset:256\n"
"s_lshl_b32 s54, s41, 11\ns_add_u32 s54, s54, 1024\ns_waitcnt lgkmcnt(6)\nv_mfma_f32_16x16x32_bf16 v[64:67], v[96:99], a[144:147], v[64:67]\n"
"buffer_store_dwordx4 v[152:155], v13, s[16:19], s54 offen sc1\nv_mfma_f32_16x16x32_bf16 v[68:71], v[96:99], a[176:179], v[68:71]\nds_read_b128 v[96:99], v40 offset:16384\ns_add_u32 s54, s54, 14336\n"
"v_mfma_f32_16x16x32_bf16 v[72:75], v[100:103], a[144:147], v[72:75]\nbuffer_store_dwordx4 v[156:159], v13, s[16:19], s54 offen sc1\nv_mfma_f32_16x16x32_bf16 v[76:79], v[100:103], a[176:179], v[76:79]\nds_read_b128 v[100:103], v44 offset:16384\n"
"s_add_u32 m0, s32, 4096\ns_waitcnt lgkmcnt(6)\nv_mfma_f32_16x16x32_bf16 v[64:67], v[104:107], a[148:151], v[64:67]\ns_mov_b32 s53, 128\n"
"v_mfma_f32_16x16x32_bf16 v[68:71], v[104:107], a[180:183], v[68:71]\nds_read_b128 v[104:107], v41 offset:16384\nbuffer_load_dwordx4 v10, s[12:15], s53 offen lds\nv_mfma_f32_16x16x32_bf16 v[72:75], v[108:111], a[148:151], v[72:75]\n"
"s_add_u32 m0, s32, 8192\nv_mfma_f32_16x16x32_bf16 v[76:79], v[108:111], a[180:183], v[76:79]\nds_read_b128 v[108:111], v45 offset:16384\ns_mov_b32 s53, 256\n"
"s_waitcnt lgkmcnt(6)\nv_mfma_f32_16x16x32_bf16 v[64:67], v[112:115], a[152:155], v[64:67]\nbuffer_load_dwordx4 v10, s[12:15], s53 offen lds\nv_mfma_f32_16x16x32_bf16 v[68:71], v[112:115], a[184:187], v[68:71]\n"
"ds_read_b128 v[112:115], v42 offset:16384\ns_add_u32 m0, s32, 12288\nv_mfma_f32_16x16x32_bf16 v[72:75], v[116:119], a[152:155], v[72:75]\ns_mov_b32 s53, 384\n"
"v_mfma_f32_16x16x32_bf16 v[76:79], v[116:119], a[184:187], v[76:79]\nds_read_b128 v[116:119], v46 offset:16384\nbuffer_load_dwordx4 v10, s[12:15], s53 offen lds\ns_waitcnt lgkmcnt(6)\n"
"v_mfma_f32_16x16x32_bf16 v[64:67], v[120:123], a[156:159], v[64:67]\nv_mfma_f32_16x16x32_bf16 v[68:71], v[120:123], a[188:191], v[68:71]\nds_read_b128 v[120:123], v43 offset:16384\nv_mfma_f32_16x16x32_bf16 v[72:75], v[124:127], a[156:159], v[72:75]\n"
"v_mfma_f32_16x16x32_bf16 v[76:79], v[124:127], a[188:191], v[76:79]\nds_read_b128 v[124:127], v47 offset:16384\ns_waitcnt lgkmcnt(6)\nv_mfma_f32_16x16x32_bf16 v[80:83], v[96:99], a[128:131], 0\n"
"s_add_u32 m0, s32, 16384\nv_mfma_f32_16x16x32_bf16 v[84:87], v[96:99], a[160:163], 0\nds_read_b128 v[96:99], v40 offset:16640\ns_mov_b32 s53, 512\n"
"v_mfma_f32_16x16x32_bf16 v[88:91], v[100:103], a[128:131], 0\nbuffer_load_dwordx4 v10, s[12:15], s53 offen lds\nv_mfma_f32_16x16x32_bf16 v[92:95], v[100:103], a[160:163], 0\nds_read_b128 v[100:103], v44 offset:16640\n"
"v_mul_f32 v64, s35, v64\nv_mul_f32 v65, s35, v65\ns_waitcnt lgkmcnt(6)\nv_mfma_f32_16x16x32_bf16 v[80:83], v[104:107], a[132:135], v[80:83]\n"
"v_mul_f32 v66, s35, v66\nv_mul_f32 v67, s35, v67\nv_mfma_f32_16x16x32_bf16 v[84:87], v[104:107], a[164:167], v[84:87]\nds_read_b128 v[104:107], v41 offset:16640\n"
"v_cvt_pk_bf16_f32 v160, v64, v65\nv_cvt_pk_bf16_f32 v161, v66, v67\nv_mfma_f32_16x16x32_bf16 v[88:91], v[108:111], a[132:135], v[88:91]\nv_mul_f32 v72, s35, v72\n"
"v_mul_f32 v73, s35, v73\nv_mfma_f32_16x16x32_bf16 v[92:95], v[108:111], a[164:167], v[92:95]\nds_read_b128 v[108:111], v45 offset:16640\nv_mul_f32 v74, s35, v74\n"
"v_mul_f32 v75, s35, v75\ns_waitcnt lgkmcnt(6)\nv_mfma_f32_16x16x32_bf16 v[80:83], v[112:115], a[136:139], v[80:83]\nv_cvt_pk_bf16_f32 v162, v72, v73\n"
"v_cvt_pk_bf16_f32 v163, v74, v75\nv_mfma_f32_16x16x32_bf16 v[84:87], v[112:115], a[168:171], v[84:87]\nds_read_b128 v[112:115], v42 offset:16640\nv_mul_f32 v68, s35, v68\n"
"v_mul_f32 v69, s35, v69\nv_mfma_f32_16x16x32_bf16 v[88:91], v[116:119], a[136:139], v[88:91]\nv_mul_f32 v70, s35, v70\nv_mul_f32 v71, s35, v71\n"
"v_mfma_f32_16x16x32_bf16 v[92:95], v[116:119], a[168:171], v[92:95]\nds_read_b128 v[116:119], v46 offset:16640\nv_cvt_pk_bf16_f32 v164, v68, v69\nv_cvt_pk_bf16_f32 v165, v70, v71\n"
"s_waitcnt lgkmcnt(6)\nv_mfma_f32_16x16x32_bf16 v[80:83], v[120:123], a[140:143], v[80:83]\nv_mul_f32 v76, s35, v76\nv_mul_f32 v77, s35, v77\n"
"v_mfma_f32_16x16x32_bf16 v[84:87], v[120:123], a[172:175], v[84:87]\nds_read_b128 v[120:123], v43 offset:16640\nv_mul_f32 v78, s35, v78\nv_mul_f32 v79, s35, v79\n"
"v_mfma_f32_16x16x32_bf16 v[88:91], v[124:127], a[140:143], v[88:91]\nv_cvt_pk_bf16_f32 v166, v76, v77\nv_cvt_pk_bf16_f32 v167, v78, v79\nv_mfma_f32_16x16x32_bf16 v[92:95], v[124:127], a[172:175], v[92:95]\n"
"ds_read_b128 v[124:127], v47 offset:16640\ns_lshl_b32 s54, s42, 11\ns_add_u32 s54, s54, 0\ns_waitcnt vmcnt(7)\n"
"s_barrier\ns_waitcnt lgkmcnt(6)\nv_mfma_f32_16x16x32_bf16 v[80:83], v[96:99], a[144:147], v[80:83]\nbuffer_store_dwordx4 v[160:163], v13, s[16:19], s54 offen sc1\n"
"v_mfma_f32_16x16x32_bf16 v[84:87], v[96:99], a[176:179], v[84:87]\nds_read_b128 v[96:99], v40 offset:32768\ns_add_u32 s54, s54, 14336\nv_mfma_f32_16x16x32_bf16 v[88:91], v[100:103], a[144:147], v[88:91]\n"
"buffer_store_dwordx4 v[164:167], v13, s[16:19], s54 offen sc1\nv_mfma_f32_16x16x32_bf16 v[92:95], v[100:103], a[176:179], v[92:95]\nds_read_b128 v[100:103], v44 offset:32768\ns_add_u32 m0, s32, 20480\n"
"s_waitcnt lgkmcnt(6)\nv_mfma_f32_16x16x32_bf16 v[80:83], v[104:107], a[148:151], v[80:83]\ns_mov_b32 s53, 640\nv_mfma_f32_16x16x32_bf16 v[84:87], v[104:107], a[180:183], v[84:87]\n"
"ds_read_b128 v[104:107], v41 offset:32768\nbuffer_load_dwordx4 v10, s[12:15], s53 offen lds\nv_mfma_f32_16x16x32_bf16 v[88:91], v[108:111], a[148:151], v[88:91]\ns_add_u32 m0, s32, 24576\n"
"v_mfma_f32_16x16x32_bf16 v[92:95], v[108:111], a[180:183], v[92:95]\nds_read_b128 v[108:111], v45 offset:32768\ns_mov_b32 s53, 768\ns_waitcnt lgkmcnt(6)\n"
"v_mfma_f32_16x16x32_bf16 v[80:83], v[112:115], a[152:155], v[80:83]\nbuffer_load_dwordx4 v10, s[12:15], s53 offen lds\nv_mfma_f32_16x16x32_bf16 v[84:87], v[112:115], a[184:187], v[84:87]\nds_read_b128 v[112:115], v42 offset:32768\n"
"v_mfma_f32_16x16x32_bf16 v[88:91], v[116:119], a[152:155], v[88:91]\nv_mfma_f32_16x16x32_bf16 v[92:95], v[116:119], a[184:187], v[92:95]\nds_read_b128 v[116:119], v46 offset:32768\ns_waitcnt lgkmcnt(6)\n"
"v_mfma_f32_16x16x32_bf16 v[80:83], v[120:123], a[156:159], v[80:83]\nv_mfma_f32_16x16x32_bf16 v[84:87], v[120:123], a[188:191], v[84:87]\nds_read_b128 v[120:123], v43 offset:32768\nv_mfma_f32_16x16x32_bf16 v[88:91], v[124:127], a[156:159], v[88:91]\n"
"v_mfma_f32_16x16x32_bf16 v[92:95], v[124:127], a[188:191], v[92:95]\nds_read_b128 v[124:127], v47 offset:32768\ns_cmp_lg_u32 s34, 2\ns_cbranch_scc1 fg0_main\n"
"s_nop 7\nv_mul_f32 v80, s35, v80\nv_mul_f32 v81, s35, v81\nv_mul_f32 v82, s35, v82\n"
"v_mul_f32 v83, s35, v83\nv_cvt_pk_bf16_f32 v168, v80, v81\nv_cvt_pk_bf16_f32 v169, v82, v83\nv_mul_f32 v88, s35, v88\n"
"v_mul_f32 v89, s35, v89\nv_mul_f32 v90, s35, v90\nv_mul_f32 v91, s35, v91\nv_cvt_pk_bf16_f32 v170, v88, v89\n"
"v_cvt_pk_bf16_f32 v171, v90, v91\nv_mul_f32 v84, s35, v84\nv_mul_f32 v85, s35, v85\nv_mul_f32 v86, s35, v86\n"
"v_mul_f32 v87, s35, v87\nv_cvt_pk_bf16_f32 v172, v84, v85\nv_cvt_pk_bf16_f32 v173, v86, v87\nv_mul_f32 v92, s35, v92\n"
"v_mul_f32 v93, s35, v93\nv_mul_f32 v94, s35, v94\nv_mul_f32 v95, s35, v95\nv_cvt_pk_bf16_f32 v174, v92, v93\n"
"v_cvt_pk_bf16_f32 v175, v94, v95\ns_lshl_b32 s54, s42, 11\ns_add_u32 s54, s54, 1024\nbuffer_store_dwordx4 v[168:171], v13, s[16:19], s54 offen sc1\n"
"s_add_u32 s54, s54, 14336\nbuffer_store_dwordx4 v[172:175], v13, s[16:19], s54 offen sc1\nv_mov_b32 v36, s28\ns_waitcnt vmcnt(0) lgkmcnt(0)\n"
"s_barrier\nbuffer_store_dword v36, v19, s[24:27], 0 offen sc1\ns_branch fg0_end\nfg0_main:\n"
"s_waitcnt lgkmcnt(6)\nv_mfma_f32_16x16x32_bf16 v[64:67], v[96:99], a[128:131], 0\ns_add_u32 m0, s32, 32768\nv_mfma_f32_16x16x32_bf16 v[68:71], v[96:99], a[160:163], 0\n"
"ds_read_b128 v[96:99], v40 offset:33024\ns_mov_b32 s53, 28672\nv_mfma_f32_16x16x32_bf16 v[72:75], v[100:103], a[128:131], 0\nbuffer_load_dwordx4 v10, s[12:15], s53 offen lds\n"
"v_mfma_f32_16x16x32_bf16 v[76:79], v[100:103], a[160:163], 0\nds_read_b128 v[100:103], v44 offset:33024\nv_mul_f32 v80, s35, v80\nv_mul_f32 v81, s35, v81\n"
"s_waitcnt lgkmcnt(6)\nv_mfma_f32_16x16x32_bf16 v[64:67], v[104:107], a[132:135], v[64:67]\nv_mul_f32 v82, s35, v82\nv_mul_f32 v83, s35, v83\n"
"v_mfma_f32_16x16x32_bf16 v[68:71], v[104:107], a[164:167], v[68:71]\nds_read_b128 v[104:107], v41 offset:33024\nv_cvt_pk_bf16_f32 v168, v80, v81\nv_cvt_pk_bf16_f32 v169, v82, v83\n"
"v_mfma_f32_16x16x32_bf16 v[72:75], v[108:111], a[132:135], v[72:75]\nv_mul_f32 v88, s35, v88\nv_mul_f32 v89, s35, v89\nv_mfma_f32_16x16x32_bf16 v[76:79], v[108:111], a[164:167], v[76:79]\n"
"ds_read_b128 v[108:111], v45 offset:33024\nv_mul_f32 v90, s35, v90\nv_mul_f32 v91, s35, v91\ns_waitcnt lgkmcnt(6)\n"
"v_mfma_f32_16x16x32_bf16 v[64:67], v[112:115], a[136:139], v[64:67]\nv_cvt_pk_bf16_f32 v170, v88, v89\nv_cvt_pk_bf16_f32 v171, v90, v91\nv_mfma_f32_16x16x32_bf16 v[68:71], v[112:115], a[168:171], v[68:71]\n"
"ds_read_b128 v[112:115], v42 offset:33024\nv_mul_f32 v84, s35, v84\nv_mul_f32 v85, s35, v85\nv_mfma_f32_16x16x32_bf16 v[72:75], v[116:119], a[136:139], v[72:75]\n"
"v_mul_f32 v86, s35, v86\nv_mul_f32 v87, s35, v87\nv_mfma_f32_16x16x32_bf16 v[76:79], v[116:119], a[168:171], v[76:79]\nds_read_b128 v[116:119], v46 offset:33024\n"
"v_cvt_pk_bf16_f32 v172, v84, v85\nv_cvt_pk_bf16_f32 v173, v86, v87\ns_waitcnt lgkmcnt(6)\nv_mfma_f32_16x16x32_bf16 v[64:67], v[120:123], a[140:143], v[64:67]\n"
"v_mul_f32 v92, s35, v92\nv_mul_f32 v93, s35, v93\nv_mfma_f32_16x16x32_bf16 v[68:71], v[120:123], a[172:175], v[68:71]\nds_read_b128 v[120:123], v43 offset:33024\n"
"v_mul_f32 v94, s35, v94\nv_mul_f32 v95, s35, v95\nv_mfma_f32_16x16x32_bf16 v[72:75], v[124:127], a[140:143], v[72:75]\nv_cvt_pk_bf16_f32 v174, v92, v93\n"
"v_cvt_pk_bf16_f32 v175, v94, v95\nv_mfma_f32_16x16x32_bf16 v[76:79], v[124:127], a[172:175], v[76:79]\nds_read_b128 v[124:127], v47 offset:33024\ns_lshl_b32 s54, s42, 11\n"
"s_add_u32 s54, s54, 1024\ns_waitcnt lgkmcnt(6)\nv_mfma_f32_16x16x32_bf16 v[64:67], v[96:99], a[144:147], v[64:67]\nbuffer_store_dwordx4 v[168:171], v13, s[16:19], s54 offen sc1\n"
"v_mfma_f32_16x16x32_bf16 v[68:71], v[96:99], a[176:179], v[68:71]\nds_read_b128 v[96:99], v40 offset:49152\ns_add_u32 s54, s54, 14336\nv_mfma_f32_16x16x32_bf16 v[72:75], v[100:103], a[144:147], v[72:75]\n"
"buffer_store_dwordx4 v[172:175], v13, s[16:19], s54 offen sc1\nv_mfma_f32_16x16x32_bf16 v[76:79], v[100:103], a[176:179], v[76:79]\nds_read_b128 v[100:103], v44 offset:49152\ns_add_u32 m0, s32, 36864\n"
"s_waitcnt lgkmcnt(6)\nv_mfma_f32_16x16x32_bf16 v[64:67], v[104:107], a[148:151], v[64:67]\ns_mov_b32 s53, 28800\nv_mfma_f32_16x16x32_bf16 v[68:71], v[104:107], a[180:183], v[68:71]\n"
"ds_read_b128 v[104:107], v41 offset:49152\nbuffer_load_dwordx4 v10, s[12:15], s53 offen lds\nv_mfma_f32_16x16x32_bf16 v[72:75], v[108:111], a[148:151], v[72:75]\ns_add_u32 m0, s32, 40960\n"
"v_mfma_f32_16x16x32_bf16 v[76:79], v[108:111], a[180:183], v[76:79]\nds_read_b128 v[108:111], v45 offset:49152\ns_mov_b32 s53, 28928\ns_waitcnt lgkmcnt(6)\n"
"v_mfma_f32_16x16x32_bf16 v[64:67], v[112:115], a[152:155], v[64:67]\nbuffer_load_dwordx4 v10, s[12:15], s53 offen lds\nv_mfma_f32_16x16x32_bf16 v[68:71], v[112:115], a[184:187], v[68:71]\nds_read_b128 v[112:115], v42 offset:49152\n"
"s_add_u32 m0, s32, 45056\nv_mfma_f32_16x16x32_bf16 v[72:75], v[116:119], a[152:155], v[72:75]\ns_mov_b32 s53, 29056\nv_mfma_f32_16x16x32_bf16 v[76:79], v[116:119], a[184:187], v[76:79]\n"
"ds_read_b128 v[116:119], v46 offset:49152\nbuffer_load_dwordx4 v10, s[12:15], s53 offen lds\ns_waitcnt lgkmcnt(6)\nv_mfma_f32_16x16x32_bf16 v[64:67], v[120:123], a[156:159], v[64:67]\n"
"v_mfma_f32_16x16x32_bf16 v[68:71], v[120:123], a[188:191], v[68:71]\nds_read_b128 v[120:123], v43 offset:49152\nv_mfma_f32_16x16x32_bf16 v[72:75], v[124:127], a[156:159], v[72:75]\nv_mfma_f32_16x16x32_bf16 v[76:79], v[124:127], a[188:191], v[76:79]\n"
"ds_read_b128 v[124:127], v47 offset:49152\ns_waitcnt lgkmcnt(6)\nv_mfma_f32_16x16x32_bf16 v[80:83], v[96:99], a[128:131], 0\ns_add_u32 m0, s32, 49152\n"
"v_mfma_f32_16x16x32_bf16 v[84:87], v[96:99], a[160:163], 0\nds_read_b128 v[96:99], v40 offset:49408\ns_mov_b32 s53, 29184\nv_mfma_f32_16x16x32_bf16 v[88:91], v[100:103], a[128:131], 0\n"
"buffer_load_dwordx4 v10, s[12:15], s53 offen lds\nv_mfma_f32_16x16x32_bf16 v[92:95], v[100:103], a[160:163], 0\nds_read_b128 v[100:103], v44 offset:49408\nv_mul_f32 v64, s35, v64\n"
"v_mul_f32 v65, s35, v65\ns_waitcnt lgkmcnt(6)\nv_mfma_f32_16x16x32_bf16 v[80:83], v[104:107], a[132:135], v[80:83]\nv_mul_f32 v66, s35, v66\n"
"v_mul_f32 v67, s35, v67\nv_mfma_f32_16x16x32_bf16 v[84:87], v[104:107], a[164:167], v[84:87]\nds_read_b128 v[104:107], v41 offset:49408\nv_cvt_pk_bf16_f32 v176, v64, v65\n"
"v_cvt_pk_bf16_f32 v177, v66, v67\nv_mfma_f32_16x16x32_bf16 v[88:91], v[108:111], a[132:135], v[88:91]\nv_mul_f32 v72, s35, v72\nv_mul_f32 v73, s35, v73\n"
"v_mfma_f32_16x16x32_bf16 v[92:95], v[108:111], a[164:167], v[92:95]\nds_read_b128 v[108:111], v45 offset:49408\nv_mul_f32 v74, s35, v74\nv_mul_f32 v75, s35, v75\n"
"s_waitcnt lgkmcnt(6)\nv_mfma_f32_16x16x32_bf16 v[80:83], v[112:115], a[136:139], v[80:83]\nv_cvt_pk_bf16_f32 v178, v72, v73\nv_cvt_pk_bf16_f32 v179, v74, v75\n"
"v_mfma_f32_16x16x32_bf16 v[84:87], v[112:115], a[168:171], v[84:87]\nds_read_b128 v[112:115], v42 offset:49408\nv_mul_f32 v68, s35, v68\nv_mul_f32 v69, s35, v69\n"
"v_mfma_f32_16x16x32_bf16 v[88:91], v[116:119], a[136:139], v[88:91]\nv_mul_f32 v70, s35, v70\nv_mul_f32 v71, s35, v71\nv_mfma_f32_16x16x32_bf16 v[92:95], v[116:119], a[168:171], v[92:95]\n"
"ds_read_b128 v[116:119], v46 offset:49408\nv_cvt_pk_bf16_f32 v180, v68, v69\nv_cvt_pk_bf16_f32 v181, v70, v71\ns_waitcnt lgkmcnt(6)\n"
"v_mfma_f32_16x16x32_bf16 v[80:83], v[120:123], a[140:143], v[80:83]\nv_mul_f32 v76, s35, v76\nv_mul_f32 v77, s35, v77\nv_mfma_f32_16x16x32_bf16 v[84:87], v[120:123], a[172:175], v[84:87]\n"
"ds_read_b128 v[120:123], v43 offset:49408\nv_mul_f32 v78, s35, v78\nv_mul_f32 v79, s35, v79\nv_mfma_f32_16x16x32_bf16 v[88:91], v[124:127], a[140:143], v[88:91]\n"
"v_cvt_pk_bf16_f32 v182, v76, v77\nv_cvt_pk_bf16_f32 v183, v78, v79\nv_mfma_f32_16x16x32_bf16 v[92:95], v[124:127], a[172:175], v[92:95]\nds_read_b128 v[124:127], v47 offset:49408\n"
"s_lshl_b32 s54, s43, 11\ns_add_u32 s54, s54, 0\ns_waitcnt lgkmcnt(6)\nv_mfma_f32_16x16x32_bf16 v[80:83], v[96:99], a[144:147], v[80:83]\n"
"buffer_store_dwordx4 v[176:179], v13, s[16:19], s54 offen sc1\nv_mfma_f32_16x16x32_bf16 v[84:87], v[96:99], a[176:179], v[84:87]\ns_add_u32 s54, s54, 14336\nv_mfma_f32_16x16x32_bf16 v[88:91], v[100:103], a[144:147], v[88:91]\n"
"buffer_store_dwordx4 v[180:183], v13, s[16:19], s54 offen sc1\nv_mfma_f32_16x16x32_bf16 v[92:95], v[100:103], a[176:179], v[92:95]\ns_add_u32 m0, s32, 53248\ns_waitcnt lgkmcnt(4)\n"
"v_mfma_f32_16x16x32_bf16 v[80:83], v[104:107], a[148:151], v[80:83]\ns_mov_b32 s53, 29312\nv_mfma_f32_16x16x32_bf16 v[84:87], v[104:107], a[180:183], v[84:87]\nbuffer_load_dwordx4 v10, s[12:15], s53 offen lds\n"
"v_mfma_f32_16x16x32_bf16 v[88:91], v[108:111], a[148:151], v[88:91]\ns_add_u32 m0, s32, 57344\nv_mfma_f32_16x16x32_bf16 v[92:95], v[108:111], a[180:183], v[92:95]\ns_mov_b32 s53, 29440\n"
"s_waitcnt lgkmcnt(2)\nv_mfma_f32_16x16x32_bf16 v[80:83], v[112:115], a[152:155], v[80:83]\nbuffer_load_dwordx4 v10, s[12:15], s53 offen lds\nv_mfma_f32_16x16x32_bf16 v[84:87], v[112:115], a[184:187], v[84:87]\n"
"buffer_load_dword v37, v21, s[24:27], 0 offen sc1\nv_mfma_f32_16x16x32_bf16 v[88:91], v[116:119], a[152:155], v[88:91]\nv_mfma_f32_16x16x32_bf16 v[92:95], v[116:119], a[184:187], v[92:95]\ns_waitcnt lgkmcnt(0)\n"
"v_mfma_f32_16x16x32_bf16 v[80:83], v[120:123], a[156:159], v[80:83]\nv_mfma_f32_16x16x32_bf16 v[84:87], v[120:123], a[188:191], v[84:87]\nv_mfma_f32_16x16x32_bf16 v[88:91], v[124:127], a[156:159], v[88:91]\nv_mfma_f32_16x16x32_bf16 v[92:95], v[124:127], a[188:191], v[92:95]\n"
"s_nop 7\nv_mul_f32 v80, s35, v80\nv_mul_f32 v81, s35, v81\nv_mul_f32 v82, s35, v82\n"
"v_mul_f32 v83, s35, v83\nv_cvt_pk_bf16_f32 v184, v80, v81\nv_cvt_pk_bf16_f32 v185, v82, v83\nv_mul_f32 v88, s35, v88\n"
"v_mul_f32 v89, s35, v89\nv_mul_f32 v90, s35, v90\nv_mul_f32 v91, s35, v91\nv_cvt_pk_bf16_f32 v186, v88, v89\n"
"v_cvt_pk_bf16_f32 v187, v90, v91\nv_mul_f32 v84, s35, v84\nv_mul_f32 v85, s35, v85\nv_mul_f32 v86, s35, v86\n"
"v_mul_f32 v87, s35, v87\nv_cvt_pk_bf16_f32 v188, v84, v85\nv_cvt_pk_bf16_f32 v189, v86, v87\nv_mul_f32 v92, s35, v92\n"
"v_mul_f32 v93, s35, v93\nv_mul_f32 v94, s35, v94\nv_mul_f32 v95, s35, v95\nv_cvt_pk_bf16_f32 v190, v92, v93\n"
"v_cvt_pk_bf16_f32 v191, v94, v95\ns_lshl_b32 s54, s43, 11\ns_add_u32 s54, s54, 1024\nbuffer_store_dwordx4 v[184:187], v13, s[16:19], s54 offen sc1\n"
"s_add_u32 s54, s54, 14336\nbuffer_store_dwordx4 v[188:191], v13, s[16:19], s54 offen sc1\ns_waitcnt vmcnt(2)\nv_mov_b32 v36, s28\n"
"v_cmp_ne_u32 vcc, s28, v37\ns_cbranch_vccz fg0_e1\ns_waitcnt vmcnt(0)\ns_barrier\n"
"buffer_store_dword v36, v19, s[24:27], 0 offen sc1\nfg0_po:\nv_cmp_ne_u32 vcc, s28, v37\ns_cbranch_vccz fg0_pook\n"
"s_add_u32 s55, s55, 1\ns_cmp_gt_u32 s55, 262144\ns_cbranch_scc0 fg0_po2\ns_endpgm\n"
"fg0_po2:\ns_sleep 1\nbuffer_load_dword v37, v21, s[24:27], 0 offen sc1\ns_waitcnt vmcnt(0)\n"
"s_branch fg0_po\nfg0_pook:\ns_mov_b32 s53, 8192\nbuffer_load_dwordx4 v[192:195], v13, s[16:19], s53 offen sc1\n"
"s_mov_b32 s53, 22528\nbuffer_load_dwordx4 v[196:199], v13, s[16:19], s53 offen sc1\ns_mov_b32 s53, 9216\nbuffer_load_dwordx4 v[200:203], v13, s[16:19], s53 offen sc1\n"
"s_mov_b32 s53, 23552\nbuffer_load_dwordx4 v[204:207], v13, s[16:19], s53 offen sc1\ns_mov_b32 s53, 10240\nbuffer_load_dwordx4 v[208:211], v13, s[16:19], s53 offen sc1\n"
"s_mov_b32 s53, 24576\nbuffer_load_dwordx4 v[212:215], v13, s[16:19], s53 offen sc1\ns_mov_b32 s53, 11264\nbuffer_load_dwordx4 v[216:219], v13, s[16:19], s53 offen sc1\n"
"s_mov_b32 s53, 25600\nbuffer_load_dwordx4 v[220:223], v13, s[16:19], s53 offen sc1\ns_mov_b32 s53, 12288\nbuffer_load_dwordx4 v[224:227], v13, s[16:19], s53 offen sc1\n"
"s_mov_b32 s53, 26624\nbuffer_load_dwordx4 v[228:231], v13, s[16:19], s53 offen sc1\ns_mov_b32 s53, 13312\nbuffer_load_dwordx4 v[232:235], v13, s[16:19], s53 offen sc1\n"
"s_mov_b32 s53, 27648\nbuffer_load_dwordx4 v[236:239], v13, s[16:19], s53 offen sc1\nbuffer_store_dword v36, v19, s[24:27], 0 offen sc1\ns_branch fg0_e2\n"
"fg0_e1:\ns_mov_b32 s53, 8192\nbuffer_load_dwordx4 v[192:195], v13, s[16:19], s53 offen sc1\ns_mov_b32 s53, 22528\n"
"buffer_load_dwordx4 v[196:199], v13, s[16:19], s53 offen sc1\ns_mov_b32 s53, 9216\nbuffer_load_dwordx4 v[200:203], v13, s[16:19], s53 offen sc1\ns_mov_b32 s53, 23552\n"
"buffer_load_dwordx4 v[204:207], v13, s[16:19], s53 offen sc1\ns_mov_b32 s53, 10240\nbuffer_load_dwordx4 v[208:211], v13, s[16:19], s53 offen sc1\ns_mov_b32 s53, 24576\n"
"buffer_load_dwordx4 v[212:215], v13, s[16:19], s53 offen sc1\ns_mov_b32 s53, 11264\nbuffer_load_dwordx4 v[216:219], v13, s[16:19], s53 offen sc1\ns_mov_b32 s53, 25600\n"
"buffer_load_dwordx4 v[220:223], v13, s[16:19], s53 offen sc1\ns_mov_b32 s53, 12288\nbuffer_load_dwordx4 v[224:227], v13, s[16:19], s53 offen sc1\ns_mov_b32 s53, 26624\n"
"buffer_load_dwordx4 v[228:231], v13, s[16:19], s53 offen sc1\ns_mov_b32 s53, 13312\nbuffer_load_dwordx4 v[232:235], v13, s[16:19], s53 offen sc1\ns_mov_b32 s53, 27648\n"
"buffer_load_dwordx4 v[236:239], v13, s[16:19], s53 offen sc1\ns_waitcnt vmcnt(12)\ns_barrier\nbuffer_store_dword v36, v19, s[24:27], 0 offen sc1\n"
"fg0_e2:\ns_add_u32 m0, s32, 65536\ns_mov_b32 s53, 57344\nbuffer_load_dwordx4 v10, s[12:15], s53 offen lds\n"
"s_add_u32 m0, s32, 69632\ns_mov_b32 s53, 57472\nbuffer_load_dwordx4 v10, s[12:15], s53 offen lds\ns_add_u32 m0, s32, 73728\n"
"s_mov_b32 s53, 57600\nbuffer_load_dwordx4 v10, s[12:15], s53 offen lds\ns_add_u32 m0, s32, 77824\ns_mov_b32 s53, 57728\n"
"buffer_load_dwordx4 v10, s[12:15], s53 offen lds\ns_add_u32 m0, s32, 81920\ns_mov_b32 s53, 57856\nbuffer_load_dwordx4 v10, s[12:15], s53 offen lds\n"
"s_add_u32 m0, s32, 86016\ns_mov_b32 s53, 57984\nbuffer_load_dwordx4 v10, s[12:15], s53 offen lds\ns_add_u32 m0, s32, 90112\n"
"s_mov_b32 s53, 58112\nbuffer_load_dwordx4 v10, s[12:15], s53 offen lds\ns_sub_u32 s48, s28, 1\ns_mov_b32 s49, 86016\n"
"s_mov_b32 s50, 0\ns_mov_b32 s51, 0\ns_add_u32 s52, s32, 98304\ns_add_u32 s47, s32, 131072\n"
"ds_read_b128 v[40:43], v32 offset:0\nds_read_b128 v[44:47], v33 offset:0\nds_read_b128 v[48:51], v34 offset:0\nds_read_b128 v[52:55], v35 offset:0\n"
"s_barrier\ns_waitcnt lgkmcnt(3)\nv_mfma_f32_16x16x32_bf16 v[64:67], v[128:131], v[40:43], 0\nv_mov_b32 v36, s48\n"
"s_add_u32 s48, s48, 1\ns_nop 0\nbuffer_store_dword v36, v19, s[24:27], s33 offen sc1\nv_mfma_f32_16x16x32_bf16 v[68:71], v[132:135], v[40:43], 0\n"
"ds_read_b128 v[40:43], v32 offset:4096\ns_waitcnt lgkmcnt(3)\nv_mfma_f32_16x16x32_bf16 v[72:75], v[128:131], v[44:47], 0\nv_mfma_f32_16x16x32_bf16 v[76:79], v[132:135], v[44:47], 0\n"
"ds_read_b128 v[44:47], v33 offset:4096\ns_waitcnt lgkmcnt(3)\nv_mfma_f32_16x16x32_bf16 v[64:67], v[136:139], v[48:51], v[64:67]\ns_add_u32 m0, s52, 0\n"
"v_mfma_f32_16x16x32_bf16 v[68:71], v[140:143], v[48:51], v[68:71]\nds_read_b128 v[48:51], v34 offset:4096\ns_add_u32 s53, s49, 0\ns_waitcnt lgkmcnt(3)\n"
"v_mfma_f32_16x16x32_bf16 v[72:75], v[136:139], v[52:55], v[72:75]\nbuffer_load_dwordx4 v10, s[12:15], s53 offen lds\nv_mfma_f32_16x16x32_bf16 v[76:79], v[140:143], v[52:55], v[76:79]\nds_read_b128 v[52:55], v35 offset:4096\n"
"s_add_u32 m0, s52, 4096\ns_waitcnt lgkmcnt(3)\nv_mfma_f32_16x16x32_bf16 v[64:67], v[144:147], v[40:43], v[64:67]\ns_add_u32 s53, s49, 128\n"
"v_mfma_f32_16x16x32_bf16 v[68:71], v[148:151], v[40:43], v[68:71]\nds_read_b128 v[40:43], v32 offset:8192\nbuffer_load_dwordx4 v10, s[12:15], s53 offen lds\ns_waitcnt lgkmcnt(3)\n"
"v_mfma_f32_16x16x32_bf16 v[72:75], v[144:147], v[44:47], v[72:75]\ns_add_u32 m0, s52, 8192\nv_mfma_f32_16x16x32_bf16 v[76:79], v[148:151], v[44:47], v[76:79]\nds_read_b128 v[44:47], v33 offset:8192\n"
"s_add_u32 s53, s49, 256\ns_waitcnt lgkmcnt(3)\nv_mfma_f32_16x16x32_bf16 v[64:67], v[152:155], v[48:51], v[64:67]\nbuffer_load_dwordx4 v10, s[12:15], s53 offen lds\n"
"v_mfma_f32_16x16x32_bf16 v[68:71], v[156:159], v[48:51], v[68:71]\nds_read_b128 v[48:51], v34 offset:8192\ns_add_u32 m0, s52, 12288\ns_waitcnt lgkmcnt(3)\n"
"v_mfma_f32_16x16x32_bf16 v[72:75], v[152:155], v[52:55], v[72:75]\ns_add_u32 s53, s49, 384\nv_mfma_f32_16x16x32_bf16 v[76:79], v[156:159], v[52:55], v[76:79]\nds_read_b128 v[52:55], v35 offset:8192\n"
"buffer_load_dwordx4 v10, s[12:15], s53 offen lds\ns_waitcnt lgkmcnt(3)\nv_mfma_f32_16x16x32_bf16 v[64:67], v[160:163], v[40:43], v[64:67]\ns_add_u32 m0, s52, 16384\n"
"v_mfma_f32_16x16x32_bf16 v[68:71], v[164:167], v[40:43], v[68:71]\nds_read_b128 v[40:43], v32 offset:12288\ns_add_u32 s53, s49, 512\ns_waitcnt lgkmcnt(3)\n"
"v_mfma_f32_16x16x32_bf16 v[72:75], v[160:163], v[44:47], v[72:75]\nbuffer_load_dwordx4 v10, s[12:15], s53 offen lds\nv_mfma_f32_16x16x32_bf16 v[76:79], v[164:167], v[44:47], v[76:79]\nds_read_b128 v[44:47], v33 offset:12288\n"
"s_add_u32 m0, s52, 20480\ns_waitcnt lgkmcnt(3)\nv_mfma_f32_16x16x32_bf16 v[64:67], v[168:171], v[48:51], v[64:67]\ns_add_u32 s53, s49, 640\n"
"v_mfma_f32_16x16x32_bf16 v[68:71], v[172:175], v[48:51], v[68:71]\nds_read_b128 v[48:51], v34 offset:12288\nbuffer_load_dwordx4 v10, s[12:15], s53 offen lds\ns_waitcnt lgkmcnt(3)\n"
"v_mfma_f32_16x16x32_bf16 v[72:75], v[168:171], v[52:55], v[72:75]\ns_add_u32 m0, s52, 24576\nv_mfma_f32_16x16x32_bf16 v[76:79], v[172:175], v[52:55], v[76:79]\nds_read_b128 v[52:55], v35 offset:12288\n"
"s_add_u32 s53, s49, 768\ns_waitcnt lgkmcnt(3)\nv_mfma_f32_16x16x32_bf16 v[64:67], v[176:179], v[40:43], v[64:67]\nbuffer_load_dwordx4 v10, s[12:15], s53 offen lds\n"
"v_mfma_f32_16x16x32_bf16 v[68:71], v[180:183], v[40:43], v[68:71]\nds_read_b128 v[40:43], v32 offset:16384\ns_waitcnt lgkmcnt(3)\nv_mfma_f32_16x16x32_bf16 v[72:75], v[176:179], v[44:47], v[72:75]\n"
"v_mfma_f32_16x16x32_bf16 v[76:79], v[180:183], v[44:47], v[76:79]\nds_read_b128 v[44:47], v33 offset:16384\ns_waitcnt lgkmcnt(3)\nv_mfma_f32_16x16x32_bf16 v[64:67], v[184:187], v[48:51], v[64:67]\n"
"v_mfma_f32_16x16x32_bf16 v[68:71], v[188:191], v[48:51], v[68:71]\nds_read_b128 v[48:51], v34 offset:16384\ns_waitcnt lgkmcnt(3)\nv_mfma_f32_16x16x32_bf16 v[72:75], v[184:187], v[52:55], v[72:75]\n"
"v_mfma_f32_16x16x32_bf16 v[76:79], v[188:191], v[52:55], v[76:79]\nds_read_b128 v[52:55], v35 offset:16384\ns_waitcnt vmcnt(16)\ns_waitcnt lgkmcnt(3)\n"
"v_mfma_f32_16x16x32_bf16 v[64:67], v[192:195], v[40:43], v[64:67]\nv_mfma_f32_16x16x32_bf16 v[68:71], v[196:199], v[40:43], v[68:71]\nds_read_b128 v[40:43], v32 offset:20480\ns_waitcnt lgkmcnt(3)\n"
"v_mfma_f32_16x16x32_bf16 v[72:75], v[192:195], v[44:47], v[72:75]\nv_mfma_f32_16x16x32_bf16 v[76:79], v[196:199], v[44:47], v[76:79]\nds_read_b128 v[44:47], v33 offset:20480\ns_waitcnt lgkmcnt(3)\n"
"v_mfma_f32_16x16x32_bf16 v[64:67], v[200:203], v[48:51], v[64:67]\nv_mfma_f32_16x16x32_bf16 v[68:71], v[204:207], v[48:51], v[68:71]\nds_read_b128 v[48:51], v34 offset:20480\ns_waitcnt lgkmcnt(3)\n"
"v_mfma_f32_16x16x32_bf16 v[72:75], v[200:203], v[52:55], v[72:75]\nv_mfma_f32_16x16x32_bf16 v[76:79], v[204:207], v[52:55], v[76:79]\nds_read_b128 v[52:55], v35 offset:20480\ns_waitcnt lgkmcnt(3)\n"
"v_mfma_f32_16x16x32_bf16 v[64:67], v[208:211], v[40:43], v[64:67]\nv_mfma_f32_16x16x32_bf16 v[68:71], v[212:215], v[40:43], v[68:71]\nds_read_b128 v[40:43], v32 offset:24576\ns_waitcnt lgkmcnt(3)\n"
"v_mfma_f32_16x16x32_bf16 v[72:75], v[208:211], v[44:47], v[72:75]\nv_mfma_f32_16x16x32_bf16 v[76:79], v[212:215], v[44:47], v[76:79]\nds_read_b128 v[44:47], v33 offset:24576\ns_waitcnt lgkmcnt(3)\n"
"v_mfma_f32_16x16x32_bf16 v[64:67], v[216:219], v[48:51], v[64:67]\ns_add_u32 s54, s51, 32768\ns_cmp_eq_u32 s54, 163840\nv_mfma_f32_16x16x32_bf16 v[68:71], v[220:223], v[48:51], v[68:71]\n"
"s_cselect_b32 s54, 0, s54\ns_sub_u32 s54, s54, s51\nds_read_b128 v[48:51], v34 offset:24576\ns_waitcnt lgkmcnt(3)\n"
"v_mfma_f32_16x16x32_bf16 v[72:75], v[216:219], v[52:55], v[72:75]\nv_mfma_f32_16x16x32_bf16 v[76:79], v[220:223], v[52:55], v[76:79]\nds_read_b128 v[52:55], v35 offset:24576\ns_waitcnt lgkmcnt(3)\n"
"v_mfma_f32_16x16x32_bf16 v[64:67], v[224:227], v[40:43], v[64:67]\nv_add_u32 v32, s54, v32\nv_mfma_f32_16x16x32_bf16 v[68:71], v[228:231], v[40:43], v[68:71]\nds_read_b128 v[40:43], v32 offset:0\n"
"s_waitcnt lgkmcnt(3)\nv_mfma_f32_16x16x32_bf16 v[72:75], v[224:227], v[44:47], v[72:75]\nv_add_u32 v33, s54, v33\nv_mfma_f32_16x16x32_bf16 v[76:79], v[228:231], v[44:47], v[76:79]\n"
"ds_read_b128 v[44:47], v33 offset:0\ns_waitcnt lgkmcnt(3)\nv_mfma_f32_16x16x32_bf16 v[64:67], v[232:235], v[48:51], v[64:67]\nv_add_u32 v34, s54, v34\n"
"v_mfma_f32_16x16x32_bf16 v[68:71], v[236:239], v[48:51], v[68:71]\nds_read_b128 v[48:51], v34 offset:0\ns_waitcnt lgkmcnt(3)\nv_mfma_f32_16x16x32_bf16 v[72:75], v[232:235], v[52:55], v[72:75]\n"
"v_add_u32 v35, s54, v35\nv_mfma_f32_16x16x32_bf16 v[76:79], v[236:239], v[52:55], v[76:79]\nds_read_b128 v[52:55], v35 offset:0\ns_mov_b32 s52, s47\n"
"s_add_u32 s47, s51, s32\ns_add_u32 s51, s51, s54\ns_add_u32 s49, s49, 28672\ns_waitcnt vmcnt(8)\n"
"s_barrier\ns_waitcnt lgkmcnt(3)\nv_mfma_f32_16x16x32_bf16 v[96:99], v[128:131], v[40:43], 0\nv_mov_b32 v36, s48\n"
"s_add_u32 s48, s48, 1\ns_nop 0\nbuffer_store_dword v36, v19, s[24:27], s33 offen sc1\nv_mfma_f32_16x16x32_bf16 v[100:103], v[132:135], v[40:43], 0\n"
"ds_read_b128 v[40:43], v32 offset:4096\ns_waitcnt lgkmcnt(3)\nv_mfma_f32_16x16x32_bf16 v[104:107], v[128:131], v[44:47], 0\nv_mfma_f32_16x16x32_bf16 v[108:111], v[132:135], v[44:47], 0\n"
"ds_read_b128 v[44:47], v33 offset:4096\nv_cvt_pk_bf16_f32 v56, v64, v65\ns_waitcnt lgkmcnt(3)\nv_mfma_f32_16x16x32_bf16 v[96:99], v[136:139], v[48:51], v[96:99]\n"
"v_cvt_pk_bf16_f32 v57, v66, v67\nv_mfma_f32_16x16x32_bf16 v[100:103], v[140:143], v[48:51], v[100:103]\nds_read_b128 v[48:51], v34 offset:4096\nv_cvt_pk_bf16_f32 v58, v68, v69\n"
"s_waitcnt lgkmcnt(3)\nv_mfma_f32_16x16x32_bf16 v[104:107], v[136:139], v[52:55], v[104:107]\nv_cvt_pk_bf16_f32 v59, v70, v71\nv_mfma_f32_16x16x32_bf16 v[108:111], v[140:143], v[52:55], v[108:111]\n"
"ds_read_b128 v[52:55], v35 offset:4096\ns_waitcnt lgkmcnt(3)\nv_mfma_f32_16x16x32_bf16 v[96:99], v[144:147], v[40:43], v[96:99]\nv_mfma_f32_16x16x32_bf16 v[100:103], v[148:151], v[40:43], v[100:103]\n"
"ds_read_b128 v[40:43], v32 offset:8192\nbuffer_store_dwordx4 v[56:59], v15, s[20:23], s50 offen sc1\ns_waitcnt lgkmcnt(3)\nv_mfma_f32_16x16x32_bf16 v[104:107], v[144:147], v[44:47], v[104:107]\n"
"v_cvt_pk_bf16_f32 v60, v72, v73\nv_mfma_f32_16x16x32_bf16 v[108:111], v[148:151], v[44:47], v[108:111]\nds_read_b128 v[44:47], v33 offset:8192\nv_cvt_pk_bf16_f32 v61, v74, v75\n"
"s_waitcnt lgkmcnt(3)\nv_mfma_f32_16x16x32_bf16 v[96:99], v[152:155], v[48:51], v[96:99]\nv_cvt_pk_bf16_f32 v62, v76, v77\nv_mfma_f32_16x16x32_bf16 v[100:103], v[156:159], v[48:51], v[100:103]\n"
"ds_read_b128 v[48:51], v34 offset:8192\nv_cvt_pk_bf16_f32 v63, v78, v79\ns_waitcnt lgkmcnt(3)\nv_mfma_f32_16x16x32_bf16 v[104:107], v[152:155], v[52:55], v[104:107]\n"
"v_mfma_f32_16x16x32_bf16 v[108:111], v[156:159], v[52:55], v[108:111]\nds_read_b128 v[52:55], v35 offset:8192\ns_waitcnt lgkmcnt(3)\nv_mfma_f32_16x16x32_bf16 v[96:99], v[160:163], v[40:43], v[96:99]\n"
"buffer_store_dwordx4 v[60:63], v16, s[20:23], s50 offen sc1\nv_mfma_f32_16x16x32_bf16 v[100:103], v[164:167], v[40:43], v[100:103]\nds_read_b128 v[40:43], v32 offset:12288\ns_add_u32 s50, s50, s36\n"
"s_waitcnt lgkmcnt(3)\nv_mfma_f32_16x16x32_bf16 v[104:107], v[160:163], v[44:47], v[104:107]\ns_add_u32 m0, s52, 0\nv_mfma_f32_16x16x32_bf16 v[108:111], v[164:167], v[44:47], v[108:111]\n"
"ds_read_b128 v[44:47], v33 offset:12288\ns_add_u32 s53, s49, 0\ns_waitcnt lgkmcnt(3)\nv_mfma_f32_16x16x32_bf16 v[96:99], v[168:171], v[48:51], v[96:99]\n"
"buffer_load_dwordx4 v10, s[12:15], s53 offen lds\nv_mfma_f32_16x16x32_bf16 v[100:103], v[172:175], v[48:51], v[100:103]\nds_read_b128 v[48:51], v34 offset:12288\ns_add_u32 m0, s52, 4096\n"
"s_waitcnt lgkmcnt(3)\nv_mfma_f32_16x16x32_bf16 v[104:107], v[168:171], v[52:55], v[104:107]\ns_add_u32 s53, s49, 128\nv_mfma_f32_16x16x32_bf16 v[108:111], v[172:175], v[52:55], v[108:111]\n"
"ds_read_b128 v[52:55], v35 offset:12288\nbuffer_load_dwordx4 v10, s[12:15], s53 offen lds\ns_waitcnt lgkmcnt(3)\nv_mfma_f32_16x16x32_bf16 v[96:99], v[176:179], v[40:43], v[96:99]\n"
"s_add_u32 m0, s52, 8192\nv_mfma_f32_16x16x32_bf16 v[100:103], v[180:183], v[40:43], v[100:103]\nds_read_b128 v[40:43], v32 offset:16384\ns_add_u32 s53, s49, 256\n"
"s_waitcnt lgkmcnt(3)\nv_mfma_f32_16x16x32_bf16 v[104:107], v[176:179], v[44:47], v[104:107]\nbuffer_load_dwordx4 v10, s[12:15], s53 offen lds\nv_mfma_f32_16x16x32_bf16 v[108:111], v[180:183], v[44:47], v[108:111]\n"
"ds_read_b128 v[44:47], v33 offset:16384\ns_add_u32 m0, s52, 12288\ns_waitcnt lgkmcnt(3)\nv_mfma_f32_16x16x32_bf16 v[96:99], v[184:187], v[48:51], v[96:99]\n"
"s_add_u32 s53, s49, 384\nv_mfma_f32_16x16x32_bf16 v[100:103], v[188:191], v[48:51], v[100:103]\nds_read_b128 v[48:51], v34 offset:16384\nbuffer_load_dwordx4 v10, s[12:15], s53 offen lds\n"
"s_waitcnt lgkmcnt(3)\nv_mfma_f32_16x16x32_bf16 v[104:107], v[184:187], v[52:55], v[104:107]\ns_add_u32 m0, s52, 16384\nv_mfma_f32_16x16x32_bf16 v[108:111], v[188:191], v[52:55], v[108:111]\n"
"ds_read_b128 v[52:55], v35 offset:16384\ns_add_u32 s53, s49, 512\ns_waitcnt lgkmcnt(3)\nv_mfma_f32_16x16x32_bf16 v[96:99], v[192:195], v[40:43], v[96:99]\n"
"buffer_load_dwordx4 v10, s[12:15], s53 offen lds\nv_mfma_f32_16x16x32_bf16 v[100:103], v[196:199], v[40:43], v[100:103]\nds_read_b128 v[40:43], v32 offset:20480\ns_add_u32 m0, s52, 20480\n"
"s_waitcnt lgkmcnt(3)\nv_mfma_f32_16x16x32_bf16 v[104:107], v[192:195], v[44:47], v[104:107]\ns_add_u32 s53, s49, 640\nv_mfma_f32_16x16x32_bf16 v[108:111], v[196:199], v[44:47], v[108:111]\n"
"ds_read_b128 v[44:47], v33 offset:20480\nbuffer_load_dwordx4 v10, s[12:15], s53 offen lds\ns_waitcnt lgkmcnt(3)\nv_mfma_f32_16x16x32_bf16 v[96:99], v[200:203], v[48:51], v[96:99]\n"
"s_add_u32 m0, s52, 24576\nv_mfma_f32_16x16x32_bf16 v[100:103], v[204:207], v[48:51], v[100:103]\nds_read_b128 v[48:51], v34 offset:20480\ns_add_u32 s53, s49, 768\n"
"s_waitcnt lgkmcnt(3)\nv_mfma_f32_16x16x32_bf16 v[104:107], v[200:203], v[52:55], v[104:107]\nbuffer_load_dwordx4 v10, s[12:15], s53 offen lds\nv_mfma_f32_16x16x32_bf16 v[108:111], v[204:207], v[52:55], v[108:111]\n"
"ds_read_b128 v[52:55], v35 offset:20480\ns_waitcnt lgkmcnt(3)\nv_mfma_f32_16x16x32_bf16 v[96:99], v[208:211], v[40:43], v[96:99]\nv_mfma_f32_16x16x32_bf16 v[100:103], v[212:215], v[40:43], v[100:103]\n"
"ds_read_b128 v[40:43], v32 offset:24576\ns_waitcnt lgkmcnt(3)\nv_mfma_f32_16x16x32_bf16 v[104:107], v[208:211], v[44:47], v[104:107]\nv_mfma_f32_16x16x32_bf16 v[108:111], v[212:215], v[44:47], v[108:111]\n"
"ds_read_b128 v[44:47], v33 offset:24576\ns_waitcnt lgkmcnt(3)\nv_mfma_f32_16x16x32_bf16 v[96:99], v[216:219], v[48:51], v[96:99]\ns_add_u32 s54, s51, 32768\n"
"s_cmp_eq_u32 s54, 163840\nv_mfma_f32_16x16x32_bf16 v[100:103], v[220:223], v[48:51], v[100:103]\ns_cselect_b32 s54, 0, s54\ns_sub_u32 s54, s54, s51\n"
"ds_read_b128 v[48:51], v34 offset:24576\ns_waitcnt lgkmcnt(3)\nv_mfma_f32_16x16x32_bf16 v[104:107], v[216:219], v[52:55], v[104:107]\nv_mfma_f32_16x16x32_bf16 v[108:111], v[220:223], v[52:55], v[108:111]\n"
"ds_read_b128 v[52:55], v35 offset:24576\ns_waitcnt lgkmcnt(3)\nv_mfma_f32_16x16x32_bf16 v[96:99], v[224:227], v[40:43], v[96:99]\nv_add_u32 v32, s54, v32\n"
"v_mfma_f32_16x16x32_bf16 v[100:103], v[228:231], v[40:43], v[100:103]\nds_read_b128 v[40:43], v32 offset:0\ns_waitcnt lgkmcnt(3)\nv_mfma_f32_16x16x32_bf16 v[104:107], v[224:227], v[44:47], v[104:107]\n"
"v_add_u32 v33, s54, v33\nv_mfma_f32_16x16x32_bf16 v[108:111], v[228:231], v[44:47], v[108:111]\nds_read_b128 v[44:47], v33 offset:0\ns_waitcnt lgkmcnt(3)\n"
"v_mfma_f32_16x16x32_bf16 v[96:99], v[232:235], v[48:51], v[96:99]\nv_add_u32 v34, s54, v34\nv_mfma_f32_16x16x32_bf16 v[100:103], v[236:239], v[48:51], v[100:103]\nds_read_b128 v[48:51], v34 offset:0\n"
"s_waitcnt lgkmcnt(3)\nv_mfma_f32_16x16x32_bf16 v[104:107], v[232:235], v[52:55], v[104:107]\nv_add_u32 v35, s54, v35\nv_mfma_f32_16x16x32_bf16 v[108:111], v[236:239], v[52:55], v[108:111]\n"
"ds_read_b128 v[52:55], v35 offset:0\ns_mov_b32 s52, s47\ns_add_u32 s47, s51, s32\ns_add_u32 s51, s51, s54\n"
"s_add_u32 s49, s49, 28672\ns_waitcnt vmcnt(7)\ns_barrier\ns_waitcnt lgkmcnt(3)\n"
"v_mfma_f32_16x16x32_bf16 v[64:67], v[128:131], v[40:43], 0\nv_mov_b32 v36, s48\ns_add_u32 s48, s48, 1\ns_nop 0\n"
"buffer_store_dword v36, v19, s[24:27], s33 offen sc1\nv_mfma_f32_16x16x32_bf16 v[68:71], v[132:135], v[40:43], 0\nds_read_b128 v[40:43], v32 offset:4096\ns_waitcnt lgkmcnt(3)\n"
"v_mfma_f32_16x16x32_bf16 v[72:75], v[128:131], v[44:47], 0\nv_mfma_f32_16x16x32_bf16 v[76:79], v[132:135], v[44:47], 0\nds_read_b128 v[44:47], v33 offset:4096\nv_cvt_pk_bf16_f32 v56, v96, v97\n"
"s_waitcnt lgkmcnt(3)\nv_mfma_f32_16x16x32_bf16 v[64:67], v[136:139], v[48:51], v[64:67]\nv_cvt_pk_bf16_f32 v57, v98, v99\nv_mfma_f32_16x16x32_bf16 v[68:71], v[140:143], v[48:51], v[68:71]\n"
"ds_read_b128 v[48:51], v34 offset:4096\nv_cvt_pk_bf16_f32 v58, v100, v101\ns_waitcnt lgkmcnt(3)\nv_mfma_f32_16x16x32_bf16 v[72:75], v[136:139], v[52:55], v[72:75]\n"
"v_cvt_pk_bf16_f32 v59, v102, v103\nv_mfma_f32_16x16x32_bf16 v[76:79], v[140:143], v[52:55], v[76:79]\nds_read_b128 v[52:55], v35 offset:4096\ns_waitcnt lgkmcnt(3)\n"
"v_mfma_f32_16x16x32_bf16 v[64:67], v[144:147], v[40:43], v[64:67]\nv_mfma_f32_16x16x32_bf16 v[68:71], v[148:151], v[40:43], v[68:71]\nds_read_b128 v[40:43], v32 offset:8192\nbuffer_store_dwordx4 v[56:59], v15, s[20:23], s50 offen sc1\n"
"s_waitcnt lgkmcnt(3)\nv_mfma_f32_16x16x32_bf16 v[72:75], v[144:147], v[44:47], v[72:75]\nv_cvt_pk_bf16_f32 v60, v104, v105\nv_mfma_f32_16x16x32_bf16 v[76:79], v[148:151], v[44:47], v[76:79]\n"
"ds_read_b128 v[44:47], v33 offset:8192\nv_cvt_pk_bf16_f32 v61, v106, v107\ns_waitcnt lgkmcnt(3)\nv_mfma_f32_16x16x32_bf16 v[64:67], v[152:155], v[48:51], v[64:67]\n"
"v_cvt_pk_bf16_f32 v62, v108, v109\nv_mfma_f32_16x16x32_bf16 v[68:71], v[156:159], v[48:51], v[68:71]\nds_read_b128 v[48:51], v34 offset:8192\nv_cvt_pk_bf16_f32 v63, v110, v111\n"
"s_waitcnt lgkmcnt(3)\nv_mfma_f32_16x16x32_bf16 v[72:75], v[152:155], v[52:55], v[72:75]\nv_mfma_f32_16x16x32_bf16 v[76:79], v[156:159], v[52:55], v[76:79]\nds_read_b128 v[52:55], v35 offset:8192\n"
"s_waitcnt lgkmcnt(3)\nv_mfma_f32_16x16x32_bf16 v[64:67], v[160:163], v[40:43], v[64:67]\nbuffer_store_dwordx4 v[60:63], v16, s[20:23], s50 offen sc1\nv_mfma_f32_16x16x32_bf16 v[68:71], v[164:167], v[40:43], v[68:71]\n"
"ds_read_b128 v[40:43], v32 offset:12288\ns_add_u32 s50, s50, s36\ns_waitcnt lgkmcnt(3)\nv_mfma_f32_16x16x32_bf16 v[72:75], v[160:163], v[44:47], v[72:75]\n"
"s_add_u32 m0, s52, 0\nv_mfma_f32_16x16x32_bf16 v[76:79], v[164:167], v[44:47], v[76:79]\nds_read_b128 v[44:47], v33 offset:12288\ns_add_u32 s53, s49, 0\n"
"s_waitcnt lgkmcnt(3)\nv_mfma_f32_16x16x32_bf16 v[64:67], v[168:171], v[48:51], v[64:67]\nbuffer_load_dwordx4 v10, s[12:15], s53 offen lds\nv_mfma_f32_16x16x32_bf16 v[68:71], v[172:175], v[48:51], v[68:71]\n"
"ds_read_b128 v[48:51], v34 offset:12288\ns_add_u32 m0, s52, 4096\ns_waitcnt lgkmcnt(3)\nv_mfma_f32_16x16x32_bf16 v[72:75], v[168:171], v[52:55], v[72:75]\n"
"s_add_u32 s53, s49, 128\nv_mfma_f32_16x16x32_bf16 v[76:79], v[172:175], v[52:55], v[76:79]\nds_read_b128 v[52:55], v35 offset:12288\nbuffer_load_dwordx4 v10, s[12:15], s53 offen lds\n"
"s_waitcnt lgkmcnt(3)\nv_mfma_f32_16x16x32_bf16 v[64:67], v[176:179], v[40:43], v[64:67]\ns_add_u32 m0, s52, 8192\nv_mfma_f32_16x16x32_bf16 v[68:71], v[180:183], v[40:43], v[68:71]\n"
"ds_read_b128 v[40:43], v32 offset:16384\ns_add_u32 s53, s49, 256\ns_waitcnt lgkmcnt(3)\nv_mfma_f32_16x16x32_bf16 v[72:75], v[176:179], v[44:47], v[72:75]\n"
"buffer_load_dwordx4 v10, s[12:15], s53 offen lds\nv_mfma_f32_16x16x32_bf16 v[76:79], v[180:183], v[44:47], v[76:79]\nds_read_b128 v[44:47], v33 offset:16384\ns_add_u32 m0, s52, 12288\n"
"s_waitcnt lgkmcnt(3)\nv_mfma_f32_16x16x32_bf16 v[64:67], v[184:187], v[48:51], v[64:67]\ns_add_u32 s53, s49, 384\nv_mfma_f32_16x16x32_bf16 v[68:71], v[188:191], v[48:51], v[68:71]\n"
"ds_read_b128 v[48:51], v34 offset:16384\nbuffer_load_dwordx4 v10, s[12:15], s53 offen lds\ns_waitcnt lgkmcnt(3)\nv_mfma_f32_16x16x32_bf16 v[72:75], v[184:187], v[52:55], v[72:75]\n"
"s_add_u32 m0, s52, 16384\nv_mfma_f32_16x16x32_bf16 v[76:79], v[188:191], v[52:55], v[76:79]\nds_read_b128 v[52:55], v35 offset:16384\ns_add_u32 s53, s49, 512\n"
"s_waitcnt lgkmcnt(3)\nv_mfma_f32_16x16x32_bf16 v[64:67], v[192:195], v[40:43], v[64:67]\nbuffer_load_dwordx4 v10, s[12:15], s53 offen lds\nv_mfma_f32_16x16x32_bf16 v[68:71], v[196:199], v[40:43], v[68:71]\n"
"ds_read_b128 v[40:43], v32 offset:20480\ns_add_u32 m0, s52, 20480\ns_waitcnt lgkmcnt(3)\nv_mfma_f32_16x16x32_bf16 v[72:75], v[192:195], v[44:47], v[72:75]\n"
"s_add_u32 s53, s49, 640\nv_mfma_f32_16x16x32_bf16 v[76:79], v[196:199], v[44:47], v[76:79]\nds_read_b128 v[44:47], v33 offset:20480\nbuffer_load_dwordx4 v10, s[12:15], s53 offen lds\n"
"s_waitcnt lgkmcnt(3)\nv_mfma_f32_16x16x32_bf16 v[64:67], v[200:203], v[48:51], v[64:67]\ns_add_u32 m0, s52, 24576\nv_mfma_f32_16x16x32_bf16 v[68:71], v[204:207], v[48:51], v[68:71]\n"
"ds_read_b128 v[48:51], v34 offset:20480\ns_add_u32 s53, s49, 768\ns_waitcnt lgkmcnt(3)\nv_mfma_f32_16x16x32_bf16 v[72:75], v[200:203], v[52:55], v[72:75]\n"
"buffer_load_dwordx4 v10, s[12:15], s53 offen lds\nv_mfma_f32_16x16x32_bf16 v[76:79], v[204:207], v[52:55], v[76:79]\nds_read_b128 v[52:55], v35 offset:20480\ns_waitcnt lgkmcnt(3)\n"
"v_mfma_f32_16x16x32_bf16 v[64:67], v[208:211], v[40:43], v[64:67]\nv_mfma_f32_16x16x32_bf16 v[68:71], v[212:215], v[40:43], v[68:71]\nds_read_b128 v[40:43], v32 offset:24576\ns_waitcnt lgkmcnt(3)\n"
"v_mfma_f32_16x16x32_bf16 v[72:75], v[208:211], v[44:47], v[72:75]\nv_mfma_f32_16x16x32_bf16 v[76:79], v[212:215], v[44:47], v[76:79]\nds_read_b128 v[44:47], v33 offset:24576\ns_waitcnt lgkmcnt(3)\n"
"v_mfma_f32_16x16x32_bf16 v[64:67], v[216:219], v[48:51], v[64:67]\ns_add_u32 s54, s51, 32768\ns_cmp_eq_u32 s54, 163840\nv_mfma_f32_16x16x32_bf16 v[68:71], v[220:223], v[48:51], v[68:71]\n"
"s_cselect_b32 s54, 0, s54\ns_sub_u32 s54, s54, s51\nds_read_b128 v[48:51], v34 offset:24576\ns_waitcnt lgkmcnt(3)\n"
"v_mfma_f32_16x16x32_bf16 v[72:75], v[216:219], v[52:55], v[72:75]\nv_mfma_f32_16x16x32_bf16 v[76:79], v[220:223], v[52:55], v[76:79]\nds_read_b128 v[52:55], v35 offset:24576\ns_waitcnt lgkmcnt(3)\n"
"v_mfma_f32_16x16x32_bf16 v[64:67], v[224:227], v[40:43], v[64:67]\nv_add_u32 v32, s54, v32\nv_mfma_f32_16x16x32_bf16 v[68:71], v[228:231], v[40:43], v[68:71]\nds_read_b128 v[40:43], v32 offset:0\n"
"s_waitcnt lgkmcnt(3)\nv_mfma_f32_16x16x32_bf16 v[72:75], v[224:227], v[44:47], v[72:75]\nv_add_u32 v33, s54, v33\nv_mfma_f32_16x16x32_bf16 v[76:79], v[228:231], v[44:47], v[76:79]\n"
"ds_read_b128 v[44:47], v33 offset:0\ns_waitcnt lgkmcnt(3)\nv_mfma_f32_16x16x32_bf16 v[64:67], v[232:235], v[48:51], v[64:67]\nv_add_u32 v34, s54, v34\n"
"v_mfma_f32_16x16x32_bf16 v[68:71], v[236:239], v[48:51], v[68:71]\nds_read_b128 v[48:51], v34 offset:0\ns_waitcnt lgkmcnt(3)\nv_mfma_f32_16x16x32_bf16 v[72:75], v[232:235], v[52:55], v[72:75]\n"
"v_add_u32 v35, s54, v35\nv_mfma_f32_16x16x32_bf16 v[76:79], v[236:239], v[52:55], v[76:79]\nds_read_b128 v[52:55], v35 offset:0\ns_mov_b32 s52, s47\n"
"s_add_u32 s47, s51, s32\ns_add_u32 s51, s51, s54\ns_add_u32 s49, s49, 28672\ns_waitcnt vmcnt(7)\n"
"s_barrier\ns_waitcnt lgkmcnt(3)\nv_mfma_f32_16x16x32_bf16 v[96:99], v[128:131], v[40:43], 0\nv_mov_b32 v36, s48\n"
"s_add_u32 s48, s48, 1\ns_nop 0\nbuffer_store_dword v36, v19, s[24:27], s33 offen sc1\nbuffer_load_dword v37, v20, s[24:27], 0 offen sc1\n"
"v_mfma_f32_16x16x32_bf16 v[100:103], v[132:135], v[40:43], 0\nds_read_b128 v[40:43], v32 offset:4096\ns_waitcnt lgkmcnt(3)\nv_mfma_f32_16x16x32_bf16 v[104:107], v[128:131], v[44:47], 0\n"
"v_mfma_f32_16x16x32_bf16 v[108:111], v[132:135], v[44:47], 0\nds_read_b128 v[44:47], v33 offset:4096\nv_cvt_pk_bf16_f32 v56, v64, v65\ns_waitcnt lgkmcnt(3)\n"
"v_mfma_f32_16x16x32_bf16 v[96:99], v[136:139], v[48:51], v[96:99]\nv_cvt_pk_bf16_f32 v57, v66, v67\nv_mfma_f32_16x16x32_bf16 v[100:103], v[140:143], v[48:51], v[100:103]\nds_read_b128 v[48:51], v34 offset:4096\n"
"v_cvt_pk_bf16_f32 v58, v68, v69\ns_waitcnt lgkmcnt(3)\nv_mfma_f32_16x16x32_bf16 v[104:107], v[136:139], v[52:55], v[104:107]\nv_cvt_pk_bf16_f32 v59, v70, v71\n"
"v_mfma_f32_16x16x32_bf16 v[108:111], v[140:143], v[52:55], v[108:111]\nds_read_b128 v[52:55], v35 offset:4096\ns_waitcnt lgkmcnt(3)\nv_mfma_f32_16x16x32_bf16 v[96:99], v[144:147], v[40:43], v[96:99]\n"
"v_mfma_f32_16x16x32_bf16 v[100:103], v[148:151], v[40:43], v[100:103]\nds_read_b128 v[40:43], v32 offset:8192\nbuffer_store_dwordx4 v[56:59], v15, s[20:23], s50 offen sc1\ns_waitcnt lgkmcnt(3)\n"
"v_mfma_f32_16x16x32_bf16 v[104:107], v[144:147], v[44:47], v[104:107]\nv_cvt_pk_bf16_f32 v60, v72, v73\nv_mfma_f32_16x16x32_bf16 v[108:111], v[148:151], v[44:47], v[108:111]\nds_read_b128 v[44:47], v33 offset:8192\n"
"v_cvt_pk_bf16_f32 v61, v74, v75\ns_waitcnt lgkmcnt(3)\nv_mfma_f32_16x16x32_bf16 v[96:99], v[152:155], v[48:51], v[96:99]\nv_cvt_pk_bf16_f32 v62, v76, v77\n"
"v_mfma_f32_16x16x32_bf16 v[100:103], v[156:159], v[48:51], v[100:103]\nds_read_b128 v[48:51], v34 offset:8192\nv_cvt_pk_bf16_f32 v63, v78, v79\ns_waitcnt lgkmcnt(3)\n"
"v_mfma_f32_16x16x32_bf16 v[104:107], v[152:155], v[52:55], v[104:107]\nv_mfma_f32_16x16x32_bf16 v[108:111], v[156:159], v[52:55], v[108:111]\nds_read_b128 v[52:55], v35 offset:8192\ns_waitcnt lgkmcnt(3)\n"
"v_mfma_f32_16x16x32_bf16 v[96:99], v[160:163], v[40:43], v[96:99]\nbuffer_store_dwordx4 v[60:63], v16, s[20:23], s50 offen sc1\nv_mfma_f32_16x16x32_bf16 v[100:103], v[164:167], v[40:43], v[100:103]\nds_read_b128 v[40:43], v32 offset:12288\n"
"s_add_u32 s50, s50, s36\ns_waitcnt lgkmcnt(3)\nv_mfma_f32_16x16x32_bf16 v[104:107], v[160:163], v[44:47], v[104:107]\ns_add_u32 m0, s52, 0\n"
"v_mfma_f32_16x16x32_bf16 v[108:111], v[164:167], v[44:47], v[108:111]\nds_read_b128 v[44:47], v33 offset:12288\ns_add_u32 s53, s49, 0\ns_waitcnt lgkmcnt(3)\n"
"v_mfma_f32_16x16x32_bf16 v[96:99], v[168:171], v[48:51], v[96:99]\nbuffer_load_dwordx4 v10, s[12:15], s53 offen lds\nv_mfma_f32_16x16x32_bf16 v[100:103], v[172:175], v[48:51], v[100:103]\nds_read_b128 v[48:51], v34 offset:12288\n"
"s_add_u32 m0, s52, 4096\ns_waitcnt lgkmcnt(3)\nv_mfma_f32_16x16x32_bf16 v[104:107], v[168:171], v[52:55], v[104:107]\ns_add_u32 s53, s49, 128\n"
"v_mfma_f32_16x16x32_bf16 v[108:111], v[172:175], v[52:55], v[108:111]\nds_read_b128 v[52:55], v35 offset:12288\nbuffer_load_dwordx4 v10, s[12:15], s53 offen lds\ns_waitcnt lgkmcnt(3)\n"
"v_mfma_f32_16x16x32_bf16 v[96:99], v[176:179], v[40:43], v[96:99]\ns_add_u32 m0, s52, 8192\nv_mfma_f32_16x16x32_bf16 v[100:103], v[180:183], v[40:43], v[100:103]\nds_read_b128 v[40:43], v32 offset:16384\n"
"s_add_u32 s53, s49, 256\ns_waitcnt lgkmcnt(3)\nv_mfma_f32_16x16x32_bf16 v[104:107], v[176:179], v[44:47], v[104:107]\nbuffer_load_dwordx4 v10, s[12:15], s53 offen lds\n"
"v_mfma_f32_16x16x32_bf16 v[108:111], v[180:183], v[44:47], v[108:111]\nds_read_b128 v[44:47], v33 offset:16384\ns_add_u32 m0, s52, 12288\ns_waitcnt lgkmcnt(3)\n"
"v_mfma_f32_16x16x32_bf16 v[96:99], v[184:187], v[48:51], v[96:99]\ns_add_u32 s53, s49, 384\nv_mfma_f32_16x16x32_bf16 v[100:103], v[188:191], v[48:51], v[100:103]\nds_read_b128 v[48:51], v34 offset:16384\n"
"buffer_load_dwordx4 v10, s[12:15], s53 offen lds\ns_waitcnt lgkmcnt(3)\nv_mfma_f32_16x16x32_bf16 v[104:107], v[184:187], v[52:55], v[104:107]\ns_add_u32 m0, s52, 16384\n"
"v_mfma_f32_16x16x32_bf16 v[108:111], v[188:191], v[52:55], v[108:111]\nds_read_b128 v[52:55], v35 offset:16384\ns_add_u32 s53, s49, 512\ns_waitcnt lgkmcnt(3)\n"
"v_mfma_f32_16x16x32_bf16 v[96:99], v[192:195], v[40:43], v[96:99]\nbuffer_load_dwordx4 v10, s[12:15], s53 offen lds\nv_mfma_f32_16x16x32_bf16 v[100:103], v[196:199], v[40:43], v[100:103]\nds_read_b128 v[40:43], v32 offset:20480\n"
"s_add_u32 m0, s52, 20480\ns_waitcnt lgkmcnt(3)\nv_mfma_f32_16x16x32_bf16 v[104:107], v[192:195], v[44:47], v[104:107]\ns_add_u32 s53, s49, 640\n"
"v_mfma_f32_16x16x32_bf16 v[108:111], v[196:199], v[44:47], v[108:111]\nds_read_b128 v[44:47], v33 offset:20480\nbuffer_load_dwordx4 v10, s[12:15], s53 offen lds\ns_waitcnt lgkmcnt(3)\n"
"v_mfma_f32_16x16x32_bf16 v[96:99], v[200:203], v[48:51], v[96:99]\ns_add_u32 m0, s52, 24576\nv_mfma_f32_16x16x32_bf16 v[100:103], v[204:207], v[48:51], v[100:103]\nds_read_b128 v[48:51], v34 offset:20480\n"
"s_add_u32 s53, s49, 768\ns_waitcnt lgkmcnt(3)\nv_mfma_f32_16x16x32_bf16 v[104:107], v[200:203], v[52:55], v[104:107]\nbuffer_load_dwordx4 v10, s[12:15], s53 offen lds\n"
"v_mfma_f32_16x16x32_bf16 v[108:111], v[204:207], v[52:55], v[108:111]\nds_read_b128 v[52:55], v35 offset:20480\ns_waitcnt lgkmcnt(3)\nv_mfma_f32_16x16x32_bf16 v[96:99], v[208:211], v[40:43], v[96:99]\n"
"v_mfma_f32_16x16x32_bf16 v[100:103], v[212:215], v[40:43], v[100:103]\nds_read_b128 v[40:43], v32 offset:24576\ns_waitcnt lgkmcnt(3)\nv_mfma_f32_16x16x32_bf16 v[104:107], v[208:211], v[44:47], v[104:107]\n"
"v_mfma_f32_16x16x32_bf16 v[108:111], v[212:215], v[44:47], v[108:111]\nds_read_b128 v[44:47], v33 offset:24576\ns_waitcnt lgkmcnt(3)\nv_mfma_f32_16x16x32_bf16 v[96:99], v[216:219], v[48:51], v[96:99]\n"
"s_add_u32 s54, s51, 32768\ns_cmp_eq_u32 s54, 163840\nv_mfma_f32_16x16x32_bf16 v[100:103], v[220:223], v[48:51], v[100:103]\ns_cselect_b32 s54, 0, s54\n"
"s_sub_u32 s54, s54, s51\nds_read_b128 v[48:51], v34 offset:24576\ns_waitcnt lgkmcnt(3)\nv_mfma_f32_16x16x32_bf16 v[104:107], v[216:219], v[52:55], v[104:107]\n"
"v_mfma_f32_16x16x32_bf16 v[108:111], v[220:223], v[52:55], v[108:111]\nds_read_b128 v[52:55], v35 offset:24576\ns_waitcnt lgkmcnt(3)\nv_mfma_f32_16x16x32_bf16 v[96:99], v[224:227], v[40:43], v[96:99]\n"
"v_add_u32 v32, s54, v32\nv_mfma_f32_16x16x32_bf16 v[100:103], v[228:231], v[40:43], v[100:103]\nds_read_b128 v[40:43], v32 offset:0\ns_waitcnt lgkmcnt(3)\n"
"v_mfma_f32_16x16x32_bf16 v[104:107], v[224:227], v[44:47], v[104:107]\nv_add_u32 v33, s54, v33\nv_mfma_f32_16x16x32_bf16 v[108:111], v[228:231], v[44:47], v[108:111]\nds_read_b128 v[44:47], v33 offset:0\n"
"s_waitcnt lgkmcnt(3)\nv_mfma_f32_16x16x32_bf16 v[96:99], v[232:235], v[48:51], v[96:99]\nv_add_u32 v34, s54, v34\nv_mfma_f32_16x16x32_bf16 v[100:103], v[236:239], v[48:51], v[100:103]\n"
"ds_read_b128 v[48:51], v34 offset:0\ns_waitcnt lgkmcnt(3)\nv_mfma_f32_16x16x32_bf16 v[104:107], v[232:235], v[52:55], v[104:107]\nv_add_u32 v35, s54, v35\n"
"v_mfma_f32_16x16x32_bf16 v[108:111], v[236:239], v[52:55], v[108:111]\nds_read_b128 v[52:55], v35 offset:0\ns_mov_b32 s52, s47\ns_add_u32 s47, s51, s32\n"
"s_add_u32 s51, s51, s54\ns_add_u32 s49, s49, 28672\ns_waitcnt vmcnt(7)\nfg0_pf:\n"
"v_cmp_ne_u32 vcc, s28, v37\ns_cbranch_vccz fg0_pfok\ns_add_u32 s55, s55, 1\ns_cmp_gt_u32 s55, 262144\n"
"s_cbranch_scc0 fg0_pf2\ns_endpgm\nfg0_pf2:\ns_sleep 1\n"
"buffer_load_dword v37, v20, s[24:27], 0 offen sc1\ns_waitcnt vmcnt(0)\ns_branch fg0_pf\nfg0_pfok:\n"
"s_barrier\ns_waitcnt lgkmcnt(3)\nv_mfma_f32_16x16x32_bf16 v[64:67], v[128:131], v[40:43], 0\nv_mov_b32 v36, s48\n"
"s_add_u32 s48, s48, 1\ns_nop 0\nbuffer_store_dword v36, v19, s[24:27], s33 offen sc1\nv_mfma_f32_16x16x32_bf16 v[68:71], v[132:135], v[40:43], 0\n"
"ds_read_b128 v[40:43], v32 offset:4096\ns_waitcnt lgkmcnt(3)\nv_mfma_f32_16x16x32_bf16 v[72:75], v[128:131], v[44:47], 0\nv_mfma_f32_16x16x32_bf16 v[76:79], v[132:135], v[44:47], 0\n"
"ds_read_b128 v[44:47], v33 offset:4096\nv_cvt_pk_bf16_f32 v56, v96, v97\ns_waitcnt lgkmcnt(3)\nv_mfma_f32_16x16x32_bf16 v[64:67], v[136:139], v[48:51], v[64:67]\n"
"v_cvt_pk_bf16_f32 v57, v98, v99\nv_mfma_f32_16x16x32_bf16 v[68:71], v[140:143], v[48:51], v[68:71]\nds_read_b128 v[48:51], v34 offset:4096\nv_cvt_pk_bf16_f32 v58, v100, v101\n"
"s_waitcnt lgkmcnt(3)\nv_mfma_f32_16x16x32_bf16 v[72:75], v[136:139], v[52:55], v[72:75]\nv_cvt_pk_bf16_f32 v59, v102, v103\nv_mfma_f32_16x16x32_bf16 v[76:79], v[140:143], v[52:55], v[76:79]\n"
"ds_read_b128 v[52:55], v35 offset:4096\ns_waitcnt lgkmcnt(3)\nv_mfma_f32_16x16x32_bf16 v[64:67], v[144:147], v[40:43], v[64:67]\nv_mfma_f32_16x16x32_bf16 v[68:71], v[148:151], v[40:43], v[68:71]\n"
"ds_read_b128 v[40:43], v32 offset:8192\nbuffer_store_dwordx4 v[56:59], v15, s[20:23], s50 offen sc1\ns_waitcnt lgkmcnt(3)\nv_mfma_f32_16x16x32_bf16 v[72:75], v[144:147], v[44:47], v[72:75]\n"
"v_cvt_pk_bf16_f32 v60, v104, v105\nv_mfma_f32_16x16x32_bf16 v[76:79], v[148:151], v[44:47], v[76:79]\nds_read_b128 v[44:47], v33 offset:8192\nv_cvt_pk_bf16_f32 v61, v106, v107\n"
"s_waitcnt lgkmcnt(3)\nv_mfma_f32_16x16x32_bf16 v[64:67], v[152:155], v[48:51], v[64:67]\nv_cvt_pk_bf16_f32 v62, v108, v109\nv_mfma_f32_16x16x32_bf16 v[68:71], v[156:159], v[48:51], v[68:71]\n"
"ds_read_b128 v[48:51], v34 offset:8192\nv_cvt_pk_bf16_f32 v63, v110, v111\ns_waitcnt lgkmcnt(3)\nv_mfma_f32_16x16x32_bf16 v[72:75], v[152:155], v[52:55], v[72:75]\n"
"v_mfma_f32_16x16x32_bf16 v[76:79], v[156:159], v[52:55], v[76:79]\nds_read_b128 v[52:55], v35 offset:8192\ns_waitcnt lgkmcnt(3)\nv_mfma_f32_16x16x32_bf16 v[64:67], v[160:163], v[40:43], v[64:67]\n"
"buffer_store_dwordx4 v[60:63], v16, s[20:23], s50 offen sc1\nv_mfma_f32_16x16x32_bf16 v[68:71], v[164:167], v[40:43], v[68:71]\nds_read_b128 v[40:43], v32 offset:12288\ns_add_u32 s50, s50, s36\n"
"s_waitcnt lgkmcnt(3)\nv_mfma_f32_16x16x32_bf16 v[72:75], v[160:163], v[44:47], v[72:75]\ns_add_u32 m0, s52, 0\nv_mfma_f32_16x16x32_bf16 v[76:79], v[164:167], v[44:47], v[76:79]\n"
"ds_read_b128 v[44:47], v33 offset:12288\ns_add_u32 s53, s49, 0\ns_waitcnt lgkmcnt(3)\nv_mfma_f32_16x16x32_bf16 v[64:67], v[168:171], v[48:51], v[64:67]\n"
"buffer_load_dwordx4 v10, s[12:15], s53 offen lds\nv_mfma_f32_16x16x32_bf16 v[68:71], v[172:175], v[48:51], v[68:71]\nds_read_b128 v[48:51], v34 offset:12288\ns_add_u32 m0, s52, 4096\n"
"s_waitcnt lgkmcnt(3)\nv_mfma_f32_16x16x32_bf16 v[72:75], v[168:171], v[52:55], v[72:75]\ns_add_u32 s53, s49, 128\nv_mfma_f32_16x16x32_bf16 v[76:79], v[172:175], v[52:55], v[76:79]\n"
"ds_read_b128 v[52:55], v35 offset:12288\nbuffer_load_dwordx4 v10, s[12:15], s53 offen lds\ns_waitcnt lgkmcnt(3)\nv_mfma_f32_16x16x32_bf16 v[64:67], v[176:179], v[40:43], v[64:67]\n"
"s_add_u32 m0, s52, 8192\nv_mfma_f32_16x16x32_bf16 v[68:71], v[180:183], v[40:43], v[68:71]\nds_read_b128 v[40:43], v32 offset:16384\ns_add_u32 s53, s49, 256\n"
"s_waitcnt lgkmcnt(3)\nv_mfma_f32_16x16x32_bf16 v[72:75], v[176:179], v[44:47], v[72:75]\nbuffer_load_dwordx4 v10, s[12:15], s53 offen lds\nv_mfma_f32_16x16x32_bf16 v[76:79], v[180:183], v[44:47], v[76:79]\n"
"ds_read_b128 v[44:47], v33 offset:16384\ns_add_u32 m0, s52, 12288\ns_waitcnt lgkmcnt(3)\nv_mfma_f32_16x16x32_bf16 v[64:67], v[184:187], v[48:51], v[64:67]\n"
"s_add_u32 s53, s49, 384\nv_mfma_f32_16x16x32_bf16 v[68:71], v[188:191], v[48:51], v[68:71]\nds_read_b128 v[48:51], v34 offset:16384\nbuffer_load_dwordx4 v10, s[12:15], s53 offen lds\n"
"s_waitcnt lgkmcnt(3)\nv_mfma_f32_16x16x32_bf16 v[72:75], v[184:187], v[52:55], v[72:75]\ns_add_u32 m0, s52, 16384\nv_mfma_f32_16x16x32_bf16 v[76:79], v[188:191], v[52:55], v[76:79]\n"
"ds_read_b128 v[52:55], v35 offset:16384\ns_add_u32 s53, s49, 512\ns_waitcnt lgkmcnt(3)\nv_mfma_f32_16x16x32_bf16 v[64:67], v[192:195], v[40:43], v[64:67]\n"
"buffer_load_dwordx4 v10, s[12:15], s53 offen lds\nv_mfma_f32_16x16x32_bf16 v[68:71], v[196:199], v[40:43], v[68:71]\nds_read_b128 v[40:43], v32 offset:20480\ns_add_u32 m0, s52, 20480\n"
"s_waitcnt lgkmcnt(3)\nv_mfma_f32_16x16x32_bf16 v[72:75], v[192:195], v[44:47], v[72:75]\ns_add_u32 s53, s49, 640\nv_mfma_f32_16x16x32_bf16 v[76:79], v[196:199], v[44:47], v[76:79]\n"
"ds_read_b128 v[44:47], v33 offset:20480\nbuffer_load_dwordx4 v10, s[12:15], s53 offen lds\ns_waitcnt lgkmcnt(3)\nv_mfma_f32_16x16x32_bf16 v[64:67], v[200:203], v[48:51], v[64:67]\n"
"s_add_u32 m0, s52, 24576\nv_mfma_f32_16x16x32_bf16 v[68:71], v[204:207], v[48:51], v[68:71]\nds_read_b128 v[48:51], v34 offset:20480\ns_add_u32 s53, s49, 768\n"
"s_waitcnt lgkmcnt(3)\nv_mfma_f32_16x16x32_bf16 v[72:75], v[200:203], v[52:55], v[72:75]\nbuffer_load_dwordx4 v10, s[12:15], s53 offen lds\nv_mfma_f32_16x16x32_bf16 v[76:79], v[204:207], v[52:55], v[76:79]\n"
"ds_read_b128 v[52:55], v35 offset:20480\ns_mov_b32 s53, 0\nbuffer_load_dwordx4 a[0:3], v14, s[16:19], s53 offen sc1\ns_waitcnt lgkmcnt(3)\n"
"v_mfma_f32_16x16x32_bf16 v[64:67], v[208:211], v[40:43], v[64:67]\ns_mov_b32 s53, 14336\nbuffer_load_dwordx4 a[4:7], v14, s[16:19], s53 offen sc1\nv_mfma_f32_16x16x32_bf16 v[68:71], v[212:215], v[40:43], v[68:71]\n"
"ds_read_b128 v[40:43], v32 offset:24576\ns_mov_b32 s53, 1024\nbuffer_load_dwordx4 a[8:11], v14, s[16:19], s53 offen sc1\ns_waitcnt lgkmcnt(3)\n"
"v_mfma_f32_16x16x32_bf16 v[72:75], v[208:211], v[44:47], v[72:75]\ns_mov_b32 s53, 15360\nbuffer_load_dwordx4 a[12:15], v14, s[16:19], s53 offen sc1\nv_mfma_f32_16x16x32_bf16 v[76:79], v[212:215], v[44:47], v[76:79]\n"
"ds_read_b128 v[44:47], v33 offset:24576\ns_mov_b32 s53, 2048\nbuffer_load_dwordx4 a[16:19], v14, s[16:19], s53 offen sc1\ns_waitcnt lgkmcnt(3)\n"
"v_mfma_f32_16x16x32_bf16 v[64:67], v[216:219], v[48:51], v[64:67]\ns_add_u32 s54, s51, 32768\ns_cmp_eq_u32 s54, 163840\nv_mfma_f32_16x16x32_bf16 v[68:71], v[220:223], v[48:51], v[68:71]\n"
"s_cselect_b32 s54, 0, s54\ns_sub_u32 s54, s54, s51\nds_read_b128 v[48:51], v34 offset:24576\ns_waitcnt lgkmcnt(3)\n"
"v_mfma_f32_16x16x32_bf16 v[72:75], v[216:219], v[52:55], v[72:75]\nv_mfma_f32_16x16x32_bf16 v[76:79], v[220:223], v[52:55], v[76:79]\nds_read_b128 v[52:55], v35 offset:24576\ns_waitcnt lgkmcnt(3)\n"
"v_mfma_f32_16x16x32_bf16 v[64:67], v[224:227], v[40:43], v[64:67]\nv_add_u32 v32, s54, v32\nv_mfma_f32_16x16x32_bf16 v[68:71], v[228:231], v[40:43], v[68:71]\nds_read_b128 v[40:43], v32 offset:0\n"
"s_waitcnt lgkmcnt(3)\nv_mfma_f32_16x16x32_bf16 v[72:75], v[224:227], v[44:47], v[72:75]\nv_add_u32 v33, s54, v33\nv_mfma_f32_16x16x32_bf16 v[76:79], v[228:231], v[44:47], v[76:79]\n"
"ds_read_b128 v[44:47], v33 offset:0\ns_waitcnt lgkmcnt(3)\nv_mfma_f32_16x16x32_bf16 v[64:67], v[232:235], v[48:51], v[64:67]\nv_add_u32 v34, s54, v34\n"
"v_mfma_f32_16x16x32_bf16 v[68:71], v[236:239], v[48:51], v[68:71]\nds_read_b128 v[48:51], v34 offset:0\ns_waitcnt lgkmcnt(3)\nv_mfma_f32_16x16x32_bf16 v[72:75], v[232:235], v[52:55], v[72:75]\n"
"v_add_u32 v35, s54, v35\nv_mfma_f32_16x16x32_bf16 v[76:79], v[236:239], v[52:55], v[76:79]\nds_read_b128 v[52:55], v35 offset:0\ns_mov_b32 s52, s47\n"
"s_add_u32 s47, s51, s32\ns_add_u32 s51, s51, s54\ns_add_u32 s49, s49, 28672\ns_waitcnt vmcnt(12)\n"
"s_barrier\ns_waitcnt lgkmcnt(3)\nv_mfma_f32_16x16x32_bf16 v[96:99], v[128:131], v[40:43], 0\nv_mov_b32 v36, s48\n"
"s_add_u32 s48, s48, 1\ns_nop 0\nbuffer_store_dword v36, v19, s[24:27], s33 offen sc1\nv_mfma_f32_16x16x32_bf16 v[100:103], v[132:135], v[40:43], 0\n"
"ds_read_b128 v[40:43], v32 offset:4096\ns_waitcnt lgkmcnt(3)\nv_mfma_f32_16x16x32_bf16 v[104:107], v[128:131], v[44:47], 0\nv_mfma_f32_16x16x32_bf16 v[108:111], v[132:135], v[44:47], 0\n"
"ds_read_b128 v[44:47], v33 offset:4096\nv_cvt_pk_bf16_f32 v56, v64, v65\ns_waitcnt lgkmcnt(3)\nv_mfma_f32_16x16x32_bf16 v[96:99], v[136:139], v[48:51], v[96:99]\n"
"v_cvt_pk_bf16_f32 v57, v66, v67\nv_mfma_f32_16x16x32_bf16 v[100:103], v[140:143], v[48:51], v[100:103]\nds_read_b128 v[48:51], v34 offset:4096\nv_cvt_pk_bf16_f32 v58, v68, v69\n"
"s_waitcnt lgkmcnt(3)\nv_mfma_f32_16x16x32_bf16 v[104:107], v[136:139], v[52:55], v[104:107]\nv_cvt_pk_bf16_f32 v59, v70, v71\nv_mfma_f32_16x16x32_bf16 v[108:111], v[140:143], v[52:55], v[108:111]\n"
"ds_read_b128 v[52:55], v35 offset:4096\ns_waitcnt lgkmcnt(3)\nv_mfma_f32_16x16x32_bf16 v[96:99], v[144:147], v[40:43], v[96:99]\nv_mfma_f32_16x16x32_bf16 v[100:103], v[148:151], v[40:43], v[100:103]\n"
"ds_read_b128 v[40:43], v32 offset:8192\nbuffer_store_dwordx4 v[56:59], v15, s[20:23], s50 offen sc1\ns_waitcnt lgkmcnt(3)\nv_mfma_f32_16x16x32_bf16 v[104:107], v[144:147], v[44:47], v[104:107]\n"
"v_cvt_pk_bf16_f32 v60, v72, v73\nv_mfma_f32_16x16x32_bf16 v[108:111], v[148:151], v[44:47], v[108:111]\nds_read_b128 v[44:47], v33 offset:8192\nv_cvt_pk_bf16_f32 v61, v74, v75\n"
"s_waitcnt lgkmcnt(3)\nv_mfma_f32_16x16x32_bf16 v[96:99], v[152:155], v[48:51], v[96:99]\nv_cvt_pk_bf16_f32 v62, v76, v77\nv_mfma_f32_16x16x32_bf16 v[100:103], v[156:159], v[48:51], v[100:103]\n"
"ds_read_b128 v[48:51], v34 offset:8192\nv_cvt_pk_bf16_f32 v63, v78, v79\ns_waitcnt lgkmcnt(3)\nv_mfma_f32_16x16x32_bf16 v[104:107], v[152:155], v[52:55], v[104:107]\n"
"v_mfma_f32_16x16x32_bf16 v[108:111], v[156:159], v[52:55], v[108:111]\nds_read_b128 v[52:55], v35 offset:8192\ns_waitcnt lgkmcnt(3)\nv_mfma_f32_16x16x32_bf16 v[96:99], v[160:163], v[40:43], v[96:99]\n"
"buffer_store_dwordx4 v[60:63], v16, s[20:23], s50 offen sc1\nv_mfma_f32_16x16x32_bf16 v[100:103], v[164:167], v[40:43], v[100:103]\nds_read_b128 v[40:43], v32 offset:12288\ns_add_u32 s50, s50, s36\n"
"s_waitcnt lgkmcnt(3)\nv_mfma_f32_16x16x32_bf16 v[104:107], v[160:163], v[44:47], v[104:107]\ns_add_u32 m0, s52, 0\nv_mfma_f32_16x16x32_bf16 v[108:111], v[164:167], v[44:47], v[108:111]\n"
"ds_read_b128 v[44:47], v33 offset:12288\ns_add_u32 s53, s49, 0\ns_waitcnt lgkmcnt(3)\nv_mfma_f32_16x16x32_bf16 v[96:99], v[168:171], v[48:51], v[96:99]\n"
"buffer_load_dwordx4 v10, s[12:15], s53 offen lds\nv_mfma_f32_16x16x32_bf16 v[100:103], v[172:175], v[48:51], v[100:103]\nds_read_b128 v[48:51], v34 offset:12288\ns_add_u32 m0, s52, 4096\n"
"s_waitcnt lgkmcnt(3)\nv_mfma_f32_16x16x32_bf16 v[104:107], v[168:171], v[52:55], v[104:107]\ns_add_u32 s53, s49, 128\nv_mfma_f32_16x16x32_bf16 v[108:111], v[172:175], v[52:55], v[108:111]\n"
"ds_read_b128 v[52:55], v35 offset:12288\nbuffer_load_dwordx4 v10, s[12:15], s53 offen lds\ns_waitcnt lgkmcnt(3)\nv_mfma_f32_16x16x32_bf16 v[96:99], v[176:179], v[40:43], v[96:99]\n"
"s_add_u32 m0, s52, 8192\nv_mfma_f32_16x16x32_bf16 v[100:103], v[180:183], v[40:43], v[100:103]\nds_read_b128 v[40:43], v32 offset:16384\ns_add_u32 s53, s49, 256\n"
"s_waitcnt lgkmcnt(3)\nv_mfma_f32_16x16x32_bf16 v[104:107], v[176:179], v[44:47], v[104:107]\nbuffer_load_dwordx4 v10, s[12:15], s53 offen lds\nv_mfma_f32_16x16x32_bf16 v[108:111], v[180:183], v[44:47], v[108:111]\n"
"ds_read_b128 v[44:47], v33 offset:16384\ns_add_u32 m0, s52, 12288\ns_waitcnt lgkmcnt(3)\nv_mfma_f32_16x16x32_bf16 v[96:99], v[184:187], v[48:51], v[96:99]\n"
"s_add_u32 s53, s49, 384\nv_mfma_f32_16x16x32_bf16 v[100:103], v[188:191], v[48:51], v[100:103]\nds_read_b128 v[48:51], v34 offset:16384\nbuffer_load_dwordx4 v10, s[12:15], s53 offen lds\n"
"s_waitcnt lgkmcnt(3)\nv_mfma_f32_16x16x32_bf16 v[104:107], v[184:187], v[52:55], v[104:107]\ns_add_u32 m0, s52, 16384\nv_mfma_f32_16x16x32_bf16 v[108:111], v[188:191], v[52:55], v[108:111]\n"
"ds_read_b128 v[52:55], v35 offset:16384\ns_add_u32 s53, s49, 512\ns_waitcnt lgkmcnt(3)\nv_mfma_f32_16x16x32_bf16 v[96:99], v[192:195], v[40:43], v[96:99]\n"
"buffer_load_dwordx4 v10, s[12:15], s53 offen lds\nv_mfma_f32_16x16x32_bf16 v[100:103], v[196:199], v[40:43], v[100:103]\nds_read_b128 v[40:43], v32 offset:20480\ns_add_u32 m0, s52, 20480\n"
"s_waitcnt lgkmcnt(3)\nv_mfma_f32_16x16x32_bf16 v[104:107], v[192:195], v[44:47], v[104:107]\ns_add_u32 s53, s49, 640\nv_mfma_f32_16x16x32_bf16 v[108:111], v[196:199], v[44:47], v[108:111]\n"
"ds_read_b128 v[44:47], v33 offset:20480\nbuffer_load_dwordx4 v10, s[12:15], s53 offen lds\ns_waitcnt lgkmcnt(3)\nv_mfma_f32_16x16x32_bf16 v[96:99], v[200:203], v[48:51], v[96:99]\n"
"s_add_u32 m0, s52, 24576\nv_mfma_f32_16x16x32_bf16 v[100:103], v[204:207], v[48:51], v[100:103]\nds_read_b128 v[48:51], v34 offset:20480\ns_add_u32 s53, s49, 768\n"
"s_waitcnt lgkmcnt(3)\nv_mfma_f32_16x16x32_bf16 v[104:107], v[200:203], v[52:55], v[104:107]\nbuffer_load_dwordx4 v10, s[12:15], s53 offen lds\nv_mfma_f32_16x16x32_bf16 v[108:111], v[204:207], v[52:55], v[108:111]\n"
"ds_read_b128 v[52:55], v35 offset:20480\ns_mov_b32 s53, 16384\nbuffer_load_dwordx4 a[20:23], v14, s[16:19], s53 offen sc1\ns_waitcnt lgkmcnt(3)\n"
"v_mfma_f32_16x16x32_bf16 v[96:99], v[208:211], v[40:43], v[96:99]\ns_mov_b32 s53, 3072\nbuffer_load_dwordx4 a[24:27], v14, s[16:19], s53 offen sc1\nv_mfma_f32_16x16x32_bf16 v[100:103], v[212:215], v[40:43], v[100:103]\n"
"ds_read_b128 v[40:43], v32 offset:24576\ns_mov_b32 s53, 17408\nbuffer_load_dwordx4 a[28:31], v14, s[16:19], s53 offen sc1\ns_waitcnt lgkmcnt(3)\n"
"v_mfma_f32_16x16x32_bf16 v[104:107], v[208:211], v[44:47], v[104:107]\ns_mov_b32 s53, 4096\nbuffer_load_dwordx4 a[32:35], v14, s[16:19], s53 offen sc1\nv_mfma_f32_16x16x32_bf16 v[108:111], v[212:215], v[44:47], v[108:111]\n"
"ds_read_b128 v[44:47], v33 offset:24576\ns_mov_b32 s53, 18432\nbuffer_load_dwordx4 a[36:39], v14, s[16:19], s53 offen sc1\ns_waitcnt lgkmcnt(3)\n"
"v_mfma_f32_16x16x32_bf16 v[96:99], v[216:219], v[48:51], v[96:99]\ns_add_u32 s54, s51, 32768\ns_cmp_eq_u32 s54, 163840\nv_mfma_f32_16x16x32_bf16 v[100:103], v[220:223], v[48:51], v[100:103]\n"
"s_cselect_b32 s54, 0, s54\ns_sub_u32 s54, s54, s51\nds_read_b128 v[48:51], v34 offset:24576\ns_waitcnt lgkmcnt(3)\n"
"v_mfma_f32_16x16x32_bf16 v[104:107], v[216:219], v[52:55], v[104:107]\nv_mfma_f32_16x16x32_bf16 v[108:111], v[220:223], v[52:55], v[108:111]\nds_read_b128 v[52:55], v35 offset:24576\ns_waitcnt lgkmcnt(3)\n"
"v_mfma_f32_16x16x32_bf16 v[96:99], v[224:227], v[40:43], v[96:99]\nv_add_u32 v32, s54, v32\nv_mfma_f32_16x16x32_bf16 v[100:103], v[228:231], v[40:43], v[100:103]\nds_read_b128 v[40:43], v32 offset:0\n"
"s_waitcnt lgkmcnt(3)\nv_mfma_f32_16x16x32_bf16 v[104:107], v[224:227], v[44:47], v[104:107]\nv_add_u32 v33, s54, v33\nv_mfma_f32_16x16x32_bf16 v[108:111], v[228:231], v[44:47], v[108:111]\n"
"ds_read_b128 v[44:47], v33 offset:0\ns_waitcnt lgkmcnt(3)\nv_mfma_f32_16x16x32_bf16 v[96:99], v[232:235], v[48:51], v[96:99]\nv_add_u32 v34, s54, v34\n"
"v_mfma_f32_16x16x32_bf16 v[100:103], v[236:239], v[48:51], v[100:103]\nds_read_b128 v[48:51], v34 offset:0\ns_waitcnt lgkmcnt(3)\nv_mfma_f32_16x16x32_bf16 v[104:107], v[232:235], v[52:55], v[104:107]\n"
"v_add_u32 v35, s54, v35\nv_mfma_f32_16x16x32_bf16 v[108:111], v[236:239], v[52:55], v[108:111]\nds_read_b128 v[52:55], v35 offset:0\ns_mov_b32 s52, s47\n"
"s_add_u32 s47, s51, s32\ns_add_u32 s51, s51, s54\ns_add_u32 s49, s49, 28672\ns_waitcnt vmcnt(12)\n"
"s_barrier\ns_waitcnt lgkmcnt(3)\nv_mfma_f32_16x16x32_bf16 v[64:67], v[128:131], v[40:43], 0\nv_mov_b32 v36, s48\n"
"s_add_u32 s48, s48, 1\ns_nop 0\nbuffer_store_dword v36, v19, s[24:27], s33 offen sc1\nv_mfma_f32_16x16x32_bf16 v[68:71], v[132:135], v[40:43], 0\n"
"ds_read_b128 v[40:43], v32 offset:4096\ns_waitcnt lgkmcnt(3)\nv_mfma_f32_16x16x32_bf16 v[72:75], v[128:131], v[44:47], 0\nv_mfma_f32_16x16x32_bf16 v[76:79], v[132:135], v[44:47], 0\n"
"ds_read_b128 v[44:47], v33 offset:4096\nv_cvt_pk_bf16_f32 v56, v96, v97\ns_waitcnt lgkmcnt(3)\nv_mfma_f32_16x16x32_bf16 v[64:67], v[136:139], v[48:51], v[64:67]\n"
"v_cvt_pk_bf16_f32 v57, v98, v99\nv_mfma_f32_16x16x32_bf16 v[68:71], v[140:143], v[48:51], v[68:71]\nds_read_b128 v[48:51], v34 offset:4096\nv_cvt_pk_bf16_f32 v58, v100, v101\n"
"s_waitcnt lgkmcnt(3)\nv_mfma_f32_16x16x32_bf16 v[72:75], v[136:139], v[52:55], v[72:75]\nv_cvt_pk_bf16_f32 v59, v102, v103\nv_mfma_f32_16x16x32_bf16 v[76:79], v[140:143], v[52:55], v[76:79]\n"
"ds_read_b128 v[52:55], v35 offset:4096\ns_waitcnt lgkmcnt(3)\nv_mfma_f32_16x16x32_bf16 v[64:67], v[144:147], v[40:43], v[64:67]\nv_mfma_f32_16x16x32_bf16 v[68:71], v[148:151], v[40:43], v[68:71]\n"
"ds_read_b128 v[40:43], v32 offset:8192\nbuffer_store_dwordx4 v[56:59], v15, s[20:23], s50 offen sc1\ns_waitcnt lgkmcnt(3)\nv_mfma_f32_16x16x32_bf16 v[72:75], v[144:147], v[44:47], v[72:75]\n"
"v_cvt_pk_bf16_f32 v60, v104, v105\nv_mfma_f32_16x16x32_bf16 v[76:79], v[148:151], v[44:47], v[76:79]\nds_read_b128 v[44:47], v33 offset:8192\nv_cvt_pk_bf16_f32 v61, v106, v107\n"
"s_waitcnt lgkmcnt(3)\nv_mfma_f32_16x16x32_bf16 v[64:67], v[152:155], v[48:51], v[64:67]\nv_cvt_pk_bf16_f32 v62, v108, v109\nv_mfma_f32_16x16x32_bf16 v[68:71], v[156:159], v[48:51], v[68:71]\n"
"ds_read_b128 v[48:51], v34 offset:8192\nv_cvt_pk_bf16_f32 v63, v110, v111\ns_waitcnt lgkmcnt(3)\nv_mfma_f32_16x16x32_bf16 v[72:75], v[152:155], v[52:55], v[72:75]\n"
"v_mfma_f32_16x16x32_bf16 v[76:79], v[156:159], v[52:55], v[76:79]\nds_read_b128 v[52:55], v35 offset:8192\ns_waitcnt lgkmcnt(3)\nv_mfma_f32_16x16x32_bf16 v[64:67], v[160:163], v[40:43], v[64:67]\n"
"buffer_store_dwordx4 v[60:63], v16, s[20:23], s50 offen sc1\nv_mfma_f32_16x16x32_bf16 v[68:71], v[164:167], v[40:43], v[68:71]\nds_read_b128 v[40:43], v32 offset:12288\ns_add_u32 s50, s50, s36\n"
"s_waitcnt lgkmcnt(3)\nv_mfma_f32_16x16x32_bf16 v[72:75], v[160:163], v[44:47], v[72:75]\ns_add_u32 m0, s52, 0\nv_mfma_f32_16x16x32_bf16 v[76:79], v[164:167], v[44:47], v[76:79]\n"
"ds_read_b128 v[44:47], v33 offset:12288\ns_add_u32 s53, s49, 0\ns_waitcnt lgkmcnt(3)\nv_mfma_f32_16x16x32_bf16 v[64:67], v[168:171], v[48:51], v[64:67]\n"
"buffer_load_dwordx4 v10, s[12:15], s53 offen lds\nv_mfma_f32_16x16x32_bf16 v[68:71], v[172:175], v[48:51], v[68:71]\nds_read_b128 v[48:51], v34 offset:12288\ns_add_u32 m0, s52, 4096\n"
"s_waitcnt lgkmcnt(3)\nv_mfma_f32_16x16x32_bf16 v[72:75], v[168:171], v[52:55], v[72:75]\ns_add_u32 s53, s49, 128\nv_mfma_f32_16x16x32_bf16 v[76:79], v[172:175], v[52:55], v[76:79]\n"
"ds_read_b128 v[52:55], v35 offset:12288\nbuffer_load_dwordx4 v10, s[12:15], s53 offen lds\ns_waitcnt lgkmcnt(3)\nv_mfma_f32_16x16x32_bf16 v[64:67], v[176:179], v[40:43], v[64:67]\n"
"s_add_u32 m0, s52, 8192\nv_mfma_f32_16x16x32_bf16 v[68:71], v[180:183], v[40:43], v[68:71]\nds_read_b128 v[40:43], v32 offset:16384\ns_add_u32 s53, s49, 256\n"
"s_waitcnt lgkmcnt(3)\nv_mfma_f32_16x16x32_bf16 v[72:75], v[176:179], v[44:47], v[72:75]\nbuffer_load_dwordx4 v10, s[12:15], s53 offen lds\nv_mfma_f32_16x16x32_bf16 v[76:79], v[180:183], v[44:47], v[76:79]\n"
"ds_read_b128 v[44:47], v33 offset:16384\ns_add_u32 m0, s52, 12288\ns_waitcnt lgkmcnt(3)\nv_mfma_f32_16x16x32_bf16 v[64:67], v[184:187], v[48:51], v[64:67]\n"
"s_add_u32 s53, s49, 384\nv_mfma_f32_16x16x32_bf16 v[68:71], v[188:191], v[48:51], v[68:71]\nds_read_b128 v[48:51], v34 offset:16384\nbuffer_load_dwordx4 v10, s[12:15], s53 offen lds\n"
"s_waitcnt lgkmcnt(3)\nv_mfma_f32_16x16x32_bf16 v[72:75], v[184:187], v[52:55], v[72:75]\ns_add_u32 m0, s52, 16384\nv_mfma_f32_16x16x32_bf16 v[76:79], v[188:191], v[52:55], v[76:79]\n"
"ds_read_b128 v[52:55], v35 offset:16384\ns_add_u32 s53, s49, 512\ns_waitcnt lgkmcnt(3)\nv_mfma_f32_16x16x32_bf16 v[64:67], v[192:195], v[40:43], v[64:67]\n"
"buffer_load_dwordx4 v10, s[12:15], s53 offen lds\nv_mfma_f32_16x16x32_bf16 v[68:71], v[196:199], v[40:43], v[68:71]\nds_read_b128 v[40:43], v32 offset:20480\ns_add_u32 m0, s52, 20480\n"
"s_waitcnt lgkmcnt(3)\nv_mfma_f32_16x16x32_bf16 v[72:75], v[192:195], v[44:47], v[72:75]\ns_add_u32 s53, s49, 640\nv_mfma_f32_16x16x32_bf16 v[76:79], v[196:199], v[44:47], v[76:79]\n"
"ds_read_b128 v[44:47], v33 offset:20480\nbuffer_load_dwordx4 v10, s[12:15], s53 offen lds\ns_waitcnt lgkmcnt(3)\nv_mfma_f32_16x16x32_bf16 v[64:67], v[200:203], v[48:51], v[64:67]\n"
"s_add_u32 m0, s52, 24576\nv_mfma_f32_16x16x32_bf16 v[68:71], v[204:207], v[48:51], v[68:71]\nds_read_b128 v[48:51], v34 offset:20480\ns_add_u32 s53, s49, 768\n"
"s_waitcnt lgkmcnt(3)\nv_mfma_f32_16x16x32_bf16 v[72:75], v[200:203], v[52:55], v[72:75]\nbuffer_load_dwordx4 v10, s[12:15], s53 offen lds\nv_mfma_f32_16x16x32_bf16 v[76:79], v[204:207], v[52:55], v[76:79]\n"
"ds_read_b128 v[52:55], v35 offset:20480\nglobal_load_dwordx4 v[240:243], v23, s[38:39] offset:0\ns_waitcnt lgkmcnt(3)\nv_mfma_f32_16x16x32_bf16 v[64:67], v[208:211], v[40:43], v[64:67]\n"
"s_mov_b32 s53, 5120\nbuffer_load_dwordx4 a[40:43], v14, s[16:19], s53 offen sc1\nv_mfma_f32_16x16x32_bf16 v[68:71], v[212:215], v[40:43], v[68:71]\nds_read_b128 v[40:43], v32 offset:24576\n"
"s_mov_b32 s53, 19456\nbuffer_load_dwordx4 a[44:47], v14, s[16:19], s53 offen sc1\ns_waitcnt lgkmcnt(3)\nv_mfma_f32_16x16x32_bf16 v[72:75], v[208:211], v[44:47], v[72:75]\n"
"s_mov_b32 s53, 6144\nbuffer_load_dwordx4 a[48:51], v14, s[16:19], s53 offen sc1\nv_mfma_f32_16x16x32_bf16 v[76:79], v[212:215], v[44:47], v[76:79]\nds_read_b128 v[44:47], v33 offset:24576\n"
"s_mov_b32 s53, 20480\nbuffer_load_dwordx4 a[52:55], v14, s[16:19], s53 offen sc1\ns_waitcnt lgkmcnt(3)\nv_mfma_f32_16x16x32_bf16 v[64:67], v[216:219], v[48:51], v[64:67]\n"
"s_add_u32 s54, s51, 32768\ns_cmp_eq_u32 s54, 163840\nv_mfma_f32_16x16x32_bf16 v[68:71], v[220:223], v[48:51], v[68:71]\ns_cselect_b32 s54, 0, s54\n"
"s_sub_u32 s54, s54, s51\nds_read_b128 v[48:51], v34 offset:24576\ns_mov_b32 s53, 7168\nbuffer_load_dwordx4 a[56:59], v14, s[16:19], s53 offen sc1\n"
"s_waitcnt lgkmcnt(3)\nv_mfma_f32_16x16x32_bf16 v[72:75], v[216:219], v[52:55], v[72:75]\nv_mfma_f32_16x16x32_bf16 v[76:79], v[220:223], v[52:55], v[76:79]\nds_read_b128 v[52:55], v35 offset:24576\n"
"s_waitcnt lgkmcnt(3)\nv_mfma_f32_16x16x32_bf16 v[64:67], v[224:227], v[40:43], v[64:67]\nv_add_u32 v32, s54, v32\nv_mfma_f32_16x16x32_bf16 v[68:71], v[228:231], v[40:43], v[68:71]\n"
"ds_read_b128 v[40:43], v32 offset:0\ns_waitcnt lgkmcnt(3)\nv_mfma_f32_16x16x32_bf16 v[72:75], v[224:227], v[44:47], v[72:75]\nv_add_u32 v33, s54, v33\n"
"v_mfma_f32_16x16x32_bf16 v[76:79], v[228:231], v[44:47], v[76:79]\nds_read_b128 v[44:47], v33 offset:0\ns_waitcnt lgkmcnt(3)\nv_mfma_f32_16x16x32_bf16 v[64:67], v[232:235], v[48:51], v[64:67]\n"
"v_add_u32 v34, s54, v34\nv_mfma_f32_16x16x32_bf16 v[68:71], v[236:239], v[48:51], v[68:71]\nds_read_b128 v[48:51], v34 offset:0\ns_waitcnt lgkmcnt(3)\n"
"v_mfma_f32_16x16x32_bf16 v[72:75], v[232:235], v[52:55], v[72:75]\nv_add_u32 v35, s54, v35\nv_mfma_f32_16x16x32_bf16 v[76:79], v[236:239], v[52:55], v[76:79]\nds_read_b128 v[52:55], v35 offset:0\n"
"s_mov_b32 s52, s47\ns_add_u32 s47, s51, s32\ns_add_u32 s51, s51, s54\ns_add_u32 s49, s49, 28672\n"
"s_waitcnt vmcnt(13)\ns_barrier\ns_waitcnt lgkmcnt(3)\nv_mfma_f32_16x16x32_bf16 v[96:99], v[128:131], v[40:43], 0\n"
"v_mov_b32 v36, s48\ns_add_u32 s48, s48, 1\ns_nop 0\nbuffer_store_dword v36, v19, s[24:27], s33 offen sc1\n"
"v_mfma_f32_16x16x32_bf16 v[100:103], v[132:135], v[40:43], 0\nds_read_b128 v[40:43], v32 offset:4096\ns_waitcnt lgkmcnt(3)\nv_mfma_f32_16x16x32_bf16 v[104:107], v[128:131], v[44:47], 0\n"
"v_mfma_f32_16x16x32_bf16 v[108:111], v[132:135], v[44:47], 0\nds_read_b128 v[44:47], v33 offset:4096\nv_cvt_pk_bf16_f32 v56, v64, v65\ns_waitcnt lgkmcnt(3)\n"
"v_mfma_f32_16x16x32_bf16 v[96:99], v[136:139], v[48:51], v[96:99]\nv_cvt_pk_bf16_f32 v57, v66, v67\nv_mfma_f32_16x16x32_bf16 v[100:103], v[140:143], v[48:51], v[100:103]\nds_read_b128 v[48:51], v34 offset:4096\n"
"v_cvt_pk_bf16_f32 v58, v68, v69\ns_waitcnt lgkmcnt(3)\nv_mfma_f32_16x16x32_bf16 v[104:107], v[136:139], v[52:55], v[104:107]\nv_cvt_pk_bf16_f32 v59, v70, v71\n"
"v_mfma_f32_16x16x32_bf16 v[108:111], v[140:143], v[52:55], v[108:111]\nds_read_b128 v[52:55], v35 offset:4096\ns_waitcnt lgkmcnt(3)\nv_mfma_f32_16x16x32_bf16 v[96:99], v[144:147], v[40:43], v[96:99]\n"
"v_mfma_f32_16x16x32_bf16 v[100:103], v[148:151], v[40:43], v[100:103]\nds_read_b128 v[40:43], v32 offset:8192\nbuffer_store_dwordx4 v[56:59], v15, s[20:23], s50 offen sc1\ns_waitcnt lgkmcnt(3)\n"
"v_mfma_f32_16x16x32_bf16 v[104:107], v[144:147], v[44:47], v[104:107]\nv_cvt_pk_bf16_f32 v60, v72, v73\nv_mfma_f32_16x16x32_bf16 v[108:111], v[148:151], v[44:47], v[108:111]\nds_read_b128 v[44:47], v33 offset:8192\n"
"v_cvt_pk_bf16_f32 v61, v74, v75\ns_waitcnt lgkmcnt(3)\nv_mfma_f32_16x16x32_bf16 v[96:99], v[152:155], v[48:51], v[96:99]\nv_cvt_pk_bf16_f32 v62, v76, v77\n"
"v_mfma_f32_16x16x32_bf16 v[100:103], v[156:159], v[48:51], v[100:103]\nds_read_b128 v[48:51], v34 offset:8192\nv_cvt_pk_bf16_f32 v63, v78, v79\ns_waitcnt lgkmcnt(3)\n"
"v_mfma_f32_16x16x32_bf16 v[104:107], v[152:155], v[52:55], v[104:107]\nv_mfma_f32_16x16x32_bf16 v[108:111], v[156:159], v[52:55], v[108:111]\nds_read_b128 v[52:55], v35 offset:8192\ns_waitcnt lgkmcnt(3)\n"
"v_mfma_f32_16x16x32_bf16 v[96:99], v[160:163], v[40:43], v[96:99]\nbuffer_store_dwordx4 v[60:63], v16, s[20:23], s50 offen sc1\nv_mfma_f32_16x16x32_bf16 v[100:103], v[164:167], v[40:43], v[100:103]\nds_read_b128 v[40:43], v32 offset:12288\n"
"s_add_u32 s50, s50, s36\ns_waitcnt lgkmcnt(3)\nv_mfma_f32_16x16x32_bf16 v[104:107], v[160:163], v[44:47], v[104:107]\ns_add_u32 m0, s52, 0\n"
"v_mfma_f32_16x16x32_bf16 v[108:111], v[164:167], v[44:47], v[108:111]\nds_read_b128 v[44:47], v33 offset:12288\ns_add_u32 s53, s49, 0\ns_waitcnt lgkmcnt(3)\n"
"v_mfma_f32_16x16x32_bf16 v[96:99], v[168:171], v[48:51], v[96:99]\nbuffer_load_dwordx4 v10, s[12:15], s53 offen lds\nv_mfma_f32_16x16x32_bf16 v[100:103], v[172:175], v[48:51], v[100:103]\nds_read_b128 v[48:51], v34 offset:12288\n"
"s_add_u32 m0, s52, 4096\ns_waitcnt lgkmcnt(3)\nv_mfma_f32_16x16x32_bf16 v[104:107], v[168:171], v[52:55], v[104:107]\ns_add_u32 s53, s49, 128\n"
"v_mfma_f32_16x16x32_bf16 v[108:111], v[172:175], v[52:55], v[108:111]\nds_read_b128 v[52:55], v35 offset:12288\nbuffer_load_dwordx4 v10, s[12:15], s53 offen lds\ns_waitcnt lgkmcnt(3)\n"
"v_mfma_f32_16x16x32_bf16 v[96:99], v[176:179], v[40:43], v[96:99]\ns_add_u32 m0, s52, 8192\nv_mfma_f32_16x16x32_bf16 v[100:103], v[180:183], v[40:43], v[100:103]\nds_read_b128 v[40:43], v32 offset:16384\n"
"s_add_u32 s53, s49, 256\ns_waitcnt lgkmcnt(3)\nv_mfma_f32_16x16x32_bf16 v[104:107], v[176:179], v[44:47], v[104:107]\nbuffer_load_dwordx4 v10, s[12:15], s53 offen lds\n"
"v_mfma_f32_16x16x32_bf16 v[108:111], v[180:183], v[44:47], v[108:111]\nds_read_b128 v[44:47], v33 offset:16384\ns_add_u32 m0, s52, 12288\ns_waitcnt lgkmcnt(3)\n"
"v_mfma_f32_16x16x32_bf16 v[96:99], v[184:187], v[48:51], v[96:99]\ns_add_u32 s53, s49, 384\nv_mfma_f32_16x16x32_bf16 v[100:103], v[188:191], v[48:51], v[100:103]\nds_read_b128 v[48:51], v34 offset:16384\n"
"buffer_load_dwordx4 v10, s[12:15], s53 offen lds\ns_waitcnt lgkmcnt(3)\nv_mfma_f32_16x16x32_bf16 v[104:107], v[184:187], v[52:55], v[104:107]\ns_add_u32 m0, s52, 16384\n"
"v_mfma_f32_16x16x32_bf16 v[108:111], v[188:191], v[52:55], v[108:111]\nds_read_b128 v[52:55], v35 offset:16384\ns_add_u32 s53, s49, 512\ns_waitcnt lgkmcnt(3)\n"
"v_mfma_f32_16x16x32_bf16 v[96:99], v[192:195], v[40:43], v[96:99]\nbuffer_load_dwordx4 v10, s[12:15], s53 offen lds\nv_mfma_f32_16x16x32_bf16 v[100:103], v[196:199], v[40:43], v[100:103]\nds_read_b128 v[40:43], v32 offset:20480\n"
"s_add_u32 m0, s52, 20480\ns_waitcnt lgkmcnt(3)\nv_mfma_f32_16x16x32_bf16 v[104:107], v[192:195], v[44:47], v[104:107]\ns_add_u32 s53, s49, 640\n"
"v_mfma_f32_16x16x32_bf16 v[108:111], v[196:199], v[44:47], v[108:111]\nds_read_b128 v[44:47], v33 offset:20480\nbuffer_load_dwordx4 v10, s[12:15], s53 offen lds\ns_waitcnt lgkmcnt(3)\n"
"v_mfma_f32_16x16x32_bf16 v[96:99], v[200:203], v[48:51], v[96:99]\ns_add_u32 m0, s52, 24576\nv_mfma_f32_16x16x32_bf16 v[100:103], v[204:207], v[48:51], v[100:103]\nds_read_b128 v[48:51], v34 offset:20480\n"
"s_add_u32 s53, s49, 768\ns_waitcnt lgkmcnt(3)\nv_mfma_f32_16x16x32_bf16 v[104:107], v[200:203], v[52:55], v[104:107]\nbuffer_load_dwordx4 v10, s[12:15], s53 offen lds\n"
"v_mfma_f32_16x16x32_bf16 v[108:111], v[204:207], v[52:55], v[108:111]\nds_read_b128 v[52:55], v35 offset:20480\nglobal_load_dwordx4 v[240:243], v23, s[38:39] offset:1024\ns_waitcnt lgkmcnt(3)\n"
"v_mfma_f32_16x16x32_bf16 v[96:99], v[208:211], v[40:43], v[96:99]\ns_mov_b32 s53, 21504\nbuffer_load_dwordx4 a[60:63], v14, s[16:19], s53 offen sc1\nv_mfma_f32_16x16x32_bf16 v[100:103], v[212:215], v[40:43], v[100:103]\n"
"ds_read_b128 v[40:43], v32 offset:24576\ns_mov_b32 s53, 8192\nbuffer_load_dwordx4 a[64:67], v14, s[16:19], s53 offen sc1\ns_waitcnt lgkmcnt(3)\n"
"v_mfma_f32_16x16x32_bf16 v[104:107], v[208:211], v[44:47], v[104:107]\ns_mov_b32 s53, 22528\nbuffer_load_dwordx4 a[68:71], v14, s[16:19], s53 offen sc1\nv_mfma_f32_16x16x32_bf16 v[108:111], v[212:215], v[44:47], v[108:111]\n"
"ds_read_b128 v[44:47], v33 offset:24576\ns_mov_b32 s53, 9216\nbuffer_load_dwordx4 a[72:75], v14, s[16:19], s53 offen sc1\ns_waitcnt lgkmcnt(3)\n"
"v_mfma_f32_16x16x32_bf16 v[96:99], v[216:219], v[48:51], v[96:99]\ns_add_u32 s54, s51, 32768\ns_cmp_eq_u32 s54, 163840\nv_mfma_f32_16x16x32_bf16 v[100:103], v[220:223], v[48:51], v[100:103]\n"
"s_cselect_b32 s54, 0, s54\ns_sub_u32 s54, s54, s51\nds_read_b128 v[48:51], v34 offset:24576\ns_mov_b32 s53, 23552\n"
"buffer_load_dwordx4 a[76:79], v14, s[16:19], s53 offen sc1\ns_waitcnt lgkmcnt(3)\nv_mfma_f32_16x16x32_bf16 v[104:107], v[216:219], v[52:55], v[104:107]\nv_mfma_f32_16x16x32_bf16 v[108:111], v[220:223], v[52:55], v[108:111]\n"
"ds_read_b128 v[52:55], v35 offset:24576\ns_waitcnt lgkmcnt(3)\nv_mfma_f32_16x16x32_bf16 v[96:99], v[224:227], v[40:43], v[96:99]\nv_add_u32 v32, s54, v32\n"
"v_mfma_f32_16x16x32_bf16 v[100:103], v[228:231], v[40:43], v[100:103]\nds_read_b128 v[40:43], v32 offset:0\ns_waitcnt lgkmcnt(3)\nv_mfma_f32_16x16x32_bf16 v[104:107], v[224:227], v[44:47], v[104:107]\n"
"v_add_u32 v33, s54, v33\nv_mfma_f32_16x16x32_bf16 v[108:111], v[228:231], v[44:47], v[108:111]\nds_read_b128 v[44:47], v33 offset:0\ns_waitcnt lgkmcnt(3)\n"
"v_mfma_f32_16x16x32_bf16 v[96:99], v[232:235], v[48:51], v[96:99]\nv_add_u32 v34, s54, v34\nv_mfma_f32_16x16x32_bf16 v[100:103], v[236:239], v[48:51], v[100:103]\nds_read_b128 v[48:51], v34 offset:0\n"
"s_waitcnt lgkmcnt(3)\nv_mfma_f32_16x16x32_bf16 v[104:107], v[232:235], v[52:55], v[104:107]\nv_add_u32 v35, s54, v35\nv_mfma_f32_16x16x32_bf16 v[108:111], v[236:239], v[52:55], v[108:111]\n"
"ds_read_b128 v[52:55], v35 offset:0\ns_mov_b32 s52, s47\ns_add_u32 s47, s51, s32\ns_add_u32 s51, s51, s54\n"
"s_add_u32 s49, s49, 28672\ns_waitcnt vmcnt(13)\ns_barrier\ns_waitcnt lgkmcnt(3)\n"
"v_mfma_f32_16x16x32_bf16 v[64:67], v[128:131], v[40:43], 0\nv_mov_b32 v36, s48\ns_add_u32 s48, s48, 1\ns_nop 0\n"
"buffer_store_dword v36, v19, s[24:27], s33 offen sc1\nv_mfma_f32_16x16x32_bf16 v[68:71], v[132:135], v[40:43], 0\nds_read_b128 v[40:43], v32 offset:4096\ns_waitcnt lgkmcnt(3)\n"
"v_mfma_f32_16x16x32_bf16 v[72:75], v[128:131], v[44:47], 0\nv_mfma_f32_16x16x32_bf16 v[76:79], v[132:135], v[44:47], 0\nds_read_b128 v[44:47], v33 offset:4096\nv_cvt_pk_bf16_f32 v56, v96, v97\n"
"s_waitcnt lgkmcnt(3)\nv_mfma_f32_16x16x32_bf16 v[64:67], v[136:139], v[48:51], v[64:67]\nv_cvt_pk_bf16_f32 v57, v98, v99\nv_mfma_f32_16x16x32_bf16 v[68:71], v[140:143], v[48:51], v[68:71]\n"
"ds_read_b128 v[48:51], v34 offset:4096\nv_cvt_pk_bf16_f32 v58, v100, v101\ns_waitcnt lgkmcnt(3)\nv_mfma_f32_16x16x32_bf16 v[72:75], v[136:139], v[52:55], v[72:75]\n"
"v_cvt_pk_bf16_f32 v59, v102, v103\nv_mfma_f32_16x16x32_bf16 v[76:79], v[140:143], v[52:55], v[76:79]\nds_read_b128 v[52:55], v35 offset:4096\ns_waitcnt lgkmcnt(3)\n"
"v_mfma_f32_16x16x32_bf16 v[64:67], v[144:147], v[40:43], v[64:67]\nv_mfma_f32_16x16x32_bf16 v[68:71], v[148:151], v[40:43], v[68:71]\nds_read_b128 v[40:43], v32 offset:8192\nbuffer_store_dwordx4 v[56:59], v15, s[20:23], s50 offen sc1\n"
"s_waitcnt lgkmcnt(3)\nv_mfma_f32_16x16x32_bf16 v[72:75], v[144:147], v[44:47], v[72:75]\nv_cvt_pk_bf16_f32 v60, v104, v105\nv_mfma_f32_16x16x32_bf16 v[76:79], v[148:151], v[44:47], v[76:79]\n"
"ds_read_b128 v[44:47], v33 offset:8192\nv_cvt_pk_bf16_f32 v61, v106, v107\ns_waitcnt lgkmcnt(3)\nv_mfma_f32_16x16x32_bf16 v[64:67], v[152:155], v[48:51], v[64:67]\n"
"v_cvt_pk_bf16_f32 v62, v108, v109\nv_mfma_f32_16x16x32_bf16 v[68:71], v[156:159], v[48:51], v[68:71]\nds_read_b128 v[48:51], v34 offset:8192\nv_cvt_pk_bf16_f32 v63, v110, v111\n"
"s_waitcnt lgkmcnt(3)\nv_mfma_f32_16x16x32_bf16 v[72:75], v[152:155], v[52:55], v[72:75]\nv_mfma_f32_16x16x32_bf16 v[76:79], v[156:159], v[52:55], v[76:79]\nds_read_b128 v[52:55], v35 offset:8192\n"
"s_waitcnt lgkmcnt(3)\nv_mfma_f32_16x16x32_bf16 v[64:67], v[160:163], v[40:43], v[64:67]\nbuffer_store_dwordx4 v[60:63], v16, s[20:23], s50 offen sc1\nv_mfma_f32_16x16x32_bf16 v[68:71], v[164:167], v[40:43], v[68:71]\n"
"ds_read_b128 v[40:43], v32 offset:12288\ns_add_u32 s50, s50, s36\ns_waitcnt lgkmcnt(3)\nv_mfma_f32_16x16x32_bf16 v[72:75], v[160:163], v[44:47], v[72:75]\n"
"s_add_u32 m0, s52, 0\nv_mfma_f32_16x16x32_bf16 v[76:79], v[164:167], v[44:47], v[76:79]\nds_read_b128 v[44:47], v33 offset:12288\ns_add_u32 s53, s49, 0\n"
"s_waitcnt lgkmcnt(3)\nv_mfma_f32_16x16x32_bf16 v[64:67], v[168:171], v[48:51], v[64:67]\nbuffer_load_dwordx4 v10, s[12:15], s53 offen lds\nv_mfma_f32_16x16x32_bf16 v[68:71], v[172:175], v[48:51], v[68:71]\n"
"ds_read_b128 v[48:51], v34 offset:12288\ns_add_u32 m0, s52, 4096\ns_waitcnt lgkmcnt(3)\nv_mfma_f32_16x16x32_bf16 v[72:75], v[168:171], v[52:55], v[72:75]\n"
"s_add_u32 s53, s49, 128\nv_mfma_f32_16x16x32_bf16 v[76:79], v[172:175], v[52:55], v[76:79]\nds_read_b128 v[52:55], v35 offset:12288\nbuffer_load_dwordx4 v10, s[12:15], s53 offen lds\n"
"s_waitcnt lgkmcnt(3)\nv_mfma_f32_16x16x32_bf16 v[64:67], v[176:179], v[40:43], v[64:67]\ns_add_u32 m0, s52, 8192\nv_mfma_f32_16x16x32_bf16 v[68:71], v[180:183], v[40:43], v[68:71]\n"
"ds_read_b128 v[40:43], v32 offset:16384\ns_add_u32 s53, s49, 256\ns_waitcnt lgkmcnt(3)\nv_mfma_f32_16x16x32_bf16 v[72:75], v[176:179], v[44:47], v[72:75]\n"
"buffer_load_dwordx4 v10, s[12:15], s53 offen lds\nv_mfma_f32_16x16x32_bf16 v[76:79], v[180:183], v[44:47], v[76:79]\nds_read_b128 v[44:47], v33 offset:16384\ns_add_u32 m0, s52, 12288\n"
"s_waitcnt lgkmcnt(3)\nv_mfma_f32_16x16x32_bf16 v[64:67], v[184:187], v[48:51], v[64:67]\ns_add_u32 s53, s49, 384\nv_mfma_f32_16x16x32_bf16 v[68:71], v[188:191], v[48:51], v[68:71]\n"
"ds_read_b128 v[48:51], v34 offset:16384\nbuffer_load_dwordx4 v10, s[12:15], s53 offen lds\ns_waitcnt lgkmcnt(3)\nv_mfma_f32_16x16x32_bf16 v[72:75], v[184:187], v[52:55], v[72:75]\n"
"s_add_u32 m0, s52, 16384\nv_mfma_f32_16x16x32_bf16 v[76:79], v[188:191], v[52:55], v[76:79]\nds_read_b128 v[52:55], v35 offset:16384\ns_add_u32 s53, s49, 512\n"
"s_waitcnt lgkmcnt(3)\nv_mfma_f32_16x16x32_bf16 v[64:67], v[192:195], v[40:43], v[64:67]\nbuffer_load_dwordx4 v10, s[12:15], s53 offen lds\nv_mfma_f32_16x16x32_bf16 v[68:71], v[196:199], v[40:43], v[68:71]\n"
"ds_read_b128 v[40:43], v32 offset:20480\ns_add_u32 m0, s52, 20480\ns_waitcnt lgkmcnt(3)\nv_mfma_f32_16x16x32_bf16 v[72:75], v[192:195], v[44:47], v[72:75]\n"
"s_add_u32 s53, s49, 640\nv_mfma_f32_16x16x32_bf16 v[76:79], v[196:199], v[44:47], v[76:79]\nds_read_b128 v[44:47], v33 offset:20480\nbuffer_load_dwordx4 v10, s[12:15], s53 offen lds\n"
"s_waitcnt lgkmcnt(3)\nv_mfma_f32_16x16x32_bf16 v[64:67], v[200:203], v[48:51], v[64:67]\ns_add_u32 m0, s52, 24576\nv_mfma_f32_16x16x32_bf16 v[68:71], v[204:207], v[48:51], v[68:71]\n"
"ds_read_b128 v[48:51], v34 offset:20480\ns_add_u32 s53, s49, 768\ns_waitcnt lgkmcnt(3)\nv_mfma_f32_16x16x32_bf16 v[72:75], v[200:203], v[52:55], v[72:75]\n"
"buffer_load_dwordx4 v10, s[12:15], s53 offen lds\nv_mfma_f32_16x16x32_bf16 v[76:79], v[204:207], v[52:55], v[76:79]\nds_read_b128 v[52:55], v35 offset:20480\nglobal_load_dwordx4 v[240:243], v23, s[38:39] offset:2048\n"
"s_waitcnt lgkmcnt(3)\nv_mfma_f32_16x16x32_bf16 v[64:67], v[208:211], v[40:43], v[64:67]\ns_mov_b32 s53, 10240\nbuffer_load_dwordx4 a[80:83], v14, s[16:19], s53 offen sc1\n"
"v_mfma_f32_16x16x32_bf16 v[68:71], v[212:215], v[40:43], v[68:71]\nds_read_b128 v[40:43], v32 offset:24576\ns_mov_b32 s53, 24576\nbuffer_load_dwordx4 a[84:87], v14, s[16:19], s53 offen sc1\n"
"s_waitcnt lgkmcnt(3)\nv_mfma_f32_16x16x32_bf16 v[72:75], v[208:211], v[44:47], v[72:75]\ns_mov_b32 s53, 11264\nbuffer_load_dwordx4 a[88:91], v14, s[16:19], s53 offen sc1\n"
"v_mfma_f32_16x16x32_bf16 v[76:79], v[212:215], v[44:47], v[76:79]\nds_read_b128 v[44:47], v33 offset:24576\ns_mov_b32 s53, 25600\nbuffer_load_dwordx4 a[92:95], v14, s[16:19], s53 offen sc1\n"
"s_waitcnt lgkmcnt(3)\nv_mfma_f32_16x16x32_bf16 v[64:67], v[216:219], v[48:51], v[64:67]\ns_add_u32 s54, s51, 32768\ns_cmp_eq_u32 s54, 163840\n"
"v_mfma_f32_16x16x32_bf16 v[68:71], v[220:223], v[48:51], v[68:71]\ns_cselect_b32 s54, 0, s54\ns_sub_u32 s54, s54, s51\nds_read_b128 v[48:51], v34 offset:24576\n"
"s_mov_b32 s53, 12288\nbuffer_load_dwordx4 a[96:99], v14, s[16:19], s53 offen sc1\ns_waitcnt lgkmcnt(3)\nv_mfma_f32_16x16x32_bf16 v[72:75], v[216:219], v[52:55], v[72:75]\n"
"v_mfma_f32_16x16x32_bf16 v[76:79], v[220:223], v[52:55], v[76:79]\nds_read_b128 v[52:55], v35 offset:24576\ns_waitcnt lgkmcnt(3)\nv_mfma_f32_16x16x32_bf16 v[64:67], v[224:227], v[40:43], v[64:67]\n"
"v_add_u32 v32, s54, v32\nv_mfma_f32_16x16x32_bf16 v[68:71], v[228:231], v[40:43], v[68:71]\nds_read_b128 v[40:43], v32 offset:0\ns_waitcnt lgkmcnt(3)\n"
"v_mfma_f32_16x16x32_bf16 v[72:75], v[224:227], v[44:47], v[72:75]\nv_add_u32 v33, s54, v33\nv_mfma_f32_16x16x32_bf16 v[76:79], v[228:231], v[44:47], v[76:79]\nds_read_b128 v[44:47], v33 offset:0\n"
"s_waitcnt lgkmcnt(3)\nv_mfma_f32_16x16x32_bf16 v[64:67], v[232:235], v[48:51], v[64:67]\nv_add_u32 v34, s54, v34\nv_mfma_f32_16x16x32_bf16 v[68:71], v[236:239], v[48:51], v[68:71]\n"
"ds_read_b128 v[48:51], v34 offset:0\ns_waitcnt lgkmcnt(3)\nv_mfma_f32_16x16x32_bf16 v[72:75], v[232:235], v[52:55], v[72:75]\nv_add_u32 v35, s54, v35\n"
"v_mfma_f32_16x16x32_bf16 v[76:79], v[236:239], v[52:55], v[76:79]\nds_read_b128 v[52:55], v35 offset:0\ns_mov_b32 s52, s47\ns_add_u32 s47, s51, s32\n"
"s_add_u32 s51, s51, s54\ns_add_u32 s49, s49, 28672\ns_waitcnt vmcnt(13)\ns_barrier\n"
"s_waitcnt lgkmcnt(3)\nv_mfma_f32_16x16x32_bf16 v[96:99], v[128:131], v[40:43], 0\nv_mov_b32 v36, s48\ns_add_u32 s48, s48, 1\n"
"s_nop 0\nbuffer_store_dword v36, v19, s[24:27], s33 offen sc1\nv_mfma_f32_16x16x32_bf16 v[100:103], v[132:135], v[40:43], 0\nds_read_b128 v[40:43], v32 offset:4096\n"
"s_waitcnt lgkmcnt(3)\nv_mfma_f32_16x16x32_bf16 v[104:107], v[128:131], v[44:47], 0\nv_mfma_f32_16x16x32_bf16 v[108:111], v[132:135], v[44:47], 0\nds_read_b128 v[44:47], v33 offset:4096\n"
"v_cvt_pk_bf16_f32 v56, v64, v65\ns_waitcnt lgkmcnt(3)\nv_mfma_f32_16x16x32_bf16 v[96:99], v[136:139], v[48:51], v[96:99]\nv_cvt_pk_bf16_f32 v57, v66, v67\n"
"v_mfma_f32_16x16x32_bf16 v[100:103], v[140:143], v[48:51], v[100:103]\nds_read_b128 v[48:51], v34 offset:4096\nv_cvt_pk_bf16_f32 v58, v68, v69\ns_waitcnt lgkmcnt(3)\n"
"v_mfma_f32_16x16x32_bf16 v[104:107], v[136:139], v[52:55], v[104:107]\nv_cvt_pk_bf16_f32 v59, v70, v71\nv_mfma_f32_16x16x32_bf16 v[108:111], v[140:143], v[52:55], v[108:111]\nds_read_b128 v[52:55], v35 offset:4096\n"
"s_waitcnt lgkmcnt(3)\nv_mfma_f32_16x16x32_bf16 v[96:99], v[144:147], v[40:43], v[96:99]\nv_mfma_f32_16x16x32_bf16 v[100:103], v[148:151], v[40:43], v[100:103]\nds_read_b128 v[40:43], v32 offset:8192\n"
"buffer_store_dwordx4 v[56:59], v15, s[20:23], s50 offen sc1\ns_waitcnt lgkmcnt(3)\nv_mfma_f32_16x16x32_bf16 v[104:107], v[144:147], v[44:47], v[104:107]\nv_cvt_pk_bf16_f32 v60, v72, v73\n"
"v_mfma_f32_16x16x32_bf16 v[108:111], v[148:151], v[44:47], v[108:111]\nds_read_b128 v[44:47], v33 offset:8192\nv_cvt_pk_bf16_f32 v61, v74, v75\ns_waitcnt lgkmcnt(3)\n"
"v_mfma_f32_16x16x32_bf16 v[96:99], v[152:155], v[48:51], v[96:99]\nv_cvt_pk_bf16_f32 v62, v76, v77\nv_mfma_f32_16x16x32_bf16 v[100:103], v[156:159], v[48:51], v[100:103]\nds_read_b128 v[48:51], v34 offset:8192\n"
"v_cvt_pk_bf16_f32 v63, v78, v79\ns_waitcnt lgkmcnt(3)\nv_mfma_f32_16x16x32_bf16 v[104:107], v[152:155], v[52:55], v[104:107]\nv_mfma_f32_16x16x32_bf16 v[108:111], v[156:159], v[52:55], v[108:111]\n"
"ds_read_b128 v[52:55], v35 offset:8192\ns_waitcnt lgkmcnt(3)\nv_mfma_f32_16x16x32_bf16 v[96:99], v[160:163], v[40:43], v[96:99]\nbuffer_store_dwordx4 v[60:63], v16, s[20:23], s50 offen sc1\n"
"v_mfma_f32_16x16x32_bf16 v[100:103], v[164:167], v[40:43], v[100:103]\nds_read_b128 v[40:43], v32 offset:12288\ns_add_u32 s50, s50, s36\ns_waitcnt lgkmcnt(3)\n"
"v_mfma_f32_16x16x32_bf16 v[104:107], v[160:163], v[44:47], v[104:107]\ns_add_u32 m0, s52, 0\nv_mfma_f32_16x16x32_bf16 v[108:111], v[164:167], v[44:47], v[108:111]\nds_read_b128 v[44:47], v33 offset:12288\n"
"s_add_u32 s53, s49, 0\ns_waitcnt lgkmcnt(3)\nv_mfma_f32_16x16x32_bf16 v[96:99], v[168:171], v[48:51], v[96:99]\nbuffer_load_dwordx4 v10, s[12:15], s53 offen lds\n"
"v_mfma_f32_16x16x32_bf16 v[100:103], v[172:175], v[48:51], v[100:103]\nds_read_b128 v[48:51], v34 offset:12288\ns_add_u32 m0, s52, 4096\ns_waitcnt lgkmcnt(3)\n"
"v_mfma_f32_16x16x32_bf16 v[104:107], v[168:171], v[52:55], v[104:107]\ns_add_u32 s53, s49, 128\nv_mfma_f32_16x16x32_bf16 v[108:111], v[172:175], v[52:55], v[108:111]\nds_read_b128 v[52:55], v35 offset:12288\n"
"buffer_load_dwordx4 v10, s[12:15], s53 offen lds\ns_waitcnt lgkmcnt(3)\nv_mfma_f32_16x16x32_bf16 v[96:99], v[176:179], v[40:43], v[96:99]\ns_add_u32 m0, s52, 8192\n"
"v_mfma_f32_16x16x32_bf16 v[100:103], v[180:183], v[40:43], v[100:103]\nds_read_b128 v[40:43], v32 offset:16384\ns_add_u32 s53, s49, 256\ns_waitcnt lgkmcnt(3)\n"
"v_mfma_f32_16x16x32_bf16 v[104:107], v[176:179], v[44:47], v[104:107]\nbuffer_load_dwordx4 v10, s[12:15], s53 offen lds\nv_mfma_f32_16x16x32_bf16 v[108:111], v[180:183], v[44:47], v[108:111]\nds_read_b128 v[44:47], v33 offset:16384\n"
"s_add_u32 m0, s52, 12288\ns_waitcnt lgkmcnt(3)\nv_mfma_f32_16x16x32_bf16 v[96:99], v[184:187], v[48:51], v[96:99]\ns_add_u32 s53, s49, 384\n"
"v_mfma_f32_16x16x32_bf16 v[100:103], v[188:191], v[48:51], v[100:103]\nds_read_b128 v[48:51], v34 offset:16384\nbuffer_load_dwordx4 v10, s[12:15], s53 offen lds\ns_waitcnt lgkmcnt(3)\n"
"v_mfma_f32_16x16x32_bf16 v[104:107], v[184:187], v[52:55], v[104:107]\ns_add_u32 m0, s52, 16384\nv_mfma_f32_16x16x32_bf16 v[108:111], v[188:191], v[52:55], v[108:111]\nds_read_b128 v[52:55], v35 offset:16384\n"
"s_add_u32 s53, s49, 512\ns_waitcnt lgkmcnt(3)\nv_mfma_f32_16x16x32_bf16 v[96:99], v[192:195], v[40:43], v[96:99]\nbuffer_load_dwordx4 v10, s[12:15], s53 offen lds\n"
"v_mfma_f32_16x16x32_bf16 v[100:103], v[196:199], v[40:43], v[100:103]\nds_read_b128 v[40:43], v32 offset:20480\ns_add_u32 m0, s52, 20480\ns_waitcnt lgkmcnt(3)\n"
"v_mfma_f32_16x16x32_bf16 v[104:107], v[192:195], v[44:47], v[104:107]\ns_add_u32 s53, s49, 640\nv_mfma_f32_16x16x32_bf16 v[108:111], v[196:199], v[44:47], v[108:111]\nds_read_b128 v[44:47], v33 offset:20480\n"
"buffer_load_dwordx4 v10, s[12:15], s53 offen lds\ns_waitcnt lgkmcnt(3)\nv_mfma_f32_16x16x32_bf16 v[96:99], v[200:203], v[48:51], v[96:99]\ns_add_u32 m0, s52, 24576\n"
"v_mfma_f32_16x16x32_bf16 v[100:103], v[204:207], v[48:51], v[100:103]\nds_read_b128 v[48:51], v34 offset:20480\ns_add_u32 s53, s49, 768\ns_waitcnt lgkmcnt(3)\n"
"v_mfma_f32_16x16x32_bf16 v[104:107], v[200:203], v[52:55], v[104:107]\nbuffer_load_dwordx4 v10, s[12:15], s53 offen lds\nv_mfma_f32_16x16x32_bf16 v[108:111], v[204:207], v[52:55], v[108:111]\nds_read_b128 v[52:55], v35 offset:20480\n"
"global_load_dwordx4 v[240:243], v23, s[38:39] offset:3072\ns_waitcnt lgkmcnt(3)\nv_mfma_f32_16x16x32_bf16 v[96:99], v[208:211], v[40:43], v[96:99]\ns_mov_b32 s53, 26624\n"
"buffer_load_dwordx4 a[100:103], v14, s[16:19], s53 offen sc1\nv_mfma_f32_16x16x32_bf16 v[100:103], v[212:215], v[40:43], v[100:103]\nds_read_b128 v[40:43], v32 offset:24576\ns_mov_b32 s53, 13312\n"
"buffer_load_dwordx4 a[104:107], v14, s[16:19], s53 offen sc1\ns_waitcnt lgkmcnt(3)\nv_mfma_f32_16x16x32_bf16 v[104:107], v[208:211], v[44:47], v[104:107]\ns_mov_b32 s53, 27648\n"
"buffer_load_dwordx4 a[108:111], v14, s[16:19], s53 offen sc1\nv_mfma_f32_16x16x32_bf16 v[108:111], v[212:215], v[44:47], v[108:111]\nds_read_b128 v[44:47], v33 offset:24576\ns_waitcnt lgkmcnt(3)\n"
"v_mfma_f32_16x16x32_bf16 v[96:99], v[216:219], v[48:51], v[96:99]\ns_add_u32 s54, s51, 32768\ns_cmp_eq_u32 s54, 163840\nv_mfma_f32_16x16x32_bf16 v[100:103], v[220:223], v[48:51], v[100:103]\n"
"s_cselect_b32 s54, 0, s54\ns_sub_u32 s54, s54, s51\nds_read_b128 v[48:51], v34 offset:24576\ns_waitcnt lgkmcnt(3)\n"
"v_mfma_f32_16x16x32_bf16 v[104:107], v[216:219], v[52:55], v[104:107]\nv_mfma_f32_16x16x32_bf16 v[108:111], v[220:223], v[52:55], v[108:111]\nds_read_b128 v[52:55], v35 offset:24576\ns_waitcnt lgkmcnt(3)\n"
"v_mfma_f32_16x16x32_bf16 v[96:99], v[224:227], v[40:43], v[96:99]\nv_add_u32 v32, s54, v32\nv_mfma_f32_16x16x32_bf16 v[100:103], v[228:231], v[40:43], v[100:103]\nds_read_b128 v[40:43], v32 offset:0\n"
"s_waitcnt lgkmcnt(3)\nv_mfma_f32_16x16x32_bf16 v[104:107], v[224:227], v[44:47], v[104:107]\nv_add_u32 v33, s54, v33\nv_mfma_f32_16x16x32_bf16 v[108:111], v[228:231], v[44:47], v[108:111]\n"
"ds_read_b128 v[44:47], v33 offset:0\ns_waitcnt lgkmcnt(3)\nv_mfma_f32_16x16x32_bf16 v[96:99], v[232:235], v[48:51], v[96:99]\nv_add_u32 v34, s54, v34\n"
"v_mfma_f32_16x16x32_bf16 v[100:103], v[236:239], v[48:51], v[100:103]\nds_read_b128 v[48:51], v34 offset:0\ns_waitcnt lgkmcnt(3)\nv_mfma_f32_16x16x32_bf16 v[104:107], v[232:235], v[52:55], v[104:107]\n"
"v_add_u32 v35, s54, v35\nv_mfma_f32_16x16x32_bf16 v[108:111], v[236:239], v[52:55], v[108:111]\nds_read_b128 v[52:55], v35 offset:0\ns_mov_b32 s52, s47\n"
"s_add_u32 s47, s51, s32\ns_add_u32 s51, s51, s54\ns_add_u32 s49, s49, 28672\ns_cmp_lg_u32 s34, 0\n"
"s_cbranch_scc1 fg0_stay\ns_nop 7\nv_cvt_pk_bf16_f32 v56, v96, v97\nv_cvt_pk_bf16_f32 v57, v98, v99\n"
"v_cvt_pk_bf16_f32 v58, v100, v101\nv_cvt_pk_bf16_f32 v59, v102, v103\ns_nop 0\ns_nop 0\n"
"buffer_store_dwordx4 v[56:59], v15, s[20:23], s50 offen sc1\nv_cvt_pk_bf16_f32 v60, v104, v105\nv_cvt_pk_bf16_f32 v61, v106, v107\nv_cvt_pk_bf16_f32 v62, v108, v109\n"
"v_cvt_pk_bf16_f32 v63, v110, v111\ns_nop 0\ns_nop 0\nbuffer_store_dwordx4 v[60:63], v16, s[20:23], s50 offen sc1\n"
"s_add_u32 s48, s28, 64\nv_mov_b32 v36, s48\ns_waitcnt vmcnt(0) lgkmcnt(0)\ns_barrier\n"
"buffer_store_dword v36, v19, s[24:27], s33 offen sc1\ns_branch fg0_end\nfg0_stay:\ns_waitcnt vmcnt(0)\n"
"s_barrier\ns_waitcnt lgkmcnt(3)\nv_mfma_f32_16x16x32_bf16 v[64:67], v[128:131], v[40:43], 0\nv_mov_b32 v36, s48\n"
"s_add_u32 s48, s48, 1\ns_nop 0\nbuffer_store_dword v36, v19, s[24:27], s33 offen sc1\nv_mfma_f32_16x16x32_bf16 v[68:71], v[132:135], v[40:43], 0\n"
"v_mfma_f32_16x16x32_bf16 v[72:75], a[0:3], v[40:43], 0\nv_mfma_f32_16x16x32_bf16 v[76:79], a[4:7], v[40:43], 0\nds_read_b128 v[40:43], v32 offset:4096\nv_cvt_pk_bf16_f32 v56, v96, v97\n"
"s_waitcnt lgkmcnt(3)\nv_mfma_f32_16x16x32_bf16 v[80:83], v[128:131], v[44:47], 0\nv_cvt_pk_bf16_f32 v57, v98, v99\nv_mfma_f32_16x16x32_bf16 v[84:87], v[132:135], v[44:47], 0\n"
"v_cvt_pk_bf16_f32 v58, v100, v101\nv_mfma_f32_16x16x32_bf16 v[88:91], a[0:3], v[44:47], 0\nv_cvt_pk_bf16_f32 v59, v102, v103\nv_mfma_f32_16x16x32_bf16 v[92:95], a[4:7], v[44:47], 0\n"
"ds_read_b128 v[44:47], v33 offset:4096\ns_waitcnt lgkmcnt(3)\nv_mfma_f32_16x16x32_bf16 v[64:67], v[136:139], v[48:51], v[64:67]\nv_mfma_f32_16x16x32_bf16 v[68:71], v[140:143], v[48:51], v[68:71]\n"
"buffer_store_dwordx4 v[56:59], v15, s[20:23], s50 offen sc1\nv_mfma_f32_16x16x32_bf16 v[72:75], a[8:11], v[48:51], v[72:75]\nv_cvt_pk_bf16_f32 v60, v104, v105\nv_mfma_f32_16x16x32_bf16 v[76:79], a[12:15], v[48:51], v[76:79]\n"
"ds_read_b128 v[48:51], v34 offset:4096\nv_cvt_pk_bf16_f32 v61, v106, v107\ns_waitcnt lgkmcnt(3)\nv_mfma_f32_16x16x32_bf16 v[80:83], v[136:139], v[52:55], v[80:83]\n"
"v_cvt_pk_bf16_f32 v62, v108, v109\nv_mfma_f32_16x16x32_bf16 v[84:87], v[140:143], v[52:55], v[84:87]\nv_cvt_pk_bf16_f32 v63, v110, v111\nv_mfma_f32_16x16x32_bf16 v[88:91], a[8:11], v[52:55], v[88:91]\n"
"v_mfma_f32_16x16x32_bf16 v[92:95], a[12:15], v[52:55], v[92:95]\nds_read_b128 v[52:55], v35 offset:4096\ns_waitcnt lgkmcnt(3)\nv_mfma_f32_16x16x32_bf16 v[64:67], v[144:147], v[40:43], v[64:67]\n"
"buffer_store_dwordx4 v[60:63], v16, s[20:23], s50 offen sc1\nv_mfma_f32_16x16x32_bf16 v[68:71], v[148:151], v[40:43], v[68:71]\ns_add_u32 s50, s50, s36\nv_mfma_f32_16x16x32_bf16 v[72:75], a[16:19], v[40:43], v[72:75]\n"
"s_add_u32 m0, s52, 0\nv_mfma_f32_16x16x32_bf16 v[76:79], a[20:23], v[40:43], v[76:79]\nds_read_b128 v[40:43], v32 offset:8192\ns_add_u32 s53, s49, 0\n"
"s_waitcnt lgkmcnt(3)\nv_mfma_f32_16x16x32_bf16 v[80:83], v[144:147], v[44:47], v[80:83]\nbuffer_load_dwordx4 v10, s[12:15], s53 offen lds\nv_mfma_f32_16x16x32_bf16 v[84:87], v[148:151], v[44:47], v[84:87]\n"
"v_mfma_f32_16x16x32_bf16 v[88:91], a[16:19], v[44:47], v[88:91]\nv_mfma_f32_16x16x32_bf16 v[92:95], a[20:23], v[44:47], v[92:95]\nds_read_b128 v[44:47], v33 offset:8192\ns_waitcnt lgkmcnt(3)\n"
"v_mfma_f32_16x16x32_bf16 v[64:67], v[152:155], v[48:51], v[64:67]\nv_mfma_f32_16x16x32_bf16 v[68:71], v[156:159], v[48:51], v[68:71]\nv_mfma_f32_16x16x32_bf16 v[72:75], a[24:27], v[48:51], v[72:75]\nv_mfma_f32_16x16x32_bf16 v[76:79], a[28:31], v[48:51], v[76:79]\n"
"ds_read_b128 v[48:51], v34 offset:8192\ns_waitcnt lgkmcnt(3)\nv_mfma_f32_16x16x32_bf16 v[80:83], v[152:155], v[52:55], v[80:83]\nv_mfma_f32_16x16x32_bf16 v[84:87], v[156:159], v[52:55], v[84:87]\n"
"s_add_u32 m0, s52, 4096\nv_mfma_f32_16x16x32_bf16 v[88:91], a[24:27], v[52:55], v[88:91]\ns_add_u32 s53, s49, 128\nv_mfma_f32_16x16x32_bf16 v[92:95], a[28:31], v[52:55], v[92:95]\n"
"ds_read_b128 v[52:55], v35 offset:8192\nbuffer_load_dwordx4 v10, s[12:15], s53 offen lds\ns_waitcnt lgkmcnt(3)\nv_mfma_f32_16x16x32_bf16 v[64:67], v[160:163], v[40:43], v[64:67]\n"
"v_mfma_f32_16x16x32_bf16 v[68:71], v[164:167], v[40:43], v[68:71]\nv_mfma_f32_16x16x32_bf16 v[72:75], a[32:35], v[40:43], v[72:75]\nv_mfma_f32_16x16x32_bf16 v[76:79], a[36:39], v[40:43], v[76:79]\nds_read_b128 v[40:43], v32 offset:12288\n"
"s_waitcnt lgkmcnt(3)\nv_mfma_f32_16x16x32_bf16 v[80:83], v[160:163], v[44:47], v[80:83]\nv_mfma_f32_16x16x32_bf16 v[84:87], v[164:167], v[44:47], v[84:87]\nv_mfma_f32_16x16x32_bf16 v[88:91], a[32:35], v[44:47], v[88:91]\n"
"v_mfma_f32_16x16x32_bf16 v[92:95], a[36:39], v[44:47], v[92:95]\nds_read_b128 v[44:47], v33 offset:12288\ns_waitcnt lgkmcnt(3)\nv_mfma_f32_16x16x32_bf16 v[64:67], v[168:171], v[48:51], v[64:67]\n"
"s_add_u32 m0, s52, 8192\nv_mfma_f32_16x16x32_bf16 v[68:71], v[172:175], v[48:51], v[68:71]\ns_add_u32 s53, s49, 256\nv_mfma_f32_16x16x32_bf16 v[72:75], a[40:43], v[48:51], v[72:75]\n"
"buffer_load_dwordx4 v10, s[12:15], s53 offen lds\nv_mfma_f32_16x16x32_bf16 v[76:79], a[44:47], v[48:51], v[76:79]\nds_read_b128 v[48:51], v34 offset:12288\ns_waitcnt lgkmcnt(3)\n"
"v_mfma_f32_16x16x32_bf16 v[80:83], v[168:171], v[52:55], v[80:83]\nv_mfma_f32_16x16x32_bf16 v[84:87], v[172:175], v[52:55], v[84:87]\nv_mfma_f32_16x16x32_bf16 v[88:91], a[40:43], v[52:55], v[88:91]\nv_mfma_f32_16x16x32_bf16 v[92:95], a[44:47], v[52:55], v[92:95]\n"
"ds_read_b128 v[52:55], v35 offset:12288\ns_waitcnt lgkmcnt(3)\nv_mfma_f32_16x16x32_bf16 v[64:67], v[176:179], v[40:43], v[64:67]\nv_mfma_f32_16x16x32_bf16 v[68:71], v[180:183], v[40:43], v[68:71]\n"
"v_mfma_f32_16x16x32_bf16 v[72:75], a[48:51], v[40:43], v[72:75]\nv_mfma_f32_16x16x32_bf16 v[76:79], a[52:55], v[40:43], v[76:79]\nds_read_b128 v[40:43], v32 offset:16384\ns_add_u32 m0, s52, 12288\n"
"s_waitcnt lgkmcnt(3)\nv_mfma_f32_16x16x32_bf16 v[80:83], v[176:179], v[44:47], v[80:83]\ns_add_u32 s53, s49, 384\nv_mfma_f32_16x16x32_bf16 v[84:87], v[180:183], v[44:47], v[84:87]\n"
"buffer_load_dwordx4 v10, s[12:15], s53 offen lds\nv_mfma_f32_16x16x32_bf16 v[88:91], a[48:51], v[44:47], v[88:91]\nv_mfma_f32_16x16x32_bf16 v[92:95], a[52:55], v[44:47], v[92:95]\nds_read_b128 v[44:47], v33 offset:16384\n"
"s_waitcnt lgkmcnt(3)\nv_mfma_f32_16x16x32_bf16 v[64:67], v[184:187], v[48:51], v[64:67]\nv_mfma_f32_16x16x32_bf16 v[68:71], v[188:191], v[48:51], v[68:71]\nv_mfma_f32_16x16x32_bf16 v[72:75], a[56:59], v[48:51], v[72:75]\n"
"v_mfma_f32_16x16x32_bf16 v[76:79], a[60:63], v[48:51], v[76:79]\nds_read_b128 v[48:51], v34 offset:16384\ns_waitcnt lgkmcnt(3)\nv_mfma_f32_16x16x32_bf16 v[80:83], v[184:187], v[52:55], v[80:83]\n"
"v_mfma_f32_16x16x32_bf16 v[84:87], v[188:191], v[52:55], v[84:87]\nv_mfma_f32_16x16x32_bf16 v[88:91], a[56:59], v[52:55], v[88:91]\ns_add_u32 m0, s52, 16384\nv_mfma_f32_16x16x32_bf16 v[92:95], a[60:63], v[52:55], v[92:95]\n"
"ds_read_b128 v[52:55], v35 offset:16384\ns_add_u32 s53, s49, 512\ns_waitcnt lgkmcnt(3)\nv_mfma_f32_16x16x32_bf16 v[64:67], v[192:195], v[40:43], v[64:67]\n"
"buffer_load_dwordx4 v10, s[12:15], s53 offen lds\nv_mfma_f32_16x16x32_bf16 v[68:71], v[196:199], v[40:43], v[68:71]\nv_mfma_f32_16x16x32_bf16 v[72:75], a[64:67], v[40:43], v[72:75]\nv_mfma_f32_16x16x32_bf16 v[76:79], a[68:71], v[40:43], v[76:79]\n"
"ds_read_b128 v[40:43], v32 offset:20480\ns_waitcnt lgkmcnt(3)\nv_mfma_f32_16x16x32_bf16 v[80:83], v[192:195], v[44:47], v[80:83]\nv_mfma_f32_16x16x32_bf16 v[84:87], v[196:199], v[44:47], v[84:87]\n"
"v_mfma_f32_16x16x32_bf16 v[88:91], a[64:67], v[44:47], v[88:91]\nv_mfma_f32_16x16x32_bf16 v[92:95], a[68:71], v[44:47], v[92:95]\nds_read_b128 v[44:47], v33 offset:20480\ns_waitcnt lgkmcnt(3)\n"
"v_mfma_f32_16x16x32_bf16 v[64:67], v[200:203], v[48:51], v[64:67]\nv_mfma_f32_16x16x32_bf16 v[68:71], v[204:207], v[48:51], v[68:71]\ns_add_u32 m0, s52, 20480\nv_mfma_f32_16x16x32_bf16 v[72:75], a[72:75], v[48:51], v[72:75]\n"
"s_add_u32 s53, s49, 640\nv_mfma_f32_16x16x32_bf16 v[76:79], a[76:79], v[48:51], v[76:79]\nds_read_b128 v[48:51], v34 offset:20480\nbuffer_load_dwordx4 v10, s[12:15], s53 offen lds\n"
"s_waitcnt lgkmcnt(3)\nv_mfma_f32_16x16x32_bf16 v[80:83], v[200:203], v[52:55], v[80:83]\nv_mfma_f32_16x16x32_bf16 v[84:87], v[204:207], v[52:55], v[84:87]\nv_mfma_f32_16x16x32_bf16 v[88:91], a[72:75], v[52:55], v[88:91]\n"
"v_mfma_f32_16x16x32_bf16 v[92:95], a[76:79], v[52:55], v[92:95]\nds_read_b128 v[52:55], v35 offset:20480\ns_waitcnt lgkmcnt(3)\nv_mfma_f32_16x16x32_bf16 v[64:67], v[208:211], v[40:43], v[64:67]\n"
"v_mfma_f32_16x16x32_bf16 v[68:71], v[212:215], v[40:43], v[68:71]\nv_mfma_f32_16x16x32_bf16 v[72:75], a[80:83], v[40:43], v[72:75]\nv_mfma_f32_16x16x32_bf16 v[76:79], a[84:87], v[40:43], v[76:79]\nds_read_b128 v[40:43], v32 offset:24576\n"
"s_waitcnt lgkmcnt(3)\nv_mfma_f32_16x16x32_bf16 v[80:83], v[208:211], v[44:47], v[80:83]\ns_add_u32 m0, s52, 24576\nv_mfma_f32_16x16x32_bf16 v[84:87], v[212:215], v[44:47], v[84:87]\n"
"s_add_u32 s53, s49, 768\nv_mfma_f32_16x16x32_bf16 v[88:91], a[80:83], v[44:47], v[88:91]\nbuffer_load_dwordx4 v10, s[12:15], s53 offen lds\nv_mfma_f32_16x16x32_bf16 v[92:95], a[84:87], v[44:47], v[92:95]\n"
"ds_read_b128 v[44:47], v33 offset:24576\ns_waitcnt lgkmcnt(3)\nv_mfma_f32_16x16x32_bf16 v[64:67], v[216:219], v[48:51], v[64:67]\ns_add_u32 s54, s51, 32768\n"
"s_cmp_eq_u32 s54, 163840\nv_mfma_f32_16x16x32_bf16 v[68:71], v[220:223], v[48:51], v[68:71]\nv_mfma_f32_16x16x32_bf16 v[72:75], a[88:91], v[48:51], v[72:75]\nv_mfma_f32_16x16x32_bf16 v[76:79], a[92:95], v[48:51], v[76:79]\n"
"s_cselect_b32 s54, 0, s54\ns_sub_u32 s54, s54, s51\nds_read_b128 v[48:51], v34 offset:24576\ns_waitcnt lgkmcnt(3)\n"
"v_mfma_f32_16x16x32_bf16 v[80:83], v[216:219], v[52:55], v[80:83]\nv_mfma_f32_16x16x32_bf16 v[84:87], v[220:223], v[52:55], v[84:87]\nv_mfma_f32_16x16x32_bf16 v[88:91], a[88:91], v[52:55], v[88:91]\nv_mfma_f32_16x16x32_bf16 v[92:95], a[92:95], v[52:55], v[92:95]\n"
"ds_read_b128 v[52:55], v35 offset:24576\ns_waitcnt lgkmcnt(3)\nv_mfma_f32_16x16x32_bf16 v[64:67], v[224:227], v[40:43], v[64:67]\nv_add_u32 v32, s54, v32\n"
"v_mfma_f32_16x16x32_bf16 v[68:71], v[228:231], v[40:43], v[68:71]\nv_mfma_f32_16x16x32_bf16 v[72:75], a[96:99], v[40:43], v[72:75]\nv_mfma_f32_16x16x32_bf16 v[76:79], a[100:103], v[40:43], v[76:79]\nds_read_b128 v[40:43], v32 offset:0\n"
"s_waitcnt lgkmcnt(3)\nv_mfma_f32_16x16x32_bf16 v[80:83], v[224:227], v[44:47], v[80:83]\nv_add_u32 v33, s54, v33\nv_mfma_f32_16x16x32_bf16 v[84:87], v[228:231], v[44:47], v[84:87]\n"
"v_mfma_f32_16x16x32_bf16 v[88:91], a[96:99], v[44:47], v[88:91]\nv_mfma_f32_16x16x32_bf16 v[92:95], a[100:103], v[44:47], v[92:95]\nds_read_b128 v[44:47], v33 offset:0\ns_waitcnt lgkmcnt(3)\n"
"v_mfma_f32_16x16x32_bf16 v[64:67], v[232:235], v[48:51], v[64:67]\nv_add_u32 v34, s54, v34\nv_mfma_f32_16x16x32_bf16 v[68:71], v[236:239], v[48:51], v[68:71]\nv_mfma_f32_16x16x32_bf16 v[72:75], a[104:107], v[48:51], v[72:75]\n"
"v_mfma_f32_16x16x32_bf16 v[76:79], a[108:111], v[48:51], v[76:79]\nds_read_b128 v[48:51], v34 offset:0\ns_waitcnt lgkmcnt(3)\nv_mfma_f32_16x16x32_bf16 v[80:83], v[232:235], v[52:55], v[80:83]\n"
"v_add_u32 v35, s54, v35\nv_mfma_f32_16x16x32_bf16 v[84:87], v[236:239], v[52:55], v[84:87]\nv_mfma_f32_16x16x32_bf16 v[88:91], a[104:107], v[52:55], v[88:91]\nv_mfma_f32_16x16x32_bf16 v[92:95], a[108:111], v[52:55], v[92:95]\n"
"ds_read_b128 v[52:55], v35 offset:0\ns_mov_b32 s52, s47\ns_add_u32 s47, s51, s32\ns_add_u32 s51, s51, s54\n"
"s_add_u32 s49, s49, 28672\ns_waitcnt vmcnt(7)\ns_barrier\ns_waitcnt lgkmcnt(3)\n"
"v_mfma_f32_16x16x32_bf16 v[96:99], v[128:131], v[40:43], 0\nv_mov_b32 v36, s48\ns_add_u32 s48, s48, 1\ns_nop 0\n"
"buffer_store_dword v36, v19, s[24:27], s33 offen sc1\nv_mfma_f32_16x16x32_bf16 v[100:103], v[132:135], v[40:43], 0\nv_mfma_f32_16x16x32_bf16 v[104:107], a[0:3], v[40:43], 0\nv_mfma_f32_16x16x32_bf16 v[108:111], a[4:7], v[40:43], 0\n"
"ds_read_b128 v[40:43], v32 offset:4096\nv_cvt_pk_bf16_f32 v56, v64, v65\ns_waitcnt lgkmcnt(3)\nv_mfma_f32_16x16x32_bf16 v[112:115], v[128:131], v[44:47], 0\n"
"v_cvt_pk_bf16_f32 v57, v66, v67\nv_mfma_f32_16x16x32_bf16 v[116:119], v[132:135], v[44:47], 0\nv_cvt_pk_bf16_f32 v58, v68, v69\nv_mfma_f32_16x16x32_bf16 v[120:123], a[0:3], v[44:47], 0\n"
"v_cvt_pk_bf16_f32 v59, v70, v71\nv_mfma_f32_16x16x32_bf16 v[124:127], a[4:7], v[44:47], 0\nds_read_b128 v[44:47], v33 offset:4096\ns_waitcnt lgkmcnt(3)\n"
"v_mfma_f32_16x16x32_bf16 v[96:99], v[136:139], v[48:51], v[96:99]\nv_mfma_f32_16x16x32_bf16 v[100:103], v[140:143], v[48:51], v[100:103]\nbuffer_store_dwordx4 v[56:59], v15, s[20:23], s50 offen sc1\nv_mfma_f32_16x16x32_bf16 v[104:107], a[8:11], v[48:51], v[104:107]\n"
"v_cvt_pk_bf16_f32 v60, v80, v81\nv_mfma_f32_16x16x32_bf16 v[108:111], a[12:15], v[48:51], v[108:111]\nds_read_b128 v[48:51], v34 offset:4096\nv_cvt_pk_bf16_f32 v61, v82, v83\n"
"s_waitcnt lgkmcnt(3)\nv_mfma_f32_16x16x32_bf16 v[112:115], v[136:139], v[52:55], v[112:115]\nv_cvt_pk_bf16_f32 v62, v84, v85\nv_mfma_f32_16x16x32_bf16 v[116:119], v[140:143], v[52:55], v[116:119]\n"
"v_cvt_pk_bf16_f32 v63, v86, v87\nv_mfma_f32_16x16x32_bf16 v[120:123], a[8:11], v[52:55], v[120:123]\nv_mfma_f32_16x16x32_bf16 v[124:127], a[12:15], v[52:55], v[124:127]\nds_read_b128 v[52:55], v35 offset:4096\n"
"s_waitcnt lgkmcnt(3)\nv_mfma_f32_16x16x32_bf16 v[96:99], v[144:147], v[40:43], v[96:99]\nbuffer_store_dwordx4 v[60:63], v16, s[20:23], s50 offen sc1\nv_mfma_f32_16x16x32_bf16 v[100:103], v[148:151], v[40:43], v[100:103]\n"
"v_cvt_pk_bf16_f32 v56, v72, v73\nv_mfma_f32_16x16x32_bf16 v[104:107], a[16:19], v[40:43], v[104:107]\nv_cvt_pk_bf16_f32 v57, v74, v75\nv_mfma_f32_16x16x32_bf16 v[108:111], a[20:23], v[40:43], v[108:111]\n"
"ds_read_b128 v[40:43], v32 offset:8192\nv_cvt_pk_bf16_f32 v58, v76, v77\ns_waitcnt lgkmcnt(3)\nv_mfma_f32_16x16x32_bf16 v[112:115], v[144:147], v[44:47], v[112:115]\n"
"v_cvt_pk_bf16_f32 v59, v78, v79\nv_mfma_f32_16x16x32_bf16 v[116:119], v[148:151], v[44:47], v[116:119]\nv_mfma_f32_16x16x32_bf16 v[120:123], a[16:19], v[44:47], v[120:123]\nv_mfma_f32_16x16x32_bf16 v[124:127], a[20:23], v[44:47], v[124:127]\n"
"ds_read_b128 v[44:47], v33 offset:8192\nbuffer_store_dwordx4 v[56:59], v17, s[20:23], s50 offen sc1\ns_waitcnt lgkmcnt(3)\nv_mfma_f32_16x16x32_bf16 v[96:99], v[152:155], v[48:51], v[96:99]\n"
"v_cvt_pk_bf16_f32 v60, v88, v89\nv_mfma_f32_16x16x32_bf16 v[100:103], v[156:159], v[48:51], v[100:103]\nv_cvt_pk_bf16_f32 v61, v90, v91\nv_mfma_f32_16x16x32_bf16 v[104:107], a[24:27], v[48:51], v[104:107]\n"
"v_cvt_pk_bf16_f32 v62, v92, v93\nv_mfma_f32_16x16x32_bf16 v[108:111], a[28:31], v[48:51], v[108:111]\nds_read_b128 v[48:51], v34 offset:8192\nv_cvt_pk_bf16_f32 v63, v94, v95\n"
"s_waitcnt lgkmcnt(3)\nv_mfma_f32_16x16x32_bf16 v[112:115], v[152:155], v[52:55], v[112:115]\nv_mfma_f32_16x16x32_bf16 v[116:119], v[156:159], v[52:55], v[116:119]\nv_mfma_f32_16x16x32_bf16 v[120:123], a[24:27], v[52:55], v[120:123]\n"
"buffer_store_dwordx4 v[60:63], v18, s[20:23], s50 offen sc1\nv_mfma_f32_16x16x32_bf16 v[124:127], a[28:31], v[52:55], v[124:127]\nds_read_b128 v[52:55], v35 offset:8192\ns_add_u32 s50, s50, s36\n"
"s_waitcnt lgkmcnt(3)\nv_mfma_f32_16x16x32_bf16 v[96:99], v[160:163], v[40:43], v[96:99]\ns_add_u32 m0, s52, 0\nv_mfma_f32_16x16x32_bf16 v[100:103], v[164:167], v[40:43], v[100:103]\n"
"s_add_u32 s53, s49, 0\nv_mfma_f32_16x16x32_bf16 v[104:107], a[32:35], v[40:43], v[104:107]\nbuffer_load_dwordx4 v10, s[12:15], s53 offen lds\nv_mfma_f32_16x16x32_bf16 v[108:111], a[36:39], v[40:43], v[108:111]\n"
"ds_read_b128 v[40:43], v32 offset:12288\ns_waitcnt lgkmcnt(3)\nv_mfma_f32_16x16x32_bf16 v[112:115], v[160:163], v[44:47], v[112:115]\nv_mfma_f32_16x16x32_bf16 v[116:119], v[164:167], v[44:47], v[116:119]\n"
"v_mfma_f32_16x16x32_bf16 v[120:123], a[32:35], v[44:47], v[120:123]\nv_mfma_f32_16x16x32_bf16 v[124:127], a[36:39], v[44:47], v[124:127]\nds_read_b128 v[44:47], v33 offset:12288\ns_waitcnt lgkmcnt(3)\n"
"v_mfma_f32_16x16x32_bf16 v[96:99], v[168:171], v[48:51], v[96:99]\nv_mfma_f32_16x16x32_bf16 v[100:103], v[172:175], v[48:51], v[100:103]\nv_mfma_f32_16x16x32_bf16 v[104:107], a[40:43], v[48:51], v[104:107]\nv_mfma_f32_16x16x32_bf16 v[108:111], a[44:47], v[48:51], v[108:111]\n"
"ds_read_b128 v[48:51], v34 offset:12288\ns_add_u32 m0, s52, 4096\ns_waitcnt lgkmcnt(3)\nv_mfma_f32_16x16x32_bf16 v[112:115], v[168:171], v[52:55], v[112:115]\n"
"s_add_u32 s53, s49, 128\nv_mfma_f32_16x16x32_bf16 v[116:119], v[172:175], v[52:55], v[116:119]\nbuffer_load_dwordx4 v10, s[12:15], s53 offen lds\nv_mfma_f32_16x16x32_bf16 v[120:123], a[40:43], v[52:55], v[120:123]\n"
"v_mfma_f32_16x16x32_bf16 v[124:127], a[44:47], v[52:55], v[124:127]\nds_read_b128 v[52:55], v35 offset:12288\ns_waitcnt lgkmcnt(3)\nv_mfma_f32_16x16x32_bf16 v[96:99], v[176:179], v[40:43], v[96:99]\n"
"v_mfma_f32_16x16x32_bf16 v[100:103], v[180:183], v[40:43], v[100:103]\nv_mfma_f32_16x16x32_bf16 v[104:107], a[48:51], v[40:43], v[104:107]\nv_mfma_f32_16x16x32_bf16 v[108:111], a[52:55], v[40:43], v[108:111]\nds_read_b128 v[40:43], v32 offset:16384\n"
"s_waitcnt lgkmcnt(3)\nv_mfma_f32_16x16x32_bf16 v[112:115], v[176:179], v[44:47], v[112:115]\nv_mfma_f32_16x16x32_bf16 v[116:119], v[180:183], v[44:47], v[116:119]\nv_mfma_f32_16x16x32_bf16 v[120:123], a[48:51], v[44:47], v[120:123]\n"
"s_add_u32 m0, s52, 8192\nv_mfma_f32_16x16x32_bf16 v[124:127], a[52:55], v[44:47], v[124:127]\nds_read_b128 v[44:47], v33 offset:16384\ns_add_u32 s53, s49, 256\n"
"s_waitcnt lgkmcnt(3)\nv_mfma_f32_16x16x32_bf16 v[96:99], v[184:187], v[48:51], v[96:99]\nbuffer_load_dwordx4 v10, s[12:15], s53 offen lds\nv_mfma_f32_16x16x32_bf16 v[100:103], v[188:191], v[48:51], v[100:103]\n"
"v_mfma_f32_16x16x32_bf16 v[104:107], a[56:59], v[48:51], v[104:107]\nv_mfma_f32_16x16x32_bf16 v[108:111], a[60:63], v[48:51], v[108:111]\nds_read_b128 v[48:51], v34 offset:16384\ns_waitcnt lgkmcnt(3)\n"
"v_mfma_f32_16x16x32_bf16 v[112:115], v[184:187], v[52:55], v[112:115]\nv_mfma_f32_16x16x32_bf16 v[116:119], v[188:191], v[52:55], v[116:119]\nv_mfma_f32_16x16x32_bf16 v[120:123], a[56:59], v[52:55], v[120:123]\nv_mfma_f32_16x16x32_bf16 v[124:127], a[60:63], v[52:55], v[124:127]\n"
"ds_read_b128 v[52:55], v35 offset:16384\ns_waitcnt lgkmcnt(3)\nv_mfma_f32_16x16x32_bf16 v[96:99], v[192:195], v[40:43], v[96:99]\nv_mfma_f32_16x16x32_bf16 v[100:103], v[196:199], v[40:43], v[100:103]\n"
"s_add_u32 m0, s52, 12288\nv_mfma_f32_16x16x32_bf16 v[104:107], a[64:67], v[40:43], v[104:107]\ns_add_u32 s53, s49, 384\nv_mfma_f32_16x16x32_bf16 v[108:111], a[68:71], v[40:43], v[108:111]\n"
"ds_read_b128 v[40:43], v32 offset:20480\nbuffer_load_dwordx4 v10, s[12:15], s53 offen lds\ns_waitcnt lgkmcnt(3)\nv_mfma_f32_16x16x32_bf16 v[112:115], v[192:195], v[44:47], v[112:115]\n"
"v_mfma_f32_16x16x32_bf16 v[116:119], v[196:199], v[44:47], v[116:119]\nv_mfma_f32_16x16x32_bf16 v[120:123], a[64:67], v[44:47], v[120:123]\nv_mfma_f32_16x16x32_bf16 v[124:127], a[68:71], v[44:47], v[124:127]\nds_read_b128 v[44:47], v33 offset:20480\n"
"s_waitcnt lgkmcnt(3)\nv_mfma_f32_16x16x32_bf16 v[96:99], v[200:203], v[48:51], v[96:99]\nv_mfma_f32_16x16x32_bf16 v[100:103], v[204:207], v[48:51], v[100:103]\nv_mfma_f32_16x16x32_bf16 v[104:107], a[72:75], v[48:51], v[104:107]\n"
"v_mfma_f32_16x16x32_bf16 v[108:111], a[76:79], v[48:51], v[108:111]\nds_read_b128 v[48:51], v34 offset:20480\ns_waitcnt lgkmcnt(3)\nv_mfma_f32_16x16x32_bf16 v[112:115], v[200:203], v[52:55], v[112:115]\n"
"s_add_u32 m0, s52, 16384\nv_mfma_f32_16x16x32_bf16 v[116:119], v[204:207], v[52:55], v[116:119]\ns_add_u32 s53, s49, 512\nv_mfma_f32_16x16x32_bf16 v[120:123], a[72:75], v[52:55], v[120:123]\n"
"buffer_load_dwordx4 v10, s[12:15], s53 offen lds\nv_mfma_f32_16x16x32_bf16 v[124:127], a[76:79], v[52:55], v[124:127]\nds_read_b128 v[52:55], v35 offset:20480\ns_waitcnt lgkmcnt(3)\n"
"v_mfma_f32_16x16x32_bf16 v[96:99], v[208:211], v[40:43], v[96:99]\nv_mfma_f32_16x16x32_bf16 v[100:103], v[212:215], v[40:43], v[100:103]\nv_mfma_f32_16x16x32_bf16 v[104:107], a[80:83], v[40:43], v[104:107]\nv_mfma_f32_16x16x32_bf16 v[108:111], a[84:87], v[40:43], v[108:111]\n"
"ds_read_b128 v[40:43], v32 offset:24576\ns_waitcnt lgkmcnt(3)\nv_mfma_f32_16x16x32_bf16 v[112:115], v[208:211], v[44:47], v[112:115]\nv_mfma_f32_16x16x32_bf16 v[116:119], v[212:215], v[44:47], v[116:119]\n"
"v_mfma_f32_16x16x32_bf16 v[120:123], a[80:83], v[44:47], v[120:123]\nv_mfma_f32_16x16x32_bf16 v[124:127], a[84:87], v[44:47], v[124:127]\nds_read_b128 v[44:47], v33 offset:24576\ns_add_u32 m0, s52, 20480\n"
"s_waitcnt lgkmcnt(3)\nv_mfma_f32_16x16x32_bf16 v[96:99], v[216:219], v[48:51], v[96:99]\ns_add_u32 s54, s51, 32768\ns_cmp_eq_u32 s54, 163840\n"
"v_mfma_f32_16x16x32_bf16 v[100:103], v[220:223], v[48:51], v[100:103]\nv_mfma_f32_16x16x32_bf16 v[104:107], a[88:91], v[48:51], v[104:107]\nv_mfma_f32_16x16x32_bf16 v[108:111], a[92:95], v[48:51], v[108:111]\ns_cselect_b32 s54, 0, s54\n"
"s_sub_u32 s54, s54, s51\nds_read_b128 v[48:51], v34 offset:24576\ns_add_u32 s53, s49, 640\ns_waitcnt lgkmcnt(3)\n"
"v_mfma_f32_16x16x32_bf16 v[112:115], v[216:219], v[52:55], v[112:115]\nbuffer_load_dwordx4 v10, s[12:15], s53 offen lds\nv_mfma_f32_16x16x32_bf16 v[116:119], v[220:223], v[52:55], v[116:119]\nv_mfma_f32_16x16x32_bf16 v[120:123], a[88:91], v[52:55], v[120:123]\n"
"v_mfma_f32_16x16x32_bf16 v[124:127], a[92:95], v[52:55], v[124:127]\nds_read_b128 v[52:55], v35 offset:24576\ns_waitcnt lgkmcnt(3)\nv_mfma_f32_16x16x32_bf16 v[96:99], v[224:227], v[40:43], v[96:99]\n"
"v_add_u32 v32, s54, v32\nv_mfma_f32_16x16x32_bf16 v[100:103], v[228:231], v[40:43], v[100:103]\nv_mfma_f32_16x16x32_bf16 v[104:107], a[96:99], v[40:43], v[104:107]\nv_mfma_f32_16x16x32_bf16 v[108:111], a[100:103], v[40:43], v[108:111]\n"
"ds_read_b128 v[40:43], v32 offset:0\ns_waitcnt lgkmcnt(3)\nv_mfma_f32_16x16x32_bf16 v[112:115], v[224:227], v[44:47], v[112:115]\nv_add_u32 v33, s54, v33\n"
"v_mfma_f32_16x16x32_bf16 v[116:119], v[228:231], v[44:47], v[116:119]\ns_add_u32 m0, s52, 24576\nv_mfma_f32_16x16x32_bf16 v[120:123], a[96:99], v[44:47], v[120:123]\ns_add_u32 s53, s49, 768\n"
"v_mfma_f32_16x16x32_bf16 v[124:127], a[100:103], v[44:47], v[124:127]\nds_read_b128 v[44:47], v33 offset:0\nbuffer_load_dwordx4 v10, s[12:15], s53 offen lds\ns_waitcnt lgkmcnt(3)\n"
"v_mfma_f32_16x16x32_bf16 v[96:99], v[232:235], v[48:51], v[96:99]\nv_add_u32 v34, s54, v34\nv_mfma_f32_16x16x32_bf16 v[100:103], v[236:239], v[48:51], v[100:103]\nv_mfma_f32_16x16x32_bf16 v[104:107], a[104:107], v[48:51], v[104:107]\n"
"v_mfma_f32_16x16x32_bf16 v[108:111], a[108:111], v[48:51], v[108:111]\nds_read_b128 v[48:51], v34 offset:0\ns_waitcnt lgkmcnt(3)\nv_mfma_f32_16x16x32_bf16 v[112:115], v[232:235], v[52:55], v[112:115]\n"
"v_add_u32 v35, s54, v35\nv_mfma_f32_16x16x32_bf16 v[116:119], v[236:239], v[52:55], v[116:119]\nv_mfma_f32_16x16x32_bf16 v[120:123], a[104:107], v[52:55], v[120:123]\nv_mfma_f32_16x16x32_bf16 v[124:127], a[108:111], v[52:55], v[124:127]\n"
"ds_read_b128 v[52:55], v35 offset:0\ns_mov_b32 s52, s47\ns_add_u32 s47, s51, s32\ns_add_u32 s51, s51, s54\n"
"s_add_u32 s49, s49, 28672\n.p2align 6\nfg0_loop:\ns_waitcnt vmcnt(7)\n"
"s_barrier\ns_waitcnt lgkmcnt(3)\nv_mfma_f32_16x16x32_bf16 v[64:67], v[128:131], v[40:43], 0\nv_mov_b32 v36, s48\n"
"s_add_u32 s48, s48, 1\ns_nop 0\nbuffer_store_dword v36, v19, s[24:27], s33 offen sc1\nv_mfma_f32_16x16x32_bf16 v[68:71], v[132:135], v[40:43], 0\n"
"v_mfma_f32_16x16x32_bf16 v[72:75], a[0:3], v[40:43], 0\nv_mfma_f32_16x16x32_bf16 v[76:79], a[4:7], v[40:43], 0\nds_read_b128 v[40:43], v32 offset:4096\nv_cvt_pk_bf16_f32 v56, v96, v97\n"
"s_waitcnt lgkmcnt(3)\nv_mfma_f32_16x16x32_bf16 v[80:83], v[128:131], v[44:47], 0\nv_cvt_pk_bf16_f32 v57, v98, v99\nv_mfma_f32_16x16x32_bf16 v[84:87], v[132:135], v[44:47], 0\n"
"v_cvt_pk_bf16_f32 v58, v100, v101\nv_mfma_f32_16x16x32_bf16 v[88:91], a[0:3], v[44:47], 0\nv_cvt_pk_bf16_f32 v59, v102, v103\nv_mfma_f32_16x16x32_bf16 v[92:95], a[4:7], v[44:47], 0\n"
"ds_read_b128 v[44:47], v33 offset:4096\ns_waitcnt lgkmcnt(3)\nv_mfma_f32_16x16x32_bf16 v[64:67], v[136:139], v[48:51], v[64:67]\nv_mfma_f32_16x16x32_bf16 v[68:71], v[140:143], v[48:51], v[68:71]\n"
"buffer_store_dwordx4 v[56:59], v15, s[20:23], s50 offen sc1\nv_mfma_f32_16x16x32_bf16 v[72:75], a[8:11], v[48:51], v[72:75]\nv_cvt_pk_bf16_f32 v60, v112, v113\nv_mfma_f32_16x16x32_bf16 v[76:79], a[12:15], v[48:51], v[76:79]\n"
"ds_read_b128 v[48:51], v34 offset:4096\nv_cvt_pk_bf16_f32 v61, v114, v115\ns_waitcnt lgkmcnt(3)\nv_mfma_f32_16x16x32_bf16 v[80:83], v[136:139], v[52:55], v[80:83]\n"
"v_cvt_pk_bf16_f32 v62, v116, v117\nv_mfma_f32_16x16x32_bf16 v[84:87], v[140:143], v[52:55], v[84:87]\nv_cvt_pk_bf16_f32 v63, v118, v119\nv_mfma_f32_16x16x32_bf16 v[88:91], a[8:11], v[52:55], v[88:91]\n"
"v_mfma_f32_16x16x32_bf16 v[92:95], a[12:15], v[52:55], v[92:95]\nds_read_b128 v[52:55], v35 offset:4096\ns_waitcnt lgkmcnt(3)\nv_mfma_f32_16x16x32_bf16 v[64:67], v[144:147], v[40:43], v[64:67]\n"
"buffer_store_dwordx4 v[60:63], v16, s[20:23], s50 offen sc1\nv_mfma_f32_16x16x32_bf16 v[68:71], v[148:151], v[40:43], v[68:71]\nv_cvt_pk_bf16_f32 v56, v104, v105\nv_mfma_f32_16x16x32_bf16 v[72:75], a[16:19], v[40:43], v[72:75]\n"
"v_cvt_pk_bf16_f32 v57, v106, v107\nv_mfma_f32_16x16x32_bf16 v[76:79], a[20:23], v[40:43], v[76:79]\nds_read_b128 v[40:43], v32 offset:8192\nv_cvt_pk_bf16_f32 v58, v108, v109\n"
"s_waitcnt lgkmcnt(3)\nv_mfma_f32_16x16x32_bf16 v[80:83], v[144:147], v[44:47], v[80:83]\nv_cvt_pk_bf16_f32 v59, v110, v111\nv_mfma_f32_16x16x32_bf16 v[84:87], v[148:151], v[44:47], v[84:87]\n"
"v_mfma_f32_16x16x32_bf16 v[88:91], a[16:19], v[44:47], v[88:91]\nv_mfma_f32_16x16x32_bf16 v[92:95], a[20:23], v[44:47], v[92:95]\nds_read_b128 v[44:47], v33 offset:8192\nbuffer_store_dwordx4 v[56:59], v17, s[20:23], s50 offen sc1\n"
"s_waitcnt lgkmcnt(3)\nv_mfma_f32_16x16x32_bf16 v[64:67], v[152:155], v[48:51], v[64:67]\nv_cvt_pk_bf16_f32 v60, v120, v121\nv_mfma_f32_16x16x32_bf16 v[68:71], v[156:159], v[48:51], v[68:71]\n"
"v_cvt_pk_bf16_f32 v61, v122, v123\nv_mfma_f32_16x16x32_bf16 v[72:75], a[24:27], v[48:51], v[72:75]\nv_cvt_pk_bf16_f32 v62, v124, v125\nv_mfma_f32_16x16x32_bf16 v[76:79], a[28:31], v[48:51], v[76:79]\n"
"ds_read_b128 v[48:51], v34 offset:8192\nv_cvt_pk_bf16_f32 v63, v126, v127\ns_waitcnt lgkmcnt(3)\nv_mfma_f32_16x16x32_bf16 v[80:83], v[152:155], v[52:55], v[80:83]\n"
"v_mfma_f32_16x16x32_bf16 v[84:87], v[156:159], v[52:55], v[84:87]\nv_mfma_f32_16x16x32_bf16 v[88:91], a[24:27], v[52:55], v[88:91]\nbuffer_store_dwordx4 v[60:63], v18, s[20:23], s50 offen sc1\nv_mfma_f32_16x16x32_bf16 v[92:95], a[28:31], v[52:55], v[92:95]\n"
"ds_read_b128 v[52:55], v35 offset:8192\ns_add_u32 s50, s50, s36\ns_waitcnt lgkmcnt(3)\nv_mfma_f32_16x16x32_bf16 v[64:67], v[160:163], v[40:43], v[64:67]\n"
"s_add_u32 m0, s52, 0\nv_mfma_f32_16x16x32_bf16 v[68:71], v[164:167], v[40:43], v[68:71]\ns_add_u32 s53, s49, 0\nv_mfma_f32_16x16x32_bf16 v[72:75], a[32:35], v[40:43], v[72:75]\n"
"buffer_load_dwordx4 v10, s[12:15], s53 offen lds\nv_mfma_f32_16x16x32_bf16 v[76:79], a[36:39], v[40:43], v[76:79]\nds_read_b128 v[40:43], v32 offset:12288\ns_waitcnt lgkmcnt(3)\n"
"v_mfma_f32_16x16x32_bf16 v[80:83], v[160:163], v[44:47], v[80:83]\nv_mfma_f32_16x16x32_bf16 v[84:87], v[164:167], v[44:47], v[84:87]\nv_mfma_f32_16x16x32_bf16 v[88:91], a[32:35], v[44:47], v[88:91]\nv_mfma_f32_16x16x32_bf16 v[92:95], a[36:39], v[44:47], v[92:95]\n"
"ds_read_b128 v[44:47], v33 offset:12288\ns_waitcnt lgkmcnt(3)\nv_mfma_f32_16x16x32_bf16 v[64:67], v[168:171], v[48:51], v[64:67]\nv_mfma_f32_16x16x32_bf16 v[68:71], v[172:175], v[48:51], v[68:71]\n"
"v_mfma_f32_16x16x32_bf16 v[72:75], a[40:43], v[48:51], v[72:75]\nv_mfma_f32_16x16x32_bf16 v[76:79], a[44:47], v[48:51], v[76:79]\nds_read_b128 v[48:51], v34 offset:12288\ns_add_u32 m0, s52, 4096\n"
"s_waitcnt lgkmcnt(3)\nv_mfma_f32_16x16x32_bf16 v[80:83], v[168:171], v[52:55], v[80:83]\ns_add_u32 s53, s49, 128\nv_mfma_f32_16x16x32_bf16 v[84:87], v[172:175], v[52:55], v[84:87]\n"
"buffer_load_dwordx4 v10, s[12:15], s53 offen lds\nv_mfma_f32_16x16x32_bf16 v[88:91], a[40:43], v[52:55], v[88:91]\nv_mfma_f32_16x16x32_bf16 v[92:95], a[44:47], v[52:55], v[92:95]\nds_read_b128 v[52:55], v35 offset:12288\n"
"s_waitcnt lgkmcnt(3)\nv_mfma_f32_16x16x32_bf16 v[64:67], v[176:179], v[40:43], v[64:67]\nv_mfma_f32_16x16x32_bf16 v[68:71], v[180:183], v[40:43], v[68:71]\nv_mfma_f32_16x16x32_bf16 v[72:75], a[48:51], v[40:43], v[72:75]\n"
"v_mfma_f32_16x16x32_bf16 v[76:79], a[52:55], v[40:43], v[76:79]\nds_read_b128 v[40:43], v32 offset:16384\ns_waitcnt lgkmcnt(3)\nv_mfma_f32_16x16x32_bf16 v[80:83], v[176:179], v[44:47], v[80:83]\n"
"v_mfma_f32_16x16x32_bf16 v[84:87], v[180:183], v[44:47], v[84:87]\nv_mfma_f32_16x16x32_bf16 v[88:91], a[48:51], v[44:47], v[88:91]\ns_add_u32 m0, s52, 8192\nv_mfma_f32_16x16x32_bf16 v[92:95], a[52:55], v[44:47], v[92:95]\n"
"ds_read_b128 v[44:47], v33 offset:16384\ns_add_u32 s53, s49, 256\ns_waitcnt lgkmcnt(3)\nv_mfma_f32_16x16x32_bf16 v[64:67], v[184:187], v[48:51], v[64:67]\n"
"buffer_load_dwordx4 v10, s[12:15], s53 offen lds\nv_mfma_f32_16x16x32_bf16 v[68:71], v[188:191], v[48:51], v[68:71]\nv_mfma_f32_16x16x32_bf16 v[72:75], a[56:59], v[48:51], v[72:75]\nv_mfma_f32_16x16x32_bf16 v[76:79], a[60:63], v[48:51], v[76:79]\n"
"ds_read_b128 v[48:51], v34 offset:16384\ns_waitcnt lgkmcnt(3)\nv_mfma_f32_16x16x32_bf16 v[80:83], v[184:187], v[52:55], v[80:83]\nv_mfma_f32_16x16x32_bf16 v[84:87], v[188:191], v[52:55], v[84:87]\n"
"v_mfma_f32_16x16x32_bf16 v[88:91], a[56:59], v[52:55], v[88:91]\nv_mfma_f32_16x16x32_bf16 v[92:95], a[60:63], v[52:55], v[92:95]\nds_read_b128 v[52:55], v35 offset:16384\ns_waitcnt lgkmcnt(3)\n"
"v_mfma_f32_16x16x32_bf16 v[64:67], v[192:195], v[40:43], v[64:67]\nv_mfma_f32_16x16x32_bf16 v[68:71], v[196:199], v[40:43], v[68:71]\ns_add_u32 m0, s52, 12288\nv_mfma_f32_16x16x32_bf16 v[72:75], a[64:67], v[40:43], v[72:75]\n"
"s_add_u32 s53, s49, 384\nv_mfma_f32_16x16x32_bf16 v[76:79], a[68:71], v[40:43], v[76:79]\nds_read_b128 v[40:43], v32 offset:20480\nbuffer_load_dwordx4 v10, s[12:15], s53 offen lds\n"
"s_waitcnt lgkmcnt(3)\nv_mfma_f32_16x16x32_bf16 v[80:83], v[192:195], v[44:47], v[80:83]\nv_mfma_f32_16x16x32_bf16 v[84:87], v[196:199], v[44:47], v[84:87]\nv_mfma_f32_16x16x32_bf16 v[88:91], a[64:67], v[44:47], v[88:91]\n"
"v_mfma_f32_16x16x32_bf16 v[92:95], a[68:71], v[44:47], v[92:95]\nds_read_b128 v[44:47], v33 offset:20480\ns_waitcnt lgkmcnt(3)\nv_mfma_f32_16x16x32_bf16 v[64:67], v[200:203], v[48:51], v[64:67]\n"
"v_mfma_f32_16x16x32_bf16 v[68:71], v[204:207], v[48:51], v[68:71]\nv_mfma_f32_16x16x32_bf16 v[72:75], a[72:75], v[48:51], v[72:75]\nv_mfma_f32_16x16x32_bf16 v[76:79], a[76:79], v[48:51], v[76:79]\nds_read_b128 v[48:51], v34 offset:20480\n"
"s_waitcnt lgkmcnt(3)\nv_mfma_f32_16x16x32_bf16 v[80:83], v[200:203], v[52:55], v[80:83]\ns_add_u32 m0, s52, 16384\nv_mfma_f32_16x16x32_bf16 v[84:87], v[204:207], v[52:55], v[84:87]\n"
"s_add_u32 s53, s49, 512\nv_mfma_f32_16x16x32_bf16 v[88:91], a[72:75], v[52:55], v[88:91]\nbuffer_load_dwordx4 v10, s[12:15], s53 offen lds\nv_mfma_f32_16x16x32_bf16 v[92:95], a[76:79], v[52:55], v[92:95]\n"
"ds_read_b128 v[52:55], v35 offset:20480\ns_waitcnt lgkmcnt(3)\nv_mfma_f32_16x16x32_bf16 v[64:67], v[208:211], v[40:43], v[64:67]\nv_mfma_f32_16x16x32_bf16 v[68:71], v[212:215], v[40:43], v[68:71]\n"
"v_mfma_f32_16x16x32_bf16 v[72:75], a[80:83], v[40:43], v[72:75]\nv_mfma_f32_16x16x32_bf16 v[76:79], a[84:87], v[40:43], v[76:79]\nds_read_b128 v[40:43], v32 offset:24576\ns_waitcnt lgkmcnt(3)\n"
"v_mfma_f32_16x16x32_bf16 v[80:83], v[208:211], v[44:47], v[80:83]\nv_mfma_f32_16x16x32_bf16 v[84:87], v[212:215], v[44:47], v[84:87]\nv_mfma_f32_16x16x32_bf16 v[88:91], a[80:83], v[44:47], v[88:91]\nv_mfma_f32_16x16x32_bf16 v[92:95], a[84:87], v[44:47], v[92:95]\n"
"ds_read_b128 v[44:47], v33 offset:24576\ns_add_u32 m0, s52, 20480\ns_waitcnt lgkmcnt(3)\nv_mfma_f32_16x16x32_bf16 v[64:67], v[216:219], v[48:51], v[64:67]\n"
"s_add_u32 s54, s51, 32768\ns_cmp_eq_u32 s54, 163840\nv_mfma_f32_16x16x32_bf16 v[68:71], v[220:223], v[48:51], v[68:71]\nv_mfma_f32_16x16x32_bf16 v[72:75], a[88:91], v[48:51], v[72:75]\n"
"v_mfma_f32_16x16x32_bf16 v[76:79], a[92:95], v[48:51], v[76:79]\ns_cselect_b32 s54, 0, s54\ns_sub_u32 s54, s54, s51\nds_read_b128 v[48:51], v34 offset:24576\n"
"s_add_u32 s53, s49, 640\ns_waitcnt lgkmcnt(3)\nv_mfma_f32_16x16x32_bf16 v[80:83], v[216:219], v[52:55], v[80:83]\nbuffer_load_dwordx4 v10, s[12:15], s53 offen lds\n"
"v_mfma_f32_16x16x32_bf16 v[84:87], v[220:223], v[52:55], v[84:87]\nv_mfma_f32_16x16x32_bf16 v[88:91], a[88:91], v[52:55], v[88:91]\nv_mfma_f32_16x16x32_bf16 v[92:95], a[92:95], v[52:55], v[92:95]\nds_read_b128 v[52:55], v35 offset:24576\n"
"s_waitcnt lgkmcnt(3)\nv_mfma_f32_16x16x32_bf16 v[64:67], v[224:227], v[40:43], v[64:67]\nv_add_u32 v32, s54, v32\nv_mfma_f32_16x16x32_bf16 v[68:71], v[228:231], v[40:43], v[68:71]\n"
"v_mfma_f32_16x16x32_bf16 v[72:75], a[96:99], v[40:43], v[72:75]\nv_mfma_f32_16x16x32_bf16 v[76:79], a[100:103], v[40:43], v[76:79]\nds_read_b128 v[40:43], v32 offset:0\ns_waitcnt lgkmcnt(3)\n"
"v_mfma_f32_16x16x32_bf16 v[80:83], v[224:227], v[44:47], v[80:83]\nv_add_u32 v33, s54, v33\nv_mfma_f32_16x16x32_bf16 v[84:87], v[228:231], v[44:47], v[84:87]\ns_add_u32 m0, s52, 24576\n"
"v_mfma_f32_16x16x32_bf16 v[88:91], a[96:99], v[44:47], v[88:91]\ns_add_u32 s53, s49, 768\nv_mfma_f32_16x16x32_bf16 v[92:95], a[100:103], v[44:47], v[92:95]\nds_read_b128 v[44:47], v33 offset:0\n"
"buffer_load_dwordx4 v10, s[12:15], s53 offen lds\ns_waitcnt lgkmcnt(3)\nv_mfma_f32_16x16x32_bf16 v[64:67], v[232:235], v[48:51], v[64:67]\nv_add_u32 v34, s54, v34\n"
"v_mfma_f32_16x16x32_bf16 v[68:71], v[236:239], v[48:51], v[68:71]\nv_mfma_f32_16x16x32_bf16 v[72:75], a[104:107], v[48:51], v[72:75]\nv_mfma_f32_16x16x32_bf16 v[76:79], a[108:111], v[48:51], v[76:79]\nds_read_b128 v[48:51], v34 offset:0\n"
"s_waitcnt lgkmcnt(3)\nv_mfma_f32_16x16x32_bf16 v[80:83], v[232:235], v[52:55], v[80:83]\nv_add_u32 v35, s54, v35\nv_mfma_f32_16x16x32_bf16 v[84:87], v[236:239], v[52:55], v[84:87]\n"
"v_mfma_f32_16x16x32_bf16 v[88:91], a[104:107], v[52:55], v[88:91]\nv_mfma_f32_16x16x32_bf16 v[92:95], a[108:111], v[52:55], v[92:95]\nds_read_b128 v[52:55], v35 offset:0\ns_mov_b32 s52, s47\n"
"s_add_u32 s47, s51, s32\ns_add_u32 s51, s51, s54\ns_add_u32 s49, s49, 28672\ns_waitcnt vmcnt(7)\n"
"s_barrier\ns_waitcnt lgkmcnt(3)\nv_mfma_f32_16x16x32_bf16 v[96:99], v[128:131], v[40:43], 0\ns_add_u32 s48, s48, 1\n"
"v_mfma_f32_16x16x32_bf16 v[100:103], v[132:135], v[40:43], 0\nv_mfma_f32_16x16x32_bf16 v[104:107], a[0:3], v[40:43], 0\nv_mfma_f32_16x16x32_bf16 v[108:111], a[4:7], v[40:43], 0\nds_read_b128 v[40:43], v32 offset:4096\n"
"v_cvt_pk_bf16_f32 v56, v64, v65\ns_waitcnt lgkmcnt(3)\nv_mfma_f32_16x16x32_bf16 v[112:115], v[128:131], v[44:47], 0\nv_cvt_pk_bf16_f32 v57, v66, v67\n"
"v_mfma_f32_16x16x32_bf16 v[116:119], v[132:135], v[44:47], 0\nv_cvt_pk_bf16_f32 v58, v68, v69\nv_mfma_f32_16x16x32_bf16 v[120:123], a[0:3], v[44:47], 0\nv_cvt_pk_bf16_f32 v59, v70, v71\n"
"v_mfma_f32_16x16x32_bf16 v[124:127], a[4:7], v[44:47], 0\nds_read_b128 v[44:47], v33 offset:4096\ns_waitcnt lgkmcnt(3)\nv_mfma_f32_16x16x32_bf16 v[96:99], v[136:139], v[48:51], v[96:99]\n"
"v_mfma_f32_16x16x32_bf16 v[100:103], v[140:143], v[48:51], v[100:103]\nbuffer_store_dwordx4 v[56:59], v15, s[20:23], s50 offen sc1\nv_mfma_f32_16x16x32_bf16 v[104:107], a[8:11], v[48:51], v[104:107]\nv_cvt_pk_bf16_f32 v60, v80, v81\n"
"v_mfma_f32_16x16x32_bf16 v[108:111], a[12:15], v[48:51], v[108:111]\nds_read_b128 v[48:51], v34 offset:4096\nv_cvt_pk_bf16_f32 v61, v82, v83\ns_waitcnt lgkmcnt(3)\n"
"v_mfma_f32_16x16x32_bf16 v[112:115], v[136:139], v[52:55], v[112:115]\nv_cvt_pk_bf16_f32 v62, v84, v85\nv_mfma_f32_16x16x32_bf16 v[116:119], v[140:143], v[52:55], v[116:119]\nv_cvt_pk_bf16_f32 v63, v86, v87\n"
"v_mfma_f32_16x16x32_bf16 v[120:123], a[8:11], v[52:55], v[120:123]\nv_mfma_f32_16x16x32_bf16 v[124:127], a[12:15], v[52:55], v[124:127]\nds_read_b128 v[52:55], v35 offset:4096\ns_waitcnt lgkmcnt(3)\n"
"v_mfma_f32_16x16x32_bf16 v[96:99], v[144:147], v[40:43], v[96:99]\nbuffer_store_dwordx4 v[60:63], v16, s[20:23], s50 offen sc1\nv_mfma_f32_16x16x32_bf16 v[100:103], v[148:151], v[40:43], v[100:103]\nv_cvt_pk_bf16_f32 v56, v72, v73\n"
"v_mfma_f32_16x16x32_bf16 v[104:107], a[16:19], v[40:43], v[104:107]\nv_cvt_pk_bf16_f32 v57, v74, v75\nv_mfma_f32_16x16x32_bf16 v[108:111], a[20:23], v[40:43], v[108:111]\nds_read_b128 v[40:43], v32 offset:8192\n"
"v_cvt_pk_bf16_f32 v58, v76, v77\ns_waitcnt lgkmcnt(3)\nv_mfma_f32_16x16x32_bf16 v[112:115], v[144:147], v[44:47], v[112:115]\nv_cvt_pk_bf16_f32 v59, v78, v79\n"
"v_mfma_f32_16x16x32_bf16 v[116:119], v[148:151], v[44:47], v[116:119]\nv_mfma_f32_16x16x32_bf16 v[120:123], a[16:19], v[44:47], v[120:123]\nv_mfma_f32_16x16x32_bf16 v[124:127], a[20:23], v[44:47], v[124:127]\nds_read_b128 v[44:47], v33 offset:8192\n"
"buffer_store_dwordx4 v[56:59], v17, s[20:23], s50 offen sc1\ns_waitcnt lgkmcnt(3)\nv_mfma_f32_16x16x32_bf16 v[96:99], v[152:155], v[48:51], v[96:99]\nv_cvt_pk_bf16_f32 v60, v88, v89\n"
"v_mfma_f32_16x16x32_bf16 v[100:103], v[156:159], v[48:51], v[100:103]\nv_cvt_pk_bf16_f32 v61, v90, v91\nv_mfma_f32_16x16x32_bf16 v[104:107], a[24:27], v[48:51], v[104:107]\nv_cvt_pk_bf16_f32 v62, v92, v93\n"
"v_mfma_f32_16x16x32_bf16 v[108:111], a[28:31], v[48:51], v[108:111]\nds_read_b128 v[48:51], v34 offset:8192\nv_cvt_pk_bf16_f32 v63, v94, v95\ns_waitcnt lgkmcnt(3)\n"
"v_mfma_f32_16x16x32_bf16 v[112:115], v[152:155], v[52:55], v[112:115]\nv_mfma_f32_16x16x32_bf16 v[116:119], v[156:159], v[52:55], v[116:119]\nv_mfma_f32_16x16x32_bf16 v[120:123], a[24:27], v[52:55], v[120:123]\nbuffer_store_dwordx4 v[60:63], v18, s[20:23], s50 offen sc1\n"
"v_mfma_f32_16x16x32_bf16 v[124:127], a[28:31], v[52:55], v[124:127]\nds_read_b128 v[52:55], v35 offset:8192\ns_add_u32 s50, s50, s36\ns_waitcnt lgkmcnt(3)\n"
"v_mfma_f32_16x16x32_bf16 v[96:99], v[160:163], v[40:43], v[96:99]\ns_add_u32 m0, s52, 0\nv_mfma_f32_16x16x32_bf16 v[100:103], v[164:167], v[40:43], v[100:103]\ns_add_u32 s53, s49, 0\n"
"v_mfma_f32_16x16x32_bf16 v[104:107], a[32:35], v[40:43], v[104:107]\nbuffer_load_dwordx4 v10, s[12:15], s53 offen lds\nv_mfma_f32_16x16x32_bf16 v[108:111], a[36:39], v[40:43], v[108:111]\nds_read_b128 v[40:43], v32 offset:12288\n"
"s_waitcnt lgkmcnt(3)\nv_mfma_f32_16x16x32_bf16 v[112:115], v[160:163], v[44:47], v[112:115]\nv_mfma_f32_16x16x32_bf16 v[116:119], v[164:167], v[44:47], v[116:119]\nv_mfma_f32_16x16x32_bf16 v[120:123], a[32:35], v[44:47], v[120:123]\n"
"v_mfma_f32_16x16x32_bf16 v[124:127], a[36:39], v[44:47], v[124:127]\nds_read_b128 v[44:47], v33 offset:12288\ns_waitcnt lgkmcnt(3)\nv_mfma_f32_16x16x32_bf16 v[96:99], v[168:171], v[48:51], v[96:99]\n"
"v_mfma_f32_16x16x32_bf16 v[100:103], v[172:175], v[48:51], v[100:103]\nv_mfma_f32_16x16x32_bf16 v[104:107], a[40:43], v[48:51], v[104:107]\nv_mfma_f32_16x16x32_bf16 v[108:111], a[44:47], v[48:51], v[108:111]\nds_read_b128 v[48:51], v34 offset:12288\n"
"s_add_u32 m0, s52, 4096\ns_waitcnt lgkmcnt(3)\nv_mfma_f32_16x16x32_bf16 v[112:115], v[168:171], v[52:55], v[112:115]\ns_add_u32 s53, s49, 128\n"
"v_mfma_f32_16x16x32_bf16 v[116:119], v[172:175], v[52:55], v[116:119]\nbuffer_load_dwordx4 v10, s[12:15], s53 offen lds\nv_mfma_f32_16x16x32_bf16 v[120:123], a[40:43], v[52:55], v[120:123]\nv_mfma_f32_16x16x32_bf16 v[124:127], a[44:47], v[52:55], v[124:127]\n"
"ds_read_b128 v[52:55], v35 offset:12288\ns_waitcnt lgkmcnt(3)\nv_mfma_f32_16x16x32_bf16 v[96:99], v[176:179], v[40:43], v[96:99]\nv_mfma_f32_16x16x32_bf16 v[100:103], v[180:183], v[40:43], v[100:103]\n"
"v_mfma_f32_16x16x32_bf16 v[104:107], a[48:51], v[40:43], v[104:107]\nv_mfma_f32_16x16x32_bf16 v[108:111], a[52:55], v[40:43], v[108:111]\nds_read_b128 v[40:43], v32 offset:16384\ns_waitcnt lgkmcnt(3)\n"
"v_mfma_f32_16x16x32_bf16 v[112:115], v[176:179], v[44:47], v[112:115]\nv_mfma_f32_16x16x32_bf16 v[116:119], v[180:183], v[44:47], v[116:119]\nv_mfma_f32_16x16x32_bf16 v[120:123], a[48:51], v[44:47], v[120:123]\ns_add_u32 m0, s52, 8192\n"
"v_mfma_f32_16x16x32_bf16 v[124:127], a[52:55], v[44:47], v[124:127]\nds_read_b128 v[44:47], v33 offset:16384\ns_add_u32 s53, s49, 256\ns_waitcnt lgkmcnt(3)\n"
"v_mfma_f32_16x16x32_bf16 v[96:99], v[184:187], v[48:51], v[96:99]\nbuffer_load_dwordx4 v10, s[12:15], s53 offen lds\nv_mfma_f32_16x16x32_bf16 v[100:103], v[188:191], v[48:51], v[100:103]\nv_mfma_f32_16x16x32_bf16 v[104:107], a[56:59], v[48:51], v[104:107]\n"
"v_mfma_f32_16x16x32_bf16 v[108:111], a[60:63], v[48:51], v[108:111]\nds_read_b128 v[48:51], v34 offset:16384\ns_waitcnt lgkmcnt(3)\nv_mfma_f32_16x16x32_bf16 v[112:115], v[184:187], v[52:55], v[112:115]\n"
"v_mfma_f32_16x16x32_bf16 v[116:119], v[188:191], v[52:55], v[116:119]\nv_mfma_f32_16x16x32_bf16 v[120:123], a[56:59], v[52:55], v[120:123]\nv_mfma_f32_16x16x32_bf16 v[124:127], a[60:63], v[52:55], v[124:127]\nds_read_b128 v[52:55], v35 offset:16384\n"
"s_waitcnt lgkmcnt(3)\nv_mfma_f32_16x16x32_bf16 v[96:99], v[192:195], v[40:43], v[96:99]\nv_mfma_f32_16x16x32_bf16 v[100:103], v[196:199], v[40:43], v[100:103]\ns_add_u32 m0, s52, 12288\n"
"v_mfma_f32_16x16x32_bf16 v[104:107], a[64:67], v[40:43], v[104:107]\ns_add_u32 s53, s49, 384\nv_mfma_f32_16x16x32_bf16 v[108:111], a[68:71], v[40:43], v[108:111]\nds_read_b128 v[40:43], v32 offset:20480\n"
"buffer_load_dwordx4 v10, s[12:15], s53 offen lds\ns_waitcnt lgkmcnt(3)\nv_mfma_f32_16x16x32_bf16 v[112:115], v[192:195], v[44:47], v[112:115]\nv_mfma_f32_16x16x32_bf16 v[116:119], v[196:199], v[44:47], v[116:119]\n"
"v_mfma_f32_16x16x32_bf16 v[120:123], a[64:67], v[44:47], v[120:123]\nv_mfma_f32_16x16x32_bf16 v[124:127], a[68:71], v[44:47], v[124:127]\nds_read_b128 v[44:47], v33 offset:20480\ns_waitcnt lgkmcnt(3)\n"
"v_mfma_f32_16x16x32_bf16 v[96:99], v[200:203], v[48:51], v[96:99]\nv_mfma_f32_16x16x32_bf16 v[100:103], v[204:207], v[48:51], v[100:103]\nv_mfma_f32_16x16x32_bf16 v[104:107], a[72:75], v[48:51], v[104:107]\nv_mfma_f32_16x16x32_bf16 v[108:111], a[76:79], v[48:51], v[108:111]\n"
"ds_read_b128 v[48:51], v34 offset:20480\ns_waitcnt lgkmcnt(3)\nv_mfma_f32_16x16x32_bf16 v[112:115], v[200:203], v[52:55], v[112:115]\ns_add_u32 m0, s52, 16384\n"
"v_mfma_f32_16x16x32_bf16 v[116:119], v[204:207], v[52:55], v[116:119]\ns_add_u32 s53, s49, 512\nv_mfma_f32_16x16x32_bf16 v[120:123], a[72:75], v[52:55], v[120:123]\nbuffer_load_dwordx4 v10, s[12:15], s53 offen lds\n"
"v_mfma_f32_16x16x32_bf16 v[124:127], a[76:79], v[52:55], v[124:127]\nds_read_b128 v[52:55], v35 offset:20480\ns_waitcnt lgkmcnt(3)\nv_mfma_f32_16x16x32_bf16 v[96:99], v[208:211], v[40:43], v[96:99]\n"
"v_mfma_f32_16x16x32_bf16 v[100:103], v[212:215], v[40:43], v[100:103]\nv_mfma_f32_16x16x32_bf16 v[104:107], a[80:83], v[40:43], v[104:107]\nv_mfma_f32_16x16x32_bf16 v[108:111], a[84:87], v[40:43], v[108:111]\nds_read_b128 v[40:43], v32 offset:24576\n"
"s_waitcnt lgkmcnt(3)\nv_mfma_f32_16x16x32_bf16 v[112:115], v[208:211], v[44:47], v[112:115]\nv_mfma_f32_16x16x32_bf16 v[116:119], v[212:215], v[44:47], v[116:119]\nv_mfma_f32_16x16x32_bf16 v[120:123], a[80:83], v[44:47], v[120:123]\n"
"v_mfma_f32_16x16x32_bf16 v[124:127], a[84:87], v[44:47], v[124:127]\nds_read_b128 v[44:47], v33 offset:24576\ns_add_u32 m0, s52, 20480\ns_waitcnt lgkmcnt(3)\n"
"v_mfma_f32_16x16x32_bf16 v[96:99], v[216:219], v[48:51], v[96:99]\ns_add_u32 s54, s51, 32768\ns_cmp_eq_u32 s54, 163840\nv_mfma_f32_16x16x32_bf16 v[100:103], v[220:223], v[48:51], v[100:103]\n"
"v_mfma_f32_16x16x32_bf16 v[104:107], a[88:91], v[48:51], v[104:107]\nv_mfma_f32_16x16x32_bf16 v[108:111], a[92:95], v[48:51], v[108:111]\ns_cselect_b32 s54, 0, s54\ns_sub_u32 s54, s54, s51\n"
"ds_read_b128 v[48:51], v34 offset:24576\ns_add_u32 s53, s49, 640\ns_waitcnt lgkmcnt(3)\nv_mfma_f32_16x16x32_bf16 v[112:115], v[216:219], v[52:55], v[112:115]\n"
"buffer_load_dwordx4 v10, s[12:15], s53 offen lds\nv_mfma_f32_16x16x32_bf16 v[116:119], v[220:223], v[52:55], v[116:119]\nv_mfma_f32_16x16x32_bf16 v[120:123], a[88:91], v[52:55], v[120:123]\nv_mfma_f32_16x16x32_bf16 v[124:127], a[92:95], v[52:55], v[124:127]\n"
"ds_read_b128 v[52:55], v35 offset:24576\ns_waitcnt lgkmcnt(3)\nv_mfma_f32_16x16x32_bf16 v[96:99], v[224:227], v[40:43], v[96:99]\nv_add_u32 v32, s54, v32\n"
"v_mfma_f32_16x16x32_bf16 v[100:103], v[228:231], v[40:43], v[100:103]\nv_mfma_f32_16x16x32_bf16 v[104:107], a[96:99], v[40:43], v[104:107]\nv_mfma_f32_16x16x32_bf16 v[108:111], a[100:103], v[40:43], v[108:111]\nds_read_b128 v[40:43], v32 offset:0\n"
"s_waitcnt lgkmcnt(3)\nv_mfma_f32_16x16x32_bf16 v[112:115], v[224:227], v[44:47], v[112:115]\nv_add_u32 v33, s54, v33\nv_mfma_f32_16x16x32_bf16 v[116:119], v[228:231], v[44:47], v[116:119]\n"
"s_add_u32 m0, s52, 24576\nv_mfma_f32_16x16x32_bf16 v[120:123], a[96:99], v[44:47], v[120:123]\ns_add_u32 s53, s49, 768\nv_mfma_f32_16x16x32_bf16 v[124:127], a[100:103], v[44:47], v[124:127]\n"
"ds_read_b128 v[44:47], v33 offset:0\nbuffer_load_dwordx4 v10, s[12:15], s53 offen lds\ns_waitcnt lgkmcnt(3)\nv_mfma_f32_16x16x32_bf16 v[96:99], v[232:235], v[48:51], v[96:99]\n"
"v_add_u32 v34, s54, v34\nv_mfma_f32_16x16x32_bf16 v[100:103], v[236:239], v[48:51], v[100:103]\nv_mfma_f32_16x16x32_bf16 v[104:107], a[104:107], v[48:51], v[104:107]\nv_mfma_f32_16x16x32_bf16 v[108:111], a[108:111], v[48:51], v[108:111]\n"
"ds_read_b128 v[48:51], v34 offset:0\ns_waitcnt lgkmcnt(3)\nv_mfma_f32_16x16x32_bf16 v[112:115], v[232:235], v[52:55], v[112:115]\nv_add_u32 v35, s54, v35\n"
"v_mfma_f32_16x16x32_bf16 v[116:119], v[236:239], v[52:55], v[116:119]\nv_mfma_f32_16x16x32_bf16 v[120:123], a[104:107], v[52:55], v[120:123]\nv_mfma_f32_16x16x32_bf16 v[124:127], a[108:111], v[52:55], v[124:127]\nds_read_b128 v[52:55], v35 offset:0\n"
"s_mov_b32 s52, s47\ns_add_u32 s47, s51, s32\ns_add_u32 s51, s51, s54\ns_add_u32 s49, s49, 28672\n"
"s_cmp_lt_u32 s49, 1921024\ns_cbranch_scc1 fg0_loop\ns_nop 7\nv_cvt_pk_bf16_f32 v56, v96, v97\n"
"v_cvt_pk_bf16_f32 v57, v98, v99\nv_cvt_pk_bf16_f32 v58, v100, v101\nv_cvt_pk_bf16_f32 v59, v102, v103\ns_nop 0\n"
"s_nop 0\nbuffer_store_dwordx4 v[56:59], v15, s[20:23], s50 offen sc1\nv_cvt_pk_bf16_f32 v60, v112, v113\nv_cvt_pk_bf16_f32 v61, v114, v115\n"
"v_cvt_pk_bf16_f32 v62, v116, v117\nv_cvt_pk_bf16_f32 v63, v118, v119\ns_nop 0\ns_nop 0\n"
"buffer_store_dwordx4 v[60:63], v16, s[20:23], s50 offen sc1\nv_cvt_pk_bf16_f32 v56, v104, v105\nv_cvt_pk_bf16_f32 v57, v106, v107\nv_cvt_pk_bf16_f32 v58, v108, v109\n"
"v_cvt_pk_bf16_f32 v59, v110, v111\ns_nop 0\ns_nop 0\nbuffer_store_dwordx4 v[56:59], v17, s[20:23], s50 offen sc1\n"
"v_cvt_pk_bf16_f32 v60, v120, v121\nv_cvt_pk_bf16_f32 v61, v122, v123\nv_cvt_pk_bf16_f32 v62, v124, v125\nv_cvt_pk_bf16_f32 v63, v126, v127\n"
"s_nop 0\ns_nop 0\nbuffer_store_dwordx4 v[60:63], v18, s[20:23], s50 offen sc1\ns_add_u32 s48, s28, 64\n"
"v_mov_b32 v36, s48\ns_waitcnt vmcnt(0) lgkmcnt(0)\ns_barrier\nbuffer_store_dword v36, v19, s[24:27], s33 offen sc1\n"
"fg0_end:\n"
    :  : "{s[8:23]}"(s0), "{s[24:39]}"(s1), "{v[8:23]}"(v0) : "v24", "v25", "v26", "v27", "v28", "v29", "v30", "v31", "v32", "v33", "v34", "v35", "v36", "v37", "v38", "v39", "v40", "v41", "v42", "v43", "v44", "v45", "v46", "v47", "v48", "v49", "v50", "v51", "v52", "v53", "v54", "v55", "v56", "v57", "v58", "v59", "v60", "v61", "v62", "v63", "v64", "v65", "v66", "v67", "v68", "v69", "v70", "v71", "v72", "v73", "v74", "v75", "v76", "v77", "v78", "v79", "v80", "v81", "v82", "v83", "v84", "v85", "v86", "v87", "v88", "v89", "v90", "v91", "v92", "v93", "v94", "v95", "v96", "v97", "v98", "v99", "v100", "v101", "v102", "v103", "v104", "v105", "v106", "v107", "v108", "v109", "v110", "v111", "v112", "v113", "v114", "v115", "v116", "v117", "v118", "v119", "v120", "v121", "v122", "v123", "v124", "v125", "v126", "v127", "v128", "v129", "v130", "v131", "v132", "v133", "v134", "v135", "v136", "v137", "v138", "v139", "v140", "v141", "v142", "v143", "v144", "v145", "v146", "v147", "v148", "v149", "v150", "v151", "v152", "v153", "v154", "v155", "v156", "v157", "v158", "v159", "v160", "v161", "v162", "v163", "v164", "v165", "v166", "v167", "v168", "v169", "v170", "v171", "v172", "v173", "v174", "v175", "v176", "v177", "v178", "v179", "v180", "v181", "v182", "v183", "v184", "v185", "v186", "v187", "v188", "v189", "v190", "v191", "v192", "v193", "v194", "v195", "v196", "v197", "v198", "v199", "v200", "v201", "v202", "v203", "v204", "v205", "v206", "v207", "v208", "v209", "v210", "v211", "v212", "v213", "v214", "v215", "v216", "v217", "v218", "v219", "v220", "v221", "v222", "v223", "v224", "v225", "v226", "v227", "v228", "v229", "v230", "v231", "v232", "v233", "v234", "v235", "v236", "v237", "v238", "v239", "v240", "v241", "v242", "v243", "a0", "a1", "a2", "a3", "a4", "a5", "a6", "a7", "a8", "a9", "a10", "a11", "a12", "a13", "a14", "a15", "a16", "a17", "a18", "a19", "a20", "a21", "a22", "a23", "a24", "a25", "a26", "a27", "a28", "a29", "a30", "a31", "a32", "a33", "a34", "a35", "a36", "a37", "a38", "a39", "a40", "a41", "a42", "a43", "a44", "a45", "a46", "a47", "a48", "a49", "a50", "a51", "a52", "a53", "a54", "a55", "a56", "a57", "a58", "a59", "a60", "a61", "a62", "a63", "a64", "a65", "a66", "a67", "a68", "a69", "a70", "a71", "a72", "a73", "a74", "a75", "a76", "a77", "a78", "a79", "a80", "a81", "a82", "a83", "a84", "a85", "a86", "a87", "a88", "a89", "a90", "a91", "a92", "a93", "a94", "a95", "a96", "a97", "a98", "a99", "a100", "a101", "a102", "a103", "a104", "a105", "a106", "a107", "a108", "a109", "a110", "a111", "a112", "a113", "a114", "a115", "a116", "a117", "a118", "a119", "a120", "a121", "a122", "a123", "a124", "a125", "a126", "a127", "a128", "a129", "a130", "a131", "a132", "a133", "a134", "a135", "a136", "a137", "a138", "a139", "a140", "a141", "a142", "a143", "a144", "a145", "a146", "a147", "a148", "a149", "a150", "a151", "a152", "a153", "a154", "a155", "a156", "a157", "a158", "a159", "a160", "a161", "a162", "a163", "a164", "a165", "a166", "a167", "a168", "a169", "a170", "a171", "a172", "a173", "a174", "a175", "a176", "a177", "a178", "a179", "a180", "a181", "a182", "a183", "a184", "a185", "a186", "a187", "a188", "a189", "a190", "a191", "s40", "s41", "s42", "s43", "s44", "s45", "s46", "s47", "s48", "s49", "s50", "s51", "s52", "s53", "s54", "s55", "s56", "s57", "s58", "s59", "s60", "s61", "vcc", "scc", "memory");
    return o;
}
DI unsigned gemm_asm1(const GCtx& c) {
    const u16v s0 = {RFL(c.rw1[0]), RFL(c.rw1[1]), RFL(c.rw1[2]), RFL(c.rw1[3]), RFL(c.rx[0]), RFL(c.rx[1]), RFL(c.rx[2]), RFL(c.rx[3]), RFL(c.rexp[0]), RFL(c.rexp[1]), RFL(c.rexp[2]), RFL(c.rexp[3]), RFL(c.rout[0]), RFL(c.rout[1]), RFL(c.rout[2]), RFL(c.rout[3])}, s1 = {RFL(c.rhb[0]), RFL(c.rhb[1]), RFL(c.rhb[2]), RFL(c.rhb[3]), RFL(c.tag), RFL(c.rot), RFL(c.sw1), RFL(c.mw), RFL(c.mx), RFL(c.sfg), RFL(c.stay), RFL(c.csc), RFL(c.ostr), RFL(c.kb), RFL(c.p1), RFL(c.p2)}, v0 = {c.vr0, c.vr1, c.vx, c.wa, c.xa, c.vexp, c.vimp, c.vo0, c.vo1, c.vo2, c.vo3, c.vfl, c.vpf, c.vpo, c.vk, c.p5};
    unsigned o = 0;
    asm volatile(
"s_nop 4\ns_add_u32 s40, s29, 0\ns_add_u32 s41, s29, 1\ns_add_u32 s42, s29, 2\n"
"s_add_u32 s43, s29, 3\nv_xor_b32 v24, 0, v11\nv_xor_b32 v25, 64, v11\nv_xor_b32 v26, 128, v11\n"
"v_xor_b32 v27, 192, v11\nv_xor_b32 v28, 2064, v11\nv_xor_b32 v29, 2128, v11\nv_xor_b32 v30, 2192, v11\n"
"v_xor_b32 v31, 2256, v11\nv_xor_b32 v40, 32, v22\nds_read_b64_tr_b16 a[128:129], v22 offset:0\nds_read_b64_tr_b16 a[130:131], v22 offset:512\n"
"ds_read_b64_tr_b16 a[132:133], v22 offset:4096\nds_read_b64_tr_b16 a[134:135], v22 offset:4608\nds_read_b64_tr_b16 a[136:137], v22 offset:8192\nds_read_b64_tr_b16 a[138:139], v22 offset:8704\n"
"ds_read_b64_tr_b16 a[140:141], v22 offset:12288\nds_read_b64_tr_b16 a[142:143], v22 offset:12800\ns_lshl_b32 s53, s41, 19\ns_add_u32 s53, s53, s30\n"
"s_add_u32 m0, s31, 65536\ns_add_u32 s53, s53, 0\nbuffer_load_dwordx4 v8, s[8:11], s53 offen lds\nbuffer_load_dwordx4 v9, s[8:11], s53 offen offset:1024 lds\n"
"s_waitcnt lgkmcnt(0)\nds_read_b64_tr_b16 a[144:145], v22 offset:16384\nds_read_b64_tr_b16 a[146:147], v22 offset:16896\nds_read_b64_tr_b16 a[148:149], v22 offset:20480\n"
"ds_read_b64_tr_b16 a[150:151], v22 offset:20992\nds_read_b64_tr_b16 a[152:153], v22 offset:24576\nds_read_b64_tr_b16 a[154:155], v22 offset:25088\nds_read_b64_tr_b16 a[156:157], v22 offset:28672\n"
"ds_read_b64_tr_b16 a[158:159], v22 offset:29184\ns_lshl_b32 s53, s41, 19\ns_add_u32 s53, s53, s30\ns_add_u32 m0, s31, 73728\n"
"s_add_u32 s53, s53, 131072\nbuffer_load_dwordx4 v8, s[8:11], s53 offen lds\nbuffer_load_dwordx4 v9, s[8:11], s53 offen offset:1024 lds\ns_waitcnt lgkmcnt(0)\n"
"ds_read_b64_tr_b16 a[160:161], v40 offset:0\nds_read_b64_tr_b16 a[162:163], v40 offset:512\nds_read_b64_tr_b16 a[164:165], v40 offset:4096\nds_read_b64_tr_b16 a[166:167], v40 offset:4608\n"
"ds_read_b64_tr_b16 a[168:169], v40 offset:8192\nds_read_b64_tr_b16 a[170:171], v40 offset:8704\nds_read_b64_tr_b16 a[172:173], v40 offset:12288\nds_read_b64_tr_b16 a[174:175], v40 offset:12800\n"
"s_lshl_b32 s53, s41, 19\ns_add_u32 s53, s53, s30\ns_add_u32 m0, s31, 81920\ns_add_u32 s53, s53, 262144\n"
"buffer_load_dwordx4 v8, s[8:11], s53 offen lds\nbuffer_load_dwordx4 v9, s[8:11], s53 offen offset:1024 lds\ns_waitcnt lgkmcnt(0)\nds_read_b64_tr_b16 a[176:177], v40 offset:16384\n"
"ds_read_b64_tr_b16 a[178:179], v40 offset:16896\nds_read_b64_tr_b16 a[180:181], v40 offset:20480\nds_read_b64_tr_b16 a[182:183], v40 offset:20992\nds_read_b64_tr_b16 a[184:185], v40 offset:24576\n"
"ds_read_b64_tr_b16 a[186:187], v40 offset:25088\nds_read_b64_tr_b16 a[188:189], v40 offset:28672\nds_read_b64_tr_b16 a[190:191], v40 offset:29184\ns_lshl_b32 s53, s41, 19\n"
"s_add_u32 s53, s53, s30\ns_add_u32 m0, s31, 90112\ns_add_u32 s53, s53, 393216\nbuffer_load_dwordx4 v8, s[8:11], s53 offen lds\n"
"buffer_load_dwordx4 v9, s[8:11], s53 offen offset:1024 lds\ns_waitcnt lgkmcnt(0)\ns_waitcnt lgkmcnt(0)\nv_add_u32 v40, 65536, v24\n"
"v_add_u32 v41, 65536, v25\nv_add_u32 v42, 65536, v26\nv_add_u32 v43, 65536, v27\nv_add_u32 v44, 65536, v28\n"
"v_add_u32 v45, 65536, v29\nv_add_u32 v46, 65536, v30\nv_add_u32 v47, 65536, v31\nv_xor_b32 v32, 0, v12\n"
"v_xor_b32 v33, 512, v12\nv_xor_b32 v34, 64, v12\nv_xor_b32 v35, 576, v12\ns_mov_b32 s55, 0\n"
"s_waitcnt vmcnt(10)\ns_barrier\nds_read_b128 v[96:99], v24 offset:0\nds_read_b128 v[100:103], v28 offset:0\n"
"ds_read_b128 v[104:107], v25 offset:0\nds_read_b128 v[108:111], v29 offset:0\nds_read_b128 v[112:115], v26 offset:0\nds_read_b128 v[116:119], v30 offset:0\n"
"ds_read_b128 v[120:123], v27 offset:0\nds_read_b128 v[124:127], v31 offset:0\ns_waitcnt lgkmcnt(6)\nv_mfma_f32_16x16x32_bf16 v[64:67], v[96:99], a[128:131], 0\n"
"s_lshl_b32 s53, s42, 19\ns_add_u32 s53, s53, s30\nv_mfma_f32_16x16x32_bf16 v[68:71], v[96:99], a[160:163], 0\nds_read_b128 v[96:99], v24 offset:256\n"
"s_add_u32 m0, s31, 98304\ns_add_u32 s53, s53, 0\nv_mfma_f32_16x16x32_bf16 v[72:75], v[100:103], a[128:131], 0\nbuffer_load_dwordx4 v8, s[8:11], s53 offen lds\n"
"v_mfma_f32_16x16x32_bf16 v[76:79], v[100:103], a[160:163], 0\nds_read_b128 v[100:103], v28 offset:256\nbuffer_load_dwordx4 v9, s[8:11], s53 offen offset:1024 lds\ns_waitcnt lgkmcnt(6)\n"
"v_mfma_f32_16x16x32_bf16 v[64:67], v[104:107], a[132:135], v[64:67]\ns_lshl_b32 s53, s42, 19\ns_add_u32 s53, s53, s30\nv_mfma_f32_16x16x32_bf16 v[68:71], v[104:107], a[164:167], v[68:71]\n"
"ds_read_b128 v[104:107], v25 offset:256\ns_add_u32 m0, s31, 106496\ns_add_u32 s53, s53, 131072\nv_mfma_f32_16x16x32_bf16 v[72:75], v[108:111], a[132:135], v[72:75]\n"
"buffer_load_dwordx4 v8, s[8:11], s53 offen lds\nv_mfma_f32_16x16x32_bf16 v[76:79], v[108:111], a[164:167], v[76:79]\nds_read_b128 v[108:111], v29 offset:256\nbuffer_load_dwordx4 v9, s[8:11], s53 offen offset:1024 lds\n"
"s_waitcnt lgkmcnt(6)\nv_mfma_f32_16x16x32_bf16 v[64:67], v[112:115], a[136:139], v[64:67]\nv_mfma_f32_16x16x32_bf16 v[68:71], v[112:115], a[168:171], v[68:71]\nds_read_b128 v[112:115], v26 offset:256\n"
"v_mfma_f32_16x16x32_bf16 v[72:75], v[116:119], a[136:139], v[72:75]\nv_mfma_f32_16x16x32_bf16 v[76:79], v[116:119], a[168:171], v[76:79]\nds_read_b128 v[116:119], v30 offset:256\ns_waitcnt lgkmcnt(6)\n"
"v_mfma_f32_16x16x32_bf16 v[64:67], v[120:123], a[140:143], v[64:67]\nv_mfma_f32_16x16x32_bf16 v[68:71], v[120:123], a[172:175], v[68:71]\nds_read_b128 v[120:123], v27 offset:256\nv_mfma_f32_16x16x32_bf16 v[72:75], v[124:127], a[140:143], v[72:75]\n"
"v_mfma_f32_16x16x32_bf16 v[76:79], v[124:127], a[172:175], v[76:79]\nds_read_b128 v[124:127], v31 offset:256\ns_waitcnt lgkmcnt(6)\nv_mfma_f32_16x16x32_bf16 v[64:67], v[96:99], a[144:147], v[64:67]\n"
"v_mfma_f32_16x16x32_bf16 v[68:71], v[96:99], a[176:179], v[68:71]\nds_read_b128 v[96:99], v24 offset:16384\nv_mfma_f32_16x16x32_bf16 v[72:75], v[100:103], a[144:147], v[72:75]\nv_mfma_f32_16x16x32_bf16 v[76:79], v[100:103], a[176:179], v[76:79]\n"
"ds_read_b128 v[100:103], v28 offset:16384\ns_waitcnt lgkmcnt(6)\nv_mfma_f32_16x16x32_bf16 v[64:67], v[104:107], a[148:151], v[64:67]\nv_mfma_f32_16x16x32_bf16 v[68:71], v[104:107], a[180:183], v[68:71]\n"
"ds_read_b128 v[104:107], v25 offset:16384\nv_mfma_f32_16x16x32_bf16 v[72:75], v[108:111], a[148:151], v[72:75]\nv_mfma_f32_16x16x32_bf16 v[76:79], v[108:111], a[180:183], v[76:79]\nds_read_b128 v[108:111], v29 offset:16384\n"
"s_waitcnt lgkmcnt(6)\nv_mfma_f32_16x16x32_bf16 v[64:67], v[112:115], a[152:155], v[64:67]\nv_mfma_f32_16x16x32_bf16 v[68:71], v[112:115], a[184:187], v[68:71]\nds_read_b128 v[112:115], v26 offset:16384\n"
"v_mfma_f32_16x16x32_bf16 v[72:75], v[116:119], a[152:155], v[72:75]\nv_mfma_f32_16x16x32_bf16 v[76:79], v[116:119], a[184:187], v[76:79]\nds_read_b128 v[116:119], v30 offset:16384\ns_waitcnt lgkmcnt(6)\n"
"v_mfma_f32_16x16x32_bf16 v[64:67], v[120:123], a[156:159], v[64:67]\nv_mfma_f32_16x16x32_bf16 v[68:71], v[120:123], a[188:191], v[68:71]\nds_read_b128 v[120:123], v27 offset:16384\nv_mfma_f32_16x16x32_bf16 v[72:75], v[124:127], a[156:159], v[72:75]\n"
"v_mfma_f32_16x16x32_bf16 v[76:79], v[124:127], a[188:191], v[76:79]\nds_read_b128 v[124:127], v31 offset:16384\ns_waitcnt lgkmcnt(6)\nv_mfma_f32_16x16x32_bf16 v[80:83], v[96:99], a[128:131], 0\n"
"s_lshl_b32 s53, s42, 19\ns_add_u32 s53, s53, s30\nv_mfma_f32_16x16x32_bf16 v[84:87], v[96:99], a[160:163], 0\nds_read_b128 v[96:99], v24 offset:16640\n"
"s_add_u32 m0, s31, 114688\ns_add_u32 s53, s53, 262144\nv_mfma_f32_16x16x32_bf16 v[88:91], v[100:103], a[128:131], 0\nbuffer_load_dwordx4 v8, s[8:11], s53 offen lds\n"
"v_mfma_f32_16x16x32_bf16 v[92:95], v[100:103], a[160:163], 0\nds_read_b128 v[100:103], v28 offset:16640\nbuffer_load_dwordx4 v9, s[8:11], s53 offen offset:1024 lds\ns_waitcnt lgkmcnt(6)\n"
"v_mfma_f32_16x16x32_bf16 v[80:83], v[104:107], a[132:135], v[80:83]\nv_cvt_pk_bf16_f32 v128, v64, v65\nv_cvt_pk_bf16_f32 v129, v66, v67\nv_mfma_f32_16x16x32_bf16 v[84:87], v[104:107], a[164:167], v[84:87]\n"
"ds_read_b128 v[104:107], v25 offset:16640\nv_cvt_pk_bf16_f32 v130, v72, v73\nv_cvt_pk_bf16_f32 v131, v74, v75\nv_mfma_f32_16x16x32_bf16 v[88:91], v[108:111], a[132:135], v[88:91]\n"
"v_cvt_pk_bf16_f32 v132, v68, v69\nv_cvt_pk_bf16_f32 v133, v70, v71\nv_mfma_f32_16x16x32_bf16 v[92:95], v[108:111], a[164:167], v[92:95]\nds_read_b128 v[108:111], v29 offset:16640\n"
"v_cvt_pk_bf16_f32 v134, v76, v77\nv_cvt_pk_bf16_f32 v135, v78, v79\ns_waitcnt lgkmcnt(6)\nv_mfma_f32_16x16x32_bf16 v[80:83], v[112:115], a[136:139], v[80:83]\n"
"s_lshl_b32 s54, s40, 11\ns_add_u32 s54, s54, 0\nv_mfma_f32_16x16x32_bf16 v[84:87], v[112:115], a[168:171], v[84:87]\nds_read_b128 v[112:115], v26 offset:16640\n"
"buffer_store_dwordx4 v[128:131], v13, s[16:19], s54 offen sc1\nv_mfma_f32_16x16x32_bf16 v[88:91], v[116:119], a[136:139], v[88:91]\ns_add_u32 s54, s54, 262144\nv_mfma_f32_16x16x32_bf16 v[92:95], v[116:119], a[168:171], v[92:95]\n"
"ds_read_b128 v[116:119], v30 offset:16640\nbuffer_store_dwordx4 v[132:135], v13, s[16:19], s54 offen sc1\ns_waitcnt lgkmcnt(6)\nv_mfma_f32_16x16x32_bf16 v[80:83], v[120:123], a[140:143], v[80:83]\n"
"s_lshl_b32 s53, s42, 19\ns_add_u32 s53, s53, s30\nv_mfma_f32_16x16x32_bf16 v[84:87], v[120:123], a[172:175], v[84:87]\nds_read_b128 v[120:123], v27 offset:16640\n"
"s_add_u32 m0, s31, 122880\ns_add_u32 s53, s53, 393216\nv_mfma_f32_16x16x32_bf16 v[88:91], v[124:127], a[140:143], v[88:91]\nbuffer_load_dwordx4 v8, s[8:11], s53 offen lds\n"
"v_mfma_f32_16x16x32_bf16 v[92:95], v[124:127], a[172:175], v[92:95]\nds_read_b128 v[124:127], v31 offset:16640\nbuffer_load_dwordx4 v9, s[8:11], s53 offen offset:1024 lds\ns_waitcnt vmcnt(10)\n"
"s_barrier\ns_waitcnt lgkmcnt(6)\nv_mfma_f32_16x16x32_bf16 v[80:83], v[96:99], a[144:147], v[80:83]\nv_mfma_f32_16x16x32_bf16 v[84:87], v[96:99], a[176:179], v[84:87]\n"
"ds_read_b128 v[96:99], v24 offset:32768\nv_mfma_f32_16x16x32_bf16 v[88:91], v[100:103], a[144:147], v[88:91]\nv_mfma_f32_16x16x32_bf16 v[92:95], v[100:103], a[176:179], v[92:95]\nds_read_b128 v[100:103], v28 offset:32768\n"
"s_waitcnt lgkmcnt(6)\nv_mfma_f32_16x16x32_bf16 v[80:83], v[104:107], a[148:151], v[80:83]\nv_mfma_f32_16x16x32_bf16 v[84:87], v[104:107], a[180:183], v[84:87]\nds_read_b128 v[104:107], v25 offset:32768\n"
"v_mfma_f32_16x16x32_bf16 v[88:91], v[108:111], a[148:151], v[88:91]\nv_mfma_f32_16x16x32_bf16 v[92:95], v[108:111], a[180:183], v[92:95]\nds_read_b128 v[108:111], v29 offset:32768\ns_waitcnt lgkmcnt(6)\n"
"v_mfma_f32_16x16x32_bf16 v[80:83], v[112:115], a[152:155], v[80:83]\nv_mfma_f32_16x16x32_bf16 v[84:87], v[112:115], a[184:187], v[84:87]\nds_read_b128 v[112:115], v26 offset:32768\nv_mfma_f32_16x16x32_bf16 v[88:91], v[116:119], a[152:155], v[88:91]\n"
"v_mfma_f32_16x16x32_bf16 v[92:95], v[116:119], a[184:187], v[92:95]\nds_read_b128 v[116:119], v30 offset:32768\ns_waitcnt lgkmcnt(6)\nv_mfma_f32_16x16x32_bf16 v[80:83], v[120:123], a[156:159], v[80:83]\n"
"v_mfma_f32_16x16x32_bf16 v[84:87], v[120:123], a[188:191], v[84:87]\nds_read_b128 v[120:123], v27 offset:32768\nv_mfma_f32_16x16x32_bf16 v[88:91], v[124:127], a[156:159], v[88:91]\nv_mfma_f32_16x16x32_bf16 v[92:95], v[124:127], a[188:191], v[92:95]\n"
"ds_read_b128 v[124:127], v31 offset:32768\ns_waitcnt lgkmcnt(6)\nv_mfma_f32_16x16x32_bf16 v[64:67], v[96:99], a[128:131], 0\ns_lshl_b32 s53, s43, 19\n"
"s_add_u32 s53, s53, s30\nv_mfma_f32_16x16x32_bf16 v[68:71], v[96:99], a[160:163], 0\nds_read_b128 v[96:99], v24 offset:33024\ns_add_u32 m0, s31, 131072\n"
"s_add_u32 s53, s53, 0\nv_mfma_f32_16x16x32_bf16 v[72:75], v[100:103], a[128:131], 0\nbuffer_load_dwordx4 v8, s[8:11], s53 offen lds\nv_mfma_f32_16x16x32_bf16 v[76:79], v[100:103], a[160:163], 0\n"
"ds_read_b128 v[100:103], v28 offset:33024\nbuffer_load_dwordx4 v9, s[8:11], s53 offen offset:1024 lds\ns_waitcnt lgkmcnt(6)\nv_mfma_f32_16x16x32_bf16 v[64:67], v[104:107], a[132:135], v[64:67]\n"
"v_cvt_pk_bf16_f32 v136, v80, v81\nv_cvt_pk_bf16_f32 v137, v82, v83\nv_mfma_f32_16x16x32_bf16 v[68:71], v[104:107], a[164:167], v[68:71]\nds_read_b128 v[104:107], v25 offset:33024\n"
"v_cvt_pk_bf16_f32 v138, v88, v89\nv_cvt_pk_bf16_f32 v139, v90, v91\nv_mfma_f32_16x16x32_bf16 v[72:75], v[108:111], a[132:135], v[72:75]\nv_cvt_pk_bf16_f32 v140, v84, v85\n"
"v_cvt_pk_bf16_f32 v141, v86, v87\nv_mfma_f32_16x16x32_bf16 v[76:79], v[108:111], a[164:167], v[76:79]\nds_read_b128 v[108:111], v29 offset:33024\nv_cvt_pk_bf16_f32 v142, v92, v93\n"
"v_cvt_pk_bf16_f32 v143, v94, v95\ns_waitcnt lgkmcnt(6)\nv_mfma_f32_16x16x32_bf16 v[64:67], v[112:115], a[136:139], v[64:67]\ns_lshl_b32 s54, s40, 11\n"
"s_add_u32 s54, s54, 1024\nv_mfma_f32_16x16x32_bf16 v[68:71], v[112:115], a[168:171], v[68:71]\nds_read_b128 v[112:115], v26 offset:33024\nbuffer_store_dwordx4 v[136:139], v13, s[16:19], s54 offen sc1\n"
"v_mfma_f32_16x16x32_bf16 v[72:75], v[116:119], a[136:139], v[72:75]\ns_add_u32 s54, s54, 262144\nv_mfma_f32_16x16x32_bf16 v[76:79], v[116:119], a[168:171], v[76:79]\nds_read_b128 v[116:119], v30 offset:33024\n"
"buffer_store_dwordx4 v[140:143], v13, s[16:19], s54 offen sc1\ns_waitcnt lgkmcnt(6)\nv_mfma_f32_16x16x32_bf16 v[64:67], v[120:123], a[140:143], v[64:67]\ns_lshl_b32 s53, s43, 19\n"
"s_add_u32 s53, s53, s30\nv_mfma_f32_16x16x32_bf16 v[68:71], v[120:123], a[172:175], v[68:71]\nds_read_b128 v[120:123], v27 offset:33024\ns_add_u32 m0, s31, 139264\n"
"s_add_u32 s53, s53, 131072\nv_mfma_f32_16x16x32_bf16 v[72:75], v[124:127], a[140:143], v[72:75]\nbuffer_load_dwordx4 v8, s[8:11], s53 offen lds\nv_mfma_f32_16x16x32_bf16 v[76:79], v[124:127], a[172:175], v[76:79]\n"
"ds_read_b128 v[124:127], v31 offset:33024\nbuffer_load_dwordx4 v9, s[8:11], s53 offen offset:1024 lds\ns_waitcnt lgkmcnt(6)\nv_mfma_f32_16x16x32_bf16 v[64:67], v[96:99], a[144:147], v[64:67]\n"
"v_mfma_f32_16x16x32_bf16 v[68:71], v[96:99], a[176:179], v[68:71]\nds_read_b128 v[96:99], v24 offset:49152\nv_mfma_f32_16x16x32_bf16 v[72:75], v[100:103], a[144:147], v[72:75]\nv_mfma_f32_16x16x32_bf16 v[76:79], v[100:103], a[176:179], v[76:79]\n"
"ds_read_b128 v[100:103], v28 offset:49152\ns_waitcnt lgkmcnt(6)\nv_mfma_f32_16x16x32_bf16 v[64:67], v[104:107], a[148:151], v[64:67]\nv_mfma_f32_16x16x32_bf16 v[68:71], v[104:107], a[180:183], v[68:71]\n"
"ds_read_b128 v[104:107], v25 offset:49152\nv_mfma_f32_16x16x32_bf16 v[72:75], v[108:111], a[148:151], v[72:75]\nv_mfma_f32_16x16x32_bf16 v[76:79], v[108:111], a[180:183], v[76:79]\nds_read_b128 v[108:111], v29 offset:49152\n"
"s_waitcnt lgkmcnt(6)\nv_mfma_f32_16x16x32_bf16 v[64:67], v[112:115], a[152:155], v[64:67]\nv_mfma_f32_16x16x32_bf16 v[68:71], v[112:115], a[184:187], v[68:71]\nds_read_b128 v[112:115], v26 offset:49152\n"
"v_mfma_f32_16x16x32_bf16 v[72:75], v[116:119], a[152:155], v[72:75]\nv_mfma_f32_16x16x32_bf16 v[76:79], v[116:119], a[184:187], v[76:79]\nds_read_b128 v[116:119], v30 offset:49152\ns_waitcnt lgkmcnt(6)\n"
"v_mfma_f32_16x16x32_bf16 v[64:67], v[120:123], a[156:159], v[64:67]\nv_mfma_f32_16x16x32_bf16 v[68:71], v[120:123], a[188:191], v[68:71]\nds_read_b128 v[120:123], v27 offset:49152\nv_mfma_f32_16x16x32_bf16 v[72:75], v[124:127], a[156:159], v[72:75]\n"
"v_mfma_f32_16x16x32_bf16 v[76:79], v[124:127], a[188:191], v[76:79]\nds_read_b128 v[124:127], v31 offset:49152\ns_waitcnt lgkmcnt(6)\nv_mfma_f32_16x16x32_bf16 v[80:83], v[96:99], a[128:131], 0\n"
"s_lshl_b32 s53, s43, 19\ns_add_u32 s53, s53, s30\nv_mfma_f32_16x16x32_bf16 v[84:87], v[96:99], a[160:163], 0\nds_read_b128 v[96:99], v24 offset:49408\n"
"s_add_u32 m0, s31, 147456\ns_add_u32 s53, s53, 262144\nv_mfma_f32_16x16x32_bf16 v[88:91], v[100:103], a[128:131], 0\nbuffer_load_dwordx4 v8, s[8:11], s53 offen lds\n"
"v_mfma_f32_16x16x32_bf16 v[92:95], v[100:103], a[160:163], 0\nds_read_b128 v[100:103], v28 offset:49408\nbuffer_load_dwordx4 v9, s[8:11], s53 offen offset:1024 lds\ns_waitcnt lgkmcnt(6)\n"
"v_mfma_f32_16x16x32_bf16 v[80:83], v[104:107], a[132:135], v[80:83]\nv_cvt_pk_bf16_f32 v144, v64, v65\nv_cvt_pk_bf16_f32 v145, v66, v67\nv_mfma_f32_16x16x32_bf16 v[84:87], v[104:107], a[164:167], v[84:87]\n"
"ds_read_b128 v[104:107], v25 offset:49408\nv_cvt_pk_bf16_f32 v146, v72, v73\nv_cvt_pk_bf16_f32 v147, v74, v75\nv_mfma_f32_16x16x32_bf16 v[88:91], v[108:111], a[132:135], v[88:91]\n"
"v_cvt_pk_bf16_f32 v148, v68, v69\nv_cvt_pk_bf16_f32 v149, v70, v71\nv_mfma_f32_16x16x32_bf16 v[92:95], v[108:111], a[164:167], v[92:95]\nds_read_b128 v[108:111], v29 offset:49408\n"
"v_cvt_pk_bf16_f32 v150, v76, v77\nv_cvt_pk_bf16_f32 v151, v78, v79\ns_waitcnt lgkmcnt(6)\nv_mfma_f32_16x16x32_bf16 v[80:83], v[112:115], a[136:139], v[80:83]\n"
"s_lshl_b32 s54, s41, 11\ns_add_u32 s54, s54, 0\nv_mfma_f32_16x16x32_bf16 v[84:87], v[112:115], a[168:171], v[84:87]\nds_read_b128 v[112:115], v26 offset:49408\n"
"buffer_store_dwordx4 v[144:147], v13, s[16:19], s54 offen sc1\nv_mfma_f32_16x16x32_bf16 v[88:91], v[116:119], a[136:139], v[88:91]\ns_add_u32 s54, s54, 262144\nv_mfma_f32_16x16x32_bf16 v[92:95], v[116:119], a[168:171], v[92:95]\n"
"ds_read_b128 v[116:119], v30 offset:49408\nbuffer_store_dwordx4 v[148:151], v13, s[16:19], s54 offen sc1\ns_waitcnt lgkmcnt(6)\nv_mfma_f32_16x16x32_bf16 v[80:83], v[120:123], a[140:143], v[80:83]\n"
"s_lshl_b32 s53, s43, 19\ns_add_u32 s53, s53, s30\nv_mfma_f32_16x16x32_bf16 v[84:87], v[120:123], a[172:175], v[84:87]\nds_read_b128 v[120:123], v27 offset:49408\n"
"s_add_u32 m0, s31, 155648\ns_add_u32 s53, s53, 393216\nv_mfma_f32_16x16x32_bf16 v[88:91], v[124:127], a[140:143], v[88:91]\nbuffer_load_dwordx4 v8, s[8:11], s53 offen lds\n"
"v_mfma_f32_16x16x32_bf16 v[92:95], v[124:127], a[172:175], v[92:95]\nds_read_b128 v[124:127], v31 offset:49408\nbuffer_load_dwordx4 v9, s[8:11], s53 offen offset:1024 lds\ns_waitcnt vmcnt(12)\n"
"s_barrier\ns_waitcnt lgkmcnt(6)\nv_mfma_f32_16x16x32_bf16 v[80:83], v[96:99], a[144:147], v[80:83]\nv_mfma_f32_16x16x32_bf16 v[84:87], v[96:99], a[176:179], v[84:87]\n"
"ds_read_b128 v[96:99], v40 offset:0\nv_mfma_f32_16x16x32_bf16 v[88:91], v[100:103], a[144:147], v[88:91]\nv_mfma_f32_16x16x32_bf16 v[92:95], v[100:103], a[176:179], v[92:95]\nds_read_b128 v[100:103], v44 offset:0\n"
"s_waitcnt lgkmcnt(6)\nv_mfma_f32_16x16x32_bf16 v[80:83], v[104:107], a[148:151], v[80:83]\nv_mfma_f32_16x16x32_bf16 v[84:87], v[104:107], a[180:183], v[84:87]\nds_read_b128 v[104:107], v41 offset:0\n"
"v_mfma_f32_16x16x32_bf16 v[88:91], v[108:111], a[148:151], v[88:91]\nv_mfma_f32_16x16x32_bf16 v[92:95], v[108:111], a[180:183], v[92:95]\nds_read_b128 v[108:111], v45 offset:0\ns_waitcnt lgkmcnt(6)\n"
"v_mfma_f32_16x16x32_bf16 v[80:83], v[112:115], a[152:155], v[80:83]\nv_mfma_f32_16x16x32_bf16 v[84:87], v[112:115], a[184:187], v[84:87]\nds_read_b128 v[112:115], v42 offset:0\nv_mfma_f32_16x16x32_bf16 v[88:91], v[116:119], a[152:155], v[88:91]\n"
"v_mfma_f32_16x16x32_bf16 v[92:95], v[116:119], a[184:187], v[92:95]\nds_read_b128 v[116:119], v46 offset:0\ns_waitcnt lgkmcnt(6)\nv_mfma_f32_16x16x32_bf16 v[80:83], v[120:123], a[156:159], v[80:83]\n"
"v_mfma_f32_16x16x32_bf16 v[84:87], v[120:123], a[188:191], v[84:87]\nds_read_b128 v[120:123], v43 offset:0\nv_mfma_f32_16x16x32_bf16 v[88:91], v[124:127], a[156:159], v[88:91]\nv_mfma_f32_16x16x32_bf16 v[92:95], v[124:127], a[188:191], v[92:95]\n"
"ds_read_b128 v[124:127], v47 offset:0\ns_waitcnt lgkmcnt(6)\nv_mfma_f32_16x16x32_bf16 v[64:67], v[96:99], a[128:131], 0\ns_add_u32 m0, s32, 0\n"
"v_mfma_f32_16x16x32_bf16 v[68:71], v[96:99], a[160:163], 0\nds_read_b128 v[96:99], v40 offset:256\ns_mov_b32 s53, 0\nv_mfma_f32_16x16x32_bf16 v[72:75], v[100:103], a[128:131], 0\n"
"buffer_load_dwordx4 v10, s[12:15], s53 offen lds\nv_mfma_f32_16x16x32_bf16 v[76:79], v[100:103], a[160:163], 0\nds_read_b128 v[100:103], v44 offset:256\nv_cvt_pk_bf16_f32 v152, v80, v81\n"
"v_cvt_pk_bf16_f32 v153, v82, v83\ns_waitcnt lgkmcnt(6)\nv_mfma_f32_16x16x32_bf16 v[64:67], v[104:107], a[132:135], v[64:67]\nv_cvt_pk_bf16_f32 v154, v88, v89\n"
"v_cvt_pk_bf16_f32 v155, v90, v91\nv_mfma_f32_16x16x32_bf16 v[68:71], v[104:107], a[164:167], v[68:71]\nds_read_b128 v[104:107], v41 offset:256\nv_cvt_pk_bf16_f32 v156, v84, v85\n"
"v_cvt_pk_bf16_f32 v157, v86, v87\nv_mfma_f32_16x16x32_bf16 v[72:75], v[108:111], a[132:135], v[72:75]\nv_cvt_pk_bf16_f32 v158, v92, v93\nv_cvt_pk_bf16_f32 v159, v94, v95\n"
"v_mfma_f32_16x16x32_bf16 v[76:79], v[108:111], a[164:167], v[76:79]\nds_read_b128 v[108:111], v45 offset:256\ns_lshl_b32 s54, s41, 11\ns_add_u32 s54, s54, 1024\n"
"s_waitcnt lgkmcnt(6)\nv_mfma_f32_16x16x32_bf16 v[64:67], v[112:115], a[136:139], v[64:67]\nbuffer_store_dwordx4 v[152:155], v13, s[16:19], s54 offen sc1\nv_mfma_f32_16x16x32_bf16 v[68:71], v[112:115], a[168:171], v[68:71]\n"
"ds_read_b128 v[112:115], v42 offset:256\ns_add_u32 s54, s54, 262144\nv_mfma_f32_16x16x32_bf16 v[72:75], v[116:119], a[136:139], v[72:75]\nbuffer_store_dwordx4 v[156:159], v13, s[16:19], s54 offen sc1\n"
"v_mfma_f32_16x16x32_bf16 v[76:79], v[116:119], a[168:171], v[76:79]\nds_read_b128 v[116:119], v46 offset:256\ns_add_u32 m0, s32, 4096\ns_waitcnt lgkmcnt(6)\n"
"v_mfma_f32_16x16x32_bf16 v[64:67], v[120:123], a[140:143], v[64:67]\ns_mov_b32 s53, 128\nv_mfma_f32_16x16x32_bf16 v[68:71], v[120:123], a[172:175], v[68:71]\nds_read_b128 v[120:123], v43 offset:256\n"
"buffer_load_dwordx4 v10, s[12:15], s53 offen lds\nv_mfma_f32_16x16x32_bf16 v[72:75], v[124:127], a[140:143], v[72:75]\ns_add_u32 m0, s32, 8192\nv_mfma_f32_16x16x32_bf16 v[76:79], v[124:127], a[172:175], v[76:79]\n"
"ds_read_b128 v[124:127], v47 offset:256\ns_mov_b32 s53, 256\ns_waitcnt lgkmcnt(6)\nv_mfma_f32_16x16x32_bf16 v[64:67], v[96:99], a[144:147], v[64:67]\n"
"buffer_load_dwordx4 v10, s[12:15], s53 offen lds\nv_mfma_f32_16x16x32_bf16 v[68:71], v[96:99], a[176:179], v[68:71]\nds_read_b128 v[96:99], v40 offset:16384\ns_add_u32 m0, s32, 12288\n"
"v_mfma_f32_16x16x32_bf16 v[72:75], v[100:103], a[144:147], v[72:75]\ns_mov_b32 s53, 384\nv_mfma_f32_16x16x32_bf16 v[76:79], v[100:103], a[176:179], v[76:79]\nds_read_b128 v[100:103], v44 offset:16384\n"
"buffer_load_dwordx4 v10, s[12:15], s53 offen lds\ns_waitcnt lgkmcnt(6)\nv_mfma_f32_16x16x32_bf16 v[64:67], v[104:107], a[148:151], v[64:67]\nv_mfma_f32_16x16x32_bf16 v[68:71], v[104:107], a[180:183], v[68:71]\n"
"ds_read_b128 v[104:107], v41 offset:16384\nv_mfma_f32_16x16x32_bf16 v[72:75], v[108:111], a[148:151], v[72:75]\nv_mfma_f32_16x16x32_bf16 v[76:79], v[108:111], a[180:183], v[76:79]\nds_read_b128 v[108:111], v45 offset:16384\n"
"s_waitcnt lgkmcnt(6)\nv_mfma_f32_16x16x32_bf16 v[64:67], v[112:115], a[152:155], v[64:67]\nv_mfma_f32_16x16x32_bf16 v[68:71], v[112:115], a[184:187], v[68:71]\nds_read_b128 v[112:115], v42 offset:16384\n"
"v_mfma_f32_16x16x32_bf16 v[72:75], v[116:119], a[152:155], v[72:75]\nv_mfma_f32_16x16x32_bf16 v[76:79], v[116:119], a[184:187], v[76:79]\nds_read_b128 v[116:119], v46 offset:16384\ns_waitcnt lgkmcnt(6)\n"
"v_mfma_f32_16x16x32_bf16 v[64:67], v[120:123], a[156:159], v[64:67]\nv_mfma_f32_16x16x32_bf16 v[68:71], v[120:123], a[188:191], v[68:71]\nds_read_b128 v[120:123], v43 offset:16384\nv_mfma_f32_16x16x32_bf16 v[72:75], v[124:127], a[156:159], v[72:75]\n"
"v_mfma_f32_16x16x32_bf16 v[76:79], v[124:127], a[188:191], v[76:79]\nds_read_b128 v[124:127], v47 offset:16384\ns_waitcnt lgkmcnt(6)\nv_mfma_f32_16x16x32_bf16 v[80:83], v[96:99], a[128:131], 0\n"
"s_add_u32 m0, s32, 16384\nv_mfma_f32_16x16x32_bf16 v[84:87], v[96:99], a[160:163], 0\nds_read_b128 v[96:99], v40 offset:16640\ns_mov_b32 s53, 512\n"
"v_mfma_f32_16x16x32_bf16 v[88:91], v[100:103], a[128:131], 0\nbuffer_load_dwordx4 v10, s[12:15], s53 offen lds\nv_mfma_f32_16x16x32_bf16 v[92:95], v[100:103], a[160:163], 0\nds_read_b128 v[100:103], v44 offset:16640\n"
"v_cvt_pk_bf16_f32 v160, v64, v65\nv_cvt_pk_bf16_f32 v161, v66, v67\ns_waitcnt lgkmcnt(6)\nv_mfma_f32_16x16x32_bf16 v[80:83], v[104:107], a[132:135], v[80:83]\n"
"v_cvt_pk_bf16_f32 v162, v72, v73\nv_cvt_pk_bf16_f32 v163, v74, v75\nv_mfma_f32_16x16x32_bf16 v[84:87], v[104:107], a[164:167], v[84:87]\nds_read_b128 v[104:107], v41 offset:16640\n"
"v_cvt_pk_bf16_f32 v164, v68, v69\nv_cvt_pk_bf16_f32 v165, v70, v71\nv_mfma_f32_16x16x32_bf16 v[88:91], v[108:111], a[132:135], v[88:91]\nv_cvt_pk_bf16_f32 v166, v76, v77\n"
"v_cvt_pk_bf16_f32 v167, v78, v79\nv_mfma_f32_16x16x32_bf16 v[92:95], v[108:111], a[164:167], v[92:95]\nds_read_b128 v[108:111], v45 offset:16640\ns_lshl_b32 s54, s42, 11\n"
"s_add_u32 s54, s54, 0\ns_waitcnt lgkmcnt(6)\nv_mfma_f32_16x16x32_bf16 v[80:83], v[112:115], a[136:139], v[80:83]\nbuffer_store_dwordx4 v[160:163], v13, s[16:19], s54 offen sc1\n"
"v_mfma_f32_16x16x32_bf16 v[84:87], v[112:115], a[168:171], v[84:87]\nds_read_b128 v[112:115], v42 offset:16640\ns_add_u32 s54, s54, 262144\nv_mfma_f32_16x16x32_bf16 v[88:91], v[116:119], a[136:139], v[88:91]\n"
"buffer_store_dwordx4 v[164:167], v13, s[16:19], s54 offen sc1\nv_mfma_f32_16x16x32_bf16 v[92:95], v[116:119], a[168:171], v[92:95]\nds_read_b128 v[116:119], v46 offset:16640\ns_add_u32 m0, s32, 20480\n"
"s_waitcnt lgkmcnt(6)\nv_mfma_f32_16x16x32_bf16 v[80:83], v[120:123], a[140:143], v[80:83]\ns_mov_b32 s53, 640\nv_mfma_f32_16x16x32_bf16 v[84:87], v[120:123], a[172:175], v[84:87]\n"
"ds_read_b128 v[120:123], v43 offset:16640\nbuffer_load_dwordx4 v10, s[12:15], s53 offen lds\nv_mfma_f32_16x16x32_bf16 v[88:91], v[124:127], a[140:143], v[88:91]\ns_add_u32 m0, s32, 24576\n"
"v_mfma_f32_16x16x32_bf16 v[92:95], v[124:127], a[172:175], v[92:95]\nds_read_b128 v[124:127], v47 offset:16640\ns_mov_b32 s53, 768\ns_waitcnt vmcnt(10)\n"
"s_barrier\ns_waitcnt lgkmcnt(6)\nv_mfma_f32_16x16x32_bf16 v[80:83], v[96:99], a[144:147], v[80:83]\nbuffer_load_dwordx4 v10, s[12:15], s53 offen lds\n"
"v_mfma_f32_16x16x32_bf16 v[84:87], v[96:99], a[176:179], v[84:87]\nds_read_b128 v[96:99], v40 offset:32768\nv_mfma_f32_16x16x32_bf16 v[88:91], v[100:103], a[144:147], v[88:91]\nv_mfma_f32_16x16x32_bf16 v[92:95], v[100:103], a[176:179], v[92:95]\n"
"ds_read_b128 v[100:103], v44 offset:32768\ns_waitcnt lgkmcnt(6)\nv_mfma_f32_16x16x32_bf16 v[80:83], v[104:107], a[148:151], v[80:83]\nv_mfma_f32_16x16x32_bf16 v[84:87], v[104:107], a[180:183], v[84:87]\n"
"ds_read_b128 v[104:107], v41 offset:32768\nv_mfma_f32_16x16x32_bf16 v[88:91], v[108:111], a[148:151], v[88:91]\nv_mfma_f32_16x16x32_bf16 v[92:95], v[108:111], a[180:183], v[92:95]\nds_read_b128 v[108:111], v45 offset:32768\n"
"s_waitcnt lgkmcnt(6)\nv_mfma_f32_16x16x32_bf16 v[80:83], v[112:115], a[152:155], v[80:83]\nv_mfma_f32_16x16x32_bf16 v[84:87], v[112:115], a[184:187], v[84:87]\nds_read_b128 v[112:115], v42 offset:32768\n"
"v_mfma_f32_16x16x32_bf16 v[88:91], v[116:119], a[152:155], v[88:91]\nv_mfma_f32_16x16x32_bf16 v[92:95], v[116:119], a[184:187], v[92:95]\nds_read_b128 v[116:119], v46 offset:32768\ns_waitcnt lgkmcnt(6)\n"
"v_mfma_f32_16x16x32_bf16 v[80:83], v[120:123], a[156:159], v[80:83]\nv_mfma_f32_16x16x32_bf16 v[84:87], v[120:123], a[188:191], v[84:87]\nds_read_b128 v[120:123], v43 offset:32768\nv_mfma_f32_16x16x32_bf16 v[88:91], v[124:127], a[156:159], v[88:91]\n"
"v_mfma_f32_16x16x32_bf16 v[92:95], v[124:127], a[188:191], v[92:95]\nds_read_b128 v[124:127], v47 offset:32768\ns_cmp_lg_u32 s34, 2\ns_cbranch_scc1 fg1_main\n"
"s_nop 7\nv_cvt_pk_bf16_f32 v168, v80, v81\nv_cvt_pk_bf16_f32 v169, v82, v83\nv_cvt_pk_bf16_f32 v170, v88, v89\n"
"v_cvt_pk_bf16_f32 v171, v90, v91\nv_cvt_pk_bf16_f32 v172, v84, v85\nv_cvt_pk_bf16_f32 v173, v86, v87\nv_cvt_pk_bf16_f32 v174, v92, v93\n"
"v_cvt_pk_bf16_f32 v175, v94, v95\ns_lshl_b32 s54, s42, 11\ns_add_u32 s54, s54, 1024\nbuffer_store_dwordx4 v[168:171], v13, s[16:19], s54 offen sc1\n"
"s_add_u32 s54, s54, 262144\nbuffer_store_dwordx4 v[172:175], v13, s[16:19], s54 offen sc1\nv_mov_b32 v36, s28\ns_waitcnt vmcnt(0) lgkmcnt(0)\n"
"s_barrier\nbuffer_store_dword v36, v19, s[24:27], 0 offen sc1\ns_branch fg1_end\nfg1_main:\n"
"s_waitcnt lgkmcnt(6)\nv_mfma_f32_16x16x32_bf16 v[64:67], v[96:99], a[128:131], 0\ns_add_u32 m0, s32, 32768\nv_mfma_f32_16x16x32_bf16 v[68:71], v[96:99], a[160:163], 0\n"
"ds_read_b128 v[96:99], v40 offset:33024\ns_mov_b32 s53, 28672\nv_mfma_f32_16x16x32_bf16 v[72:75], v[100:103], a[128:131], 0\nbuffer_load_dwordx4 v10, s[12:15], s53 offen lds\n"
"v_mfma_f32_16x16x32_bf16 v[76:79], v[100:103], a[160:163], 0\nds_read_b128 v[100:103], v44 offset:33024\nv_cvt_pk_bf16_f32 v168, v80, v81\nv_cvt_pk_bf16_f32 v169, v82, v83\n"
"s_waitcnt lgkmcnt(6)\nv_mfma_f32_16x16x32_bf16 v[64:67], v[104:107], a[132:135], v[64:67]\nv_cvt_pk_bf16_f32 v170, v88, v89\nv_cvt_pk_bf16_f32 v171, v90, v91\n"
"v_mfma_f32_16x16x32_bf16 v[68:71], v[104:107], a[164:167], v[68:71]\nds_read_b128 v[104:107], v41 offset:33024\nv_cvt_pk_bf16_f32 v172, v84, v85\nv_cvt_pk_bf16_f32 v173, v86, v87\n"
"v_mfma_f32_16x16x32_bf16 v[72:75], v[108:111], a[132:135], v[72:75]\nv_cvt_pk_bf16_f32 v174, v92, v93\nv_cvt_pk_bf16_f32 v175, v94, v95\nv_mfma_f32_16x16x32_bf16 v[76:79], v[108:111], a[164:167], v[76:79]\n"
"ds_read_b128 v[108:111], v45 offset:33024\ns_lshl_b32 s54, s42, 11\ns_add_u32 s54, s54, 1024\ns_waitcnt lgkmcnt(6)\n"
"v_mfma_f32_16x16x32_bf16 v[64:67], v[112:115], a[136:139], v[64:67]\nbuffer_store_dwordx4 v[168:171], v13, s[16:19], s54 offen sc1\nv_mfma_f32_16x16x32_bf16 v[68:71], v[112:115], a[168:171], v[68:71]\nds_read_b128 v[112:115], v42 offset:33024\n"
"s_add_u32 s54, s54, 262144\nv_mfma_f32_16x16x32_bf16 v[72:75], v[116:119], a[136:139], v[72:75]\nbuffer_store_dwordx4 v[172:175], v13, s[16:19], s54 offen sc1\nv_mfma_f32_16x16x32_bf16 v[76:79], v[116:119], a[168:171], v[76:79]\n"
"ds_read_b128 v[116:119], v46 offset:33024\ns_add_u32 m0, s32, 36864\ns_waitcnt lgkmcnt(6)\nv_mfma_f32_16x16x32_bf16 v[64:67], v[120:123], a[140:143], v[64:67]\n"
"s_mov_b32 s53, 28800\nv_mfma_f32_16x16x32_bf16 v[68:71], v[120:123], a[172:175], v[68:71]\nds_read_b128 v[120:123], v43 offset:33024\nbuffer_load_dwordx4 v10, s[12:15], s53 offen lds\n"
"v_mfma_f32_16x16x32_bf16 v[72:75], v[124:127], a[140:143], v[72:75]\ns_add_u32 m0, s32, 40960\nv_mfma_f32_16x16x32_bf16 v[76:79], v[124:127], a[172:175], v[76:79]\nds_read_b128 v[124:127], v47 offset:33024\n"
"s_mov_b32 s53, 28928\ns_waitcnt lgkmcnt(6)\nv_mfma_f32_16x16x32_bf16 v[64:67], v[96:99], a[144:147], v[64:67]\nbuffer_load_dwordx4 v10, s[12:15], s53 offen lds\n"
"v_mfma_f32_16x16x32_bf16 v[68:71], v[96:99], a[176:179], v[68:71]\nds_read_b128 v[96:99], v40 offset:49152\ns_add_u32 m0, s32, 45056\nv_mfma_f32_16x16x32_bf16 v[72:75], v[100:103], a[144:147], v[72:75]\n"
"s_mov_b32 s53, 29056\nv_mfma_f32_16x16x32_bf16 v[76:79], v[100:103], a[176:179], v[76:79]\nds_read_b128 v[100:103], v44 offset:49152\nbuffer_load_dwordx4 v10, s[12:15], s53 offen lds\n"
"s_waitcnt lgkmcnt(6)\nv_mfma_f32_16x16x32_bf16 v[64:67], v[104:107], a[148:151], v[64:67]\nv_mfma_f32_16x16x32_bf16 v[68:71], v[104:107], a[180:183], v[68:71]\nds_read_b128 v[104:107], v41 offset:49152\n"
"v_mfma_f32_16x16x32_bf16 v[72:75], v[108:111], a[148:151], v[72:75]\nv_mfma_f32_16x16x32_bf16 v[76:79], v[108:111], a[180:183], v[76:79]\nds_read_b128 v[108:111], v45 offset:49152\ns_waitcnt lgkmcnt(6)\n"
"v_mfma_f32_16x16x32_bf16 v[64:67], v[112:115], a[152:155], v[64:67]\nv_mfma_f32_16x16x32_bf16 v[68:71], v[112:115], a[184:187], v[68:71]\nds_read_b128 v[112:115], v42 offset:49152\nv_mfma_f32_16x16x32_bf16 v[72:75], v[116:119], a[152:155], v[72:75]\n"
"v_mfma_f32_16x16x32_bf16 v[76:79], v[116:119], a[184:187], v[76:79]\nds_read_b128 v[116:119], v46 offset:49152\ns_waitcnt lgkmcnt(6)\nv_mfma_f32_16x16x32_bf16 v[64:67], v[120:123], a[156:159], v[64:67]\n"
"v_mfma_f32_16x16x32_bf16 v[68:71], v[120:123], a[188:191], v[68:71]\nds_read_b128 v[120:123], v43 offset:49152\nv_mfma_f32_16x16x32_bf16 v[72:75], v[124:127], a[156:159], v[72:75]\nv_mfma_f32_16x16x32_bf16 v[76:79], v[124:127], a[188:191], v[76:79]\n"
"ds_read_b128 v[124:127], v47 offset:49152\ns_waitcnt lgkmcnt(6)\nv_mfma_f32_16x16x32_bf16 v[80:83], v[96:99], a[128:131], 0\ns_add_u32 m0, s32, 49152\n"
"v_mfma_f32_16x16x32_bf16 v[84:87], v[96:99], a[160:163], 0\nds_read_b128 v[96:99], v40 offset:49408\ns_mov_b32 s53, 29184\nv_mfma_f32_16x16x32_bf16 v[88:91], v[100:103], a[128:131], 0\n"
"buffer_load_dwordx4 v10, s[12:15], s53 offen lds\nv_mfma_f32_16x16x32_bf16 v[92:95], v[100:103], a[160:163], 0\nds_read_b128 v[100:103], v44 offset:49408\nv_cvt_pk_bf16_f32 v176, v64, v65\n"
"v_cvt_pk_bf16_f32 v177, v66, v67\ns_waitcnt lgkmcnt(6)\nv_mfma_f32_16x16x32_bf16 v[80:83], v[104:107], a[132:135], v[80:83]\nv_cvt_pk_bf16_f32 v178, v72, v73\n"
"v_cvt_pk_bf16_f32 v179, v74, v75\nv_mfma_f32_16x16x32_bf16 v[84:87], v[104:107], a[164:167], v[84:87]\nds_read_b128 v[104:107], v41 offset:49408\nv_cvt_pk_bf16_f32 v180, v68, v69\n"
"v_cvt_pk_bf16_f32 v181, v70, v71\nv_mfma_f32_16x16x32_bf16 v[88:91], v[108:111], a[132:135], v[88:91]\nv_cvt_pk_bf16_f32 v182, v76, v77\nv_cvt_pk_bf16_f32 v183, v78, v79\n"
"v_mfma_f32_16x16x32_bf16 v[92:95], v[108:111], a[164:167], v[92:95]\nds_read_b128 v[108:111], v45 offset:49408\ns_lshl_b32 s54, s43, 11\ns_add_u32 s54, s54, 0\n"
"s_waitcnt lgkmcnt(6)\nv_mfma_f32_16x16x32_bf16 v[80:83], v[112:115], a[136:139], v[80:83]\nbuffer_store_dwordx4 v[176:179], v13, s[16:19], s54 offen sc1\nv_mfma_f32_16x16x32_bf16 v[84:87], v[112:115], a[168:171], v[84:87]\n"
"ds_read_b128 v[112:115], v42 offset:49408\ns_add_u32 s54, s54, 262144\nv_mfma_f32_16x16x32_bf16 v[88:91], v[116:119], a[136:139], v[88:91]\nbuffer_store_dwordx4 v[180:183], v13, s[16:19], s54 offen sc1\n"
"v_mfma_f32_16x16x32_bf16 v[92:95], v[116:119], a[168:171], v[92:95]\nds_read_b128 v[116:119], v46 offset:49408\ns_add_u32 m0, s32, 53248\ns_waitcnt lgkmcnt(6)\n"
"v_mfma_f32_16x16x32_bf16 v[80:83], v[120:123], a[140:143], v[80:83]\ns_mov_b32 s53, 29312\nv_mfma_f32_16x16x32_bf16 v[84:87], v[120:123], a[172:175], v[84:87]\nds_read_b128 v[120:123], v43 offset:49408\n"
"buffer_load_dwordx4 v10, s[12:15], s53 offen lds\nv_mfma_f32_16x16x32_bf16 v[88:91], v[124:127], a[140:143], v[88:91]\ns_add_u32 m0, s32, 57344\nv_mfma_f32_16x16x32_bf16 v[92:95], v[124:127], a[172:175], v[92:95]\n"
"ds_read_b128 v[124:127], v47 offset:49408\ns_mov_b32 s53, 29440\ns_waitcnt lgkmcnt(6)\nv_mfma_f32_16x16x32_bf16 v[80:83], v[96:99], a[144:147], v[80:83]\n"
"buffer_load_dwordx4 v10, s[12:15], s53 offen lds\nv_mfma_f32_16x16x32_bf16 v[84:87], v[96:99], a[176:179], v[84:87]\nbuffer_load_dword v37, v21, s[24:27], 0 offen sc1\nv_mfma_f32_16x16x32_bf16 v[88:91], v[100:103], a[144:147], v[88:91]\n"
"v_mfma_f32_16x16x32_bf16 v[92:95], v[100:103], a[176:179], v[92:95]\ns_waitcnt lgkmcnt(4)\nv_mfma_f32_16x16x32_bf16 v[80:83], v[104:107], a[148:151], v[80:83]\nv_mfma_f32_16x16x32_bf16 v[84:87], v[104:107], a[180:183], v[84:87]\n"
"v_mfma_f32_16x16x32_bf16 v[88:91], v[108:111], a[148:151], v[88:91]\nv_mfma_f32_16x16x32_bf16 v[92:95], v[108:111], a[180:183], v[92:95]\ns_waitcnt lgkmcnt(2)\nv_mfma_f32_16x16x32_bf16 v[80:83], v[112:115], a[152:155], v[80:83]\n"
"v_mfma_f32_16x16x32_bf16 v[84:87], v[112:115], a[184:187], v[84:87]\nv_mfma_f32_16x16x32_bf16 v[88:91], v[116:119], a[152:155], v[88:91]\nv_mfma_f32_16x16x32_bf16 v[92:95], v[116:119], a[184:187], v[92:95]\ns_waitcnt lgkmcnt(0)\n"
"v_mfma_f32_16x16x32_bf16 v[80:83], v[120:123], a[156:159], v[80:83]\nv_mfma_f32_16x16x32_bf16 v[84:87], v[120:123], a[188:191], v[84:87]\nv_mfma_f32_16x16x32_bf16 v[88:91], v[124:127], a[156:159], v[88:91]\nv_mfma_f32_16x16x32_bf16 v[92:95], v[124:127], a[188:191], v[92:95]\n"
"s_nop 7\nv_cvt_pk_bf16_f32 v184, v80, v81\nv_cvt_pk_bf16_f32 v185, v82, v83\nv_cvt_pk_bf16_f32 v186, v88, v89\n"
"v_cvt_pk_bf16_f32 v187, v90, v91\nv_cvt_pk_bf16_f32 v188, v84, v85\nv_cvt_pk_bf16_f32 v189, v86, v87\nv_cvt_pk_bf16_f32 v190, v92, v93\n"
"v_cvt_pk_bf16_f32 v191, v94, v95\ns_lshl_b32 s54, s43, 11\ns_add_u32 s54, s54, 1024\nbuffer_store_dwordx4 v[184:187], v13, s[16:19], s54 offen sc1\n"
"s_add_u32 s54, s54, 262144\nbuffer_store_dwordx4 v[188:191], v13, s[16:19], s54 offen sc1\ns_waitcnt vmcnt(2)\nv_mov_b32 v36, s28\n"
"v_cmp_ne_u32 vcc, s28, v37\ns_cbranch_vccz fg1_e1\ns_waitcnt vmcnt(0)\ns_barrier\n"
"buffer_store_dword v36, v19, s[24:27], 0 offen sc1\nfg1_po:\nv_cmp_ne_u32 vcc, s28, v37\ns_cbranch_vccz fg1_pook\n"
"s_add_u32 s55, s55, 1\ns_cmp_gt_u32 s55, 262144\ns_cbranch_scc0 fg1_po2\ns_endpgm\n"
"fg1_po2:\ns_sleep 1\nbuffer_load_dword v37, v21, s[24:27], 0 offen sc1\ns_waitcnt vmcnt(0)\n"
"s_branch fg1_po\nfg1_pook:\ns_mov_b32 s53, 8192\nbuffer_load_dwordx4 v[192:195], v13, s[16:19], s53 offen sc1\n"
"s_mov_b32 s53, 270336\nbuffer_load_dwordx4 v[196:199], v13, s[16:19], s53 offen sc1\ns_mov_b32 s53, 9216\nbuffer_load_dwordx4 v[200:203], v13, s[16:19], s53 offen sc1\n"
"s_mov_b32 s53, 271360\nbuffer_load_dwordx4 v[204:207], v13, s[16:19], s53 offen sc1\ns_mov_b32 s53, 10240\nbuffer_load_dwordx4 v[208:211], v13, s[16:19], s53 offen sc1\n"
"s_mov_b32 s53, 272384\nbuffer_load_dwordx4 v[212:215], v13, s[16:19], s53 offen sc1\ns_mov_b32 s53, 11264\nbuffer_load_dwordx4 v[216:219], v13, s[16:19], s53 offen sc1\n"
"s_mov_b32 s53, 273408\nbuffer_load_dwordx4 v[220:223], v13, s[16:19], s53 offen sc1\ns_mov_b32 s53, 12288\nbuffer_load_dwordx4 v[224:227], v13, s[16:19], s53 offen sc1\n"
"s_mov_b32 s53, 274432\nbuffer_load_dwordx4 v[228:231], v13, s[16:19], s53 offen sc1\ns_mov_b32 s53, 13312\nbuffer_load_dwordx4 v[232:235], v13, s[16:19], s53 offen sc1\n"
"s_mov_b32 s53, 275456\nbuffer_load_dwordx4 v[236:239], v13, s[16:19], s53 offen sc1\nbuffer_store_dword v36, v19, s[24:27], 0 offen sc1\ns_branch fg1_e2\n"
"fg1_e1:\ns_mov_b32 s53, 8192\nbuffer_load_dwordx4 v[192:195], v13, s[16:19], s53 offen sc1\ns_mov_b32 s53, 270336\n"
"buffer_load_dwordx4 v[196:199], v13, s[16:19], s53 offen sc1\ns_mov_b32 s53, 9216\nbuffer_load_dwordx4 v[200:203], v13, s[16:19], s53 offen sc1\ns_mov_b32 s53, 271360\n"
"buffer_load_dwordx4 v[204:207], v13, s[16:19], s53 offen sc1\ns_mov_b32 s53, 10240\nbuffer_load_dwordx4 v[208:211], v13, s[16:19], s53 offen sc1\ns_mov_b32 s53, 272384\n"
"buffer_load_dwordx4 v[212:215], v13, s[16:19], s53 offen sc1\ns_mov_b32 s53, 11264\nbuffer_load_dwordx4 v[216:219], v13, s[16:19], s53 offen sc1\ns_mov_b32 s53, 273408\n"
"buffer_load_dwordx4 v[220:223], v13, s[16:19], s53 offen sc1\ns_mov_b32 s53, 12288\nbuffer_load_dwordx4 v[224:227], v13, s[16:19], s53 offen sc1\ns_mov_b32 s53, 274432\n"
"buffer_load_dwordx4 v[228:231], v13, s[16:19], s53 offen sc1\ns_mov_b32 s53, 13312\nbuffer_load_dwordx4 v[232:235], v13, s[16:19], s53 offen sc1\ns_mov_b32 s53, 275456\n"
"buffer_load_dwordx4 v[236:239], v13, s[16:19], s53 offen sc1\ns_waitcnt vmcnt(12)\ns_barrier\nbuffer_store_dword v36, v19, s[24:27], 0 offen sc1\n"
"fg1_e2:\ns_add_u32 m0, s32, 65536\ns_mov_b32 s53, 57344\nbuffer_load_dwordx4 v10, s[12:15], s53 offen lds\n"
"s_add_u32 m0, s32, 69632\ns_mov_b32 s53, 57472\nbuffer_load_dwordx4 v10, s[12:15], s53 offen lds\ns_add_u32 m0, s32, 73728\n"
"s_mov_b32 s53, 57600\nbuffer_load_dwordx4 v10, s[12:15], s53 offen lds\ns_add_u32 m0, s32, 77824\ns_mov_b32 s53, 57728\n"
"buffer_load_dwordx4 v10, s[12:15], s53 offen lds\ns_add_u32 m0, s32, 81920\ns_mov_b32 s53, 57856\nbuffer_load_dwordx4 v10, s[12:15], s53 offen lds\n"
"s_add_u32 m0, s32, 86016\ns_mov_b32 s53, 57984\nbuffer_load_dwordx4 v10, s[12:15], s53 offen lds\ns_add_u32 m0, s32, 90112\n"
"s_mov_b32 s53, 58112\nbuffer_load_dwordx4 v10, s[12:15], s53 offen lds\ns_sub_u32 s48, s28, 1\ns_mov_b32 s49, 86016\n"
"s_mov_b32 s50, 0\ns_mov_b32 s51, 0\ns_add_u32 s52, s32, 98304\ns_add_u32 s47, s32, 131072\n"
"ds_read_b128 v[40:43], v32 offset:0\nds_read_b128 v[44:47], v33 offset:0\nds_read_b128 v[48:51], v34 offset:0\nds_read_b128 v[52:55], v35 offset:0\n"
"s_barrier\ns_waitcnt lgkmcnt(3)\nv_mfma_f32_16x16x32_bf16 v[64:67], v[40:43], v[128:131], 0\nv_mov_b32 v36, s48\n"
"s_add_u32 s48, s48, 1\ns_nop 0\nbuffer_store_dword v36, v19, s[24:27], s33 offen sc1\nv_mfma_f32_16x16x32_bf16 v[68:71], v[40:43], v[132:135], 0\n"
"ds_read_b128 v[40:43], v32 offset:4096\ns_waitcnt lgkmcnt(3)\nv_mfma_f32_16x16x32_bf16 v[72:75], v[44:47], v[128:131], 0\nv_mfma_f32_16x16x32_bf16 v[76:79], v[44:47], v[132:135], 0\n"
"ds_read_b128 v[44:47], v33 offset:4096\ns_waitcnt lgkmcnt(3)\nv_mfma_f32_16x16x32_bf16 v[64:67], v[48:51], v[136:139], v[64:67]\ns_add_u32 m0, s52, 0\n"
"v_mfma_f32_16x16x32_bf16 v[68:71], v[48:51], v[140:143], v[68:71]\nds_read_b128 v[48:51], v34 offset:4096\ns_add_u32 s53, s49, 0\ns_waitcnt lgkmcnt(3)\n"
"v_mfma_f32_16x16x32_bf16 v[72:75], v[52:55], v[136:139], v[72:75]\nbuffer_load_dwordx4 v10, s[12:15], s53 offen lds\nv_mfma_f32_16x16x32_bf16 v[76:79], v[52:55], v[140:143], v[76:79]\nds_read_b128 v[52:55], v35 offset:4096\n"
"s_add_u32 m0, s52, 4096\ns_waitcnt lgkmcnt(3)\nv_mfma_f32_16x16x32_bf16 v[64:67], v[40:43], v[144:147], v[64:67]\ns_add_u32 s53, s49, 128\n"
"v_mfma_f32_16x16x32_bf16 v[68:71], v[40:43], v[148:151], v[68:71]\nds_read_b128 v[40:43], v32 offset:8192\nbuffer_load_dwordx4 v10, s[12:15], s53 offen lds\ns_waitcnt lgkmcnt(3)\n"
"v_mfma_f32_16x16x32_bf16 v[72:75], v[44:47], v[144:147], v[72:75]\ns_add_u32 m0, s52, 8192\nv_mfma_f32_16x16x32_bf16 v[76:79], v[44:47], v[148:151], v[76:79]\nds_read_b128 v[44:47], v33 offset:8192\n"
"s_add_u32 s53, s49, 256\ns_waitcnt lgkmcnt(3)\nv_mfma_f32_16x16x32_bf16 v[64:67], v[48:51], v[152:155], v[64:67]\nbuffer_load_dwordx4 v10, s[12:15], s53 offen lds\n"
"v_mfma_f32_16x16x32_bf16 v[68:71], v[48:51], v[156:159], v[68:71]\nds_read_b128 v[48:51], v34 offset:8192\ns_add_u32 m0, s52, 12288\ns_waitcnt lgkmcnt(3)\n"
"v_mfma_f32_16x16x32_bf16 v[72:75], v[52:55], v[152:155], v[72:75]\ns_add_u32 s53, s49, 384\nv_mfma_f32_16x16x32_bf16 v[76:79], v[52:55], v[156:159], v[76:79]\nds_read_b128 v[52:55], v35 offset:8192\n"
"buffer_load_dwordx4 v10, s[12:15], s53 offen lds\ns_waitcnt lgkmcnt(3)\nv_mfma_f32_16x16x32_bf16 v[64:67], v[40:43], v[160:163], v[64:67]\ns_add_u32 m0, s52, 16384\n"
"v_mfma_f32_16x16x32_bf16 v[68:71], v[40:43], v[164:167], v[68:71]\nds_read_b128 v[40:43], v32 offset:12288\ns_add_u32 s53, s49, 512\ns_waitcnt lgkmcnt(3)\n"
"v_mfma_f32_16x16x32_bf16 v[72:75], v[44:47], v[160:163], v[72:75]\nbuffer_load_dwordx4 v10, s[12:15], s53 offen lds\nv_mfma_f32_16x16x32_bf16 v[76:79], v[44:47], v[164:167], v[76:79]\nds_read_b128 v[44:47], v33 offset:12288\n"
"s_add_u32 m0, s52, 20480\ns_waitcnt lgkmcnt(3)\nv_mfma_f32_16x16x32_bf16 v[64:67], v[48:51], v[168:171], v[64:67]\ns_add_u32 s53, s49, 640\n"
"v_mfma_f32_16x16x32_bf16 v[68:71], v[48:51], v[172:175], v[68:71]\nds_read_b128 v[48:51], v34 offset:12288\nbuffer_load_dwordx4 v10, s[12:15], s53 offen lds\ns_waitcnt lgkmcnt(3)\n"
"v_mfma_f32_16x16x32_bf16 v[72:75], v[52:55], v[168:171], v[72:75]\ns_add_u32 m0, s52, 24576\nv_mfma_f32_16x16x32_bf16 v[76:79], v[52:55], v[172:175], v[76:79]\nds_read_b128 v[52:55], v35 offset:12288\n"
"s_add_u32 s53, s49, 768\ns_waitcnt lgkmcnt(3)\nv_mfma_f32_16x16x32_bf16 v[64:67], v[40:43], v[176:179], v[64:67]\nbuffer_load_dwordx4 v10, s[12:15], s53 offen lds\n"
"v_mfma_f32_16x16x32_bf16 v[68:71], v[40:43], v[180:183], v[68:71]\nds_read_b128 v[40:43], v32 offset:16384\ns_waitcnt lgkmcnt(3)\nv_mfma_f32_16x16x32_bf16 v[72:75], v[44:47], v[176:179], v[72:75]\n"
"v_mfma_f32_16x16x32_bf16 v[76:79], v[44:47], v[180:183], v[76:79]\nds_read_b128 v[44:47], v33 offset:16384\ns_waitcnt lgkmcnt(3)\nv_mfma_f32_16x16x32_bf16 v[64:67], v[48:51], v[184:187], v[64:67]\n"
"v_mfma_f32_16x16x32_bf16 v[68:71], v[48:51], v[188:191], v[68:71]\nds_read_b128 v[48:51], v34 offset:16384\ns_waitcnt lgkmcnt(3)\nv_mfma_f32_16x16x32_bf16 v[72:75], v[52:55], v[184:187], v[72:75]\n"
"v_mfma_f32_16x16x32_bf16 v[76:79], v[52:55], v[188:191], v[76:79]\nds_read_b128 v[52:55], v35 offset:16384\ns_waitcnt vmcnt(16)\ns_waitcnt lgkmcnt(3)\n"
"v_mfma_f32_16x16x32_bf16 v[64:67], v[40:43], v[192:195], v[64:67]\nv_mfma_f32_16x16x32_bf16 v[68:71], v[40:43], v[196:199], v[68:71]\nds_read_b128 v[40:43], v32 offset:20480\ns_waitcnt lgkmcnt(3)\n"
"v_mfma_f32_16x16x32_bf16 v[72:75], v[44:47], v[192:195], v[72:75]\nv_mfma_f32_16x16x32_bf16 v[76:79], v[44:47], v[196:199], v[76:79]\nds_read_b128 v[44:47], v33 offset:20480\ns_waitcnt lgkmcnt(3)\n"
"v_mfma_f32_16x16x32_bf16 v[64:67], v[48:51], v[200:203], v[64:67]\nv_mfma_f32_16x16x32_bf16 v[68:71], v[48:51], v[204:207], v[68:71]\nds_read_b128 v[48:51], v34 offset:20480\ns_waitcnt lgkmcnt(3)\n"
"v_mfma_f32_16x16x32_bf16 v[72:75], v[52:55], v[200:203], v[72:75]\nv_mfma_f32_16x16x32_bf16 v[76:79], v[52:55], v[204:207], v[76:79]\nds_read_b128 v[52:55], v35 offset:20480\ns_waitcnt lgkmcnt(3)\n"
"v_mfma_f32_16x16x32_bf16 v[64:67], v[40:43], v[208:211], v[64:67]\nv_mfma_f32_16x16x32_bf16 v[68:71], v[40:43], v[212:215], v[68:71]\nds_read_b128 v[40:43], v32 offset:24576\ns_waitcnt lgkmcnt(3)\n"
"v_mfma_f32_16x16x32_bf16 v[72:75], v[44:47], v[208:211], v[72:75]\nv_mfma_f32_16x16x32_bf16 v[76:79], v[44:47], v[212:215], v[76:79]\nds_read_b128 v[44:47], v33 offset:24576\ns_waitcnt lgkmcnt(3)\n"
"v_mfma_f32_16x16x32_bf16 v[64:67], v[48:51], v[216:219], v[64:67]\ns_add_u32 s54, s51, 32768\ns_cmp_eq_u32 s54, 163840\nv_mfma_f32_16x16x32_bf16 v[68:71], v[48:51], v[220:223], v[68:71]\n"
"s_cselect_b32 s54, 0, s54\ns_sub_u32 s54, s54, s51\nds_read_b128 v[48:51], v34 offset:24576\ns_waitcnt lgkmcnt(3)\n"
"v_mfma_f32_16x16x32_bf16 v[72:75], v[52:55], v[216:219], v[72:75]\nv_mfma_f32_16x16x32_bf16 v[76:79], v[52:55], v[220:223], v[76:79]\nds_read_b128 v[52:55], v35 offset:24576\ns_waitcnt lgkmcnt(3)\n"
"v_mfma_f32_16x16x32_bf16 v[64:67], v[40:43], v[224:227], v[64:67]\nv_add_u32 v32, s54, v32\nv_mfma_f32_16x16x32_bf16 v[68:71], v[40:43], v[228:231], v[68:71]\nds_read_b128 v[40:43], v32 offset:0\n"
"s_waitcnt lgkmcnt(3)\nv_mfma_f32_16x16x32_bf16 v[72:75], v[44:47], v[224:227], v[72:75]\nv_add_u32 v33, s54, v33\nv_mfma_f32_16x16x32_bf16 v[76:79], v[44:47], v[228:231], v[76:79]\n"
"ds_read_b128 v[44:47], v33 offset:0\ns_waitcnt lgkmcnt(3)\nv_mfma_f32_16x16x32_bf16 v[64:67], v[48:51], v[232:235], v[64:67]\nv_add_u32 v34, s54, v34\n"
"v_mfma_f32_16x16x32_bf16 v[68:71], v[48:51], v[236:239], v[68:71]\nds_read_b128 v[48:51], v34 offset:0\ns_waitcnt lgkmcnt(3)\nv_mfma_f32_16x16x32_bf16 v[72:75], v[52:55], v[232:235], v[72:75]\n"
"v_add_u32 v35, s54, v35\nv_mfma_f32_16x16x32_bf16 v[76:79], v[52:55], v[236:239], v[76:79]\nds_read_b128 v[52:55], v35 offset:0\ns_mov_b32 s52, s47\n"
"s_add_u32 s47, s51, s32\ns_add_u32 s51, s51, s54\ns_add_u32 s49, s49, 28672\ns_waitcnt vmcnt(8)\n"
"s_barrier\ns_waitcnt lgkmcnt(3)\nv_mfma_f32_16x16x32_bf16 v[96:99], v[40:43], v[128:131], 0\nv_mov_b32 v36, s48\n"
"s_add_u32 s48, s48, 1\ns_nop 0\nbuffer_store_dword v36, v19, s[24:27], s33 offen sc1\nv_mfma_f32_16x16x32_bf16 v[100:103], v[40:43], v[132:135], 0\n"
"ds_read_b128 v[40:43], v32 offset:4096\ns_waitcnt lgkmcnt(3)\nv_mfma_f32_16x16x32_bf16 v[104:107], v[44:47], v[128:131], 0\nv_mfma_f32_16x16x32_bf16 v[108:111], v[44:47], v[132:135], 0\n"
"ds_read_b128 v[44:47], v33 offset:4096\nv_cvt_pk_bf16_f32 v56, v64, v65\ns_waitcnt lgkmcnt(3)\nv_mfma_f32_16x16x32_bf16 v[96:99], v[48:51], v[136:139], v[96:99]\n"
"v_cvt_pk_bf16_f32 v57, v66, v67\nv_mfma_f32_16x16x32_bf16 v[100:103], v[48:51], v[140:143], v[100:103]\nds_read_b128 v[48:51], v34 offset:4096\nv_cvt_pk_bf16_f32 v58, v72, v73\n"
"s_waitcnt lgkmcnt(3)\nv_mfma_f32_16x16x32_bf16 v[104:107], v[52:55], v[136:139], v[104:107]\nv_cvt_pk_bf16_f32 v59, v74, v75\nv_mfma_f32_16x16x32_bf16 v[108:111], v[52:55], v[140:143], v[108:111]\n"
"ds_read_b128 v[52:55], v35 offset:4096\ns_waitcnt lgkmcnt(3)\nv_mfma_f32_16x16x32_bf16 v[96:99], v[40:43], v[144:147], v[96:99]\nv_mfma_f32_16x16x32_bf16 v[100:103], v[40:43], v[148:151], v[100:103]\n"
"ds_read_b128 v[40:43], v32 offset:8192\nbuffer_store_dwordx4 v[56:59], v15, s[20:23], s50 offen sc1\ns_waitcnt lgkmcnt(3)\nv_mfma_f32_16x16x32_bf16 v[104:107], v[44:47], v[144:147], v[104:107]\n"
"v_cvt_pk_bf16_f32 v60, v68, v69\nv_mfma_f32_16x16x32_bf16 v[108:111], v[44:47], v[148:151], v[108:111]\nds_read_b128 v[44:47], v33 offset:8192\nv_cvt_pk_bf16_f32 v61, v70, v71\n"
"s_waitcnt lgkmcnt(3)\nv_mfma_f32_16x16x32_bf16 v[96:99], v[48:51], v[152:155], v[96:99]\nv_cvt_pk_bf16_f32 v62, v76, v77\nv_mfma_f32_16x16x32_bf16 v[100:103], v[48:51], v[156:159], v[100:103]\n"
"ds_read_b128 v[48:51], v34 offset:8192\nv_cvt_pk_bf16_f32 v63, v78, v79\ns_waitcnt lgkmcnt(3)\nv_mfma_f32_16x16x32_bf16 v[104:107], v[52:55], v[152:155], v[104:107]\n"
"v_mfma_f32_16x16x32_bf16 v[108:111], v[52:55], v[156:159], v[108:111]\nds_read_b128 v[52:55], v35 offset:8192\ns_waitcnt lgkmcnt(3)\nv_mfma_f32_16x16x32_bf16 v[96:99], v[40:43], v[160:163], v[96:99]\n"
"buffer_store_dwordx4 v[60:63], v16, s[20:23], s50 offen sc1\nv_mfma_f32_16x16x32_bf16 v[100:103], v[40:43], v[164:167], v[100:103]\nds_read_b128 v[40:43], v32 offset:12288\ns_add_u32 s50, s50, s36\n"
"s_waitcnt lgkmcnt(3)\nv_mfma_f32_16x16x32_bf16 v[104:107], v[44:47], v[160:163], v[104:107]\ns_add_u32 m0, s52, 0\nv_mfma_f32_16x16x32_bf16 v[108:111], v[44:47], v[164:167], v[108:111]\n"
"ds_read_b128 v[44:47], v33 offset:12288\ns_add_u32 s53, s49, 0\ns_waitcnt lgkmcnt(3)\nv_mfma_f32_16x16x32_bf16 v[96:99], v[48:51], v[168:171], v[96:99]\n"
"buffer_load_dwordx4 v10, s[12:15], s53 offen lds\nv_mfma_f32_16x16x32_bf16 v[100:103], v[48:51], v[172:175], v[100:103]\nds_read_b128 v[48:51], v34 offset:12288\ns_add_u32 m0, s52, 4096\n"
"s_waitcnt lgkmcnt(3)\nv_mfma_f32_16x16x32_bf16 v[104:107], v[52:55], v[168:171], v[104:107]\ns_add_u32 s53, s49, 128\nv_mfma_f32_16x16x32_bf16 v[108:111], v[52:55], v[172:175], v[108:111]\n"
"ds_read_b128 v[52:55], v35 offset:12288\nbuffer_load_dwordx4 v10, s[12:15], s53 offen lds\ns_waitcnt lgkmcnt(3)\nv_mfma_f32_16x16x32_bf16 v[96:99], v[40:43], v[176:179], v[96:99]\n"
"s_add_u32 m0, s52, 8192\nv_mfma_f32_16x16x32_bf16 v[100:103], v[40:43], v[180:183], v[100:103]\nds_read_b128 v[40:43], v32 offset:16384\ns_add_u32 s53, s49, 256\n"
"s_waitcnt lgkmcnt(3)\nv_mfma_f32_16x16x32_bf16 v[104:107], v[44:47], v[176:179], v[104:107]\nbuffer_load_dwordx4 v10, s[12:15], s53 offen lds\nv_mfma_f32_16x16x32_bf16 v[108:111], v[44:47], v[180:183], v[108:111]\n"
"ds_read_b128 v[44:47], v33 offset:16384\ns_add_u32 m0, s52, 12288\ns_waitcnt lgkmcnt(3)\nv_mfma_f32_16x16x32_bf16 v[96:99], v[48:51], v[184:187], v[96:99]\n"
"s_add_u32 s53, s49, 384\nv_mfma_f32_16x16x32_bf16 v[100:103], v[48:51], v[188:191], v[100:103]\nds_read_b128 v[48:51], v34 offset:16384\nbuffer_load_dwordx4 v10, s[12:15], s53 offen lds\n"
"s_waitcnt lgkmcnt(3)\nv_mfma_f32_16x16x32_bf16 v[104:107], v[52:55], v[184:187], v[104:107]\ns_add_u32 m0, s52, 16384\nv_mfma_f32_16x16x32_bf16 v[108:111], v[52:55], v[188:191], v[108:111]\n"
"ds_read_b128 v[52:55], v35 offset:16384\ns_add_u32 s53, s49, 512\ns_waitcnt lgkmcnt(3)\nv_mfma_f32_16x16x32_bf16 v[96:99], v[40:43], v[192:195], v[96:99]\n"
"buffer_load_dwordx4 v10, s[12:15], s53 offen lds\nv_mfma_f32_16x16x32_bf16 v[100:103], v[40:43], v[196:199], v[100:103]\nds_read_b128 v[40:43], v32 offset:20480\ns_add_u32 m0, s52, 20480\n"
"s_waitcnt lgkmcnt(3)\nv_mfma_f32_16x16x32_bf16 v[104:107], v[44:47], v[192:195], v[104:107]\ns_add_u32 s53, s49, 640\nv_mfma_f32_16x16x32_bf16 v[108:111], v[44:47], v[196:199], v[108:111]\n"
"ds_read_b128 v[44:47], v33 offset:20480\nbuffer_load_dwordx4 v10, s[12:15], s53 offen lds\ns_waitcnt lgkmcnt(3)\nv_mfma_f32_16x16x32_bf16 v[96:99], v[48:51], v[200:203], v[96:99]\n"
"s_add_u32 m0, s52, 24576\nv_mfma_f32_16x16x32_bf16 v[100:103], v[48:51], v[204:207], v[100:103]\nds_read_b128 v[48:51], v34 offset:20480\ns_add_u32 s53, s49, 768\n"
"s_waitcnt lgkmcnt(3)\nv_mfma_f32_16x16x32_bf16 v[104:107], v[52:55], v[200:203], v[104:107]\nbuffer_load_dwordx4 v10, s[12:15], s53 offen lds\nv_mfma_f32_16x16x32_bf16 v[108:111], v[52:55], v[204:207], v[108:111]\n"
"ds_read_b128 v[52:55], v35 offset:20480\ns_waitcnt lgkmcnt(3)\nv_mfma_f32_16x16x32_bf16 v[96:99], v[40:43], v[208:211], v[96:99]\nv_mfma_f32_16x16x32_bf16 v[100:103], v[40:43], v[212:215], v[100:103]\n"
"ds_read_b128 v[40:43], v32 offset:24576\ns_waitcnt lgkmcnt(3)\nv_mfma_f32_16x16x32_bf16 v[104:107], v[44:47], v[208:211], v[104:107]\nv_mfma_f32_16x16x32_bf16 v[108:111], v[44:47], v[212:215], v[108:111]\n"
"ds_read_b128 v[44:47], v33 offset:24576\ns_waitcnt lgkmcnt(3)\nv_mfma_f32_16x16x32_bf16 v[96:99], v[48:51], v[216:219], v[96:99]\ns_add_u32 s54, s51, 32768\n"
"s_cmp_eq_u32 s54, 163840\nv_mfma_f32_16x16x32_bf16 v[100:103], v[48:51], v[220:223], v[100:103]\ns_cselect_b32 s54, 0, s54\ns_sub_u32 s54, s54, s51\n"
"ds_read_b128 v[48:51], v34 offset:24576\ns_waitcnt lgkmcnt(3)\nv_mfma_f32_16x16x32_bf16 v[104:107], v[52:55], v[216:219], v[104:107]\nv_mfma_f32_16x16x32_bf16 v[108:111], v[52:55], v[220:223], v[108:111]\n"
"ds_read_b128 v[52:55], v35 offset:24576\ns_waitcnt lgkmcnt(3)\nv_mfma_f32_16x16x32_bf16 v[96:99], v[40:43], v[224:227], v[96:99]\nv_add_u32 v32, s54, v32\n"
"v_mfma_f32_16x16x32_bf16 v[100:103], v[40:43], v[228:231], v[100:103]\nds_read_b128 v[40:43], v32 offset:0\ns_waitcnt lgkmcnt(3)\nv_mfma_f32_16x16x32_bf16 v[104:107], v[44:47], v[224:227], v[104:107]\n"
"v_add_u32 v33, s54, v33\nv_mfma_f32_16x16x32_bf16 v[108:111], v[44:47], v[228:231], v[108:111]\nds_read_b128 v[44:47], v33 offset:0\ns_waitcnt lgkmcnt(3)\n"
"v_mfma_f32_16x16x32_bf16 v[96:99], v[48:51], v[232:235], v[96:99]\nv_add_u32 v34, s54, v34\nv_mfma_f32_16x16x32_bf16 v[100:103], v[48:51], v[236:239], v[100:103]\nds_read_b128 v[48:51], v34 offset:0\n"
"s_waitcnt lgkmcnt(3)\nv_mfma_f32_16x16x32_bf16 v[104:107], v[52:55], v[232:235], v[104:107]\nv_add_u32 v35, s54, v35\nv_mfma_f32_16x16x32_bf16 v[108:111], v[52:55], v[236:239], v[108:111]\n"
"ds_read_b128 v[52:55], v35 offset:0\ns_mov_b32 s52, s47\ns_add_u32 s47, s51, s32\ns_add_u32 s51, s51, s54\n"
"s_add_u32 s49, s49, 28672\ns_waitcnt vmcnt(7)\ns_barrier\ns_waitcnt lgkmcnt(3)\n"
"v_mfma_f32_16x16x32_bf16 v[64:67], v[40:43], v[128:131], 0\nv_mov_b32 v36, s48\ns_add_u32 s48, s48, 1\ns_nop 0\n"
"buffer_store_dword v36, v19, s[24:27], s33 offen sc1\nv_mfma_f32_16x16x32_bf16 v[68:71], v[40:43], v[132:135], 0\nds_read_b128 v[40:43], v32 offset:4096\ns_waitcnt lgkmcnt(3)\n"
"v_mfma_f32_16x16x32_bf16 v[72:75], v[44:47], v[128:131], 0\nv_mfma_f32_16x16x32_bf16 v[76:79], v[44:47], v[132:135], 0\nds_read_b128 v[44:47], v33 offset:4096\nv_cvt_pk_bf16_f32 v56, v96, v97\n"
"s_waitcnt lgkmcnt(3)\nv_mfma_f32_16x16x32_bf16 v[64:67], v[48:51], v[136:139], v[64:67]\nv_cvt_pk_bf16_f32 v57, v98, v99\nv_mfma_f32_16x16x32_bf16 v[68:71], v[48:51], v[140:143], v[68:71]\n"
"ds_read_b128 v[48:51], v34 offset:4096\nv_cvt_pk_bf16_f32 v58, v104, v105\ns_waitcnt lgkmcnt(3)\nv_mfma_f32_16x16x32_bf16 v[72:75], v[52:55], v[136:139], v[72:75]\n"
"v_cvt_pk_bf16_f32 v59, v106, v107\nv_mfma_f32_16x16x32_bf16 v[76:79], v[52:55], v[140:143], v[76:79]\nds_read_b128 v[52:55], v35 offset:4096\ns_waitcnt lgkmcnt(3)\n"
"v_mfma_f32_16x16x32_bf16 v[64:67], v[40:43], v[144:147], v[64:67]\nv_mfma_f32_16x16x32_bf16 v[68:71], v[40:43], v[148:151], v[68:71]\nds_read_b128 v[40:43], v32 offset:8192\nbuffer_store_dwordx4 v[56:59], v15, s[20:23], s50 offen sc1\n"
"s_waitcnt lgkmcnt(3)\nv_mfma_f32_16x16x32_bf16 v[72:75], v[44:47], v[144:147], v[72:75]\nv_cvt_pk_bf16_f32 v60, v100, v101\nv_mfma_f32_16x16x32_bf16 v[76:79], v[44:47], v[148:151], v[76:79]\n"
"ds_read_b128 v[44:47], v33 offset:8192\nv_cvt_pk_bf16_f32 v61, v102, v103\ns_waitcnt lgkmcnt(3)\nv_mfma_f32_16x16x32_bf16 v[64:67], v[48:51], v[152:155], v[64:67]\n"
"v_cvt_pk_bf16_f32 v62, v108, v109\nv_mfma_f32_16x16x32_bf16 v[68:71], v[48:51], v[156:159], v[68:71]\nds_read_b128 v[48:51], v34 offset:8192\nv_cvt_pk_bf16_f32 v63, v110, v111\n"
"s_waitcnt lgkmcnt(3)\nv_mfma_f32_16x16x32_bf16 v[72:75], v[52:55], v[152:155], v[72:75]\nv_mfma_f32_16x16x32_bf16 v[76:79], v[52:55], v[156:159], v[76:79]\nds_read_b128 v[52:55], v35 offset:8192\n"
"s_waitcnt lgkmcnt(3)\nv_mfma_f32_16x16x32_bf16 v[64:67], v[40:43], v[160:163], v[64:67]\nbuffer_store_dwordx4 v[60:63], v16, s[20:23], s50 offen sc1\nv_mfma_f32_16x16x32_bf16 v[68:71], v[40:43], v[164:167], v[68:71]\n"
"ds_read_b128 v[40:43], v32 offset:12288\ns_add_u32 s50, s50, s36\ns_waitcnt lgkmcnt(3)\nv_mfma_f32_16x16x32_bf16 v[72:75], v[44:47], v[160:163], v[72:75]\n"
"s_add_u32 m0, s52, 0\nv_mfma_f32_16x16x32_bf16 v[76:79], v[44:47], v[164:167], v[76:79]\nds_read_b128 v[44:47], v33 offset:12288\ns_add_u32 s53, s49, 0\n"
"s_waitcnt lgkmcnt(3)\nv_mfma_f32_16x16x32_bf16 v[64:67], v[48:51], v[168:171], v[64:67]\nbuffer_load_dwordx4 v10, s[12:15], s53 offen lds\nv_mfma_f32_16x16x32_bf16 v[68:71], v[48:51], v[172:175], v[68:71]\n"
"ds_read_b128 v[48:51], v34 offset:12288\ns_add_u32 m0, s52, 4096\ns_waitcnt lgkmcnt(3)\nv_mfma_f32_16x16x32_bf16 v[72:75], v[52:55], v[168:171], v[72:75]\n"
"s_add_u32 s53, s49, 128\nv_mfma_f32_16x16x32_bf16 v[76:79], v[52:55], v[172:175], v[76:79]\nds_read_b128 v[52:55], v35 offset:12288\nbuffer_load_dwordx4 v10, s[12:15], s53 offen lds\n"
"s_waitcnt lgkmcnt(3)\nv_mfma_f32_16x16x32_bf16 v[64:67], v[40:43], v[176:179], v[64:67]\ns_add_u32 m0, s52, 8192\nv_mfma_f32_16x16x32_bf16 v[68:71], v[40:43], v[180:183], v[68:71]\n"
"ds_read_b128 v[40:43], v32 offset:16384\ns_add_u32 s53, s49, 256\ns_waitcnt lgkmcnt(3)\nv_mfma_f32_16x16x32_bf16 v[72:75], v[44:47], v[176:179], v[72:75]\n"
"buffer_load_dwordx4 v10, s[12:15], s53 offen lds\nv_mfma_f32_16x16x32_bf16 v[76:79], v[44:47], v[180:183], v[76:79]\nds_read_b128 v[44:47], v33 offset:16384\ns_add_u32 m0, s52, 12288\n"
"s_waitcnt lgkmcnt(3)\nv_mfma_f32_16x16x32_bf16 v[64:67], v[48:51], v[184:187], v[64:67]\ns_add_u32 s53, s49, 384\nv_mfma_f32_16x16x32_bf16 v[68:71], v[48:51], v[188:191], v[68:71]\n"
"ds_read_b128 v[48:51], v34 offset:16384\nbuffer_load_dwordx4 v10, s[12:15], s53 offen lds\ns_waitcnt lgkmcnt(3)\nv_mfma_f32_16x16x32_bf16 v[72:75], v[52:55], v[184:187], v[72:75]\n"
"s_add_u32 m0, s52, 16384\nv_mfma_f32_16x16x32_bf16 v[76:79], v[52:55], v[188:191], v[76:79]\nds_read_b128 v[52:55], v35 offset:16384\ns_add_u32 s53, s49, 512\n"
"s_waitcnt lgkmcnt(3)\nv_mfma_f32_16x16x32_bf16 v[64:67], v[40:43], v[192:195], v[64:67]\nbuffer_load_dwordx4 v10, s[12:15], s53 offen lds\nv_mfma_f32_16x16x32_bf16 v[68:71], v[40:43], v[196:199], v[68:71]\n"
"ds_read_b128 v[40:43], v32 offset:20480\ns_add_u32 m0, s52, 20480\ns_waitcnt lgkmcnt(3)\nv_mfma_f32_16x16x32_bf16 v[72:75], v[44:47], v[192:195], v[72:75]\n"
"s_add_u32 s53, s49, 640\nv_mfma_f32_16x16x32_bf16 v[76:79], v[44:47], v[196:199], v[76:79]\nds_read_b128 v[44:47], v33 offset:20480\nbuffer_load_dwordx4 v10, s[12:15], s53 offen lds\n"
"s_waitcnt lgkmcnt(3)\nv_mfma_f32_16x16x32_bf16 v[64:67], v[48:51], v[200:203], v[64:67]\ns_add_u32 m0, s52, 24576\nv_mfma_f32_16x16x32_bf16 v[68:71], v[48:51], v[204:207], v[68:71]\n"
"ds_read_b128 v[48:51], v34 offset:20480\ns_add_u32 s53, s49, 768\ns_waitcnt lgkmcnt(3)\nv_mfma_f32_16x16x32_bf16 v[72:75], v[52:55], v[200:203], v[72:75]\n"
"buffer_load_dwordx4 v10, s[12:15], s53 offen lds\nv_mfma_f32_16x16x32_bf16 v[76:79], v[52:55], v[204:207], v[76:79]\nds_read_b128 v[52:55], v35 offset:20480\ns_waitcnt lgkmcnt(3)\n"
"v_mfma_f32_16x16x32_bf16 v[64:67], v[40:43], v[208:211], v[64:67]\nv_mfma_f32_16x16x32_bf16 v[68:71], v[40:43], v[212:215], v[68:71]\nds_read_b128 v[40:43], v32 offset:24576\ns_waitcnt lgkmcnt(3)\n"
"v_mfma_f32_16x16x32_bf16 v[72:75], v[44:47], v[208:211], v[72:75]\nv_mfma_f32_16x16x32_bf16 v[76:79], v[44:47], v[212:215], v[76:79]\nds_read_b128 v[44:47], v33 offset:24576\ns_waitcnt lgkmcnt(3)\n"
"v_mfma_f32_16x16x32_bf16 v[64:67], v[48:51], v[216:219], v[64:67]\ns_add_u32 s54, s51, 32768\ns_cmp_eq_u32 s54, 163840\nv_mfma_f32_16x16x32_bf16 v[68:71], v[48:51], v[220:223], v[68:71]\n"
"s_cselect_b32 s54, 0, s54\ns_sub_u32 s54, s54, s51\nds_read_b128 v[48:51], v34 offset:24576\ns_waitcnt lgkmcnt(3)\n"
"v_mfma_f32_16x16x32_bf16 v[72:75], v[52:55], v[216:219], v[72:75]\nv_mfma_f32_16x16x32_bf16 v[76:79], v[52:55], v[220:223], v[76:79]\nds_read_b128 v[52:55], v35 offset:24576\ns_waitcnt lgkmcnt(3)\n"
"v_mfma_f32_16x16x32_bf16 v[64:67], v[40:43], v[224:227], v[64:67]\nv_add_u32 v32, s54, v32\nv_mfma_f32_16x16x32_bf16 v[68:71], v[40:43], v[228:231], v[68:71]\nds_read_b128 v[40:43], v32 offset:0\n"
"s_waitcnt lgkmcnt(3)\nv_mfma_f32_16x16x32_bf16 v[72:75], v[44:47], v[224:227], v[72:75]\nv_add_u32 v33, s54, v33\nv_mfma_f32_16x16x32_bf16 v[76:79], v[44:47], v[228:231], v[76:79]\n"
"ds_read_b128 v[44:47], v33 offset:0\ns_waitcnt lgkmcnt(3)\nv_mfma_f32_16x16x32_bf16 v[64:67], v[48:51], v[232:235], v[64:67]\nv_add_u32 v34, s54, v34\n"
"v_mfma_f32_16x16x32_bf16 v[68:71], v[48:51], v[236:239], v[68:71]\nds_read_b128 v[48:51], v34 offset:0\ns_waitcnt lgkmcnt(3)\nv_mfma_f32_16x16x32_bf16 v[72:75], v[52:55], v[232:235], v[72:75]\n"
"v_add_u32 v35, s54, v35\nv_mfma_f32_16x16x32_bf16 v[76:79], v[52:55], v[236:239], v[76:79]\nds_read_b128 v[52:55], v35 offset:0\ns_mov_b32 s52, s47\n"
"s_add_u32 s47, s51, s32\ns_add_u32 s51, s51, s54\ns_add_u32 s49, s49, 28672\ns_waitcnt vmcnt(7)\n"
"s_barrier\ns_waitcnt lgkmcnt(3)\nv_mfma_f32_16x16x32_bf16 v[96:99], v[40:43], v[128:131], 0\nv_mov_b32 v36, s48\n"
"s_add_u32 s48, s48, 1\ns_nop 0\nbuffer_store_dword v36, v19, s[24:27], s33 offen sc1\nbuffer_load_dword v37, v20, s[24:27], 0 offen sc1\n"
"v_mfma_f32_16x16x32_bf16 v[100:103], v[40:43], v[132:135], 0\nds_read_b128 v[40:43], v32 offset:4096\ns_waitcnt lgkmcnt(3)\nv_mfma_f32_16x16x32_bf16 v[104:107], v[44:47], v[128:131], 0\n"
"v_mfma_f32_16x16x32_bf16 v[108:111], v[44:47], v[132:135], 0\nds_read_b128 v[44:47], v33 offset:4096\nv_cvt_pk_bf16_f32 v56, v64, v65\ns_waitcnt lgkmcnt(3)\n"
"v_mfma_f32_16x16x32_bf16 v[96:99], v[48:51], v[136:139], v[96:99]\nv_cvt_pk_bf16_f32 v57, v66, v67\nv_mfma_f32_16x16x32_bf16 v[100:103], v[48:51], v[140:143], v[100:103]\nds_read_b128 v[48:51], v34 offset:4096\n"
"v_cvt_pk_bf16_f32 v58, v72, v73\ns_waitcnt lgkmcnt(3)\nv_mfma_f32_16x16x32_bf16 v[104:107], v[52:55], v[136:139], v[104:107]\nv_cvt_pk_bf16_f32 v59, v74, v75\n"
"v_mfma_f32_16x16x32_bf16 v[108:111], v[52:55], v[140:143], v[108:111]\nds_read_b128 v[52:55], v35 offset:4096\ns_waitcnt lgkmcnt(3)\nv_mfma_f32_16x16x32_bf16 v[96:99], v[40:43], v[144:147], v[96:99]\n"
"v_mfma_f32_16x16x32_bf16 v[100:103], v[40:43], v[148:151], v[100:103]\nds_read_b128 v[40:43], v32 offset:8192\nbuffer_store_dwordx4 v[56:59], v15, s[20:23], s50 offen sc1\ns_waitcnt lgkmcnt(3)\n"
"v_mfma_f32_16x16x32_bf16 v[104:107], v[44:47], v[144:147], v[104:107]\nv_cvt_pk_bf16_f32 v60, v68, v69\nv_mfma_f32_16x16x32_bf16 v[108:111], v[44:47], v[148:151], v[108:111]\nds_read_b128 v[44:47], v33 offset:8192\n"
"v_cvt_pk_bf16_f32 v61, v70, v71\ns_waitcnt lgkmcnt(3)\nv_mfma_f32_16x16x32_bf16 v[96:99], v[48:51], v[152:155], v[96:99]\nv_cvt_pk_bf16_f32 v62, v76, v77\n"
"v_mfma_f32_16x16x32_bf16 v[100:103], v[48:51], v[156:159], v[100:103]\nds_read_b128 v[48:51], v34 offset:8192\nv_cvt_pk_bf16_f32 v63, v78, v79\ns_waitcnt lgkmcnt(3)\n"
"v_mfma_f32_16x16x32_bf16 v[104:107], v[52:55], v[152:155], v[104:107]\nv_mfma_f32_16x16x32_bf16 v[108:111], v[52:55], v[156:159], v[108:111]\nds_read_b128 v[52:55], v35 offset:8192\ns_waitcnt lgkmcnt(3)\n"
"v_mfma_f32_16x16x32_bf16 v[96:99], v[40:43], v[160:163], v[96:99]\nbuffer_store_dwordx4 v[60:63], v16, s[20:23], s50 offen sc1\nv_mfma_f32_16x16x32_bf16 v[100:103], v[40:43], v[164:167], v[100:103]\nds_read_b128 v[40:43], v32 offset:12288\n"
"s_add_u32 s50, s50, s36\ns_waitcnt lgkmcnt(3)\nv_mfma_f32_16x16x32_bf16 v[104:107], v[44:47], v[160:163], v[104:107]\ns_add_u32 m0, s52, 0\n"
"v_mfma_f32_16x16x32_bf16 v[108:111], v[44:47], v[164:167], v[108:111]\nds_read_b128 v[44:47], v33 offset:12288\ns_add_u32 s53, s49, 0\ns_waitcnt lgkmcnt(3)\n"
"v_mfma_f32_16x16x32_bf16 v[96:99], v[48:51], v[168:171], v[96:99]\nbuffer_load_dwordx4 v10, s[12:15], s53 offen lds\nv_mfma_f32_16x16x32_bf16 v[100:103], v[48:51], v[172:175], v[100:103]\nds_read_b128 v[48:51], v34 offset:12288\n"
"s_add_u32 m0, s52, 4096\ns_waitcnt lgkmcnt(3)\nv_mfma_f32_16x16x32_bf16 v[104:107], v[52:55], v[168:171], v[104:107]\ns_add_u32 s53, s49, 128\n"
"v_mfma_f32_16x16x32_bf16 v[108:111], v[52:55], v[172:175], v[108:111]\nds_read_b128 v[52:55], v35 offset:12288\nbuffer_load_dwordx4 v10, s[12:15], s53 offen lds\ns_waitcnt lgkmcnt(3)\n"
"v_mfma_f32_16x16x32_bf16 v[96:99], v[40:43], v[176:179], v[96:99]\ns_add_u32 m0, s52, 8192\nv_mfma_f32_16x16x32_bf16 v[100:103], v[40:43], v[180:183], v[100:103]\nds_read_b128 v[40:43], v32 offset:16384\n"
"s_add_u32 s53, s49, 256\ns_waitcnt lgkmcnt(3)\nv_mfma_f32_16x16x32_bf16 v[104:107], v[44:47], v[176:179], v[104:107]\nbuffer_load_dwordx4 v10, s[12:15], s53 offen lds\n"
"v_mfma_f32_16x16x32_bf16 v[108:111], v[44:47], v[180:183], v[108:111]\nds_read_b128 v[44:47], v33 offset:16384\ns_add_u32 m0, s52, 12288\ns_waitcnt lgkmcnt(3)\n"
"v_mfma_f32_16x16x32_bf16 v[96:99], v[48:51], v[184:187], v[96:99]\ns_add_u32 s53, s49, 384\nv_mfma_f32_16x16x32_bf16 v[100:103], v[48:51], v[188:191], v[100:103]\nds_read_b128 v[48:51], v34 offset:16384\n"
"buffer_load_dwordx4 v10, s[12:15], s53 offen lds\ns_waitcnt lgkmcnt(3)\nv_mfma_f32_16x16x32_bf16 v[104:107], v[52:55], v[184:187], v[104:107]\ns_add_u32 m0, s52, 16384\n"
"v_mfma_f32_16x16x32_bf16 v[108:111], v[52:55], v[188:191], v[108:111]\nds_read_b128 v[52:55], v35 offset:16384\ns_add_u32 s53, s49, 512\ns_waitcnt lgkmcnt(3)\n"
"v_mfma_f32_16x16x32_bf16 v[96:99], v[40:43], v[192:195], v[96:99]\nbuffer_load_dwordx4 v10, s[12:15], s53 offen lds\nv_mfma_f32_16x16x32_bf16 v[100:103], v[40:43], v[196:199], v[100:103]\nds_read_b128 v[40:43], v32 offset:20480\n"
"s_add_u32 m0, s52, 20480\ns_waitcnt lgkmcnt(3)\nv_mfma_f32_16x16x32_bf16 v[104:107], v[44:47], v[192:195], v[104:107]\ns_add_u32 s53, s49, 640\n"
"v_mfma_f32_16x16x32_bf16 v[108:111], v[44:47], v[196:199], v[108:111]\nds_read_b128 v[44:47], v33 offset:20480\nbuffer_load_dwordx4 v10, s[12:15], s53 offen lds\ns_waitcnt lgkmcnt(3)\n"
"v_mfma_f32_16x16x32_bf16 v[96:99], v[48:51], v[200:203], v[96:99]\ns_add_u32 m0, s52, 24576\nv_mfma_f32_16x16x32_bf16 v[100:103], v[48:51], v[204:207], v[100:103]\nds_read_b128 v[48:51], v34 offset:20480\n"
"s_add_u32 s53, s49, 768\ns_waitcnt lgkmcnt(3)\nv_mfma_f32_16x16x32_bf16 v[104:107], v[52:55], v[200:203], v[104:107]\nbuffer_load_dwordx4 v10, s[12:15], s53 offen lds\n"
"v_mfma_f32_16x16x32_bf16 v[108:111], v[52:55], v[204:207], v[108:111]\nds_read_b128 v[52:55], v35 offset:20480\ns_waitcnt lgkmcnt(3)\nv_mfma_f32_16x16x32_bf16 v[96:99], v[40:43], v[208:211], v[96:99]\n"
"v_mfma_f32_16x16x32_bf16 v[100:103], v[40:43], v[212:215], v[100:103]\nds_read_b128 v[40:43], v32 offset:24576\ns_waitcnt lgkmcnt(3)\nv_mfma_f32_16x16x32_bf16 v[104:107], v[44:47], v[208:211], v[104:107]\n"
"v_mfma_f32_16x16x32_bf16 v[108:111], v[44:47], v[212:215], v[108:111]\nds_read_b128 v[44:47], v33 offset:24576\ns_waitcnt lgkmcnt(3)\nv_mfma_f32_16x16x32_bf16 v[96:99], v[48:51], v[216:219], v[96:99]\n"
"s_add_u32 s54, s51, 32768\ns_cmp_eq_u32 s54, 163840\nv_mfma_f32_16x16x32_bf16 v[100:103], v[48:51], v[220:223], v[100:103]\ns_cselect_b32 s54, 0, s54\n"
"s_sub_u32 s54, s54, s51\nds_read_b128 v[48:51], v34 offset:24576\ns_waitcnt lgkmcnt(3)\nv_mfma_f32_16x16x32_bf16 v[104:107], v[52:55], v[216:219], v[104:107]\n"
"v_mfma_f32_16x16x32_bf16 v[108:111], v[52:55], v[220:223], v[108:111]\nds_read_b128 v[52:55], v35 offset:24576\ns_waitcnt lgkmcnt(3)\nv_mfma_f32_16x16x32_bf16 v[96:99], v[40:43], v[224:227], v[96:99]\n"
"v_add_u32 v32, s54, v32\nv_mfma_f32_16x16x32_bf16 v[100:103], v[40:43], v[228:231], v[100:103]\nds_read_b128 v[40:43], v32 offset:0\ns_waitcnt lgkmcnt(3)\n"
"v_mfma_f32_16x16x32_bf16 v[104:107], v[44:47], v[224:227], v[104:107]\nv_add_u32 v33, s54, v33\nv_mfma_f32_16x16x32_bf16 v[108:111], v[44:47], v[228:231], v[108:111]\nds_read_b128 v[44:47], v33 offset:0\n"
"s_waitcnt lgkmcnt(3)\nv_mfma_f32_16x16x32_bf16 v[96:99], v[48:51], v[232:235], v[96:99]\nv_add_u32 v34, s54, v34\nv_mfma_f32_16x16x32_bf16 v[100:103], v[48:51], v[236:239], v[100:103]\n"
"ds_read_b128 v[48:51], v34 offset:0\ns_waitcnt lgkmcnt(3)\nv_mfma_f32_16x16x32_bf16 v[104:107], v[52:55], v[232:235], v[104:107]\nv_add_u32 v35, s54, v35\n"
"v_mfma_f32_16x16x32_bf16 v[108:111], v[52:55], v[236:239], v[108:111]\nds_read_b128 v[52:55], v35 offset:0\ns_mov_b32 s52, s47\ns_add_u32 s47, s51, s32\n"
"s_add_u32 s51, s51, s54\ns_add_u32 s49, s49, 28672\ns_waitcnt vmcnt(7)\nfg1_pf:\n"
"v_cmp_ne_u32 vcc, s28, v37\ns_cbranch_vccz fg1_pfok\ns_add_u32 s55, s55, 1\ns_cmp_gt_u32 s55, 262144\n"
"s_cbranch_scc0 fg1_pf2\ns_endpgm\nfg1_pf2:\ns_sleep 1\n"
"buffer_load_dword v37, v20, s[24:27], 0 offen sc1\ns_waitcnt vmcnt(0)\ns_branch fg1_pf\nfg1_pfok:\n"
"s_barrier\ns_waitcnt lgkmcnt(3)\nv_mfma_f32_16x16x32_bf16 v[64:67], v[40:43], v[128:131], 0\nv_mov_b32 v36, s48\n"
"s_add_u32 s48, s48, 1\ns_nop 0\nbuffer_store_dword v36, v19, s[24:27], s33 offen sc1\nv_mfma_f32_16x16x32_bf16 v[68:71], v[40:43], v[132:135], 0\n"
"ds_read_b128 v[40:43], v32 offset:4096\ns_waitcnt lgkmcnt(3)\nv_mfma_f32_16x16x32_bf16 v[72:75], v[44:47], v[128:131], 0\nv_mfma_f32_16x16x32_bf16 v[76:79], v[44:47], v[132:135], 0\n"
"ds_read_b128 v[44:47], v33 offset:4096\nv_cvt_pk_bf16_f32 v56, v96, v97\ns_waitcnt lgkmcnt(3)\nv_mfma_f32_16x16x32_bf16 v[64:67], v[48:51], v[136:139], v[64:67]\n"
"v_cvt_pk_bf16_f32 v57, v98, v99\nv_mfma_f32_16x16x32_bf16 v[68:71], v[48:51], v[140:143], v[68:71]\nds_read_b128 v[48:51], v34 offset:4096\nv_cvt_pk_bf16_f32 v58, v104, v105\n"
"s_waitcnt lgkmcnt(3)\nv_mfma_f32_16x16x32_bf16 v[72:75], v[52:55], v[136:139], v[72:75]\nv_cvt_pk_bf16_f32 v59, v106, v107\nv_mfma_f32_16x16x32_bf16 v[76:79], v[52:55], v[140:143], v[76:79]\n"
"ds_read_b128 v[52:55], v35 offset:4096\ns_waitcnt lgkmcnt(3)\nv_mfma_f32_16x16x32_bf16 v[64:67], v[40:43], v[144:147], v[64:67]\nv_mfma_f32_16x16x32_bf16 v[68:71], v[40:43], v[148:151], v[68:71]\n"
"ds_read_b128 v[40:43], v32 offset:8192\nbuffer_store_dwordx4 v[56:59], v15, s[20:23], s50 offen sc1\ns_waitcnt lgkmcnt(3)\nv_mfma_f32_16x16x32_bf16 v[72:75], v[44:47], v[144:147], v[72:75]\n"
"v_cvt_pk_bf16_f32 v60, v100, v101\nv_mfma_f32_16x16x32_bf16 v[76:79], v[44:47], v[148:151], v[76:79]\nds_read_b128 v[44:47], v33 offset:8192\nv_cvt_pk_bf16_f32 v61, v102, v103\n"
"s_waitcnt lgkmcnt(3)\nv_mfma_f32_16x16x32_bf16 v[64:67], v[48:51], v[152:155], v[64:67]\nv_cvt_pk_bf16_f32 v62, v108, v109\nv_mfma_f32_16x16x32_bf16 v[68:71], v[48:51], v[156:159], v[68:71]\n"
"ds_read_b128 v[48:51], v34 offset:8192\nv_cvt_pk_bf16_f32 v63, v110, v111\ns_waitcnt lgkmcnt(3)\nv_mfma_f32_16x16x32_bf16 v[72:75], v[52:55], v[152:155], v[72:75]\n"
"v_mfma_f32_16x16x32_bf16 v[76:79], v[52:55], v[156:159], v[76:79]\nds_read_b128 v[52:55], v35 offset:8192\ns_waitcnt lgkmcnt(3)\nv_mfma_f32_16x16x32_bf16 v[64:67], v[40:43], v[160:163], v[64:67]\n"
"buffer_store_dwordx4 v[60:63], v16, s[20:23], s50 offen sc1\nv_mfma_f32_16x16x32_bf16 v[68:71], v[40:43], v[164:167], v[68:71]\nds_read_b128 v[40:43], v32 offset:12288\ns_add_u32 s50, s50, s36\n"
"s_waitcnt lgkmcnt(3)\nv_mfma_f32_16x16x32_bf16 v[72:75], v[44:47], v[160:163], v[72:75]\ns_add_u32 m0, s52, 0\nv_mfma_f32_16x16x32_bf16 v[76:79], v[44:47], v[164:167], v[76:79]\n"
"ds_read_b128 v[44:47], v33 offset:12288\ns_add_u32 s53, s49, 0\ns_waitcnt lgkmcnt(3)\nv_mfma_f32_16x16x32_bf16 v[64:67], v[48:51], v[168:171], v[64:67]\n"
"buffer_load_dwordx4 v10, s[12:15], s53 offen lds\nv_mfma_f32_16x16x32_bf16 v[68:71], v[48:51], v[172:175], v[68:71]\nds_read_b128 v[48:51], v34 offset:12288\ns_add_u32 m0, s52, 4096\n"
"s_waitcnt lgkmcnt(3)\nv_mfma_f32_16x16x32_bf16 v[72:75], v[52:55], v[168:171], v[72:75]\ns_add_u32 s53, s49, 128\nv_mfma_f32_16x16x32_bf16 v[76:79], v[52:55], v[172:175], v[76:79]\n"
"ds_read_b128 v[52:55], v35 offset:12288\nbuffer_load_dwordx4 v10, s[12:15], s53 offen lds\ns_waitcnt lgkmcnt(3)\nv_mfma_f32_16x16x32_bf16 v[64:67], v[40:43], v[176:179], v[64:67]\n"
"s_add_u32 m0, s52, 8192\nv_mfma_f32_16x16x32_bf16 v[68:71], v[40:43], v[180:183], v[68:71]\nds_read_b128 v[40:43], v32 offset:16384\ns_add_u32 s53, s49, 256\n"
"s_waitcnt lgkmcnt(3)\nv_mfma_f32_16x16x32_bf16 v[72:75], v[44:47], v[176:179], v[72:75]\nbuffer_load_dwordx4 v10, s[12:15], s53 offen lds\nv_mfma_f32_16x16x32_bf16 v[76:79], v[44:47], v[180:183], v[76:79]\n"
"ds_read_b128 v[44:47], v33 offset:16384\ns_add_u32 m0, s52, 12288\ns_waitcnt lgkmcnt(3)\nv_mfma_f32_16x16x32_bf16 v[64:67], v[48:51], v[184:187], v[64:67]\n"
"s_add_u32 s53, s49, 384\nv_mfma_f32_16x16x32_bf16 v[68:71], v[48:51], v[188:191], v[68:71]\nds_read_b128 v[48:51], v34 offset:16384\nbuffer_load_dwordx4 v10, s[12:15], s53 offen lds\n"
"s_waitcnt lgkmcnt(3)\nv_mfma_f32_16x16x32_bf16 v[72:75], v[52:55], v[184:187], v[72:75]\ns_add_u32 m0, s52, 16384\nv_mfma_f32_16x16x32_bf16 v[76:79], v[52:55], v[188:191], v[76:79]\n"
"ds_read_b128 v[52:55], v35 offset:16384\ns_add_u32 s53, s49, 512\ns_waitcnt lgkmcnt(3)\nv_mfma_f32_16x16x32_bf16 v[64:67], v[40:43], v[192:195], v[64:67]\n"
"buffer_load_dwordx4 v10, s[12:15], s53 offen lds\nv_mfma_f32_16x16x32_bf16 v[68:71], v[40:43], v[196:199], v[68:71]\nds_read_b128 v[40:43], v32 offset:20480\ns_add_u32 m0, s52, 20480\n"
"s_waitcnt lgkmcnt(3)\nv_mfma_f32_16x16x32_bf16 v[72:75], v[44:47], v[192:195], v[72:75]\ns_add_u32 s53, s49, 640\nv_mfma_f32_16x16x32_bf16 v[76:79], v[44:47], v[196:199], v[76:79]\n"
"ds_read_b128 v[44:47], v33 offset:20480\nbuffer_load_dwordx4 v10, s[12:15], s53 offen lds\ns_waitcnt lgkmcnt(3)\nv_mfma_f32_16x16x32_bf16 v[64:67], v[48:51], v[200:203], v[64:67]\n"
"s_add_u32 m0, s52, 24576\nv_mfma_f32_16x16x32_bf16 v[68:71], v[48:51], v[204:207], v[68:71]\nds_read_b128 v[48:51], v34 offset:20480\ns_add_u32 s53, s49, 768\n"
"s_waitcnt lgkmcnt(3)\nv_mfma_f32_16x16x32_bf16 v[72:75], v[52:55], v[200:203], v[72:75]\nbuffer_load_dwordx4 v10, s[12:15], s53 offen lds\nv_mfma_f32_16x16x32_bf16 v[76:79], v[52:55], v[204:207], v[76:79]\n"
"ds_read_b128 v[52:55], v35 offset:20480\ns_mov_b32 s53, 0\nbuffer_load_dwordx4 a[0:3], v14, s[16:19], s53 offen sc1\ns_waitcnt lgkmcnt(3)\n"
"v_mfma_f32_16x16x32_bf16 v[64:67], v[40:43], v[208:211], v[64:67]\ns_mov_b32 s53, 262144\nbuffer_load_dwordx4 a[4:7], v14, s[16:19], s53 offen sc1\nv_mfma_f32_16x16x32_bf16 v[68:71], v[40:43], v[212:215], v[68:71]\n"
"ds_read_b128 v[40:43], v32 offset:24576\ns_mov_b32 s53, 1024\nbuffer_load_dwordx4 a[8:11], v14, s[16:19], s53 offen sc1\ns_waitcnt lgkmcnt(3)\n"
"v_mfma_f32_16x16x32_bf16 v[72:75], v[44:47], v[208:211], v[72:75]\ns_mov_b32 s53, 263168\nbuffer_load_dwordx4 a[12:15], v14, s[16:19], s53 offen sc1\nv_mfma_f32_16x16x32_bf16 v[76:79], v[44:47], v[212:215], v[76:79]\n"
"ds_read_b128 v[44:47], v33 offset:24576\ns_mov_b32 s53, 2048\nbuffer_load_dwordx4 a[16:19], v14, s[16:19], s53 offen sc1\ns_waitcnt lgkmcnt(3)\n"
"v_mfma_f32_16x16x32_bf16 v[64:67], v[48:51], v[216:219], v[64:67]\ns_add_u32 s54, s51, 32768\ns_cmp_eq_u32 s54, 163840\nv_mfma_f32_16x16x32_bf16 v[68:71], v[48:51], v[220:223], v[68:71]\n"
"s_cselect_b32 s54, 0, s54\ns_sub_u32 s54, s54, s51\nds_read_b128 v[48:51], v34 offset:24576\ns_waitcnt lgkmcnt(3)\n"
"v_mfma_f32_16x16x32_bf16 v[72:75], v[52:55], v[216:219], v[72:75]\nv_mfma_f32_16x16x32_bf16 v[76:79], v[52:55], v[220:223], v[76:79]\nds_read_b128 v[52:55], v35 offset:24576\ns_waitcnt lgkmcnt(3)\n"
"v_mfma_f32_16x16x32_bf16 v[64:67], v[40:43], v[224:227], v[64:67]\nv_add_u32 v32, s54, v32\nv_mfma_f32_16x16x32_bf16 v[68:71], v[40:43], v[228:231], v[68:71]\nds_read_b128 v[40:43], v32 offset:0\n"
"s_waitcnt lgkmcnt(3)\nv_mfma_f32_16x16x32_bf16 v[72:75], v[44:47], v[224:227], v[72:75]\nv_add_u32 v33, s54, v33\nv_mfma_f32_16x16x32_bf16 v[76:79], v[44:47], v[228:231], v[76:79]\n"
"ds_read_b128 v[44:47], v33 offset:0\ns_waitcnt lgkmcnt(3)\nv_mfma_f32_16x16x32_bf16 v[64:67], v[48:51], v[232:235], v[64:67]\nv_add_u32 v34, s54, v34\n"
"v_mfma_f32_16x16x32_bf16 v[68:71], v[48:51], v[236:239], v[68:71]\nds_read_b128 v[48:51], v34 offset:0\ns_waitcnt lgkmcnt(3)\nv_mfma_f32_16x16x32_bf16 v[72:75], v[52:55], v[232:235], v[72:75]\n"
"v_add_u32 v35, s54, v35\nv_mfma_f32_16x16x32_bf16 v[76:79], v[52:55], v[236:239], v[76:79]\nds_read_b128 v[52:55], v35 offset:0\ns_mov_b32 s52, s47\n"
"s_add_u32 s47, s51, s32\ns_add_u32 s51, s51, s54\ns_add_u32 s49, s49, 28672\ns_waitcnt vmcnt(12)\n"
"s_barrier\ns_waitcnt lgkmcnt(3)\nv_mfma_f32_16x16x32_bf16 v[96:99], v[40:43], v[128:131], 0\nv_mov_b32 v36, s48\n"
"s_add_u32 s48, s48, 1\ns_nop 0\nbuffer_store_dword v36, v19, s[24:27], s33 offen sc1\nv_mfma_f32_16x16x32_bf16 v[100:103], v[40:43], v[132:135], 0\n"
"ds_read_b128 v[40:43], v32 offset:4096\ns_waitcnt lgkmcnt(3)\nv_mfma_f32_16x16x32_bf16 v[104:107], v[44:47], v[128:131], 0\nv_mfma_f32_16x16x32_bf16 v[108:111], v[44:47], v[132:135], 0\n"
"ds_read_b128 v[44:47], v33 offset:4096\nv_cvt_pk_bf16_f32 v56, v64, v65\ns_waitcnt lgkmcnt(3)\nv_mfma_f32_16x16x32_bf16 v[96:99], v[48:51], v[136:139], v[96:99]\n"
"v_cvt_pk_bf16_f32 v57, v66, v67\nv_mfma_f32_16x16x32_bf16 v[100:103], v[48:51], v[140:143], v[100:103]\nds_read_b128 v[48:51], v34 offset:4096\nv_cvt_pk_bf16_f32 v58, v72, v73\n"
"s_waitcnt lgkmcnt(3)\nv_mfma_f32_16x16x32_bf16 v[104:107], v[52:55], v[136:139], v[104:107]\nv_cvt_pk_bf16_f32 v59, v74, v75\nv_mfma_f32_16x16x32_bf16 v[108:111], v[52:55], v[140:143], v[108:111]\n"
"ds_read_b128 v[52:55], v35 offset:4096\ns_waitcnt lgkmcnt(3)\nv_mfma_f32_16x16x32_bf16 v[96:99], v[40:43], v[144:147], v[96:99]\nv_mfma_f32_16x16x32_bf16 v[100:103], v[40:43], v[148:151], v[100:103]\n"
"ds_read_b128 v[40:43], v32 offset:8192\nbuffer_store_dwordx4 v[56:59], v15, s[20:23], s50 offen sc1\ns_waitcnt lgkmcnt(3)\nv_mfma_f32_16x16x32_bf16 v[104:107], v[44:47], v[144:147], v[104:107]\n"
"v_cvt_pk_bf16_f32 v60, v68, v69\nv_mfma_f32_16x16x32_bf16 v[108:111], v[44:47], v[148:151], v[108:111]\nds_read_b128 v[44:47], v33 offset:8192\nv_cvt_pk_bf16_f32 v61, v70, v71\n"
"s_waitcnt lgkmcnt(3)\nv_mfma_f32_16x16x32_bf16 v[96:99], v[48:51], v[152:155], v[96:99]\nv_cvt_pk_bf16_f32 v62, v76, v77\nv_mfma_f32_16x16x32_bf16 v[100:103], v[48:51], v[156:159], v[100:103]\n"
"ds_read_b128 v[48:51], v34 offset:8192\nv_cvt_pk_bf16_f32 v63, v78, v79\ns_waitcnt lgkmcnt(3)\nv_mfma_f32_16x16x32_bf16 v[104:107], v[52:55], v[152:155], v[104:107]\n"
"v_mfma_f32_16x16x32_bf16 v[108:111], v[52:55], v[156:159], v[108:111]\nds_read_b128 v[52:55], v35 offset:8192\ns_waitcnt lgkmcnt(3)\nv_mfma_f32_16x16x32_bf16 v[96:99], v[40:43], v[160:163], v[96:99]\n"
"buffer_store_dwordx4 v[60:63], v16, s[20:23], s50 offen sc1\nv_mfma_f32_16x16x32_bf16 v[100:103], v[40:43], v[164:167], v[100:103]\nds_read_b128 v[40:43], v32 offset:12288\ns_add_u32 s50, s50, s36\n"
"s_waitcnt lgkmcnt(3)\nv_mfma_f32_16x16x32_bf16 v[104:107], v[44:47], v[160:163], v[104:107]\ns_add_u32 m0, s52, 0\nv_mfma_f32_16x16x32_bf16 v[108:111], v[44:47], v[164:167], v[108:111]\n"
"ds_read_b128 v[44:47], v33 offset:12288\ns_add_u32 s53, s49, 0\ns_waitcnt lgkmcnt(3)\nv_mfma_f32_16x16x32_bf16 v[96:99], v[48:51], v[168:171], v[96:99]\n"
"buffer_load_dwordx4 v10, s[12:15], s53 offen lds\nv_mfma_f32_16x16x32_bf16 v[100:103], v[48:51], v[172:175], v[100:103]\nds_read_b128 v[48:51], v34 offset:12288\ns_add_u32 m0, s52, 4096\n"
"s_waitcnt lgkmcnt(3)\nv_mfma_f32_16x16x32_bf16 v[104:107], v[52:55], v[168:171], v[104:107]\ns_add_u32 s53, s49, 128\nv_mfma_f32_16x16x32_bf16 v[108:111], v[52:55], v[172:175], v[108:111]\n"
"ds_read_b128 v[52:55], v35 offset:12288\nbuffer_load_dwordx4 v10, s[12:15], s53 offen lds\ns_waitcnt lgkmcnt(3)\nv_mfma_f32_16x16x32_bf16 v[96:99], v[40:43], v[176:179], v[96:99]\n"
"s_add_u32 m0, s52, 8192\nv_mfma_f32_16x16x32_bf16 v[100:103], v[40:43], v[180:183], v[100:103]\nds_read_b128 v[40:43], v32 offset:16384\ns_add_u32 s53, s49, 256\n"
"s_waitcnt lgkmcnt(3)\nv_mfma_f32_16x16x32_bf16 v[104:107], v[44:47], v[176:179], v[104:107]\nbuffer_load_dwordx4 v10, s[12:15], s53 offen lds\nv_mfma_f32_16x16x32_bf16 v[108:111], v[44:47], v[180:183], v[108:111]\n"
"ds_read_b128 v[44:47], v33 offset:16384\ns_add_u32 m0, s52, 12288\ns_waitcnt lgkmcnt(3)\nv_mfma_f32_16x16x32_bf16 v[96:99], v[48:51], v[184:187], v[96:99]\n"
"s_add_u32 s53, s49, 384\nv_mfma_f32_16x16x32_bf16 v[100:103], v[48:51], v[188:191], v[100:103]\nds_read_b128 v[48:51], v34 offset:16384\nbuffer_load_dwordx4 v10, s[12:15], s53 offen lds\n"
"s_waitcnt lgkmcnt(3)\nv_mfma_f32_16x16x32_bf16 v[104:107], v[52:55], v[184:187], v[104:107]\ns_add_u32 m0, s52, 16384\nv_mfma_f32_16x16x32_bf16 v[108:111], v[52:55], v[188:191], v[108:111]\n"
"ds_read_b128 v[52:55], v35 offset:16384\ns_add_u32 s53, s49, 512\ns_waitcnt lgkmcnt(3)\nv_mfma_f32_16x16x32_bf16 v[96:99], v[40:43], v[192:195], v[96:99]\n"
"buffer_load_dwordx4 v10, s[12:15], s53 offen lds\nv_mfma_f32_16x16x32_bf16 v[100:103], v[40:43], v[196:199], v[100:103]\nds_read_b128 v[40:43], v32 offset:20480\ns_add_u32 m0, s52, 20480\n"
"s_waitcnt lgkmcnt(3)\nv_mfma_f32_16x16x32_bf16 v[104:107], v[44:47], v[192:195], v[104:107]\ns_add_u32 s53, s49, 640\nv_mfma_f32_16x16x32_bf16 v[108:111], v[44:47], v[196:199], v[108:111]\n"
"ds_read_b128 v[44:47], v33 offset:20480\nbuffer_load_dwordx4 v10, s[12:15], s53 offen lds\ns_waitcnt lgkmcnt(3)\nv_mfma_f32_16x16x32_bf16 v[96:99], v[48:51], v[200:203], v[96:99]\n"
"s_add_u32 m0, s52, 24576\nv_mfma_f32_16x16x32_bf16 v[100:103], v[48:51], v[204:207], v[100:103]\nds_read_b128 v[48:51], v34 offset:20480\ns_add_u32 s53, s49, 768\n"
"s_waitcnt lgkmcnt(3)\nv_mfma_f32_16x16x32_bf16 v[104:107], v[52:55], v[200:203], v[104:107]\nbuffer_load_dwordx4 v10, s[12:15], s53 offen lds\nv_mfma_f32_16x16x32_bf16 v[108:111], v[52:55], v[204:207], v[108:111]\n"
"ds_read_b128 v[52:55], v35 offset:20480\ns_mov_b32 s53, 264192\nbuffer_load_dwordx4 a[20:23], v14, s[16:19], s53 offen sc1\ns_waitcnt lgkmcnt(3)\n"
"v_mfma_f32_16x16x32_bf16 v[96:99], v[40:43], v[208:211], v[96:99]\ns_mov_b32 s53, 3072\nbuffer_load_dwordx4 a[24:27], v14, s[16:19], s53 offen sc1\nv_mfma_f32_16x16x32_bf16 v[100:103], v[40:43], v[212:215], v[100:103]\n"
"ds_read_b128 v[40:43], v32 offset:24576\ns_mov_b32 s53, 265216\nbuffer_load_dwordx4 a[28:31], v14, s[16:19], s53 offen sc1\ns_waitcnt lgkmcnt(3)\n"
"v_mfma_f32_16x16x32_bf16 v[104:107], v[44:47], v[208:211], v[104:107]\ns_mov_b32 s53, 4096\nbuffer_load_dwordx4 a[32:35], v14, s[16:19], s53 offen sc1\nv_mfma_f32_16x16x32_bf16 v[108:111], v[44:47], v[212:215], v[108:111]\n"
"ds_read_b128 v[44:47], v33 offset:24576\ns_mov_b32 s53, 266240\nbuffer_load_dwordx4 a[36:39], v14, s[16:19], s53 offen sc1\ns_waitcnt lgkmcnt(3)\n"
"v_mfma_f32_16x16x32_bf16 v[96:99], v[48:51], v[216:219], v[96:99]\ns_add_u32 s54, s51, 32768\ns_cmp_eq_u32 s54, 163840\nv_mfma_f32_16x16x32_bf16 v[100:103], v[48:51], v[220:223], v[100:103]\n"
"s_cselect_b32 s54, 0, s54\ns_sub_u32 s54, s54, s51\nds_read_b128 v[48:51], v34 offset:24576\ns_waitcnt lgkmcnt(3)\n"
"v_mfma_f32_16x16x32_bf16 v[104:107], v[52:55], v[216:219], v[104:107]\nv_mfma_f32_16x16x32_bf16 v[108:111], v[52:55], v[220:223], v[108:111]\nds_read_b128 v[52:55], v35 offset:24576\ns_waitcnt lgkmcnt(3)\n"
"v_mfma_f32_16x16x32_bf16 v[96:99], v[40:43], v[224:227], v[96:99]\nv_add_u32 v32, s54, v32\nv_mfma_f32_16x16x32_bf16 v[100:103], v[40:43], v[228:231], v[100:103]\nds_read_b128 v[40:43], v32 offset:0\n"
"s_waitcnt lgkmcnt(3)\nv_mfma_f32_16x16x32_bf16 v[104:107], v[44:47], v[224:227], v[104:107]\nv_add_u32 v33, s54, v33\nv_mfma_f32_16x16x32_bf16 v[108:111], v[44:47], v[228:231], v[108:111]\n"
"ds_read_b128 v[44:47], v33 offset:0\ns_waitcnt lgkmcnt(3)\nv_mfma_f32_16x16x32_bf16 v[96:99], v[48:51], v[232:235], v[96:99]\nv_add_u32 v34, s54, v34\n"
"v_mfma_f32_16x16x32_bf16 v[100:103], v[48:51], v[236:239], v[100:103]\nds_read_b128 v[48:51], v34 offset:0\ns_waitcnt lgkmcnt(3)\nv_mfma_f32_16x16x32_bf16 v[104:107], v[52:55], v[232:235], v[104:107]\n"
"v_add_u32 v35, s54, v35\nv_mfma_f32_16x16x32_bf16 v[108:111], v[52:55], v[236:239], v[108:111]\nds_read_b128 v[52:55], v35 offset:0\ns_mov_b32 s52, s47\n"
"s_add_u32 s47, s51, s32\ns_add_u32 s51, s51, s54\ns_add_u32 s49, s49, 28672\ns_waitcnt vmcnt(12)\n"
"s_barrier\ns_waitcnt lgkmcnt(3)\nv_mfma_f32_16x16x32_bf16 v[64:67], v[40:43], v[128:131], 0\nv_mov_b32 v36, s48\n"
"s_add_u32 s48, s48, 1\ns_nop 0\nbuffer_store_dword v36, v19, s[24:27], s33 offen sc1\nv_mfma_f32_16x16x32_bf16 v[68:71], v[40:43], v[132:135], 0\n"
"ds_read_b128 v[40:43], v32 offset:4096\ns_waitcnt lgkmcnt(3)\nv_mfma_f32_16x16x32_bf16 v[72:75], v[44:47], v[128:131], 0\nv_mfma_f32_16x16x32_bf16 v[76:79], v[44:47], v[132:135], 0\n"
"ds_read_b128 v[44:47], v33 offset:4096\nv_cvt_pk_bf16_f32 v56, v96, v97\ns_waitcnt lgkmcnt(3)\nv_mfma_f32_16x16x32_bf16 v[64:67], v[48:51], v[136:139], v[64:67]\n"
"v_cvt_pk_bf16_f32 v57, v98, v99\nv_mfma_f32_16x16x32_bf16 v[68:71], v[48:51], v[140:143], v[68:71]\nds_read_b128 v[48:51], v34 offset:4096\nv_cvt_pk_bf16_f32 v58, v104, v105\n"
"s_waitcnt lgkmcnt(3)\nv_mfma_f32_16x16x32_bf16 v[72:75], v[52:55], v[136:139], v[72:75]\nv_cvt_pk_bf16_f32 v59, v106, v107\nv_mfma_f32_16x16x32_bf16 v[76:79], v[52:55], v[140:143], v[76:79]\n"
"ds_read_b128 v[52:55], v35 offset:4096\ns_waitcnt lgkmcnt(3)\nv_mfma_f32_16x16x32_bf16 v[64:67], v[40:43], v[144:147], v[64:67]\nv_mfma_f32_16x16x32_bf16 v[68:71], v[40:43], v[148:151], v[68:71]\n"
"ds_read_b128 v[40:43], v32 offset:8192\nbuffer_store_dwordx4 v[56:59], v15, s[20:23], s50 offen sc1\ns_waitcnt lgkmcnt(3)\nv_mfma_f32_16x16x32_bf16 v[72:75], v[44:47], v[144:147], v[72:75]\n"
"v_cvt_pk_bf16_f32 v60, v100, v101\nv_mfma_f32_16x16x32_bf16 v[76:79], v[44:47], v[148:151], v[76:79]\nds_read_b128 v[44:47], v33 offset:8192\nv_cvt_pk_bf16_f32 v61, v102, v103\n"
"s_waitcnt lgkmcnt(3)\nv_mfma_f32_16x16x32_bf16 v[64:67], v[48:51], v[152:155], v[64:67]\nv_cvt_pk_bf16_f32 v62, v108, v109\nv_mfma_f32_16x16x32_bf16 v[68:71], v[48:51], v[156:159], v[68:71]\n"
"ds_read_b128 v[48:51], v34 offset:8192\nv_cvt_pk_bf16_f32 v63, v110, v111\ns_waitcnt lgkmcnt(3)\nv_mfma_f32_16x16x32_bf16 v[72:75], v[52:55], v[152:155], v[72:75]\n"
"v_mfma_f32_16x16x32_bf16 v[76:79], v[52:55], v[156:159], v[76:79]\nds_read_b128 v[52:55], v35 offset:8192\ns_waitcnt lgkmcnt(3)\nv_mfma_f32_16x16x32_bf16 v[64:67], v[40:43], v[160:163], v[64:67]\n"
"buffer_store_dwordx4 v[60:63], v16, s[20:23], s50 offen sc1\nv_mfma_f32_16x16x32_bf16 v[68:71], v[40:43], v[164:167], v[68:71]\nds_read_b128 v[40:43], v32 offset:12288\ns_add_u32 s50, s50, s36\n"
"s_waitcnt lgkmcnt(3)\nv_mfma_f32_16x16x32_bf16 v[72:75], v[44:47], v[160:163], v[72:75]\ns_add_u32 m0, s52, 0\nv_mfma_f32_16x16x32_bf16 v[76:79], v[44:47], v[164:167], v[76:79]\n"
"ds_read_b128 v[44:47], v33 offset:12288\ns_add_u32 s53, s49, 0\ns_waitcnt lgkmcnt(3)\nv_mfma_f32_16x16x32_bf16 v[64:67], v[48:51], v[168:171], v[64:67]\n"
"buffer_load_dwordx4 v10, s[12:15], s53 offen lds\nv_mfma_f32_16x16x32_bf16 v[68:71], v[48:51], v[172:175], v[68:71]\nds_read_b128 v[48:51], v34 offset:12288\ns_add_u32 m0, s52, 4096\n"
"s_waitcnt lgkmcnt(3)\nv_mfma_f32_16x16x32_bf16 v[72:75], v[52:55], v[168:171], v[72:75]\ns_add_u32 s53, s49, 128\nv_mfma_f32_16x16x32_bf16 v[76:79], v[52:55], v[172:175], v[76:79]\n"
"ds_read_b128 v[52:55], v35 offset:12288\nbuffer_load_dwordx4 v10, s[12:15], s53 offen lds\ns_waitcnt lgkmcnt(3)\nv_mfma_f32_16x16x32_bf16 v[64:67], v[40:43], v[176:179], v[64:67]\n"
"s_add_u32 m0, s52, 8192\nv_mfma_f32_16x16x32_bf16 v[68:71], v[40:43], v[180:183], v[68:71]\nds_read_b128 v[40:43], v32 offset:16384\ns_add_u32 s53, s49, 256\n"
"s_waitcnt lgkmcnt(3)\nv_mfma_f32_16x16x32_bf16 v[72:75], v[44:47], v[176:179], v[72:75]\nbuffer_load_dwordx4 v10, s[12:15], s53 offen lds\nv_mfma_f32_16x16x32_bf16 v[76:79], v[44:47], v[180:183], v[76:79]\n"
"ds_read_b128 v[44:47], v33 offset:16384\ns_add_u32 m0, s52, 12288\ns_waitcnt lgkmcnt(3)\nv_mfma_f32_16x16x32_bf16 v[64:67], v[48:51], v[184:187], v[64:67]\n"
"s_add_u32 s53, s49, 384\nv_mfma_f32_16x16x32_bf16 v[68:71], v[48:51], v[188:191], v[68:71]\nds_read_b128 v[48:51], v34 offset:16384\nbuffer_load_dwordx4 v10, s[12:15], s53 offen lds\n"
"s_waitcnt lgkmcnt(3)\nv_mfma_f32_16x16x32_bf16 v[72:75], v[52:55], v[184:187], v[72:75]\ns_add_u32 m0, s52, 16384\nv_mfma_f32_16x16x32_bf16 v[76:79], v[52:55], v[188:191], v[76:79]\n"
"ds_read_b128 v[52:55], v35 offset:16384\ns_add_u32 s53, s49, 512\ns_waitcnt lgkmcnt(3)\nv_mfma_f32_16x16x32_bf16 v[64:67], v[40:43], v[192:195], v[64:67]\n"
"buffer_load_dwordx4 v10, s[12:15], s53 offen lds\nv_mfma_f32_16x16x32_bf16 v[68:71], v[40:43], v[196:199], v[68:71]\nds_read_b128 v[40:43], v32 offset:20480\ns_add_u32 m0, s52, 20480\n"
"s_waitcnt lgkmcnt(3)\nv_mfma_f32_16x16x32_bf16 v[72:75], v[44:47], v[192:195], v[72:75]\ns_add_u32 s53, s49, 640\nv_mfma_f32_16x16x32_bf16 v[76:79], v[44:47], v[196:199], v[76:79]\n"
"ds_read_b128 v[44:47], v33 offset:20480\nbuffer_load_dwordx4 v10, s[12:15], s53 offen lds\ns_waitcnt lgkmcnt(3)\nv_mfma_f32_16x16x32_bf16 v[64:67], v[48:51], v[200:203], v[64:67]\n"
"s_add_u32 m0, s52, 24576\nv_mfma_f32_16x16x32_bf16 v[68:71], v[48:51], v[204:207], v[68:71]\nds_read_b128 v[48:51], v34 offset:20480\ns_add_u32 s53, s49, 768\n"
"s_waitcnt lgkmcnt(3)\nv_mfma_f32_16x16x32_bf16 v[72:75], v[52:55], v[200:203], v[72:75]\nbuffer_load_dwordx4 v10, s[12:15], s53 offen lds\nv_mfma_f32_16x16x32_bf16 v[76:79], v[52:55], v[204:207], v[76:79]\n"
"ds_read_b128 v[52:55], v35 offset:20480\nglobal_load_dwordx4 v[240:243], v23, s[38:39] offset:0\ns_waitcnt lgkmcnt(3)\nv_mfma_f32_16x16x32_bf16 v[64:67], v[40:43], v[208:211], v[64:67]\n"
"s_mov_b32 s53, 5120\nbuffer_load_dwordx4 a[40:43], v14, s[16:19], s53 offen sc1\nv_mfma_f32_16x16x32_bf16 v[68:71], v[40:43], v[212:215], v[68:71]\nds_read_b128 v[40:43], v32 offset:24576\n"
"s_mov_b32 s53, 267264\nbuffer_load_dwordx4 a[44:47], v14, s[16:19], s53 offen sc1\ns_waitcnt lgkmcnt(3)\nv_mfma_f32_16x16x32_bf16 v[72:75], v[44:47], v[208:211], v[72:75]\n"
"s_mov_b32 s53, 6144\nbuffer_load_dwordx4 a[48:51], v14, s[16:19], s53 offen sc1\nv_mfma_f32_16x16x32_bf16 v[76:79], v[44:47], v[212:215], v[76:79]\nds_read_b128 v[44:47], v33 offset:24576\n"
"s_mov_b32 s53, 268288\nbuffer_load_dwordx4 a[52:55], v14, s[16:19], s53 offen sc1\ns_waitcnt lgkmcnt(3)\nv_mfma_f32_16x16x32_bf16 v[64:67], v[48:51], v[216:219], v[64:67]\n"
"s_add_u32 s54, s51, 32768\ns_cmp_eq_u32 s54, 163840\nv_mfma_f32_16x16x32_bf16 v[68:71], v[48:51], v[220:223], v[68:71]\ns_cselect_b32 s54, 0, s54\n"
"s_sub_u32 s54, s54, s51\nds_read_b128 v[48:51], v34 offset:24576\ns_mov_b32 s53, 7168\nbuffer_load_dwordx4 a[56:59], v14, s[16:19], s53 offen sc1\n"
"s_waitcnt lgkmcnt(3)\nv_mfma_f32_16x16x32_bf16 v[72:75], v[52:55], v[216:219], v[72:75]\nv_mfma_f32_16x16x32_bf16 v[76:79], v[52:55], v[220:223], v[76:79]\nds_read_b128 v[52:55], v35 offset:24576\n"
"s_waitcnt lgkmcnt(3)\nv_mfma_f32_16x16x32_bf16 v[64:67], v[40:43], v[224:227], v[64:67]\nv_add_u32 v32, s54, v32\nv_mfma_f32_16x16x32_bf16 v[68:71], v[40:43], v[228:231], v[68:71]\n"
"ds_read_b128 v[40:43], v32 offset:0\ns_waitcnt lgkmcnt(3)\nv_mfma_f32_16x16x32_bf16 v[72:75], v[44:47], v[224:227], v[72:75]\nv_add_u32 v33, s54, v33\n"
"v_mfma_f32_16x16x32_bf16 v[76:79], v[44:47], v[228:231], v[76:79]\nds_read_b128 v[44:47], v33 offset:0\ns_waitcnt lgkmcnt(3)\nv_mfma_f32_16x16x32_bf16 v[64:67], v[48:51], v[232:235], v[64:67]\n"
"v_add_u32 v34, s54, v34\nv_mfma_f32_16x16x32_bf16 v[68:71], v[48:51], v[236:239], v[68:71]\nds_read_b128 v[48:51], v34 offset:0\ns_waitcnt lgkmcnt(3)\n"
"v_mfma_f32_16x16x32_bf16 v[72:75], v[52:55], v[232:235], v[72:75]\nv_add_u32 v35, s54, v35\nv_mfma_f32_16x16x32_bf16 v[76:79], v[52:55], v[236:239], v[76:79]\nds_read_b128 v[52:55], v35 offset:0\n"
"s_mov_b32 s52, s47\ns_add_u32 s47, s51, s32\ns_add_u32 s51, s51, s54\ns_add_u32 s49, s49, 28672\n"
"s_waitcnt vmcnt(13)\ns_barrier\ns_waitcnt lgkmcnt(3)\nv_mfma_f32_16x16x32_bf16 v[96:99], v[40:43], v[128:131], 0\n"
"v_mov_b32 v36, s48\ns_add_u32 s48, s48, 1\ns_nop 0\nbuffer_store_dword v36, v19, s[24:27], s33 offen sc1\n"
"v_mfma_f32_16x16x32_bf16 v[100:103], v[40:43], v[132:135], 0\nds_read_b128 v[40:43], v32 offset:4096\ns_waitcnt lgkmcnt(3)\nv_mfma_f32_16x16x32_bf16 v[104:107], v[44:47], v[128:131], 0\n"
"v_mfma_f32_16x16x32_bf16 v[108:111], v[44:47], v[132:135], 0\nds_read_b128 v[44:47], v33 offset:4096\nv_cvt_pk_bf16_f32 v56, v64, v65\ns_waitcnt lgkmcnt(3)\n"
"v_mfma_f32_16x16x32_bf16 v[96:99], v[48:51], v[136:139], v[96:99]\nv_cvt_pk_bf16_f32 v57, v66, v67\nv_mfma_f32_16x16x32_bf16 v[100:103], v[48:51], v[140:143], v[100:103]\nds_read_b128 v[48:51], v34 offset:4096\n"
"v_cvt_pk_bf16_f32 v58, v72, v73\ns_waitcnt lgkmcnt(3)\nv_mfma_f32_16x16x32_bf16 v[104:107], v[52:55], v[136:139], v[104:107]\nv_cvt_pk_bf16_f32 v59, v74, v75\n"
"v_mfma_f32_16x16x32_bf16 v[108:111], v[52:55], v[140:143], v[108:111]\nds_read_b128 v[52:55], v35 offset:4096\ns_waitcnt lgkmcnt(3)\nv_mfma_f32_16x16x32_bf16 v[96:99], v[40:43], v[144:147], v[96:99]\n"
"v_mfma_f32_16x16x32_bf16 v[100:103], v[40:43], v[148:151], v[100:103]\nds_read_b128 v[40:43], v32 offset:8192\nbuffer_store_dwordx4 v[56:59], v15, s[20:23], s50 offen sc1\ns_waitcnt lgkmcnt(3)\n"
"v_mfma_f32_16x16x32_bf16 v[104:107], v[44:47], v[144:147], v[104:107]\nv_cvt_pk_bf16_f32 v60, v68, v69\nv_mfma_f32_16x16x32_bf16 v[108:111], v[44:47], v[148:151], v[108:111]\nds_read_b128 v[44:47], v33 offset:8192\n"
"v_cvt_pk_bf16_f32 v61, v70, v71\ns_waitcnt lgkmcnt(3)\nv_mfma_f32_16x16x32_bf16 v[96:99], v[48:51], v[152:155], v[96:99]\nv_cvt_pk_bf16_f32 v62, v76, v77\n"
"v_mfma_f32_16x16x32_bf16 v[100:103], v[48:51], v[156:159], v[100:103]\nds_read_b128 v[48:51], v34 offset:8192\nv_cvt_pk_bf16_f32 v63, v78, v79\ns_waitcnt lgkmcnt(3)\n"
"v_mfma_f32_16x16x32_bf16 v[104:107], v[52:55], v[152:155], v[104:107]\nv_mfma_f32_16x16x32_bf16 v[108:111], v[52:55], v[156:159], v[108:111]\nds_read_b128 v[52:55], v35 offset:8192\ns_waitcnt lgkmcnt(3)\n"
"v_mfma_f32_16x16x32_bf16 v[96:99], v[40:43], v[160:163], v[96:99]\nbuffer_store_dwordx4 v[60:63], v16, s[20:23], s50 offen sc1\nv_mfma_f32_16x16x32_bf16 v[100:103], v[40:43], v[164:167], v[100:103]\nds_read_b128 v[40:43], v32 offset:12288\n"
"s_add_u32 s50, s50, s36\ns_waitcnt lgkmcnt(3)\nv_mfma_f32_16x16x32_bf16 v[104:107], v[44:47], v[160:163], v[104:107]\ns_add_u32 m0, s52, 0\n"
"v_mfma_f32_16x16x32_bf16 v[108:111], v[44:47], v[164:167], v[108:111]\nds_read_b128 v[44:47], v33 offset:12288\ns_add_u32 s53, s49, 0\ns_waitcnt lgkmcnt(3)\n"
"v_mfma_f32_16x16x32_bf16 v[96:99], v[48:51], v[168:171], v[96:99]\nbuffer_load_dwordx4 v10, s[12:15], s53 offen lds\nv_mfma_f32_16x16x32_bf16 v[100:103], v[48:51], v[172:175], v[100:103]\nds_read_b128 v[48:51], v34 offset:12288\n"
"s_add_u32 m0, s52, 4096\ns_waitcnt lgkmcnt(3)\nv_mfma_f32_16x16x32_bf16 v[104:107], v[52:55], v[168:171], v[104:107]\ns_add_u32 s53, s49, 128\n"
"v_mfma_f32_16x16x32_bf16 v[108:111], v[52:55], v[172:175], v[108:111]\nds_read_b128 v[52:55], v35 offset:12288\nbuffer_load_dwordx4 v10, s[12:15], s53 offen lds\ns_waitcnt lgkmcnt(3)\n"
"v_mfma_f32_16x16x32_bf16 v[96:99], v[40:43], v[176:179], v[96:99]\ns_add_u32 m0, s52, 8192\nv_mfma_f32_16x16x32_bf16 v[100:103], v[40:43], v[180:183], v[100:103]\nds_read_b128 v[40:43], v32 offset:16384\n"
"s_add_u32 s53, s49, 256\ns_waitcnt lgkmcnt(3)\nv_mfma_f32_16x16x32_bf16 v[104:107], v[44:47], v[176:179], v[104:107]\nbuffer_load_dwordx4 v10, s[12:15], s53 offen lds\n"
"v_mfma_f32_16x16x32_bf16 v[108:111], v[44:47], v[180:183], v[108:111]\nds_read_b128 v[44:47], v33 offset:16384\ns_add_u32 m0, s52, 12288\ns_waitcnt lgkmcnt(3)\n"
"v_mfma_f32_16x16x32_bf16 v[96:99], v[48:51], v[184:187], v[96:99]\ns_add_u32 s53, s49, 384\nv_mfma_f32_16x16x32_bf16 v[100:103], v[48:51], v[188:191], v[100:103]\nds_read_b128 v[48:51], v34 offset:16384\n"
"buffer_load_dwordx4 v10, s[12:15], s53 offen lds\ns_waitcnt lgkmcnt(3)\nv_mfma_f32_16x16x32_bf16 v[104:107], v[52:55], v[184:187], v[104:107]\ns_add_u32 m0, s52, 16384\n"
"v_mfma_f32_16x16x32_bf16 v[108:111], v[52:55], v[188:191], v[108:111]\nds_read_b128 v[52:55], v35 offset:16384\ns_add_u32 s53, s49, 512\ns_waitcnt lgkmcnt(3)\n"
"v_mfma_f32_16x16x32_bf16 v[96:99], v[40:43], v[192:195], v[96:99]\nbuffer_load_dwordx4 v10, s[12:15], s53 offen lds\nv_mfma_f32_16x16x32_bf16 v[100:103], v[40:43], v[196:199], v[100:103]\nds_read_b128 v[40:43], v32 offset:20480\n"
"s_add_u32 m0, s52, 20480\ns_waitcnt lgkmcnt(3)\nv_mfma_f32_16x16x32_bf16 v[104:107], v[44:47], v[192:195], v[104:107]\ns_add_u32 s53, s49, 640\n"
"v_mfma_f32_16x16x32_bf16 v[108:111], v[44:47], v[196:199], v[108:111]\nds_read_b128 v[44:47], v33 offset:20480\nbuffer_load_dwordx4 v10, s[12:15], s53 offen lds\ns_waitcnt lgkmcnt(3)\n"
"v_mfma_f32_16x16x32_bf16 v[96:99], v[48:51], v[200:203], v[96:99]\ns_add_u32 m0, s52, 24576\nv_mfma_f32_16x16x32_bf16 v[100:103], v[48:51], v[204:207], v[100:103]\nds_read_b128 v[48:51], v34 offset:20480\n"
"s_add_u32 s53, s49, 768\ns_waitcnt lgkmcnt(3)\nv_mfma_f32_16x16x32_bf16 v[104:107], v[52:55], v[200:203], v[104:107]\nbuffer_load_dwordx4 v10, s[12:15], s53 offen lds\n"
"v_mfma_f32_16x16x32_bf16 v[108:111], v[52:55], v[204:207], v[108:111]\nds_read_b128 v[52:55], v35 offset:20480\nglobal_load_dwordx4 v[240:243], v23, s[38:39] offset:1024\ns_waitcnt lgkmcnt(3)\n"
"v_mfma_f32_16x16x32_bf16 v[96:99], v[40:43], v[208:211], v[96:99]\ns_mov_b32 s53, 269312\nbuffer_load_dwordx4 a[60:63], v14, s[16:19], s53 offen sc1\nv_mfma_f32_16x16x32_bf16 v[100:103], v[40:43], v[212:215], v[100:103]\n"
"ds_read_b128 v[40:43], v32 offset:24576\ns_mov_b32 s53, 8192\nbuffer_load_dwordx4 a[64:67], v14, s[16:19], s53 offen sc1\ns_waitcnt lgkmcnt(3)\n"
"v_mfma_f32_16x16x32_bf16 v[104:107], v[44:47], v[208:211], v[104:107]\ns_mov_b32 s53, 270336\nbuffer_load_dwordx4 a[68:71], v14, s[16:19], s53 offen sc1\nv_mfma_f32_16x16x32_bf16 v[108:111], v[44:47], v[212:215], v[108:111]\n"
"ds_read_b128 v[44:47], v33 offset:24576\ns_mov_b32 s53, 9216\nbuffer_load_dwordx4 a[72:75], v14, s[16:19], s53 offen sc1\ns_waitcnt lgkmcnt(3)\n"
"v_mfma_f32_16x16x32_bf16 v[96:99], v[48:51], v[216:219], v[96:99]\ns_add_u32 s54, s51, 32768\ns_cmp_eq_u32 s54, 163840\nv_mfma_f32_16x16x32_bf16 v[100:103], v[48:51], v[220:223], v[100:103]\n"
"s_cselect_b32 s54, 0, s54\ns_sub_u32 s54, s54, s51\nds_read_b128 v[48:51], v34 offset:24576\ns_mov_b32 s53, 271360\n"
"buffer_load_dwordx4 a[76:79], v14, s[16:19], s53 offen sc1\ns_waitcnt lgkmcnt(3)\nv_mfma_f32_16x16x32_bf16 v[104:107], v[52:55], v[216:219], v[104:107]\nv_mfma_f32_16x16x32_bf16 v[108:111], v[52:55], v[220:223], v[108:111]\n"
"ds_read_b128 v[52:55], v35 offset:24576\ns_waitcnt lgkmcnt(3)\nv_mfma_f32_16x16x32_bf16 v[96:99], v[40:43], v[224:227], v[96:99]\nv_add_u32 v32, s54, v32\n"
"v_mfma_f32_16x16x32_bf16 v[100:103], v[40:43], v[228:231], v[100:103]\nds_read_b128 v[40:43], v32 offset:0\ns_waitcnt lgkmcnt(3)\nv_mfma_f32_16x16x32_bf16 v[104:107], v[44:47], v[224:227], v[104:107]\n"
"v_add_u32 v33, s54, v33\nv_mfma_f32_16x16x32_bf16 v[108:111], v[44:47], v[228:231], v[108:111]\nds_read_b128 v[44:47], v33 offset:0\ns_waitcnt lgkmcnt(3)\n"
"v_mfma_f32_16x16x32_bf16 v[96:99], v[48:51], v[232:235], v[96:99]\nv_add_u32 v34, s54, v34\nv_mfma_f32_16x16x32_bf16 v[100:103], v[48:51], v[236:239], v[100:103]\nds_read_b128 v[48:51], v34 offset:0\n"
"s_waitcnt lgkmcnt(3)\nv_mfma_f32_16x16x32_bf16 v[104:107], v[52:55], v[232:235], v[104:107]\nv_add_u32 v35, s54, v35\nv_mfma_f32_16x16x32_bf16 v[108:111], v[52:55], v[236:239], v[108:111]\n"
"ds_read_b128 v[52:55], v35 offset:0\ns_mov_b32 s52, s47\ns_add_u32 s47, s51, s32\ns_add_u32 s51, s51, s54\n"
"s_add_u32 s49, s49, 28672\ns_waitcnt vmcnt(13)\ns_barrier\ns_waitcnt lgkmcnt(3)\n"
"v_mfma_f32_16x16x32_bf16 v[64:67], v[40:43], v[128:131], 0\nv_mov_b32 v36, s48\ns_add_u32 s48, s48, 1\ns_nop 0\n"
"buffer_store_dword v36, v19, s[24:27], s33 offen sc1\nv_mfma_f32_16x16x32_bf16 v[68:71], v[40:43], v[132:135], 0\nds_read_b128 v[40:43], v32 offset:4096\ns_waitcnt lgkmcnt(3)\n"
"v_mfma_f32_16x16x32_bf16 v[72:75], v[44:47], v[128:131], 0\nv_mfma_f32_16x16x32_bf16 v[76:79], v[44:47], v[132:135], 0\nds_read_b128 v[44:47], v33 offset:4096\nv_cvt_pk_bf16_f32 v56, v96, v97\n"
"s_waitcnt lgkmcnt(3)\nv_mfma_f32_16x16x32_bf16 v[64:67], v[48:51], v[136:139], v[64:67]\nv_cvt_pk_bf16_f32 v57, v98, v99\nv_mfma_f32_16x16x32_bf16 v[68:71], v[48:51], v[140:143], v[68:71]\n"
"ds_read_b128 v[48:51], v34 offset:4096\nv_cvt_pk_bf16_f32 v58, v104, v105\ns_waitcnt lgkmcnt(3)\nv_mfma_f32_16x16x32_bf16 v[72:75], v[52:55], v[136:139], v[72:75]\n"
"v_cvt_pk_bf16_f32 v59, v106, v107\nv_mfma_f32_16x16x32_bf16 v[76:79], v[52:55], v[140:143], v[76:79]\nds_read_b128 v[52:55], v35 offset:4096\ns_waitcnt lgkmcnt(3)\n"
"v_mfma_f32_16x16x32_bf16 v[64:67], v[40:43], v[144:147], v[64:67]\nv_mfma_f32_16x16x32_bf16 v[68:71], v[40:43], v[148:151], v[68:71]\nds_read_b128 v[40:43], v32 offset:8192\nbuffer_store_dwordx4 v[56:59], v15, s[20:23], s50 offen sc1\n"
"s_waitcnt lgkmcnt(3)\nv_mfma_f32_16x16x32_bf16 v[72:75], v[44:47], v[144:147], v[72:75]\nv_cvt_pk_bf16_f32 v60, v100, v101\nv_mfma_f32_16x16x32_bf16 v[76:79], v[44:47], v[148:151], v[76:79]\n"
"ds_read_b128 v[44:47], v33 offset:8192\nv_cvt_pk_bf16_f32 v61, v102, v103\ns_waitcnt lgkmcnt(3)\nv_mfma_f32_16x16x32_bf16 v[64:67], v[48:51], v[152:155], v[64:67]\n"
"v_cvt_pk_bf16_f32 v62, v108, v109\nv_mfma_f32_16x16x32_bf16 v[68:71], v[48:51], v[156:159], v[68:71]\nds_read_b128 v[48:51], v34 offset:8192\nv_cvt_pk_bf16_f32 v63, v110, v111\n"
"s_waitcnt lgkmcnt(3)\nv_mfma_f32_16x16x32_bf16 v[72:75], v[52:55], v[152:155], v[72:75]\nv_mfma_f32_16x16x32_bf16 v[76:79], v[52:55], v[156:159], v[76:79]\nds_read_b128 v[52:55], v35 offset:8192\n"
"s_waitcnt lgkmcnt(3)\nv_mfma_f32_16x16x32_bf16 v[64:67], v[40:43], v[160:163], v[64:67]\nbuffer_store_dwordx4 v[60:63], v16, s[20:23], s50 offen sc1\nv_mfma_f32_16x16x32_bf16 v[68:71], v[40:43], v[164:167], v[68:71]\n"
"ds_read_b128 v[40:43], v32 offset:12288\ns_add_u32 s50, s50, s36\ns_waitcnt lgkmcnt(3)\nv_mfma_f32_16x16x32_bf16 v[72:75], v[44:47], v[160:163], v[72:75]\n"
"s_add_u32 m0, s52, 0\nv_mfma_f32_16x16x32_bf16 v[76:79], v[44:47], v[164:167], v[76:79]\nds_read_b128 v[44:47], v33 offset:12288\ns_add_u32 s53, s49, 0\n"
"s_waitcnt lgkmcnt(3)\nv_mfma_f32_16x16x32_bf16 v[64:67], v[48:51], v[168:171], v[64:67]\nbuffer_load_dwordx4 v10, s[12:15], s53 offen lds\nv_mfma_f32_16x16x32_bf16 v[68:71], v[48:51], v[172:175], v[68:71]\n"
"ds_read_b128 v[48:51], v34 offset:12288\ns_add_u32 m0, s52, 4096\ns_waitcnt lgkmcnt(3)\nv_mfma_f32_16x16x32_bf16 v[72:75], v[52:55], v[168:171], v[72:75]\n"
"s_add_u32 s53, s49, 128\nv_mfma_f32_16x16x32_bf16 v[76:79], v[52:55], v[172:175], v[76:79]\nds_read_b128 v[52:55], v35 offset:12288\nbuffer_load_dwordx4 v10, s[12:15], s53 offen lds\n"
"s_waitcnt lgkmcnt(3)\nv_mfma_f32_16x16x32_bf16 v[64:67], v[40:43], v[176:179], v[64:67]\ns_add_u32 m0, s52, 8192\nv_mfma_f32_16x16x32_bf16 v[68:71], v[40:43], v[180:183], v[68:71]\n"
"ds_read_b128 v[40:43], v32 offset:16384\ns_add_u32 s53, s49, 256\ns_waitcnt lgkmcnt(3)\nv_mfma_f32_16x16x32_bf16 v[72:75], v[44:47], v[176:179], v[72:75]\n"
"buffer_load_dwordx4 v10, s[12:15], s53 offen lds\nv_mfma_f32_16x16x32_bf16 v[76:79], v[44:47], v[180:183], v[76:79]\nds_read_b128 v[44:47], v33 offset:16384\ns_add_u32 m0, s52, 12288\n"
"s_waitcnt lgkmcnt(3)\nv_mfma_f32_16x16x32_bf16 v[64:67], v[48:51], v[184:187], v[64:67]\ns_add_u32 s53, s49, 384\nv_mfma_f32_16x16x32_bf16 v[68:71], v[48:51], v[188:191], v[68:71]\n"
"ds_read_b128 v[48:51], v34 offset:16384\nbuffer_load_dwordx4 v10, s[12:15], s53 offen lds\ns_waitcnt lgkmcnt(3)\nv_mfma_f32_16x16x32_bf16 v[72:75], v[52:55], v[184:187], v[72:75]\n"
"s_add_u32 m0, s52, 16384\nv_mfma_f32_16x16x32_bf16 v[76:79], v[52:55], v[188:191], v[76:79]\nds_read_b128 v[52:55], v35 offset:16384\ns_add_u32 s53, s49, 512\n"
"s_waitcnt lgkmcnt(3)\nv_mfma_f32_16x16x32_bf16 v[64:67], v[40:43], v[192:195], v[64:67]\nbuffer_load_dwordx4 v10, s[12:15], s53 offen lds\nv_mfma_f32_16x16x32_bf16 v[68:71], v[40:43], v[196:199], v[68:71]\n"
"ds_read_b128 v[40:43], v32 offset:20480\ns_add_u32 m0, s52, 20480\ns_waitcnt lgkmcnt(3)\nv_mfma_f32_16x16x32_bf16 v[72:75], v[44:47], v[192:195], v[72:75]\n"
"s_add_u32 s53, s49, 640\nv_mfma_f32_16x16x32_bf16 v[76:79], v[44:47], v[196:199], v[76:79]\nds_read_b128 v[44:47], v33 offset:20480\nbuffer_load_dwordx4 v10, s[12:15], s53 offen lds\n"
"s_waitcnt lgkmcnt(3)\nv_mfma_f32_16x16x32_bf16 v[64:67], v[48:51], v[200:203], v[64:67]\ns_add_u32 m0, s52, 24576\nv_mfma_f32_16x16x32_bf16 v[68:71], v[48:51], v[204:207], v[68:71]\n"
"ds_read_b128 v[48:51], v34 offset:20480\ns_add_u32 s53, s49, 768\ns_waitcnt lgkmcnt(3)\nv_mfma_f32_16x16x32_bf16 v[72:75], v[52:55], v[200:203], v[72:75]\n"
"buffer_load_dwordx4 v10, s[12:15], s53 offen lds\nv_mfma_f32_16x16x32_bf16 v[76:79], v[52:55], v[204:207], v[76:79]\nds_read_b128 v[52:55], v35 offset:20480\nglobal_load_dwordx4 v[240:243], v23, s[38:39] offset:2048\n"
"s_waitcnt lgkmcnt(3)\nv_mfma_f32_16x16x32_bf16 v[64:67], v[40:43], v[208:211], v[64:67]\ns_mov_b32 s53, 10240\nbuffer_load_dwordx4 a[80:83], v14, s[16:19], s53 offen sc1\n"
"v_mfma_f32_16x16x32_bf16 v[68:71], v[40:43], v[212:215], v[68:71]\nds_read_b128 v[40:43], v32 offset:24576\ns_mov_b32 s53, 272384\nbuffer_load_dwordx4 a[84:87], v14, s[16:19], s53 offen sc1\n"
"s_waitcnt lgkmcnt(3)\nv_mfma_f32_16x16x32_bf16 v[72:75], v[44:47], v[208:211], v[72:75]\ns_mov_b32 s53, 11264\nbuffer_load_dwordx4 a[88:91], v14, s[16:19], s53 offen sc1\n"
"v_mfma_f32_16x16x32_bf16 v[76:79], v[44:47], v[212:215], v[76:79]\nds_read_b128 v[44:47], v33 offset:24576\ns_mov_b32 s53, 273408\nbuffer_load_dwordx4 a[92:95], v14, s[16:19], s53 offen sc1\n"
"s_waitcnt lgkmcnt(3)\nv_mfma_f32_16x16x32_bf16 v[64:67], v[48:51], v[216:219], v[64:67]\ns_add_u32 s54, s51, 32768\ns_cmp_eq_u32 s54, 163840\n"
"v_mfma_f32_16x16x32_bf16 v[68:71], v[48:51], v[220:223], v[68:71]\ns_cselect_b32 s54, 0, s54\ns_sub_u32 s54, s54, s51\nds_read_b128 v[48:51], v34 offset:24576\n"
"s_mov_b32 s53, 12288\nbuffer_load_dwordx4 a[96:99], v14, s[16:19], s53 offen sc1\ns_waitcnt lgkmcnt(3)\nv_mfma_f32_16x16x32_bf16 v[72:75], v[52:55], v[216:219], v[72:75]\n"
"v_mfma_f32_16x16x32_bf16 v[76:79], v[52:55], v[220:223], v[76:79]\nds_read_b128 v[52:55], v35 offset:24576\ns_waitcnt lgkmcnt(3)\nv_mfma_f32_16x16x32_bf16 v[64:67], v[40:43], v[224:227], v[64:67]\n"
"v_add_u32 v32, s54, v32\nv_mfma_f32_16x16x32_bf16 v[68:71], v[40:43], v[228:231], v[68:71]\nds_read_b128 v[40:43], v32 offset:0\ns_waitcnt lgkmcnt(3)\n"
"v_mfma_f32_16x16x32_bf16 v[72:75], v[44:47], v[224:227], v[72:75]\nv_add_u32 v33, s54, v33\nv_mfma_f32_16x16x32_bf16 v[76:79], v[44:47], v[228:231], v[76:79]\nds_read_b128 v[44:47], v33 offset:0\n"
"s_waitcnt lgkmcnt(3)\nv_mfma_f32_16x16x32_bf16 v[64:67], v[48:51], v[232:235], v[64:67]\nv_add_u32 v34, s54, v34\nv_mfma_f32_16x16x32_bf16 v[68:71], v[48:51], v[236:239], v[68:71]\n"
"ds_read_b128 v[48:51], v34 offset:0\ns_waitcnt lgkmcnt(3)\nv_mfma_f32_16x16x32_bf16 v[72:75], v[52:55], v[232:235], v[72:75]\nv_add_u32 v35, s54, v35\n"
"v_mfma_f32_16x16x32_bf16 v[76:79], v[52:55], v[236:239], v[76:79]\nds_read_b128 v[52:55], v35 offset:0\ns_mov_b32 s52, s47\ns_add_u32 s47, s51, s32\n"
"s_add_u32 s51, s51, s54\ns_add_u32 s49, s49, 28672\ns_waitcnt vmcnt(13)\ns_barrier\n"
"s_waitcnt lgkmcnt(3)\nv_mfma_f32_16x16x32_bf16 v[96:99], v[40:43], v[128:131], 0\nv_mov_b32 v36, s48\ns_add_u32 s48, s48, 1\n"
"s_nop 0\nbuffer_store_dword v36, v19, s[24:27], s33 offen sc1\nv_mfma_f32_16x16x32_bf16 v[100:103], v[40:43], v[132:135], 0\nds_read_b128 v[40:43], v32 offset:4096\n"
"s_waitcnt lgkmcnt(3)\nv_mfma_f32_16x16x32_bf16 v[104:107], v[44:47], v[128:131], 0\nv_mfma_f32_16x16x32_bf16 v[108:111], v[44:47], v[132:135], 0\nds_read_b128 v[44:47], v33 offset:4096\n"
"v_cvt_pk_bf16_f32 v56, v64, v65\ns_waitcnt lgkmcnt(3)\nv_mfma_f32_16x16x32_bf16 v[96:99], v[48:51], v[136:139], v[96:99]\nv_cvt_pk_bf16_f32 v57, v66, v67\n"
"v_mfma_f32_16x16x32_bf16 v[100:103], v[48:51], v[140:143], v[100:103]\nds_read_b128 v[48:51], v34 offset:4096\nv_cvt_pk_bf16_f32 v58, v72, v73\ns_waitcnt lgkmcnt(3)\n"
"v_mfma_f32_16x16x32_bf16 v[104:107], v[52:55], v[136:139], v[104:107]\nv_cvt_pk_bf16_f32 v59, v74, v75\nv_mfma_f32_16x16x32_bf16 v[108:111], v[52:55], v[140:143], v[108:111]\nds_read_b128 v[52:55], v35 offset:4096\n"
"s_waitcnt lgkmcnt(3)\nv_mfma_f32_16x16x32_bf16 v[96:99], v[40:43], v[144:147], v[96:99]\nv_mfma_f32_16x16x32_bf16 v[100:103], v[40:43], v[148:151], v[100:103]\nds_read_b128 v[40:43], v32 offset:8192\n"
"buffer_store_dwordx4 v[56:59], v15, s[20:23], s50 offen sc1\ns_waitcnt lgkmcnt(3)\nv_mfma_f32_16x16x32_bf16 v[104:107], v[44:47], v[144:147], v[104:107]\nv_cvt_pk_bf16_f32 v60, v68, v69\n"
"v_mfma_f32_16x16x32_bf16 v[108:111], v[44:47], v[148:151], v[108:111]\nds_read_b128 v[44:47], v33 offset:8192\nv_cvt_pk_bf16_f32 v61, v70, v71\ns_waitcnt lgkmcnt(3)\n"
"v_mfma_f32_16x16x32_bf16 v[96:99], v[48:51], v[152:155], v[96:99]\nv_cvt_pk_bf16_f32 v62, v76, v77\nv_mfma_f32_16x16x32_bf16 v[100:103], v[48:51], v[156:159], v[100:103]\nds_read_b128 v[48:51], v34 offset:8192\n"
"v_cvt_pk_bf16_f32 v63, v78, v79\ns_waitcnt lgkmcnt(3)\nv_mfma_f32_16x16x32_bf16 v[104:107], v[52:55], v[152:155], v[104:107]\nv_mfma_f32_16x16x32_bf16 v[108:111], v[52:55], v[156:159], v[108:111]\n"
"ds_read_b128 v[52:55], v35 offset:8192\ns_waitcnt lgkmcnt(3)\nv_mfma_f32_16x16x32_bf16 v[96:99], v[40:43], v[160:163], v[96:99]\nbuffer_store_dwordx4 v[60:63], v16, s[20:23], s50 offen sc1\n"
"v_mfma_f32_16x16x32_bf16 v[100:103], v[40:43], v[164:167], v[100:103]\nds_read_b128 v[40:43], v32 offset:12288\ns_add_u32 s50, s50, s36\ns_waitcnt lgkmcnt(3)\n"
"v_mfma_f32_16x16x32_bf16 v[104:107], v[44:47], v[160:163], v[104:107]\ns_add_u32 m0, s52, 0\nv_mfma_f32_16x16x32_bf16 v[108:111], v[44:47], v[164:167], v[108:111]\nds_read_b128 v[44:47], v33 offset:12288\n"
"s_add_u32 s53, s49, 0\ns_waitcnt lgkmcnt(3)\nv_mfma_f32_16x16x32_bf16 v[96:99], v[48:51], v[168:171], v[96:99]\nbuffer_load_dwordx4 v10, s[12:15], s53 offen lds\n"
"v_mfma_f32_16x16x32_bf16 v[100:103], v[48:51], v[172:175], v[100:103]\nds_read_b128 v[48:51], v34 offset:12288\ns_add_u32 m0, s52, 4096\ns_waitcnt lgkmcnt(3)\n"
"v_mfma_f32_16x16x32_bf16 v[104:107], v[52:55], v[168:171], v[104:107]\ns_add_u32 s53, s49, 128\nv_mfma_f32_16x16x32_bf16 v[108:111], v[52:55], v[172:175], v[108:111]\nds_read_b128 v[52:55], v35 offset:12288\n"
"buffer_load_dwordx4 v10, s[12:15], s53 offen lds\ns_waitcnt lgkmcnt(3)\nv_mfma_f32_16x16x32_bf16 v[96:99], v[40:43], v[176:179], v[96:99]\ns_add_u32 m0, s52, 8192\n"
"v_mfma_f32_16x16x32_bf16 v[100:103], v[40:43], v[180:183], v[100:103]\nds_read_b128 v[40:43], v32 offset:16384\ns_add_u32 s53, s49, 256\ns_waitcnt lgkmcnt(3)\n"
"v_mfma_f32_16x16x32_bf16 v[104:107], v[44:47], v[176:179], v[104:107]\nbuffer_load_dwordx4 v10, s[12:15], s53 offen lds\nv_mfma_f32_16x16x32_bf16 v[108:111], v[44:47], v[180:183], v[108:111]\nds_read_b128 v[44:47], v33 offset:16384\n"
"s_add_u32 m0, s52, 12288\ns_waitcnt lgkmcnt(3)\nv_mfma_f32_16x16x32_bf16 v[96:99], v[48:51], v[184:187], v[96:99]\ns_add_u32 s53, s49, 384\n"
"v_mfma_f32_16x16x32_bf16 v[100:103], v[48:51], v[188:191], v[100:103]\nds_read_b128 v[48:51], v34 offset:16384\nbuffer_load_dwordx4 v10, s[12:15], s53 offen lds\ns_waitcnt lgkmcnt(3)\n"
"v_mfma_f32_16x16x32_bf16 v[104:107], v[52:55], v[184:187], v[104:107]\ns_add_u32 m0, s52, 16384\nv_mfma_f32_16x16x32_bf16 v[108:111], v[52:55], v[188:191], v[108:111]\nds_read_b128 v[52:55], v35 offset:16384\n"
"s_add_u32 s53, s49, 512\ns_waitcnt lgkmcnt(3)\nv_mfma_f32_16x16x32_bf16 v[96:99], v[40:43], v[192:195], v[96:99]\nbuffer_load_dwordx4 v10, s[12:15], s53 offen lds\n"
"v_mfma_f32_16x16x32_bf16 v[100:103], v[40:43], v[196:199], v[100:103]\nds_read_b128 v[40:43], v32 offset:20480\ns_add_u32 m0, s52, 20480\ns_waitcnt lgkmcnt(3)\n"
"v_mfma_f32_16x16x32_bf16 v[104:107], v[44:47], v[192:195], v[104:107]\ns_add_u32 s53, s49, 640\nv_mfma_f32_16x16x32_bf16 v[108:111], v[44:47], v[196:199], v[108:111]\nds_read_b128 v[44:47], v33 offset:20480\n"
"buffer_load_dwordx4 v10, s[12:15], s53 offen lds\ns_waitcnt lgkmcnt(3)\nv_mfma_f32_16x16x32_bf16 v[96:99], v[48:51], v[200:203], v[96:99]\ns_add_u32 m0, s52, 24576\n"
"v_mfma_f32_16x16x32_bf16 v[100:103], v[48:51], v[204:207], v[100:103]\nds_read_b128 v[48:51], v34 offset:20480\ns_add_u32 s53, s49, 768\ns_waitcnt lgkmcnt(3)\n"
"v_mfma_f32_16x16x32_bf16 v[104:107], v[52:55], v[200:203], v[104:107]\nbuffer_load_dwordx4 v10, s[12:15], s53 offen lds\nv_mfma_f32_16x16x32_bf16 v[108:111], v[52:55], v[204:207], v[108:111]\nds_read_b128 v[52:55], v35 offset:20480\n"
"global_load_dwordx4 v[240:243], v23, s[38:39] offset:3072\ns_waitcnt lgkmcnt(3)\nv_mfma_f32_16x16x32_bf16 v[96:99], v[40:43], v[208:211], v[96:99]\ns_mov_b32 s53, 274432\n"
"buffer_load_dwordx4 a[100:103], v14, s[16:19], s53 offen sc1\nv_mfma_f32_16x16x32_bf16 v[100:103], v[40:43], v[212:215], v[100:103]\nds_read_b128 v[40:43], v32 offset:24576\ns_mov_b32 s53, 13312\n"
"buffer_load_dwordx4 a[104:107], v14, s[16:19], s53 offen sc1\ns_waitcnt lgkmcnt(3)\nv_mfma_f32_16x16x32_bf16 v[104:107], v[44:47], v[208:211], v[104:107]\ns_mov_b32 s53, 275456\n"
"buffer_load_dwordx4 a[108:111], v14, s[16:19], s53 offen sc1\nv_mfma_f32_16x16x32_bf16 v[108:111], v[44:47], v[212:215], v[108:111]\nds_read_b128 v[44:47], v33 offset:24576\ns_waitcnt lgkmcnt(3)\n"
"v_mfma_f32_16x16x32_bf16 v[96:99], v[48:51], v[216:219], v[96:99]\ns_add_u32 s54, s51, 32768\ns_cmp_eq_u32 s54, 163840\nv_mfma_f32_16x16x32_bf16 v[100:103], v[48:51], v[220:223], v[100:103]\n"
"s_cselect_b32 s54, 0, s54\ns_sub_u32 s54, s54, s51\nds_read_b128 v[48:51], v34 offset:24576\ns_waitcnt lgkmcnt(3)\n"
"v_mfma_f32_16x16x32_bf16 v[104:107], v[52:55], v[216:219], v[104:107]\nv_mfma_f32_16x16x32_bf16 v[108:111], v[52:55], v[220:223], v[108:111]\nds_read_b128 v[52:55], v35 offset:24576\ns_waitcnt lgkmcnt(3)\n"
"v_mfma_f32_16x16x32_bf16 v[96:99], v[40:43], v[224:227], v[96:99]\nv_add_u32 v32, s54, v32\nv_mfma_f32_16x16x32_bf16 v[100:103], v[40:43], v[228:231], v[100:103]\nds_read_b128 v[40:43], v32 offset:0\n"
"s_waitcnt lgkmcnt(3)\nv_mfma_f32_16x16x32_bf16 v[104:107], v[44:47], v[224:227], v[104:107]\nv_add_u32 v33, s54, v33\nv_mfma_f32_16x16x32_bf16 v[108:111], v[44:47], v[228:231], v[108:111]\n"
"ds_read_b128 v[44:47], v33 offset:0\ns_waitcnt lgkmcnt(3)\nv_mfma_f32_16x16x32_bf16 v[96:99], v[48:51], v[232:235], v[96:99]\nv_add_u32 v34, s54, v34\n"
"v_mfma_f32_16x16x32_bf16 v[100:103], v[48:51], v[236:239], v[100:103]\nds_read_b128 v[48:51], v34 offset:0\ns_waitcnt lgkmcnt(3)\nv_mfma_f32_16x16x32_bf16 v[104:107], v[52:55], v[232:235], v[104:107]\n"
"v_add_u32 v35, s54, v35\nv_mfma_f32_16x16x32_bf16 v[108:111], v[52:55], v[236:239], v[108:111]\nds_read_b128 v[52:55], v35 offset:0\ns_mov_b32 s52, s47\n"
"s_add_u32 s47, s51, s32\ns_add_u32 s51, s51, s54\ns_add_u32 s49, s49, 28672\ns_cmp_lg_u32 s34, 0\n"
"s_cbranch_scc1 fg1_stay\ns_nop 7\nv_cvt_pk_bf16_f32 v56, v96, v97\nv_cvt_pk_bf16_f32 v57, v98, v99\n"
"v_cvt_pk_bf16_f32 v58, v104, v105\nv_cvt_pk_bf16_f32 v59, v106, v107\ns_nop 0\ns_nop 0\n"
"buffer_store_dwordx4 v[56:59], v15, s[20:23], s50 offen sc1\nv_cvt_pk_bf16_f32 v60, v100, v101\nv_cvt_pk_bf16_f32 v61, v102, v103\nv_cvt_pk_bf16_f32 v62, v108, v109\n"
"v_cvt_pk_bf16_f32 v63, v110, v111\ns_nop 0\ns_nop 0\nbuffer_store_dwordx4 v[60:63], v16, s[20:23], s50 offen sc1\n"
"s_add_u32 s48, s28, 64\nv_mov_b32 v36, s48\ns_waitcnt vmcnt(0) lgkmcnt(0)\ns_barrier\n"
"buffer_store_dword v36, v19, s[24:27], s33 offen sc1\ns_branch fg1_end\nfg1_stay:\ns_waitcnt vmcnt(0)\n"
"s_barrier\ns_waitcnt lgkmcnt(3)\nv_mfma_f32_16x16x32_bf16 v[64:67], v[40:43], v[128:131], 0\nv_mov_b32 v36, s48\n"
"s_add_u32 s48, s48, 1\ns_nop 0\nbuffer_store_dword v36, v19, s[24:27], s33 offen sc1\nv_mfma_f32_16x16x32_bf16 v[68:71], v[40:43], v[132:135], 0\n"
"v_mfma_f32_16x16x32_bf16 v[72:75], v[40:43], a[0:3], 0\nv_mfma_f32_16x16x32_bf16 v[76:79], v[40:43], a[4:7], 0\nds_read_b128 v[40:43], v32 offset:4096\nv_cvt_pk_bf16_f32 v56, v96, v97\n"
"s_waitcnt lgkmcnt(3)\nv_mfma_f32_16x16x32_bf16 v[80:83], v[44:47], v[128:131], 0\nv_cvt_pk_bf16_f32 v57, v98, v99\nv_mfma_f32_16x16x32_bf16 v[84:87], v[44:47], v[132:135], 0\n"
"v_cvt_pk_bf16_f32 v58, v104, v105\nv_mfma_f32_16x16x32_bf16 v[88:91], v[44:47], a[0:3], 0\nv_cvt_pk_bf16_f32 v59, v106, v107\nv_mfma_f32_16x16x32_bf16 v[92:95], v[44:47], a[4:7], 0\n"
"ds_read_b128 v[44:47], v33 offset:4096\ns_waitcnt lgkmcnt(3)\nv_mfma_f32_16x16x32_bf16 v[64:67], v[48:51], v[136:139], v[64:67]\nv_mfma_f32_16x16x32_bf16 v[68:71], v[48:51], v[140:143], v[68:71]\n"
"buffer_store_dwordx4 v[56:59], v15, s[20:23], s50 offen sc1\nv_mfma_f32_16x16x32_bf16 v[72:75], v[48:51], a[8:11], v[72:75]\nv_cvt_pk_bf16_f32 v60, v100, v101\nv_mfma_f32_16x16x32_bf16 v[76:79], v[48:51], a[12:15], v[76:79]\n"
"ds_read_b128 v[48:51], v34 offset:4096\nv_cvt_pk_bf16_f32 v61, v102, v103\ns_waitcnt lgkmcnt(3)\nv_mfma_f32_16x16x32_bf16 v[80:83], v[52:55], v[136:139], v[80:83]\n"
"v_cvt_pk_bf16_f32 v62, v108, v109\nv_mfma_f32_16x16x32_bf16 v[84:87], v[52:55], v[140:143], v[84:87]\nv_cvt_pk_bf16_f32 v63, v110, v111\nv_mfma_f32_16x16x32_bf16 v[88:91], v[52:55], a[8:11], v[88:91]\n"
"v_mfma_f32_16x16x32_bf16 v[92:95], v[52:55], a[12:15], v[92:95]\nds_read_b128 v[52:55], v35 offset:4096\ns_waitcnt lgkmcnt(3)\nv_mfma_f32_16x16x32_bf16 v[64:67], v[40:43], v[144:147], v[64:67]\n"
"buffer_store_dwordx4 v[60:63], v16, s[20:23], s50 offen sc1\nv_mfma_f32_16x16x32_bf16 v[68:71], v[40:43], v[148:151], v[68:71]\ns_add_u32 s50, s50, s36\nv_mfma_f32_16x16x32_bf16 v[72:75], v[40:43], a[16:19], v[72:75]\n"
"s_add_u32 m0, s52, 0\nv_mfma_f32_16x16x32_bf16 v[76:79], v[40:43], a[20:23], v[76:79]\nds_read_b128 v[40:43], v32 offset:8192\ns_add_u32 s53, s49, 0\n"
"s_waitcnt lgkmcnt(3)\nv_mfma_f32_16x16x32_bf16 v[80:83], v[44:47], v[144:147], v[80:83]\nbuffer_load_dwordx4 v10, s[12:15], s53 offen lds\nv_mfma_f32_16x16x32_bf16 v[84:87], v[44:47], v[148:151], v[84:87]\n"
"v_mfma_f32_16x16x32_bf16 v[88:91], v[44:47], a[16:19], v[88:91]\nv_mfma_f32_16x16x32_bf16 v[92:95], v[44:47], a[20:23], v[92:95]\nds_read_b128 v[44:47], v33 offset:8192\ns_waitcnt lgkmcnt(3)\n"
"v_mfma_f32_16x16x32_bf16 v[64:67], v[48:51], v[152:155], v[64:67]\nv_mfma_f32_16x16x32_bf16 v[68:71], v[48:51], v[156:159], v[68:71]\nv_mfma_f32_16x16x32_bf16 v[72:75], v[48:51], a[24:27], v[72:75]\nv_mfma_f32_16x16x32_bf16 v[76:79], v[48:51], a[28:31], v[76:79]\n"
"ds_read_b128 v[48:51], v34 offset:8192\ns_waitcnt lgkmcnt(3)\nv_mfma_f32_16x16x32_bf16 v[80:83], v[52:55], v[152:155], v[80:83]\nv_mfma_f32_16x16x32_bf16 v[84:87], v[52:55], v[156:159], v[84:87]\n"
"s_add_u32 m0, s52, 4096\nv_mfma_f32_16x16x32_bf16 v[88:91], v[52:55], a[24:27], v[88:91]\ns_add_u32 s53, s49, 128\nv_mfma_f32_16x16x32_bf16 v[92:95], v[52:55], a[28:31], v[92:95]\n"
"ds_read_b128 v[52:55], v35 offset:8192\nbuffer_load_dwordx4 v10, s[12:15], s53 offen lds\ns_waitcnt lgkmcnt(3)\nv_mfma_f32_16x16x32_bf16 v[64:67], v[40:43], v[160:163], v[64:67]\n"
"v_mfma_f32_16x16x32_bf16 v[68:71], v[40:43], v[164:167], v[68:71]\nv_mfma_f32_16x16x32_bf16 v[72:75], v[40:43], a[32:35], v[72:75]\nv_mfma_f32_16x16x32_bf16 v[76:79], v[40:43], a[36:39], v[76:79]\nds_read_b128 v[40:43], v32 offset:12288\n"
"s_waitcnt lgkmcnt(3)\nv_mfma_f32_16x16x32_bf16 v[80:83], v[44:47], v[160:163], v[80:83]\nv_mfma_f32_16x16x32_bf16 v[84:87], v[44:47], v[164:167], v[84:87]\nv_mfma_f32_16x16x32_bf16 v[88:91], v[44:47], a[32:35], v[88:91]\n"
"v_mfma_f32_16x16x32_bf16 v[92:95], v[44:47], a[36:39], v[92:95]\nds_read_b128 v[44:47], v33 offset:12288\ns_waitcnt lgkmcnt(3)\nv_mfma_f32_16x16x32_bf16 v[64:67], v[48:51], v[168:171], v[64:67]\n"
"s_add_u32 m0, s52, 8192\nv_mfma_f32_16x16x32_bf16 v[68:71], v[48:51], v[172:175], v[68:71]\ns_add_u32 s53, s49, 256\nv_mfma_f32_16x16x32_bf16 v[72:75], v[48:51], a[40:43], v[72:75]\n"
"buffer_load_dwordx4 v10, s[12:15], s53 offen lds\nv_mfma_f32_16x16x32_bf16 v[76:79], v[48:51], a[44:47], v[76:79]\nds_read_b128 v[48:51], v34 offset:12288\ns_waitcnt lgkmcnt(3)\n"
"v_mfma_f32_16x16x32_bf16 v[80:83], v[52:55], v[168:171], v[80:83]\nv_mfma_f32_16x16x32_bf16 v[84:87], v[52:55], v[172:175], v[84:87]\nv_mfma_f32_16x16x32_bf16 v[88:91], v[52:55], a[40:43], v[88:91]\nv_mfma_f32_16x16x32_bf16 v[92:95], v[52:55], a[44:47], v[92:95]\n"
"ds_read_b128 v[52:55], v35 offset:12288\ns_waitcnt lgkmcnt(3)\nv_mfma_f32_16x16x32_bf16 v[64:67], v[40:43], v[176:179], v[64:67]\nv_mfma_f32_16x16x32_bf16 v[68:71], v[40:43], v[180:183], v[68:71]\n"
"v_mfma_f32_16x16x32_bf16 v[72:75], v[40:43], a[48:51], v[72:75]\nv_mfma_f32_16x16x32_bf16 v[76:79], v[40:43], a[52:55], v[76:79]\nds_read_b128 v[40:43], v32 offset:16384\ns_add_u32 m0, s52, 12288\n"
"s_waitcnt lgkmcnt(3)\nv_mfma_f32_16x16x32_bf16 v[80:83], v[44:47], v[176:179], v[80:83]\ns_add_u32 s53, s49, 384\nv_mfma_f32_16x16x32_bf16 v[84:87], v[44:47], v[180:183], v[84:87]\n"
"buffer_load_dwordx4 v10, s[12:15], s53 offen lds\nv_mfma_f32_16x16x32_bf16 v[88:91], v[44:47], a[48:51], v[88:91]\nv_mfma_f32_16x16x32_bf16 v[92:95], v[44:47], a[52:55], v[92:95]\nds_read_b128 v[44:47], v33 offset:16384\n"
"s_waitcnt lgkmcnt(3)\nv_mfma_f32_16x16x32_bf16 v[64:67], v[48:51], v[184:187], v[64:67]\nv_mfma_f32_16x16x32_bf16 v[68:71], v[48:51], v[188:191], v[68:71]\nv_mfma_f32_16x16x32_bf16 v[72:75], v[48:51], a[56:59], v[72:75]\n"
"v_mfma_f32_16x16x32_bf16 v[76:79], v[48:51], a[60:63], v[76:79]\nds_read_b128 v[48:51], v34 offset:16384\ns_waitcnt lgkmcnt(3)\nv_mfma_f32_16x16x32_bf16 v[80:83], v[52:55], v[184:187], v[80:83]\n"
"v_mfma_f32_16x16x32_bf16 v[84:87], v[52:55], v[188:191], v[84:87]\nv_mfma_f32_16x16x32_bf16 v[88:91], v[52:55], a[56:59], v[88:91]\ns_add_u32 m0, s52, 16384\nv_mfma_f32_16x16x32_bf16 v[92:95], v[52:55], a[60:63], v[92:95]\n"
"ds_read_b128 v[52:55], v35 offset:16384\ns_add_u32 s53, s49, 512\ns_waitcnt lgkmcnt(3)\nv_mfma_f32_16x16x32_bf16 v[64:67], v[40:43], v[192:195], v[64:67]\n"
"buffer_load_dwordx4 v10, s[12:15], s53 offen lds\nv_mfma_f32_16x16x32_bf16 v[68:71], v[40:43], v[196:199], v[68:71]\nv_mfma_f32_16x16x32_bf16 v[72:75], v[40:43], a[64:67], v[72:75]\nv_mfma_f32_16x16x32_bf16 v[76:79], v[40:43], a[68:71], v[76:79]\n"
"ds_read_b128 v[40:43], v32 offset:20480\ns_waitcnt lgkmcnt(3)\nv_mfma_f32_16x16x32_bf16 v[80:83], v[44:47], v[192:195], v[80:83]\nv_mfma_f32_16x16x32_bf16 v[84:87], v[44:47], v[196:199], v[84:87]\n"
"v_mfma_f32_16x16x32_bf16 v[88:91], v[44:47], a[64:67], v[88:91]\nv_mfma_f32_16x16x32_bf16 v[92:95], v[44:47], a[68:71], v[92:95]\nds_read_b128 v[44:47], v33 offset:20480\ns_waitcnt lgkmcnt(3)\n"
"v_mfma_f32_16x16x32_bf16 v[64:67], v[48:51], v[200:203], v[64:67]\nv_mfma_f32_16x16x32_bf16 v[68:71], v[48:51], v[204:207], v[68:71]\ns_add_u32 m0, s52, 20480\nv_mfma_f32_16x16x32_bf16 v[72:75], v[48:51], a[72:75], v[72:75]\n"
"s_add_u32 s53, s49, 640\nv_mfma_f32_16x16x32_bf16 v[76:79], v[48:51], a[76:79], v[76:79]\nds_read_b128 v[48:51], v34 offset:20480\nbuffer_load_dwordx4 v10, s[12:15], s53 offen lds\n"
"s_waitcnt lgkmcnt(3)\nv_mfma_f32_16x16x32_bf16 v[80:83], v[52:55], v[200:203], v[80:83]\nv_mfma_f32_16x16x32_bf16 v[84:87], v[52:55], v[204:207], v[84:87]\nv_mfma_f32_16x16x32_bf16 v[88:91], v[52:55], a[72:75], v[88:91]\n"
"v_mfma_f32_16x16x32_bf16 v[92:95], v[52:55], a[76:79], v[92:95]\nds_read_b128 v[52:55], v35 offset:20480\ns_waitcnt lgkmcnt(3)\nv_mfma_f32_16x16x32_bf16 v[64:67], v[40:43], v[208:211], v[64:67]\n"
"v_mfma_f32_16x16x32_bf16 v[68:71], v[40:43], v[212:215], v[68:71]\nv_mfma_f32_16x16x32_bf16 v[72:75], v[40:43], a[80:83], v[72:75]\nv_mfma_f32_16x16x32_bf16 v[76:79], v[40:43], a[84:87], v[76:79]\nds_read_b128 v[40:43], v32 offset:24576\n"
"s_waitcnt lgkmcnt(3)\nv_mfma_f32_16x16x32_bf16 v[80:83], v[44:47], v[208:211], v[80:83]\ns_add_u32 m0, s52, 24576\nv_mfma_f32_16x16x32_bf16 v[84:87], v[44:47], v[212:215], v[84:87]\n"
"s_add_u32 s53, s49, 768\nv_mfma_f32_16x16x32_bf16 v[88:91], v[44:47], a[80:83], v[88:91]\nbuffer_load_dwordx4 v10, s[12:15], s53 offen lds\nv_mfma_f32_16x16x32_bf16 v[92:95], v[44:47], a[84:87], v[92:95]\n"
"ds_read_b128 v[44:47], v33 offset:24576\ns_waitcnt lgkmcnt(3)\nv_mfma_f32_16x16x32_bf16 v[64:67], v[48:51], v[216:219], v[64:67]\ns_add_u32 s54, s51, 32768\n"
"s_cmp_eq_u32 s54, 163840\nv_mfma_f32_16x16x32_bf16 v[68:71], v[48:51], v[220:223], v[68:71]\nv_mfma_f32_16x16x32_bf16 v[72:75], v[48:51], a[88:91], v[72:75]\nv_mfma_f32_16x16x32_bf16 v[76:79], v[48:51], a[92:95], v[76:79]\n"
"s_cselect_b32 s54, 0, s54\ns_sub_u32 s54, s54, s51\nds_read_b128 v[48:51], v34 offset:24576\ns_waitcnt lgkmcnt(3)\n"
"v_mfma_f32_16x16x32_bf16 v[80:83], v[52:55], v[216:219], v[80:83]\nv_mfma_f32_16x16x32_bf16 v[84:87], v[52:55], v[220:223], v[84:87]\nv_mfma_f32_16x16x32_bf16 v[88:91], v[52:55], a[88:91], v[88:91]\nv_mfma_f32_16x16x32_bf16 v[92:95], v[52:55], a[92:95], v[92:95]\n"
"ds_read_b128 v[52:55], v35 offset:24576\ns_waitcnt lgkmcnt(3)\nv_mfma_f32_16x16x32_bf16 v[64:67], v[40:43], v[224:227], v[64:67]\nv_add_u32 v32, s54, v32\n"
"v_mfma_f32_16x16x32_bf16 v[68:71], v[40:43], v[228:231], v[68:71]\nv_mfma_f32_16x16x32_bf16 v[72:75], v[40:43], a[96:99], v[72:75]\nv_mfma_f32_16x16x32_bf16 v[76:79], v[40:43], a[100:103], v[76:79]\nds_read_b128 v[40:43], v32 offset:0\n"
"s_waitcnt lgkmcnt(3)\nv_mfma_f32_16x16x32_bf16 v[80:83], v[44:47], v[224:227], v[80:83]\nv_add_u32 v33, s54, v33\nv_mfma_f32_16x16x32_bf16 v[84:87], v[44:47], v[228:231], v[84:87]\n"
"v_mfma_f32_16x16x32_bf16 v[88:91], v[44:47], a[96:99], v[88:91]\nv_mfma_f32_16x16x32_bf16 v[92:95], v[44:47], a[100:103], v[92:95]\nds_read_b128 v[44:47], v33 offset:0\ns_waitcnt lgkmcnt(3)\n"
"v_mfma_f32_16x16x32_bf16 v[64:67], v[48:51], v[232:235], v[64:67]\nv_add_u32 v34, s54, v34\nv_mfma_f32_16x16x32_bf16 v[68:71], v[48:51], v[236:239], v[68:71]\nv_mfma_f32_16x16x32_bf16 v[72:75], v[48:51], a[104:107], v[72:75]\n"
"v_mfma_f32_16x16x32_bf16 v[76:79], v[48:51], a[108:111], v[76:79]\nds_read_b128 v[48:51], v34 offset:0\ns_waitcnt lgkmcnt(3)\nv_mfma_f32_16x16x32_bf16 v[80:83], v[52:55], v[232:235], v[80:83]\n"
"v_add_u32 v35, s54, v35\nv_mfma_f32_16x16x32_bf16 v[84:87], v[52:55], v[236:239], v[84:87]\nv_mfma_f32_16x16x32_bf16 v[88:91], v[52:55], a[104:107], v[88:91]\nv_mfma_f32_16x16x32_bf16 v[92:95], v[52:55], a[108:111], v[92:95]\n"
"ds_read_b128 v[52:55], v35 offset:0\ns_mov_b32 s52, s47\ns_add_u32 s47, s51, s32\ns_add_u32 s51, s51, s54\n"
"s_add_u32 s49, s49, 28672\ns_waitcnt vmcnt(7)\ns_barrier\ns_waitcnt lgkmcnt(3)\n"
"v_mfma_f32_16x16x32_bf16 v[96:99], v[40:43], v[128:131], 0\nv_mov_b32 v36, s48\ns_add_u32 s48, s48, 1\ns_nop 0\n"
"buffer_store_dword v36, v19, s[24:27], s33 offen sc1\nv_mfma_f32_16x16x32_bf16 v[100:103], v[40:43], v[132:135], 0\nv_mfma_f32_16x16x32_bf16 v[104:107], v[40:43], a[0:3], 0\nv_mfma_f32_16x16x32_bf16 v[108:111], v[40:43], a[4:7], 0\n"
"ds_read_b128 v[40:43], v32 offset:4096\nv_cvt_pk_bf16_f32 v56, v64, v65\ns_waitcnt lgkmcnt(3)\nv_mfma_f32_16x16x32_bf16 v[112:115], v[44:47], v[128:131], 0\n"
"v_cvt_pk_bf16_f32 v57, v66, v67\nv_mfma_f32_16x16x32_bf16 v[116:119], v[44:47], v[132:135], 0\nv_cvt_pk_bf16_f32 v58, v80, v81\nv_mfma_f32_16x16x32_bf16 v[120:123], v[44:47], a[0:3], 0\n"
"v_cvt_pk_bf16_f32 v59, v82, v83\nv_mfma_f32_16x16x32_bf16 v[124:127], v[44:47], a[4:7], 0\nds_read_b128 v[44:47], v33 offset:4096\ns_waitcnt lgkmcnt(3)\n"
"v_mfma_f32_16x16x32_bf16 v[96:99], v[48:51], v[136:139], v[96:99]\nv_mfma_f32_16x16x32_bf16 v[100:103], v[48:51], v[140:143], v[100:103]\nbuffer_store_dwordx4 v[56:59], v15, s[20:23], s50 offen sc1\nv_mfma_f32_16x16x32_bf16 v[104:107], v[48:51], a[8:11], v[104:107]\n"
"v_cvt_pk_bf16_f32 v60, v68, v69\nv_mfma_f32_16x16x32_bf16 v[108:111], v[48:51], a[12:15], v[108:111]\nds_read_b128 v[48:51], v34 offset:4096\nv_cvt_pk_bf16_f32 v61, v70, v71\n"
"s_waitcnt lgkmcnt(3)\nv_mfma_f32_16x16x32_bf16 v[112:115], v[52:55], v[136:139], v[112:115]\nv_cvt_pk_bf16_f32 v62, v84, v85\nv_mfma_f32_16x16x32_bf16 v[116:119], v[52:55], v[140:143], v[116:119]\n"
"v_cvt_pk_bf16_f32 v63, v86, v87\nv_mfma_f32_16x16x32_bf16 v[120:123], v[52:55], a[8:11], v[120:123]\nv_mfma_f32_16x16x32_bf16 v[124:127], v[52:55], a[12:15], v[124:127]\nds_read_b128 v[52:55], v35 offset:4096\n"
"s_waitcnt lgkmcnt(3)\nv_mfma_f32_16x16x32_bf16 v[96:99], v[40:43], v[144:147], v[96:99]\nbuffer_store_dwordx4 v[60:63], v16, s[20:23], s50 offen sc1\nv_mfma_f32_16x16x32_bf16 v[100:103], v[40:43], v[148:151], v[100:103]\n"
"v_cvt_pk_bf16_f32 v56, v72, v73\nv_mfma_f32_16x16x32_bf16 v[104:107], v[40:43], a[16:19], v[104:107]\nv_cvt_pk_bf16_f32 v57, v74, v75\nv_mfma_f32_16x16x32_bf16 v[108:111], v[40:43], a[20:23], v[108:111]\n"
"ds_read_b128 v[40:43], v32 offset:8192\nv_cvt_pk_bf16_f32 v58, v88, v89\ns_waitcnt lgkmcnt(3)\nv_mfma_f32_16x16x32_bf16 v[112:115], v[44:47], v[144:147], v[112:115]\n"
"v_cvt_pk_bf16_f32 v59, v90, v91\nv_mfma_f32_16x16x32_bf16 v[116:119], v[44:47], v[148:151], v[116:119]\nv_mfma_f32_16x16x32_bf16 v[120:123], v[44:47], a[16:19], v[120:123]\nv_mfma_f32_16x16x32_bf16 v[124:127], v[44:47], a[20:23], v[124:127]\n"
"ds_read_b128 v[44:47], v33 offset:8192\nbuffer_store_dwordx4 v[56:59], v17, s[20:23], s50 offen sc1\ns_waitcnt lgkmcnt(3)\nv_mfma_f32_16x16x32_bf16 v[96:99], v[48:51], v[152:155], v[96:99]\n"
"v_cvt_pk_bf16_f32 v60, v76, v77\nv_mfma_f32_16x16x32_bf16 v[100:103], v[48:51], v[156:159], v[100:103]\nv_cvt_pk_bf16_f32 v61, v78, v79\nv_mfma_f32_16x16x32_bf16 v[104:107], v[48:51], a[24:27], v[104:107]\n"
"v_cvt_pk_bf16_f32 v62, v92, v93\nv_mfma_f32_16x16x32_bf16 v[108:111], v[48:51], a[28:31], v[108:111]\nds_read_b128 v[48:51], v34 offset:8192\nv_cvt_pk_bf16_f32 v63, v94, v95\n"
"s_waitcnt lgkmcnt(3)\nv_mfma_f32_16x16x32_bf16 v[112:115], v[52:55], v[152:155], v[112:115]\nv_mfma_f32_16x16x32_bf16 v[116:119], v[52:55], v[156:159], v[116:119]\nv_mfma_f32_16x16x32_bf16 v[120:123], v[52:55], a[24:27], v[120:123]\n"
"buffer_store_dwordx4 v[60:63], v18, s[20:23], s50 offen sc1\nv_mfma_f32_16x16x32_bf16 v[124:127], v[52:55], a[28:31], v[124:127]\nds_read_b128 v[52:55], v35 offset:8192\ns_add_u32 s50, s50, s36\n"
"s_waitcnt lgkmcnt(3)\nv_mfma_f32_16x16x32_bf16 v[96:99], v[40:43], v[160:163], v[96:99]\ns_add_u32 m0, s52, 0\nv_mfma_f32_16x16x32_bf16 v[100:103], v[40:43], v[164:167], v[100:103]\n"
"s_add_u32 s53, s49, 0\nv_mfma_f32_16x16x32_bf16 v[104:107], v[40:43], a[32:35], v[104:107]\nbuffer_load_dwordx4 v10, s[12:15], s53 offen lds\nv_mfma_f32_16x16x32_bf16 v[108:111], v[40:43], a[36:39], v[108:111]\n"
"ds_read_b128 v[40:43], v32 offset:12288\ns_waitcnt lgkmcnt(3)\nv_mfma_f32_16x16x32_bf16 v[112:115], v[44:47], v[160:163], v[112:115]\nv_mfma_f32_16x16x32_bf16 v[116:119], v[44:47], v[164:167], v[116:119]\n"
"v_mfma_f32_16x16x32_bf16 v[120:123], v[44:47], a[32:35], v[120:123]\nv_mfma_f32_16x16x32_bf16 v[124:127], v[44:47], a[36:39], v[124:127]\nds_read_b128 v[44:47], v33 offset:12288\ns_waitcnt lgkmcnt(3)\n"
"v_mfma_f32_16x16x32_bf16 v[96:99], v[48:51], v[168:171], v[96:99]\nv_mfma_f32_16x16x32_bf16 v[100:103], v[48:51], v[172:175], v[100:103]\nv_mfma_f32_16x16x32_bf16 v[104:107], v[48:51], a[40:43], v[104:107]\nv_mfma_f32_16x16x32_bf16 v[108:111], v[48:51], a[44:47], v[108:111]\n"
"ds_read_b128 v[48:51], v34 offset:12288\ns_add_u32 m0, s52, 4096\ns_waitcnt lgkmcnt(3)\nv_mfma_f32_16x16x32_bf16 v[112:115], v[52:55], v[168:171], v[112:115]\n"
"s_add_u32 s53, s49, 128\nv_mfma_f32_16x16x32_bf16 v[116:119], v[52:55], v[172:175], v[116:119]\nbuffer_load_dwordx4 v10, s[12:15], s53 offen lds\nv_mfma_f32_16x16x32_bf16 v[120:123], v[52:55], a[40:43], v[120:123]\n"
"v_mfma_f32_16x16x32_bf16 v[124:127], v[52:55], a[44:47], v[124:127]\nds_read_b128 v[52:55], v35 offset:12288\ns_waitcnt lgkmcnt(3)\nv_mfma_f32_16x16x32_bf16 v[96:99], v[40:43], v[176:179], v[96:99]\n"
"v_mfma_f32_16x16x32_bf16 v[100:103], v[40:43], v[180:183], v[100:103]\nv_mfma_f32_16x16x32_bf16 v[104:107], v[40:43], a[48:51], v[104:107]\nv_mfma_f32_16x16x32_bf16 v[108:111], v[40:43], a[52:55], v[108:111]\nds_read_b128 v[40:43], v32 offset:16384\n"
"s_waitcnt lgkmcnt(3)\nv_mfma_f32_16x16x32_bf16 v[112:115], v[44:47], v[176:179], v[112:115]\nv_mfma_f32_16x16x32_bf16 v[116:119], v[44:47], v[180:183], v[116:119]\nv_mfma_f32_16x16x32_bf16 v[120:123], v[44:47], a[48:51], v[120:123]\n"
"s_add_u32 m0, s52, 8192\nv_mfma_f32_16x16x32_bf16 v[124:127], v[44:47], a[52:55], v[124:127]\nds_read_b128 v[44:47], v33 offset:16384\ns_add_u32 s53, s49, 256\n"
"s_waitcnt lgkmcnt(3)\nv_mfma_f32_16x16x32_bf16 v[96:99], v[48:51], v[184:187], v[96:99]\nbuffer_load_dwordx4 v10, s[12:15], s53 offen lds\nv_mfma_f32_16x16x32_bf16 v[100:103], v[48:51], v[188:191], v[100:103]\n"
"v_mfma_f32_16x16x32_bf16 v[104:107], v[48:51], a[56:59], v[104:107]\nv_mfma_f32_16x16x32_bf16 v[108:111], v[48:51], a[60:63], v[108:111]\nds_read_b128 v[48:51], v34 offset:16384\ns_waitcnt lgkmcnt(3)\n"
"v_mfma_f32_16x16x32_bf16 v[112:115], v[52:55], v[184:187], v[112:115]\nv_mfma_f32_16x16x32_bf16 v[116:119], v[52:55], v[188:191], v[116:119]\nv_mfma_f32_16x16x32_bf16 v[120:123], v[52:55], a[56:59], v[120:123]\nv_mfma_f32_16x16x32_bf16 v[124:127], v[52:55], a[60:63], v[124:127]\n"
"ds_read_b128 v[52:55], v35 offset:16384\ns_waitcnt lgkmcnt(3)\nv_mfma_f32_16x16x32_bf16 v[96:99], v[40:43], v[192:195], v[96:99]\nv_mfma_f32_16x16x32_bf16 v[100:103], v[40:43], v[196:199], v[100:103]\n"
"s_add_u32 m0, s52, 12288\nv_mfma_f32_16x16x32_bf16 v[104:107], v[40:43], a[64:67], v[104:107]\ns_add_u32 s53, s49, 384\nv_mfma_f32_16x16x32_bf16 v[108:111], v[40:43], a[68:71], v[108:111]\n"
"ds_read_b128 v[40:43], v32 offset:20480\nbuffer_load_dwordx4 v10, s[12:15], s53 offen lds\ns_waitcnt lgkmcnt(3)\nv_mfma_f32_16x16x32_bf16 v[112:115], v[44:47], v[192:195], v[112:115]\n"
"v_mfma_f32_16x16x32_bf16 v[116:119], v[44:47], v[196:199], v[116:119]\nv_mfma_f32_16x16x32_bf16 v[120:123], v[44:47], a[64:67], v[120:123]\nv_mfma_f32_16x16x32_bf16 v[124:127], v[44:47], a[68:71], v[124:127]\nds_read_b128 v[44:47], v33 offset:20480\n"
"s_waitcnt lgkmcnt(3)\nv_mfma_f32_16x16x32_bf16 v[96:99], v[48:51], v[200:203], v[96:99]\nv_mfma_f32_16x16x32_bf16 v[100:103], v[48:51], v[204:207], v[100:103]\nv_mfma_f32_16x16x32_bf16 v[104:107], v[48:51], a[72:75], v[104:107]\n"
"v_mfma_f32_16x16x32_bf16 v[108:111], v[48:51], a[76:79], v[108:111]\nds_read_b128 v[48:51], v34 offset:20480\ns_waitcnt lgkmcnt(3)\nv_mfma_f32_16x16x32_bf16 v[112:115], v[52:55], v[200:203], v[112:115]\n"
"s_add_u32 m0, s52, 16384\nv_mfma_f32_16x16x32_bf16 v[116:119], v[52:55], v[204:207], v[116:119]\ns_add_u32 s53, s49, 512\nv_mfma_f32_16x16x32_bf16 v[120:123], v[52:55], a[72:75], v[120:123]\n"
"buffer_load_dwordx4 v10, s[12:15], s53 offen lds\nv_mfma_f32_16x16x32_bf16 v[124:127], v[52:55], a[76:79], v[124:127]\nds_read_b128 v[52:55], v35 offset:20480\ns_waitcnt lgkmcnt(3)\n"
"v_mfma_f32_16x16x32_bf16 v[96:99], v[40:43], v[208:211], v[96:99]\nv_mfma_f32_16x16x32_bf16 v[100:103], v[40:43], v[212:215], v[100:103]\nv_mfma_f32_16x16x32_bf16 v[104:107], v[40:43], a[80:83], v[104:107]\nv_mfma_f32_16x16x32_bf16 v[108:111], v[40:43], a[84:87], v[108:111]\n"
"ds_read_b128 v[40:43], v32 offset:24576\ns_waitcnt lgkmcnt(3)\nv_mfma_f32_16x16x32_bf16 v[112:115], v[44:47], v[208:211], v[112:115]\nv_mfma_f32_16x16x32_bf16 v[116:119], v[44:47], v[212:215], v[116:119]\n"
"v_mfma_f32_16x16x32_bf16 v[120:123], v[44:47], a[80:83], v[120:123]\nv_mfma_f32_16x16x32_bf16 v[124:127], v[44:47], a[84:87], v[124:127]\nds_read_b128 v[44:47], v33 offset:24576\ns_add_u32 m0, s52, 20480\n"
"s_waitcnt lgkmcnt(3)\nv_mfma_f32_16x16x32_bf16 v[96:99], v[48:51], v[216:219], v[96:99]\ns_add_u32 s54, s51, 32768\ns_cmp_eq_u32 s54, 163840\n"
"v_mfma_f32_16x16x32_bf16 v[100:103], v[48:51], v[220:223], v[100:103]\nv_mfma_f32_16x16x32_bf16 v[104:107], v[48:51], a[88:91], v[104:107]\nv_mfma_f32_16x16x32_bf16 v[108:111], v[48:51], a[92:95], v[108:111]\ns_cselect_b32 s54, 0, s54\n"
"s_sub_u32 s54, s54, s51\nds_read_b128 v[48:51], v34 offset:24576\ns_add_u32 s53, s49, 640\ns_waitcnt lgkmcnt(3)\n"
"v_mfma_f32_16x16x32_bf16 v[112:115], v[52:55], v[216:219], v[112:115]\nbuffer_load_dwordx4 v10, s[12:15], s53 offen lds\nv_mfma_f32_16x16x32_bf16 v[116:119], v[52:55], v[220:223], v[116:119]\nv_mfma_f32_16x16x32_bf16 v[120:123], v[52:55], a[88:91], v[120:123]\n"
"v_mfma_f32_16x16x32_bf16 v[124:127], v[52:55], a[92:95], v[124:127]\nds_read_b128 v[52:55], v35 offset:24576\ns_waitcnt lgkmcnt(3)\nv_mfma_f32_16x16x32_bf16 v[96:99], v[40:43], v[224:227], v[96:99]\n"
"v_add_u32 v32, s54, v32\nv_mfma_f32_16x16x32_bf16 v[100:103], v[40:43], v[228:231], v[100:103]\nv_mfma_f32_16x16x32_bf16 v[104:107], v[40:43], a[96:99], v[104:107]\nv_mfma_f32_16x16x32_bf16 v[108:111], v[40:43], a[100:103], v[108:111]\n"
"ds_read_b128 v[40:43], v32 offset:0\ns_waitcnt lgkmcnt(3)\nv_mfma_f32_16x16x32_bf16 v[112:115], v[44:47], v[224:227], v[112:115]\nv_add_u32 v33, s54, v33\n"
"v_mfma_f32_16x16x32_bf16 v[116:119], v[44:47], v[228:231], v[116:119]\ns_add_u32 m0, s52, 24576\nv_mfma_f32_16x16x32_bf16 v[120:123], v[44:47], a[96:99], v[120:123]\ns_add_u32 s53, s49, 768\n"
"v_mfma_f32_16x16x32_bf16 v[124:127], v[44:47], a[100:103], v[124:127]\nds_read_b128 v[44:47], v33 offset:0\nbuffer_load_dwordx4 v10, s[12:15], s53 offen lds\ns_waitcnt lgkmcnt(3)\n"
"v_mfma_f32_16x16x32_bf16 v[96:99], v[48:51], v[232:235], v[96:99]\nv_add_u32 v34, s54, v34\nv_mfma_f32_16x16x32_bf16 v[100:103], v[48:51], v[236:239], v[100:103]\nv_mfma_f32_16x16x32_bf16 v[104:107], v[48:51], a[104:107], v[104:107]\n"
"v_mfma_f32_16x16x32_bf16 v[108:111], v[48:51], a[108:111], v[108:111]\nds_read_b128 v[48:51], v34 offset:0\ns_waitcnt lgkmcnt(3)\nv_mfma_f32_16x16x32_bf16 v[112:115], v[52:55], v[232:235], v[112:115]\n"
"v_add_u32 v35, s54, v35\nv_mfma_f32_16x16x32_bf16 v[116:119], v[52:55], v[236:239], v[116:119]\nv_mfma_f32_16x16x32_bf16 v[120:123], v[52:55], a[104:107], v[120:123]\nv_mfma_f32_16x16x32_bf16 v[124:127], v[52:55], a[108:111], v[124:127]\n"
"ds_read_b128 v[52:55], v35 offset:0\ns_mov_b32 s52, s47\ns_add_u32 s47, s51, s32\ns_add_u32 s51, s51, s54\n"
"s_add_u32 s49, s49, 28672\n.p2align 6\nfg1_loop:\ns_waitcnt vmcnt(7)\n"
"s_barrier\ns_waitcnt lgkmcnt(3)\nv_mfma_f32_16x16x32_bf16 v[64:67], v[40:43], v[128:131], 0\nv_mov_b32 v36, s48\n"
"s_add_u32 s48, s48, 1\ns_nop 0\nbuffer_store_dword v36, v19, s[24:27], s33 offen sc1\nv_mfma_f32_16x16x32_bf16 v[68:71], v[40:43], v[132:135], 0\n"
"v_mfma_f32_16x16x32_bf16 v[72:75], v[40:43], a[0:3], 0\nv_mfma_f32_16x16x32_bf16 v[76:79], v[40:43], a[4:7], 0\nds_read_b128 v[40:43], v32 offset:4096\nv_cvt_pk_bf16_f32 v56, v96, v97\n"
"s_waitcnt lgkmcnt(3)\nv_mfma_f32_16x16x32_bf16 v[80:83], v[44:47], v[128:131], 0\nv_cvt_pk_bf16_f32 v57, v98, v99\nv_mfma_f32_16x16x32_bf16 v[84:87], v[44:47], v[132:135], 0\n"
"v_cvt_pk_bf16_f32 v58, v112, v113\nv_mfma_f32_16x16x32_bf16 v[88:91], v[44:47], a[0:3], 0\nv_cvt_pk_bf16_f32 v59, v114, v115\nv_mfma_f32_16x16x32_bf16 v[92:95], v[44:47], a[4:7], 0\n"
"ds_read_b128 v[44:47], v33 offset:4096\ns_waitcnt lgkmcnt(3)\nv_mfma_f32_16x16x32_bf16 v[64:67], v[48:51], v[136:139], v[64:67]\nv_mfma_f32_16x16x32_bf16 v[68:71], v[48:51], v[140:143], v[68:71]\n"
"buffer_store_dwordx4 v[56:59], v15, s[20:23], s50 offen sc1\nv_mfma_f32_16x16x32_bf16 v[72:75], v[48:51], a[8:11], v[72:75]\nv_cvt_pk_bf16_f32 v60, v100, v101\nv_mfma_f32_16x16x32_bf16 v[76:79], v[48:51], a[12:15], v[76:79]\n"
"ds_read_b128 v[48:51], v34 offset:4096\nv_cvt_pk_bf16_f32 v61, v102, v103\ns_waitcnt lgkmcnt(3)\nv_mfma_f32_16x16x32_bf16 v[80:83], v[52:55], v[136:139], v[80:83]\n"
"v_cvt_pk_bf16_f32 v62, v116, v117\nv_mfma_f32_16x16x32_bf16 v[84:87], v[52:55], v[140:143], v[84:87]\nv_cvt_pk_bf16_f32 v63, v118, v119\nv_mfma_f32_16x16x32_bf16 v[88:91], v[52:55], a[8:11], v[88:91]\n"
"v_mfma_f32_16x16x32_bf16 v[92:95], v[52:55], a[12:15], v[92:95]\nds_read_b128 v[52:55], v35 offset:4096\ns_waitcnt lgkmcnt(3)\nv_mfma_f32_16x16x32_bf16 v[64:67], v[40:43], v[144:147], v[64:67]\n"
"buffer_store_dwordx4 v[60:63], v16, s[20:23], s50 offen sc1\nv_mfma_f32_16x16x32_bf16 v[68:71], v[40:43], v[148:151], v[68:71]\nv_cvt_pk_bf16_f32 v56, v104, v105\nv_mfma_f32_16x16x32_bf16 v[72:75], v[40:43], a[16:19], v[72:75]\n"
"v_cvt_pk_bf16_f32 v57, v106, v107\nv_mfma_f32_16x16x32_bf16 v[76:79], v[40:43], a[20:23], v[76:79]\nds_read_b128 v[40:43], v32 offset:8192\nv_cvt_pk_bf16_f32 v58, v120, v121\n"
"s_waitcnt lgkmcnt(3)\nv_mfma_f32_16x16x32_bf16 v[80:83], v[44:47], v[144:147], v[80:83]\nv_cvt_pk_bf16_f32 v59, v122, v123\nv_mfma_f32_16x16x32_bf16 v[84:87], v[44:47], v[148:151], v[84:87]\n"
"v_mfma_f32_16x16x32_bf16 v[88:91], v[44:47], a[16:19], v[88:91]\nv_mfma_f32_16x16x32_bf16 v[92:95], v[44:47], a[20:23], v[92:95]\nds_read_b128 v[44:47], v33 offset:8192\nbuffer_store_dwordx4 v[56:59], v17, s[20:23], s50 offen sc1\n"
"s_waitcnt lgkmcnt(3)\nv_mfma_f32_16x16x32_bf16 v[64:67], v[48:51], v[152:155], v[64:67]\nv_cvt_pk_bf16_f32 v60, v108, v109\nv_mfma_f32_16x16x32_bf16 v[68:71], v[48:51], v[156:159], v[68:71]\n"
"v_cvt_pk_bf16_f32 v61, v110, v111\nv_mfma_f32_16x16x32_bf16 v[72:75], v[48:51], a[24:27], v[72:75]\nv_cvt_pk_bf16_f32 v62, v124, v125\nv_mfma_f32_16x16x32_bf16 v[76:79], v[48:51], a[28:31], v[76:79]\n"
"ds_read_b128 v[48:51], v34 offset:8192\nv_cvt_pk_bf16_f32 v63, v126, v127\ns_waitcnt lgkmcnt(3)\nv_mfma_f32_16x16x32_bf16 v[80:83], v[52:55], v[152:155], v[80:83]\n"
"v_mfma_f32_16x16x32_bf16 v[84:87], v[52:55], v[156:159], v[84:87]\nv_mfma_f32_16x16x32_bf16 v[88:91], v[52:55], a[24:27], v[88:91]\nbuffer_store_dwordx4 v[60:63], v18, s[20:23], s50 offen sc1\nv_mfma_f32_16x16x32_bf16 v[92:95], v[52:55], a[28:31], v[92:95]\n"
"ds_read_b128 v[52:55], v35 offset:8192\ns_add_u32 s50, s50, s36\ns_waitcnt lgkmcnt(3)\nv_mfma_f32_16x16x32_bf16 v[64:67], v[40:43], v[160:163], v[64:67]\n"
"s_add_u32 m0, s52, 0\nv_mfma_f32_16x16x32_bf16 v[68:71], v[40:43], v[164:167], v[68:71]\ns_add_u32 s53, s49, 0\nv_mfma_f32_16x16x32_bf16 v[72:75], v[40:43], a[32:35], v[72:75]\n"
"buffer_load_dwordx4 v10, s[12:15], s53 offen lds\nv_mfma_f32_16x16x32_bf16 v[76:79], v[40:43], a[36:39], v[76:79]\nds_read_b128 v[40:43], v32 offset:12288\ns_waitcnt lgkmcnt(3)\n"
"v_mfma_f32_16x16x32_bf16 v[80:83], v[44:47], v[160:163], v[80:83]\nv_mfma_f32_16x16x32_bf16 v[84:87], v[44:47], v[164:167], v[84:87]\nv_mfma_f32_16x16x32_bf16 v[88:91], v[44:47], a[32:35], v[88:91]\nv_mfma_f32_16x16x32_bf16 v[92:95], v[44:47], a[36:39], v[92:95]\n"
"ds_read_b128 v[44:47], v33 offset:12288\ns_waitcnt lgkmcnt(3)\nv_mfma_f32_16x16x32_bf16 v[64:67], v[48:51], v[168:171], v[64:67]\nv_mfma_f32_16x16x32_bf16 v[68:71], v[48:51], v[172:175], v[68:71]\n"
"v_mfma_f32_16x16x32_bf16 v[72:75], v[48:51], a[40:43], v[72:75]\nv_mfma_f32_16x16x32_bf16 v[76:79], v[48:51], a[44:47], v[76:79]\nds_read_b128 v[48:51], v34 offset:12288\ns_add_u32 m0, s52, 4096\n"
"s_waitcnt lgkmcnt(3)\nv_mfma_f32_16x16x32_bf16 v[80:83], v[52:55], v[168:171], v[80:83]\ns_add_u32 s53, s49, 128\nv_mfma_f32_16x16x32_bf16 v[84:87], v[52:55], v[172:175], v[84:87]\n"
"buffer_load_dwordx4 v10, s[12:15], s53 offen lds\nv_mfma_f32_16x16x32_bf16 v[88:91], v[52:55], a[40:43], v[88:91]\nv_mfma_f32_16x16x32_bf16 v[92:95], v[52:55], a[44:47], v[92:95]\nds_read_b128 v[52:55], v35 offset:12288\n"
"s_waitcnt lgkmcnt(3)\nv_mfma_f32_16x16x32_bf16 v[64:67], v[40:43], v[176:179], v[64:67]\nv_mfma_f32_16x16x32_bf16 v[68:71], v[40:43], v[180:183], v[68:71]\nv_mfma_f32_16x16x32_bf16 v[72:75], v[40:43], a[48:51], v[72:75]\n"
"v_mfma_f32_16x16x32_bf16 v[76:79], v[40:43], a[52:55], v[76:79]\nds_read_b128 v[40:43], v32 offset:16384\ns_waitcnt lgkmcnt(3)\nv_mfma_f32_16x16x32_bf16 v[80:83], v[44:47], v[176:179], v[80:83]\n"
"v_mfma_f32_16x16x32_bf16 v[84:87], v[44:47], v[180:183], v[84:87]\nv_mfma_f32_16x16x32_bf16 v[88:91], v[44:47], a[48:51], v[88:91]\ns_add_u32 m0, s52, 8192\nv_mfma_f32_16x16x32_bf16 v[92:95], v[44:47], a[52:55], v[92:95]\n"
"ds_read_b128 v[44:47], v33 offset:16384\ns_add_u32 s53, s49, 256\ns_waitcnt lgkmcnt(3)\nv_mfma_f32_16x16x32_bf16 v[64:67], v[48:51], v[184:187], v[64:67]\n"
"buffer_load_dwordx4 v10, s[12:15], s53 offen lds\nv_mfma_f32_16x16x32_bf16 v[68:71], v[48:51], v[188:191], v[68:71]\nv_mfma_f32_16x16x32_bf16 v[72:75], v[48:51], a[56:59], v[72:75]\nv_mfma_f32_16x16x32_bf16 v[76:79], v[48:51], a[60:63], v[76:79]\n"
"ds_read_b128 v[48:51], v34 offset:16384\ns_waitcnt lgkmcnt(3)\nv_mfma_f32_16x16x32_bf16 v[80:83], v[52:55], v[184:187], v[80:83]\nv_mfma_f32_16x16x32_bf16 v[84:87], v[52:55], v[188:191], v[84:87]\n"
"v_mfma_f32_16x16x32_bf16 v[88:91], v[52:55], a[56:59], v[88:91]\nv_mfma_f32_16x16x32_bf16 v[92:95], v[52:55], a[60:63], v[92:95]\nds_read_b128 v[52:55], v35 offset:16384\ns_waitcnt lgkmcnt(3)\n"
"v_mfma_f32_16x16x32_bf16 v[64:67], v[40:43], v[192:195], v[64:67]\nv_mfma_f32_16x16x32_bf16 v[68:71], v[40:43], v[196:199], v[68:71]\ns_add_u32 m0, s52, 12288\nv_mfma_f32_16x16x32_bf16 v[72:75], v[40:43], a[64:67], v[72:75]\n"
"s_add_u32 s53, s49, 384\nv_mfma_f32_16x16x32_bf16 v[76:79], v[40:43], a[68:71], v[76:79]\nds_read_b128 v[40:43], v32 offset:20480\nbuffer_load_dwordx4 v10, s[12:15], s53 offen lds\n"
"s_waitcnt lgkmcnt(3)\nv_mfma_f32_16x16x32_bf16 v[80:83], v[44:47], v[192:195], v[80:83]\nv_mfma_f32_16x16x32_bf16 v[84:87], v[44:47], v[196:199], v[84:87]\nv_mfma_f32_16x16x32_bf16 v[88:91], v[44:47], a[64:67], v[88:91]\n"
"v_mfma_f32_16x16x32_bf16 v[92:95], v[44:47], a[68:71], v[92:95]\nds_read_b128 v[44:47], v33 offset:20480\ns_waitcnt lgkmcnt(3)\nv_mfma_f32_16x16x32_bf16 v[64:67], v[48:51], v[200:203], v[64:67]\n"
"v_mfma_f32_16x16x32_bf16 v[68:71], v[48:51], v[204:207], v[68:71]\nv_mfma_f32_16x16x32_bf16 v[72:75], v[48:51], a[72:75], v[72:75]\nv_mfma_f32_16x16x32_bf16 v[76:79], v[48:51], a[76:79], v[76:79]\nds_read_b128 v[48:51], v34 offset:20480\n"
"s_waitcnt lgkmcnt(3)\nv_mfma_f32_16x16x32_bf16 v[80:83], v[52:55], v[200:203], v[80:83]\ns_add_u32 m0, s52, 16384\nv_mfma_f32_16x16x32_bf16 v[84:87], v[52:55], v[204:207], v[84:87]\n"
"s_add_u32 s53, s49, 512\nv_mfma_f32_16x16x32_bf16 v[88:91], v[52:55], a[72:75], v[88:91]\nbuffer_load_dwordx4 v10, s[12:15], s53 offen lds\nv_mfma_f32_16x16x32_bf16 v[92:95], v[52:55], a[76:79], v[92:95]\n"
"ds_read_b128 v[52:55], v35 offset:20480\ns_waitcnt lgkmcnt(3)\nv_mfma_f32_16x16x32_bf16 v[64:67], v[40:43], v[208:211], v[64:67]\nv_mfma_f32_16x16x32_bf16 v[68:71], v[40:43], v[212:215], v[68:71]\n"
"v_mfma_f32_16x16x32_bf16 v[72:75], v[40:43], a[80:83], v[72:75]\nv_mfma_f32_16x16x32_bf16 v[76:79], v[40:43], a[84:87], v[76:79]\nds_read_b128 v[40:43], v32 offset:24576\ns_waitcnt lgkmcnt(3)\n"
"v_mfma_f32_16x16x32_bf16 v[80:83], v[44:47], v[208:211], v[80:83]\nv_mfma_f32_16x16x32_bf16 v[84:87], v[44:47], v[212:215], v[84:87]\nv_mfma_f32_16x16x32_bf16 v[88:91], v[44:47], a[80:83], v[88:91]\nv_mfma_f32_16x16x32_bf16 v[92:95], v[44:47], a[84:87], v[92:95]\n"
"ds_read_b128 v[44:47], v33 offset:24576\ns_add_u32 m0, s52, 20480\ns_waitcnt lgkmcnt(3)\nv_mfma_f32_16x16x32_bf16 v[64:67], v[48:51], v[216:219], v[64:67]\n"
"s_add_u32 s54, s51, 32768\ns_cmp_eq_u32 s54, 163840\nv_mfma_f32_16x16x32_bf16 v[68:71], v[48:51], v[220:223], v[68:71]\nv_mfma_f32_16x16x32_bf16 v[72:75], v[48:51], a[88:91], v[72:75]\n"
"v_mfma_f32_16x16x32_bf16 v[76:79], v[48:51], a[92:95], v[76:79]\ns_cselect_b32 s54, 0, s54\ns_sub_u32 s54, s54, s51\nds_read_b128 v[48:51], v34 offset:24576\n"
"s_add_u32 s53, s49, 640\ns_waitcnt lgkmcnt(3)\nv_mfma_f32_16x16x32_bf16 v[80:83], v[52:55], v[216:219], v[80:83]\nbuffer_load_dwordx4 v10, s[12:15], s53 offen lds\n"
"v_mfma_f32_16x16x32_bf16 v[84:87], v[52:55], v[220:223], v[84:87]\nv_mfma_f32_16x16x32_bf16 v[88:91], v[52:55], a[88:91], v[88:91]\nv_mfma_f32_16x16x32_bf16 v[92:95], v[52:55], a[92:95], v[92:95]\nds_read_b128 v[52:55], v35 offset:24576\n"
"s_waitcnt lgkmcnt(3)\nv_mfma_f32_16x16x32_bf16 v[64:67], v[40:43], v[224:227], v[64:67]\nv_add_u32 v32, s54, v32\nv_mfma_f32_16x16x32_bf16 v[68:71], v[40:43], v[228:231], v[68:71]\n"
"v_mfma_f32_16x16x32_bf16 v[72:75], v[40:43], a[96:99], v[72:75]\nv_mfma_f32_16x16x32_bf16 v[76:79], v[40:43], a[100:103], v[76:79]\nds_read_b128 v[40:43], v32 offset:0\ns_waitcnt lgkmcnt(3)\n"
"v_mfma_f32_16x16x32_bf16 v[80:83], v[44:47], v[224:227], v[80:83]\nv_add_u32 v33, s54, v33\nv_mfma_f32_16x16x32_bf16 v[84:87], v[44:47], v[228:231], v[84:87]\ns_add_u32 m0, s52, 24576\n"
"v_mfma_f32_16x16x32_bf16 v[88:91], v[44:47], a[96:99], v[88:91]\ns_add_u32 s53, s49, 768\nv_mfma_f32_16x16x32_bf16 v[92:95], v[44:47], a[100:103], v[92:95]\nds_read_b128 v[44:47], v33 offset:0\n"
"buffer_load_dwordx4 v10, s[12:15], s53 offen lds\ns_waitcnt lgkmcnt(3)\nv_mfma_f32_16x16x32_bf16 v[64:67], v[48:51], v[232:235], v[64:67]\nv_add_u32 v34, s54, v34\n"
"v_mfma_f32_16x16x32_bf16 v[68:71], v[48:51], v[236:239], v[68:71]\nv_mfma_f32_16x16x32_bf16 v[72:75], v[48:51], a[104:107], v[72:75]\nv_mfma_f32_16x16x32_bf16 v[76:79], v[48:51], a[108:111], v[76:79]\nds_read_b128 v[48:51], v34 offset:0\n"
"s_waitcnt lgkmcnt(3)\nv_mfma_f32_16x16x32_bf16 v[80:83], v[52:55], v[232:235], v[80:83]\nv_add_u32 v35, s54, v35\nv_mfma_f32_16x16x32_bf16 v[84:87], v[52:55], v[236:239], v[84:87]\n"
"v_mfma_f32_16x16x32_bf16 v[88:91], v[52:55], a[104:107], v[88:91]\nv_mfma_f32_16x16x32_bf16 v[92:95], v[52:55], a[108:111], v[92:95]\nds_read_b128 v[52:55], v35 offset:0\ns_mov_b32 s52, s47\n"
"s_add_u32 s47, s51, s32\ns_add_u32 s51, s51, s54\ns_add_u32 s49, s49, 28672\ns_waitcnt vmcnt(7)\n"
"s_barrier\ns_waitcnt lgkmcnt(3)\nv_mfma_f32_16x16x32_bf16 v[96:99], v[40:43], v[128:131], 0\ns_add_u32 s48, s48, 1\n"
"v_mfma_f32_16x16x32_bf16 v[100:103], v[40:43], v[132:135], 0\nv_mfma_f32_16x16x32_bf16 v[104:107], v[40:43], a[0:3], 0\nv_mfma_f32_16x16x32_bf16 v[108:111], v[40:43], a[4:7], 0\nds_read_b128 v[40:43], v32 offset:4096\n"
"v_cvt_pk_bf16_f32 v56, v64, v65\ns_waitcnt lgkmcnt(3)\nv_mfma_f32_16x16x32_bf16 v[112:115], v[44:47], v[128:131], 0\nv_cvt_pk_bf16_f32 v57, v66, v67\n"
"v_mfma_f32_16x16x32_bf16 v[116:119], v[44:47], v[132:135], 0\nv_cvt_pk_bf16_f32 v58, v80, v81\nv_mfma_f32_16x16x32_bf16 v[120:123], v[44:47], a[0:3], 0\nv_cvt_pk_bf16_f32 v59, v82, v83\n"
"v_mfma_f32_16x16x32_bf16 v[124:127], v[44:47], a[4:7], 0\nds_read_b128 v[44:47], v33 offset:4096\ns_waitcnt lgkmcnt(3)\nv_mfma_f32_16x16x32_bf16 v[96:99], v[48:51], v[136:139], v[96:99]\n"
"v_mfma_f32_16x16x32_bf16 v[100:103], v[48:51], v[140:143], v[100:103]\nbuffer_store_dwordx4 v[56:59], v15, s[20:23], s50 offen sc1\nv_mfma_f32_16x16x32_bf16 v[104:107], v[48:51], a[8:11], v[104:107]\nv_cvt_pk_bf16_f32 v60, v68, v69\n"
"v_mfma_f32_16x16x32_bf16 v[108:111], v[48:51], a[12:15], v[108:111]\nds_read_b128 v[48:51], v34 offset:4096\nv_cvt_pk_bf16_f32 v61, v70, v71\ns_waitcnt lgkmcnt(3)\n"
"v_mfma_f32_16x16x32_bf16 v[112:115], v[52:55], v[136:139], v[112:115]\nv_cvt_pk_bf16_f32 v62, v84, v85\nv_mfma_f32_16x16x32_bf16 v[116:119], v[52:55], v[140:143], v[116:119]\nv_cvt_pk_bf16_f32 v63, v86, v87\n"
"v_mfma_f32_16x16x32_bf16 v[120:123], v[52:55], a[8:11], v[120:123]\nv_mfma_f32_16x16x32_bf16 v[124:127], v[52:55], a[12:15], v[124:127]\nds_read_b128 v[52:55], v35 offset:4096\ns_waitcnt lgkmcnt(3)\n"
"v_mfma_f32_16x16x32_bf16 v[96:99], v[40:43], v[144:147], v[96:99]\nbuffer_store_dwordx4 v[60:63], v16, s[20:23], s50 offen sc1\nv_mfma_f32_16x16x32_bf16 v[100:103], v[40:43], v[148:151], v[100:103]\nv_cvt_pk_bf16_f32 v56, v72, v73\n"
"v_mfma_f32_16x16x32_bf16 v[104:107], v[40:43], a[16:19], v[104:107]\nv_cvt_pk_bf16_f32 v57, v74, v75\nv_mfma_f32_16x16x32_bf16 v[108:111], v[40:43], a[20:23], v[108:111]\nds_read_b128 v[40:43], v32 offset:8192\n"
"v_cvt_pk_bf16_f32 v58, v88, v89\ns_waitcnt lgkmcnt(3)\nv_mfma_f32_16x16x32_bf16 v[112:115], v[44:47], v[144:147], v[112:115]\nv_cvt_pk_bf16_f32 v59, v90, v91\n"
"v_mfma_f32_16x16x32_bf16 v[116:119], v[44:47], v[148:151], v[116:119]\nv_mfma_f32_16x16x32_bf16 v[120:123], v[44:47], a[16:19], v[120:123]\nv_mfma_f32_16x16x32_bf16 v[124:127], v[44:47], a[20:23], v[124:127]\nds_read_b128 v[44:47], v33 offset:8192\n"
"buffer_store_dwordx4 v[56:59], v17, s[20:23], s50 offen sc1\ns_waitcnt lgkmcnt(3)\nv_mfma_f32_16x16x32_bf16 v[96:99], v[48:51], v[152:155], v[96:99]\nv_cvt_pk_bf16_f32 v60, v76, v77\n"
"v_mfma_f32_16x16x32_bf16 v[100:103], v[48:51], v[156:159], v[100:103]\nv_cvt_pk_bf16_f32 v61, v78, v79\nv_mfma_f32_16x16x32_bf16 v[104:107], v[48:51], a[24:27], v[104:107]\nv_cvt_pk_bf16_f32 v62, v92, v93\n"
"v_mfma_f32_16x16x32_bf16 v[108:111], v[48:51], a[28:31], v[108:111]\nds_read_b128 v[48:51], v34 offset:8192\nv_cvt_pk_bf16_f32 v63, v94, v95\ns_waitcnt lgkmcnt(3)\n"
"v_mfma_f32_16x16x32_bf16 v[112:115], v[52:55], v[152:155], v[112:115]\nv_mfma_f32_16x16x32_bf16 v[116:119], v[52:55], v[156:159], v[116:119]\nv_mfma_f32_16x16x32_bf16 v[120:123], v[52:55], a[24:27], v[120:123]\nbuffer_store_dwordx4 v[60:63], v18, s[20:23], s50 offen sc1\n"
"v_mfma_f32_16x16x32_bf16 v[124:127], v[52:55], a[28:31], v[124:127]\nds_read_b128 v[52:55], v35 offset:8192\ns_add_u32 s50, s50, s36\ns_waitcnt lgkmcnt(3)\n"
"v_mfma_f32_16x16x32_bf16 v[96:99], v[40:43], v[160:163], v[96:99]\ns_add_u32 m0, s52, 0\nv_mfma_f32_16x16x32_bf16 v[100:103], v[40:43], v[164:167], v[100:103]\ns_add_u32 s53, s49, 0\n"
"v_mfma_f32_16x16x32_bf16 v[104:107], v[40:43], a[32:35], v[104:107]\nbuffer_load_dwordx4 v10, s[12:15], s53 offen lds\nv_mfma_f32_16x16x32_bf16 v[108:111], v[40:43], a[36:39], v[108:111]\nds_read_b128 v[40:43], v32 offset:12288\n"
"s_waitcnt lgkmcnt(3)\nv_mfma_f32_16x16x32_bf16 v[112:115], v[44:47], v[160:163], v[112:115]\nv_mfma_f32_16x16x32_bf16 v[116:119], v[44:47], v[164:167], v[116:119]\nv_mfma_f32_16x16x32_bf16 v[120:123], v[44:47], a[32:35], v[120:123]\n"
"v_mfma_f32_16x16x32_bf16 v[124:127], v[44:47], a[36:39], v[124:127]\nds_read_b128 v[44:47], v33 offset:12288\ns_waitcnt lgkmcnt(3)\nv_mfma_f32_16x16x32_bf16 v[96:99], v[48:51], v[168:171], v[96:99]\n"
"v_mfma_f32_16x16x32_bf16 v[100:103], v[48:51], v[172:175], v[100:103]\nv_mfma_f32_16x16x32_bf16 v[104:107], v[48:51], a[40:43], v[104:107]\nv_mfma_f32_16x16x32_bf16 v[108:111], v[48:51], a[44:47], v[108:111]\nds_read_b128 v[48:51], v34 offset:12288\n"
"s_add_u32 m0, s52, 4096\ns_waitcnt lgkmcnt(3)\nv_mfma_f32_16x16x32_bf16 v[112:115], v[52:55], v[168:171], v[112:115]\ns_add_u32 s53, s49, 128\n"
"v_mfma_f32_16x16x32_bf16 v[116:119], v[52:55], v[172:175], v[116:119]\nbuffer_load_dwordx4 v10, s[12:15], s53 offen lds\nv_mfma_f32_16x16x32_bf16 v[120:123], v[52:55], a[40:43], v[120:123]\nv_mfma_f32_16x16x32_bf16 v[124:127], v[52:55], a[44:47], v[124:127]\n"
"ds_read_b128 v[52:55], v35 offset:12288\ns_waitcnt lgkmcnt(3)\nv_mfma_f32_16x16x32_bf16 v[96:99], v[40:43], v[176:179], v[96:99]\nv_mfma_f32_16x16x32_bf16 v[100:103], v[40:43], v[180:183], v[100:103]\n"
"v_mfma_f32_16x16x32_bf16 v[104:107], v[40:43], a[48:51], v[104:107]\nv_mfma_f32_16x16x32_bf16 v[108:111], v[40:43], a[52:55], v[108:111]\nds_read_b128 v[40:43], v32 offset:16384\ns_waitcnt lgkmcnt(3)\n"
"v_mfma_f32_16x16x32_bf16 v[112:115], v[44:47], v[176:179], v[112:115]\nv_mfma_f32_16x16x32_bf16 v[116:119], v[44:47], v[180:183], v[116:119]\nv_mfma_f32_16x16x32_bf16 v[120:123], v[44:47], a[48:51], v[120:123]\ns_add_u32 m0, s52, 8192\n"
"v_mfma_f32_16x16x32_bf16 v[124:127], v[44:47], a[52:55], v[124:127]\nds_read_b128 v[44:47], v33 offset:16384\ns_add_u32 s53, s49, 256\ns_waitcnt lgkmcnt(3)\n"
"v_mfma_f32_16x16x32_bf16 v[96:99], v[48:51], v[184:187], v[96:99]\nbuffer_load_dwordx4 v10, s[12:15], s53 offen lds\nv_mfma_f32_16x16x32_bf16 v[100:103], v[48:51], v[188:191], v[100:103]\nv_mfma_f32_16x16x32_bf16 v[104:107], v[48:51], a[56:59], v[104:107]\n"
"v_mfma_f32_16x16x32_bf16 v[108:111], v[48:51], a[60:63], v[108:111]\nds_read_b128 v[48:51], v34 offset:16384\ns_waitcnt lgkmcnt(3)\nv_mfma_f32_16x16x32_bf16 v[112:115], v[52:55], v[184:187], v[112:115]\n"
"v_mfma_f32_16x16x32_bf16 v[116:119], v[52:55], v[188:191], v[116:119]\nv_mfma_f32_16x16x32_bf16 v[120:123], v[52:55], a[56:59], v[120:123]\nv_mfma_f32_16x16x32_bf16 v[124:127], v[52:55], a[60:63], v[124:127]\nds_read_b128 v[52:55], v35 offset:16384\n"
"s_waitcnt lgkmcnt(3)\nv_mfma_f32_16x16x32_bf16 v[96:99], v[40:43], v[192:195], v[96:99]\nv_mfma_f32_16x16x32_bf16 v[100:103], v[40:43], v[196:199], v[100:103]\ns_add_u32 m0, s52, 12288\n"
"v_mfma_f32_16x16x32_bf16 v[104:107], v[40:43], a[64:67], v[104:107]\ns_add_u32 s53, s49, 384\nv_mfma_f32_16x16x32_bf16 v[108:111], v[40:43], a[68:71], v[108:111]\nds_read_b128 v[40:43], v32 offset:20480\n"
"buffer_load_dwordx4 v10, s[12:15], s53 offen lds\ns_waitcnt lgkmcnt(3)\nv_mfma_f32_16x16x32_bf16 v[112:115], v[44:47], v[192:195], v[112:115]\nv_mfma_f32_16x16x32_bf16 v[116:119], v[44:47], v[196:199], v[116:119]\n"
"v_mfma_f32_16x16x32_bf16 v[120:123], v[44:47], a[64:67], v[120:123]\nv_mfma_f32_16x16x32_bf16 v[124:127], v[44:47], a[68:71], v[124:127]\nds_read_b128 v[44:47], v33 offset:20480\ns_waitcnt lgkmcnt(3)\n"
"v_mfma_f32_16x16x32_bf16 v[96:99], v[48:51], v[200:203], v[96:99]\nv_mfma_f32_16x16x32_bf16 v[100:103], v[48:51], v[204:207], v[100:103]\nv_mfma_f32_16x16x32_bf16 v[104:107], v[48:51], a[72:75], v[104:107]\nv_mfma_f32_16x16x32_bf16 v[108:111], v[48:51], a[76:79], v[108:111]\n"
"ds_read_b128 v[48:51], v34 offset:20480\ns_waitcnt lgkmcnt(3)\nv_mfma_f32_16x16x32_bf16 v[112:115], v[52:55], v[200:203], v[112:115]\ns_add_u32 m0, s52, 16384\n"
"v_mfma_f32_16x16x32_bf16 v[116:119], v[52:55], v[204:207], v[116:119]\ns_add_u32 s53, s49, 512\nv_mfma_f32_16x16x32_bf16 v[120:123], v[52:55], a[72:75], v[120:123]\nbuffer_load_dwordx4 v10, s[12:15], s53 offen lds\n"
"v_mfma_f32_16x16x32_bf16 v[124:127], v[52:55], a[76:79], v[124:127]\nds_read_b128 v[52:55], v35 offset:20480\ns_waitcnt lgkmcnt(3)\nv_mfma_f32_16x16x32_bf16 v[96:99], v[40:43], v[208:211], v[96:99]\n"
"v_mfma_f32_16x16x32_bf16 v[100:103], v[40:43], v[212:215], v[100:103]\nv_mfma_f32_16x16x32_bf16 v[104:107], v[40:43], a[80:83], v[104:107]\nv_mfma_f32_16x16x32_bf16 v[108:111], v[40:43], a[84:87], v[108:111]\nds_read_b128 v[40:43], v32 offset:24576\n"
"s_waitcnt lgkmcnt(3)\nv_mfma_f32_16x16x32_bf16 v[112:115], v[44:47], v[208:211], v[112:115]\nv_mfma_f32_16x16x32_bf16 v[116:119], v[44:47], v[212:215], v[116:119]\nv_mfma_f32_16x16x32_bf16 v[120:123], v[44:47], a[80:83], v[120:123]\n"
"v_mfma_f32_16x16x32_bf16 v[124:127], v[44:47], a[84:87], v[124:127]\nds_read_b128 v[44:47], v33 offset:24576\ns_add_u32 m0, s52, 20480\ns_waitcnt lgkmcnt(3)\n"
"v_mfma_f32_16x16x32_bf16 v[96:99], v[48:51], v[216:219], v[96:99]\ns_add_u32 s54, s51, 32768\ns_cmp_eq_u32 s54, 163840\nv_mfma_f32_16x16x32_bf16 v[100:103], v[48:51], v[220:223], v[100:103]\n"
"v_mfma_f32_16x16x32_bf16 v[104:107], v[48:51], a[88:91], v[104:107]\nv_mfma_f32_16x16x32_bf16 v[108:111], v[48:51], a[92:95], v[108:111]\ns_cselect_b32 s54, 0, s54\ns_sub_u32 s54, s54, s51\n"
"ds_read_b128 v[48:51], v34 offset:24576\ns_add_u32 s53, s49, 640\ns_waitcnt lgkmcnt(3)\nv_mfma_f32_16x16x32_bf16 v[112:115], v[52:55], v[216:219], v[112:115]\n"
"buffer_load_dwordx4 v10, s[12:15], s53 offen lds\nv_mfma_f32_16x16x32_bf16 v[116:119], v[52:55], v[220:223], v[116:119]\nv_mfma_f32_16x16x32_bf16 v[120:123], v[52:55], a[88:91], v[120:123]\nv_mfma_f32_16x16x32_bf16 v[124:127], v[52:55], a[92:95], v[124:127]\n"
"ds_read_b128 v[52:55], v35 offset:24576\ns_waitcnt lgkmcnt(3)\nv_mfma_f32_16x16x32_bf16 v[96:99], v[40:43], v[224:227], v[96:99]\nv_add_u32 v32, s54, v32\n"
"v_mfma_f32_16x16x32_bf16 v[100:103], v[40:43], v[228:231], v[100:103]\nv_mfma_f32_16x16x32_bf16 v[104:107], v[40:43], a[96:99], v[104:107]\nv_mfma_f32_16x16x32_bf16 v[108:111], v[40:43], a[100:103], v[108:111]\nds_read_b128 v[40:43], v32 offset:0\n"
"s_waitcnt lgkmcnt(3)\nv_mfma_f32_16x16x32_bf16 v[112:115], v[44:47], v[224:227], v[112:115]\nv_add_u32 v33, s54, v33\nv_mfma_f32_16x16x32_bf16 v[116:119], v[44:47], v[228:231], v[116:119]\n"
"s_add_u32 m0, s52, 24576\nv_mfma_f32_16x16x32_bf16 v[120:123], v[44:47], a[96:99], v[120:123]\ns_add_u32 s53, s49, 768\nv_mfma_f32_16x16x32_bf16 v[124:127], v[44:47], a[100:103], v[124:127]\n"
"ds_read_b128 v[44:47], v33 offset:0\nbuffer_load_dwordx4 v10, s[12:15], s53 offen lds\ns_waitcnt lgkmcnt(3)\nv_mfma_f32_16x16x32_bf16 v[96:99], v[48:51], v[232:235], v[96:99]\n"
"v_add_u32 v34, s54, v34\nv_mfma_f32_16x16x32_bf16 v[100:103], v[48:51], v[236:239], v[100:103]\nv_mfma_f32_16x16x32_bf16 v[104:107], v[48:51], a[104:107], v[104:107]\nv_mfma_f32_16x16x32_bf16 v[108:111], v[48:51], a[108:111], v[108:111]\n"
"ds_read_b128 v[48:51], v34 offset:0\ns_waitcnt lgkmcnt(3)\nv_mfma_f32_16x16x32_bf16 v[112:115], v[52:55], v[232:235], v[112:115]\nv_add_u32 v35, s54, v35\n"
"v_mfma_f32_16x16x32_bf16 v[116:119], v[52:55], v[236:239], v[116:119]\nv_mfma_f32_16x16x32_bf16 v[120:123], v[52:55], a[104:107], v[120:123]\nv_mfma_f32_16x16x32_bf16 v[124:127], v[52:55], a[108:111], v[124:127]\nds_read_b128 v[52:55], v35 offset:0\n"
"s_mov_b32 s52, s47\ns_add_u32 s47, s51, s32\ns_add_u32 s51, s51, s54\ns_add_u32 s49, s49, 28672\n"
"s_cmp_lt_u32 s49, 1921024\ns_cbranch_scc1 fg1_loop\ns_nop 7\nv_cvt_pk_bf16_f32 v56, v96, v97\n"
"v_cvt_pk_bf16_f32 v57, v98, v99\nv_cvt_pk_bf16_f32 v58, v112, v113\nv_cvt_pk_bf16_f32 v59, v114, v115\ns_nop 0\n"
"s_nop 0\nbuffer_store_dwordx4 v[56:59], v15, s[20:23], s50 offen sc1\nv_cvt_pk_bf16_f32 v60, v100, v101\nv_cvt_pk_bf16_f32 v61, v102, v103\n"
"v_cvt_pk_bf16_f32 v62, v116, v117\nv_cvt_pk_bf16_f32 v63, v118, v119\ns_nop 0\ns_nop 0\n"
"buffer_store_dwordx4 v[60:63], v16, s[20:23], s50 offen sc1\nv_cvt_pk_bf16_f32 v56, v104, v105\nv_cvt_pk_bf16_f32 v57, v106, v107\nv_cvt_pk_bf16_f32 v58, v120, v121\n"
"v_cvt_pk_bf16_f32 v59, v122, v123\ns_nop 0\ns_nop 0\nbuffer_store_dwordx4 v[56:59], v17, s[20:23], s50 offen sc1\n"
"v_cvt_pk_bf16_f32 v60, v108, v109\nv_cvt_pk_bf16_f32 v61, v110, v111\nv_cvt_pk_bf16_f32 v62, v124, v125\nv_cvt_pk_bf16_f32 v63, v126, v127\n"
"s_nop 0\ns_nop 0\nbuffer_store_dwordx4 v[60:63], v18, s[20:23], s50 offen sc1\ns_add_u32 s48, s28, 64\n"
"v_mov_b32 v36, s48\ns_waitcnt vmcnt(0) lgkmcnt(0)\ns_barrier\nbuffer_store_dword v36, v19, s[24:27], s33 offen sc1\n"
"fg1_end:\n"
    :  : "{s[8:23]}"(s0), "{s[24:39]}"(s1), "{v[8:23]}"(v0) : "v24", "v25", "v26", "v27", "v28", "v29", "v30", "v31", "v32", "v33", "v34", "v35", "v36", "v37", "v38", "v39", "v40", "v41", "v42", "v43", "v44", "v45", "v46", "v47", "v48", "v49", "v50", "v51", "v52", "v53", "v54", "v55", "v56", "v57", "v58", "v59", "v60", "v61", "v62", "v63", "v64", "v65", "v66", "v67", "v68", "v69", "v70", "v71", "v72", "v73", "v74", "v75", "v76", "v77", "v78", "v79", "v80", "v81", "v82", "v83", "v84", "v85", "v86", "v87", "v88", "v89", "v90", "v91", "v92", "v93", "v94", "v95", "v96", "v97", "v98", "v99", "v100", "v101", "v102", "v103", "v104", "v105", "v106", "v107", "v108", "v109", "v110", "v111", "v112", "v113", "v114", "v115", "v116", "v117", "v118", "v119", "v120", "v121", "v122", "v123", "v124", "v125", "v126", "v127", "v128", "v129", "v130", "v131", "v132", "v133", "v134", "v135", "v136", "v137", "v138", "v139", "v140", "v141", "v142", "v143", "v144", "v145", "v146", "v147", "v148", "v149", "v150", "v151", "v152", "v153", "v154", "v155", "v156", "v157", "v158", "v159", "v160", "v161", "v162", "v163", "v164", "v165", "v166", "v167", "v168", "v169", "v170", "v171", "v172", "v173", "v174", "v175", "v176", "v177", "v178", "v179", "v180", "v181", "v182", "v183", "v184", "v185", "v186", "v187", "v188", "v189", "v190", "v191", "v192", "v193", "v194", "v195", "v196", "v197", "v198", "v199", "v200", "v201", "v202", "v203", "v204", "v205", "v206", "v207", "v208", "v209", "v210", "v211", "v212", "v213", "v214", "v215", "v216", "v217", "v218", "v219", "v220", "v221", "v222", "v223", "v224", "v225", "v226", "v227", "v228", "v229", "v230", "v231", "v232", "v233", "v234", "v235", "v236", "v237", "v238", "v239", "v240", "v241", "v242", "v243", "a0", "a1", "a2", "a3", "a4", "a5", "a6", "a7", "a8", "a9", "a10", "a11", "a12", "a13", "a14", "a15", "a16", "a17", "a18", "a19", "a20", "a21", "a22", "a23", "a24", "a25", "a26", "a27", "a28", "a29", "a30", "a31", "a32", "a33", "a34", "a35", "a36", "a37", "a38", "a39", "a40", "a41", "a42", "a43", "a44", "a45", "a46", "a47", "a48", "a49", "a50", "a51", "a52", "a53", "a54", "a55", "a56", "a57", "a58", "a59", "a60", "a61", "a62", "a63", "a64", "a65", "a66", "a67", "a68", "a69", "a70", "a71", "a72", "a73", "a74", "a75", "a76", "a77", "a78", "a79", "a80", "a81", "a82", "a83", "a84", "a85", "a86", "a87", "a88", "a89", "a90", "a91", "a92", "a93", "a94", "a95", "a96", "a97", "a98", "a99", "a100", "a101", "a102", "a103", "a104", "a105", "a106", "a107", "a108", "a109", "a110", "a111", "a112", "a113", "a114", "a115", "a116", "a117", "a118", "a119", "a120", "a121", "a122", "a123", "a124", "a125", "a126", "a127", "a128", "a129", "a130", "a131", "a132", "a133", "a134", "a135", "a136", "a137", "a138", "a139", "a140", "a141", "a142", "a143", "a144", "a145", "a146", "a147", "a148", "a149", "a150", "a151", "a152", "a153", "a154", "a155", "a156", "a157", "a158", "a159", "a160", "a161", "a162", "a163", "a164", "a165", "a166", "a167", "a168", "a169", "a170", "a171", "a172", "a173", "a174", "a175", "a176", "a177", "a178", "a179", "a180", "a181", "a182", "a183", "a184", "a185", "a186", "a187", "a188", "a189", "a190", "a191", "s40", "s41", "s42", "s43", "s44", "s45", "s46", "s47", "s48", "s49", "s50", "s51", "s52", "s53", "s54", "s55", "s56", "s57", "s58", "s59", "s60", "s61", "vcc", "scc", "memory");
    return o;
}
struct OCtx { u4 r; unsigned sg, ssx, sr, su, sum, nt, hx, p0, p1, p2, p3, p4; unsigned vg, vsx, vu, vum, vx, vt, vpx, vpm, vpr, vo, p5, p6, p7, p8, p9, pa; };
DI void o_asm(const OCtx& c) {
    const u16v s0 = {RFL(c.r[0]), RFL(c.r[1]), RFL(c.r[2]), RFL(c.r[3]), RFL(c.sg), RFL(c.ssx), RFL(c.sr), RFL(c.su), RFL(c.sum), RFL(c.nt), RFL(c.hx), RFL(c.p0), RFL(c.p1), RFL(c.p2), RFL(c.p3), RFL(c.p4)}, v0 = {c.vg, c.vsx, c.vu, c.vum, c.vx, c.vt, c.vpx, c.vpm, c.vpr, c.vo, c.p5, c.p6, c.p7, c.p8, c.p9, c.pa};
    asm volatile(
"s_nop 4\ns_xor_b32 s72, s18, 0\ns_mul_i32 s72, s72, s14\ns_add_u32 s72, s72, s12\n"
"buffer_load_dwordx4 a[0:3], v8, s[8:11], s72 offen offset:0 sc1\nbuffer_load_dwordx4 a[4:7], v8, s[8:11], s72 offen offset:512 sc1\nbuffer_load_dwordx4 a[8:11], v8, s[8:11], s72 offen offset:1024 sc1\nbuffer_load_dwordx4 a[12:15], v8, s[8:11], s72 offen offset:1536 sc1\n"
"buffer_load_dwordx4 a[16:19], v8, s[8:11], s72 offen offset:2048 sc1\nbuffer_load_dwordx4 a[20:23], v8, s[8:11], s72 offen offset:2560 sc1\nbuffer_load_dwordx4 a[24:27], v8, s[8:11], s72 offen offset:3072 sc1\nbuffer_load_dwordx4 a[28:31], v8, s[8:11], s72 offen offset:3584 sc1\n"
"s_add_u32 s72, s72, 4096\nbuffer_load_dwordx4 a[32:35], v8, s[8:11], s72 offen offset:0 sc1\nbuffer_load_dwordx4 a[36:39], v8, s[8:11], s72 offen offset:512 sc1\nbuffer_load_dwordx4 a[40:43], v8, s[8:11], s72 offen offset:1024 sc1\n"
"buffer_load_dwordx4 a[44:47], v8, s[8:11], s72 offen offset:1536 sc1\nbuffer_load_dwordx4 a[48:51], v8, s[8:11], s72 offen offset:2048 sc1\nbuffer_load_dwordx4 a[52:55], v8, s[8:11], s72 offen offset:2560 sc1\nbuffer_load_dwordx4 a[56:59], v8, s[8:11], s72 offen offset:3072 sc1\n"
"buffer_load_dwordx4 a[60:63], v8, s[8:11], s72 offen offset:3584 sc1\ns_add_u32 s72, s72, 4096\nbuffer_load_dwordx4 a[64:67], v8, s[8:11], s72 offen offset:0 sc1\nbuffer_load_dwordx4 a[68:71], v8, s[8:11], s72 offen offset:512 sc1\n"
"buffer_load_dwordx4 a[72:75], v8, s[8:11], s72 offen offset:1024 sc1\nbuffer_load_dwordx4 a[76:79], v8, s[8:11], s72 offen offset:1536 sc1\nbuffer_load_dwordx4 a[80:83], v8, s[8:11], s72 offen offset:2048 sc1\nbuffer_load_dwordx4 a[84:87], v8, s[8:11], s72 offen offset:2560 sc1\n"
"buffer_load_dwordx4 a[88:91], v8, s[8:11], s72 offen offset:3072 sc1\nbuffer_load_dwordx4 a[92:95], v8, s[8:11], s72 offen offset:3584 sc1\ns_add_u32 s72, s72, 4096\nbuffer_load_dwordx4 a[96:99], v8, s[8:11], s72 offen offset:0 sc1\n"
"buffer_load_dwordx4 a[100:103], v8, s[8:11], s72 offen offset:512 sc1\nbuffer_load_dwordx4 a[104:107], v8, s[8:11], s72 offen offset:1024 sc1\nbuffer_load_dwordx4 a[108:111], v8, s[8:11], s72 offen offset:1536 sc1\ns_xor_b32 s73, s18, 0\n"
"s_lshl_b32 s73, s73, 2\ns_mul_i32 s73, s73, s19\ns_add_u32 s73, s73, s13\nbuffer_load_dwordx4 v[64:67], v9, s[8:11], s73 offen sc1\n"
"s_add_u32 s73, s73, s19\nbuffer_load_dwordx4 v[68:71], v9, s[8:11], s73 offen sc1\ns_add_u32 s73, s73, s19\nbuffer_load_dwordx4 v[72:75], v9, s[8:11], s73 offen sc1\n"
"s_add_u32 s73, s73, s19\nbuffer_load_dwordx4 v[76:79], v9, s[8:11], s73 offen sc1\ns_xor_b32 s72, s18, 1\ns_mul_i32 s72, s72, s14\n"
"s_add_u32 s72, s72, s12\nbuffer_load_dwordx4 a[112:115], v8, s[8:11], s72 offen offset:0 sc1\nbuffer_load_dwordx4 a[116:119], v8, s[8:11], s72 offen offset:512 sc1\nbuffer_load_dwordx4 a[120:123], v8, s[8:11], s72 offen offset:1024 sc1\n"
"buffer_load_dwordx4 a[124:127], v8, s[8:11], s72 offen offset:1536 sc1\nbuffer_load_dwordx4 a[128:131], v8, s[8:11], s72 offen offset:2048 sc1\nbuffer_load_dwordx4 a[132:135], v8, s[8:11], s72 offen offset:2560 sc1\nbuffer_load_dwordx4 a[136:139], v8, s[8:11], s72 offen offset:3072 sc1\n"
"buffer_load_dwordx4 a[140:143], v8, s[8:11], s72 offen offset:3584 sc1\ns_add_u32 s72, s72, 4096\nbuffer_load_dwordx4 a[144:147], v8, s[8:11], s72 offen offset:0 sc1\nbuffer_load_dwordx4 a[148:151], v8, s[8:11], s72 offen offset:512 sc1\n"
"buffer_load_dwordx4 a[152:155], v8, s[8:11], s72 offen offset:1024 sc1\nbuffer_load_dwordx4 a[156:159], v8, s[8:11], s72 offen offset:1536 sc1\nbuffer_load_dwordx4 a[160:163], v8, s[8:11], s72 offen offset:2048 sc1\nbuffer_load_dwordx4 a[164:167], v8, s[8:11], s72 offen offset:2560 sc1\n"
"buffer_load_dwordx4 a[168:171], v8, s[8:11], s72 offen offset:3072 sc1\nbuffer_load_dwordx4 a[172:175], v8, s[8:11], s72 offen offset:3584 sc1\ns_add_u32 s72, s72, 4096\nbuffer_load_dwordx4 a[176:179], v8, s[8:11], s72 offen offset:0 sc1\n"
"buffer_load_dwordx4 a[180:183], v8, s[8:11], s72 offen offset:512 sc1\nbuffer_load_dwordx4 a[184:187], v8, s[8:11], s72 offen offset:1024 sc1\nbuffer_load_dwordx4 a[188:191], v8, s[8:11], s72 offen offset:1536 sc1\nbuffer_load_dwordx4 a[192:195], v8, s[8:11], s72 offen offset:2048 sc1\n"
"buffer_load_dwordx4 a[196:199], v8, s[8:11], s72 offen offset:2560 sc1\nbuffer_load_dwordx4 a[200:203], v8, s[8:11], s72 offen offset:3072 sc1\nbuffer_load_dwordx4 a[204:207], v8, s[8:11], s72 offen offset:3584 sc1\ns_add_u32 s72, s72, 4096\n"
"buffer_load_dwordx4 a[208:211], v8, s[8:11], s72 offen offset:0 sc1\nbuffer_load_dwordx4 a[212:215], v8, s[8:11], s72 offen offset:512 sc1\nbuffer_load_dwordx4 a[216:219], v8, s[8:11], s72 offen offset:1024 sc1\nbuffer_load_dwordx4 a[220:223], v8, s[8:11], s72 offen offset:1536 sc1\n"
"s_add_u32 s76, s15, 0\nv_xor_b32 v24, 0, v12\ns_add_u32 s77, s15, 4096\nv_xor_b32 v25, 32, v12\n"
"s_add_u32 s78, s15, 8192\nv_xor_b32 v26, 64, v12\ns_add_u32 s79, s15, 12288\nv_xor_b32 v27, 96, v12\n"
"s_mov_b32 s80, s16\nv_cmp_gt_i32 s[40:41], 0, v13\nv_cmp_gt_i32 s[42:43], 1, v13\nv_cmp_gt_i32 s[44:45], 2, v13\n"
"v_cmp_gt_i32 s[46:47], 3, v13\nv_cmp_gt_i32 s[48:49], 4, v13\nv_cmp_gt_i32 s[50:51], 5, v13\nv_cmp_gt_i32 s[52:53], 6, v13\n"
"v_cmp_gt_i32 s[54:55], 7, v13\nv_cmp_gt_i32 s[56:57], 16, v13\nv_cmp_gt_i32 s[58:59], 17, v13\nv_cmp_gt_i32 s[60:61], 18, v13\n"
"v_cmp_gt_i32 s[62:63], 19, v13\nv_cmp_gt_i32 s[64:65], 20, v13\nv_cmp_gt_i32 s[66:67], 21, v13\nv_cmp_gt_i32 s[68:69], 22, v13\n"
"v_cmp_gt_i32 s[70:71], 23, v13\ns_barrier\nds_read_b128 v[128:131], v24 offset:0\nds_read_b128 v[132:135], v25 offset:0\n"
"ds_read_b128 v[136:139], v26 offset:0\nds_read_b128 v[140:143], v27 offset:0\nds_read_b128 v[144:147], v24 offset:4096\nds_read_b128 v[148:151], v25 offset:4096\n"
"ds_read_b128 v[152:155], v26 offset:4096\nds_read_b128 v[156:159], v27 offset:4096\nds_read_b128 v[160:163], v24 offset:8192\nds_read_b128 v[164:167], v25 offset:8192\n"
"ds_read_b128 v[168:171], v26 offset:8192\nds_read_b128 v[172:175], v27 offset:8192\ns_waitcnt lgkmcnt(0)\nds_read_b128 v[176:179], v24 offset:12288\n"
"ds_read_b128 v[180:183], v25 offset:12288\nds_read_b128 v[184:187], v26 offset:12288\nds_read_b128 v[188:191], v27 offset:12288\nds_read_b128 v[192:195], v24 offset:16384\n"
"ds_read_b128 v[196:199], v25 offset:16384\nds_read_b128 v[200:203], v26 offset:16384\nds_read_b128 v[204:207], v27 offset:16384\nds_read_b128 v[208:211], v24 offset:20480\n"
"ds_read_b128 v[212:215], v25 offset:20480\nds_read_b128 v[216:219], v26 offset:20480\nds_read_b128 v[220:223], v27 offset:20480\ns_waitcnt lgkmcnt(0)\n"
"ds_read_b128 v[224:227], v24 offset:24576\nds_read_b128 v[228:231], v25 offset:24576\nds_read_b128 v[232:235], v26 offset:24576\nds_read_b128 v[236:239], v27 offset:24576\n"
"v_mov_b32 v31, 0xff800000\ns_waitcnt lgkmcnt(0)\ns_waitcnt vmcnt(59)\nv_mfma_f32_32x32x16_bf16 v[40:55], a[0:3], v[128:131], 0\n"
"s_xor_b32 s73, s18, 1\ns_lshl_b32 s73, s73, 2\ns_mul_i32 s73, s73, s19\ns_add_u32 s73, s73, s13\n"
"buffer_load_dwordx4 v[80:83], v9, s[8:11], s73 offen sc1\ns_add_u32 s73, s73, s19\nbuffer_load_dwordx4 v[84:87], v9, s[8:11], s73 offen sc1\ns_add_u32 s73, s73, s19\n"
"buffer_load_dwordx4 v[88:91], v9, s[8:11], s73 offen sc1\ns_add_u32 s73, s73, s19\nbuffer_load_dwordx4 v[92:95], v9, s[8:11], s73 offen sc1\ns_xor_b32 s72, s18, 2\n"
"s_mul_i32 s72, s72, s14\ns_add_u32 s72, s72, s12\nbuffer_load_dwordx4 a[0:3], v8, s[8:11], s72 offen offset:0 sc1\ns_waitcnt vmcnt(63)\n"
"v_mfma_f32_32x32x16_bf16 v[40:55], a[4:7], v[132:135], v[40:55]\nbuffer_load_dwordx4 a[4:7], v8, s[8:11], s72 offen offset:512 sc1\ns_waitcnt vmcnt(63)\nv_mfma_f32_32x32x16_bf16 v[40:55], a[8:11], v[136:139], v[40:55]\n"
"buffer_load_dwordx4 a[8:11], v8, s[8:11], s72 offen offset:1024 sc1\ns_waitcnt vmcnt(63)\nv_mfma_f32_32x32x16_bf16 v[40:55], a[12:15], v[140:143], v[40:55]\nbuffer_load_dwordx4 a[12:15], v8, s[8:11], s72 offen offset:1536 sc1\n"
"s_waitcnt vmcnt(63)\nv_mfma_f32_32x32x16_bf16 v[40:55], a[16:19], v[144:147], v[40:55]\nbuffer_load_dwordx4 a[16:19], v8, s[8:11], s72 offen offset:2048 sc1\ns_waitcnt vmcnt(63)\n"
"v_mfma_f32_32x32x16_bf16 v[40:55], a[20:23], v[148:151], v[40:55]\nbuffer_load_dwordx4 a[20:23], v8, s[8:11], s72 offen offset:2560 sc1\ns_waitcnt vmcnt(63)\nv_mfma_f32_32x32x16_bf16 v[40:55], a[24:27], v[152:155], v[40:55]\n"
"buffer_load_dwordx4 a[24:27], v8, s[8:11], s72 offen offset:3072 sc1\ns_waitcnt vmcnt(63)\nv_mfma_f32_32x32x16_bf16 v[40:55], a[28:31], v[156:159], v[40:55]\nbuffer_load_dwordx4 a[28:31], v8, s[8:11], s72 offen offset:3584 sc1\n"
"s_waitcnt vmcnt(63)\nv_mfma_f32_32x32x16_bf16 v[40:55], a[32:35], v[160:163], v[40:55]\ns_add_u32 s72, s72, 4096\nbuffer_load_dwordx4 a[32:35], v8, s[8:11], s72 offen offset:0 sc1\n"
"s_waitcnt vmcnt(63)\nv_mfma_f32_32x32x16_bf16 v[40:55], a[36:39], v[164:167], v[40:55]\nbuffer_load_dwordx4 a[36:39], v8, s[8:11], s72 offen offset:512 sc1\ns_waitcnt vmcnt(63)\n"
"v_mfma_f32_32x32x16_bf16 v[40:55], a[40:43], v[168:171], v[40:55]\nbuffer_load_dwordx4 a[40:43], v8, s[8:11], s72 offen offset:1024 sc1\ns_waitcnt vmcnt(63)\nv_mfma_f32_32x32x16_bf16 v[40:55], a[44:47], v[172:175], v[40:55]\n"
"buffer_load_dwordx4 a[44:47], v8, s[8:11], s72 offen offset:1536 sc1\ns_waitcnt vmcnt(63)\nv_mfma_f32_32x32x16_bf16 v[40:55], a[48:51], v[176:179], v[40:55]\nbuffer_load_dwordx4 a[48:51], v8, s[8:11], s72 offen offset:2048 sc1\n"
"s_waitcnt vmcnt(63)\nv_mfma_f32_32x32x16_bf16 v[40:55], a[52:55], v[180:183], v[40:55]\nbuffer_load_dwordx4 a[52:55], v8, s[8:11], s72 offen offset:2560 sc1\ns_waitcnt vmcnt(63)\n"
"v_mfma_f32_32x32x16_bf16 v[40:55], a[56:59], v[184:187], v[40:55]\nbuffer_load_dwordx4 a[56:59], v8, s[8:11], s72 offen offset:3072 sc1\ns_waitcnt vmcnt(63)\nv_mfma_f32_32x32x16_bf16 v[40:55], a[60:63], v[188:191], v[40:55]\n"
"buffer_load_dwordx4 a[60:63], v8, s[8:11], s72 offen offset:3584 sc1\ns_waitcnt vmcnt(63)\nv_mfma_f32_32x32x16_bf16 v[40:55], a[64:67], v[192:195], v[40:55]\ns_add_u32 s72, s72, 4096\n"
"buffer_load_dwordx4 a[64:67], v8, s[8:11], s72 offen offset:0 sc1\ns_waitcnt vmcnt(63)\nv_mfma_f32_32x32x16_bf16 v[40:55], a[68:71], v[196:199], v[40:55]\nbuffer_load_dwordx4 a[68:71], v8, s[8:11], s72 offen offset:512 sc1\n"
"s_waitcnt vmcnt(63)\nv_mfma_f32_32x32x16_bf16 v[40:55], a[72:75], v[200:203], v[40:55]\nbuffer_load_dwordx4 a[72:75], v8, s[8:11], s72 offen offset:1024 sc1\ns_waitcnt vmcnt(63)\n"
"v_mfma_f32_32x32x16_bf16 v[40:55], a[76:79], v[204:207], v[40:55]\nbuffer_load_dwordx4 a[76:79], v8, s[8:11], s72 offen offset:1536 sc1\ns_waitcnt vmcnt(63)\nv_mfma_f32_32x32x16_bf16 v[40:55], a[80:83], v[208:211], v[40:55]\n"
"buffer_load_dwordx4 a[80:83], v8, s[8:11], s72 offen offset:2048 sc1\ns_waitcnt vmcnt(63)\nv_mfma_f32_32x32x16_bf16 v[40:55], a[84:87], v[212:215], v[40:55]\nbuffer_load_dwordx4 a[84:87], v8, s[8:11], s72 offen offset:2560 sc1\n"
"s_waitcnt vmcnt(63)\nv_mfma_f32_32x32x16_bf16 v[40:55], a[88:91], v[216:219], v[40:55]\nbuffer_load_dwordx4 a[88:91], v8, s[8:11], s72 offen offset:3072 sc1\ns_waitcnt vmcnt(63)\n"
"v_mfma_f32_32x32x16_bf16 v[40:55], a[92:95], v[220:223], v[40:55]\nbuffer_load_dwordx4 a[92:95], v8, s[8:11], s72 offen offset:3584 sc1\ns_waitcnt vmcnt(63)\nv_mfma_f32_32x32x16_bf16 v[40:55], a[96:99], v[224:227], v[40:55]\n"
"s_add_u32 s72, s72, 4096\nbuffer_load_dwordx4 a[96:99], v8, s[8:11], s72 offen offset:0 sc1\ns_waitcnt vmcnt(63)\nv_mfma_f32_32x32x16_bf16 v[40:55], a[100:103], v[228:231], v[40:55]\n"
"buffer_load_dwordx4 a[100:103], v8, s[8:11], s72 offen offset:512 sc1\ns_waitcnt vmcnt(63)\nv_mfma_f32_32x32x16_bf16 v[40:55], a[104:107], v[232:235], v[40:55]\nbuffer_load_dwordx4 a[104:107], v8, s[8:11], s72 offen offset:1024 sc1\n"
"s_waitcnt vmcnt(63)\nv_mfma_f32_32x32x16_bf16 v[40:55], a[108:111], v[236:239], v[40:55]\nbuffer_load_dwordx4 a[108:111], v8, s[8:11], s72 offen offset:1536 sc1\ns_waitcnt vmcnt(60)\n"
"v_add_f32 v64, v64, v68\nv_add_f32 v72, v72, v76\nv_add_f32 v65, v65, v69\nv_add_f32 v73, v73, v77\n"
"v_add_f32 v66, v66, v70\nv_add_f32 v74, v74, v78\nv_add_f32 v67, v67, v71\nv_add_f32 v75, v75, v79\n"
"v_add_f32 v64, v64, v72\nv_add_f32 v65, v65, v73\nv_add_f32 v66, v66, v74\nv_add_f32 v67, v67, v75\n"
"v_max3_f32 v28, v64, v65, v66\nv_max_f32 v28, v28, v67\nv_cndmask_b32 v40, v40, v31, s[40:41]\nv_cndmask_b32 v41, v41, v31, s[42:43]\n"
"v_cndmask_b32 v42, v42, v31, s[44:45]\nv_cndmask_b32 v43, v43, v31, s[46:47]\nv_cndmask_b32 v44, v44, v31, s[48:49]\nv_cndmask_b32 v45, v45, v31, s[50:51]\n"
"v_cndmask_b32 v46, v46, v31, s[52:53]\nv_cndmask_b32 v47, v47, v31, s[54:55]\nv_cndmask_b32 v48, v48, v31, s[56:57]\nv_cndmask_b32 v49, v49, v31, s[58:59]\n"
"v_cndmask_b32 v50, v50, v31, s[60:61]\nv_cndmask_b32 v51, v51, v31, s[62:63]\nv_cndmask_b32 v52, v52, v31, s[64:65]\nv_cndmask_b32 v53, v53, v31, s[66:67]\n"
"v_cndmask_b32 v54, v54, v31, s[68:69]\nv_cndmask_b32 v55, v55, v31, s[70:71]\nv_max3_f32 v28, v28, v40, v41\nv_max3_f32 v28, v28, v42, v43\n"
"v_max3_f32 v28, v28, v44, v45\nv_max3_f32 v28, v28, v46, v47\nv_max3_f32 v28, v28, v48, v49\nv_max3_f32 v28, v28, v50, v51\n"
"v_max3_f32 v28, v28, v52, v53\nv_max3_f32 v28, v28, v54, v55\nv_mov_b32 v30, v28\ns_nop 1\n"
"v_permlane32_swap_b32 v28, v30\nv_max_f32 v28, v28, v30\nv_sub_f32 v40, v40, v28\nv_sub_f32 v41, v41, v28\n"
"v_sub_f32 v42, v42, v28\nv_sub_f32 v43, v43, v28\nv_sub_f32 v44, v44, v28\nv_sub_f32 v45, v45, v28\n"
"v_sub_f32 v46, v46, v28\nv_sub_f32 v47, v47, v28\nv_sub_f32 v48, v48, v28\nv_sub_f32 v49, v49, v28\n"
"v_sub_f32 v50, v50, v28\nv_sub_f32 v51, v51, v28\nv_sub_f32 v52, v52, v28\nv_sub_f32 v53, v53, v28\n"
"v_sub_f32 v54, v54, v28\nv_sub_f32 v55, v55, v28\nv_sub_f32 v64, v64, v28\nv_sub_f32 v65, v65, v28\n"
"v_sub_f32 v66, v66, v28\nv_sub_f32 v67, v67, v28\nv_exp_f32 v40, v40\nv_exp_f32 v41, v41\n"
"v_exp_f32 v42, v42\nv_exp_f32 v43, v43\nv_exp_f32 v44, v44\nv_exp_f32 v45, v45\n"
"v_exp_f32 v46, v46\nv_exp_f32 v47, v47\nv_exp_f32 v48, v48\nv_exp_f32 v49, v49\n"
"v_exp_f32 v50, v50\nv_exp_f32 v51, v51\nv_exp_f32 v52, v52\nv_exp_f32 v53, v53\n"
"v_exp_f32 v54, v54\nv_exp_f32 v55, v55\nv_exp_f32 v64, v64\nv_exp_f32 v65, v65\n"
"v_exp_f32 v66, v66\nv_exp_f32 v67, v67\nv_add_f32 v29, v40, v41\nv_add_f32 v30, v42, v43\n"
"v_add_f32 v29, v29, v44\nv_add_f32 v30, v30, v45\nv_add_f32 v29, v29, v46\nv_add_f32 v30, v30, v47\n"
"v_add_f32 v29, v29, v48\nv_add_f32 v30, v30, v49\nv_add_f32 v29, v29, v50\nv_add_f32 v30, v30, v51\n"
"v_add_f32 v29, v29, v52\nv_add_f32 v30, v30, v53\nv_add_f32 v29, v29, v54\nv_add_f32 v30, v30, v55\n"
"v_add_f32 v29, v29, v64\nv_add_f32 v30, v30, v65\nv_add_f32 v29, v29, v66\nv_add_f32 v30, v30, v67\n"
"v_add_f32 v29, v29, v30\nv_mov_b32 v30, v29\ns_nop 1\nv_permlane32_swap_b32 v29, v30\n"
"v_add_f32 v29, v29, v30\nv_rcp_f32 v29, v29\ns_nop 0\nv_mul_f32 v40, v40, v29\n"
"v_mul_f32 v41, v41, v29\nv_mul_f32 v42, v42, v29\nv_mul_f32 v43, v43, v29\nv_mul_f32 v44, v44, v29\n"
"v_mul_f32 v45, v45, v29\nv_mul_f32 v46, v46, v29\nv_mul_f32 v47, v47, v29\nv_mul_f32 v48, v48, v29\n"
"v_mul_f32 v49, v49, v29\nv_mul_f32 v50, v50, v29\nv_mul_f32 v51, v51, v29\nv_mul_f32 v52, v52, v29\n"
"v_mul_f32 v53, v53, v29\nv_mul_f32 v54, v54, v29\nv_mul_f32 v55, v55, v29\nv_mul_f32 v64, v64, v29\n"
"v_mul_f32 v65, v65, v29\nv_mul_f32 v66, v66, v29\nv_mul_f32 v67, v67, v29\nv_cvt_pk_bf16_f32 v56, v40, v41\n"
"v_cvt_pk_bf16_f32 v57, v42, v43\nv_cvt_pk_bf16_f32 v58, v44, v45\nv_cvt_pk_bf16_f32 v59, v46, v47\nv_cvt_pk_bf16_f32 v60, v48, v49\n"
"v_cvt_pk_bf16_f32 v61, v50, v51\nv_cvt_pk_bf16_f32 v62, v52, v53\nv_cvt_pk_bf16_f32 v63, v54, v55\nv_cvt_pk_bf16_f32 v32, v64, v65\n"
"v_cvt_pk_bf16_f32 v36, v66, v67\nv_xor_b32 v30, 0, v14\nds_write_b128 v30, v[56:59]\nds_write_b128 v30, v[60:63] offset:1024\n"
"s_waitcnt vmcnt(59)\nv_mfma_f32_32x32x16_bf16 v[40:55], a[112:115], v[128:131], 0\ns_xor_b32 s73, s18, 2\ns_lshl_b32 s73, s73, 2\n"
"s_mul_i32 s73, s73, s19\ns_add_u32 s73, s73, s13\nbuffer_load_dwordx4 v[96:99], v9, s[8:11], s73 offen sc1\ns_add_u32 s73, s73, s19\n"
"buffer_load_dwordx4 v[100:103], v9, s[8:11], s73 offen sc1\ns_add_u32 s73, s73, s19\nbuffer_load_dwordx4 v[104:107], v9, s[8:11], s73 offen sc1\ns_add_u32 s73, s73, s19\n"
"buffer_load_dwordx4 v[108:111], v9, s[8:11], s73 offen sc1\ns_xor_b32 s72, s18, 3\ns_mul_i32 s72, s72, s14\ns_add_u32 s72, s72, s12\n"
"buffer_load_dwordx4 a[112:115], v8, s[8:11], s72 offen offset:0 sc1\ns_waitcnt vmcnt(63)\nv_mfma_f32_32x32x16_bf16 v[40:55], a[116:119], v[132:135], v[40:55]\nbuffer_load_dwordx4 a[116:119], v8, s[8:11], s72 offen offset:512 sc1\n"
"s_waitcnt vmcnt(63)\nv_mfma_f32_32x32x16_bf16 v[40:55], a[120:123], v[136:139], v[40:55]\nbuffer_load_dwordx4 a[120:123], v8, s[8:11], s72 offen offset:1024 sc1\ns_waitcnt vmcnt(63)\n"
"v_mfma_f32_32x32x16_bf16 v[40:55], a[124:127], v[140:143], v[40:55]\nbuffer_load_dwordx4 a[124:127], v8, s[8:11], s72 offen offset:1536 sc1\ns_waitcnt vmcnt(63)\nv_mfma_f32_32x32x16_bf16 v[40:55], a[128:131], v[144:147], v[40:55]\n"
"buffer_load_dwordx4 a[128:131], v8, s[8:11], s72 offen offset:2048 sc1\ns_waitcnt vmcnt(63)\nv_mfma_f32_32x32x16_bf16 v[40:55], a[132:135], v[148:151], v[40:55]\nbuffer_load_dwordx4 a[132:135], v8, s[8:11], s72 offen offset:2560 sc1\n"
"s_waitcnt vmcnt(63)\nv_mfma_f32_32x32x16_bf16 v[40:55], a[136:139], v[152:155], v[40:55]\nbuffer_load_dwordx4 a[136:139], v8, s[8:11], s72 offen offset:3072 sc1\ns_waitcnt vmcnt(63)\n"
"v_mfma_f32_32x32x16_bf16 v[40:55], a[140:143], v[156:159], v[40:55]\nbuffer_load_dwordx4 a[140:143], v8, s[8:11], s72 offen offset:3584 sc1\ns_waitcnt vmcnt(63)\nv_mfma_f32_32x32x16_bf16 v[40:55], a[144:147], v[160:163], v[40:55]\n"
"s_add_u32 s72, s72, 4096\nbuffer_load_dwordx4 a[144:147], v8, s[8:11], s72 offen offset:0 sc1\ns_waitcnt vmcnt(63)\nv_mfma_f32_32x32x16_bf16 v[40:55], a[148:151], v[164:167], v[40:55]\n"
"buffer_load_dwordx4 a[148:151], v8, s[8:11], s72 offen offset:512 sc1\ns_waitcnt vmcnt(63)\nv_mfma_f32_32x32x16_bf16 v[40:55], a[152:155], v[168:171], v[40:55]\nbuffer_load_dwordx4 a[152:155], v8, s[8:11], s72 offen offset:1024 sc1\n"
"s_waitcnt vmcnt(63)\nv_mfma_f32_32x32x16_bf16 v[40:55], a[156:159], v[172:175], v[40:55]\nbuffer_load_dwordx4 a[156:159], v8, s[8:11], s72 offen offset:1536 sc1\ns_waitcnt vmcnt(63)\n"
"v_mfma_f32_32x32x16_bf16 v[40:55], a[160:163], v[176:179], v[40:55]\nbuffer_load_dwordx4 a[160:163], v8, s[8:11], s72 offen offset:2048 sc1\ns_waitcnt vmcnt(63)\nv_mfma_f32_32x32x16_bf16 v[40:55], a[164:167], v[180:183], v[40:55]\n"
"buffer_load_dwordx4 a[164:167], v8, s[8:11], s72 offen offset:2560 sc1\ns_waitcnt vmcnt(63)\nv_mfma_f32_32x32x16_bf16 v[40:55], a[168:171], v[184:187], v[40:55]\nbuffer_load_dwordx4 a[168:171], v8, s[8:11], s72 offen offset:3072 sc1\n"
"s_waitcnt vmcnt(63)\nv_mfma_f32_32x32x16_bf16 v[40:55], a[172:175], v[188:191], v[40:55]\nbuffer_load_dwordx4 a[172:175], v8, s[8:11], s72 offen offset:3584 sc1\ns_waitcnt vmcnt(63)\n"
"v_mfma_f32_32x32x16_bf16 v[40:55], a[176:179], v[192:195], v[40:55]\ns_add_u32 s72, s72, 4096\nbuffer_load_dwordx4 a[176:179], v8, s[8:11], s72 offen offset:0 sc1\ns_waitcnt vmcnt(63)\n"
"v_mfma_f32_32x32x16_bf16 v[40:55], a[180:183], v[196:199], v[40:55]\nbuffer_load_dwordx4 a[180:183], v8, s[8:11], s72 offen offset:512 sc1\ns_waitcnt vmcnt(63)\nv_mfma_f32_32x32x16_bf16 v[40:55], a[184:187], v[200:203], v[40:55]\n"
"buffer_load_dwordx4 a[184:187], v8, s[8:11], s72 offen offset:1024 sc1\ns_waitcnt vmcnt(63)\nv_mfma_f32_32x32x16_bf16 v[40:55], a[188:191], v[204:207], v[40:55]\nbuffer_load_dwordx4 a[188:191], v8, s[8:11], s72 offen offset:1536 sc1\n"
"s_waitcnt vmcnt(63)\nv_mfma_f32_32x32x16_bf16 v[40:55], a[192:195], v[208:211], v[40:55]\nbuffer_load_dwordx4 a[192:195], v8, s[8:11], s72 offen offset:2048 sc1\ns_waitcnt vmcnt(63)\n"
"v_mfma_f32_32x32x16_bf16 v[40:55], a[196:199], v[212:215], v[40:55]\nbuffer_load_dwordx4 a[196:199], v8, s[8:11], s72 offen offset:2560 sc1\ns_waitcnt vmcnt(63)\nv_mfma_f32_32x32x16_bf16 v[40:55], a[200:203], v[216:219], v[40:55]\n"
"buffer_load_dwordx4 a[200:203], v8, s[8:11], s72 offen offset:3072 sc1\ns_waitcnt vmcnt(63)\nv_mfma_f32_32x32x16_bf16 v[40:55], a[204:207], v[220:223], v[40:55]\nbuffer_load_dwordx4 a[204:207], v8, s[8:11], s72 offen offset:3584 sc1\n"
"s_waitcnt vmcnt(63)\nv_mfma_f32_32x32x16_bf16 v[40:55], a[208:211], v[224:227], v[40:55]\ns_add_u32 s72, s72, 4096\nbuffer_load_dwordx4 a[208:211], v8, s[8:11], s72 offen offset:0 sc1\n"
"s_waitcnt vmcnt(63)\nv_mfma_f32_32x32x16_bf16 v[40:55], a[212:215], v[228:231], v[40:55]\nbuffer_load_dwordx4 a[212:215], v8, s[8:11], s72 offen offset:512 sc1\ns_waitcnt vmcnt(63)\n"
"v_mfma_f32_32x32x16_bf16 v[40:55], a[216:219], v[232:235], v[40:55]\nbuffer_load_dwordx4 a[216:219], v8, s[8:11], s72 offen offset:1024 sc1\ns_waitcnt vmcnt(63)\nv_mfma_f32_32x32x16_bf16 v[40:55], a[220:223], v[236:239], v[40:55]\n"
"buffer_load_dwordx4 a[220:223], v8, s[8:11], s72 offen offset:1536 sc1\ns_waitcnt vmcnt(60)\nv_add_f32 v80, v80, v84\nv_add_f32 v88, v88, v92\n"
"v_add_f32 v81, v81, v85\nv_add_f32 v89, v89, v93\nv_add_f32 v82, v82, v86\nv_add_f32 v90, v90, v94\n"
"v_add_f32 v83, v83, v87\nv_add_f32 v91, v91, v95\nv_add_f32 v80, v80, v88\nv_add_f32 v81, v81, v89\n"
"v_add_f32 v82, v82, v90\nv_add_f32 v83, v83, v91\nv_max3_f32 v28, v80, v81, v82\nv_max_f32 v28, v28, v83\n"
"v_cndmask_b32 v40, v40, v31, s[40:41]\nv_cndmask_b32 v41, v41, v31, s[42:43]\nv_cndmask_b32 v42, v42, v31, s[44:45]\nv_cndmask_b32 v43, v43, v31, s[46:47]\n"
"v_cndmask_b32 v44, v44, v31, s[48:49]\nv_cndmask_b32 v45, v45, v31, s[50:51]\nv_cndmask_b32 v46, v46, v31, s[52:53]\nv_cndmask_b32 v47, v47, v31, s[54:55]\n"
"v_cndmask_b32 v48, v48, v31, s[56:57]\nv_cndmask_b32 v49, v49, v31, s[58:59]\nv_cndmask_b32 v50, v50, v31, s[60:61]\nv_cndmask_b32 v51, v51, v31, s[62:63]\n"
"v_cndmask_b32 v52, v52, v31, s[64:65]\nv_cndmask_b32 v53, v53, v31, s[66:67]\nv_cndmask_b32 v54, v54, v31, s[68:69]\nv_cndmask_b32 v55, v55, v31, s[70:71]\n"
"v_max3_f32 v28, v28, v40, v41\nv_max3_f32 v28, v28, v42, v43\nv_max3_f32 v28, v28, v44, v45\nv_max3_f32 v28, v28, v46, v47\n"
"v_max3_f32 v28, v28, v48, v49\nv_max3_f32 v28, v28, v50, v51\nv_max3_f32 v28, v28, v52, v53\nv_max3_f32 v28, v28, v54, v55\n"
"v_mov_b32 v30, v28\ns_nop 1\nv_permlane32_swap_b32 v28, v30\nv_max_f32 v28, v28, v30\n"
"v_sub_f32 v40, v40, v28\nv_sub_f32 v41, v41, v28\nv_sub_f32 v42, v42, v28\nv_sub_f32 v43, v43, v28\n"
"v_sub_f32 v44, v44, v28\nv_sub_f32 v45, v45, v28\nv_sub_f32 v46, v46, v28\nv_sub_f32 v47, v47, v28\n"
"v_sub_f32 v48, v48, v28\nv_sub_f32 v49, v49, v28\nv_sub_f32 v50, v50, v28\nv_sub_f32 v51, v51, v28\n"
"v_sub_f32 v52, v52, v28\nv_sub_f32 v53, v53, v28\nv_sub_f32 v54, v54, v28\nv_sub_f32 v55, v55, v28\n"
"v_sub_f32 v80, v80, v28\nv_sub_f32 v81, v81, v28\nv_sub_f32 v82, v82, v28\nv_sub_f32 v83, v83, v28\n"
"v_exp_f32 v40, v40\nv_exp_f32 v41, v41\nv_exp_f32 v42, v42\nv_exp_f32 v43, v43\n"
"v_exp_f32 v44, v44\nv_exp_f32 v45, v45\nv_exp_f32 v46, v46\nv_exp_f32 v47, v47\n"
"v_exp_f32 v48, v48\nv_exp_f32 v49, v49\nv_exp_f32 v50, v50\nv_exp_f32 v51, v51\n"
"v_exp_f32 v52, v52\nv_exp_f32 v53, v53\nv_exp_f32 v54, v54\nv_exp_f32 v55, v55\n"
"v_exp_f32 v80, v80\nv_exp_f32 v81, v81\nv_exp_f32 v82, v82\nv_exp_f32 v83, v83\n"
"v_add_f32 v29, v40, v41\nv_add_f32 v30, v42, v43\nv_add_f32 v29, v29, v44\nv_add_f32 v30, v30, v45\n"
"v_add_f32 v29, v29, v46\nv_add_f32 v30, v30, v47\nv_add_f32 v29, v29, v48\nv_add_f32 v30, v30, v49\n"
"v_add_f32 v29, v29, v50\nv_add_f32 v30, v30, v51\nv_add_f32 v29, v29, v52\nv_add_f32 v30, v30, v53\n"
"v_add_f32 v29, v29, v54\nv_add_f32 v30, v30, v55\nv_add_f32 v29, v29, v80\nv_add_f32 v30, v30, v81\n"
"v_add_f32 v29, v29, v82\nv_add_f32 v30, v30, v83\nv_add_f32 v29, v29, v30\nv_mov_b32 v30, v29\n"
"s_nop 1\nv_permlane32_swap_b32 v29, v30\nv_add_f32 v29, v29, v30\nv_rcp_f32 v29, v29\n"
"s_nop 0\nv_mul_f32 v40, v40, v29\nv_mul_f32 v41, v41, v29\nv_mul_f32 v42, v42, v29\n"
"v_mul_f32 v43, v43, v29\nv_mul_f32 v44, v44, v29\nv_mul_f32 v45, v45, v29\nv_mul_f32 v46, v46, v29\n"
"v_mul_f32 v47, v47, v29\nv_mul_f32 v48, v48, v29\nv_mul_f32 v49, v49, v29\nv_mul_f32 v50, v50, v29\n"
"v_mul_f32 v51, v51, v29\nv_mul_f32 v52, v52, v29\nv_mul_f32 v53, v53, v29\nv_mul_f32 v54, v54, v29\n"
"v_mul_f32 v55, v55, v29\nv_mul_f32 v80, v80, v29\nv_mul_f32 v81, v81, v29\nv_mul_f32 v82, v82, v29\n"
"v_mul_f32 v83, v83, v29\nv_cvt_pk_bf16_f32 v56, v40, v41\nv_cvt_pk_bf16_f32 v57, v42, v43\nv_cvt_pk_bf16_f32 v58, v44, v45\n"
"v_cvt_pk_bf16_f32 v59, v46, v47\nv_cvt_pk_bf16_f32 v60, v48, v49\nv_cvt_pk_bf16_f32 v61, v50, v51\nv_cvt_pk_bf16_f32 v62, v52, v53\n"
"v_cvt_pk_bf16_f32 v63, v54, v55\nv_cvt_pk_bf16_f32 v33, v80, v81\nv_cvt_pk_bf16_f32 v37, v82, v83\nv_xor_b32 v30, 2048, v14\n"
"ds_write_b128 v30, v[56:59]\nds_write_b128 v30, v[60:63] offset:1024\ns_waitcnt vmcnt(59)\nv_mfma_f32_32x32x16_bf16 v[40:55], a[0:3], v[128:131], 0\n"
"s_xor_b32 s73, s18, 3\ns_lshl_b32 s73, s73, 2\ns_mul_i32 s73, s73, s19\ns_add_u32 s73, s73, s13\n"
"buffer_load_dwordx4 v[112:115], v9, s[8:11], s73 offen sc1\ns_add_u32 s73, s73, s19\nbuffer_load_dwordx4 v[116:119], v9, s[8:11], s73 offen sc1\ns_add_u32 s73, s73, s19\n"
"buffer_load_dwordx4 v[120:123], v9, s[8:11], s73 offen sc1\ns_add_u32 s73, s73, s19\nbuffer_load_dwordx4 v[124:127], v9, s[8:11], s73 offen sc1\nbuffer_load_dwordx4 a[0:3], v10, s[8:11], s76 offen offset:0 sc1\n"
"s_waitcnt vmcnt(63)\nv_mfma_f32_32x32x16_bf16 v[40:55], a[4:7], v[132:135], v[40:55]\nbuffer_load_dwordx4 a[4:7], v10, s[8:11], s76 offen offset:512 sc1\ns_waitcnt vmcnt(63)\n"
"v_mfma_f32_32x32x16_bf16 v[40:55], a[8:11], v[136:139], v[40:55]\nbuffer_load_dwordx4 a[8:11], v10, s[8:11], s76 offen offset:1024 sc1\ns_waitcnt vmcnt(63)\nv_mfma_f32_32x32x16_bf16 v[40:55], a[12:15], v[140:143], v[40:55]\n"
"buffer_load_dwordx4 a[12:15], v10, s[8:11], s76 offen offset:1536 sc1\ns_waitcnt vmcnt(63)\nv_mfma_f32_32x32x16_bf16 v[40:55], a[16:19], v[144:147], v[40:55]\nbuffer_load_dwordx4 a[16:19], v10, s[8:11], s76 offen offset:2048 sc1\n"
"s_waitcnt vmcnt(63)\nv_mfma_f32_32x32x16_bf16 v[40:55], a[20:23], v[148:151], v[40:55]\nbuffer_load_dwordx4 a[20:23], v10, s[8:11], s76 offen offset:2560 sc1\ns_waitcnt vmcnt(63)\n"
"v_mfma_f32_32x32x16_bf16 v[40:55], a[24:27], v[152:155], v[40:55]\nbuffer_load_dwordx4 a[24:27], v10, s[8:11], s76 offen offset:3072 sc1\ns_waitcnt vmcnt(63)\nv_mfma_f32_32x32x16_bf16 v[40:55], a[28:31], v[156:159], v[40:55]\n"
"buffer_load_dwordx4 a[28:31], v10, s[8:11], s76 offen offset:3584 sc1\ns_waitcnt vmcnt(63)\nv_mfma_f32_32x32x16_bf16 v[40:55], a[32:35], v[160:163], v[40:55]\nbuffer_load_dwordx4 a[32:35], v10, s[8:11], s77 offen offset:0 sc1\n"
"s_waitcnt vmcnt(63)\nv_mfma_f32_32x32x16_bf16 v[40:55], a[36:39], v[164:167], v[40:55]\nbuffer_load_dwordx4 a[36:39], v10, s[8:11], s77 offen offset:512 sc1\ns_waitcnt vmcnt(63)\n"
"v_mfma_f32_32x32x16_bf16 v[40:55], a[40:43], v[168:171], v[40:55]\nbuffer_load_dwordx4 a[40:43], v10, s[8:11], s77 offen offset:1024 sc1\ns_waitcnt vmcnt(63)\nv_mfma_f32_32x32x16_bf16 v[40:55], a[44:47], v[172:175], v[40:55]\n"
"buffer_load_dwordx4 a[44:47], v10, s[8:11], s77 offen offset:1536 sc1\ns_waitcnt vmcnt(63)\nv_mfma_f32_32x32x16_bf16 v[40:55], a[48:51], v[176:179], v[40:55]\nbuffer_load_dwordx4 a[48:51], v10, s[8:11], s77 offen offset:2048 sc1\n"
"s_waitcnt vmcnt(63)\nv_mfma_f32_32x32x16_bf16 v[40:55], a[52:55], v[180:183], v[40:55]\nbuffer_load_dwordx4 a[52:55], v10, s[8:11], s77 offen offset:2560 sc1\ns_waitcnt vmcnt(63)\n"
"v_mfma_f32_32x32x16_bf16 v[40:55], a[56:59], v[184:187], v[40:55]\nbuffer_load_dwordx4 a[56:59], v10, s[8:11], s77 offen offset:3072 sc1\ns_waitcnt vmcnt(63)\nv_mfma_f32_32x32x16_bf16 v[40:55], a[60:63], v[188:191], v[40:55]\n"
"buffer_load_dwordx4 a[60:63], v10, s[8:11], s77 offen offset:3584 sc1\ns_waitcnt vmcnt(63)\nv_mfma_f32_32x32x16_bf16 v[40:55], a[64:67], v[192:195], v[40:55]\nbuffer_load_dwordx4 a[64:67], v10, s[8:11], s78 offen offset:0 sc1\n"
"s_waitcnt vmcnt(63)\nv_mfma_f32_32x32x16_bf16 v[40:55], a[68:71], v[196:199], v[40:55]\nbuffer_load_dwordx4 a[68:71], v10, s[8:11], s78 offen offset:512 sc1\ns_waitcnt vmcnt(63)\n"
"v_mfma_f32_32x32x16_bf16 v[40:55], a[72:75], v[200:203], v[40:55]\nbuffer_load_dwordx4 a[72:75], v10, s[8:11], s78 offen offset:1024 sc1\ns_waitcnt vmcnt(63)\nv_mfma_f32_32x32x16_bf16 v[40:55], a[76:79], v[204:207], v[40:55]\n"
"buffer_load_dwordx4 a[76:79], v10, s[8:11], s78 offen offset:1536 sc1\ns_waitcnt vmcnt(63)\nv_mfma_f32_32x32x16_bf16 v[40:55], a[80:83], v[208:211], v[40:55]\nbuffer_load_dwordx4 a[80:83], v10, s[8:11], s78 offen offset:2048 sc1\n"
"s_waitcnt vmcnt(63)\nv_mfma_f32_32x32x16_bf16 v[40:55], a[84:87], v[212:215], v[40:55]\nbuffer_load_dwordx4 a[84:87], v10, s[8:11], s78 offen offset:2560 sc1\ns_waitcnt vmcnt(63)\n"
"v_mfma_f32_32x32x16_bf16 v[40:55], a[88:91], v[216:219], v[40:55]\nbuffer_load_dwordx4 a[88:91], v10, s[8:11], s78 offen offset:3072 sc1\ns_waitcnt vmcnt(63)\nv_mfma_f32_32x32x16_bf16 v[40:55], a[92:95], v[220:223], v[40:55]\n"
"buffer_load_dwordx4 a[92:95], v10, s[8:11], s78 offen offset:3584 sc1\ns_waitcnt vmcnt(63)\nv_mfma_f32_32x32x16_bf16 v[40:55], a[96:99], v[224:227], v[40:55]\nbuffer_load_dwordx4 a[96:99], v10, s[8:11], s79 offen offset:0 sc1\n"
"s_waitcnt vmcnt(63)\nv_mfma_f32_32x32x16_bf16 v[40:55], a[100:103], v[228:231], v[40:55]\nbuffer_load_dwordx4 a[100:103], v10, s[8:11], s79 offen offset:512 sc1\ns_waitcnt vmcnt(63)\n"
"v_mfma_f32_32x32x16_bf16 v[40:55], a[104:107], v[232:235], v[40:55]\nbuffer_load_dwordx4 a[104:107], v10, s[8:11], s79 offen offset:1024 sc1\ns_waitcnt vmcnt(63)\nv_mfma_f32_32x32x16_bf16 v[40:55], a[108:111], v[236:239], v[40:55]\n"
"buffer_load_dwordx4 a[108:111], v10, s[8:11], s79 offen offset:1536 sc1\ns_waitcnt vmcnt(60)\nv_add_f32 v96, v96, v100\nv_add_f32 v104, v104, v108\n"
"v_add_f32 v97, v97, v101\nv_add_f32 v105, v105, v109\nv_add_f32 v98, v98, v102\nv_add_f32 v106, v106, v110\n"
"v_add_f32 v99, v99, v103\nv_add_f32 v107, v107, v111\nv_add_f32 v96, v96, v104\nv_add_f32 v97, v97, v105\n"
"v_add_f32 v98, v98, v106\nv_add_f32 v99, v99, v107\nv_max3_f32 v28, v96, v97, v98\nv_max_f32 v28, v28, v99\n"
"v_cndmask_b32 v40, v40, v31, s[40:41]\nv_cndmask_b32 v41, v41, v31, s[42:43]\nv_cndmask_b32 v42, v42, v31, s[44:45]\nv_cndmask_b32 v43, v43, v31, s[46:47]\n"
"v_cndmask_b32 v44, v44, v31, s[48:49]\nv_cndmask_b32 v45, v45, v31, s[50:51]\nv_cndmask_b32 v46, v46, v31, s[52:53]\nv_cndmask_b32 v47, v47, v31, s[54:55]\n"
"v_cndmask_b32 v48, v48, v31, s[56:57]\nv_cndmask_b32 v49, v49, v31, s[58:59]\nv_cndmask_b32 v50, v50, v31, s[60:61]\nv_cndmask_b32 v51, v51, v31, s[62:63]\n"
"v_cndmask_b32 v52, v52, v31, s[64:65]\nv_cndmask_b32 v53, v53, v31, s[66:67]\nv_cndmask_b32 v54, v54, v31, s[68:69]\nv_cndmask_b32 v55, v55, v31, s[70:71]\n"
"v_max3_f32 v28, v28, v40, v41\nv_max3_f32 v28, v28, v42, v43\nv_max3_f32 v28, v28, v44, v45\nv_max3_f32 v28, v28, v46, v47\n"
"v_max3_f32 v28, v28, v48, v49\nv_max3_f32 v28, v28, v50, v51\nv_max3_f32 v28, v28, v52, v53\nv_max3_f32 v28, v28, v54, v55\n"
"v_mov_b32 v30, v28\ns_nop 1\nv_permlane32_swap_b32 v28, v30\nv_max_f32 v28, v28, v30\n"
"v_sub_f32 v40, v40, v28\nv_sub_f32 v41, v41, v28\nv_sub_f32 v42, v42, v28\nv_sub_f32 v43, v43, v28\n"
"v_sub_f32 v44, v44, v28\nv_sub_f32 v45, v45, v28\nv_sub_f32 v46, v46, v28\nv_sub_f32 v47, v47, v28\n"
"v_sub_f32 v48, v48, v28\nv_sub_f32 v49, v49, v28\nv_sub_f32 v50, v50, v28\nv_sub_f32 v51, v51, v28\n"
"v_sub_f32 v52, v52, v28\nv_sub_f32 v53, v53, v28\nv_sub_f32 v54, v54, v28\nv_sub_f32 v55, v55, v28\n"
"v_sub_f32 v96, v96, v28\nv_sub_f32 v97, v97, v28\nv_sub_f32 v98, v98, v28\nv_sub_f32 v99, v99, v28\n"
"v_exp_f32 v40, v40\nv_exp_f32 v41, v41\nv_exp_f32 v42, v42\nv_exp_f32 v43, v43\n"
"v_exp_f32 v44, v44\nv_exp_f32 v45, v45\nv_exp_f32 v46, v46\nv_exp_f32 v47, v47\n"
"v_exp_f32 v48, v48\nv_exp_f32 v49, v49\nv_exp_f32 v50, v50\nv_exp_f32 v51, v51\n"
"v_exp_f32 v52, v52\nv_exp_f32 v53, v53\nv_exp_f32 v54, v54\nv_exp_f32 v55, v55\n"
"v_exp_f32 v96, v96\nv_exp_f32 v97, v97\nv_exp_f32 v98, v98\nv_exp_f32 v99, v99\n"
"v_add_f32 v29, v40, v41\nv_add_f32 v30, v42, v43\nv_add_f32 v29, v29, v44\nv_add_f32 v30, v30, v45\n"
"v_add_f32 v29, v29, v46\nv_add_f32 v30, v30, v47\nv_add_f32 v29, v29, v48\nv_add_f32 v30, v30, v49\n"
"v_add_f32 v29, v29, v50\nv_add_f32 v30, v30, v51\nv_add_f32 v29, v29, v52\nv_add_f32 v30, v30, v53\n"
"v_add_f32 v29, v29, v54\nv_add_f32 v30, v30, v55\nv_add_f32 v29, v29, v96\nv_add_f32 v30, v30, v97\n"
"v_add_f32 v29, v29, v98\nv_add_f32 v30, v30, v99\nv_add_f32 v29, v29, v30\nv_mov_b32 v30, v29\n"
"s_nop 1\nv_permlane32_swap_b32 v29, v30\nv_add_f32 v29, v29, v30\nv_rcp_f32 v29, v29\n"
"s_nop 0\nv_mul_f32 v40, v40, v29\nv_mul_f32 v41, v41, v29\nv_mul_f32 v42, v42, v29\n"
"v_mul_f32 v43, v43, v29\nv_mul_f32 v44, v44, v29\nv_mul_f32 v45, v45, v29\nv_mul_f32 v46, v46, v29\n"
"v_mul_f32 v47, v47, v29\nv_mul_f32 v48, v48, v29\nv_mul_f32 v49, v49, v29\nv_mul_f32 v50, v50, v29\n"
"v_mul_f32 v51, v51, v29\nv_mul_f32 v52, v52, v29\nv_mul_f32 v53, v53, v29\nv_mul_f32 v54, v54, v29\n"
"v_mul_f32 v55, v55, v29\nv_mul_f32 v96, v96, v29\nv_mul_f32 v97, v97, v29\nv_mul_f32 v98, v98, v29\n"
"v_mul_f32 v99, v99, v29\nv_cvt_pk_bf16_f32 v56, v40, v41\nv_cvt_pk_bf16_f32 v57, v42, v43\nv_cvt_pk_bf16_f32 v58, v44, v45\n"
"v_cvt_pk_bf16_f32 v59, v46, v47\nv_cvt_pk_bf16_f32 v60, v48, v49\nv_cvt_pk_bf16_f32 v61, v50, v51\nv_cvt_pk_bf16_f32 v62, v52, v53\n"
"v_cvt_pk_bf16_f32 v63, v54, v55\nv_cvt_pk_bf16_f32 v34, v96, v97\nv_cvt_pk_bf16_f32 v38, v98, v99\nv_xor_b32 v30, 4096, v14\n"
"ds_write_b128 v30, v[56:59]\nds_write_b128 v30, v[60:63] offset:1024\ns_waitcnt vmcnt(59)\nv_mfma_f32_32x32x16_bf16 v[40:55], a[112:115], v[128:131], 0\n"
"buffer_load_dwordx4 a[112:115], v10, s[8:11], s79 offen offset:2048 sc1\ns_waitcnt vmcnt(59)\nv_mfma_f32_32x32x16_bf16 v[40:55], a[116:119], v[132:135], v[40:55]\nbuffer_load_dwordx4 a[116:119], v10, s[8:11], s79 offen offset:2560 sc1\n"
"s_waitcnt vmcnt(59)\nv_mfma_f32_32x32x16_bf16 v[40:55], a[120:123], v[136:139], v[40:55]\nbuffer_load_dwordx4 a[120:123], v10, s[8:11], s79 offen offset:3072 sc1\ns_waitcnt vmcnt(59)\n"
"v_mfma_f32_32x32x16_bf16 v[40:55], a[124:127], v[140:143], v[40:55]\nbuffer_load_dwordx4 a[124:127], v10, s[8:11], s79 offen offset:3584 sc1\ns_waitcnt vmcnt(59)\nv_mfma_f32_32x32x16_bf16 v[40:55], a[128:131], v[144:147], v[40:55]\n"
"buffer_load_dwordx4 a[128:131], v11, s[8:11], s80 offen offset:0 sc1\ns_waitcnt vmcnt(59)\nv_mfma_f32_32x32x16_bf16 v[40:55], a[132:135], v[148:151], v[40:55]\nbuffer_load_dwordx4 a[132:135], v11, s[8:11], s80 offen offset:256 sc1\n"
"s_waitcnt vmcnt(59)\nv_mfma_f32_32x32x16_bf16 v[40:55], a[136:139], v[152:155], v[40:55]\nbuffer_load_dwordx4 a[136:139], v11, s[8:11], s80 offen offset:1024 sc1\ns_waitcnt vmcnt(59)\n"
"v_mfma_f32_32x32x16_bf16 v[40:55], a[140:143], v[156:159], v[40:55]\nbuffer_load_dwordx4 a[140:143], v11, s[8:11], s80 offen offset:1280 sc1\ns_waitcnt vmcnt(59)\nv_mfma_f32_32x32x16_bf16 v[40:55], a[144:147], v[160:163], v[40:55]\n"
"buffer_load_dwordx4 a[144:147], v11, s[8:11], s80 offen offset:2048 sc1\ns_waitcnt vmcnt(59)\nv_mfma_f32_32x32x16_bf16 v[40:55], a[148:151], v[164:167], v[40:55]\nbuffer_load_dwordx4 a[148:151], v11, s[8:11], s80 offen offset:2304 sc1\n"
"s_waitcnt vmcnt(59)\nv_mfma_f32_32x32x16_bf16 v[40:55], a[152:155], v[168:171], v[40:55]\nbuffer_load_dwordx4 a[152:155], v11, s[8:11], s80 offen offset:3072 sc1\ns_waitcnt vmcnt(59)\n"
"v_mfma_f32_32x32x16_bf16 v[40:55], a[156:159], v[172:175], v[40:55]\nbuffer_load_dwordx4 a[156:159], v11, s[8:11], s80 offen offset:3328 sc1\ns_waitcnt vmcnt(59)\nv_mfma_f32_32x32x16_bf16 v[40:55], a[160:163], v[176:179], v[40:55]\n"
"s_waitcnt vmcnt(58)\nv_mfma_f32_32x32x16_bf16 v[40:55], a[164:167], v[180:183], v[40:55]\ns_waitcnt vmcnt(57)\nv_mfma_f32_32x32x16_bf16 v[40:55], a[168:171], v[184:187], v[40:55]\n"
"s_waitcnt vmcnt(56)\nv_mfma_f32_32x32x16_bf16 v[40:55], a[172:175], v[188:191], v[40:55]\ns_waitcnt vmcnt(55)\nv_mfma_f32_32x32x16_bf16 v[40:55], a[176:179], v[192:195], v[40:55]\n"
"s_waitcnt vmcnt(54)\nv_mfma_f32_32x32x16_bf16 v[40:55], a[180:183], v[196:199], v[40:55]\ns_waitcnt vmcnt(53)\nv_mfma_f32_32x32x16_bf16 v[40:55], a[184:187], v[200:203], v[40:55]\n"
"s_waitcnt vmcnt(52)\nv_mfma_f32_32x32x16_bf16 v[40:55], a[188:191], v[204:207], v[40:55]\ns_waitcnt vmcnt(51)\nv_mfma_f32_32x32x16_bf16 v[40:55], a[192:195], v[208:211], v[40:55]\n"
"s_waitcnt vmcnt(50)\nv_mfma_f32_32x32x16_bf16 v[40:55], a[196:199], v[212:215], v[40:55]\ns_waitcnt vmcnt(49)\nv_mfma_f32_32x32x16_bf16 v[40:55], a[200:203], v[216:219], v[40:55]\n"
"s_waitcnt vmcnt(48)\nv_mfma_f32_32x32x16_bf16 v[40:55], a[204:207], v[220:223], v[40:55]\ns_waitcnt vmcnt(47)\nv_mfma_f32_32x32x16_bf16 v[40:55], a[208:211], v[224:227], v[40:55]\n"
"s_waitcnt vmcnt(46)\nv_mfma_f32_32x32x16_bf16 v[40:55], a[212:215], v[228:231], v[40:55]\ns_waitcnt vmcnt(45)\nv_mfma_f32_32x32x16_bf16 v[40:55], a[216:219], v[232:235], v[40:55]\n"
"s_waitcnt vmcnt(44)\nv_mfma_f32_32x32x16_bf16 v[40:55], a[220:223], v[236:239], v[40:55]\ns_waitcnt vmcnt(40)\nv_add_f32 v112, v112, v116\n"
"v_add_f32 v120, v120, v124\nv_add_f32 v113, v113, v117\nv_add_f32 v121, v121, v125\nv_add_f32 v114, v114, v118\n"
"v_add_f32 v122, v122, v126\nv_add_f32 v115, v115, v119\nv_add_f32 v123, v123, v127\nv_add_f32 v112, v112, v120\n"
"v_add_f32 v113, v113, v121\nv_add_f32 v114, v114, v122\nv_add_f32 v115, v115, v123\nv_max3_f32 v28, v112, v113, v114\n"
"v_max_f32 v28, v28, v115\nv_cndmask_b32 v40, v40, v31, s[40:41]\nv_cndmask_b32 v41, v41, v31, s[42:43]\nv_cndmask_b32 v42, v42, v31, s[44:45]\n"
"v_cndmask_b32 v43, v43, v31, s[46:47]\nv_cndmask_b32 v44, v44, v31, s[48:49]\nv_cndmask_b32 v45, v45, v31, s[50:51]\nv_cndmask_b32 v46, v46, v31, s[52:53]\n"
"v_cndmask_b32 v47, v47, v31, s[54:55]\nv_cndmask_b32 v48, v48, v31, s[56:57]\nv_cndmask_b32 v49, v49, v31, s[58:59]\nv_cndmask_b32 v50, v50, v31, s[60:61]\n"
"v_cndmask_b32 v51, v51, v31, s[62:63]\nv_cndmask_b32 v52, v52, v31, s[64:65]\nv_cndmask_b32 v53, v53, v31, s[66:67]\nv_cndmask_b32 v54, v54, v31, s[68:69]\n"
"v_cndmask_b32 v55, v55, v31, s[70:71]\nv_max3_f32 v28, v28, v40, v41\nv_max3_f32 v28, v28, v42, v43\nv_max3_f32 v28, v28, v44, v45\n"
"v_max3_f32 v28, v28, v46, v47\nv_max3_f32 v28, v28, v48, v49\nv_max3_f32 v28, v28, v50, v51\nv_max3_f32 v28, v28, v52, v53\n"
"v_max3_f32 v28, v28, v54, v55\nv_mov_b32 v30, v28\ns_nop 1\nv_permlane32_swap_b32 v28, v30\n"
"v_max_f32 v28, v28, v30\nv_sub_f32 v40, v40, v28\nv_sub_f32 v41, v41, v28\nv_sub_f32 v42, v42, v28\n"
"v_sub_f32 v43, v43, v28\nv_sub_f32 v44, v44, v28\nv_sub_f32 v45, v45, v28\nv_sub_f32 v46, v46, v28\n"
"v_sub_f32 v47, v47, v28\nv_sub_f32 v48, v48, v28\nv_sub_f32 v49, v49, v28\nv_sub_f32 v50, v50, v28\n"
"v_sub_f32 v51, v51, v28\nv_sub_f32 v52, v52, v28\nv_sub_f32 v53, v53, v28\nv_sub_f32 v54, v54, v28\n"
"v_sub_f32 v55, v55, v28\nv_sub_f32 v112, v112, v28\nv_sub_f32 v113, v113, v28\nv_sub_f32 v114, v114, v28\n"
"v_sub_f32 v115, v115, v28\nv_exp_f32 v40, v40\nv_exp_f32 v41, v41\nv_exp_f32 v42, v42\n"
"v_exp_f32 v43, v43\nv_exp_f32 v44, v44\nv_exp_f32 v45, v45\nv_exp_f32 v46, v46\n"
"v_exp_f32 v47, v47\nv_exp_f32 v48, v48\nv_exp_f32 v49, v49\nv_exp_f32 v50, v50\n"
"v_exp_f32 v51, v51\nv_exp_f32 v52, v52\nv_exp_f32 v53, v53\nv_exp_f32 v54, v54\n"
"v_exp_f32 v55, v55\nv_exp_f32 v112, v112\nv_exp_f32 v113, v113\nv_exp_f32 v114, v114\n"
"v_exp_f32 v115, v115\nv_add_f32 v29, v40, v41\nv_add_f32 v30, v42, v43\nv_add_f32 v29, v29, v44\n"
"v_add_f32 v30, v30, v45\nv_add_f32 v29, v29, v46\nv_add_f32 v30, v30, v47\nv_add_f32 v29, v29, v48\n"
"v_add_f32 v30, v30, v49\nv_add_f32 v29, v29, v50\nv_add_f32 v30, v30, v51\nv_add_f32 v29, v29, v52\n"
"v_add_f32 v30, v30, v53\nv_add_f32 v29, v29, v54\nv_add_f32 v30, v30, v55\nv_add_f32 v29, v29, v112\n"
"v_add_f32 v30, v30, v113\nv_add_f32 v29, v29, v114\nv_add_f32 v30, v30, v115\nv_add_f32 v29, v29, v30\n"
"v_mov_b32 v30, v29\ns_nop 1\nv_permlane32_swap_b32 v29, v30\nv_add_f32 v29, v29, v30\n"
"v_rcp_f32 v29, v29\ns_nop 0\nv_mul_f32 v40, v40, v29\nv_mul_f32 v41, v41, v29\n"
"v_mul_f32 v42, v42, v29\nv_mul_f32 v43, v43, v29\nv_mul_f32 v44, v44, v29\nv_mul_f32 v45, v45, v29\n"
"v_mul_f32 v46, v46, v29\nv_mul_f32 v47, v47, v29\nv_mul_f32 v48, v48, v29\nv_mul_f32 v49, v49, v29\n"
"v_mul_f32 v50, v50, v29\nv_mul_f32 v51, v51, v29\nv_mul_f32 v52, v52, v29\nv_mul_f32 v53, v53, v29\n"
"v_mul_f32 v54, v54, v29\nv_mul_f32 v55, v55, v29\nv_mul_f32 v112, v112, v29\nv_mul_f32 v113, v113, v29\n"
"v_mul_f32 v114, v114, v29\nv_mul_f32 v115, v115, v29\nv_cvt_pk_bf16_f32 v56, v40, v41\nv_cvt_pk_bf16_f32 v57, v42, v43\n"
"v_cvt_pk_bf16_f32 v58, v44, v45\nv_cvt_pk_bf16_f32 v59, v46, v47\nv_cvt_pk_bf16_f32 v60, v48, v49\nv_cvt_pk_bf16_f32 v61, v50, v51\n"
"v_cvt_pk_bf16_f32 v62, v52, v53\nv_cvt_pk_bf16_f32 v63, v54, v55\nv_cvt_pk_bf16_f32 v35, v112, v113\nv_cvt_pk_bf16_f32 v39, v114, v115\n"
"v_xor_b32 v30, 6144, v14\nds_write_b128 v30, v[56:59]\nds_write_b128 v30, v[60:63] offset:1024\nds_write_b128 v15, v[32:35]\n"
"ds_write_b128 v15, v[36:39] offset:1024\ns_waitcnt lgkmcnt(0)\ns_barrier\nds_read_b128 v[96:99], v16 offset:0\n"
"ds_read_b128 v[100:103], v16 offset:1024\nds_read_b128 v[104:107], v16 offset:2048\nds_read_b128 v[108:111], v16 offset:3072\nds_read_b128 v[112:115], v16 offset:4096\n"
"ds_read_b128 v[116:119], v16 offset:5120\nds_read_b128 v[120:123], v16 offset:6144\nds_read_b128 v[124:127], v16 offset:7168\nds_read_b128 v[128:131], v16 offset:8192\n"
"ds_read_b128 v[132:135], v16 offset:9216\nds_read_b128 v[136:139], v16 offset:10240\nds_read_b128 v[140:143], v16 offset:11264\ns_waitcnt lgkmcnt(0)\n"
"ds_read_b128 v[144:147], v16 offset:12288\nds_read_b128 v[148:151], v16 offset:13312\nds_read_b128 v[152:155], v16 offset:14336\nds_read_b128 v[156:159], v16 offset:15360\n"
"ds_read_b128 v[160:163], v16 offset:16384\nds_read_b128 v[164:167], v16 offset:17408\nds_read_b128 v[168:171], v16 offset:18432\nds_read_b128 v[172:175], v16 offset:19456\n"
"ds_read_b128 v[176:179], v16 offset:20480\nds_read_b128 v[180:183], v16 offset:21504\nds_read_b128 v[184:187], v16 offset:22528\nds_read_b128 v[188:191], v16 offset:23552\n"
"s_waitcnt lgkmcnt(0)\nds_read_b128 v[192:195], v16 offset:24576\nds_read_b128 v[196:199], v16 offset:25600\nds_read_b128 v[200:203], v16 offset:26624\n"
"ds_read_b128 v[204:207], v16 offset:27648\nds_read_b128 v[208:211], v16 offset:28672\nds_read_b128 v[212:215], v16 offset:29696\nds_read_b128 v[216:219], v16 offset:30720\n"
"ds_read_b128 v[220:223], v16 offset:31744\nds_read_b128 v[224:227], v16 offset:32768\nds_read_b128 v[228:231], v16 offset:33792\nds_read_b128 v[232:235], v16 offset:34816\n"
"ds_read_b128 v[236:239], v16 offset:35840\ns_waitcnt lgkmcnt(0)\nds_read_b128 v[240:243], v16 offset:36864\nds_read_b128 v[244:247], v16 offset:37888\n"
"ds_read_b128 v[248:251], v16 offset:38912\nds_read_b128 v[252:255], v16 offset:39936\nv_mov_b32 v24, v17\ns_sub_u32 s72, s17, 1\n"
"s_waitcnt lgkmcnt(0)\nfo_loop:\ns_add_u32 s76, s76, s20\ns_add_u32 s77, s77, s20\n"
"s_add_u32 s78, s78, s20\ns_add_u32 s79, s79, s20\ns_add_u32 s80, s80, s21\ns_waitcnt vmcnt(39)\n"
"v_mfma_f32_32x32x16_bf16 v[40:55], a[0:3], v[96:99], 0\nbuffer_load_dwordx4 a[0:3], v10, s[8:11], s76 offen offset:0 sc1\ns_waitcnt vmcnt(39)\nv_mfma_f32_32x32x16_bf16 v[40:55], a[4:7], v[100:103], v[40:55]\n"
"buffer_load_dwordx4 a[4:7], v10, s[8:11], s76 offen offset:512 sc1\ns_waitcnt vmcnt(39)\nv_mfma_f32_32x32x16_bf16 v[40:55], a[8:11], v[104:107], v[40:55]\nbuffer_load_dwordx4 a[8:11], v10, s[8:11], s76 offen offset:1024 sc1\n"
"s_waitcnt vmcnt(39)\nv_mfma_f32_32x32x16_bf16 v[40:55], a[12:15], v[108:111], v[40:55]\nbuffer_load_dwordx4 a[12:15], v10, s[8:11], s76 offen offset:1536 sc1\ns_waitcnt vmcnt(39)\n"
"v_mfma_f32_32x32x16_bf16 v[40:55], a[16:19], v[112:115], v[40:55]\nbuffer_load_dwordx4 a[16:19], v10, s[8:11], s76 offen offset:2048 sc1\ns_waitcnt vmcnt(39)\nv_mfma_f32_32x32x16_bf16 v[40:55], a[20:23], v[116:119], v[40:55]\n"
"buffer_load_dwordx4 a[20:23], v10, s[8:11], s76 offen offset:2560 sc1\ns_waitcnt vmcnt(39)\nv_mfma_f32_32x32x16_bf16 v[40:55], a[24:27], v[120:123], v[40:55]\nbuffer_load_dwordx4 a[24:27], v10, s[8:11], s76 offen offset:3072 sc1\n"
"s_waitcnt vmcnt(39)\nv_mfma_f32_32x32x16_bf16 v[40:55], a[28:31], v[124:127], v[40:55]\nbuffer_load_dwordx4 a[28:31], v10, s[8:11], s76 offen offset:3584 sc1\ns_waitcnt vmcnt(39)\n"
"v_mfma_f32_32x32x16_bf16 v[40:55], a[32:35], v[128:131], v[40:55]\nbuffer_load_dwordx4 a[32:35], v10, s[8:11], s77 offen offset:0 sc1\ns_waitcnt vmcnt(39)\nv_mfma_f32_32x32x16_bf16 v[40:55], a[36:39], v[132:135], v[40:55]\n"
"buffer_load_dwordx4 a[36:39], v10, s[8:11], s77 offen offset:512 sc1\ns_waitcnt vmcnt(39)\nv_mfma_f32_32x32x16_bf16 v[40:55], a[40:43], v[136:139], v[40:55]\nbuffer_load_dwordx4 a[40:43], v10, s[8:11], s77 offen offset:1024 sc1\n"
"s_waitcnt vmcnt(39)\nv_mfma_f32_32x32x16_bf16 v[40:55], a[44:47], v[140:143], v[40:55]\nbuffer_load_dwordx4 a[44:47], v10, s[8:11], s77 offen offset:1536 sc1\ns_waitcnt vmcnt(39)\n"
"v_mfma_f32_32x32x16_bf16 v[40:55], a[48:51], v[144:147], v[40:55]\nbuffer_load_dwordx4 a[48:51], v10, s[8:11], s77 offen offset:2048 sc1\ns_waitcnt vmcnt(39)\nv_mfma_f32_32x32x16_bf16 v[40:55], a[52:55], v[148:151], v[40:55]\n"
"buffer_load_dwordx4 a[52:55], v10, s[8:11], s77 offen offset:2560 sc1\ns_waitcnt vmcnt(39)\nv_mfma_f32_32x32x16_bf16 v[40:55], a[56:59], v[152:155], v[40:55]\nbuffer_load_dwordx4 a[56:59], v10, s[8:11], s77 offen offset:3072 sc1\n"
"s_waitcnt vmcnt(39)\nv_mfma_f32_32x32x16_bf16 v[40:55], a[60:63], v[156:159], v[40:55]\nbuffer_load_dwordx4 a[60:63], v10, s[8:11], s77 offen offset:3584 sc1\ns_waitcnt vmcnt(39)\n"
"v_mfma_f32_32x32x16_bf16 v[40:55], a[64:67], v[160:163], v[40:55]\nbuffer_load_dwordx4 a[64:67], v10, s[8:11], s78 offen offset:0 sc1\ns_waitcnt vmcnt(39)\nv_mfma_f32_32x32x16_bf16 v[40:55], a[68:71], v[164:167], v[40:55]\n"
"buffer_load_dwordx4 a[68:71], v10, s[8:11], s78 offen offset:512 sc1\ns_waitcnt vmcnt(39)\nv_mfma_f32_32x32x16_bf16 v[40:55], a[72:75], v[168:171], v[40:55]\nbuffer_load_dwordx4 a[72:75], v10, s[8:11], s78 offen offset:1024 sc1\n"
"s_waitcnt vmcnt(39)\nv_mfma_f32_32x32x16_bf16 v[40:55], a[76:79], v[172:175], v[40:55]\nbuffer_load_dwordx4 a[76:79], v10, s[8:11], s78 offen offset:1536 sc1\ns_waitcnt vmcnt(39)\n"
"v_mfma_f32_32x32x16_bf16 v[40:55], a[80:83], v[176:179], v[40:55]\nbuffer_load_dwordx4 a[80:83], v10, s[8:11], s78 offen offset:2048 sc1\ns_waitcnt vmcnt(39)\nv_mfma_f32_32x32x16_bf16 v[40:55], a[84:87], v[180:183], v[40:55]\n"
"buffer_load_dwordx4 a[84:87], v10, s[8:11], s78 offen offset:2560 sc1\ns_waitcnt vmcnt(39)\nv_mfma_f32_32x32x16_bf16 v[40:55], a[88:91], v[184:187], v[40:55]\nbuffer_load_dwordx4 a[88:91], v10, s[8:11], s78 offen offset:3072 sc1\n"
"s_waitcnt vmcnt(39)\nv_mfma_f32_32x32x16_bf16 v[40:55], a[92:95], v[188:191], v[40:55]\nbuffer_load_dwordx4 a[92:95], v10, s[8:11], s78 offen offset:3584 sc1\ns_waitcnt vmcnt(39)\n"
"v_mfma_f32_32x32x16_bf16 v[40:55], a[96:99], v[192:195], v[40:55]\nbuffer_load_dwordx4 a[96:99], v10, s[8:11], s79 offen offset:0 sc1\ns_waitcnt vmcnt(39)\nv_mfma_f32_32x32x16_bf16 v[40:55], a[100:103], v[196:199], v[40:55]\n"
"buffer_load_dwordx4 a[100:103], v10, s[8:11], s79 offen offset:512 sc1\ns_waitcnt vmcnt(39)\nv_mfma_f32_32x32x16_bf16 v[40:55], a[104:107], v[200:203], v[40:55]\nbuffer_load_dwordx4 a[104:107], v10, s[8:11], s79 offen offset:1024 sc1\n"
"s_waitcnt vmcnt(39)\nv_mfma_f32_32x32x16_bf16 v[40:55], a[108:111], v[204:207], v[40:55]\nbuffer_load_dwordx4 a[108:111], v10, s[8:11], s79 offen offset:1536 sc1\ns_waitcnt vmcnt(39)\n"
"v_mfma_f32_32x32x16_bf16 v[40:55], a[112:115], v[208:211], v[40:55]\nbuffer_load_dwordx4 a[112:115], v10, s[8:11], s79 offen offset:2048 sc1\ns_waitcnt vmcnt(39)\nv_mfma_f32_32x32x16_bf16 v[40:55], a[116:119], v[212:215], v[40:55]\n"
"buffer_load_dwordx4 a[116:119], v10, s[8:11], s79 offen offset:2560 sc1\ns_waitcnt vmcnt(39)\nv_mfma_f32_32x32x16_bf16 v[40:55], a[120:123], v[216:219], v[40:55]\nbuffer_load_dwordx4 a[120:123], v10, s[8:11], s79 offen offset:3072 sc1\n"
"s_waitcnt vmcnt(39)\nv_mfma_f32_32x32x16_bf16 v[40:55], a[124:127], v[220:223], v[40:55]\nbuffer_load_dwordx4 a[124:127], v10, s[8:11], s79 offen offset:3584 sc1\ns_waitcnt vmcnt(39)\n"
"v_mfma_f32_32x32x16_bf16 v[40:55], a[128:131], v[224:227], v[40:55]\nbuffer_load_dwordx4 a[128:131], v11, s[8:11], s80 offen offset:0 sc1\ns_waitcnt vmcnt(39)\nv_mfma_f32_32x32x16_bf16 v[40:55], a[132:135], v[228:231], v[40:55]\n"
"buffer_load_dwordx4 a[132:135], v11, s[8:11], s80 offen offset:256 sc1\ns_waitcnt vmcnt(39)\nv_mfma_f32_32x32x16_bf16 v[40:55], a[136:139], v[232:235], v[40:55]\nbuffer_load_dwordx4 a[136:139], v11, s[8:11], s80 offen offset:1024 sc1\n"
"s_waitcnt vmcnt(39)\nv_mfma_f32_32x32x16_bf16 v[40:55], a[140:143], v[236:239], v[40:55]\nbuffer_load_dwordx4 a[140:143], v11, s[8:11], s80 offen offset:1280 sc1\ns_waitcnt vmcnt(39)\n"
"v_mfma_f32_32x32x16_bf16 v[40:55], a[144:147], v[240:243], v[40:55]\nbuffer_load_dwordx4 a[144:147], v11, s[8:11], s80 offen offset:2048 sc1\ns_waitcnt vmcnt(39)\nv_mfma_f32_32x32x16_bf16 v[40:55], a[148:151], v[244:247], v[40:55]\n"
"buffer_load_dwordx4 a[148:151], v11, s[8:11], s80 offen offset:2304 sc1\ns_waitcnt vmcnt(39)\nv_mfma_f32_32x32x16_bf16 v[40:55], a[152:155], v[248:251], v[40:55]\nbuffer_load_dwordx4 a[152:155], v11, s[8:11], s80 offen offset:3072 sc1\n"
"s_waitcnt vmcnt(39)\nv_mfma_f32_32x32x16_bf16 v[40:55], a[156:159], v[252:255], v[40:55]\nbuffer_load_dwordx4 a[156:159], v11, s[8:11], s80 offen offset:3328 sc1\ns_nop 11\n"
"v_cvt_pk_bf16_f32 v56, v40, v41\nv_cvt_pk_bf16_f32 v57, v42, v43\nv_cvt_pk_bf16_f32 v58, v44, v45\nv_cvt_pk_bf16_f32 v59, v46, v47\n"
"v_cvt_pk_bf16_f32 v60, v48, v49\nv_cvt_pk_bf16_f32 v61, v50, v51\nv_cvt_pk_bf16_f32 v62, v52, v53\nv_cvt_pk_bf16_f32 v63, v54, v55\n"
"ds_write_b128 v24, v[56:59]\nds_write_b128 v24, v[60:63] offset:32\nv_add_u32 v24, 256, v24\ns_sub_u32 s72, s72, 1\n"
"s_cmp_lg_u32 s72, 0\ns_cbranch_scc1 fo_loop\ns_waitcnt vmcnt(39)\nv_mfma_f32_32x32x16_bf16 v[40:55], a[0:3], v[96:99], 0\n"
"s_waitcnt vmcnt(38)\nv_mfma_f32_32x32x16_bf16 v[40:55], a[4:7], v[100:103], v[40:55]\ns_waitcnt vmcnt(37)\nv_mfma_f32_32x32x16_bf16 v[40:55], a[8:11], v[104:107], v[40:55]\n"
"s_waitcnt vmcnt(36)\nv_mfma_f32_32x32x16_bf16 v[40:55], a[12:15], v[108:111], v[40:55]\ns_waitcnt vmcnt(35)\nv_mfma_f32_32x32x16_bf16 v[40:55], a[16:19], v[112:115], v[40:55]\n"
"s_waitcnt vmcnt(34)\nv_mfma_f32_32x32x16_bf16 v[40:55], a[20:23], v[116:119], v[40:55]\ns_waitcnt vmcnt(33)\nv_mfma_f32_32x32x16_bf16 v[40:55], a[24:27], v[120:123], v[40:55]\n"
"s_waitcnt vmcnt(32)\nv_mfma_f32_32x32x16_bf16 v[40:55], a[28:31], v[124:127], v[40:55]\ns_waitcnt vmcnt(31)\nv_mfma_f32_32x32x16_bf16 v[40:55], a[32:35], v[128:131], v[40:55]\n"
"s_waitcnt vmcnt(30)\nv_mfma_f32_32x32x16_bf16 v[40:55], a[36:39], v[132:135], v[40:55]\ns_waitcnt vmcnt(29)\nv_mfma_f32_32x32x16_bf16 v[40:55], a[40:43], v[136:139], v[40:55]\n"
"s_waitcnt vmcnt(28)\nv_mfma_f32_32x32x16_bf16 v[40:55], a[44:47], v[140:143], v[40:55]\ns_waitcnt vmcnt(27)\nv_mfma_f32_32x32x16_bf16 v[40:55], a[48:51], v[144:147], v[40:55]\n"
"s_waitcnt vmcnt(26)\nv_mfma_f32_32x32x16_bf16 v[40:55], a[52:55], v[148:151], v[40:55]\ns_waitcnt vmcnt(25)\nv_mfma_f32_32x32x16_bf16 v[40:55], a[56:59], v[152:155], v[40:55]\n"
"s_waitcnt vmcnt(24)\nv_mfma_f32_32x32x16_bf16 v[40:55], a[60:63], v[156:159], v[40:55]\ns_waitcnt vmcnt(23)\nv_mfma_f32_32x32x16_bf16 v[40:55], a[64:67], v[160:163], v[40:55]\n"
"s_waitcnt vmcnt(22)\nv_mfma_f32_32x32x16_bf16 v[40:55], a[68:71], v[164:167], v[40:55]\ns_waitcnt vmcnt(21)\nv_mfma_f32_32x32x16_bf16 v[40:55], a[72:75], v[168:171], v[40:55]\n"
"s_waitcnt vmcnt(20)\nv_mfma_f32_32x32x16_bf16 v[40:55], a[76:79], v[172:175], v[40:55]\ns_waitcnt vmcnt(19)\nv_mfma_f32_32x32x16_bf16 v[40:55], a[80:83], v[176:179], v[40:55]\n"
"s_waitcnt vmcnt(18)\nv_mfma_f32_32x32x16_bf16 v[40:55], a[84:87], v[180:183], v[40:55]\ns_waitcnt vmcnt(17)\nv_mfma_f32_32x32x16_bf16 v[40:55], a[88:91], v[184:187], v[40:55]\n"
"s_waitcnt vmcnt(16)\nv_mfma_f32_32x32x16_bf16 v[40:55], a[92:95], v[188:191], v[40:55]\ns_waitcnt vmcnt(15)\nv_mfma_f32_32x32x16_bf16 v[40:55], a[96:99], v[192:195], v[40:55]\n"
"s_waitcnt vmcnt(14)\nv_mfma_f32_32x32x16_bf16 v[40:55], a[100:103], v[196:199], v[40:55]\ns_waitcnt vmcnt(13)\nv_mfma_f32_32x32x16_bf16 v[40:55], a[104:107], v[200:203], v[40:55]\n"
"s_waitcnt vmcnt(12)\nv_mfma_f32_32x32x16_bf16 v[40:55], a[108:111], v[204:207], v[40:55]\ns_waitcnt vmcnt(11)\nv_mfma_f32_32x32x16_bf16 v[40:55], a[112:115], v[208:211], v[40:55]\n"
"s_waitcnt vmcnt(10)\nv_mfma_f32_32x32x16_bf16 v[40:55], a[116:119], v[212:215], v[40:55]\ns_waitcnt vmcnt(9)\nv_mfma_f32_32x32x16_bf16 v[40:55], a[120:123], v[216:219], v[40:55]\n"
"s_waitcnt vmcnt(8)\nv_mfma_f32_32x32x16_bf16 v[40:55], a[124:127], v[220:223], v[40:55]\ns_waitcnt vmcnt(7)\nv_mfma_f32_32x32x16_bf16 v[40:55], a[128:131], v[224:227], v[40:55]\n"
"s_waitcnt vmcnt(6)\nv_mfma_f32_32x32x16_bf16 v[40:55], a[132:135], v[228:231], v[40:55]\ns_waitcnt vmcnt(5)\nv_mfma_f32_32x32x16_bf16 v[40:55], a[136:139], v[232:235], v[40:55]\n"
"s_waitcnt vmcnt(4)\nv_mfma_f32_32x32x16_bf16 v[40:55], a[140:143], v[236:239], v[40:55]\ns_waitcnt vmcnt(3)\nv_mfma_f32_32x32x16_bf16 v[40:55], a[144:147], v[240:243], v[40:55]\n"
"s_waitcnt vmcnt(2)\nv_mfma_f32_32x32x16_bf16 v[40:55], a[148:151], v[244:247], v[40:55]\ns_waitcnt vmcnt(1)\nv_mfma_f32_32x32x16_bf16 v[40:55], a[152:155], v[248:251], v[40:55]\n"
"s_waitcnt vmcnt(0)\nv_mfma_f32_32x32x16_bf16 v[40:55], a[156:159], v[252:255], v[40:55]\ns_nop 11\nv_cvt_pk_bf16_f32 v56, v40, v41\n"
"v_cvt_pk_bf16_f32 v57, v42, v43\nv_cvt_pk_bf16_f32 v58, v44, v45\nv_cvt_pk_bf16_f32 v59, v46, v47\nv_cvt_pk_bf16_f32 v60, v48, v49\n"
"v_cvt_pk_bf16_f32 v61, v50, v51\nv_cvt_pk_bf16_f32 v62, v52, v53\nv_cvt_pk_bf16_f32 v63, v54, v55\nds_write_b128 v24, v[56:59]\n"
"ds_write_b128 v24, v[60:63] offset:32\nv_add_u32 v24, 256, v24\n"
    :: "{s[8:23]}"(s0), "{v[8:23]}"(v0) : "v24", "v25", "v26", "v27", "v28", "v29", "v30", "v31", "v32", "v33", "v34", "v35", "v36", "v37", "v38", "v39", "v40", "v41", "v42", "v43", "v44", "v45", "v46", "v47", "v48", "v49", "v50", "v51", "v52", "v53", "v54", "v55", "v56", "v57", "v58", "v59", "v60", "v61", "v62", "v63", "v64", "v65", "v66", "v67", "v68", "v69", "v70", "v71", "v72", "v73", "v74", "v75", "v76", "v77", "v78", "v79", "v80", "v81", "v82", "v83", "v84", "v85", "v86", "v87", "v88", "v89", "v90", "v91", "v92", "v93", "v94", "v95", "v96", "v97", "v98", "v99", "v100", "v101", "v102", "v103", "v104", "v105", "v106", "v107", "v108", "v109", "v110", "v111", "v112", "v113", "v114", "v115", "v116", "v117", "v118", "v119", "v120", "v121", "v122", "v123", "v124", "v125", "v126", "v127", "v128", "v129", "v130", "v131", "v132", "v133", "v134", "v135", "v136", "v137", "v138", "v139", "v140", "v141", "v142", "v143", "v144", "v145", "v146", "v147", "v148", "v149", "v150", "v151", "v152", "v153", "v154", "v155", "v156", "v157", "v158", "v159", "v160", "v161", "v162", "v163", "v164", "v165", "v166", "v167", "v168", "v169", "v170", "v171", "v172", "v173", "v174", "v175", "v176", "v177", "v178", "v179", "v180", "v181", "v182", "v183", "v184", "v185", "v186", "v187", "v188", "v189", "v190", "v191", "v192", "v193", "v194", "v195", "v196", "v197", "v198", "v199", "v200", "v201", "v202", "v203", "v204", "v205", "v206", "v207", "v208", "v209", "v210", "v211", "v212", "v213", "v214", "v215", "v216", "v217", "v218", "v219", "v220", "v221", "v222", "v223", "v224", "v225", "v226", "v227", "v228", "v229", "v230", "v231", "v232", "v233", "v234", "v235", "v236", "v237", "v238", "v239", "v240", "v241", "v242", "v243", "v244", "v245", "v246", "v247", "v248", "v249", "v250", "v251", "v252", "v253", "v254", "v255", "a0", "a1", "a2", "a3", "a4", "a5", "a6", "a7", "a8", "a9", "a10", "a11", "a12", "a13", "a14", "a15", "a16", "a17", "a18", "a19", "a20", "a21", "a22", "a23", "a24", "a25", "a26", "a27", "a28", "a29", "a30", "a31", "a32", "a33", "a34", "a35", "a36", "a37", "a38", "a39", "a40", "a41", "a42", "a43", "a44", "a45", "a46", "a47", "a48", "a49", "a50", "a51", "a52", "a53", "a54", "a55", "a56", "a57", "a58", "a59", "a60", "a61", "a62", "a63", "a64", "a65", "a66", "a67", "a68", "a69", "a70", "a71", "a72", "a73", "a74", "a75", "a76", "a77", "a78", "a79", "a80", "a81", "a82", "a83", "a84", "a85", "a86", "a87", "a88", "a89", "a90", "a91", "a92", "a93", "a94", "a95", "a96", "a97", "a98", "a99", "a100", "a101", "a102", "a103", "a104", "a105", "a106", "a107", "a108", "a109", "a110", "a111", "a112", "a113", "a114", "a115", "a116", "a117", "a118", "a119", "a120", "a121", "a122", "a123", "a124", "a125", "a126", "a127", "a128", "a129", "a130", "a131", "a132", "a133", "a134", "a135", "a136", "a137", "a138", "a139", "a140", "a141", "a142", "a143", "a144", "a145", "a146", "a147", "a148", "a149", "a150", "a151", "a152", "a153", "a154", "a155", "a156", "a157", "a158", "a159", "a160", "a161", "a162", "a163", "a164", "a165", "a166", "a167", "a168", "a169", "a170", "a171", "a172", "a173", "a174", "a175", "a176", "a177", "a178", "a179", "a180", "a181", "a182", "a183", "a184", "a185", "a186", "a187", "a188", "a189", "a190", "a191", "a192", "a193", "a194", "a195", "a196", "a197", "a198", "a199", "a200", "a201", "a202", "a203", "a204", "a205", "a206", "a207", "a208", "a209", "a210", "a211", "a212", "a213", "a214", "a215", "a216", "a217", "a218", "a219", "a220", "a221", "a222", "a223", "s40", "s41", "s42", "s43", "s44", "s45", "s46", "s47", "s48", "s49", "s50", "s51", "s52", "s53", "s54", "s55", "s56", "s57", "s58", "s59", "s60", "s61", "s62", "s63", "s64", "s65", "s66", "s67", "s68", "s69", "s70", "s71", "s72", "s73", "s74", "s75", "s76", "s77", "s78", "s79", "s80", "s81", "vcc", "scc", "memory");
}
DI u4 pack8(const f16v& x, int s, float sc) { return (u4){pk2(x[8 * s] * sc, x[8 * s + 1] * sc), pk2(x[8 * s + 2] * sc, x[8 * s + 3] * sc), pk2(x[8 * s + 4] * sc, x[8 * s + 5] * sc), pk2(x[8 * s + 6] * sc, x[8 * s + 7] * sc)}; }
DI u4 pack8(const f16v& x, int s) { return (u4){pk2(x[8 * s], x[8 * s + 1]), pk2(x[8 * s + 2], x[8 * s + 3]), pk2(x[8 * s + 4], x[8 * s + 5]), pk2(x[8 * s + 6], x[8 * s + 7])}; }
DI void dma_pair(u4 srd, int v0, int v1, int soff, unsigned m) {
    asm volatile("s_mov_b32 m0, %4\n\ts_nop 0\n\tbuffer_load_dwordx4 %0, %2, %3 offen lds\n\tbuffer_load_dwordx4 %1, %2, %3 offen offset:1024 lds" :: "v"(v0), "v"(v1), "s"(srd), "s"(soff), "s"(m) : "memory");
}
DI void dma_one(u4 srd, int v0, int soff, unsigned m) {
    asm volatile("s_mov_b32 m0, %3\n\ts_nop 0\n\tbuffer_load_dwordx4 %0, %1, %2 offen lds" :: "v"(v0), "s"(srd), "s"(soff), "s"(m) : "memory");
}
DI void mV_va(f16v& c, u4 x, u4 y) { asm volatile("v_mfma_f32_32x32x16_bf16 %0, %1, %2, %0" : "+v"(c) : "v"(x), "a"(y)); }
DI void mV_va0(f16v& c, u4 x, u4 y) { asm volatile("v_mfma_f32_32x32x16_bf16 %0, %1, %2, 0" : "=&v"(c) : "v"(x), "a"(y)); }
DI void mV_av(f16v& c, u4 x, u4 y) { asm volatile("v_mfma_f32_32x32x16_bf16 %0, %1, %2, %0" : "+v"(c) : "a"(x), "v"(y)); }
DI void mV_av0(f16v& c, u4 x, u4 y) { asm volatile("v_mfma_f32_32x32x16_bf16 %0, %1, %2, 0" : "=&v"(c) : "a"(x), "v"(y)); }
DI void mV_vv(f16v& c, u4 x, u4 y) { asm volatile("v_mfma_f32_32x32x16_bf16 %0, %1, %2, %0" : "+v"(c) : "v"(x), "v"(y)); }
DI void mV_vv0(f16v& c, u4 x, u4 y) { asm volatile("v_mfma_f32_32x32x16_bf16 %0, %1, %2, 0" : "=&v"(c) : "v"(x), "v"(y)); }
template <int N, int PD, class RD, class MM, class EX> DI void pipe(RD rd, MM mm, EX ex) {
    u4 f[PD];
#pragma unroll
    for (int j = 0; j < PD; ++j) f[j] = rd(j);
    SB();
#pragma unroll
    for (int j = 0; j < N; ++j) { mm(j, f[j % PD]); if (j + PD < N) f[j % PD] = rd(j + PD); ex(j); SB(); }
}
DI int swzr(int row) { return ((row >> 2) & 1) | ((row & 3) << 1) | (row & 8); }
DI int swap23(int x) { return (x & ~12) | ((x & 4) << 1) | ((x & 8) >> 1); }
#define SLICE 32768
#define MFW32(x, y) asm volatile("s_nop 11" : "+v"(x), "+v"(y))
struct SideCtx {
    char LAS* sm; unsigned smb; int lane, wave;
    int vr0, vr1, vc0, vc1;
};
DI SideCtx make_ctx(int wave) {
    SideCtx c; c.sm = (char LAS*)smem_; c.smb = (unsigned)(size_t)c.sm; c.lane = fresh_lane(); c.wave = wave;
    asm volatile("" : "+s"(c.wave), "+s"(c.smb));
    const int l = c.lane;
    { int row = 4 * c.wave + (l >> 5); c.vr0 = row * 8192 + (((l & 31) ^ swzr(row)) * 16); row += 2; c.vr1 = row * 8192 + (((l & 31) ^ swzr(row)) * 16) - 1024; }
    { int row = l >> 3; c.vc0 = row * 896 + (((l & 7) ^ (((row >> 1) & 1) << 2)) * 16); c.vc1 = c.vc0 + 8 * 896 - 1024; }
    return c;
}
DI void dma_rows_q(const SideCtx& c, u4 srd, int h, int r0, int slot, int g) { dma_pair(srd, c.vr0, c.vr1, (r0 + 16 * g) * 8192 + h * 512, c.smb + slot * SLICE + (8 * g + 2 * c.wave) * 1024); }
DI void dma_cols_q(const SideCtx& c, u4 srd, int h, int c0, int slot, int g) { const int k = 8 * c.wave + 2 * g; dma_pair(srd, c.vc0, c.vc1, (h * 256 + 8 * k) * 896 + c0 * 2, c.smb + slot * SLICE + k * 1024); }
DI void dma_rows(const SideCtx& c, u4 srd, int h, int r0, int slot) {
#pragma unroll
    for (int g = 0; g < 4; ++g) dma_rows_q(c, srd, h, r0, slot, g);
}
DI void dma_cols(const SideCtx& c, u4 srd, int h, int c0, int slot) {
#pragma unroll
    for (int g = 0; g < 4; ++g) dma_cols_q(c, srd, h, c0, slot, g);
}
DI void set_flag(const SideCtx& c, u4 rhb, int off, unsigned tag) {
    waitvm<0>(); bar();
    if (c.wave == 0) sst32(tag, rhb, c.lane == 0 ? off : OOB, 0);
}

DI void slp(int n) {
#pragma nounroll
    for (int i = 0; i < n; ++i) asm volatile("s_sleep 1");
}
DI int xsrc(int wave, int lane, int k) { const int n = 64 * (7 * (wave & 1) + k) + lane, row = (n * 1171) >> 16, pos = n - 56 * row; return (row + 16 * (wave >> 1)) * 896 + ((pos ^ ((row >> 1) & 7)) * 16); }
DI int xrd(int row, int hi, int k) { return row * 896 + (((2 * k + hi) ^ ((row >> 1) & 7)) * 16); }
#define WSL(u) ((u) + 1)

DI int xswz(int type, int row) { return type ? ((row >> 1) & 1) | (((row >> 3) & 3) << 1) : (row >> 1) & 7; }
DI int xsrc8(int type, int wave, int lane) { const int row = 8 * wave + (lane >> 3); return row * 896 + (((lane & 7) ^ xswz(type, row)) * 16); }
#define W_PF 2
#define X_PF 4
DI void dma_line(u4 rw1, const SideCtx& c, int half, int h, int sub) {
    const int n = (sub * 4 + c.wave) * 48 + c.lane;
    asm volatile("s_mov_b32 m0, %3\n\ts_nop 0\n\tbuffer_load_dword %0, %1, %2 offen lds" :: "v"(c.lane < 48 ? (n >> 2) * 8192 + (n & 3) * 128 : OOB), "s"(rw1), "s"((256 * half + 64) * 8192 + h * 512), "s"(c.smb + 28672 + 256) : "memory");
}
template <int PART>
DI void gemm_first(int wave, int half, int type, int h, int sub) {
    CArgs& a = get_args();
    const SideCtx c = make_ctx(wave);
    h = __builtin_amdgcn_readfirstlane(h); sub = __builtin_amdgcn_readfirstlane(sub); half = __builtin_amdgcn_readfirstlane(half); asm volatile("" : "+s"(h), "+s"(sub), "+s"(half));
    if (PART == 0) {
        const u4 rw2 = mksrd(type ? a.Wo : a.Wq, 448 * 8192);
        const int c1 = sub == 3 ? 384 : sub * 128 + 64;
        if (type) { dma_cols(c, rw2, h, sub * 128, WSL(2)); dma_cols(c, rw2, h, c1, WSL(3)); } else { dma_rows(c, rw2, h, sub * 128, WSL(2)); dma_rows(c, rw2, h, c1, WSL(3)); }
    } else {
        const u4 rw1 = mksrd(type ? a.Wv : a.Wk, 448 * 8192);
        if (W_PF == 1) dma_line(rw1, c, half, h, sub);
        dma_rows(c, rw1, h, 256 * half, WSL(0));
        if (W_PF == 2) dma_line(rw1, c, half, h, sub);
        const int n = (((type * 4 + (h & 3)) * 4 + sub) * 4 + c.wave) * 8 + c.lane;
        asm volatile("s_mov_b32 m0, %2\n\ts_nop 0\n\tbuffer_load_dword %0, %1, 0 offen lds" :: "v"(c.lane < 8 ? n * 128 : OOB), "s"(mksrd(a.X, half ? 28672 : X_PF * 28672)), "s"(c.smb + 28672 + 256) : "memory");
    }
}
template <int TYPE>
DI void gemm_role(unsigned tag, int vb, int wave, int half, int h, int sub) {
    CArgs& a = get_args();
    const SideCtx c = make_ctx(wave);
    const int lane = c.lane, g = lane >> 4, n = lane & 15;
    h = __builtin_amdgcn_readfirstlane(h); sub = __builtin_amdgcn_readfirstlane(sub); half = __builtin_amdgcn_readfirstlane(half); asm volatile("" : "+s"(h), "+s"(sub), "+s"(half));
    const int B = 4 * sub + c.wave, prow = 8 * (n >> 2) + (n & 3);
    GCtx x;
    x.rw1 = mksrd(TYPE ? a.Wv : a.Wk, 448 * 8192); x.rx = mksrd(a.X, half ? 0 : 2048 * 896); x.rhb = mksrd(a.HB, HB_SZ);
    x.rexp = TYPE ? mksrd(a.UM, 28 * MH) : mksrd(a.SXM, 64 * SH); x.rout = mksrd(TYPE ? a.UF : a.GF, GSZ);
    x.tag = tag; x.rot = 4 * half; x.sw1 = h * 512; x.mw = c.smb + c.wave * 2048; x.mx = c.smb + c.wave * 1024;
    x.sfg = O_FG - O_FW + (h * 2 + TYPE - TYPE * 16 - h) * 256; x.stay = half ? 2 : sub < 2; x.csc = __float_as_uint(CSCALE); x.ostr = TYPE ? 16384 : 28672;
    x.p1 = (unsigned)(u64)a.Au; x.p2 = (unsigned)((u64)a.Au >> 32); x.p5 = min(((TYPE * 16 + h) * 16 + B) * 3584, 2048 * 896 - 4096) + lane * 16; x.kb = 65536 + c.wave * 16384;
    x.vr0 = c.vr0; x.vr1 = c.vr1; x.vx = xsrc8(TYPE, c.wave, lane);
    x.wa = c.smb + SLICE + prow * 512 + ((g ^ swzr(prow)) * 16);
    { const int row = TYPE ? prow : n; x.xa = c.smb + row * 128 + ((g ^ xswz(TYPE, row)) * 16); }
    auto wof = [&](int blk) { return (TYPE ? 2 * blk * MH + NC * 4096 + h * 14336 : (h * 4 + (blk >> 2)) * SH + NC * 1024 + (blk & 3) * 28672) + lane * 16; };
    auto oof = [&](int blk, int i) { return blk > 13 ? OOB : (TYPE ? (2 * blk + i) * UH + h * 1024 : h * GH + i * 14336 + blk * 1024) + lane * 16; };
    x.vexp = B > 13 ? OOB : wof(B); x.vimp = B > 5 ? OOB : wof(B + 8);
    x.vk = c.smb + (3 + (c.wave >> 1)) * SLICE + (8 * g + (n >> 2)) * 128 + (((4 * (c.wave & 1) + ((n & 3) >> 1)) ^ ((n >> 3) << 2)) * 16) + 8 * (n & 1);
    x.vo0 = oof(B, 0); x.vo1 = oof(B, 1); x.vo2 = oof(B + 8, 0); x.vo3 = oof(B + 8, 1);
    x.vfl = (c.wave == 0 && lane == 0) ? O_FW + half * 8192 + ((TYPE * 16 + h) * 4 + sub) * 64 : OOB;
    x.vpf = O_FW + (lane & 1) * 8192 + ((TYPE * 16 + h) * 4 + (sub | 2)) * 64; x.vpo = O_FW + 8192 + ((TYPE * 16 + h) * 4 + sub) * 64;
    PSTAMP(1);
    const unsigned tw = TYPE ? gemm_asm1(x) : gemm_asm0(x);
}

#define O_LAG 2
#define O_LAGN 24
DI void o_task(unsigned tag, int wave, int s) {
    {
        CArgs& a = get_args();
        const SideCtx c = make_ctx(wave);
        const int lane = c.lane, hi = lane >> 5, l31 = lane & 31, w = c.wave, pc = swap23(l31);
        s = __builtin_amdgcn_readfirstlane(s); asm volatile("" : "+s"(s));
        const u4 rhb = mksrd(a.HB, HB_SZ), rx = mksrd(a.X, 2048 * 896);
        bar();
        const int vx = xsrc8(0, w, lane);
#pragma unroll
        for (int k = 0; k < 7; ++k) dma_one(rx, vx, s * 28672 + k * 128, c.smb + k * 4096 + w * 1024);
        wait_flags(rhb, lane < 16 ? O_FH + (16 * w + lane) * 128 : OOB, tag + (s < O_LAGN ? s + O_LAG - (O_LAG - 2) * s / O_LAGN : s + 2));
        OCtx x;
        x.r = mksrd(a.HB, 0x7000000);
        x.sg = (unsigned)((char*)a.GF - a.HB) + s * 28672; x.ssx = (unsigned)((char*)a.SXM - a.HB) + s * 1024; x.hx = 4 * w;
        x.su = (unsigned)((char*)a.UF - a.HB) + s * 16384 + w * 2 * UH; x.sum = (unsigned)((char*)a.UM - a.HB) + s * 4096 + w * 2 * MH; x.nt = w < 2 ? 4 : 3;
        x.sr = GH; x.p0 = SH; x.p1 = 8 * UH; x.p2 = 8 * MH; x.p3 = x.p4 = x.p5 = x.p6 = x.p7 = x.p8 = x.p9 = x.pa = 0;
        x.vg = (pc >> 4) * 14336 + hi * 256 + (pc & 15) * 16;
        x.vsx = (l31 >> 4) * 512 + hi * 256 + (l31 & 15) * 16;
        x.vu = (pc >> 4) * UH + hi * 256 + (pc & 15) * 16; x.vum = (pc >> 4) * MH + hi * 512 + (pc & 15) * 16;
        x.vx = c.smb + l31 * 128 + ((hi ^ xswz(0, l31)) * 16); x.vt = l31 - 8 * hi;
        x.vpx = c.smb + 32768 + x.hx * 2048 + lane * 16; x.vpm = c.smb + 65536 + w * 2048 + lane * 16; x.vpr = c.smb + 32768 + lane * 16;
        x.vo = c.smb + 73728 + l31 * 912 + w * 64 + hi * 16;
        o_asm(x);
    }
    bar();
    const int tid = wave * 64 + fresh_lane();
    const u4 ro = mksrd(get_args().out, 2048 * 1792);
#pragma unroll
    for (int k = 0; k < 7; ++k) sst128(*(const u4 LAS*)(smem_ + 73728 + (tid >> 3) * 912 + (tid & 7) * 16 + k * 128), ro, (tid >> 3) * 1792 + (tid & 7) * 16, s * 32 * 1792 + 896 + k * 128);
}

DI void e_task(unsigned tag, int wave, int s, int hg) {
    CArgs& a = get_args();
    const SideCtx c = make_ctx(wave);
    const int lane = c.lane, hi = lane >> 5, l31 = lane & 31;
    s = __builtin_amdgcn_readfirstlane(s); hg = __builtin_amdgcn_readfirstlane(hg); asm volatile("" : "+s"(s), "+s"(hg));
    const int h = 4 * hg + c.wave;
    const u4 rhb = mksrd(a.HB, HB_SZ), rg = mksrd(a.GF, GSZ), rx = mksrd(a.X, 2048 * 896);
    bar();
#pragma unroll
    for (int k = 0; k < 7; ++k) dma_one(rx, xsrc(c.wave, lane, k), s * 28672, c.smb + (7 * c.wave + k) * 1024);
    wait_flags(rhb, lane < 4 ? O_FG + (h * 8 + lane) * 64 : OOB, tag + s + 1);
    u4 gf[28];
    const int key = swap23(l31), o_g = (key >> 4) * 14336 + hi * 256 + (key & 15) * 16;
#pragma unroll
    for (int k = 0; k < 28; ++k) asm volatile("buffer_load_dwordx4 %0, %1, %2, %3 offen sc1" : "=a"(gf[k]) : "v"(o_g), "s"(rg), "s"(h * GH + s * 28672 + k * 512) : "memory");
    bar();
    f16v sx;
    pipe<28, 4>([&](int k) { return *(const u4 LAS*)(c.sm + xrd(l31, hi, k & 3) + (k >> 2) * 128); },
                [&](int k, u4 f) { if (k == 0) WAITV(21, "+a"(gf[0]), "+a"(gf[1]), "+a"(gf[2]), "+a"(gf[3]), "+a"(gf[4]), "+a"(gf[5]), "+a"(gf[6]));
                                   if (k == 7) WAITV(14, "+a"(gf[7]), "+a"(gf[8]), "+a"(gf[9]), "+a"(gf[10]), "+a"(gf[11]), "+a"(gf[12]), "+a"(gf[13]));
                                   if (k == 14) WAITV(7, "+a"(gf[14]), "+a"(gf[15]), "+a"(gf[16]), "+a"(gf[17]), "+a"(gf[18]), "+a"(gf[19]), "+a"(gf[20]));
                                   if (k == 21) WAITV(0, "+a"(gf[21]), "+a"(gf[22]), "+a"(gf[23]), "+a"(gf[24]), "+a"(gf[25]), "+a"(gf[26]), "+a"(gf[27]));
                                   if (k == 0) mV_av0(sx, gf[k], f); else mV_av(sx, gf[k], f); },
                [&](int) {});
    asm volatile("s_nop 11" : "+v"(sx));
    float m = -3.0e38f;
#pragma unroll
    for (int j = 0; j < 16; ++j) { if (16 * (j >> 3) + 8 * hi + (j & 7) > l31) sx[j] = -INFINITY; m = fmaxf(m, sx[j]); }
    m = x32max(m);
    float z = 0.f;
#pragma unroll
    for (int j = 0; j < 16; ++j) { sx[j] = __builtin_amdgcn_exp2f(sx[j] - m); z += sx[j]; }
    z = x32add(z);
    const u4 rex = mksrd(a.EX, 64 * 16 * 2048), rmz = mksrd(a.MZX, 64 * 16 * 256);
    sst128(pack8(sx, 0), rex, lane * 16, (s * 16 + h) * 2048); sst128(pack8(sx, 1), rex, lane * 16, (s * 16 + h) * 2048 + 1024);
    sst64((u2){__float_as_uint(m), __float_as_uint(z)}, rmz, lane < 32 ? lane * 8 : OOB, (s * 16 + h) * 256);
    set_flag(c, rhb, O_FE + (s * 4 + hg) * 64, tag);
}

#define NA 4
#define A_SLP 0
#define A_LATE 1
#define X_JIT 4
#define P_XSLP 24
#define P_USLP 4
DI void x_task(unsigned tag, int wave, int s, int cb) {
    CArgs& a = get_args();
    const SideCtx c = make_ctx(wave);
    const int lane = c.lane, hi = lane >> 5, l31 = lane & 31, w = c.wave;
    s = __builtin_amdgcn_readfirstlane(s); cb = __builtin_amdgcn_readfirstlane(cb); asm volatile("" : "+s"(s), "+s"(cb));
    const u4 rhb = mksrd(a.HB, HB_SZ);
    const u4 ru = mksrd(a.UF, GSZ), rex = mksrd(a.EX, 64 * 16 * 2048), rmz = mksrd(a.MZX, 64 * 16 * 256);
    const u4 rum = mksrd(a.UM, 28 * MH), rsx = mksrd(a.SXM, 64 * SH);
    if (X_JIT) wait_flags(rhb, lane == 0 ? O_FH + (cb * 4 + w) * 128 : OOB, tag + s - X_JIT);
    wait_flags(rhb, lane < 8 ? O_FG + (((4 * w + (lane & 3)) * 2 + 1) * 4 + ((cb >> 1) & (lane < 4 ? 3 : 1))) * 64 : lane == 8 ? O_FE + (s * 4 + w) * 64 : OOB, lane < 8 ? tag + s + 1 : tag);
    u4 uf[4][2][2], ex[4][2]; u2 mz[4];
    const int o_u = (l31 >> 4) * UH + hi * 256 + (l31 & 15) * 16;
#pragma unroll
    for (int k = 0; k < 4; ++k) {
        const int h = 4 * w + k, sh = s * 16 + h;
#pragma unroll
        for (int sg = 0; sg < 2; ++sg) {
            ex[k][sg] = sld128(rex, lane * 16, sh * 2048 + sg * 1024);
#pragma unroll
            for (int nt = 0; nt < 2; ++nt) uf[k][sg][nt] = sld128(ru, o_u, (4 * cb + 2 * nt) * UH + sh * 1024 + sg * 512);
        }
        mz[k] = sld64(rmz, l31 * 8, sh * 256);
        asm volatile("s_sleep %0" :: "i"(P_XSLP));
    }
    waitvm<0>();
    f16v rr[4][2];
#pragma unroll
    for (int k = 0; k < 4; ++k) {
        asm volatile("" : "+v"(mz[k]), "+v"(ex[k][0]), "+v"(ex[k][1]), "+v"(uf[k][0][0]), "+v"(uf[k][0][1]), "+v"(uf[k][1][0]), "+v"(uf[k][1][1]));
        mV_vv0(rr[k][0], uf[k][0][0], ex[k][0]); mV_vv0(rr[k][1], uf[k][0][1], ex[k][0]);
        mV_vv(rr[k][0], uf[k][1][0], ex[k][1]); mV_vv(rr[k][1], uf[k][1][1], ex[k][1]);
        MFW32(rr[k][0], rr[k][1]);
    }
    wait_flags<2>(rhb, lane < 16 ? O_FH + (16 * w + lane) * 128 : s == 63 && lane < 32 ? O_FH + (16 * w + lane - 16) * 128 + 64 : OOB, tag + (s < 62 ? s + 2 : s + 1));
    u4 sp[4][4], um[2][2];
    const int o_sx = (l31 >> 4) * 512 + hi * 256 + (l31 & 15) * 16, o_um = (l31 >> 4) * MH + hi * 512 + (l31 & 15) * 16;
#pragma unroll
    for (int k = 0; k < 16; ++k) sp[k >> 2][k & 3] = sld128(rsx, o_sx, ((4 * w + (k >> 2)) * 4 + (k & 3)) * SH + s * 1024);
    if (s >= 62) wait_flags<2>(rhb, lane < 4 ? O_FP + ((4 * cb + lane) * 4 + w) * 64 : OOB, tag + 64);
#pragma unroll
    for (int k = 0; k < 4; ++k) um[k >> 1][k & 1] = sld128(rum, o_um, (4 * cb + 2 * (k & 1)) * MH + s * 4096 + w * 1024 + (k >> 1) * 256);
    u2 pm[4]; float scx[4];
#pragma unroll
    for (int k = 0; k < 4; ++k) {
        if (k == 0) WAITV(16, "+v"(sp[0][0]), "+v"(sp[0][1]), "+v"(sp[0][2]), "+v"(sp[0][3]));
        if (k == 1) WAITV(12, "+v"(sp[1][0]), "+v"(sp[1][1]), "+v"(sp[1][2]), "+v"(sp[1][3]));
        if (k == 2) WAITV(8, "+v"(sp[2][0]), "+v"(sp[2][1]), "+v"(sp[2][2]), "+v"(sp[2][3]));
        if (k == 3) WAITV(4, "+v"(sp[3][0]), "+v"(sp[3][1]), "+v"(sp[3][2]), "+v"(sp[3][3]));
        const f4 sc = (__builtin_bit_cast(f4, sp[k][0]) + __builtin_bit_cast(f4, sp[k][1])) + (__builtin_bit_cast(f4, sp[k][2]) + __builtin_bit_cast(f4, sp[k][3]));
        const float mx = __uint_as_float(mz[k][0]);
        float m = fmaxf(x32max(fmaxf(fmaxf(sc[0], sc[1]), fmaxf(sc[2], sc[3]))), mx);
        float e[4];
#pragma unroll
        for (int j = 0; j < 4; ++j) e[j] = __builtin_amdgcn_exp2f(sc[j] - m);
        const float cx = __builtin_amdgcn_exp2f(mx - m);
        const float z = x32add((e[0] + e[1]) + (e[2] + e[3])) + cx * __uint_as_float(mz[k][1]);
        const float inv = __builtin_amdgcn_rcpf(z);
        scx[k] = cx * inv;
        pm[k] = (u2){pk2(e[0] * inv, e[1] * inv), pk2(e[2] * inv, e[3] * inv)};
    }
    WAITV(0, "+v"(um[0][0]), "+v"(um[0][1]), "+v"(um[1][0]), "+v"(um[1][1]));
    f16v d0, d1;
    {
        u4 p0 = {pm[0][0], pm[1][0], pm[2][0], pm[3][0]}, p1 = {pm[0][1], pm[1][1], pm[2][1], pm[3][1]};
        asm volatile("s_nop 1\n\tv_mfma_f32_32x32x16_bf16 %0, %2, %6, 0\n\tv_mfma_f32_32x32x16_bf16 %1, %3, %6, 0\n\tv_mfma_f32_32x32x16_bf16 %0, %4, %7, %0\n\tv_mfma_f32_32x32x16_bf16 %1, %5, %7, %1"
                     : "=&v"(d0), "=&v"(d1) : "v"(um[0][0]), "v"(um[0][1]), "v"(um[1][0]), "v"(um[1][1]), "v"(p0), "v"(p1));
    }
    f16v a0 = (rr[0][0] * scx[0] + rr[1][0] * scx[1]) + (rr[2][0] * scx[2] + rr[3][0] * scx[3]);
    f16v a1 = (rr[0][1] * scx[0] + rr[1][1] * scx[1]) + (rr[2][1] * scx[2] + rr[3][1] * scx[3]);
    asm volatile("s_nop 11" : "+v"(d0), "+v"(d1), "+v"(a0), "+v"(a1));
    a0 += d0; a1 += d1;
    bar();
#pragma unroll
    for (int g = 0; g < 4; ++g) {
        *(f4 LAS*)(c.sm + w * 8704 + l31 * 272 + (8 * g + 4 * hi) * 4) = (f4){a0[4 * g], a0[4 * g + 1], a0[4 * g + 2], a0[4 * g + 3]};
        *(f4 LAS*)(c.sm + w * 8704 + l31 * 272 + 128 + (8 * g + 4 * hi) * 4) = (f4){a1[4 * g], a1[4 * g + 1], a1[4 * g + 2], a1[4 * g + 3]};
    }
    bar();
    {
        const int t = w * 8 + (lane >> 3), c8 = lane & 7;
        f4 s0 = {0.f, 0.f, 0.f, 0.f}, s1 = s0;
#pragma unroll
        for (int p = 0; p < 4; ++p) { s0 += *(const f4 LAS*)(c.sm + p * 8704 + t * 272 + c8 * 32); s1 += *(const f4 LAS*)(c.sm + p * 8704 + t * 272 + c8 * 32 + 16); }
        sst128((u4){pk2(s0[0], s0[1]), pk2(s0[2], s0[3]), pk2(s1[0], s1[1]), pk2(s1[2], s1[3])}, mksrd(a.out, 2048 * 1792), t * 1792 + c8 * 16, s * 32 * 1792 + 896 + cb * 128);
    }
}

#define PA_UT 0
#define PA_TU 32896
#define PA_KH (PA_TU + 33280)
#define PA_SC (PA_KH + 7680)
#define PA_KT (PA_SC + 128)
#define PA_RED (PA_KT + 24576)
typedef __attribute__((ext_vector_type(2))) _Float16 h2;
typedef __attribute__((ext_vector_type(8))) _Float16 h8;
DI unsigned pkh(float a, float b) { f2 v = {a, b}; return __builtin_bit_cast(unsigned, __builtin_convertvector(v, h2)); }
DI void parta_task(unsigned tag, int wave, int task) {
    CArgs& a = get_args();
    const SideCtx c = make_ctx(wave);
    char LAS* sm = c.sm;
    const int lane = c.lane, w = c.wave, tid = w * 64 + lane, hi = lane >> 5, l31 = lane & 31, l15 = lane & 15, kk = lane >> 4;
    task = __builtin_amdgcn_readfirstlane(task); asm volatile("" : "+s"(task));
    const int d0 = task * 8;
    const u4 rpa = mksrd(a.PA, 56 * 32768);
    bar();
#pragma unroll
    for (int g = 0; g < 2; ++g) {
        const int d = d0 + 4 * g + w;
        const float dt = bfr(expf(bf2f(a.Alogdt[d])));
        const u2 x0 = *(const u2*)(a.AlogA + d * 256 + lane * 4), x1 = *(const u2*)(a.Aim + d * 256 + lane * 4), x2 = *(const u2*)(a.ACr + d * 256 + lane * 4), x3 = *(const u2*)(a.ACi + d * 256 + lane * 4);
        f4 z[4]; unsigned mx = 0;
#pragma unroll
        for (int i = 0; i < 4; ++i) {
            auto el = [&](u2 x) { return bf2f((u16)(x[i >> 1] >> (16 * (i & 1)))); };
            z[i] = (f4){-bfr(expf(el(x0))) * dt * LOG2E, el(x1) * dt * 0.15915494309189535f, 2.f * el(x2) * dt, 2.f * el(x3) * dt};
            mx = max(mx, max(__float_as_uint(z[i][2]) & 0x7f800000u, __float_as_uint(z[i][3]) & 0x7f800000u));
        }
#pragma unroll
        for (int o = 1; o < 64; o <<= 1) mx = max(mx, (unsigned)__shfl_xor((int)mx, o));
        mx = min(max(mx, 0x00800000u), 0x7e800000u);
        *(unsigned LAS*)(sm + PA_SC + (4 * g + w) * 4) = mx;
        const float inv = __uint_as_float(0x7f000000u - mx);
#pragma unroll
        for (int i = 0; i < 4; ++i) { z[i][2] *= inv; z[i][3] *= inv; *(f4 LAS*)(sm + PA_RED + w * 9216 + g * 4096 + (i * 64 + lane) * 16) = z[i]; }
    }
    const u4 dsk4 = *(const u4*)(a.ADsk + d0);
    if (tid < 8) *(u4 LAS*)(sm + PA_UT + tid * 4112 + 4096) = (u4){0u, 0u, 0u, 0u};
    if (tid < 155) *(u4 LAS*)(sm + PA_KH + tid * 16) = (u4){0u, 0u, 0u, 0u};
    float qpos[4];
    qpos[0] = (float)l15;
    qpos[1] = l15 < 8 ? (float)(256 + 2 * l15) : (float)(512 + ((l15 - 8) >> 2) * 256 + (l15 & 3) * 4);
    qpos[2] = l15 < 8 ? (float)(1024 + (l15 >> 1) * 256 + (l15 & 1) * 8) : 2048.f;
    qpos[3] = (float)(16 * l15);
    auto kgen = [&](int g) __attribute__((always_inline)) {
        const char LAS* pt = sm + PA_RED + w * 9216 + g * 4096 + kk * 16;
        f4 acc[3] = {{0.f, 0.f, 0.f, 0.f}, {0.f, 0.f, 0.f, 0.f}, {0.f, 0.f, 0.f, 0.f}};
#pragma nounroll
        for (int st = 0; st < 16; ++st) {
            u4 op[4];
#pragma unroll
            for (int i = 0; i < 4; ++i) {
                const f4 z = *(const f4 LAS*)(pt + st * 256 + i * 64);
#pragma unroll
                for (int t = 0; t < 4; ++t) {
                    const float ex = __builtin_amdgcn_exp2f(z[0] * qpos[t]), rv = __builtin_amdgcn_fractf(z[1] * qpos[t]);
                    const float er = ex * __builtin_amdgcn_cosf(rv), ei = ex * __builtin_amdgcn_sinf(rv);
                    op[t][i] = t < 3 ? pkh(er, ei) : pkh(z[2] * er - z[3] * ei, -(z[2] * ei + z[3] * er));
                }
            }
#pragma unroll
            for (int t = 0; t < 3; ++t) acc[t] = __builtin_amdgcn_mfma_f32_16x16x32_f16(__builtin_bit_cast(h8, op[3]), __builtin_bit_cast(h8, op[t]), acc[t], 0, 0, 0);
        }
#pragma unroll
        for (int t = 0; t < 3; ++t) *(f4 LAS*)(sm + PA_KT + (4 * g + w) * 3072 + ((t * 16 + l15) * 16 + 4 * kk) * 4) = acc[t];
    };
    kgen(0);
    if (A_LATE && task < NA) kgen(1);
    {
        const u4 ru = mksrd(a.Au, 2048 * 896);
#pragma unroll
        for (int i = 0; i < 8; ++i) { dma_one(ru, tid * 896, i * 256 * 896 + d0 * 2, c.smb + PA_TU + i * 4096 + w * 1024); asm volatile("s_sleep %0" :: "i"(P_USLP)); }
    }
    if (!(A_LATE && task < NA)) kgen(1);
    waitvm<0>(); bar();
    u4 uv[8];
#pragma unroll
    for (int i = 0; i < 8; ++i) uv[i] = *(const u4 LAS*)(sm + PA_TU + tid * 128 + i * 16);
#pragma unroll
    for (int ch = 0; ch < 8; ++ch) {
        u4 o;
#pragma unroll
        for (int j = 0; j < 4; ++j) o[j] = __builtin_amdgcn_perm(uv[6 - 2 * j][ch >> 1], uv[7 - 2 * j][ch >> 1], (ch & 1) ? 0x07060302u : 0x05040100u);
        *(u4 LAS*)(sm + PA_UT + ch * 4112 + (255 - tid) * 16) = o;
    }
    {
        typedef __attribute__((ext_vector_type(2))) unsigned short us2;
        u4 mm;
#pragma unroll
        for (int j = 0; j < 4; ++j) {
            us2 m = {0, 0};
#pragma unroll
            for (int i = 0; i < 8; ++i) m = __builtin_elementwise_max(m, __builtin_bit_cast(us2, uv[i][j] & 0x7f807f80u));
#pragma unroll
            for (int o = 1; o < 64; o <<= 1) m = __builtin_elementwise_max(m, __builtin_bit_cast(us2, __shfl_xor(__builtin_bit_cast(int, m), o)));
            mm[j] = __builtin_bit_cast(unsigned, m);
        }
        *(u4 LAS*)(sm + PA_SC + 64 + w * 16) = mm;
    }
    int toff[8];
#pragma unroll
    for (int j = 0; j < 8; ++j) {
        const int p = (int)bfr((float)(tid * 8 + j));
        int col, ar = (p & 255) >> 4;
        if (p < 256) col = p & 15; else if (p < 512) col = 16 + ((p & 15) >> 1); else if (p < 1024) col = 24 + ((p - 512) >> 8) * 4 + ((p & 15) >> 2);
        else if (p < 2048) col = 32 + ((p - 1024) >> 8) * 2 + ((p & 15) >> 3); else col = 40;
        toff[j] = (col * 16 + ar) * 4;
    }
    bar();
    if (tid < 32) *(u4 LAS*)(sm + PA_TU + ((tid >> 2) * 260 + 256 + (tid & 3)) * 16) = (u4){0u, 0u, 0u, 0u};
    {
#pragma nounroll
        for (int ch = 0; ch < 8; ++ch) {
            if (task < NA) slp(ch ? A_SLP : (task % 10) * (A_SLP + 70) / 10);
            const u16 LAS* mp = (const u16 LAS*)(sm + PA_SC + 64) + ch;
            const unsigned ue = min(max((unsigned)max(max(mp[0], mp[8]), max(mp[16], mp[24])) << 16, 0x07800000u), 0x7e800000u) - 0x07000000u;
            {
                const u4 x0 = *(const u4 LAS*)(sm + PA_UT + ch * 4112 + tid * 16), x1 = *(const u4 LAS*)(sm + PA_UT + ch * 4112 + tid * 16 + 16);
                float kv[8];
#pragma unroll
                for (int j = 0; j < 8; ++j) kv[j] = *(const float LAS*)(sm + PA_KT + ch * 3072 + toff[j]);
                const float us = __uint_as_float(0x7f000000u - ue);
                unsigned r[8];
#pragma unroll
                for (int j = 0; j < 8; ++j) { const unsigned x = j < 4 ? x0[j & 3] : x1[j & 3]; r[j] = pkh(__uint_as_float(x << 16) * us, __uint_as_float(x & 0xffff0000u) * us); }
#pragma unroll
                for (int cp = 0; cp < 8; ++cp) {
                    u4 o;
#pragma unroll
                    for (int j = 0; j < 4; ++j) o[j] = (cp & 1) ? __builtin_amdgcn_alignbit(r[(cp >> 1) + j + 1], r[(cp >> 1) + j], 16) : r[(cp >> 1) + j];
                    *(u4 LAS*)(sm + PA_TU + (cp * 260 + tid) * 16) = o;
                }
                *(u4 LAS*)(sm + PA_KH + (31 + (tid >> 2)) * 80 + (tid & 3) * 16) = (u4){pkh(kv[0], kv[1]), pkh(kv[2], kv[3]), pkh(kv[4], kv[5]), pkh(kv[6], kv[7])};
            }
            bar();
            {
                f16v y0, y1;
                const int m00 = 2047 - l31 + 8 * hi, cp = m00 & 7;
                const char LAS* ab = sm + PA_TU + (cp * 260 + (m00 >> 3)) * 16 - 63 * 64;
                const char LAS* bb = sm + PA_KH + l31 * 80 + hi * 16;
                const char LAS* a1w = ab - w * 64; const char LAS* b1w = bb - w * 80;
                const char LAS* a2w = ab + w * 64; const char LAS* b2w = bb + w * 80;
                u4 fr[4][3];
                auto rdg = [&](int g) __attribute__((always_inline)) {
                    const int i = (g & 15) >> 1, ks = g & 1, sl = g & 3;
                    if (g < 16) { fr[sl][0] = *(const u4 LAS*)(b1w + (31 - 4 * i) * 80 + ks * 32); fr[sl][1] = *(const u4 LAS*)(a1w + (63 - 4 * i) * 64 + ks * 32);
                                  fr[sl][2] = *(const u4 LAS*)(a1w + (31 - 4 * i) * 64 + ks * 32); }
                    else { fr[sl][0] = *(const u4 LAS*)(b2w + (32 + 4 * i) * 80 + ks * 32); fr[sl][2] = *(const u4 LAS*)(a2w + (32 + 4 * i) * 64 + ks * 32); }
                };
                rdg(0); rdg(1); rdg(2); SB();
#pragma unroll
                for (int g = 0; g < 32; ++g) {
                    const int sl = g & 3;
                    if (g + 3 < 32) rdg(g + 3);
                    SB();
                    if (g == 0) asm volatile("v_mfma_f32_32x32x16_f16 %0, %2, %4, 0\n\tv_mfma_f32_32x32x16_f16 %1, %3, %4, 0" : "=&v"(y0), "=&v"(y1) : "v"(fr[0][1]), "v"(fr[0][2]), "v"(fr[0][0]));
                    else { if (g < 16) asm volatile("v_mfma_f32_32x32x16_f16 %0, %1, %2, %0" : "+v"(y0) : "v"(fr[sl][1]), "v"(fr[sl][0]));
                           asm volatile("v_mfma_f32_32x32x16_f16 %0, %1, %2, %0" : "+v"(y1) : "v"(fr[sl][2]), "v"(fr[sl][0])); }
                    SB();
                }
                MFW32(y0, y1);
#pragma unroll
                for (int g = 0; g < 4; ++g) {
                    *(f4 LAS*)(sm + PA_RED + w * 9216 + l31 * 144 + (8 * g + 4 * hi) * 4) = (f4){y0[4 * g], y0[4 * g + 1], y0[4 * g + 2], y0[4 * g + 3]};
                    *(f4 LAS*)(sm + PA_RED + w * 9216 + (32 + l31) * 144 + (8 * g + 4 * hi) * 4) = (f4){y1[4 * g], y1[4 * g + 1], y1[4 * g + 2], y1[4 * g + 3]};
                }
            }
            bar();
            {
                f4 s0 = {0.f, 0.f, 0.f, 0.f}, s1 = s0;
#pragma unroll
                for (int p = 0; p < 4; ++p) { const char LAS* q_ = sm + PA_RED + p * 9216 + (tid >> 2) * 144 + (tid & 3) * 32; s0 += *(const f4 LAS*)q_; s1 += *(const f4 LAS*)(q_ + 16); }
                const u4 uu = *(const u4 LAS*)(sm + PA_UT + ch * 4112 + (255 - tid) * 16);
                const unsigned dw = (ch & 4) ? ((ch & 2) ? dsk4[3] : dsk4[2]) : ((ch & 2) ? dsk4[1] : dsk4[0]);
                const float dsk = __uint_as_float((ch & 1) ? dw & 0xffff0000u : dw << 16);
                const float ks_ = *(const float LAS*)(sm + PA_SC + ch * 4), us_ = __uint_as_float(ue);
                float yv[8];
#pragma unroll
                for (int j = 0; j < 8; ++j) yv[j] = bfr((j < 4 ? s0[j & 3] : s1[j & 3]) * us_ * ks_) + bfr(__uint_as_float((j & 1) ? uu[3 - (j >> 1)] << 16 : uu[3 - (j >> 1)] & 0xffff0000u) * dsk);
                sst128((u4){pk2(yv[0], yv[1]), pk2(yv[2], yv[3]), pk2(yv[4], yv[5]), pk2(yv[6], yv[7])}, rpa, tid * 16, (d0 + ch) * 4096);
            }
        }
    }
    set_flag(c, mksrd(a.HB, HB_SZ), O_FA + task * 64, tag);
}
#define P_ASLP 8
DI void pa_out(unsigned tag, int wave, int t) {
    CArgs& a = get_args();
    const SideCtx c = make_ctx(wave);
    const int lane = c.lane, tid = c.wave * 64 + lane;
    t = __builtin_amdgcn_readfirstlane(t); asm volatile("" : "+s"(t));
    const u4 rpa = mksrd(a.PA, 56 * 32768), ro = mksrd(a.out, 2048 * 1792);
    wait_flags(mksrd(a.HB, HB_SZ), lane < 8 ? O_FA + ((t & ~7) + lane) * 64 : OOB, tag);
    u4 v[8];
#pragma unroll
    for (int i = 0; i < 8; ++i) v[i] = sld128(rpa, (tid >> 5) * 4096 + (tid & 31) * 16, ((t >> 3) * 64 + i * 8) * 4096 + (t & 7) * 512);
    WAITV(0, "+v"(v[0]), "+v"(v[1]), "+v"(v[2]), "+v"(v[3]), "+v"(v[4]), "+v"(v[5]), "+v"(v[6]), "+v"(v[7]));
    bar();
#pragma unroll
    for (int i = 0; i < 8; ++i) *(u4 LAS*)(c.sm + (i * 8 + (tid >> 5)) * 528 + (tid & 31) * 16) = v[i];
    bar();
#pragma unroll
    for (int k = 0; k < 8; ++k) {
        const char LAS* p = c.sm + (tid & 7) * 8 * 528 + ((tid >> 3) + 32 * k) * 2;
        u4 o;
#pragma unroll
        for (int j = 0; j < 4; ++j) o[j] = (unsigned)*(const u16 LAS*)(p + (2 * j) * 528) | ((unsigned)*(const u16 LAS*)(p + (2 * j + 1) * 528) << 16);
        sst128(o, ro, (tid >> 3) * 1792 + (tid & 7) * 16, ((t & 7) * 256 + 32 * k) * 1792 + (t >> 3) * 128);
        asm volatile("s_sleep %0" :: "i"(P_ASLP));
    }
}
#define P_A 208
#define P_B 256
#define P_R 92
#define P_EPS 64
#define P_M 0ull

#define P_OWN 1
#define P_S1 1
#define P_S3 5
#define P_STG 8
#define P_XOR 4
#define L_SCR 28672
#define V_M P_M
#define V_A P_A
#define V_B P_B

DI void chain_role(unsigned tag, int vb, int wave) {
    CArgs& a = get_args();
    const int nx = vb & 3, id = vb >> 3, W = wave;
    const int lane = fresh_lane(), q = lane >> 4, n = lane & 15;
    const u4 r = mksrd(a.HB, HB_SZ);
    for (int i = W * 64 + lane; i < 1792; i += 256) *(u4 LAS*)(smem_ + i * 16) = (u4){0u, 0u, 0u, 0u};
    const int o_need = id < 16 ? O_FW + (W >> 1) * 8192 + ((nx * 4 + (id >> 2)) * 4 + (lane & 3)) * 64 : id < 20 ? O_FG + ((nx * 4 + id - 16) * 8 + (lane & 3)) * 64
                     : id < 27 ? O_PAR + 12 : O_FW + 8192 + (W & 1) * 4096 + lane * 64;
    const unsigned want = tag + (id >= 16 && id < 20);
    unsigned win, t0;
    {
        unsigned sp = 0;
#pragma nounroll
        for (;;) {
            u4 v = sld128(r, O_PAR, 0); unsigned f = sld32(r, o_need, 0); u2 w = sld64(r, O_PAR + 512 + lane * 32, 0); WAITV(0, "+v"(v), "+v"(f), "+v"(w));
            win = w[0]; t0 = __builtin_amdgcn_readfirstlane(v[1]);
            if ((t0 ^ tag) == (unsigned)__builtin_amdgcn_readfirstlane(v[2]) && __all(f - want <= 72u && (w[0] ^ w[1]) == tag)) break;
            asm volatile("s_sleep 2" ::: "memory");
            if (++sp > SPIN_MAX) __builtin_amdgcn_endpgm();
        }
    }
#pragma unroll
    for (int o = 1; o < 64; o <<= 1) win = min(win, (unsigned)__shfl_xor((int)win, o));
    win = __builtin_amdgcn_readfirstlane(win) & 3u;
    const int wA = win * 8192, wB = wA ^ 16384, wF = 524288 + wA;
    auto copy_off = [&](int g) { return (2 + (g >> 1)) * 32768 + ((g & 1) ? wA : wB); };
    if (id == 27) {
        unsigned m[7];
#pragma unroll
        for (int k = 0; k < 7; ++k) m[k] = san(*(const unsigned*)(a.mem0 + (lane >> 3) * 448 + (4 * k + W) * 16 + 4 * (lane & 3) + 2 * ((lane >> 2) & 1)));
        bar();
#pragma unroll
        for (int k = 0; k < 7; ++k) sst32(m[k], r, copy_off(nx) + lane * 4, 3 * 131072 + (4 * k + W) * 256);
        return;
    }
    Ctx c;
    c.r = r; c.rts = r; c.t0 = t0; c.per = V_A + V_B; c.R = P_R; c.tag = tag; c.lv0 = (unsigned)(V_M); c.lv1 = (unsigned)((V_M) >> 32); c.des = P_EPS; c.role = (id < 16 ? 0 : id < 20 ? 4 : 8) + W; c.tf0 = 0;
    c.idx = (lane & 1) ? 0xEEEE : 0x4444; c.vl16 = lane * 16;
    c.vg = c.vg3 = c.vtl = c.vtr = c.vsel = c.vraw = c.vfo = c.ve = c.vs = c.vrw = c.vrr = c.vr1 = c.vr2 = c.vdbg = 0;
    c.vtb = lane < 28 ? O_ANN + lane * 128 : OOB;
    c.vts = 32768 + W * 4096 + (STAMPS ? lane * 4 + (lane < 16 ? 0 : 32768) : 0);
    if (id < 16) {
        const int hb = nx * 16 + id, h = hb >> 2, part = hb & 3;
        c.rw = mksrd(a.SXM, 64 * SH); c.sw = part * 7; c.vsel = h * 4 * SH + NC * 1024 + ((n >> 2) & 1) * 14336 + q * 256 + (4 * (n >> 3) + (n & 3)) * 16;
        c.rx = mksrd(a.SXM, 64 * SH); c.sx = hb * SH + W * 512;
        c.rg = mksrd(a.X, 2048 * 896); c.sg = 0;
        c.vm = copy_off(nx) + (W >> 1) * 4096 + lane * 16;
        c.vtl = q * 1088 + n * 8 + (W >> 1) * 512;
        const int r_ = (lane >> 2) & 3, c_ = lane & 3;
        const int rec = (h >> 1) * 1024 + part * 256 + (h & 1) * 128;
        const int raw = (h >> 2) * 32768 + wF + (h & 3) * 2048 + part * 512;
        c.vfl = lane == 0 ? O_FH + hb * 128 + W * 64 : OOB; c.vpub = OOB;
        if (W < 2) {
            c.vg = (16 * W + (n & 7)) * 896 + (112 * part + 32 * (q >> 1) + 16 * (n >> 3) + 8 * (q & 1)) * 2; c.vg3 = (q >= 2 && n >= 8) ? OOB : c.vg + 128;
            c.vtr = c_ * 1088 + (8 * q + r_) * 8;
            c.vpub = (q >> 1) * 256 + n * 16 + (q & 1) * 8;
        } else if (W == 2) {
            c.vraw = lane < 16 ? (lane < 8 ? wA : wB) + rec + (lane & 7) * 16 : lane < 32 ? raw + (lane - 16) * 16 : id < 14 ? copy_off(nx) + id * 512 + (lane - 32) * 16 : OOB;
        } else {
            c.vg = copy_off(nx) + (7 * part + 2 * (q >> 1) + (n >> 3)) * 256 + (2 * (n & 7) + (q & 1)) * 16; c.vg3 = (q >= 2 && n >= 8) ? OOB : c.vg + 1024;
            c.vtr = c_ * 1088 + (16 * (q >> 1) + 4 * (r_ >> 1) + 2 * (q & 1) + (r_ & 1)) * 8;
            c.vpub = (n < 8 ? wA : wB) + rec + (n & 7) * 16 + q * 4;
            c.vraw = n < 8 ? raw + n * 32 + q * 8 : OOB;
        }
    } else if (id < 20) {
        const int h = nx * 4 + id - 16;
        c.rw = r; c.sw = 0; c.rx = r; c.sx = 0;
        c.rg = mksrd(a.GF, GSZ); c.sg = h * GH + W * 4096;
        c.vm = copy_off(nx) + W * 2048 + lane * 16;
        c.vtl = W * 1280 + 2 * q * 160 + n * 4;
        c.vtr = ((lane >> 2) & 7) * 160 + (8 * W + 2 * (lane & 3)) * 4;
        const int rec = 32768 + (h >> 1) * 1024 + W * 256 + (h & 1) * 128;
        const int raw = (h >> 2) * 32768 + wF + (h & 3) * 2048 + W * 512 + 256;
        c.vpub = (lane < 32 ? wA : wB) + rec + (lane & 31) * 4;
        c.vraw = lane < 32 ? raw + lane * 8 : OOB;
        c.vsel = lane < 16 ? (lane < 8 ? wA : wB) + rec + (lane & 7) * 16 : lane < 32 ? raw + (lane - 16) * 16 : OOB;
        c.vfl = O_FG + (h * 8 + (lane & 7)) * 64;
    } else {
        const int pid = nx * 7 + id - 20;
        c.rw = mksrd(a.UM, 28 * MH); c.sw = pid * MH + NC * 4096 + 4 * W * 14336;
        c.rg = mksrd(a.UF, GSZ); c.sg = pid * UH + W * 4096;
        c.rx = mksrd(a.UM, 28 * MH); c.sx = pid * MH + W * 1024;
        c.tf0 = V_A;
        c.vm = copy_off(nx) + W * 2048 + lane * 16;
        c.vpub = copy_off(W) + pid * 256 + lane * 4;
        c.vs = ((nx & 1) ? wA : wB) + W * 2048 + lane * 16; c.ve = c.vs + 32768;
        auto qb = [](int x) { return x * 256 + (x & 1) * 32 + (x >> 1) * 128; };
        c.vrw = 8192 + W * 1280 + qb(q) + n * 8;
        c.vrr = 8192 + qb(lane & 3) + ((lane >> 3) + 8 * ((lane >> 2) & 1)) * 8;
        c.vr1 = W * 32768 + wF + (n >> 3) * 2048 + (n & 7) * 32 + q * 8;
        c.vr2 = W * 32768 + wF + (n >> 3) * 2048 + q * 512 + 256 + (n & 7) * 32;
        c.vg = O_FG + (((4 * W + (lane & 3)) * 2 + 1) * 4 + (pid >> 3)) * 64;
        c.vfl = lane == 0 ? O_FP + (pid * 4 + W) * 64 : OOB;
        { const int o = P_OWN ? (W - nx - 1) & 3 : W; c.vtl = 8 + 16 * (o == 3 ? P_S3 : o * P_S1); }
        c.vtr = O_FW + ((16 + 4 * W + (lane & 3)) * 4 + (pid >> 3)) * 64;
        c.vg3 = W < 3 ? c.vm + 1024 : c.vm - 6144;
        c.vsel = (pid == 0 && W == 0 && lane == 0) ? O_EPOCH : OOB;
        c.vfo = (lane == 0 && W == 0) ? O_ANN + pid * 128 : OOB;
    }
    PSTAMP(5);
    asm volatile("s_waitcnt lgkmcnt(0)" ::: "memory");
    chain_asm(c);
}

__global__ __launch_bounds__(256, 1) void fused_kernel(Args a_) {
    CArgs& a = get_args();
    const int wave = __builtin_amdgcn_readfirstlane(threadIdx.x >> 6);
#define PT(k)
    const u4 rhb = mksrd(a.HB, HB_SZ);
    unsigned tag;
    asm volatile("s_load_dword %0, %1, %2" : "=s"(tag) : "s"(a.HB), "s"(O_EPOCH) : "memory");
    const int xcc = (__builtin_amdgcn_s_getreg((31 << 11) | 20) & 7) ^ P_XOR;
    int vb = xcc + (blockIdx.x & ~7);
    const bool prober = (vb & 132) == 128 && wave == 3;
    const int po = O_PROBE + (vb & 3) * 32768 + ((vb >> 3) & 15) * 128;
    if (prober) { const int l = fresh_lane(); sst32(0u, rhb, l < 4 ? l * 8192 : OOB, po); }
    if (wave) {
        u64 pc; unsigned len;
        asm volatile("s_getpc_b64 %0\nfk_code_lo:\n\ts_max_i32 %1, fk_code_hi-fk_code_lo, 0" : "=s"(pc), "=s"(len) :: "scc");
        const u4 rc = mksrd((const void*)pc, len & ~15u);
        const int vo = (((blockIdx.x >> 3) * 3 + wave - 1) * 2) * 1024 + (threadIdx.x & 63) * 16;
#pragma unroll
        for (int i = 0; i < 2; ++i) dma_one(rc, vo + i * 1024, 0, (unsigned)(size_t)(char LAS*)smem_ + L_SCR + wave * 1024);
    }
    PT(4);
    unsigned best = 0xfffu;
#pragma nounroll
    for (int pass = 0;; ++pass) {
        gemm_first<0>(wave, (vb & 4) == 0, vb >> 7, 4 * (vb & 3) + ((vb >> 5) & 3), (vb >> 3) & 3);
        unsigned old = 0;
        PT(0);
        if (pass == 0) {
            asm volatile("s_waitcnt lgkmcnt(0)" : "+s"(tag) :: "memory");
            tag += 0x9E3779B4u; old = tag;
            if (wave == 0) asm volatile("s_nop 4\n\tbuffer_atomic_swap %0, %1, %2, %3 offen sc0 sc1" : "+v"(old) : "v"(threadIdx.x == 0 ? O_CNT : OOB), "s"(rhb), "s"((xcc * 32 + (int)(blockIdx.x >> 3)) * 128) : "memory");
        }
        if (pass == 0 && prober) {
            waitvm<18>();
#pragma nounroll
            for (int k = 0; k < 4; ++k) {
                u64 c0, c1; unsigned d;
                asm volatile("s_memtime %0\n\ts_waitcnt lgkmcnt(0)\n\ts_load_dword %2, %3, %4 glc\n\ts_waitcnt lgkmcnt(0)\n\ts_memtime %1\n\ts_waitcnt lgkmcnt(0)" : "=&s"(c0), "=&s"(c1), "=&s"(d) : "s"(a.HB), "s"(po + ((k + (vb >> 3)) & 3) * 8192) : "memory");
                best = min(best, ((unsigned)(c1 - c0) & ~3u) + (((k + (vb >> 3)) & 3) ^ (~vb & 1) * 2));
            }
        }
        PT(1);
        gemm_first<1>(wave, (vb & 4) == 0, vb >> 7, 4 * (vb & 3) + ((vb >> 5) & 3), (vb >> 3) & 3);
        PT(2);
        WAITV(10, "+v"(old));
        PT(3);
        if (pass) { bar(); break; }
        if (threadIdx.x == 0) {
            unsigned slot = (unsigned)xcc * 32u + (blockIdx.x >> 3);
            if (old == tag)
                for (int k = 0; k < 256; ++k) { slot = (slot + 1u) & 255u; if (__hip_atomic_exchange((unsigned*)(a.HB + O_CNT) + slot * 32u, tag, __ATOMIC_RELAXED, __HIP_MEMORY_SCOPE_AGENT) != tag) break; }
            *(unsigned LAS*)(smem_ + L_SCR) = (slot >> 5) + 8u * (slot & 31u);
        }
        bar();
        const int got = __builtin_amdgcn_readfirstlane(*(const unsigned LAS*)(smem_ + L_SCR));
        if (got == vb) break;
        best = 0xfffu;
        vb = got; waitvm<0>(); bar();
    }
    vb = __builtin_amdgcn_readfirstlane(vb); tag = __builtin_amdgcn_readfirstlane(tag);
    const int res = vb & 7, idx = vb >> 3;
    if (res < 4) {
        const int lane = fresh_lane(), nb = res * 32 + idx;
        if (vb == 249 && wave == 2) { const unsigned t0 = rt_now(); sst128((u4){0u, t0, t0 ^ tag, tag}, rhb, lane == 0 ? O_PAR : OOB, 0); }
        if (idx >= 16 && wave == 3) sst64((u2){best, best ^ tag}, rhb, lane == 0 ? O_PAR + 512 + ((idx - 16) * 4 + res) * 32 : OOB, 0);
        sst128((u4){FF, FF, FF, FF}, rhb, lane * 16, nb * 8192 + wave * 2048); sst128((u4){FF, FF, FF, FF}, rhb, lane * 16, nb * 8192 + wave * 2048 + 1024);
        if (nb == 0) sst128((u4){0u, 0u, 0u, 0u}, rhb, lane * 16, O_ANN + wave * 1024);
    }
    if (idx >> 4) gemm_role<1>(tag, vb, wave, res < 4, 4 * (res & 3) + ((idx >> 2) & 3), idx & 3); else gemm_role<0>(tag, vb, wave, res < 4, 4 * (res & 3) + ((idx >> 2) & 3), idx & 3);
    PSTAMP(3);
    if (res < 4 && idx < 28) chain_role(tag, vb, wave);
    constexpr int NO = 8 + NA;
    if (res < 4 && idx < 32 - NA / 4) return;
    const int f = res < 4 ? -1 : ((idx & 2) ? 0 : 64) + (idx >> 2) * 8 + (idx & 1) * 4 + (res - 4);
    if (f < 56 - NA) { parta_task(tag, wave, f < 0 ? res * (NA / 4) + 31 - idx : NA + (f & 3) * (14 - NA / 4) + (f >> 2)); PSTAMP(5); }
    if (f < 0) return;
#pragma nounroll
    for (int it = 0; it < 6; ++it) {
        int t = 0;
        if (f >= 64) t = it == 0 ? 0x3000 + 20 + f - 64 : 0;
        else if (f >= 56 - NA) { const int i = f - (56 - NA) + NO * it; t = i < 36 ? 0x1000 + i : i < 36 + NO ? 0x4000 + i - 36 : 0; }
        else if (f < 16) t = it == 0 ? 0x1000 + 36 + f : it == 1 && f < 8 ? 0x4000 + 48 + f : 0;
        else { const int i = f - 16 + (40 - NA) * it; t = i < 48 - NO ? 0x4000 + NO + i : i < 96 - NO ? 0x2000 + i - (48 - NO) : i < 116 - NO ? 0x3000 + i - (96 - NO) : 0; if (it == 0) slp((f - 16) * P_STG); }
        const int k = t >> 12, j = t & 4095;
        if (k == 1) o_task(tag, wave, j); else if (k == 2) e_task(tag, wave, 52 + (j >> 2), j & 3); else if (k == 3) x_task(tag, wave, 52 + j / 7, j % 7); else if (k == 4) pa_out(tag, wave, j);
    }
    PSTAMP(6);
}
__device__ __attribute__((used, noinline)) void fk_tail() { asm volatile("fk_code_hi:"); }

extern "C" void kernel_launch(void* const* d_in, const int* in_sizes, int n_in, void* d_out, int out_size, void* d_ws, size_t ws_size, hipStream_t stream) {
    Args a;
    a.Au = (const u16*)d_in[0]; a.AlogA = (const u16*)d_in[1]; a.Aim = (const u16*)d_in[2]; a.ACr = (const u16*)d_in[3]; a.ACi = (const u16*)d_in[4];
    a.Alogdt = (const u16*)d_in[5]; a.ADsk = (const u16*)d_in[6]; a.X = (const u16*)d_in[7]; a.mem0 = (const u16*)d_in[8];
    a.Wq = (const u16*)d_in[9]; a.Wk = (const u16*)d_in[10]; a.Wv = (const u16*)d_in[11]; a.Wo = (const u16*)d_in[12];
    a.out = (u16*)d_out;
    char* base = (char*)(((size_t)d_ws + (2u << 20) - 1) & ~(size_t)((2u << 20) - 1));
    a.HB = base;
    size_t off = HB_SZ;
    auto alloc = [&](size_t n) { char* p = base + off; off += (n + 4095) & ~(size_t)4095; return p; };
    a.TS = (unsigned*)alloc(8u << 20);
    a.UM = (u16*)alloc(28 * MH); a.SXM = (float*)alloc(64 * SH); a.GF = (u16*)alloc(GSZ); a.UF = (u16*)alloc(GSZ); a.EX = (u16*)alloc(2u << 20); a.PA = (u16*)alloc(2u << 20); a.MZX = (float*)alloc(64 * 16 * 256);
    (void)hipFuncSetAttribute((const void*)fused_kernel, hipFuncAttributeMaxDynamicSharedMemorySize, 160 * 1024);
    fused_kernel<<<256, 256, 160 * 1024, stream>>>(a);
}
```

```cpp
#define P_A 196
#define P_B 252
#include <hip/hip_runtime.h>
#include <stdint.h>
typedef unsigned short u16;
typedef unsigned long long u64;
typedef __attribute__((ext_vector_type(2))) __bf16 bf2;
typedef __attribute__((ext_vector_type(2))) float f2;
typedef __attribute__((ext_vector_type(4))) float f4;
typedef __attribute__((ext_vector_type(16))) float f16v;
typedef __attribute__((ext_vector_type(2))) unsigned u2;
typedef __attribute__((ext_vector_type(4))) unsigned u4;
typedef __attribute__((ext_vector_type(16))) unsigned u16v;
typedef __attribute__((ext_vector_type(8))) unsigned u8v;
typedef __attribute__((ext_vector_type(4))) short s4;
#define LAS __attribute__((address_space(3)))
#define DI __device__ __forceinline__
#define SB() __builtin_amdgcn_sched_barrier(0)
#define PIN(x) asm volatile("" : "+v"(x))
#define WAITV(n, ...) asm volatile("s_waitcnt vmcnt(" #n ")" : __VA_ARGS__ :: "memory")
#define LOG2E 1.4426950408889634f
#define CSCALE (LOG2E * 0.0625f)
#define FF 0xFFFFFFFFu
#define OOB 0x7f000000
#define SPIN_MAX (1u << 18)

#define HB_SZ (2 << 20)
#define O_CTL (1 << 20)
#define O_ANN O_CTL
#define O_PAR (O_CTL + 4096)
#define O_FW (O_CTL + 8192)
#define O_FG (O_CTL + 24576)
#define O_FE (O_CTL + 32768)
#define O_FH (O_CTL + 49152)
#define O_FP (O_CTL + 57344)
#define O_FA (O_CTL + 65536)
#define O_CNT (O_CTL + 73728)
#define O_EPOCH (O_CNT + 32768)
#define O_PROBE (O_CTL + 131072)

#define GH 1835008
#define UH 1048576
#define NC 8
#define MH (NC * 4096 + 16 * 14336)
#define SH (NC * 1024 + 8 * 14336)
#define GSZ (34 << 20)
struct Args {
    const u16 *Au, *AlogA, *Aim, *ACr, *ACi, *Alogdt, *ADsk, *X, *mem0, *Wq, *Wk, *Wv, *Wo;
    u16* out;
    char* HB;
    u16 *GF, *UF, *EX, *UM, *PA; float *MZX, *SXM;
    unsigned* TS;
};
typedef const Args __attribute__((address_space(4))) CArgs;
DI CArgs& get_args() { CArgs* p = (CArgs*)__builtin_amdgcn_kernarg_segment_ptr(); asm volatile("" : "+s"(p)); return *p; }
extern __shared__ __attribute__((aligned(16))) char smem_[];

DI unsigned pk2(float a, float b) { f2 v = {a, b}; return __builtin_bit_cast(unsigned, __builtin_convertvector(v, bf2)); }
DI float bfr(float a) { return __uint_as_float(pk2(a, a) & 0xffff0000u); }
DI float bf2f(u16 x) { return __uint_as_float((unsigned)x << 16); }
DI unsigned san(unsigned x) { return x < 0xFFFFFFFEu ? x : 0xFFFFFFFEu; }
DI void bar() { asm volatile("s_waitcnt lgkmcnt(0)\n\ts_barrier" ::: "memory"); SB(); }
DI float x32max(float a) { auto r = __builtin_amdgcn_permlane32_swap(__float_as_uint(a), __float_as_uint(a), false, false); return fmaxf(__uint_as_float(r[0]), __uint_as_float(r[1])); }
DI float x32add(float a) { auto r = __builtin_amdgcn_permlane32_swap(__float_as_uint(a), __float_as_uint(a), false, false); return __uint_as_float(r[0]) + __uint_as_float(r[1]); }
DI u2 trread(const char LAS* p) { return __builtin_bit_cast(u2, __builtin_amdgcn_ds_read_tr16_b64_v4i16((LAS s4*)p)); }
DI u4 mksrd(const void* p, unsigned bytes) { u64 x = (u64)p; return (u4){(unsigned)x, (unsigned)(x >> 32) & 0xffffu, bytes, 0x00020000u}; }
DI unsigned now32() { u64 t; asm volatile("s_memtime %0\n\ts_waitcnt lgkmcnt(0)" : "=s"(t) :: "memory"); return (unsigned)t; }
DI unsigned rt_now() { u64 t; asm volatile("s_memrealtime %0\n\ts_waitcnt lgkmcnt(0)" : "=s"(t) :: "memory"); return (unsigned)t; }
DI u4 ld128(u4 srd, int voff, int soff) { u4 v; asm volatile("buffer_load_dwordx4 %0, %1, %2, %3 offen sc1" : "=v"(v) : "v"(voff), "s"(srd), "s"(soff) : "memory"); return v; }
DI void st128(u4 v, u4 srd, int voff, int soff) { asm volatile("buffer_store_dwordx4 %0, %1, %2, %3 offen sc1\n\ts_nop 1" :: "v"(v), "v"(voff), "s"(srd), "s"(soff) : "memory"); }
DI void st32(unsigned v, u4 srd, int voff, int soff) { asm volatile("buffer_store_dword %0, %1, %2, %3 offen sc1" :: "v"(v), "v"(voff), "s"(srd), "s"(soff) : "memory"); }
DI u4 sld128(u4 srd, int voff, int soff) { u4 v; asm volatile("s_nop 4\n\tbuffer_load_dwordx4 %0, %1, %2, %3 offen sc1" : "=v"(v) : "v"(voff), "s"(srd), "s"(soff) : "memory"); return v; }
DI u2 sld64(u4 srd, int voff, int soff) { u2 v; asm volatile("s_nop 4\n\tbuffer_load_dwordx2 %0, %1, %2, %3 offen sc1" : "=v"(v) : "v"(voff), "s"(srd), "s"(soff) : "memory"); return v; }
DI unsigned sld32(u4 srd, int voff, int soff) { unsigned v; asm volatile("s_nop 4\n\tbuffer_load_dword %0, %1, %2, %3 offen sc1" : "=v"(v) : "v"(voff), "s"(srd), "s"(soff) : "memory"); return v; }
DI void sst128(u4 v, u4 srd, int voff, int soff) { asm volatile("s_nop 4\n\tbuffer_store_dwordx4 %0, %1, %2, %3 offen sc1\n\ts_nop 1" :: "v"(v), "v"(voff), "s"(srd), "s"(soff) : "memory"); }
DI void sst64(u2 v, u4 srd, int voff, int soff) { asm volatile("s_nop 4\n\tbuffer_store_dwordx2 %0, %1, %2, %3 offen sc1" :: "v"(v), "v"(voff), "s"(srd), "s"(soff) : "memory"); }
DI void sst32(unsigned v, u4 srd, int voff, int soff) { asm volatile("s_nop 4\n\tbuffer_store_dword %0, %1, %2, %3 offen sc1" :: "v"(v), "v"(voff), "s"(srd), "s"(soff) : "memory"); }
template <int N> DI void waitvm() { asm volatile("s_waitcnt vmcnt(%0)" :: "i"(N) : "memory"); }
DI int fresh_lane() { int l; asm volatile("v_mbcnt_lo_u32_b32 %0, -1, 0\n\tv_mbcnt_hi_u32_b32 %0, -1, %0" : "=v"(l)); __builtin_assume(l >= 0 && l < 64); return l; }
#define W_SLP 8
template <int SLP = W_SLP>
DI void wait_flags(u4 rhb, int voff_or_oob, unsigned want) {
    unsigned sp = 0;
#pragma nounroll
    for (;;) {
        unsigned v = sld32(rhb, voff_or_oob, 0); WAITV(0, "+v"(v));
        if (__all((v - want <= 72u) | (voff_or_oob >= OOB))) break;
        unsigned k = __builtin_amdgcn_readfirstlane(want - v);
        k = k > 64u ? (SLP < W_SLP ? 1u : 2u) : k;
        asm volatile("s_sleep %0" :: "i"(SLP) : "memory");
#pragma nounroll
        for (unsigned i = 1; i < k; ++i) asm volatile("s_sleep 30" ::: "memory");
        if (++sp > SPIN_MAX / 16) __builtin_amdgcn_endpgm();
    }
}
DI int need_off(int nx, int id, int W, int lane) {
    return id < 16 ? O_FW + (W >> 1) * 8192 + ((nx * 4 + (id >> 2)) * 4 + (lane & 3)) * 64 : id < 20 ? O_FG + ((nx * 4 + id - 16) * 8 + (lane & 3)) * 64
         : id < 27 ? O_FW + ((16 + 4 * W + (lane & 3)) * 4 + ((nx * 7 + id - 20) >> 3)) * 64 : O_FW + 8192 + (W & 1) * 4096 + lane * 64;
}
#define PSTAMP(k) do { } while (0)
#define RFL(x) (unsigned)__builtin_amdgcn_readfirstlane(x)
#define STAMPS 0
struct Ctx { u4 r, rw, rg, rx, rts; unsigned lv0, lv1, t0, tf0, per, R, tag, sw, sg, sx, des, role; unsigned idx, vl16, vm, vg, vg3, vtl, vtr, vsel, vpub, vraw, vfl, vfo, ve, vs, vrw, vrr, vr1, vr2, vts, vdbg, vtb; };
DI void chain_asm(const Ctx& c) {
    const u16v s0 = {RFL(c.r[0]), RFL(c.r[1]), RFL(c.r[2]), RFL(c.r[3]), RFL(c.rw[0]), RFL(c.rw[1]), RFL(c.rw[2]), RFL(c.rw[3]), RFL(c.rg[0]), RFL(c.rg[1]), RFL(c.rg[2]), RFL(c.rg[3]), RFL(c.rx[0]), RFL(c.rx[1]), RFL(c.rx[2]), RFL(c.rx[3])}, s1 = {RFL(c.rts[0]), RFL(c.rts[1]), RFL(c.rts[2]), RFL(c.rts[3]), RFL(c.lv0), RFL(c.lv1), RFL(c.t0), RFL(c.tf0), RFL(c.per), RFL(c.R), RFL(c.tag), RFL(c.sw), RFL(c.sg), RFL(c.sx), RFL(c.des), RFL(c.role)}, v0 = {c.idx, c.vl16, c.vm, c.vg, c.vg3, c.vtl, c.vtr, c.vsel, c.vpub, c.vraw, c.vfl, c.vfo, c.ve, c.vs, c.vrw, c.vrr}, v1 = {c.vr1, c.vr2, c.vts, c.vdbg, c.vtb, 0u, 0u, 0u, 0u, 0u, 0u, 0u, 0u, 0u, 0u, 0u};
    asm volatile(
"s_nop 4\ns_cmp_eq_u32 s39, 0\ns_cbranch_scc1 fk_jh0\ns_cmp_eq_u32 s39, 1\n"
"s_cbranch_scc1 fk_jh1\ns_cmp_eq_u32 s39, 2\ns_cbranch_scc1 fk_jh2\ns_cmp_eq_u32 s39, 3\n"
"s_cbranch_scc1 fk_jh3\ns_cmp_eq_u32 s39, 4\ns_cbranch_scc1 fk_jx0\ns_cmp_eq_u32 s39, 5\n"
"s_cbranch_scc1 fk_jx1\ns_cmp_eq_u32 s39, 6\ns_cbranch_scc1 fk_jx2\ns_cmp_eq_u32 s39, 7\n"
"s_cbranch_scc1 fk_jx3\ns_cmp_ge_u32 s39, 8\ns_cbranch_scc1 fk_jp0\ns_endpgm\n"
"fk_jh0:\ns_branch fk_h0\nfk_jh1:\ns_branch fk_h1\n"
"fk_jh2:\ns_branch fk_h2\nfk_jh3:\ns_branch fk_h3\n"
"fk_jx0:\ns_branch fk_x0\nfk_jx1:\ns_branch fk_x1\n"
"fk_jx2:\ns_branch fk_x2\nfk_jx3:\ns_branch fk_x3\n"
"fk_jp0:\ns_branch fk_p0\nfk_h0:\ns_mov_b32 s40, 0\n"
"s_mov_b32 s41, 0\ns_mov_b32 s42, 262144\ns_mov_b32 s43, 393216\ns_mov_b32 s44, s30\n"
"s_mov_b32 s45, 0\ns_bcnt1_i32_b64 s46, s[28:29]\ns_mov_b32 s92, 99\ns_mov_b32 s49, 0\n"
"s_mov_b32 s53, 0\ns_mov_b32 s50, s36\ns_mov_b32 s51, s37\nv_mov_b32 v131, v13\n"
"v_mov_b32 v132, v14\nv_mov_b32 v134, 24576\nv_mov_b32 v115, 0\nv_mov_b32 v120, -1\n"
"v_mov_b32 v121, -1\nv_mov_b32 v122, -1\nv_mov_b32 v123, -1\ns_add_u32 s47, s35, 0\n"
"s_and_b32 s52, s47, 1\ns_mul_i32 s52, s52, -14208\ns_mul_i32 s48, s47, 14336\ns_add_u32 s48, s48, s52\n"
"s_lshr_b32 s52, s47, 3\ns_mul_i32 s52, s52, 8192\ns_add_u32 s48, s48, s52\ns_add_u32 s47, s48, 0\n"
"buffer_load_dwordx4 a[0:3], v15, s[12:15], s47 offen sc1\nbuffer_load_dwordx4 a[4:7], v15, s[12:15], s47 offen offset:1024 sc1\ns_add_u32 s47, s48, 2048\nbuffer_load_dwordx4 a[8:11], v15, s[12:15], s47 offen sc1\n"
"buffer_load_dwordx4 a[12:15], v15, s[12:15], s47 offen offset:1024 sc1\ns_add_u32 s47, s48, 4096\nbuffer_load_dwordx4 a[16:19], v15, s[12:15], s47 offen sc1\nbuffer_load_dwordx4 a[20:23], v15, s[12:15], s47 offen offset:1024 sc1\n"
"s_add_u32 s47, s48, 6144\nbuffer_load_dwordx4 a[24:27], v15, s[12:15], s47 offen sc1\nbuffer_load_dwordx4 a[28:31], v15, s[12:15], s47 offen offset:1024 sc1\ns_add_u32 s47, s35, 1\n"
"s_and_b32 s52, s47, 1\ns_mul_i32 s52, s52, -14208\ns_mul_i32 s48, s47, 14336\ns_add_u32 s48, s48, s52\n"
"s_lshr_b32 s52, s47, 3\ns_mul_i32 s52, s52, 8192\ns_add_u32 s48, s48, s52\ns_add_u32 s47, s48, 0\n"
"buffer_load_dwordx4 a[32:35], v15, s[12:15], s47 offen sc1\nbuffer_load_dwordx4 a[36:39], v15, s[12:15], s47 offen offset:1024 sc1\ns_add_u32 s47, s48, 2048\nbuffer_load_dwordx4 a[40:43], v15, s[12:15], s47 offen sc1\n"
"buffer_load_dwordx4 a[44:47], v15, s[12:15], s47 offen offset:1024 sc1\ns_add_u32 s47, s48, 4096\nbuffer_load_dwordx4 a[48:51], v15, s[12:15], s47 offen sc1\nbuffer_load_dwordx4 a[52:55], v15, s[12:15], s47 offen offset:1024 sc1\n"
"s_add_u32 s47, s48, 6144\nbuffer_load_dwordx4 a[56:59], v15, s[12:15], s47 offen sc1\nbuffer_load_dwordx4 a[60:63], v15, s[12:15], s47 offen offset:1024 sc1\ns_add_u32 s47, s35, 2\n"
"s_and_b32 s52, s47, 1\ns_mul_i32 s52, s52, -14208\ns_mul_i32 s48, s47, 14336\ns_add_u32 s48, s48, s52\n"
"s_lshr_b32 s52, s47, 3\ns_mul_i32 s52, s52, 8192\ns_add_u32 s48, s48, s52\ns_add_u32 s47, s48, 0\n"
"buffer_load_dwordx4 a[64:67], v15, s[12:15], s47 offen sc1\nbuffer_load_dwordx4 a[68:71], v15, s[12:15], s47 offen offset:1024 sc1\ns_add_u32 s47, s48, 2048\nbuffer_load_dwordx4 a[72:75], v15, s[12:15], s47 offen sc1\n"
"buffer_load_dwordx4 a[76:79], v15, s[12:15], s47 offen offset:1024 sc1\ns_add_u32 s47, s48, 4096\nbuffer_load_dwordx4 a[80:83], v15, s[12:15], s47 offen sc1\nbuffer_load_dwordx4 a[84:87], v15, s[12:15], s47 offen offset:1024 sc1\n"
"s_add_u32 s47, s48, 6144\nbuffer_load_dwordx4 a[88:91], v15, s[12:15], s47 offen sc1\nbuffer_load_dwordx4 a[92:95], v15, s[12:15], s47 offen offset:1024 sc1\ns_add_u32 s47, s35, 3\n"
"s_and_b32 s52, s47, 1\ns_mul_i32 s52, s52, -14208\ns_mul_i32 s48, s47, 14336\ns_add_u32 s48, s48, s52\n"
"s_lshr_b32 s52, s47, 3\ns_mul_i32 s52, s52, 8192\ns_add_u32 s48, s48, s52\ns_add_u32 s47, s48, 0\n"
"buffer_load_dwordx4 a[96:99], v15, s[12:15], s47 offen sc1\nbuffer_load_dwordx4 a[100:103], v15, s[12:15], s47 offen offset:1024 sc1\ns_add_u32 s47, s48, 2048\nbuffer_load_dwordx4 a[104:107], v15, s[12:15], s47 offen sc1\n"
"buffer_load_dwordx4 a[108:111], v15, s[12:15], s47 offen offset:1024 sc1\ns_add_u32 s47, s48, 4096\nbuffer_load_dwordx4 a[112:115], v15, s[12:15], s47 offen sc1\nbuffer_load_dwordx4 a[116:119], v15, s[12:15], s47 offen offset:1024 sc1\n"
"s_add_u32 s47, s48, 6144\nbuffer_load_dwordx4 a[120:123], v15, s[12:15], s47 offen sc1\nbuffer_load_dwordx4 a[124:127], v15, s[12:15], s47 offen offset:1024 sc1\ns_add_u32 s47, s50, 7168\n"
"buffer_load_dwordx4 v[72:75], v11, s[16:19], s50 offen\nbuffer_load_dwordx4 v[76:79], v12, s[16:19], s50 offen\nbuffer_load_dwordx4 v[80:83], v11, s[16:19], s47 offen\nbuffer_load_dwordx4 v[84:87], v12, s[16:19], s47 offen\n"
"s_add_u32 s50, s50, 28672\ns_waitcnt vmcnt(0)\ns_barrier\ns_memrealtime s[54:55]\n"
"s_waitcnt lgkmcnt(0)\ns_add_u32 s44, s54, 16\ns_mov_b32 s45, 8\n.p2align 6\n"
"top_h0:\nv_mov_b32 v56, 0\nv_mov_b32 v57, 0\nv_mov_b32 v58, 0\n"
"v_mov_b32 v59, 0\nv_mov_b32 v60, 0\nv_mov_b32 v61, 0\nv_mov_b32 v62, 0\n"
"v_mov_b32 v63, 0\nv_mov_b32 v64, 0\nv_mov_b32 v65, 0\nv_mov_b32 v66, 0\n"
"v_mov_b32 v67, 0\nv_mov_b32 v68, 0\nv_mov_b32 v69, 0\nv_mov_b32 v70, 0\n"
"v_mov_b32 v71, 0\nv_mov_b32 v128, 0\ns_mul_i32 s48, s45, 6\ns_and_b32 s48, s48, -4\n"
"s_getpc_b64 s[60:61]\nspc_h0:\ns_sub_u32 s48, sgo_h0-spc_h0, s48\ns_add_u32 s60, s60, s48\n"
"s_addc_u32 s61, s61, 0\nsf_h0:\ns_memrealtime s[54:55]\ns_nop 2\n"
"s_memrealtime s[56:57]\ns_nop 2\ns_memrealtime s[58:59]\nsg_h0:\n"
"s_waitcnt lgkmcnt(4)\ns_sub_u32 s47, s54, s44\ns_add_u32 s48, s47, 12\ns_cmp_ge_u32 s48, 12\n"
"s_cbranch_scc1 so_h0\ns_memrealtime s[54:55]\ns_waitcnt lgkmcnt(4)\ns_cmp_ge_u32 s56, s44\n"
"s_cbranch_scc1 sx_h0\ns_memrealtime s[56:57]\ns_waitcnt lgkmcnt(4)\ns_cmp_ge_u32 s58, s44\n"
"s_cbranch_scc1 sx_h0\ns_memrealtime s[58:59]\ns_waitcnt lgkmcnt(4)\ns_cmp_ge_u32 s54, s44\n"
"s_cbranch_scc1 sx_h0\ns_memrealtime s[54:55]\ns_waitcnt lgkmcnt(4)\ns_cmp_ge_u32 s56, s44\n"
"s_cbranch_scc1 sx_h0\ns_memrealtime s[56:57]\ns_waitcnt lgkmcnt(4)\ns_cmp_ge_u32 s58, s44\n"
"s_cbranch_scc1 sx_h0\ns_memrealtime s[58:59]\ns_waitcnt lgkmcnt(4)\ns_cmp_ge_u32 s54, s44\n"
"s_cbranch_scc1 sx_h0\ns_memrealtime s[54:55]\ns_waitcnt lgkmcnt(4)\ns_cmp_ge_u32 s56, s44\n"
"s_cbranch_scc1 sx_h0\ns_memrealtime s[56:57]\ns_waitcnt lgkmcnt(4)\ns_cmp_ge_u32 s58, s44\n"
"s_cbranch_scc1 sx_h0\ns_memrealtime s[58:59]\ns_waitcnt lgkmcnt(4)\ns_cmp_ge_u32 s54, s44\n"
"s_cbranch_scc1 sx_h0\ns_memrealtime s[54:55]\ns_waitcnt lgkmcnt(4)\ns_cmp_ge_u32 s56, s44\n"
"s_cbranch_scc1 sx_h0\ns_memrealtime s[56:57]\ns_waitcnt lgkmcnt(4)\ns_cmp_ge_u32 s58, s44\n"
"s_cbranch_scc1 sx_h0\ns_memrealtime s[58:59]\ns_waitcnt lgkmcnt(4)\ns_cmp_ge_u32 s54, s44\n"
"s_cbranch_scc1 sx_h0\ns_memrealtime s[54:55]\ns_waitcnt lgkmcnt(4)\ns_cmp_ge_u32 s56, s44\n"
"s_cbranch_scc1 sx_h0\ns_memrealtime s[56:57]\ns_waitcnt lgkmcnt(4)\ns_cmp_ge_u32 s58, s44\n"
"s_cbranch_scc1 sx_h0\ns_memrealtime s[58:59]\ns_waitcnt lgkmcnt(4)\ns_cmp_ge_u32 s54, s44\n"
"s_cbranch_scc1 sx_h0\ns_memrealtime s[54:55]\ns_waitcnt lgkmcnt(4)\ns_cmp_ge_u32 s56, s44\n"
"s_cbranch_scc1 sx_h0\ns_memrealtime s[56:57]\ns_branch sx_h0\nso_h0:\n"
"s_waitcnt lgkmcnt(0)\ns_cmp_lt_i32 s47, 0\ns_cbranch_scc1 sf_h0\ns_branch sgo_h0\n"
"sx_h0:\ns_setpc_b64 s[60:61]\ns_nop 0\ns_nop 0\n"
"s_nop 0\ns_nop 0\ns_nop 0\ns_nop 0\n"
"s_nop 0\ns_nop 0\ns_nop 0\ns_nop 0\n"
"s_nop 0\ns_nop 0\ns_nop 0\ns_nop 0\n"
"s_nop 0\ns_nop 0\ns_nop 0\ns_nop 0\n"
"s_nop 0\ns_nop 0\ns_nop 0\ns_nop 0\n"
"s_nop 0\ns_nop 0\nsgo_h0:\nbuffer_load_dwordx4 v[40:43], v10, s[8:11], s43 offen offset:0 sc1\n"
"buffer_load_dwordx4 v[44:47], v10, s[8:11], s43 offen offset:1024 sc1\nbuffer_load_dwordx4 v[48:51], v10, s[8:11], s43 offen offset:2048 sc1\ns_nop 15\ns_nop 7\n"
"buffer_load_dwordx4 v[52:55], v10, s[8:11], s43 offen offset:3072 sc1\nlanded_h0:\ns_waitcnt vmcnt(3)\nv_smfmac_f32_16x16x64_bf16 v[56:59], v[40:43], a[0:7], v8\n"
"v_max3_u32 v128, v128, v40, v41\nv_smfmac_f32_16x16x64_bf16 v[60:63], v[40:43], a[32:39], v8\nv_max3_u32 v128, v128, v42, v43\nv_smfmac_f32_16x16x64_bf16 v[64:67], v[40:43], a[64:71], v8\n"
"v_smfmac_f32_16x16x64_bf16 v[68:71], v[40:43], a[96:103], v8\ns_waitcnt vmcnt(2)\nv_smfmac_f32_16x16x64_bf16 v[56:59], v[44:47], a[8:15], v8\nv_max3_u32 v128, v128, v44, v45\n"
"v_smfmac_f32_16x16x64_bf16 v[60:63], v[44:47], a[40:47], v8\nv_max3_u32 v128, v128, v46, v47\nv_smfmac_f32_16x16x64_bf16 v[64:67], v[44:47], a[72:79], v8\nv_smfmac_f32_16x16x64_bf16 v[68:71], v[44:47], a[104:111], v8\n"
"s_waitcnt vmcnt(1)\nv_smfmac_f32_16x16x64_bf16 v[56:59], v[48:51], a[16:23], v8\nv_max3_u32 v128, v128, v48, v49\nv_smfmac_f32_16x16x64_bf16 v[60:63], v[48:51], a[48:55], v8\n"
"v_max3_u32 v128, v128, v50, v51\nv_smfmac_f32_16x16x64_bf16 v[64:67], v[48:51], a[80:87], v8\nv_smfmac_f32_16x16x64_bf16 v[68:71], v[48:51], a[112:119], v8\ns_waitcnt vmcnt(0)\n"
"v_smfmac_f32_16x16x64_bf16 v[56:59], v[52:55], a[24:31], v8\nv_max3_u32 v128, v128, v52, v53\nv_smfmac_f32_16x16x64_bf16 v[60:63], v[52:55], a[56:63], v8\nv_max3_u32 v128, v128, v54, v55\n"
"v_smfmac_f32_16x16x64_bf16 v[64:67], v[52:55], a[88:95], v8\nv_cmp_eq_u32 vcc, -1, v128\nv_smfmac_f32_16x16x64_bf16 v[68:71], v[52:55], a[120:127], v8\nv_add_f32 v129, v56, v57\n"
"v_add_f32 v130, v58, v59\nv_cvt_pk_bf16_f32 v112, v129, v130\nv_add_f32 v129, v60, v61\nv_add_f32 v130, v62, v63\n"
"v_cvt_pk_bf16_f32 v113, v129, v130\nv_add_f32 v129, v64, v65\nv_add_f32 v130, v66, v67\nv_cvt_pk_bf16_f32 v114, v129, v130\n"
"v_add_f32 v129, v68, v69\nv_add_f32 v130, v70, v71\nv_cvt_pk_bf16_f32 v115, v129, v130\nds_write_b64 v131, v[112:113] offset:0\n"
"ds_write_b64 v131, v[114:115] offset:128\ns_cbranch_vccnz retry_h0\ns_waitcnt lgkmcnt(0)\ns_barrier\n"
"s_nop 9\nds_read_b64_tr_b16 v[88:89], v132 offset:0\nds_read_b64_tr_b16 v[90:91], v132 offset:32\nds_read_b64_tr_b16 v[92:93], v132 offset:256\n"
"ds_read_b64_tr_b16 v[94:95], v132 offset:288\nds_read_b64_tr_b16 v[96:97], v132 offset:512\nds_read_b64_tr_b16 v[98:99], v132 offset:544\nds_read_b64_tr_b16 v[100:101], v132 offset:768\n"
"ds_read_b64_tr_b16 v[102:103], v132 offset:800\ns_waitcnt lgkmcnt(6)\nv_mfma_f32_16x16x32_bf16 v[104:107], v[88:91], v[72:75], 0\nv_mfma_f32_16x16x32_bf16 v[108:111], v[88:91], v[80:83], 0\n"
"s_waitcnt lgkmcnt(4)\nv_mfma_f32_16x16x32_bf16 v[104:107], v[92:95], v[76:79], v[104:107]\nv_mfma_f32_16x16x32_bf16 v[108:111], v[92:95], v[84:87], v[108:111]\ns_waitcnt lgkmcnt(2)\n"
"v_mfma_f32_16x16x32_bf16 v[104:107], v[96:99], v[72:75], v[104:107]\nv_mfma_f32_16x16x32_bf16 v[108:111], v[96:99], v[80:83], v[108:111]\ns_waitcnt lgkmcnt(0)\nv_mfma_f32_16x16x32_bf16 v[104:107], v[100:103], v[76:79], v[104:107]\n"
"v_mfma_f32_16x16x32_bf16 v[108:111], v[100:103], v[84:87], v[108:111]\ns_nop 7\nv_add_f32_dpp v124, v106, v104 row_ror:8 row_mask:0xf bank_mask:0x3\nv_add_f32_dpp v124, v108, v110 row_ror:8 row_mask:0xf bank_mask:0xc\n"
"v_add_f32_dpp v125, v107, v105 row_ror:8 row_mask:0xf bank_mask:0x3\nv_add_f32_dpp v125, v109, v111 row_ror:8 row_mask:0xf bank_mask:0xc\ns_add_u32 s47, s50, 7168\nbuffer_load_dwordx4 v[72:75], v11, s[16:19], s50 offen\n"
"buffer_load_dwordx4 v[76:79], v12, s[16:19], s50 offen\nbuffer_load_dwordx4 v[80:83], v11, s[16:19], s47 offen\nbuffer_load_dwordx4 v[84:87], v12, s[16:19], s47 offen\ns_add_u32 s50, s50, 28672\n"
"buffer_store_dwordx2 v[124:125], v16, s[20:23], s51 offen sc1\ns_add_u32 s51, s51, 1024\ns_add_u32 s47, s40, s34\nv_mov_b32 v135, s47\n"
"buffer_store_dword v135, v18, s[8:11], 0 offen sc1\nds_read_b32 v133, v134\nv_xor_b32 v131, 8192, v131\nv_xor_b32 v132, 8192, v132\n"
"s_waitcnt lgkmcnt(0)\nv_readfirstlane_b32 s47, v133\ns_sub_u32 s47, s47, s40\ns_cmp_eq_u32 s47, 1\n"
"s_cbranch_scc1 adopt_h0\nback_h0:\ns_mul_i32 s48, s46, 8\ns_add_u32 s45, s45, s48\n"
"s_add_u32 s45, s45, s32\ns_lshr_b32 s48, s45, 4\ns_lshl_b32 s48, s48, 2\ns_add_u32 s44, s44, s48\n"
"s_and_b32 s45, s45, 15\ns_add_u32 s52, s40, 1\ns_bitcmp1_b64 s[28:29], s52\ns_subb_u32 s46, s46, 0\n"
"s_add_u32 s40, s40, 1\ns_mov_b32 s43, s41\ns_add_u32 s41, s41, 131072\ns_and_b32 s41, s41, 524287\n"
"s_add_u32 s92, s92, 1\ns_xor_b32 s42, s41, 262144\ns_waitcnt lgkmcnt(0)\ns_cmp_lt_u32 s40, 64\n"
"s_cbranch_scc1 top_h0\ns_add_u32 s47, s34, 64\nv_mov_b32 v135, s47\ns_waitcnt vmcnt(0)\n"
"buffer_store_dword v135, v18, s[8:11], 0 offen sc1\ns_branch end_h0\nretry2_h0:\ns_sleep 3\n"
"retry_h0:\ns_add_u32 s53, s53, 1\ns_add_u32 s49, s49, 1\ns_cmp_gt_u32 s49, 65536\n"
"s_cbranch_scc1 end_h0\nbuffer_load_dwordx4 v[40:43], v10, s[8:11], s43 offen offset:0 sc1\nbuffer_load_dwordx4 v[44:47], v10, s[8:11], s43 offen offset:1024 sc1\nbuffer_load_dwordx4 v[48:51], v10, s[8:11], s43 offen offset:2048 sc1\n"
"buffer_load_dwordx4 v[52:55], v10, s[8:11], s43 offen offset:3072 sc1\nv_mov_b32 v128, 0\ns_add_u32 s47, s40, 1\nv_mov_b32 v133, s47\n"
"ds_write_b32 v134, v133\ns_waitcnt vmcnt(0)\nv_max3_u32 v128, v128, v40, v41\nv_max3_u32 v128, v128, v42, v43\n"
"v_max3_u32 v128, v128, v44, v45\nv_max3_u32 v128, v128, v46, v47\nv_max3_u32 v128, v128, v48, v49\nv_max3_u32 v128, v128, v50, v51\n"
"v_max3_u32 v128, v128, v52, v53\nv_max3_u32 v128, v128, v54, v55\nv_cmp_eq_u32 vcc, -1, v128\nv_mov_b32 v56, 0\n"
"v_mov_b32 v57, 0\nv_mov_b32 v58, 0\nv_mov_b32 v59, 0\nv_mov_b32 v60, 0\n"
"v_mov_b32 v61, 0\nv_mov_b32 v62, 0\nv_mov_b32 v63, 0\nv_mov_b32 v64, 0\n"
"v_mov_b32 v65, 0\nv_mov_b32 v66, 0\nv_mov_b32 v67, 0\nv_mov_b32 v68, 0\n"
"v_mov_b32 v69, 0\nv_mov_b32 v70, 0\nv_mov_b32 v71, 0\ns_cbranch_vccnz retry2_h0\n"
"s_branch landed_h0\nadopt_h0:\nbuffer_load_dword v116, v28, s[8:11], 0 offen sc1\ns_lshl_b32 s52, s40, 26\n"
"s_or_b32 s47, s52, 0x3ffffff\ns_waitcnt vmcnt(0)\nv_cmp_ge_u32 vcc, s47, v116\ns_nop 1\n"
"v_cndmask_b32 v133, 0, v116, vcc\ns_nop 1\nv_max_u32_dpp v133, v133, v133 row_ror:8 row_mask:0xf bank_mask:0xf\ns_nop 1\n"
"v_max_u32_dpp v133, v133, v133 row_ror:4 row_mask:0xf bank_mask:0xf\ns_nop 1\nv_max_u32_dpp v133, v133, v133 row_ror:2 row_mask:0xf bank_mask:0xf\ns_nop 1\n"
"v_max_u32_dpp v133, v133, v133 row_ror:1 row_mask:0xf bank_mask:0xf\ns_nop 0\nv_readlane_b32 s47, v133, 0\nv_readlane_b32 s48, v133, 16\n"
"s_max_u32 s48, s48, s47\ns_cmp_le_u32 s48, s52\ns_cbranch_scc1 back_h0\ns_bfe_u32 s52, s48, 1179656\n"
"s_add_u32 s52, s52, s30\ns_sub_u32 s45, s52, s44\ns_and_b32 s45, s45, 3\ns_sub_u32 s44, s52, s45\n"
"s_lshl_b32 s45, s45, 2\ns_and_b32 s46, s48, 255\ns_mov_b32 s92, 0\ns_branch back_h0\n"
"end_h0:\ns_branch fk_done\nfk_h1:\ns_mov_b32 s40, 0\n"
"s_mov_b32 s41, 0\ns_mov_b32 s42, 262144\ns_mov_b32 s43, 393216\ns_mov_b32 s44, s30\n"
"s_mov_b32 s45, 0\ns_bcnt1_i32_b64 s46, s[28:29]\ns_mov_b32 s92, 99\ns_mov_b32 s49, 0\n"
"s_mov_b32 s53, 0\ns_mov_b32 s50, s36\ns_mov_b32 s51, s37\nv_mov_b32 v131, v13\n"
"v_mov_b32 v132, v14\nv_mov_b32 v134, 24576\nv_mov_b32 v115, 0\nv_mov_b32 v120, -1\n"
"v_mov_b32 v121, -1\nv_mov_b32 v122, -1\nv_mov_b32 v123, -1\ns_add_u32 s47, s35, 4\n"
"s_and_b32 s52, s47, 1\ns_mul_i32 s52, s52, -14208\ns_mul_i32 s48, s47, 14336\ns_add_u32 s48, s48, s52\n"
"s_lshr_b32 s52, s47, 3\ns_mul_i32 s52, s52, 8192\ns_add_u32 s48, s48, s52\ns_add_u32 s47, s48, 2048\n"
"buffer_load_dwordx4 a[8:11], v15, s[12:15], s47 offen sc1\nbuffer_load_dwordx4 a[12:15], v15, s[12:15], s47 offen offset:1024 sc1\ns_add_u32 s47, s48, 0\nbuffer_load_dwordx4 a[0:3], v15, s[12:15], s47 offen sc1\n"
"buffer_load_dwordx4 a[4:7], v15, s[12:15], s47 offen offset:1024 sc1\ns_add_u32 s47, s48, 6144\nbuffer_load_dwordx4 a[24:27], v15, s[12:15], s47 offen sc1\nbuffer_load_dwordx4 a[28:31], v15, s[12:15], s47 offen offset:1024 sc1\n"
"s_add_u32 s47, s48, 4096\nbuffer_load_dwordx4 a[16:19], v15, s[12:15], s47 offen sc1\nbuffer_load_dwordx4 a[20:23], v15, s[12:15], s47 offen offset:1024 sc1\ns_add_u32 s47, s35, 5\n"
"s_and_b32 s52, s47, 1\ns_mul_i32 s52, s52, -14208\ns_mul_i32 s48, s47, 14336\ns_add_u32 s48, s48, s52\n"
"s_lshr_b32 s52, s47, 3\ns_mul_i32 s52, s52, 8192\ns_add_u32 s48, s48, s52\ns_add_u32 s47, s48, 2048\n"
"buffer_load_dwordx4 a[40:43], v15, s[12:15], s47 offen sc1\nbuffer_load_dwordx4 a[44:47], v15, s[12:15], s47 offen offset:1024 sc1\ns_add_u32 s47, s48, 0\nbuffer_load_dwordx4 a[32:35], v15, s[12:15], s47 offen sc1\n"
"buffer_load_dwordx4 a[36:39], v15, s[12:15], s47 offen offset:1024 sc1\ns_add_u32 s47, s48, 6144\nbuffer_load_dwordx4 a[56:59], v15, s[12:15], s47 offen sc1\nbuffer_load_dwordx4 a[60:63], v15, s[12:15], s47 offen offset:1024 sc1\n"
"s_add_u32 s47, s48, 4096\nbuffer_load_dwordx4 a[48:51], v15, s[12:15], s47 offen sc1\nbuffer_load_dwordx4 a[52:55], v15, s[12:15], s47 offen offset:1024 sc1\ns_add_u32 s47, s35, 6\n"
"s_and_b32 s52, s47, 1\ns_mul_i32 s52, s52, -14208\ns_mul_i32 s48, s47, 14336\ns_add_u32 s48, s48, s52\n"
"s_lshr_b32 s52, s47, 3\ns_mul_i32 s52, s52, 8192\ns_add_u32 s48, s48, s52\ns_add_u32 s47, s48, 2048\n"
"buffer_load_dwordx4 a[72:75], v15, s[12:15], s47 offen sc1\nbuffer_load_dwordx4 a[76:79], v15, s[12:15], s47 offen offset:1024 sc1\ns_add_u32 s47, s48, 0\nbuffer_load_dwordx4 a[64:67], v15, s[12:15], s47 offen sc1\n"
"buffer_load_dwordx4 a[68:71], v15, s[12:15], s47 offen offset:1024 sc1\ns_add_u32 s47, s48, 6144\nbuffer_load_dwordx4 a[88:91], v15, s[12:15], s47 offen sc1\nbuffer_load_dwordx4 a[92:95], v15, s[12:15], s47 offen offset:1024 sc1\n"
"s_add_u32 s47, s48, 4096\nbuffer_load_dwordx4 a[80:83], v15, s[12:15], s47 offen sc1\nbuffer_load_dwordx4 a[84:87], v15, s[12:15], s47 offen offset:1024 sc1\ns_add_u32 s47, s50, 7168\n"
"buffer_load_dwordx4 v[72:75], v11, s[16:19], s50 offen\nbuffer_load_dwordx4 v[76:79], v12, s[16:19], s50 offen\nbuffer_load_dwordx4 v[80:83], v11, s[16:19], s47 offen\nbuffer_load_dwordx4 v[84:87], v12, s[16:19], s47 offen\n"
"s_add_u32 s50, s50, 28672\ns_waitcnt vmcnt(0)\ns_barrier\ns_memrealtime s[54:55]\n"
"s_waitcnt lgkmcnt(0)\ns_add_u32 s44, s54, 16\ns_mov_b32 s45, 8\n.p2align 6\n"
"top_h1:\nv_mov_b32 v56, 0\nv_mov_b32 v57, 0\nv_mov_b32 v58, 0\n"
"v_mov_b32 v59, 0\nv_mov_b32 v60, 0\nv_mov_b32 v61, 0\nv_mov_b32 v62, 0\n"
"v_mov_b32 v63, 0\nv_mov_b32 v64, 0\nv_mov_b32 v65, 0\nv_mov_b32 v66, 0\n"
"v_mov_b32 v67, 0\nv_mov_b32 v128, 0\ns_mul_i32 s48, s45, 6\ns_and_b32 s48, s48, -4\n"
"s_getpc_b64 s[60:61]\nspc_h1:\ns_sub_u32 s48, sgo_h1-spc_h1, s48\ns_add_u32 s60, s60, s48\n"
"s_addc_u32 s61, s61, 0\nsf_h1:\ns_memrealtime s[54:55]\ns_nop 2\n"
"s_memrealtime s[56:57]\ns_nop 2\ns_memrealtime s[58:59]\nsg_h1:\n"
"s_waitcnt lgkmcnt(4)\ns_sub_u32 s47, s54, s44\ns_add_u32 s48, s47, 12\ns_cmp_ge_u32 s48, 12\n"
"s_cbranch_scc1 so_h1\ns_memrealtime s[54:55]\ns_waitcnt lgkmcnt(4)\ns_cmp_ge_u32 s56, s44\n"
"s_cbranch_scc1 sx_h1\ns_memrealtime s[56:57]\ns_waitcnt lgkmcnt(4)\ns_cmp_ge_u32 s58, s44\n"
"s_cbranch_scc1 sx_h1\ns_memrealtime s[58:59]\ns_waitcnt lgkmcnt(4)\ns_cmp_ge_u32 s54, s44\n"
"s_cbranch_scc1 sx_h1\ns_memrealtime s[54:55]\ns_waitcnt lgkmcnt(4)\ns_cmp_ge_u32 s56, s44\n"
"s_cbranch_scc1 sx_h1\ns_memrealtime s[56:57]\ns_waitcnt lgkmcnt(4)\ns_cmp_ge_u32 s58, s44\n"
"s_cbranch_scc1 sx_h1\ns_memrealtime s[58:59]\ns_waitcnt lgkmcnt(4)\ns_cmp_ge_u32 s54, s44\n"
"s_cbranch_scc1 sx_h1\ns_memrealtime s[54:55]\ns_waitcnt lgkmcnt(4)\ns_cmp_ge_u32 s56, s44\n"
"s_cbranch_scc1 sx_h1\ns_memrealtime s[56:57]\ns_waitcnt lgkmcnt(4)\ns_cmp_ge_u32 s58, s44\n"
"s_cbranch_scc1 sx_h1\ns_memrealtime s[58:59]\ns_waitcnt lgkmcnt(4)\ns_cmp_ge_u32 s54, s44\n"
"s_cbranch_scc1 sx_h1\ns_memrealtime s[54:55]\ns_waitcnt lgkmcnt(4)\ns_cmp_ge_u32 s56, s44\n"
"s_cbranch_scc1 sx_h1\ns_memrealtime s[56:57]\ns_waitcnt lgkmcnt(4)\ns_cmp_ge_u32 s58, s44\n"
"s_cbranch_scc1 sx_h1\ns_memrealtime s[58:59]\ns_waitcnt lgkmcnt(4)\ns_cmp_ge_u32 s54, s44\n"
"s_cbranch_scc1 sx_h1\ns_memrealtime s[54:55]\ns_waitcnt lgkmcnt(4)\ns_cmp_ge_u32 s56, s44\n"
"s_cbranch_scc1 sx_h1\ns_memrealtime s[56:57]\ns_waitcnt lgkmcnt(4)\ns_cmp_ge_u32 s58, s44\n"
"s_cbranch_scc1 sx_h1\ns_memrealtime s[58:59]\ns_waitcnt lgkmcnt(4)\ns_cmp_ge_u32 s54, s44\n"
"s_cbranch_scc1 sx_h1\ns_memrealtime s[54:55]\ns_waitcnt lgkmcnt(4)\ns_cmp_ge_u32 s56, s44\n"
"s_cbranch_scc1 sx_h1\ns_memrealtime s[56:57]\ns_branch sx_h1\nso_h1:\n"
"s_waitcnt lgkmcnt(0)\ns_cmp_lt_i32 s47, 0\ns_cbranch_scc1 sf_h1\ns_branch sgo_h1\n"
"sx_h1:\ns_setpc_b64 s[60:61]\ns_nop 0\ns_nop 0\n"
"s_nop 0\ns_nop 0\ns_nop 0\ns_nop 0\n"
"s_nop 0\ns_nop 0\ns_nop 0\ns_nop 0\n"
"s_nop 0\ns_nop 0\ns_nop 0\ns_nop 0\n"
"s_nop 0\ns_nop 0\ns_nop 0\ns_nop 0\n"
"s_nop 0\ns_nop 0\ns_nop 0\ns_nop 0\n"
"s_nop 0\ns_nop 0\nsgo_h1:\ns_nop 13\n"
"buffer_load_dwordx4 v[40:43], v10, s[8:11], s43 offen offset:1024 sc1\ns_nop 5\nbuffer_load_dwordx4 v[44:47], v10, s[8:11], s43 offen offset:0 sc1\ns_nop 9\n"
"buffer_load_dwordx4 v[48:51], v10, s[8:11], s43 offen offset:3072 sc1\ns_nop 9\nbuffer_load_dwordx4 v[52:55], v10, s[8:11], s43 offen offset:2048 sc1\nlanded_h1:\n"
"s_waitcnt vmcnt(3)\nv_smfmac_f32_16x16x64_bf16 v[56:59], v[40:43], a[8:15], v8\nv_max3_u32 v128, v128, v40, v41\nv_smfmac_f32_16x16x64_bf16 v[60:63], v[40:43], a[40:47], v8\n"
"v_max3_u32 v128, v128, v42, v43\nv_smfmac_f32_16x16x64_bf16 v[64:67], v[40:43], a[72:79], v8\ns_waitcnt vmcnt(2)\nv_smfmac_f32_16x16x64_bf16 v[56:59], v[44:47], a[0:7], v8\n"
"v_max3_u32 v128, v128, v44, v45\nv_smfmac_f32_16x16x64_bf16 v[60:63], v[44:47], a[32:39], v8\nv_max3_u32 v128, v128, v46, v47\nv_smfmac_f32_16x16x64_bf16 v[64:67], v[44:47], a[64:71], v8\n"
"s_waitcnt vmcnt(1)\nv_smfmac_f32_16x16x64_bf16 v[56:59], v[48:51], a[24:31], v8\nv_max3_u32 v128, v128, v48, v49\nv_smfmac_f32_16x16x64_bf16 v[60:63], v[48:51], a[56:63], v8\n"
"v_max3_u32 v128, v128, v50, v51\nv_smfmac_f32_16x16x64_bf16 v[64:67], v[48:51], a[88:95], v8\ns_waitcnt vmcnt(0)\nv_smfmac_f32_16x16x64_bf16 v[56:59], v[52:55], a[16:23], v8\n"
"v_max3_u32 v128, v128, v52, v53\nv_smfmac_f32_16x16x64_bf16 v[60:63], v[52:55], a[48:55], v8\nv_max3_u32 v128, v128, v54, v55\nv_smfmac_f32_16x16x64_bf16 v[64:67], v[52:55], a[80:87], v8\n"
"v_add_f32 v129, v56, v57\nv_add_f32 v130, v58, v59\nv_cvt_pk_bf16_f32 v112, v129, v130\nv_add_f32 v129, v60, v61\n"
"v_add_f32 v130, v62, v63\nv_cvt_pk_bf16_f32 v113, v129, v130\nv_cmp_eq_u32 vcc, -1, v128\nv_add_f32 v129, v64, v65\n"
"v_add_f32 v130, v66, v67\nv_cvt_pk_bf16_f32 v114, v129, v130\nds_write_b64 v131, v[112:113] offset:256\nds_write_b64 v131, v[114:115] offset:384\n"
"s_cbranch_vccnz retry_h1\ns_waitcnt lgkmcnt(0)\ns_barrier\ns_nop 9\n"
"ds_read_b64_tr_b16 v[88:89], v132 offset:0\nds_read_b64_tr_b16 v[90:91], v132 offset:32\nds_read_b64_tr_b16 v[92:93], v132 offset:256\nds_read_b64_tr_b16 v[94:95], v132 offset:288\n"
"ds_read_b64_tr_b16 v[96:97], v132 offset:512\nds_read_b64_tr_b16 v[98:99], v132 offset:544\nds_read_b64_tr_b16 v[100:101], v132 offset:768\nds_read_b64_tr_b16 v[102:103], v132 offset:800\n"
"s_waitcnt lgkmcnt(6)\nv_mfma_f32_16x16x32_bf16 v[104:107], v[88:91], v[72:75], 0\nv_mfma_f32_16x16x32_bf16 v[108:111], v[88:91], v[80:83], 0\ns_waitcnt lgkmcnt(4)\n"
"v_mfma_f32_16x16x32_bf16 v[104:107], v[92:95], v[76:79], v[104:107]\nv_mfma_f32_16x16x32_bf16 v[108:111], v[92:95], v[84:87], v[108:111]\ns_waitcnt lgkmcnt(2)\nv_mfma_f32_16x16x32_bf16 v[104:107], v[96:99], v[72:75], v[104:107]\n"
"v_mfma_f32_16x16x32_bf16 v[108:111], v[96:99], v[80:83], v[108:111]\ns_waitcnt lgkmcnt(0)\nv_mfma_f32_16x16x32_bf16 v[104:107], v[100:103], v[76:79], v[104:107]\nv_mfma_f32_16x16x32_bf16 v[108:111], v[100:103], v[84:87], v[108:111]\n"
"s_nop 7\nv_add_f32_dpp v124, v106, v104 row_ror:8 row_mask:0xf bank_mask:0x3\nv_add_f32_dpp v124, v108, v110 row_ror:8 row_mask:0xf bank_mask:0xc\nv_add_f32_dpp v125, v107, v105 row_ror:8 row_mask:0xf bank_mask:0x3\n"
"v_add_f32_dpp v125, v109, v111 row_ror:8 row_mask:0xf bank_mask:0xc\ns_add_u32 s47, s50, 7168\nbuffer_load_dwordx4 v[72:75], v11, s[16:19], s50 offen\nbuffer_load_dwordx4 v[76:79], v12, s[16:19], s50 offen\n"
"buffer_load_dwordx4 v[80:83], v11, s[16:19], s47 offen\nbuffer_load_dwordx4 v[84:87], v12, s[16:19], s47 offen\ns_add_u32 s50, s50, 28672\nbuffer_store_dwordx2 v[124:125], v16, s[20:23], s51 offen sc1\n"
"s_add_u32 s51, s51, 1024\nds_read_b32 v133, v134\nv_xor_b32 v131, 8192, v131\nv_xor_b32 v132, 8192, v132\n"
"s_waitcnt lgkmcnt(0)\nv_readfirstlane_b32 s47, v133\ns_sub_u32 s47, s47, s40\ns_cmp_eq_u32 s47, 1\n"
"s_cbranch_scc1 adopt_h1\nback_h1:\ns_mul_i32 s48, s46, 8\ns_add_u32 s45, s45, s48\n"
"s_add_u32 s45, s45, s32\ns_lshr_b32 s48, s45, 4\ns_lshl_b32 s48, s48, 2\ns_add_u32 s44, s44, s48\n"
"s_and_b32 s45, s45, 15\ns_add_u32 s52, s40, 1\ns_bitcmp1_b64 s[28:29], s52\ns_subb_u32 s46, s46, 0\n"
"s_add_u32 s40, s40, 1\ns_mov_b32 s43, s41\ns_add_u32 s41, s41, 131072\ns_and_b32 s41, s41, 524287\n"
"s_add_u32 s92, s92, 1\ns_xor_b32 s42, s41, 262144\ns_waitcnt lgkmcnt(0)\ns_cmp_lt_u32 s40, 64\n"
"s_cbranch_scc1 top_h1\ns_add_u32 s47, s34, 64\nv_mov_b32 v135, s47\ns_waitcnt vmcnt(0)\n"
"buffer_store_dword v135, v18, s[8:11], 0 offen sc1\ns_branch end_h1\nretry2_h1:\ns_sleep 3\n"
"retry_h1:\ns_add_u32 s53, s53, 1\ns_add_u32 s49, s49, 1\ns_cmp_gt_u32 s49, 65536\n"
"s_cbranch_scc1 end_h1\nbuffer_load_dwordx4 v[40:43], v10, s[8:11], s43 offen offset:1024 sc1\nbuffer_load_dwordx4 v[44:47], v10, s[8:11], s43 offen offset:0 sc1\nbuffer_load_dwordx4 v[48:51], v10, s[8:11], s43 offen offset:3072 sc1\n"
"buffer_load_dwordx4 v[52:55], v10, s[8:11], s43 offen offset:2048 sc1\nv_mov_b32 v128, 0\ns_add_u32 s47, s40, 1\nv_mov_b32 v133, s47\n"
"ds_write_b32 v134, v133\ns_waitcnt vmcnt(0)\nv_max3_u32 v128, v128, v40, v41\nv_max3_u32 v128, v128, v42, v43\n"
"v_max3_u32 v128, v128, v44, v45\nv_max3_u32 v128, v128, v46, v47\nv_max3_u32 v128, v128, v48, v49\nv_max3_u32 v128, v128, v50, v51\n"
"v_max3_u32 v128, v128, v52, v53\nv_max3_u32 v128, v128, v54, v55\nv_cmp_eq_u32 vcc, -1, v128\nv_mov_b32 v56, 0\n"
"v_mov_b32 v57, 0\nv_mov_b32 v58, 0\nv_mov_b32 v59, 0\nv_mov_b32 v60, 0\n"
"v_mov_b32 v61, 0\nv_mov_b32 v62, 0\nv_mov_b32 v63, 0\nv_mov_b32 v64, 0\n"
"v_mov_b32 v65, 0\nv_mov_b32 v66, 0\nv_mov_b32 v67, 0\ns_cbranch_vccnz retry2_h1\n"
"s_branch landed_h1\nadopt_h1:\nbuffer_load_dword v116, v28, s[8:11], 0 offen sc1\ns_lshl_b32 s52, s40, 26\n"
"s_or_b32 s47, s52, 0x3ffffff\ns_waitcnt vmcnt(0)\nv_cmp_ge_u32 vcc, s47, v116\ns_nop 1\n"
"v_cndmask_b32 v133, 0, v116, vcc\ns_nop 1\nv_max_u32_dpp v133, v133, v133 row_ror:8 row_mask:0xf bank_mask:0xf\ns_nop 1\n"
"v_max_u32_dpp v133, v133, v133 row_ror:4 row_mask:0xf bank_mask:0xf\ns_nop 1\nv_max_u32_dpp v133, v133, v133 row_ror:2 row_mask:0xf bank_mask:0xf\ns_nop 1\n"
"v_max_u32_dpp v133, v133, v133 row_ror:1 row_mask:0xf bank_mask:0xf\ns_nop 0\nv_readlane_b32 s47, v133, 0\nv_readlane_b32 s48, v133, 16\n"
"s_max_u32 s48, s48, s47\ns_cmp_le_u32 s48, s52\ns_cbranch_scc1 back_h1\ns_bfe_u32 s52, s48, 1179656\n"
"s_add_u32 s52, s52, s30\ns_sub_u32 s45, s52, s44\ns_and_b32 s45, s45, 3\ns_sub_u32 s44, s52, s45\n"
"s_lshl_b32 s45, s45, 2\ns_and_b32 s46, s48, 255\ns_mov_b32 s92, 0\ns_branch back_h1\n"
"end_h1:\ns_branch fk_done\nfk_h2:\ns_mov_b32 s40, 0\n"
"s_mov_b32 s41, 0\ns_mov_b32 s42, 262144\ns_mov_b32 s43, 393216\ns_mov_b32 s44, s30\n"
"s_mov_b32 s45, 0\ns_bcnt1_i32_b64 s46, s[28:29]\ns_mov_b32 s92, 99\ns_mov_b32 s49, 0\n"
"s_mov_b32 s53, 0\ns_mov_b32 s50, s36\ns_mov_b32 s51, s37\nv_mov_b32 v127, v13\n"
"v_mov_b32 v128, v14\nv_mov_b32 v130, 24576\nv_mov_b32 v111, 0\nv_mov_b32 v116, -1\n"
"v_mov_b32 v117, -1\nv_mov_b32 v118, -1\nv_mov_b32 v119, -1\ns_add_u32 s47, s35, 0\n"
"s_and_b32 s52, s47, 1\ns_mul_i32 s52, s52, -14208\ns_mul_i32 s48, s47, 14336\ns_add_u32 s48, s48, s52\n"
"s_lshr_b32 s52, s47, 3\ns_mul_i32 s52, s52, 8192\ns_add_u32 s48, s48, s52\ns_add_u32 s47, s48, 8192\n"
"buffer_load_dwordx4 a[0:3], v15, s[12:15], s47 offen sc1\nbuffer_load_dwordx4 a[4:7], v15, s[12:15], s47 offen offset:1024 sc1\ns_add_u32 s47, s48, 10240\nbuffer_load_dwordx4 a[8:11], v15, s[12:15], s47 offen sc1\n"
"buffer_load_dwordx4 a[12:15], v15, s[12:15], s47 offen offset:1024 sc1\ns_add_u32 s47, s48, 12288\nbuffer_load_dwordx4 a[16:19], v15, s[12:15], s47 offen sc1\nbuffer_load_dwordx4 a[20:23], v15, s[12:15], s47 offen offset:1024 sc1\n"
"s_add_u32 s47, s35, 1\ns_and_b32 s52, s47, 1\ns_mul_i32 s52, s52, -14208\ns_mul_i32 s48, s47, 14336\n"
"s_add_u32 s48, s48, s52\ns_lshr_b32 s52, s47, 3\ns_mul_i32 s52, s52, 8192\ns_add_u32 s48, s48, s52\n"
"s_add_u32 s47, s48, 8192\nbuffer_load_dwordx4 a[32:35], v15, s[12:15], s47 offen sc1\nbuffer_load_dwordx4 a[36:39], v15, s[12:15], s47 offen offset:1024 sc1\ns_add_u32 s47, s48, 10240\n"
"buffer_load_dwordx4 a[40:43], v15, s[12:15], s47 offen sc1\nbuffer_load_dwordx4 a[44:47], v15, s[12:15], s47 offen offset:1024 sc1\ns_add_u32 s47, s48, 12288\nbuffer_load_dwordx4 a[48:51], v15, s[12:15], s47 offen sc1\n"
"buffer_load_dwordx4 a[52:55], v15, s[12:15], s47 offen offset:1024 sc1\ns_add_u32 s47, s35, 2\ns_and_b32 s52, s47, 1\ns_mul_i32 s52, s52, -14208\n"
"s_mul_i32 s48, s47, 14336\ns_add_u32 s48, s48, s52\ns_lshr_b32 s52, s47, 3\ns_mul_i32 s52, s52, 8192\n"
"s_add_u32 s48, s48, s52\ns_add_u32 s47, s48, 8192\nbuffer_load_dwordx4 a[64:67], v15, s[12:15], s47 offen sc1\nbuffer_load_dwordx4 a[68:71], v15, s[12:15], s47 offen offset:1024 sc1\n"
"s_add_u32 s47, s48, 10240\nbuffer_load_dwordx4 a[72:75], v15, s[12:15], s47 offen sc1\nbuffer_load_dwordx4 a[76:79], v15, s[12:15], s47 offen offset:1024 sc1\ns_add_u32 s47, s48, 12288\n"
"buffer_load_dwordx4 a[80:83], v15, s[12:15], s47 offen sc1\nbuffer_load_dwordx4 a[84:87], v15, s[12:15], s47 offen offset:1024 sc1\ns_add_u32 s47, s35, 3\ns_and_b32 s52, s47, 1\n"
"s_mul_i32 s52, s52, -14208\ns_mul_i32 s48, s47, 14336\ns_add_u32 s48, s48, s52\ns_lshr_b32 s52, s47, 3\n"
"s_mul_i32 s52, s52, 8192\ns_add_u32 s48, s48, s52\ns_add_u32 s47, s48, 8192\nbuffer_load_dwordx4 a[96:99], v15, s[12:15], s47 offen sc1\n"
"buffer_load_dwordx4 a[100:103], v15, s[12:15], s47 offen offset:1024 sc1\ns_add_u32 s47, s48, 10240\nbuffer_load_dwordx4 a[104:107], v15, s[12:15], s47 offen sc1\nbuffer_load_dwordx4 a[108:111], v15, s[12:15], s47 offen offset:1024 sc1\n"
"s_add_u32 s47, s48, 12288\nbuffer_load_dwordx4 a[112:115], v15, s[12:15], s47 offen sc1\nbuffer_load_dwordx4 a[116:119], v15, s[12:15], s47 offen offset:1024 sc1\ns_waitcnt vmcnt(0)\n"
"s_barrier\ns_memrealtime s[54:55]\ns_waitcnt lgkmcnt(0)\ns_add_u32 s44, s54, 16\n"
"s_mov_b32 s45, 8\n.p2align 6\ntop_h2:\nv_mov_b32 v52, 0\n"
"v_mov_b32 v53, 0\nv_mov_b32 v54, 0\nv_mov_b32 v55, 0\nv_mov_b32 v56, 0\n"
"v_mov_b32 v57, 0\nv_mov_b32 v58, 0\nv_mov_b32 v59, 0\nv_mov_b32 v60, 0\n"
"v_mov_b32 v61, 0\nv_mov_b32 v62, 0\nv_mov_b32 v63, 0\nv_mov_b32 v64, 0\n"
"v_mov_b32 v65, 0\nv_mov_b32 v66, 0\nv_mov_b32 v67, 0\nv_mov_b32 v124, 0\n"
"s_mul_i32 s48, s45, 6\ns_and_b32 s48, s48, -4\ns_getpc_b64 s[60:61]\nspc_h2:\n"
"s_sub_u32 s48, sgo_h2-spc_h2, s48\ns_add_u32 s60, s60, s48\ns_addc_u32 s61, s61, 0\nsf_h2:\n"
"s_memrealtime s[54:55]\ns_nop 2\ns_memrealtime s[56:57]\ns_nop 2\n"
"s_memrealtime s[58:59]\nsg_h2:\ns_waitcnt lgkmcnt(4)\ns_sub_u32 s47, s54, s44\n"
"s_add_u32 s48, s47, 12\ns_cmp_ge_u32 s48, 12\ns_cbranch_scc1 so_h2\ns_memrealtime s[54:55]\n"
"s_waitcnt lgkmcnt(4)\ns_cmp_ge_u32 s56, s44\ns_cbranch_scc1 sx_h2\ns_memrealtime s[56:57]\n"
"s_waitcnt lgkmcnt(4)\ns_cmp_ge_u32 s58, s44\ns_cbranch_scc1 sx_h2\ns_memrealtime s[58:59]\n"
"s_waitcnt lgkmcnt(4)\ns_cmp_ge_u32 s54, s44\ns_cbranch_scc1 sx_h2\ns_memrealtime s[54:55]\n"
"s_waitcnt lgkmcnt(4)\ns_cmp_ge_u32 s56, s44\ns_cbranch_scc1 sx_h2\ns_memrealtime s[56:57]\n"
"s_waitcnt lgkmcnt(4)\ns_cmp_ge_u32 s58, s44\ns_cbranch_scc1 sx_h2\ns_memrealtime s[58:59]\n"
"s_waitcnt lgkmcnt(4)\ns_cmp_ge_u32 s54, s44\ns_cbranch_scc1 sx_h2\ns_memrealtime s[54:55]\n"
"s_waitcnt lgkmcnt(4)\ns_cmp_ge_u32 s56, s44\ns_cbranch_scc1 sx_h2\ns_memrealtime s[56:57]\n"
"s_waitcnt lgkmcnt(4)\ns_cmp_ge_u32 s58, s44\ns_cbranch_scc1 sx_h2\ns_memrealtime s[58:59]\n"
"s_waitcnt lgkmcnt(4)\ns_cmp_ge_u32 s54, s44\ns_cbranch_scc1 sx_h2\ns_memrealtime s[54:55]\n"
"s_waitcnt lgkmcnt(4)\ns_cmp_ge_u32 s56, s44\ns_cbranch_scc1 sx_h2\ns_memrealtime s[56:57]\n"
"s_waitcnt lgkmcnt(4)\ns_cmp_ge_u32 s58, s44\ns_cbranch_scc1 sx_h2\ns_memrealtime s[58:59]\n"
"s_waitcnt lgkmcnt(4)\ns_cmp_ge_u32 s54, s44\ns_cbranch_scc1 sx_h2\ns_memrealtime s[54:55]\n"
"s_waitcnt lgkmcnt(4)\ns_cmp_ge_u32 s56, s44\ns_cbranch_scc1 sx_h2\ns_memrealtime s[56:57]\n"
"s_waitcnt lgkmcnt(4)\ns_cmp_ge_u32 s58, s44\ns_cbranch_scc1 sx_h2\ns_memrealtime s[58:59]\n"
"s_waitcnt lgkmcnt(4)\ns_cmp_ge_u32 s54, s44\ns_cbranch_scc1 sx_h2\ns_memrealtime s[54:55]\n"
"s_waitcnt lgkmcnt(4)\ns_cmp_ge_u32 s56, s44\ns_cbranch_scc1 sx_h2\ns_memrealtime s[56:57]\n"
"s_branch sx_h2\nso_h2:\ns_waitcnt lgkmcnt(0)\ns_cmp_lt_i32 s47, 0\n"
"s_cbranch_scc1 sf_h2\ns_branch sgo_h2\nsx_h2:\ns_setpc_b64 s[60:61]\n"
"s_nop 0\ns_nop 0\ns_nop 0\ns_nop 0\n"
"s_nop 0\ns_nop 0\ns_nop 0\ns_nop 0\n"
"s_nop 0\ns_nop 0\ns_nop 0\ns_nop 0\n"
"s_nop 0\ns_nop 0\ns_nop 0\ns_nop 0\n"
"s_nop 0\ns_nop 0\ns_nop 0\ns_nop 0\n"
"s_nop 0\ns_nop 0\ns_nop 0\ns_nop 0\n"
"sgo_h2:\ns_nop 9\nbuffer_load_dwordx4 v[40:43], v10, s[8:11], s43 offen offset:0 sc1\ns_nop 13\n"
"buffer_load_dwordx4 v[44:47], v10, s[8:11], s43 offen offset:1024 sc1\ns_nop 9\nbuffer_load_dwordx4 v[48:51], v10, s[8:11], s43 offen offset:2048 sc1\nlanded_h2:\n"
"s_waitcnt vmcnt(2)\nv_smfmac_f32_16x16x64_bf16 v[52:55], v[40:43], a[0:7], v8\nv_max3_u32 v124, v124, v40, v41\nv_smfmac_f32_16x16x64_bf16 v[56:59], v[40:43], a[32:39], v8\n"
"v_max3_u32 v124, v124, v42, v43\nv_smfmac_f32_16x16x64_bf16 v[60:63], v[40:43], a[64:71], v8\nv_smfmac_f32_16x16x64_bf16 v[64:67], v[40:43], a[96:103], v8\ns_waitcnt vmcnt(1)\n"
"v_smfmac_f32_16x16x64_bf16 v[52:55], v[44:47], a[8:15], v8\nv_max3_u32 v124, v124, v44, v45\nv_smfmac_f32_16x16x64_bf16 v[56:59], v[44:47], a[40:47], v8\nv_max3_u32 v124, v124, v46, v47\n"
"v_smfmac_f32_16x16x64_bf16 v[60:63], v[44:47], a[72:79], v8\nv_smfmac_f32_16x16x64_bf16 v[64:67], v[44:47], a[104:111], v8\ns_waitcnt vmcnt(0)\nv_smfmac_f32_16x16x64_bf16 v[52:55], v[48:51], a[16:23], v8\n"
"v_max3_u32 v124, v124, v48, v49\nv_smfmac_f32_16x16x64_bf16 v[56:59], v[48:51], a[48:55], v8\nv_max3_u32 v124, v124, v50, v51\nv_smfmac_f32_16x16x64_bf16 v[60:63], v[48:51], a[80:87], v8\n"
"v_cmp_eq_u32 vcc, -1, v124\nv_smfmac_f32_16x16x64_bf16 v[64:67], v[48:51], a[112:119], v8\nv_add_f32 v125, v52, v53\nv_add_f32 v126, v54, v55\n"
"v_cvt_pk_bf16_f32 v108, v125, v126\nv_add_f32 v125, v56, v57\nv_add_f32 v126, v58, v59\nv_cvt_pk_bf16_f32 v109, v125, v126\n"
"v_add_f32 v125, v60, v61\nv_add_f32 v126, v62, v63\nv_cvt_pk_bf16_f32 v110, v125, v126\nv_add_f32 v125, v64, v65\n"
"v_add_f32 v126, v66, v67\nv_cvt_pk_bf16_f32 v111, v125, v126\nds_write_b64 v127, v[108:109] offset:0\nds_write_b64 v127, v[110:111] offset:128\n"
"s_cbranch_vccnz retry_h2\ns_waitcnt lgkmcnt(0)\ns_barrier\nv_mov_b32 v125, 0\n"
"s_nop 1\nv_mov_b32 v125, 0\ns_nop 1\nv_mov_b32 v125, 0\n"
"s_nop 1\nv_mov_b32 v125, 0\ns_nop 1\nv_mov_b32 v125, 0\n"
"s_nop 1\nv_mov_b32 v125, 0\ns_nop 1\nv_mov_b32 v125, 0\n"
"s_nop 1\nv_mov_b32 v125, 0\ns_nop 1\nv_mov_b32 v125, 0\n"
"s_nop 1\nv_mov_b32 v125, 0\ns_nop 1\nv_mov_b32 v125, 0\n"
"s_nop 1\nv_mov_b32 v125, 0\ns_nop 1\nv_mov_b32 v125, 0\n"
"s_nop 1\nv_mov_b32 v125, 0\ns_nop 1\nv_mov_b32 v125, 0\n"
"s_nop 1\nv_mov_b32 v125, 0\ns_nop 1\nv_mov_b32 v125, 0\n"
"s_nop 1\nv_mov_b32 v125, 0\ns_nop 1\nv_mov_b32 v125, 0\n"
"s_nop 1\nv_mov_b32 v125, 0\ns_nop 1\nv_mov_b32 v125, 0\n"
"s_nop 1\nv_mov_b32 v125, 0\nbuffer_store_dwordx4 v[116:119], v17, s[8:11], s42 offen sc1\nds_read_b32 v129, v130\n"
"v_xor_b32 v127, 8192, v127\nv_xor_b32 v128, 8192, v128\ns_waitcnt lgkmcnt(0)\nv_readfirstlane_b32 s47, v129\n"
"s_sub_u32 s47, s47, s40\ns_cmp_eq_u32 s47, 1\ns_cbranch_scc1 adopt_h2\nback_h2:\n"
"s_mul_i32 s48, s46, 8\ns_add_u32 s45, s45, s48\ns_add_u32 s45, s45, s32\ns_lshr_b32 s48, s45, 4\n"
"s_lshl_b32 s48, s48, 2\ns_add_u32 s44, s44, s48\ns_and_b32 s45, s45, 15\ns_add_u32 s52, s40, 1\n"
"s_bitcmp1_b64 s[28:29], s52\ns_subb_u32 s46, s46, 0\ns_add_u32 s40, s40, 1\ns_mov_b32 s43, s41\n"
"s_add_u32 s41, s41, 131072\ns_and_b32 s41, s41, 524287\ns_add_u32 s92, s92, 1\ns_xor_b32 s42, s41, 262144\n"
"s_waitcnt lgkmcnt(0)\ns_cmp_lt_u32 s40, 64\ns_cbranch_scc1 top_h2\ns_branch end_h2\n"
"retry2_h2:\ns_sleep 3\nretry_h2:\ns_add_u32 s53, s53, 1\n"
"s_add_u32 s49, s49, 1\ns_cmp_gt_u32 s49, 65536\ns_cbranch_scc1 end_h2\nbuffer_load_dwordx4 v[40:43], v10, s[8:11], s43 offen offset:0 sc1\n"
"buffer_load_dwordx4 v[44:47], v10, s[8:11], s43 offen offset:1024 sc1\nbuffer_load_dwordx4 v[48:51], v10, s[8:11], s43 offen offset:2048 sc1\nv_mov_b32 v124, 0\ns_add_u32 s47, s40, 1\n"
"v_mov_b32 v129, s47\nds_write_b32 v130, v129\ns_waitcnt vmcnt(0)\nv_max3_u32 v124, v124, v40, v41\n"
"v_max3_u32 v124, v124, v42, v43\nv_max3_u32 v124, v124, v44, v45\nv_max3_u32 v124, v124, v46, v47\nv_max3_u32 v124, v124, v48, v49\n"
"v_max3_u32 v124, v124, v50, v51\nv_cmp_eq_u32 vcc, -1, v124\nv_mov_b32 v52, 0\nv_mov_b32 v53, 0\n"
"v_mov_b32 v54, 0\nv_mov_b32 v55, 0\nv_mov_b32 v56, 0\nv_mov_b32 v57, 0\n"
"v_mov_b32 v58, 0\nv_mov_b32 v59, 0\nv_mov_b32 v60, 0\nv_mov_b32 v61, 0\n"
"v_mov_b32 v62, 0\nv_mov_b32 v63, 0\nv_mov_b32 v64, 0\nv_mov_b32 v65, 0\n"
"v_mov_b32 v66, 0\nv_mov_b32 v67, 0\ns_cbranch_vccnz retry2_h2\ns_branch landed_h2\n"
"adopt_h2:\nbuffer_load_dword v112, v28, s[8:11], 0 offen sc1\ns_lshl_b32 s52, s40, 26\ns_or_b32 s47, s52, 0x3ffffff\n"
"s_waitcnt vmcnt(0)\nv_cmp_ge_u32 vcc, s47, v112\ns_nop 1\nv_cndmask_b32 v129, 0, v112, vcc\n"
"s_nop 1\nv_max_u32_dpp v129, v129, v129 row_ror:8 row_mask:0xf bank_mask:0xf\ns_nop 1\nv_max_u32_dpp v129, v129, v129 row_ror:4 row_mask:0xf bank_mask:0xf\n"
"s_nop 1\nv_max_u32_dpp v129, v129, v129 row_ror:2 row_mask:0xf bank_mask:0xf\ns_nop 1\nv_max_u32_dpp v129, v129, v129 row_ror:1 row_mask:0xf bank_mask:0xf\n"
"s_nop 0\nv_readlane_b32 s47, v129, 0\nv_readlane_b32 s48, v129, 16\ns_max_u32 s48, s48, s47\n"
"s_cmp_le_u32 s48, s52\ns_cbranch_scc1 back_h2\ns_bfe_u32 s52, s48, 1179656\ns_add_u32 s52, s52, s30\n"
"s_sub_u32 s45, s52, s44\ns_and_b32 s45, s45, 3\ns_sub_u32 s44, s52, s45\ns_lshl_b32 s45, s45, 2\n"
"s_and_b32 s46, s48, 255\ns_mov_b32 s92, 0\ns_branch back_h2\nend_h2:\n"
"s_branch fk_done\nfk_h3:\ns_mov_b32 s40, 0\ns_mov_b32 s41, 0\n"
"s_mov_b32 s42, 262144\ns_mov_b32 s43, 393216\ns_mov_b32 s44, s30\ns_mov_b32 s45, 0\n"
"s_bcnt1_i32_b64 s46, s[28:29]\ns_mov_b32 s92, 99\ns_mov_b32 s49, 0\ns_mov_b32 s53, 0\n"
"s_mov_b32 s50, s36\ns_mov_b32 s51, s37\nv_mov_b32 v127, v13\nv_mov_b32 v128, v14\n"
"v_mov_b32 v130, 24576\nv_mov_b32 v111, 0\nv_mov_b32 v116, -1\nv_mov_b32 v117, -1\n"
"v_mov_b32 v118, -1\nv_mov_b32 v119, -1\ns_add_u32 s47, s35, 4\ns_and_b32 s52, s47, 1\n"
"s_mul_i32 s52, s52, -14208\ns_mul_i32 s48, s47, 14336\ns_add_u32 s48, s48, s52\ns_lshr_b32 s52, s47, 3\n"
"s_mul_i32 s52, s52, 8192\ns_add_u32 s48, s48, s52\ns_add_u32 s47, s48, 10240\nbuffer_load_dwordx4 a[8:11], v15, s[12:15], s47 offen sc1\n"
"buffer_load_dwordx4 a[12:15], v15, s[12:15], s47 offen offset:1024 sc1\ns_add_u32 s47, s48, 8192\nbuffer_load_dwordx4 a[0:3], v15, s[12:15], s47 offen sc1\nbuffer_load_dwordx4 a[4:7], v15, s[12:15], s47 offen offset:1024 sc1\n"
"s_add_u32 s47, s48, 12288\nbuffer_load_dwordx4 a[16:19], v15, s[12:15], s47 offen sc1\nbuffer_load_dwordx4 a[20:23], v15, s[12:15], s47 offen offset:1024 sc1\ns_add_u32 s47, s35, 5\n"
"s_and_b32 s52, s47, 1\ns_mul_i32 s52, s52, -14208\ns_mul_i32 s48, s47, 14336\ns_add_u32 s48, s48, s52\n"
"s_lshr_b32 s52, s47, 3\ns_mul_i32 s52, s52, 8192\ns_add_u32 s48, s48, s52\ns_add_u32 s47, s48, 10240\n"
"buffer_load_dwordx4 a[40:43], v15, s[12:15], s47 offen sc1\nbuffer_load_dwordx4 a[44:47], v15, s[12:15], s47 offen offset:1024 sc1\ns_add_u32 s47, s48, 8192\nbuffer_load_dwordx4 a[32:35], v15, s[12:15], s47 offen sc1\n"
"buffer_load_dwordx4 a[36:39], v15, s[12:15], s47 offen offset:1024 sc1\ns_add_u32 s47, s48, 12288\nbuffer_load_dwordx4 a[48:51], v15, s[12:15], s47 offen sc1\nbuffer_load_dwordx4 a[52:55], v15, s[12:15], s47 offen offset:1024 sc1\n"
"s_add_u32 s47, s35, 6\ns_and_b32 s52, s47, 1\ns_mul_i32 s52, s52, -14208\ns_mul_i32 s48, s47, 14336\n"
"s_add_u32 s48, s48, s52\ns_lshr_b32 s52, s47, 3\ns_mul_i32 s52, s52, 8192\ns_add_u32 s48, s48, s52\n"
"s_add_u32 s47, s48, 10240\nbuffer_load_dwordx4 a[72:75], v15, s[12:15], s47 offen sc1\nbuffer_load_dwordx4 a[76:79], v15, s[12:15], s47 offen offset:1024 sc1\ns_add_u32 s47, s48, 8192\n"
"buffer_load_dwordx4 a[64:67], v15, s[12:15], s47 offen sc1\nbuffer_load_dwordx4 a[68:71], v15, s[12:15], s47 offen offset:1024 sc1\ns_add_u32 s47, s48, 12288\nbuffer_load_dwordx4 a[80:83], v15, s[12:15], s47 offen sc1\n"
"buffer_load_dwordx4 a[84:87], v15, s[12:15], s47 offen offset:1024 sc1\nv_lshlrev_b32 v114, 2, v9\ns_mov_b32 s52, 1064960\nbuffer_load_dword v112, v114, s[8:11], s52 offen sc1\n"
"s_add_u32 s52, s52, 4096\nbuffer_load_dword v113, v114, s[8:11], s52 offen sc1\ns_waitcnt vmcnt(0)\nar_h3:\n"
"v_xor_b32 v112, s34, v112\nv_xor_b32 v113, s34, v113\nv_or_b32 v112, v112, v113\nv_cmp_ne_u32 vcc, 0, v112\n"
"s_cbranch_vccz arok_h3\ns_add_u32 s53, s53, 0\ns_add_u32 s49, s49, 1\ns_cmp_gt_u32 s49, 65536\n"
"s_cbranch_scc1 end_h3\ns_sleep 2\nv_lshlrev_b32 v114, 2, v9\ns_mov_b32 s52, 1064960\n"
"buffer_load_dword v112, v114, s[8:11], s52 offen sc1\ns_add_u32 s52, s52, 4096\nbuffer_load_dword v113, v114, s[8:11], s52 offen sc1\ns_waitcnt vmcnt(0)\n"
"s_branch ar_h3\narok_h3:\ns_barrier\ns_memrealtime s[54:55]\n"
"s_waitcnt lgkmcnt(0)\ns_add_u32 s44, s54, 16\ns_mov_b32 s45, 8\n.p2align 6\n"
"top_h3:\nv_mov_b32 v52, 0\nv_mov_b32 v53, 0\nv_mov_b32 v54, 0\n"
"v_mov_b32 v55, 0\nv_mov_b32 v56, 0\nv_mov_b32 v57, 0\nv_mov_b32 v58, 0\n"
"v_mov_b32 v59, 0\nv_mov_b32 v60, 0\nv_mov_b32 v61, 0\nv_mov_b32 v62, 0\n"
"v_mov_b32 v63, 0\nv_mov_b32 v124, 0\ns_mul_i32 s48, s45, 6\ns_and_b32 s48, s48, -4\n"
"s_getpc_b64 s[60:61]\nspc_h3:\ns_sub_u32 s48, sgo_h3-spc_h3, s48\ns_add_u32 s60, s60, s48\n"
"s_addc_u32 s61, s61, 0\nsf_h3:\ns_memrealtime s[54:55]\ns_nop 2\n"
"s_memrealtime s[56:57]\ns_nop 2\ns_memrealtime s[58:59]\nsg_h3:\n"
"s_waitcnt lgkmcnt(4)\ns_sub_u32 s47, s54, s44\ns_add_u32 s48, s47, 12\ns_cmp_ge_u32 s48, 12\n"
"s_cbranch_scc1 so_h3\ns_memrealtime s[54:55]\ns_waitcnt lgkmcnt(4)\ns_cmp_ge_u32 s56, s44\n"
"s_cbranch_scc1 sx_h3\ns_memrealtime s[56:57]\ns_waitcnt lgkmcnt(4)\ns_cmp_ge_u32 s58, s44\n"
"s_cbranch_scc1 sx_h3\ns_memrealtime s[58:59]\ns_waitcnt lgkmcnt(4)\ns_cmp_ge_u32 s54, s44\n"
"s_cbranch_scc1 sx_h3\ns_memrealtime s[54:55]\ns_waitcnt lgkmcnt(4)\ns_cmp_ge_u32 s56, s44\n"
"s_cbranch_scc1 sx_h3\ns_memrealtime s[56:57]\ns_waitcnt lgkmcnt(4)\ns_cmp_ge_u32 s58, s44\n"
"s_cbranch_scc1 sx_h3\ns_memrealtime s[58:59]\ns_waitcnt lgkmcnt(4)\ns_cmp_ge_u32 s54, s44\n"
"s_cbranch_scc1 sx_h3\ns_memrealtime s[54:55]\ns_waitcnt lgkmcnt(4)\ns_cmp_ge_u32 s56, s44\n"
"s_cbranch_scc1 sx_h3\ns_memrealtime s[56:57]\ns_waitcnt lgkmcnt(4)\ns_cmp_ge_u32 s58, s44\n"
"s_cbranch_scc1 sx_h3\ns_memrealtime s[58:59]\ns_waitcnt lgkmcnt(4)\ns_cmp_ge_u32 s54, s44\n"
"s_cbranch_scc1 sx_h3\ns_memrealtime s[54:55]\ns_waitcnt lgkmcnt(4)\ns_cmp_ge_u32 s56, s44\n"
"s_cbranch_scc1 sx_h3\ns_memrealtime s[56:57]\ns_waitcnt lgkmcnt(4)\ns_cmp_ge_u32 s58, s44\n"
"s_cbranch_scc1 sx_h3\ns_memrealtime s[58:59]\ns_waitcnt lgkmcnt(4)\ns_cmp_ge_u32 s54, s44\n"
"s_cbranch_scc1 sx_h3\ns_memrealtime s[54:55]\ns_waitcnt lgkmcnt(4)\ns_cmp_ge_u32 s56, s44\n"
"s_cbranch_scc1 sx_h3\ns_memrealtime s[56:57]\ns_waitcnt lgkmcnt(4)\ns_cmp_ge_u32 s58, s44\n"
"s_cbranch_scc1 sx_h3\ns_memrealtime s[58:59]\ns_waitcnt lgkmcnt(4)\ns_cmp_ge_u32 s54, s44\n"
"s_cbranch_scc1 sx_h3\ns_memrealtime s[54:55]\ns_waitcnt lgkmcnt(4)\ns_cmp_ge_u32 s56, s44\n"
"s_cbranch_scc1 sx_h3\ns_memrealtime s[56:57]\ns_branch sx_h3\nso_h3:\n"
"s_waitcnt lgkmcnt(0)\ns_cmp_lt_i32 s47, 0\ns_cbranch_scc1 sf_h3\ns_branch sgo_h3\n"
"sx_h3:\ns_setpc_b64 s[60:61]\ns_nop 0\ns_nop 0\n"
"s_nop 0\ns_nop 0\ns_nop 0\ns_nop 0\n"
"s_nop 0\ns_nop 0\ns_nop 0\ns_nop 0\n"
"s_nop 0\ns_nop 0\ns_nop 0\ns_nop 0\n"
"s_nop 0\ns_nop 0\ns_nop 0\ns_nop 0\n"
"s_nop 0\ns_nop 0\ns_nop 0\ns_nop 0\n"
"s_nop 0\ns_nop 0\nsgo_h3:\ns_nop 15\n"
"s_nop 7\nbuffer_load_dwordx4 v[40:43], v10, s[8:11], s43 offen offset:1024 sc1\ns_nop 9\nbuffer_load_dwordx4 v[44:47], v10, s[8:11], s43 offen offset:0 sc1\n"
"s_nop 9\nbuffer_load_dwordx4 v[48:51], v10, s[8:11], s43 offen offset:2048 sc1\ns_nop 15\ns_nop 7\n"
"buffer_load_dwordx4 v[68:71], v11, s[8:11], s43 offen sc1\nbuffer_load_dwordx4 v[72:75], v12, s[8:11], s43 offen sc1\nv_mov_b32 v125, 0\ns_nop 1\n"
"v_mov_b32 v125, 0\ns_nop 1\nv_mov_b32 v125, 0\ns_nop 1\n"
"v_mov_b32 v125, 0\ns_nop 1\nv_mov_b32 v125, 0\ns_nop 1\n"
"v_mov_b32 v125, 0\ns_nop 1\nv_mov_b32 v125, 0\ns_nop 1\n"
"v_mov_b32 v125, 0\ns_nop 1\nv_mov_b32 v125, 0\nlanded_h3:\n"
"s_waitcnt vmcnt(4)\nv_smfmac_f32_16x16x64_bf16 v[52:55], v[40:43], a[8:15], v8\nv_max3_u32 v124, v124, v40, v41\nv_smfmac_f32_16x16x64_bf16 v[56:59], v[40:43], a[40:47], v8\n"
"v_max3_u32 v124, v124, v42, v43\nv_smfmac_f32_16x16x64_bf16 v[60:63], v[40:43], a[72:79], v8\ns_waitcnt vmcnt(3)\nv_smfmac_f32_16x16x64_bf16 v[52:55], v[44:47], a[0:7], v8\n"
"v_max3_u32 v124, v124, v44, v45\nv_smfmac_f32_16x16x64_bf16 v[56:59], v[44:47], a[32:39], v8\nv_max3_u32 v124, v124, v46, v47\nv_smfmac_f32_16x16x64_bf16 v[60:63], v[44:47], a[64:71], v8\n"
"s_waitcnt vmcnt(2)\nv_smfmac_f32_16x16x64_bf16 v[52:55], v[48:51], a[16:23], v8\nv_max3_u32 v124, v124, v48, v49\nv_smfmac_f32_16x16x64_bf16 v[56:59], v[48:51], a[48:55], v8\n"
"v_max3_u32 v124, v124, v50, v51\nv_smfmac_f32_16x16x64_bf16 v[60:63], v[48:51], a[80:87], v8\nv_add_f32 v125, v52, v53\nv_add_f32 v126, v54, v55\n"
"v_cvt_pk_bf16_f32 v108, v125, v126\nv_add_f32 v125, v56, v57\nv_add_f32 v126, v58, v59\nv_cvt_pk_bf16_f32 v109, v125, v126\n"
"v_cmp_eq_u32 vcc, -1, v124\nv_add_f32 v125, v60, v61\nv_add_f32 v126, v62, v63\nv_cvt_pk_bf16_f32 v110, v125, v126\n"
"ds_write_b64 v127, v[108:109] offset:256\nds_write_b64 v127, v[110:111] offset:384\ns_cbranch_vccnz retry_h3\ns_waitcnt lgkmcnt(0)\n"
"s_barrier\nds_read_b64_tr_b16 v[84:85], v128 offset:0\nds_read_b64_tr_b16 v[86:87], v128 offset:64\nds_read_b64_tr_b16 v[88:89], v128 offset:256\n"
"ds_read_b64_tr_b16 v[90:91], v128 offset:320\nds_read_b64_tr_b16 v[92:93], v128 offset:512\nds_read_b64_tr_b16 v[94:95], v128 offset:576\nds_read_b64_tr_b16 v[96:97], v128 offset:768\n"
"ds_read_b64_tr_b16 v[98:99], v128 offset:832\nmb_h3:\ns_waitcnt vmcnt(0)\nv_max3_u32 v124, v124, v68, v69\n"
"v_max3_u32 v124, v124, v70, v71\nv_max3_u32 v124, v124, v72, v73\nv_max3_u32 v124, v124, v74, v75\nv_cmp_eq_u32 vcc, -1, v124\n"
"s_waitcnt lgkmcnt(6)\nv_mfma_f32_16x16x32_bf16 v[100:103], v[84:87], v[68:71], 0\ns_waitcnt lgkmcnt(4)\nv_mfma_f32_16x16x32_bf16 v[100:103], v[88:91], v[72:75], v[100:103]\n"
"s_waitcnt lgkmcnt(2)\nv_mfma_f32_16x16x32_bf16 v[100:103], v[92:95], v[68:71], v[100:103]\ns_waitcnt lgkmcnt(0)\nv_mfma_f32_16x16x32_bf16 v[100:103], v[96:99], v[72:75], v[100:103]\n"
"s_cbranch_vccnz mbr_h3\ns_nop 6\nv_add_f32_dpp v120, v102, v100 row_ror:8 row_mask:0xf bank_mask:0x3\nv_add_f32_dpp v120, v100, v102 row_ror:8 row_mask:0xf bank_mask:0xc\n"
"v_add_f32_dpp v121, v103, v101 row_ror:8 row_mask:0xf bank_mask:0x3\nv_add_f32_dpp v121, v101, v103 row_ror:8 row_mask:0xf bank_mask:0xc\nv_cvt_pk_f16_f32 v125, v120, v121\nbuffer_store_dword v125, v16, s[8:11], s41 offen sc1\n"
"s_nop 15\ns_nop 15\ns_nop 15\nbuffer_store_dwordx2 v[120:121], v17, s[8:11], s41 offen sc1\n"
"ds_read_b32 v129, v130\nv_xor_b32 v127, 8192, v127\nv_xor_b32 v128, 8192, v128\ns_waitcnt lgkmcnt(0)\n"
"v_readfirstlane_b32 s47, v129\ns_sub_u32 s47, s47, s40\ns_cmp_eq_u32 s47, 1\ns_cbranch_scc1 adopt_h3\n"
"back_h3:\ns_mul_i32 s48, s46, 8\ns_add_u32 s45, s45, s48\ns_add_u32 s45, s45, s32\n"
"s_lshr_b32 s48, s45, 4\ns_lshl_b32 s48, s48, 2\ns_add_u32 s44, s44, s48\ns_and_b32 s45, s45, 15\n"
"s_add_u32 s52, s40, 1\ns_bitcmp1_b64 s[28:29], s52\ns_subb_u32 s46, s46, 0\ns_add_u32 s40, s40, 1\n"
"s_mov_b32 s43, s41\ns_add_u32 s41, s41, 131072\ns_and_b32 s41, s41, 524287\ns_add_u32 s92, s92, 1\n"
"s_xor_b32 s42, s41, 262144\ns_waitcnt lgkmcnt(0)\ns_cmp_lt_u32 s40, 64\ns_cbranch_scc1 top_h3\n"
"s_branch end_h3\nretry2_h3:\ns_sleep 3\nretry_h3:\n"
"s_add_u32 s53, s53, 1\ns_add_u32 s49, s49, 1\ns_cmp_gt_u32 s49, 65536\ns_cbranch_scc1 end_h3\n"
"buffer_load_dwordx4 v[40:43], v10, s[8:11], s43 offen offset:1024 sc1\nbuffer_load_dwordx4 v[44:47], v10, s[8:11], s43 offen offset:0 sc1\nbuffer_load_dwordx4 v[48:51], v10, s[8:11], s43 offen offset:2048 sc1\nbuffer_load_dwordx4 v[68:71], v11, s[8:11], s43 offen sc1\n"
"buffer_load_dwordx4 v[72:75], v12, s[8:11], s43 offen sc1\nv_mov_b32 v124, 0\ns_add_u32 s47, s40, 1\nv_mov_b32 v129, s47\n"
"ds_write_b32 v130, v129\ns_waitcnt vmcnt(0)\nv_max3_u32 v124, v124, v40, v41\nv_max3_u32 v124, v124, v42, v43\n"
"v_max3_u32 v124, v124, v44, v45\nv_max3_u32 v124, v124, v46, v47\nv_max3_u32 v124, v124, v48, v49\nv_max3_u32 v124, v124, v50, v51\n"
"v_cmp_eq_u32 vcc, -1, v124\nv_mov_b32 v52, 0\nv_mov_b32 v53, 0\nv_mov_b32 v54, 0\n"
"v_mov_b32 v55, 0\nv_mov_b32 v56, 0\nv_mov_b32 v57, 0\nv_mov_b32 v58, 0\n"
"v_mov_b32 v59, 0\nv_mov_b32 v60, 0\nv_mov_b32 v61, 0\nv_mov_b32 v62, 0\n"
"v_mov_b32 v63, 0\ns_cbranch_vccnz retry2_h3\ns_branch landed_h3\nadopt_h3:\n"
"buffer_load_dword v112, v28, s[8:11], 0 offen sc1\ns_lshl_b32 s52, s40, 26\ns_or_b32 s47, s52, 0x3ffffff\ns_waitcnt vmcnt(0)\n"
"v_cmp_ge_u32 vcc, s47, v112\ns_nop 1\nv_cndmask_b32 v129, 0, v112, vcc\ns_nop 1\n"
"v_max_u32_dpp v129, v129, v129 row_ror:8 row_mask:0xf bank_mask:0xf\ns_nop 1\nv_max_u32_dpp v129, v129, v129 row_ror:4 row_mask:0xf bank_mask:0xf\ns_nop 1\n"
"v_max_u32_dpp v129, v129, v129 row_ror:2 row_mask:0xf bank_mask:0xf\ns_nop 1\nv_max_u32_dpp v129, v129, v129 row_ror:1 row_mask:0xf bank_mask:0xf\ns_nop 0\n"
"v_readlane_b32 s47, v129, 0\nv_readlane_b32 s48, v129, 16\ns_max_u32 s48, s48, s47\ns_cmp_le_u32 s48, s52\n"
"s_cbranch_scc1 back_h3\ns_bfe_u32 s52, s48, 1179656\ns_add_u32 s52, s52, s30\ns_sub_u32 s45, s52, s44\n"
"s_and_b32 s45, s45, 3\ns_sub_u32 s44, s52, s45\ns_lshl_b32 s45, s45, 2\ns_and_b32 s46, s48, 255\n"
"s_mov_b32 s92, 0\ns_branch back_h3\nmbr_h3:\ns_add_u32 s53, s53, 1\n"
"s_add_u32 s49, s49, 1\ns_cmp_gt_u32 s49, 65536\ns_cbranch_scc1 end_h3\nbuffer_load_dwordx4 v[68:71], v11, s[8:11], s43 offen sc1\n"
"buffer_load_dwordx4 v[72:75], v12, s[8:11], s43 offen sc1\nv_mov_b32 v124, 0\ns_branch mb_h3\nend_h3:\n"
"s_branch fk_done\nfk_x0:\ns_mov_b32 s40, 0\ns_mov_b32 s41, 0\n"
"s_mov_b32 s42, 262144\ns_mov_b32 s43, 393216\ns_mov_b32 s44, s30\ns_mov_b32 s45, 0\n"
"s_bcnt1_i32_b64 s46, s[28:29]\ns_mov_b32 s92, 99\ns_mov_b32 s49, 0\ns_mov_b32 s53, 0\n"
"s_mov_b32 s50, s36\ns_mov_b32 s51, s37\nv_mov_b32 v81, v13\nv_mov_b32 v82, v14\n"
"v_mov_b32 v84, 24576\ns_add_u32 s51, s50, 14336\nv_mov_b32 v64, -1\nv_mov_b32 v65, -1\n"
"v_mov_b32 v66, -1\nv_mov_b32 v67, -1\nbuffer_load_dwordx4 a[0:3], v9, s[16:19], s50 offen offset:0 sc1\nbuffer_load_dwordx4 a[4:7], v9, s[16:19], s50 offen offset:1024 sc1\n"
"buffer_load_dwordx4 a[8:11], v9, s[16:19], s50 offen offset:2048 sc1\nbuffer_load_dwordx4 a[12:15], v9, s[16:19], s50 offen offset:3072 sc1\nbuffer_load_dwordx4 a[16:19], v9, s[16:19], s51 offen offset:0 sc1\nbuffer_load_dwordx4 a[20:23], v9, s[16:19], s51 offen offset:1024 sc1\n"
"buffer_load_dwordx4 a[24:27], v9, s[16:19], s51 offen offset:2048 sc1\nbuffer_load_dwordx4 a[28:31], v9, s[16:19], s51 offen offset:3072 sc1\ns_add_u32 s50, s50, 28672\ns_add_u32 s51, s51, 28672\n"
"buffer_load_dword v85, v18, s[8:11], 0 offen sc1\ns_waitcnt vmcnt(0)\nf0_x0:\ns_add_u32 s47, s40, 2\n"
"s_min_u32 s47, s47, 64\ns_add_u32 s47, s47, s34\nv_subrev_u32 v85, s47, v85\nv_cmp_lt_u32 vcc, 64, v85\n"
"s_cbranch_vccz f0ok_x0\ns_add_u32 s53, s53, 0\ns_add_u32 s49, s49, 1\ns_cmp_gt_u32 s49, 65536\n"
"s_cbranch_scc1 end_x0\ns_sleep 2\nbuffer_load_dword v85, v18, s[8:11], 0 offen sc1\ns_waitcnt vmcnt(0)\n"
"s_branch f0_x0\nf0ok_x0:\ns_barrier\ns_memrealtime s[54:55]\n"
"s_waitcnt lgkmcnt(0)\ns_add_u32 s44, s54, 16\ns_mov_b32 s45, 8\n.p2align 6\n"
"top_x0:\nv_mov_b32 v48, 0\nv_mov_b32 v49, 0\nv_mov_b32 v50, 0\n"
"v_mov_b32 v51, 0\nv_mov_b32 v52, 0\nv_mov_b32 v53, 0\nv_mov_b32 v54, 0\n"
"v_mov_b32 v55, 0\nv_mov_b32 v78, 0\ns_mul_i32 s48, s45, 6\ns_and_b32 s48, s48, -4\n"
"s_getpc_b64 s[60:61]\nspc_x0:\ns_sub_u32 s48, sgo_x0-spc_x0, s48\ns_add_u32 s60, s60, s48\n"
"s_addc_u32 s61, s61, 0\nsf_x0:\ns_memrealtime s[54:55]\ns_nop 2\n"
"s_memrealtime s[56:57]\ns_nop 2\ns_memrealtime s[58:59]\nsg_x0:\n"
"s_waitcnt lgkmcnt(4)\ns_sub_u32 s47, s54, s44\ns_add_u32 s48, s47, 12\ns_cmp_ge_u32 s48, 12\n"
"s_cbranch_scc1 so_x0\ns_memrealtime s[54:55]\ns_waitcnt lgkmcnt(4)\ns_cmp_ge_u32 s56, s44\n"
"s_cbranch_scc1 sx_x0\ns_memrealtime s[56:57]\ns_waitcnt lgkmcnt(4)\ns_cmp_ge_u32 s58, s44\n"
"s_cbranch_scc1 sx_x0\ns_memrealtime s[58:59]\ns_waitcnt lgkmcnt(4)\ns_cmp_ge_u32 s54, s44\n"
"s_cbranch_scc1 sx_x0\ns_memrealtime s[54:55]\ns_waitcnt lgkmcnt(4)\ns_cmp_ge_u32 s56, s44\n"
"s_cbranch_scc1 sx_x0\ns_memrealtime s[56:57]\ns_waitcnt lgkmcnt(4)\ns_cmp_ge_u32 s58, s44\n"
"s_cbranch_scc1 sx_x0\ns_memrealtime s[58:59]\ns_waitcnt lgkmcnt(4)\ns_cmp_ge_u32 s54, s44\n"
"s_cbranch_scc1 sx_x0\ns_memrealtime s[54:55]\ns_waitcnt lgkmcnt(4)\ns_cmp_ge_u32 s56, s44\n"
"s_cbranch_scc1 sx_x0\ns_memrealtime s[56:57]\ns_waitcnt lgkmcnt(4)\ns_cmp_ge_u32 s58, s44\n"
"s_cbranch_scc1 sx_x0\ns_memrealtime s[58:59]\ns_waitcnt lgkmcnt(4)\ns_cmp_ge_u32 s54, s44\n"
"s_cbranch_scc1 sx_x0\ns_memrealtime s[54:55]\ns_waitcnt lgkmcnt(4)\ns_cmp_ge_u32 s56, s44\n"
"s_cbranch_scc1 sx_x0\ns_memrealtime s[56:57]\ns_waitcnt lgkmcnt(4)\ns_cmp_ge_u32 s58, s44\n"
"s_cbranch_scc1 sx_x0\ns_memrealtime s[58:59]\ns_waitcnt lgkmcnt(4)\ns_cmp_ge_u32 s54, s44\n"
"s_cbranch_scc1 sx_x0\ns_memrealtime s[54:55]\ns_waitcnt lgkmcnt(4)\ns_cmp_ge_u32 s56, s44\n"
"s_cbranch_scc1 sx_x0\ns_memrealtime s[56:57]\ns_waitcnt lgkmcnt(4)\ns_cmp_ge_u32 s58, s44\n"
"s_cbranch_scc1 sx_x0\ns_memrealtime s[58:59]\ns_waitcnt lgkmcnt(4)\ns_cmp_ge_u32 s54, s44\n"
"s_cbranch_scc1 sx_x0\ns_memrealtime s[54:55]\ns_waitcnt lgkmcnt(4)\ns_cmp_ge_u32 s56, s44\n"
"s_cbranch_scc1 sx_x0\ns_memrealtime s[56:57]\ns_branch sx_x0\nso_x0:\n"
"s_waitcnt lgkmcnt(0)\ns_cmp_lt_i32 s47, 0\ns_cbranch_scc1 sf_x0\ns_branch sgo_x0\n"
"sx_x0:\ns_setpc_b64 s[60:61]\ns_nop 0\ns_nop 0\n"
"s_nop 0\ns_nop 0\ns_nop 0\ns_nop 0\n"
"s_nop 0\ns_nop 0\ns_nop 0\ns_nop 0\n"
"s_nop 0\ns_nop 0\ns_nop 0\ns_nop 0\n"
"s_nop 0\ns_nop 0\ns_nop 0\ns_nop 0\n"
"s_nop 0\ns_nop 0\ns_nop 0\ns_nop 0\n"
"s_nop 0\ns_nop 0\nsgo_x0:\nbuffer_load_dwordx4 v[40:43], v10, s[8:11], s43 offen offset:0 sc1\n"
"buffer_load_dwordx4 v[44:47], v10, s[8:11], s43 offen offset:1024 sc1\nlanded_x0:\ns_waitcnt vmcnt(1)\nv_smfmac_f32_16x16x64_bf16 v[48:51], v[40:43], a[0:7], v8\n"
"v_max3_u32 v78, v78, v40, v41\nv_smfmac_f32_16x16x64_bf16 v[52:55], v[40:43], a[16:23], v8\nv_max3_u32 v78, v78, v42, v43\ns_waitcnt vmcnt(0)\n"
"v_smfmac_f32_16x16x64_bf16 v[48:51], v[44:47], a[8:15], v8\nv_max3_u32 v78, v78, v44, v45\nv_smfmac_f32_16x16x64_bf16 v[52:55], v[44:47], a[24:31], v8\nv_max3_u32 v78, v78, v46, v47\n"
"v_cmp_eq_u32 vcc, -1, v78\ns_nop 0\nv_add_f32 v56, v48, v49\nv_add_f32 v57, v50, v51\n"
"s_nop 1\nv_add_f32 v58, v52, v53\nv_add_f32 v59, v54, v55\nds_write2_b32 v81, v56, v57 offset0:0 offset1:40\n"
"ds_write2_b32 v81, v58, v59 offset0:16 offset1:56\ns_cbranch_vccnz retry_x0\nv_mov_b32 v79, 0\ns_nop 1\n"
"v_mov_b32 v79, 0\ns_nop 1\nv_mov_b32 v79, 0\ns_waitcnt lgkmcnt(0)\n"
"s_barrier\nds_read_b64 v[68:69], v82 offset:0\nds_read_b64 v[70:71], v82 offset:1280\nds_read_b64 v[72:73], v82 offset:2560\n"
"ds_read_b64 v[74:75], v82 offset:3840\nv_mov_b32 v79, 0\ns_nop 1\nv_mov_b32 v79, 0\n"
"s_nop 1\nv_mov_b32 v79, 0\ns_nop 1\ns_waitcnt lgkmcnt(2)\n"
"v_pk_add_f32 v[68:69], v[68:69], v[70:71]\ns_waitcnt lgkmcnt(0)\nv_pk_add_f32 v[72:73], v[72:73], v[74:75]\nv_pk_add_f32 v[68:69], v[68:69], v[72:73]\n"
"v_exp_f32 v79, v68\nv_exp_f32 v80, v69\ns_nop 0\nv_cvt_pk_bf16_f32 v79, v79, v80\n"
"buffer_store_dword v79, v16, s[8:11], s41 offen sc1\ns_barrier\nbuffer_load_dwordx4 a[0:3], v9, s[16:19], s50 offen offset:0 sc1\ns_nop 15\n"
"s_nop 7\nbuffer_load_dwordx4 a[4:7], v9, s[16:19], s50 offen offset:1024 sc1\ns_nop 15\ns_nop 7\n"
"buffer_load_dwordx4 a[8:11], v9, s[16:19], s50 offen offset:2048 sc1\ns_nop 15\ns_nop 7\nbuffer_load_dwordx4 a[12:15], v9, s[16:19], s50 offen offset:3072 sc1\n"
"s_nop 15\ns_nop 7\nbuffer_load_dwordx4 a[16:19], v9, s[16:19], s51 offen offset:0 sc1\ns_nop 15\n"
"s_nop 7\nbuffer_load_dwordx4 a[20:23], v9, s[16:19], s51 offen offset:1024 sc1\ns_nop 15\ns_nop 7\n"
"buffer_load_dwordx4 a[24:27], v9, s[16:19], s51 offen offset:2048 sc1\ns_nop 15\ns_nop 7\nbuffer_load_dwordx4 a[28:31], v9, s[16:19], s51 offen offset:3072 sc1\n"
"s_nop 15\ns_nop 7\ns_add_u32 s50, s50, 28672\ns_add_u32 s51, s51, 28672\n"
"buffer_store_dwordx2 v[68:69], v17, s[8:11], s41 offen sc1\nbuffer_store_dwordx4 v[64:67], v15, s[8:11], s42 offen sc1\nds_read_b32 v83, v84\nv_xor_b32 v81, 8192, v81\n"
"v_xor_b32 v82, 8192, v82\ns_waitcnt lgkmcnt(0)\nv_readfirstlane_b32 s47, v83\ns_sub_u32 s47, s47, s40\n"
"s_cmp_eq_u32 s47, 1\ns_cbranch_scc1 adopt_x0\nback_x0:\ns_mul_i32 s48, s46, 8\n"
"s_add_u32 s45, s45, s48\ns_add_u32 s45, s45, s32\ns_lshr_b32 s48, s45, 4\ns_lshl_b32 s48, s48, 2\n"
"s_add_u32 s44, s44, s48\ns_and_b32 s45, s45, 15\ns_add_u32 s52, s40, 1\ns_bitcmp1_b64 s[28:29], s52\n"
"s_subb_u32 s46, s46, 0\ns_add_u32 s40, s40, 1\ns_mov_b32 s43, s41\ns_add_u32 s41, s41, 131072\n"
"s_and_b32 s41, s41, 524287\ns_add_u32 s92, s92, 1\ns_xor_b32 s42, s41, 262144\ns_waitcnt lgkmcnt(0)\n"
"s_cmp_lt_u32 s40, 63\ns_cbranch_scc1 top_x0\ns_branch end_x0\nretry2_x0:\n"
"s_sleep 3\nretry_x0:\ns_add_u32 s53, s53, 1\ns_add_u32 s49, s49, 1\n"
"s_cmp_gt_u32 s49, 65536\ns_cbranch_scc1 end_x0\nbuffer_load_dwordx4 v[40:43], v10, s[8:11], s43 offen offset:0 sc1\nbuffer_load_dwordx4 v[44:47], v10, s[8:11], s43 offen offset:1024 sc1\n"
"v_mov_b32 v78, 0\ns_add_u32 s47, s40, 1\nv_mov_b32 v83, s47\nds_write_b32 v84, v83\n"
"s_waitcnt vmcnt(0)\nv_max3_u32 v78, v78, v40, v41\nv_max3_u32 v78, v78, v42, v43\nv_max3_u32 v78, v78, v44, v45\n"
"v_max3_u32 v78, v78, v46, v47\nv_cmp_eq_u32 vcc, -1, v78\nv_mov_b32 v48, 0\nv_mov_b32 v49, 0\n"
"v_mov_b32 v50, 0\nv_mov_b32 v51, 0\nv_mov_b32 v52, 0\nv_mov_b32 v53, 0\n"
"v_mov_b32 v54, 0\nv_mov_b32 v55, 0\ns_cbranch_vccnz retry2_x0\ns_branch landed_x0\n"
"adopt_x0:\nbuffer_load_dword v60, v28, s[8:11], 0 offen sc1\ns_lshl_b32 s52, s40, 26\ns_or_b32 s47, s52, 0x3ffffff\n"
"s_waitcnt vmcnt(0)\nv_cmp_ge_u32 vcc, s47, v60\ns_nop 1\nv_cndmask_b32 v83, 0, v60, vcc\n"
"s_nop 1\nv_max_u32_dpp v83, v83, v83 row_ror:8 row_mask:0xf bank_mask:0xf\ns_nop 1\nv_max_u32_dpp v83, v83, v83 row_ror:4 row_mask:0xf bank_mask:0xf\n"
"s_nop 1\nv_max_u32_dpp v83, v83, v83 row_ror:2 row_mask:0xf bank_mask:0xf\ns_nop 1\nv_max_u32_dpp v83, v83, v83 row_ror:1 row_mask:0xf bank_mask:0xf\n"
"s_nop 0\nv_readlane_b32 s47, v83, 0\nv_readlane_b32 s48, v83, 16\ns_max_u32 s48, s48, s47\n"
"s_cmp_le_u32 s48, s52\ns_cbranch_scc1 back_x0\ns_bfe_u32 s52, s48, 1179656\ns_add_u32 s52, s52, s30\n"
"s_sub_u32 s45, s52, s44\ns_and_b32 s45, s45, 3\ns_sub_u32 s44, s52, s45\ns_lshl_b32 s45, s45, 2\n"
"s_and_b32 s46, s48, 255\ns_mov_b32 s92, 0\ns_branch back_x0\nend_x0:\n"
"s_branch fk_done\nfk_x1:\ns_mov_b32 s40, 0\ns_mov_b32 s41, 0\n"
"s_mov_b32 s42, 262144\ns_mov_b32 s43, 393216\ns_mov_b32 s44, s30\ns_mov_b32 s45, 0\n"
"s_bcnt1_i32_b64 s46, s[28:29]\ns_mov_b32 s92, 99\ns_mov_b32 s49, 0\ns_mov_b32 s53, 0\n"
"s_mov_b32 s50, s36\ns_mov_b32 s51, s37\nv_mov_b32 v81, v13\nv_mov_b32 v82, v14\n"
"v_mov_b32 v84, 24576\ns_add_u32 s51, s50, 14336\nv_mov_b32 v64, -1\nv_mov_b32 v65, -1\n"
"v_mov_b32 v66, -1\nv_mov_b32 v67, -1\nbuffer_load_dwordx4 a[8:11], v9, s[16:19], s50 offen offset:2048 sc1\nbuffer_load_dwordx4 a[12:15], v9, s[16:19], s50 offen offset:3072 sc1\n"
"buffer_load_dwordx4 a[0:3], v9, s[16:19], s50 offen offset:0 sc1\nbuffer_load_dwordx4 a[4:7], v9, s[16:19], s50 offen offset:1024 sc1\nbuffer_load_dwordx4 a[24:27], v9, s[16:19], s51 offen offset:2048 sc1\nbuffer_load_dwordx4 a[28:31], v9, s[16:19], s51 offen offset:3072 sc1\n"
"buffer_load_dwordx4 a[16:19], v9, s[16:19], s51 offen offset:0 sc1\nbuffer_load_dwordx4 a[20:23], v9, s[16:19], s51 offen offset:1024 sc1\ns_add_u32 s50, s50, 28672\ns_add_u32 s51, s51, 28672\n"
"buffer_load_dword v85, v18, s[8:11], 0 offen sc1\ns_waitcnt vmcnt(0)\nf0_x1:\ns_add_u32 s47, s40, 2\n"
"s_min_u32 s47, s47, 64\ns_add_u32 s47, s47, s34\nv_subrev_u32 v85, s47, v85\nv_cmp_lt_u32 vcc, 64, v85\n"
"s_cbranch_vccz f0ok_x1\ns_add_u32 s53, s53, 0\ns_add_u32 s49, s49, 1\ns_cmp_gt_u32 s49, 65536\n"
"s_cbranch_scc1 end_x1\ns_sleep 2\nbuffer_load_dword v85, v18, s[8:11], 0 offen sc1\ns_waitcnt vmcnt(0)\n"
"s_branch f0_x1\nf0ok_x1:\ns_barrier\ns_memrealtime s[54:55]\n"
"s_waitcnt lgkmcnt(0)\ns_add_u32 s44, s54, 16\ns_mov_b32 s45, 8\n.p2align 6\n"
"top_x1:\nv_mov_b32 v48, 0\nv_mov_b32 v49, 0\nv_mov_b32 v50, 0\n"
"v_mov_b32 v51, 0\nv_mov_b32 v52, 0\nv_mov_b32 v53, 0\nv_mov_b32 v54, 0\n"
"v_mov_b32 v55, 0\nv_mov_b32 v78, 0\ns_mul_i32 s48, s45, 6\ns_and_b32 s48, s48, -4\n"
"s_getpc_b64 s[60:61]\nspc_x1:\ns_sub_u32 s48, sgo_x1-spc_x1, s48\ns_add_u32 s60, s60, s48\n"
"s_addc_u32 s61, s61, 0\nsf_x1:\ns_memrealtime s[54:55]\ns_nop 2\n"
"s_memrealtime s[56:57]\ns_nop 2\ns_memrealtime s[58:59]\nsg_x1:\n"
"s_waitcnt lgkmcnt(4)\ns_sub_u32 s47, s54, s44\ns_add_u32 s48, s47, 12\ns_cmp_ge_u32 s48, 12\n"
"s_cbranch_scc1 so_x1\ns_memrealtime s[54:55]\ns_waitcnt lgkmcnt(4)\ns_cmp_ge_u32 s56, s44\n"
"s_cbranch_scc1 sx_x1\ns_memrealtime s[56:57]\ns_waitcnt lgkmcnt(4)\ns_cmp_ge_u32 s58, s44\n"
"s_cbranch_scc1 sx_x1\ns_memrealtime s[58:59]\ns_waitcnt lgkmcnt(4)\ns_cmp_ge_u32 s54, s44\n"
"s_cbranch_scc1 sx_x1\ns_memrealtime s[54:55]\ns_waitcnt lgkmcnt(4)\ns_cmp_ge_u32 s56, s44\n"
"s_cbranch_scc1 sx_x1\ns_memrealtime s[56:57]\ns_waitcnt lgkmcnt(4)\ns_cmp_ge_u32 s58, s44\n"
"s_cbranch_scc1 sx_x1\ns_memrealtime s[58:59]\ns_waitcnt lgkmcnt(4)\ns_cmp_ge_u32 s54, s44\n"
"s_cbranch_scc1 sx_x1\ns_memrealtime s[54:55]\ns_waitcnt lgkmcnt(4)\ns_cmp_ge_u32 s56, s44\n"
"s_cbranch_scc1 sx_x1\ns_memrealtime s[56:57]\ns_waitcnt lgkmcnt(4)\ns_cmp_ge_u32 s58, s44\n"
"s_cbranch_scc1 sx_x1\ns_memrealtime s[58:59]\ns_waitcnt lgkmcnt(4)\ns_cmp_ge_u32 s54, s44\n"
"s_cbranch_scc1 sx_x1\ns_memrealtime s[54:55]\ns_waitcnt lgkmcnt(4)\ns_cmp_ge_u32 s56, s44\n"
"s_cbranch_scc1 sx_x1\ns_memrealtime s[56:57]\ns_waitcnt lgkmcnt(4)\ns_cmp_ge_u32 s58, s44\n"
"s_cbranch_scc1 sx_x1\ns_memrealtime s[58:59]\ns_waitcnt lgkmcnt(4)\ns_cmp_ge_u32 s54, s44\n"
"s_cbranch_scc1 sx_x1\ns_memrealtime s[54:55]\ns_waitcnt lgkmcnt(4)\ns_cmp_ge_u32 s56, s44\n"
"s_cbranch_scc1 sx_x1\ns_memrealtime s[56:57]\ns_waitcnt lgkmcnt(4)\ns_cmp_ge_u32 s58, s44\n"
"s_cbranch_scc1 sx_x1\ns_memrealtime s[58:59]\ns_waitcnt lgkmcnt(4)\ns_cmp_ge_u32 s54, s44\n"
"s_cbranch_scc1 sx_x1\ns_memrealtime s[54:55]\ns_waitcnt lgkmcnt(4)\ns_cmp_ge_u32 s56, s44\n"
"s_cbranch_scc1 sx_x1\ns_memrealtime s[56:57]\ns_branch sx_x1\nso_x1:\n"
"s_waitcnt lgkmcnt(0)\ns_cmp_lt_i32 s47, 0\ns_cbranch_scc1 sf_x1\ns_branch sgo_x1\n"
"sx_x1:\ns_setpc_b64 s[60:61]\ns_nop 0\ns_nop 0\n"
"s_nop 0\ns_nop 0\ns_nop 0\ns_nop 0\n"
"s_nop 0\ns_nop 0\ns_nop 0\ns_nop 0\n"
"s_nop 0\ns_nop 0\ns_nop 0\ns_nop 0\n"
"s_nop 0\ns_nop 0\ns_nop 0\ns_nop 0\n"
"s_nop 0\ns_nop 0\ns_nop 0\ns_nop 0\n"
"s_nop 0\ns_nop 0\nsgo_x1:\ns_nop 5\n"
"buffer_load_dwordx4 v[40:43], v10, s[8:11], s43 offen offset:1024 sc1\nbuffer_load_dwordx4 v[44:47], v10, s[8:11], s43 offen offset:0 sc1\nlanded_x1:\ns_waitcnt vmcnt(1)\n"
"v_smfmac_f32_16x16x64_bf16 v[48:51], v[40:43], a[8:15], v8\nv_max3_u32 v78, v78, v40, v41\nv_smfmac_f32_16x16x64_bf16 v[52:55], v[40:43], a[24:31], v8\nv_max3_u32 v78, v78, v42, v43\n"
"s_waitcnt vmcnt(0)\nv_smfmac_f32_16x16x64_bf16 v[48:51], v[44:47], a[0:7], v8\nv_max3_u32 v78, v78, v44, v45\nv_smfmac_f32_16x16x64_bf16 v[52:55], v[44:47], a[16:23], v8\n"
"v_max3_u32 v78, v78, v46, v47\nv_cmp_eq_u32 vcc, -1, v78\ns_nop 0\nv_add_f32 v56, v48, v49\n"
"v_add_f32 v57, v50, v51\ns_nop 1\nv_add_f32 v58, v52, v53\nv_add_f32 v59, v54, v55\n"
"ds_write2_b32 v81, v56, v57 offset0:0 offset1:40\nds_write2_b32 v81, v58, v59 offset0:16 offset1:56\ns_cbranch_vccnz retry_x1\nv_mov_b32 v79, 0\n"
"s_nop 1\nv_mov_b32 v79, 0\ns_nop 1\nv_mov_b32 v79, 0\n"
"s_waitcnt lgkmcnt(0)\ns_barrier\nds_read_b64 v[68:69], v82 offset:0\nds_read_b64 v[70:71], v82 offset:1280\n"
"ds_read_b64 v[72:73], v82 offset:2560\nds_read_b64 v[74:75], v82 offset:3840\nv_mov_b32 v79, 0\ns_nop 1\n"
"v_mov_b32 v79, 0\ns_nop 1\nv_mov_b32 v79, 0\ns_nop 1\n"
"s_waitcnt lgkmcnt(2)\nv_pk_add_f32 v[68:69], v[68:69], v[70:71]\ns_waitcnt lgkmcnt(0)\nv_pk_add_f32 v[72:73], v[72:73], v[74:75]\n"
"v_pk_add_f32 v[68:69], v[68:69], v[72:73]\nv_exp_f32 v79, v68\nv_exp_f32 v80, v69\ns_nop 0\n"
"v_cvt_pk_bf16_f32 v79, v79, v80\nbuffer_store_dword v79, v16, s[8:11], s41 offen sc1\ns_barrier\nbuffer_load_dwordx4 a[8:11], v9, s[16:19], s50 offen offset:2048 sc1\n"
"s_nop 15\ns_nop 7\nbuffer_load_dwordx4 a[12:15], v9, s[16:19], s50 offen offset:3072 sc1\ns_nop 15\n"
"s_nop 7\nbuffer_load_dwordx4 a[0:3], v9, s[16:19], s50 offen offset:0 sc1\ns_nop 15\ns_nop 7\n"
"buffer_load_dwordx4 a[4:7], v9, s[16:19], s50 offen offset:1024 sc1\ns_nop 15\ns_nop 7\nbuffer_load_dwordx4 a[24:27], v9, s[16:19], s51 offen offset:2048 sc1\n"
"s_nop 15\ns_nop 7\nbuffer_load_dwordx4 a[28:31], v9, s[16:19], s51 offen offset:3072 sc1\ns_nop 15\n"
"s_nop 7\nbuffer_load_dwordx4 a[16:19], v9, s[16:19], s51 offen offset:0 sc1\ns_nop 15\ns_nop 7\n"
"buffer_load_dwordx4 a[20:23], v9, s[16:19], s51 offen offset:1024 sc1\ns_nop 15\ns_nop 7\ns_add_u32 s50, s50, 28672\n"
"s_add_u32 s51, s51, 28672\nbuffer_store_dwordx2 v[68:69], v17, s[8:11], s41 offen sc1\nbuffer_store_dwordx4 v[64:67], v15, s[8:11], s42 offen sc1\nds_read_b32 v83, v84\n"
"v_xor_b32 v81, 8192, v81\nv_xor_b32 v82, 8192, v82\ns_waitcnt lgkmcnt(0)\nv_readfirstlane_b32 s47, v83\n"
"s_sub_u32 s47, s47, s40\ns_cmp_eq_u32 s47, 1\ns_cbranch_scc1 adopt_x1\nback_x1:\n"
"s_mul_i32 s48, s46, 8\ns_add_u32 s45, s45, s48\ns_add_u32 s45, s45, s32\ns_lshr_b32 s48, s45, 4\n"
"s_lshl_b32 s48, s48, 2\ns_add_u32 s44, s44, s48\ns_and_b32 s45, s45, 15\ns_add_u32 s52, s40, 1\n"
"s_bitcmp1_b64 s[28:29], s52\ns_subb_u32 s46, s46, 0\ns_add_u32 s40, s40, 1\ns_mov_b32 s43, s41\n"
"s_add_u32 s41, s41, 131072\ns_and_b32 s41, s41, 524287\ns_add_u32 s92, s92, 1\ns_xor_b32 s42, s41, 262144\n"
"s_waitcnt lgkmcnt(0)\ns_cmp_lt_u32 s40, 63\ns_cbranch_scc1 top_x1\ns_branch end_x1\n"
"retry2_x1:\ns_sleep 3\nretry_x1:\ns_add_u32 s53, s53, 1\n"
"s_add_u32 s49, s49, 1\ns_cmp_gt_u32 s49, 65536\ns_cbranch_scc1 end_x1\nbuffer_load_dwordx4 v[40:43], v10, s[8:11], s43 offen offset:1024 sc1\n"
"buffer_load_dwordx4 v[44:47], v10, s[8:11], s43 offen offset:0 sc1\nv_mov_b32 v78, 0\ns_add_u32 s47, s40, 1\nv_mov_b32 v83, s47\n"
"ds_write_b32 v84, v83\ns_waitcnt vmcnt(0)\nv_max3_u32 v78, v78, v40, v41\nv_max3_u32 v78, v78, v42, v43\n"
"v_max3_u32 v78, v78, v44, v45\nv_max3_u32 v78, v78, v46, v47\nv_cmp_eq_u32 vcc, -1, v78\nv_mov_b32 v48, 0\n"
"v_mov_b32 v49, 0\nv_mov_b32 v50, 0\nv_mov_b32 v51, 0\nv_mov_b32 v52, 0\n"
"v_mov_b32 v53, 0\nv_mov_b32 v54, 0\nv_mov_b32 v55, 0\ns_cbranch_vccnz retry2_x1\n"
"s_branch landed_x1\nadopt_x1:\nbuffer_load_dword v60, v28, s[8:11], 0 offen sc1\ns_lshl_b32 s52, s40, 26\n"
"s_or_b32 s47, s52, 0x3ffffff\ns_waitcnt vmcnt(0)\nv_cmp_ge_u32 vcc, s47, v60\ns_nop 1\n"
"v_cndmask_b32 v83, 0, v60, vcc\ns_nop 1\nv_max_u32_dpp v83, v83, v83 row_ror:8 row_mask:0xf bank_mask:0xf\ns_nop 1\n"
"v_max_u32_dpp v83, v83, v83 row_ror:4 row_mask:0xf bank_mask:0xf\ns_nop 1\nv_max_u32_dpp v83, v83, v83 row_ror:2 row_mask:0xf bank_mask:0xf\ns_nop 1\n"
"v_max_u32_dpp v83, v83, v83 row_ror:1 row_mask:0xf bank_mask:0xf\ns_nop 0\nv_readlane_b32 s47, v83, 0\nv_readlane_b32 s48, v83, 16\n"
"s_max_u32 s48, s48, s47\ns_cmp_le_u32 s48, s52\ns_cbranch_scc1 back_x1\ns_bfe_u32 s52, s48, 1179656\n"
"s_add_u32 s52, s52, s30\ns_sub_u32 s45, s52, s44\ns_and_b32 s45, s45, 3\ns_sub_u32 s44, s52, s45\n"
"s_lshl_b32 s45, s45, 2\ns_and_b32 s46, s48, 255\ns_mov_b32 s92, 0\ns_branch back_x1\n"
"end_x1:\ns_branch fk_done\nfk_x2:\ns_mov_b32 s40, 0\n"
"s_mov_b32 s41, 0\ns_mov_b32 s42, 262144\ns_mov_b32 s43, 393216\ns_mov_b32 s44, s30\n"
"s_mov_b32 s45, 0\ns_bcnt1_i32_b64 s46, s[28:29]\ns_mov_b32 s92, 99\ns_mov_b32 s49, 0\n"
"s_mov_b32 s53, 0\ns_mov_b32 s50, s36\ns_mov_b32 s51, s37\nv_mov_b32 v81, v13\n"
"v_mov_b32 v82, v14\nv_mov_b32 v84, 24576\ns_add_u32 s51, s50, 14336\nv_mov_b32 v64, -1\n"
"v_mov_b32 v65, -1\nv_mov_b32 v66, -1\nv_mov_b32 v67, -1\nbuffer_load_dwordx4 a[0:3], v9, s[16:19], s50 offen offset:0 sc1\n"
"buffer_load_dwordx4 a[4:7], v9, s[16:19], s50 offen offset:1024 sc1\nbuffer_load_dwordx4 a[8:11], v9, s[16:19], s50 offen offset:2048 sc1\nbuffer_load_dwordx4 a[12:15], v9, s[16:19], s50 offen offset:3072 sc1\nbuffer_load_dwordx4 a[16:19], v9, s[16:19], s51 offen offset:0 sc1\n"
"buffer_load_dwordx4 a[20:23], v9, s[16:19], s51 offen offset:1024 sc1\nbuffer_load_dwordx4 a[24:27], v9, s[16:19], s51 offen offset:2048 sc1\nbuffer_load_dwordx4 a[28:31], v9, s[16:19], s51 offen offset:3072 sc1\ns_add_u32 s50, s50, 28672\n"
"s_add_u32 s51, s51, 28672\nbuffer_load_dword v85, v18, s[8:11], 0 offen sc1\ns_waitcnt vmcnt(0)\nf0_x2:\n"
"s_add_u32 s47, s40, 2\ns_min_u32 s47, s47, 64\ns_add_u32 s47, s47, s34\nv_subrev_u32 v85, s47, v85\n"
"v_cmp_lt_u32 vcc, 64, v85\ns_cbranch_vccz f0ok_x2\ns_add_u32 s53, s53, 0\ns_add_u32 s49, s49, 1\n"
"s_cmp_gt_u32 s49, 65536\ns_cbranch_scc1 end_x2\ns_sleep 2\nbuffer_load_dword v85, v18, s[8:11], 0 offen sc1\n"
"s_waitcnt vmcnt(0)\ns_branch f0_x2\nf0ok_x2:\ns_barrier\n"
"s_memrealtime s[54:55]\ns_waitcnt lgkmcnt(0)\ns_add_u32 s44, s54, 16\ns_mov_b32 s45, 8\n"
".p2align 6\ntop_x2:\nv_mov_b32 v48, 0\nv_mov_b32 v49, 0\n"
"v_mov_b32 v50, 0\nv_mov_b32 v51, 0\nv_mov_b32 v52, 0\nv_mov_b32 v53, 0\n"
"v_mov_b32 v54, 0\nv_mov_b32 v55, 0\nv_mov_b32 v78, 0\ns_mul_i32 s48, s45, 6\n"
"s_and_b32 s48, s48, -4\ns_getpc_b64 s[60:61]\nspc_x2:\ns_sub_u32 s48, sgo_x2-spc_x2, s48\n"
"s_add_u32 s60, s60, s48\ns_addc_u32 s61, s61, 0\nsf_x2:\ns_memrealtime s[54:55]\n"
"s_nop 2\ns_memrealtime s[56:57]\ns_nop 2\ns_memrealtime s[58:59]\n"
"sg_x2:\ns_waitcnt lgkmcnt(4)\ns_sub_u32 s47, s54, s44\ns_add_u32 s48, s47, 12\n"
"s_cmp_ge_u32 s48, 12\ns_cbranch_scc1 so_x2\ns_memrealtime s[54:55]\ns_waitcnt lgkmcnt(4)\n"
"s_cmp_ge_u32 s56, s44\ns_cbranch_scc1 sx_x2\ns_memrealtime s[56:57]\ns_waitcnt lgkmcnt(4)\n"
"s_cmp_ge_u32 s58, s44\ns_cbranch_scc1 sx_x2\ns_memrealtime s[58:59]\ns_waitcnt lgkmcnt(4)\n"
"s_cmp_ge_u32 s54, s44\ns_cbranch_scc1 sx_x2\ns_memrealtime s[54:55]\ns_waitcnt lgkmcnt(4)\n"
"s_cmp_ge_u32 s56, s44\ns_cbranch_scc1 sx_x2\ns_memrealtime s[56:57]\ns_waitcnt lgkmcnt(4)\n"
"s_cmp_ge_u32 s58, s44\ns_cbranch_scc1 sx_x2\ns_memrealtime s[58:59]\ns_waitcnt lgkmcnt(4)\n"
"s_cmp_ge_u32 s54, s44\ns_cbranch_scc1 sx_x2\ns_memrealtime s[54:55]\ns_waitcnt lgkmcnt(4)\n"
"s_cmp_ge_u32 s56, s44\ns_cbranch_scc1 sx_x2\ns_memrealtime s[56:57]\ns_waitcnt lgkmcnt(4)\n"
"s_cmp_ge_u32 s58, s44\ns_cbranch_scc1 sx_x2\ns_memrealtime s[58:59]\ns_waitcnt lgkmcnt(4)\n"
"s_cmp_ge_u32 s54, s44\ns_cbranch_scc1 sx_x2\ns_memrealtime s[54:55]\ns_waitcnt lgkmcnt(4)\n"
"s_cmp_ge_u32 s56, s44\ns_cbranch_scc1 sx_x2\ns_memrealtime s[56:57]\ns_waitcnt lgkmcnt(4)\n"
"s_cmp_ge_u32 s58, s44\ns_cbranch_scc1 sx_x2\ns_memrealtime s[58:59]\ns_waitcnt lgkmcnt(4)\n"
"s_cmp_ge_u32 s54, s44\ns_cbranch_scc1 sx_x2\ns_memrealtime s[54:55]\ns_waitcnt lgkmcnt(4)\n"
"s_cmp_ge_u32 s56, s44\ns_cbranch_scc1 sx_x2\ns_memrealtime s[56:57]\ns_waitcnt lgkmcnt(4)\n"
"s_cmp_ge_u32 s58, s44\ns_cbranch_scc1 sx_x2\ns_memrealtime s[58:59]\ns_waitcnt lgkmcnt(4)\n"
"s_cmp_ge_u32 s54, s44\ns_cbranch_scc1 sx_x2\ns_memrealtime s[54:55]\ns_waitcnt lgkmcnt(4)\n"
"s_cmp_ge_u32 s56, s44\ns_cbranch_scc1 sx_x2\ns_memrealtime s[56:57]\ns_branch sx_x2\n"
"so_x2:\ns_waitcnt lgkmcnt(0)\ns_cmp_lt_i32 s47, 0\ns_cbranch_scc1 sf_x2\n"
"s_branch sgo_x2\nsx_x2:\ns_setpc_b64 s[60:61]\ns_nop 0\n"
"s_nop 0\ns_nop 0\ns_nop 0\ns_nop 0\n"
"s_nop 0\ns_nop 0\ns_nop 0\ns_nop 0\n"
"s_nop 0\ns_nop 0\ns_nop 0\ns_nop 0\n"
"s_nop 0\ns_nop 0\ns_nop 0\ns_nop 0\n"
"s_nop 0\ns_nop 0\ns_nop 0\ns_nop 0\n"
"s_nop 0\ns_nop 0\ns_nop 0\nsgo_x2:\n"
"s_nop 11\nbuffer_load_dwordx4 v[40:43], v10, s[8:11], s43 offen offset:0 sc1\nbuffer_load_dwordx4 v[44:47], v10, s[8:11], s43 offen offset:1024 sc1\nv_mov_b32 v79, 0\n"
"s_nop 1\nv_mov_b32 v79, 0\ns_nop 1\nv_mov_b32 v79, 0\n"
"s_nop 1\nv_mov_b32 v79, 0\ns_nop 1\nv_mov_b32 v79, 0\n"
"s_nop 1\nv_mov_b32 v79, 0\ns_nop 1\nv_mov_b32 v79, 0\n"
"s_nop 1\nv_mov_b32 v79, 0\ns_nop 1\nv_mov_b32 v79, 0\n"
"s_nop 1\nv_mov_b32 v79, 0\ns_nop 1\nv_mov_b32 v79, 0\n"
"s_nop 1\nv_mov_b32 v79, 0\ns_nop 1\nv_mov_b32 v79, 0\n"
"s_nop 1\nv_mov_b32 v79, 0\ns_nop 1\nv_mov_b32 v79, 0\n"
"s_nop 1\nv_mov_b32 v79, 0\ns_nop 1\nv_mov_b32 v79, 0\n"
"s_nop 1\nv_mov_b32 v79, 0\ns_nop 1\nv_mov_b32 v79, 0\n"
"s_nop 1\nv_mov_b32 v79, 0\ns_nop 1\nv_mov_b32 v79, 0\n"
"s_nop 1\nv_mov_b32 v79, 0\ns_nop 1\nv_mov_b32 v79, 0\n"
"s_nop 1\nv_mov_b32 v79, 0\ns_nop 1\nv_mov_b32 v79, 0\n"
"s_nop 1\nv_mov_b32 v79, 0\ns_nop 1\nv_mov_b32 v79, 0\n"
"s_nop 1\nv_mov_b32 v79, 0\ns_nop 1\nv_mov_b32 v79, 0\n"
"landed_x2:\ns_waitcnt vmcnt(1)\nv_smfmac_f32_16x16x64_bf16 v[48:51], v[40:43], a[0:7], v8\nv_max3_u32 v78, v78, v40, v41\n"
"v_smfmac_f32_16x16x64_bf16 v[52:55], v[40:43], a[16:23], v8\nv_max3_u32 v78, v78, v42, v43\ns_waitcnt vmcnt(0)\nv_smfmac_f32_16x16x64_bf16 v[48:51], v[44:47], a[8:15], v8\n"
"v_max3_u32 v78, v78, v44, v45\nv_smfmac_f32_16x16x64_bf16 v[52:55], v[44:47], a[24:31], v8\nv_max3_u32 v78, v78, v46, v47\nv_cmp_eq_u32 vcc, -1, v78\n"
"s_nop 0\nv_add_f32 v56, v48, v49\nv_add_f32 v57, v50, v51\ns_nop 1\n"
"v_add_f32 v58, v52, v53\nv_add_f32 v59, v54, v55\nds_write2_b32 v81, v56, v57 offset0:0 offset1:40\nds_write2_b32 v81, v58, v59 offset0:16 offset1:56\n"
"s_cbranch_vccnz retry_x2\nv_mov_b32 v79, 0\ns_nop 1\nv_mov_b32 v79, 0\n"
"s_nop 1\nv_mov_b32 v79, 0\ns_waitcnt lgkmcnt(0)\ns_barrier\n"
"ds_read_b64 v[68:69], v82 offset:0\nds_read_b64 v[70:71], v82 offset:1280\nds_read_b64 v[72:73], v82 offset:2560\nds_read_b64 v[74:75], v82 offset:3840\n"
"v_mov_b32 v79, 0\ns_nop 1\nv_mov_b32 v79, 0\ns_nop 1\n"
"v_mov_b32 v79, 0\ns_nop 1\ns_waitcnt lgkmcnt(2)\nv_pk_add_f32 v[68:69], v[68:69], v[70:71]\n"
"s_waitcnt lgkmcnt(0)\nv_pk_add_f32 v[72:73], v[72:73], v[74:75]\nv_pk_add_f32 v[68:69], v[68:69], v[72:73]\nv_exp_f32 v79, v68\n"
"v_exp_f32 v80, v69\ns_nop 0\nv_cvt_pk_bf16_f32 v79, v79, v80\nbuffer_store_dword v79, v16, s[8:11], s41 offen sc1\n"
"s_barrier\nbuffer_load_dwordx4 a[0:3], v9, s[16:19], s50 offen offset:0 sc1\ns_nop 15\ns_nop 7\n"
"buffer_load_dwordx4 a[4:7], v9, s[16:19], s50 offen offset:1024 sc1\ns_nop 15\ns_nop 7\nbuffer_load_dwordx4 a[8:11], v9, s[16:19], s50 offen offset:2048 sc1\n"
"s_nop 15\ns_nop 7\nbuffer_load_dwordx4 a[12:15], v9, s[16:19], s50 offen offset:3072 sc1\ns_nop 15\n"
"s_nop 7\nbuffer_load_dwordx4 a[16:19], v9, s[16:19], s51 offen offset:0 sc1\ns_nop 15\ns_nop 7\n"
"buffer_load_dwordx4 a[20:23], v9, s[16:19], s51 offen offset:1024 sc1\ns_nop 15\ns_nop 7\nbuffer_load_dwordx4 a[24:27], v9, s[16:19], s51 offen offset:2048 sc1\n"
"s_nop 15\ns_nop 7\nbuffer_load_dwordx4 a[28:31], v9, s[16:19], s51 offen offset:3072 sc1\ns_nop 15\n"
"s_nop 7\ns_add_u32 s50, s50, 28672\ns_add_u32 s51, s51, 28672\nbuffer_store_dwordx2 v[68:69], v17, s[8:11], s41 offen sc1\n"
"buffer_store_dwordx4 v[64:67], v15, s[8:11], s42 offen sc1\nds_read_b32 v83, v84\nv_xor_b32 v81, 8192, v81\nv_xor_b32 v82, 8192, v82\n"
"s_waitcnt lgkmcnt(0)\nv_readfirstlane_b32 s47, v83\ns_sub_u32 s47, s47, s40\ns_cmp_eq_u32 s47, 1\n"
"s_cbranch_scc1 adopt_x2\nback_x2:\ns_mul_i32 s48, s46, 8\ns_add_u32 s45, s45, s48\n"
"s_add_u32 s45, s45, s32\ns_lshr_b32 s48, s45, 4\ns_lshl_b32 s48, s48, 2\ns_add_u32 s44, s44, s48\n"
"s_and_b32 s45, s45, 15\ns_add_u32 s52, s40, 1\ns_bitcmp1_b64 s[28:29], s52\ns_subb_u32 s46, s46, 0\n"
"s_add_u32 s40, s40, 1\ns_mov_b32 s43, s41\ns_add_u32 s41, s41, 131072\ns_and_b32 s41, s41, 524287\n"
"s_add_u32 s92, s92, 1\ns_xor_b32 s42, s41, 262144\ns_waitcnt lgkmcnt(0)\ns_cmp_lt_u32 s40, 63\n"
"s_cbranch_scc1 top_x2\ns_branch end_x2\nretry2_x2:\ns_sleep 3\n"
"retry_x2:\ns_add_u32 s53, s53, 1\ns_add_u32 s49, s49, 1\ns_cmp_gt_u32 s49, 65536\n"
"s_cbranch_scc1 end_x2\nbuffer_load_dwordx4 v[40:43], v10, s[8:11], s43 offen offset:0 sc1\nbuffer_load_dwordx4 v[44:47], v10, s[8:11], s43 offen offset:1024 sc1\nv_mov_b32 v78, 0\n"
"s_add_u32 s47, s40, 1\nv_mov_b32 v83, s47\nds_write_b32 v84, v83\ns_waitcnt vmcnt(0)\n"
"v_max3_u32 v78, v78, v40, v41\nv_max3_u32 v78, v78, v42, v43\nv_max3_u32 v78, v78, v44, v45\nv_max3_u32 v78, v78, v46, v47\n"
"v_cmp_eq_u32 vcc, -1, v78\nv_mov_b32 v48, 0\nv_mov_b32 v49, 0\nv_mov_b32 v50, 0\n"
"v_mov_b32 v51, 0\nv_mov_b32 v52, 0\nv_mov_b32 v53, 0\nv_mov_b32 v54, 0\n"
"v_mov_b32 v55, 0\ns_cbranch_vccnz retry2_x2\ns_branch landed_x2\nadopt_x2:\n"
"buffer_load_dword v60, v28, s[8:11], 0 offen sc1\ns_lshl_b32 s52, s40, 26\ns_or_b32 s47, s52, 0x3ffffff\ns_waitcnt vmcnt(0)\n"
"v_cmp_ge_u32 vcc, s47, v60\ns_nop 1\nv_cndmask_b32 v83, 0, v60, vcc\ns_nop 1\n"
"v_max_u32_dpp v83, v83, v83 row_ror:8 row_mask:0xf bank_mask:0xf\ns_nop 1\nv_max_u32_dpp v83, v83, v83 row_ror:4 row_mask:0xf bank_mask:0xf\ns_nop 1\n"
"v_max_u32_dpp v83, v83, v83 row_ror:2 row_mask:0xf bank_mask:0xf\ns_nop 1\nv_max_u32_dpp v83, v83, v83 row_ror:1 row_mask:0xf bank_mask:0xf\ns_nop 0\n"
"v_readlane_b32 s47, v83, 0\nv_readlane_b32 s48, v83, 16\ns_max_u32 s48, s48, s47\ns_cmp_le_u32 s48, s52\n"
"s_cbranch_scc1 back_x2\ns_bfe_u32 s52, s48, 1179656\ns_add_u32 s52, s52, s30\ns_sub_u32 s45, s52, s44\n"
"s_and_b32 s45, s45, 3\ns_sub_u32 s44, s52, s45\ns_lshl_b32 s45, s45, 2\ns_and_b32 s46, s48, 255\n"
"s_mov_b32 s92, 0\ns_branch back_x2\nend_x2:\ns_branch fk_done\n"
"fk_x3:\ns_mov_b32 s40, 0\ns_mov_b32 s41, 0\ns_mov_b32 s42, 262144\n"
"s_mov_b32 s43, 393216\ns_mov_b32 s44, s30\ns_mov_b32 s45, 0\ns_bcnt1_i32_b64 s46, s[28:29]\n"
"s_mov_b32 s92, 99\ns_mov_b32 s49, 0\ns_mov_b32 s53, 0\ns_mov_b32 s50, s36\n"
"s_mov_b32 s51, s37\nv_mov_b32 v77, v13\nv_mov_b32 v78, v14\nv_mov_b32 v80, 24576\n"
"s_add_u32 s51, s50, 14336\nv_mov_b32 v60, -1\nv_mov_b32 v61, -1\nv_mov_b32 v62, -1\n"
"v_mov_b32 v63, -1\nbuffer_load_dwordx4 a[0:3], v9, s[16:19], s50 offen offset:0 sc1\nbuffer_load_dwordx4 a[4:7], v9, s[16:19], s50 offen offset:1024 sc1\nbuffer_load_dwordx4 a[16:19], v9, s[16:19], s51 offen offset:0 sc1\n"
"buffer_load_dwordx4 a[20:23], v9, s[16:19], s51 offen offset:1024 sc1\ns_add_u32 s50, s50, 28672\ns_add_u32 s51, s51, 28672\nv_lshlrev_b32 v58, 2, v9\n"
"s_mov_b32 s52, 1064960\nbuffer_load_dword v56, v58, s[8:11], s52 offen sc1\ns_add_u32 s52, s52, 4096\nbuffer_load_dword v57, v58, s[8:11], s52 offen sc1\n"
"buffer_load_dword v81, v18, s[8:11], 0 offen sc1\ns_waitcnt vmcnt(0)\nar_x3:\nv_xor_b32 v56, s34, v56\n"
"v_xor_b32 v57, s34, v57\nv_or_b32 v56, v56, v57\nv_cmp_ne_u32 vcc, 0, v56\ns_cbranch_vccz arok_x3\n"
"s_add_u32 s53, s53, 0\ns_add_u32 s49, s49, 1\ns_cmp_gt_u32 s49, 65536\ns_cbranch_scc1 end_x3\n"
"s_sleep 2\nv_lshlrev_b32 v58, 2, v9\ns_mov_b32 s52, 1064960\nbuffer_load_dword v56, v58, s[8:11], s52 offen sc1\n"
"s_add_u32 s52, s52, 4096\nbuffer_load_dword v57, v58, s[8:11], s52 offen sc1\ns_waitcnt vmcnt(0)\ns_branch ar_x3\n"
"arok_x3:\nf0_x3:\ns_add_u32 s47, s40, 2\ns_min_u32 s47, s47, 64\n"
"s_add_u32 s47, s47, s34\nv_subrev_u32 v81, s47, v81\nv_cmp_lt_u32 vcc, 64, v81\ns_cbranch_vccz f0ok_x3\n"
"s_add_u32 s53, s53, 0\ns_add_u32 s49, s49, 1\ns_cmp_gt_u32 s49, 65536\ns_cbranch_scc1 end_x3\n"
"s_sleep 2\nbuffer_load_dword v81, v18, s[8:11], 0 offen sc1\ns_waitcnt vmcnt(0)\ns_branch f0_x3\n"
"f0ok_x3:\ns_barrier\ns_memrealtime s[54:55]\ns_waitcnt lgkmcnt(0)\n"
"s_add_u32 s44, s54, 16\ns_mov_b32 s45, 8\n.p2align 6\ntop_x3:\n"
"v_mov_b32 v44, 0\nv_mov_b32 v45, 0\nv_mov_b32 v46, 0\nv_mov_b32 v47, 0\n"
"v_mov_b32 v48, 0\nv_mov_b32 v49, 0\nv_mov_b32 v50, 0\nv_mov_b32 v51, 0\n"
"v_mov_b32 v74, 0\ns_mul_i32 s48, s45, 6\ns_and_b32 s48, s48, -4\ns_getpc_b64 s[60:61]\n"
"spc_x3:\ns_sub_u32 s48, sgo_x3-spc_x3, s48\ns_add_u32 s60, s60, s48\ns_addc_u32 s61, s61, 0\n"
"sf_x3:\ns_memrealtime s[54:55]\ns_nop 2\ns_memrealtime s[56:57]\n"
"s_nop 2\ns_memrealtime s[58:59]\nsg_x3:\ns_waitcnt lgkmcnt(4)\n"
"s_sub_u32 s47, s54, s44\ns_add_u32 s48, s47, 12\ns_cmp_ge_u32 s48, 12\ns_cbranch_scc1 so_x3\n"
"s_memrealtime s[54:55]\ns_waitcnt lgkmcnt(4)\ns_cmp_ge_u32 s56, s44\ns_cbranch_scc1 sx_x3\n"
"s_memrealtime s[56:57]\ns_waitcnt lgkmcnt(4)\ns_cmp_ge_u32 s58, s44\ns_cbranch_scc1 sx_x3\n"
"s_memrealtime s[58:59]\ns_waitcnt lgkmcnt(4)\ns_cmp_ge_u32 s54, s44\ns_cbranch_scc1 sx_x3\n"
"s_memrealtime s[54:55]\ns_waitcnt lgkmcnt(4)\ns_cmp_ge_u32 s56, s44\ns_cbranch_scc1 sx_x3\n"
"s_memrealtime s[56:57]\ns_waitcnt lgkmcnt(4)\ns_cmp_ge_u32 s58, s44\ns_cbranch_scc1 sx_x3\n"
"s_memrealtime s[58:59]\ns_waitcnt lgkmcnt(4)\ns_cmp_ge_u32 s54, s44\ns_cbranch_scc1 sx_x3\n"
"s_memrealtime s[54:55]\ns_waitcnt lgkmcnt(4)\ns_cmp_ge_u32 s56, s44\ns_cbranch_scc1 sx_x3\n"
"s_memrealtime s[56:57]\ns_waitcnt lgkmcnt(4)\ns_cmp_ge_u32 s58, s44\ns_cbranch_scc1 sx_x3\n"
"s_memrealtime s[58:59]\ns_waitcnt lgkmcnt(4)\ns_cmp_ge_u32 s54, s44\ns_cbranch_scc1 sx_x3\n"
"s_memrealtime s[54:55]\ns_waitcnt lgkmcnt(4)\ns_cmp_ge_u32 s56, s44\ns_cbranch_scc1 sx_x3\n"
"s_memrealtime s[56:57]\ns_waitcnt lgkmcnt(4)\ns_cmp_ge_u32 s58, s44\ns_cbranch_scc1 sx_x3\n"
"s_memrealtime s[58:59]\ns_waitcnt lgkmcnt(4)\ns_cmp_ge_u32 s54, s44\ns_cbranch_scc1 sx_x3\n"
"s_memrealtime s[54:55]\ns_waitcnt lgkmcnt(4)\ns_cmp_ge_u32 s56, s44\ns_cbranch_scc1 sx_x3\n"
"s_memrealtime s[56:57]\ns_waitcnt lgkmcnt(4)\ns_cmp_ge_u32 s58, s44\ns_cbranch_scc1 sx_x3\n"
"s_memrealtime s[58:59]\ns_waitcnt lgkmcnt(4)\ns_cmp_ge_u32 s54, s44\ns_cbranch_scc1 sx_x3\n"
"s_memrealtime s[54:55]\ns_waitcnt lgkmcnt(4)\ns_cmp_ge_u32 s56, s44\ns_cbranch_scc1 sx_x3\n"
"s_memrealtime s[56:57]\ns_branch sx_x3\nso_x3:\ns_waitcnt lgkmcnt(0)\n"
"s_cmp_lt_i32 s47, 0\ns_cbranch_scc1 sf_x3\ns_branch sgo_x3\nsx_x3:\n"
"s_setpc_b64 s[60:61]\ns_nop 0\ns_nop 0\ns_nop 0\n"
"s_nop 0\ns_nop 0\ns_nop 0\ns_nop 0\n"
"s_nop 0\ns_nop 0\ns_nop 0\ns_nop 0\n"
"s_nop 0\ns_nop 0\ns_nop 0\ns_nop 0\n"
"s_nop 0\ns_nop 0\ns_nop 0\ns_nop 0\n"
"s_nop 0\ns_nop 0\ns_nop 0\ns_nop 0\n"
"s_nop 0\nsgo_x3:\ns_nop 15\ns_nop 1\n"
"buffer_load_dwordx4 v[40:43], v10, s[8:11], s43 offen offset:0 sc1\nv_mov_b32 v75, 0\ns_nop 1\nv_mov_b32 v75, 0\n"
"s_nop 1\nv_mov_b32 v75, 0\ns_nop 1\nv_mov_b32 v75, 0\n"
"s_nop 1\nv_mov_b32 v75, 0\ns_nop 1\nv_mov_b32 v75, 0\n"
"s_nop 1\nv_mov_b32 v75, 0\ns_nop 1\nv_mov_b32 v75, 0\n"
"s_nop 1\nv_mov_b32 v75, 0\ns_nop 1\nv_mov_b32 v75, 0\n"
"s_nop 1\nv_mov_b32 v75, 0\ns_nop 1\nv_mov_b32 v75, 0\n"
"s_nop 1\nv_mov_b32 v75, 0\ns_nop 1\nv_mov_b32 v75, 0\n"
"s_nop 1\nv_mov_b32 v75, 0\ns_nop 1\nv_mov_b32 v75, 0\n"
"s_nop 1\nv_mov_b32 v75, 0\ns_nop 1\nv_mov_b32 v75, 0\n"
"s_nop 1\nv_mov_b32 v75, 0\ns_nop 1\nv_mov_b32 v75, 0\n"
"s_nop 1\nv_mov_b32 v75, 0\ns_nop 1\nv_mov_b32 v75, 0\n"
"s_nop 1\nv_mov_b32 v75, 0\ns_nop 1\nv_mov_b32 v75, 0\n"
"s_nop 1\nv_mov_b32 v75, 0\ns_nop 1\nv_mov_b32 v75, 0\n"
"s_nop 1\nv_mov_b32 v75, 0\ns_nop 1\nv_mov_b32 v75, 0\n"
"s_nop 1\nv_mov_b32 v75, 0\ns_nop 1\nv_mov_b32 v75, 0\n"
"s_nop 1\nv_mov_b32 v75, 0\ns_nop 1\nv_mov_b32 v75, 0\n"
"s_nop 0\nlanded_x3:\ns_waitcnt vmcnt(0)\nv_smfmac_f32_16x16x64_bf16 v[44:47], v[40:43], a[0:7], v8\n"
"v_max3_u32 v74, v74, v40, v41\nv_smfmac_f32_16x16x64_bf16 v[48:51], v[40:43], a[16:23], v8\nv_max3_u32 v74, v74, v42, v43\nv_cmp_eq_u32 vcc, -1, v74\n"
"s_nop 0\nv_add_f32 v52, v44, v45\nv_add_f32 v53, v46, v47\ns_nop 1\n"
"v_add_f32 v54, v48, v49\nv_add_f32 v55, v50, v51\nds_write2_b32 v77, v52, v53 offset0:0 offset1:40\nds_write2_b32 v77, v54, v55 offset0:16 offset1:56\n"
"s_cbranch_vccnz retry_x3\nv_mov_b32 v75, 0\ns_nop 1\nv_mov_b32 v75, 0\n"
"s_nop 1\nv_mov_b32 v75, 0\ns_waitcnt lgkmcnt(0)\ns_barrier\n"
"ds_read_b64 v[64:65], v78 offset:0\nds_read_b64 v[66:67], v78 offset:1280\nds_read_b64 v[68:69], v78 offset:2560\nds_read_b64 v[70:71], v78 offset:3840\n"
"v_mov_b32 v75, 0\ns_nop 1\nv_mov_b32 v75, 0\ns_nop 1\n"
"v_mov_b32 v75, 0\ns_nop 1\ns_waitcnt lgkmcnt(2)\nv_pk_add_f32 v[64:65], v[64:65], v[66:67]\n"
"s_waitcnt lgkmcnt(0)\nv_pk_add_f32 v[68:69], v[68:69], v[70:71]\nv_pk_add_f32 v[64:65], v[64:65], v[68:69]\nv_exp_f32 v75, v64\n"
"v_exp_f32 v76, v65\ns_nop 0\nv_cvt_pk_bf16_f32 v75, v75, v76\nbuffer_store_dword v75, v16, s[8:11], s41 offen sc1\n"
"s_barrier\nbuffer_load_dword v81, v18, s[8:11], 0 offen sc1\nbuffer_load_dwordx4 a[0:3], v9, s[16:19], s50 offen offset:0 sc1\ns_nop 15\n"
"s_nop 7\nbuffer_load_dwordx4 a[4:7], v9, s[16:19], s50 offen offset:1024 sc1\ns_nop 15\ns_nop 7\n"
"buffer_load_dwordx4 a[16:19], v9, s[16:19], s51 offen offset:0 sc1\ns_nop 15\ns_nop 7\nbuffer_load_dwordx4 a[20:23], v9, s[16:19], s51 offen offset:1024 sc1\n"
"s_nop 15\ns_nop 7\ns_add_u32 s50, s50, 28672\ns_add_u32 s51, s51, 28672\n"
"buffer_store_dwordx2 v[64:65], v17, s[8:11], s41 offen sc1\nbuffer_store_dwordx4 v[60:63], v15, s[8:11], s42 offen sc1\nds_read_b32 v79, v80\nv_xor_b32 v77, 8192, v77\n"
"v_xor_b32 v78, 8192, v78\ns_waitcnt lgkmcnt(0)\nv_readfirstlane_b32 s47, v79\ns_sub_u32 s47, s47, s40\n"
"s_cmp_eq_u32 s47, 1\ns_cbranch_scc1 adopt_x3\nback_x3:\ns_waitcnt vmcnt(6)\n"
"fl_x3:\ns_add_u32 s47, s40, 4\ns_min_u32 s47, s47, 64\ns_add_u32 s47, s47, s34\n"
"v_subrev_u32 v81, s47, v81\nv_cmp_lt_u32 vcc, 64, v81\ns_cbranch_vccz flok_x3\ns_add_u32 s53, s53, 0\n"
"s_add_u32 s49, s49, 1\ns_cmp_gt_u32 s49, 65536\ns_cbranch_scc1 end_x3\ns_sleep 2\n"
"buffer_load_dword v81, v18, s[8:11], 0 offen sc1\ns_waitcnt vmcnt(0)\ns_branch fl_x3\nflok_x3:\n"
"s_mul_i32 s48, s46, 8\ns_add_u32 s45, s45, s48\ns_add_u32 s45, s45, s32\ns_lshr_b32 s48, s45, 4\n"
"s_lshl_b32 s48, s48, 2\ns_add_u32 s44, s44, s48\ns_and_b32 s45, s45, 15\ns_add_u32 s52, s40, 1\n"
"s_bitcmp1_b64 s[28:29], s52\ns_subb_u32 s46, s46, 0\ns_add_u32 s40, s40, 1\ns_mov_b32 s43, s41\n"
"s_add_u32 s41, s41, 131072\ns_and_b32 s41, s41, 524287\ns_add_u32 s92, s92, 1\ns_xor_b32 s42, s41, 262144\n"
"s_waitcnt lgkmcnt(0)\ns_cmp_lt_u32 s40, 63\ns_cbranch_scc1 top_x3\ns_branch end_x3\n"
"retry2_x3:\ns_sleep 3\nretry_x3:\ns_add_u32 s53, s53, 1\n"
"s_add_u32 s49, s49, 1\ns_cmp_gt_u32 s49, 65536\ns_cbranch_scc1 end_x3\nbuffer_load_dwordx4 v[40:43], v10, s[8:11], s43 offen offset:0 sc1\n"
"v_mov_b32 v74, 0\ns_add_u32 s47, s40, 1\nv_mov_b32 v79, s47\nds_write_b32 v80, v79\n"
"s_waitcnt vmcnt(0)\nv_max3_u32 v74, v74, v40, v41\nv_max3_u32 v74, v74, v42, v43\nv_cmp_eq_u32 vcc, -1, v74\n"
"v_mov_b32 v44, 0\nv_mov_b32 v45, 0\nv_mov_b32 v46, 0\nv_mov_b32 v47, 0\n"
"v_mov_b32 v48, 0\nv_mov_b32 v49, 0\nv_mov_b32 v50, 0\nv_mov_b32 v51, 0\n"
"s_cbranch_vccnz retry2_x3\ns_branch landed_x3\nadopt_x3:\nbuffer_load_dword v56, v28, s[8:11], 0 offen sc1\n"
"s_lshl_b32 s52, s40, 26\ns_or_b32 s47, s52, 0x3ffffff\ns_waitcnt vmcnt(0)\nv_cmp_ge_u32 vcc, s47, v56\n"
"s_nop 1\nv_cndmask_b32 v79, 0, v56, vcc\ns_nop 1\nv_max_u32_dpp v79, v79, v79 row_ror:8 row_mask:0xf bank_mask:0xf\n"
"s_nop 1\nv_max_u32_dpp v79, v79, v79 row_ror:4 row_mask:0xf bank_mask:0xf\ns_nop 1\nv_max_u32_dpp v79, v79, v79 row_ror:2 row_mask:0xf bank_mask:0xf\n"
"s_nop 1\nv_max_u32_dpp v79, v79, v79 row_ror:1 row_mask:0xf bank_mask:0xf\ns_nop 0\nv_readlane_b32 s47, v79, 0\n"
"v_readlane_b32 s48, v79, 16\ns_max_u32 s48, s48, s47\ns_cmp_le_u32 s48, s52\ns_cbranch_scc1 back_x3\n"
"s_bfe_u32 s52, s48, 1179656\ns_add_u32 s52, s52, s30\ns_sub_u32 s45, s52, s44\ns_and_b32 s45, s45, 3\n"
"s_sub_u32 s44, s52, s45\ns_lshl_b32 s45, s45, 2\ns_and_b32 s46, s48, 255\ns_mov_b32 s92, 0\n"
"s_branch back_x3\nend_x3:\ns_branch fk_done\nfk_p0:\n"
"s_mov_b32 s40, 0\ns_mov_b32 s41, 0\ns_mov_b32 s42, 262144\ns_mov_b32 s43, 393216\n"
"s_mov_b32 s44, s30\ns_mov_b32 s45, 0\ns_bcnt1_i32_b64 s46, s[28:29]\ns_mov_b32 s92, 99\n"
"s_mov_b32 s49, 0\ns_mov_b32 s53, 0\ns_mov_b32 s50, s36\ns_mov_b32 s51, s37\n"
"s_sub_u32 s93, s39, 8\ns_lshl_b32 s47, s93, 1\ns_add_u32 s47, s47, 0\ns_sub_u32 s48, s47, 7\n"
"s_cmp_ge_u32 s47, 7\ns_cselect_b32 s47, s48, s47\ns_lshl_b32 s47, s47, 10\nv_add_u32 v226, s47, v9\n"
"s_lshl_b32 s47, s47, 1\ns_add_u32 s81, s47, s35\ns_lshl_b32 s47, s93, 1\ns_add_u32 s47, s47, 1\n"
"s_sub_u32 s48, s47, 7\ns_cmp_ge_u32 s47, 7\ns_cselect_b32 s47, s48, s47\ns_lshl_b32 s47, s47, 10\n"
"v_add_u32 v227, s47, v9\ns_lshl_b32 s47, s47, 1\ns_add_u32 s82, s47, s35\ns_lshl_b32 s47, s93, 1\n"
"s_add_u32 s47, s47, 2\ns_sub_u32 s48, s47, 7\ns_cmp_ge_u32 s47, 7\ns_cselect_b32 s47, s48, s47\n"
"s_lshl_b32 s47, s47, 10\nv_add_u32 v228, s47, v9\ns_lshl_b32 s47, s47, 1\ns_add_u32 s83, s47, s35\n"
"s_lshl_b32 s47, s93, 1\ns_add_u32 s47, s47, 3\ns_sub_u32 s48, s47, 7\ns_cmp_ge_u32 s47, 7\n"
"s_cselect_b32 s47, s48, s47\ns_lshl_b32 s47, s47, 10\nv_add_u32 v229, s47, v9\ns_lshl_b32 s47, s47, 1\n"
"s_add_u32 s84, s47, s35\ns_lshl_b32 s47, s93, 1\ns_add_u32 s47, s47, 4\ns_sub_u32 s48, s47, 7\n"
"s_cmp_ge_u32 s47, 7\ns_cselect_b32 s47, s48, s47\ns_lshl_b32 s47, s47, 10\nv_add_u32 v230, s47, v9\n"
"s_lshl_b32 s47, s47, 1\ns_add_u32 s85, s47, s35\ns_lshl_b32 s47, s93, 1\ns_add_u32 s47, s47, 5\n"
"s_sub_u32 s48, s47, 7\ns_cmp_ge_u32 s47, 7\ns_cselect_b32 s47, s48, s47\ns_lshl_b32 s47, s47, 10\n"
"v_add_u32 v231, s47, v9\ns_lshl_b32 s47, s47, 1\ns_add_u32 s86, s47, s35\ns_lshl_b32 s47, s93, 1\n"
"s_add_u32 s47, s47, 6\ns_sub_u32 s48, s47, 7\ns_cmp_ge_u32 s47, 7\ns_cselect_b32 s47, s48, s47\n"
"s_lshl_b32 s47, s47, 10\nv_add_u32 v232, s47, v9\ns_lshl_b32 s47, s47, 1\ns_add_u32 s87, s47, s35\n"
"buffer_load_dword v225, v14, s[8:11], 0 offen sc1\ns_add_u32 s42, s35, 8192\ns_cmp_ge_u32 s81, s42\ns_cbranch_scc1 is00_p0\n"
"s_add_u32 s47, s81, 28672\nbuffer_load_dwordx4 a[112:115], v9, s[12:15], s47 offen sc1\nbuffer_load_dwordx4 a[116:119], v9, s[12:15], s47 offen offset:1024 sc1\ns_add_u32 s47, s81, 43008\n"
"buffer_load_dwordx4 a[168:171], v9, s[12:15], s47 offen sc1\nbuffer_load_dwordx4 a[172:175], v9, s[12:15], s47 offen offset:1024 sc1\nis00_p0:\ns_cmp_ge_u32 s82, s42\n"
"s_cbranch_scc1 is01_p0\ns_add_u32 s47, s82, 28672\nbuffer_load_dwordx4 a[120:123], v9, s[12:15], s47 offen sc1\nbuffer_load_dwordx4 a[124:127], v9, s[12:15], s47 offen offset:1024 sc1\n"
"s_add_u32 s47, s82, 43008\nbuffer_load_dwordx4 a[176:179], v9, s[12:15], s47 offen sc1\nbuffer_load_dwordx4 a[180:183], v9, s[12:15], s47 offen offset:1024 sc1\nis01_p0:\n"
"s_cmp_ge_u32 s83, s42\ns_cbranch_scc1 is02_p0\ns_add_u32 s47, s83, 28672\nbuffer_load_dwordx4 a[128:131], v9, s[12:15], s47 offen sc1\n"
"buffer_load_dwordx4 a[132:135], v9, s[12:15], s47 offen offset:1024 sc1\ns_add_u32 s47, s83, 43008\nbuffer_load_dwordx4 a[184:187], v9, s[12:15], s47 offen sc1\nbuffer_load_dwordx4 a[188:191], v9, s[12:15], s47 offen offset:1024 sc1\n"
"is02_p0:\ns_cmp_ge_u32 s84, s42\ns_cbranch_scc1 is03_p0\ns_add_u32 s47, s84, 28672\n"
"buffer_load_dwordx4 a[136:139], v9, s[12:15], s47 offen sc1\nbuffer_load_dwordx4 a[140:143], v9, s[12:15], s47 offen offset:1024 sc1\ns_add_u32 s47, s84, 43008\nbuffer_load_dwordx4 a[192:195], v9, s[12:15], s47 offen sc1\n"
"buffer_load_dwordx4 a[196:199], v9, s[12:15], s47 offen offset:1024 sc1\nis03_p0:\ns_cmp_ge_u32 s85, s42\ns_cbranch_scc1 is04_p0\n"
"s_add_u32 s47, s85, 28672\nbuffer_load_dwordx4 a[144:147], v9, s[12:15], s47 offen sc1\nbuffer_load_dwordx4 a[148:151], v9, s[12:15], s47 offen offset:1024 sc1\ns_add_u32 s47, s85, 43008\n"
"buffer_load_dwordx4 a[200:203], v9, s[12:15], s47 offen sc1\nbuffer_load_dwordx4 a[204:207], v9, s[12:15], s47 offen offset:1024 sc1\nis04_p0:\ns_cmp_ge_u32 s86, s42\n"
"s_cbranch_scc1 is05_p0\ns_add_u32 s47, s86, 28672\nbuffer_load_dwordx4 a[152:155], v9, s[12:15], s47 offen sc1\nbuffer_load_dwordx4 a[156:159], v9, s[12:15], s47 offen offset:1024 sc1\n"
"s_add_u32 s47, s86, 43008\nbuffer_load_dwordx4 a[208:211], v9, s[12:15], s47 offen sc1\nbuffer_load_dwordx4 a[212:215], v9, s[12:15], s47 offen offset:1024 sc1\nis05_p0:\n"
"s_cmp_ge_u32 s87, s42\ns_cbranch_scc1 is06_p0\ns_add_u32 s47, s87, 28672\nbuffer_load_dwordx4 a[160:163], v9, s[12:15], s47 offen sc1\n"
"buffer_load_dwordx4 a[164:167], v9, s[12:15], s47 offen offset:1024 sc1\ns_add_u32 s47, s87, 43008\nbuffer_load_dwordx4 a[216:219], v9, s[12:15], s47 offen sc1\nbuffer_load_dwordx4 a[220:223], v9, s[12:15], s47 offen offset:1024 sc1\n"
"is06_p0:\nv_mov_b32 v222, 24576\ns_mov_b32 s62, 0x00ff00ff\ns_mov_b32 s63, 0x00ff00ff\n"
"v_cndmask_b32 v223, 0, -1, s[62:63]\nv_not_b32 v224, v223\nv_bfe_u32 v219, v9, 4, 1\nv_lshlrev_b32 v220, 4, v219\n"
"v_mov_b32 v218, 0x3c00\nv_lshlrev_b32 v218, v220, v218\nv_bfe_u32 v220, v9, 5, 3\nv_cmp_eq_u32 vcc, 0, v220\n"
"s_nop 1\nv_cndmask_b32 v168, 0, v218, vcc\nv_cmp_eq_u32 vcc, 2, v220\ns_nop 1\n"
"v_cndmask_b32 v169, 0, v218, vcc\nv_cmp_eq_u32 vcc, 4, v220\ns_nop 1\nv_cndmask_b32 v170, 0, v218, vcc\n"
"v_cmp_eq_u32 vcc, 6, v220\ns_nop 1\nv_cndmask_b32 v171, 0, v218, vcc\nv_cmp_eq_u32 vcc, 1, v220\n"
"s_nop 1\nv_cndmask_b32 v172, 0, v218, vcc\nv_cmp_eq_u32 vcc, 3, v220\ns_nop 1\n"
"v_cndmask_b32 v173, 0, v218, vcc\nv_cmp_eq_u32 vcc, 5, v220\ns_nop 1\nv_cndmask_b32 v174, 0, v218, vcc\n"
"v_cmp_eq_u32 vcc, 7, v220\ns_nop 1\nv_cndmask_b32 v175, 0, v218, vcc\nv_mov_b32 v218, 0x3f803f80\n"
"v_cmp_eq_u32 vcc, 0, v219\ns_nop 1\nv_cndmask_b32 v176, 0, v218, vcc\nv_cndmask_b32 v177, 0, v218, vcc\n"
"v_cndmask_b32 v178, 0, v218, vcc\nv_cndmask_b32 v179, 0, v218, vcc\nv_cndmask_b32 v214, 0, v218, vcc\nv_cmp_eq_u32 vcc, 1, v219\n"
"s_nop 1\nv_cndmask_b32 v180, 0, v218, vcc\nv_cndmask_b32 v181, 0, v218, vcc\nv_cndmask_b32 v182, 0, v218, vcc\n"
"v_cndmask_b32 v183, 0, v218, vcc\nv_cndmask_b32 v215, 0, v218, vcc\ns_mul_i32 s47, s93, 3696\ns_getpc_b64 s[60:61]\n"
"dp0_p0:\ns_add_u32 s47, s47, vb0_p0-dp0_p0+0\ns_add_u32 s60, s60, s47\ns_addc_u32 s61, s61, 0\n"
"s_mov_b64 exec, 0\ns_swappc_b64 s[88:89], s[60:61]\ns_mov_b64 exec, -1\ns_waitcnt lgkmcnt(0)\n"
"s_waitcnt vmcnt(16)\nfw_p0:\ns_add_u32 s47, s40, 0\ns_min_u32 s47, s47, 64\n"
"s_add_u32 s47, s47, s34\nv_subrev_u32 v225, s47, v225\nv_cmp_lt_u32 vcc, 64, v225\ns_cbranch_vccz fwok_p0\n"
"s_add_u32 s53, s53, 0\ns_add_u32 s49, s49, 1\ns_cmp_gt_u32 s49, 65536\ns_cbranch_scc1 end_p0\n"
"s_sleep 2\nbuffer_load_dword v225, v14, s[8:11], 0 offen sc1\ns_waitcnt vmcnt(0)\ns_branch fw_p0\n"
"fwok_p0:\ns_cmp_lt_u32 s81, s42\ns_cbranch_scc1 is10_p0\ns_add_u32 s47, s81, 0\n"
"buffer_load_dwordx4 a[0:3], v9, s[12:15], s47 offen sc1\nbuffer_load_dwordx4 a[4:7], v9, s[12:15], s47 offen offset:1024 sc1\ns_add_u32 s47, s81, 14336\nbuffer_load_dwordx4 a[56:59], v9, s[12:15], s47 offen sc1\n"
"buffer_load_dwordx4 a[60:63], v9, s[12:15], s47 offen offset:1024 sc1\nis10_p0:\ns_cmp_lt_u32 s82, s42\ns_cbranch_scc1 is11_p0\n"
"s_add_u32 s47, s82, 0\nbuffer_load_dwordx4 a[8:11], v9, s[12:15], s47 offen sc1\nbuffer_load_dwordx4 a[12:15], v9, s[12:15], s47 offen offset:1024 sc1\ns_add_u32 s47, s82, 14336\n"
"buffer_load_dwordx4 a[64:67], v9, s[12:15], s47 offen sc1\nbuffer_load_dwordx4 a[68:71], v9, s[12:15], s47 offen offset:1024 sc1\nis11_p0:\ns_cmp_lt_u32 s83, s42\n"
"s_cbranch_scc1 is12_p0\ns_add_u32 s47, s83, 0\nbuffer_load_dwordx4 a[16:19], v9, s[12:15], s47 offen sc1\nbuffer_load_dwordx4 a[20:23], v9, s[12:15], s47 offen offset:1024 sc1\n"
"s_add_u32 s47, s83, 14336\nbuffer_load_dwordx4 a[72:75], v9, s[12:15], s47 offen sc1\nbuffer_load_dwordx4 a[76:79], v9, s[12:15], s47 offen offset:1024 sc1\nis12_p0:\n"
"s_cmp_lt_u32 s84, s42\ns_cbranch_scc1 is13_p0\ns_add_u32 s47, s84, 0\nbuffer_load_dwordx4 a[24:27], v9, s[12:15], s47 offen sc1\n"
"buffer_load_dwordx4 a[28:31], v9, s[12:15], s47 offen offset:1024 sc1\ns_add_u32 s47, s84, 14336\nbuffer_load_dwordx4 a[80:83], v9, s[12:15], s47 offen sc1\nbuffer_load_dwordx4 a[84:87], v9, s[12:15], s47 offen offset:1024 sc1\n"
"is13_p0:\ns_cmp_lt_u32 s85, s42\ns_cbranch_scc1 is14_p0\ns_add_u32 s47, s85, 0\n"
"buffer_load_dwordx4 a[32:35], v9, s[12:15], s47 offen sc1\nbuffer_load_dwordx4 a[36:39], v9, s[12:15], s47 offen offset:1024 sc1\ns_add_u32 s47, s85, 14336\nbuffer_load_dwordx4 a[88:91], v9, s[12:15], s47 offen sc1\n"
"buffer_load_dwordx4 a[92:95], v9, s[12:15], s47 offen offset:1024 sc1\nis14_p0:\ns_cmp_lt_u32 s86, s42\ns_cbranch_scc1 is15_p0\n"
"s_add_u32 s47, s86, 0\nbuffer_load_dwordx4 a[40:43], v9, s[12:15], s47 offen sc1\nbuffer_load_dwordx4 a[44:47], v9, s[12:15], s47 offen offset:1024 sc1\ns_add_u32 s47, s86, 14336\n"
"buffer_load_dwordx4 a[96:99], v9, s[12:15], s47 offen sc1\nbuffer_load_dwordx4 a[100:103], v9, s[12:15], s47 offen offset:1024 sc1\nis15_p0:\ns_cmp_lt_u32 s87, s42\n"
"s_cbranch_scc1 is16_p0\ns_add_u32 s47, s87, 0\nbuffer_load_dwordx4 a[48:51], v9, s[12:15], s47 offen sc1\nbuffer_load_dwordx4 a[52:55], v9, s[12:15], s47 offen offset:1024 sc1\n"
"s_add_u32 s47, s87, 14336\nbuffer_load_dwordx4 a[104:107], v9, s[12:15], s47 offen sc1\nbuffer_load_dwordx4 a[108:111], v9, s[12:15], s47 offen offset:1024 sc1\nis16_p0:\n"
"s_mul_i32 s47, s93, 3696\ns_getpc_b64 s[60:61]\ndp1_p0:\ns_cmp_eq_u32 s93, 3\n"
"s_cbranch_scc1 dz_p0\ns_add_u32 s47, s47, vb0_p0-dp1_p0+1848\ns_add_u32 s60, s60, s47\ns_addc_u32 s61, s61, 0\n"
"s_mov_b64 exec, 0\ns_swappc_b64 s[88:89], s[60:61]\ns_mov_b64 exec, -1\ns_waitcnt lgkmcnt(0)\n"
"dz_p0:\ns_cmp_lt_u32 s81, s42\ns_cbranch_scc1 is20_p0\ns_add_u32 s47, s81, 28672\n"
"buffer_load_dwordx4 a[112:115], v9, s[12:15], s47 offen sc1\nbuffer_load_dwordx4 a[116:119], v9, s[12:15], s47 offen offset:1024 sc1\ns_add_u32 s47, s81, 43008\nbuffer_load_dwordx4 a[168:171], v9, s[12:15], s47 offen sc1\n"
"buffer_load_dwordx4 a[172:175], v9, s[12:15], s47 offen offset:1024 sc1\nis20_p0:\ns_cmp_lt_u32 s82, s42\ns_cbranch_scc1 is21_p0\n"
"s_add_u32 s47, s82, 28672\nbuffer_load_dwordx4 a[120:123], v9, s[12:15], s47 offen sc1\nbuffer_load_dwordx4 a[124:127], v9, s[12:15], s47 offen offset:1024 sc1\ns_add_u32 s47, s82, 43008\n"
"buffer_load_dwordx4 a[176:179], v9, s[12:15], s47 offen sc1\nbuffer_load_dwordx4 a[180:183], v9, s[12:15], s47 offen offset:1024 sc1\nis21_p0:\ns_cmp_lt_u32 s83, s42\n"
"s_cbranch_scc1 is22_p0\ns_add_u32 s47, s83, 28672\nbuffer_load_dwordx4 a[128:131], v9, s[12:15], s47 offen sc1\nbuffer_load_dwordx4 a[132:135], v9, s[12:15], s47 offen offset:1024 sc1\n"
"s_add_u32 s47, s83, 43008\nbuffer_load_dwordx4 a[184:187], v9, s[12:15], s47 offen sc1\nbuffer_load_dwordx4 a[188:191], v9, s[12:15], s47 offen offset:1024 sc1\nis22_p0:\n"
"s_cmp_lt_u32 s84, s42\ns_cbranch_scc1 is23_p0\ns_add_u32 s47, s84, 28672\nbuffer_load_dwordx4 a[136:139], v9, s[12:15], s47 offen sc1\n"
"buffer_load_dwordx4 a[140:143], v9, s[12:15], s47 offen offset:1024 sc1\ns_add_u32 s47, s84, 43008\nbuffer_load_dwordx4 a[192:195], v9, s[12:15], s47 offen sc1\nbuffer_load_dwordx4 a[196:199], v9, s[12:15], s47 offen offset:1024 sc1\n"
"is23_p0:\ns_cmp_lt_u32 s85, s42\ns_cbranch_scc1 is24_p0\ns_add_u32 s47, s85, 28672\n"
"buffer_load_dwordx4 a[144:147], v9, s[12:15], s47 offen sc1\nbuffer_load_dwordx4 a[148:151], v9, s[12:15], s47 offen offset:1024 sc1\ns_add_u32 s47, s85, 43008\nbuffer_load_dwordx4 a[200:203], v9, s[12:15], s47 offen sc1\n"
"buffer_load_dwordx4 a[204:207], v9, s[12:15], s47 offen offset:1024 sc1\nis24_p0:\ns_cmp_lt_u32 s86, s42\ns_cbranch_scc1 is25_p0\n"
"s_add_u32 s47, s86, 28672\nbuffer_load_dwordx4 a[152:155], v9, s[12:15], s47 offen sc1\nbuffer_load_dwordx4 a[156:159], v9, s[12:15], s47 offen offset:1024 sc1\ns_add_u32 s47, s86, 43008\n"
"buffer_load_dwordx4 a[208:211], v9, s[12:15], s47 offen sc1\nbuffer_load_dwordx4 a[212:215], v9, s[12:15], s47 offen offset:1024 sc1\nis25_p0:\ns_cmp_lt_u32 s87, s42\n"
"s_cbranch_scc1 is26_p0\ns_add_u32 s47, s87, 28672\nbuffer_load_dwordx4 a[160:163], v9, s[12:15], s47 offen sc1\nbuffer_load_dwordx4 a[164:167], v9, s[12:15], s47 offen offset:1024 sc1\n"
"s_add_u32 s47, s87, 43008\nbuffer_load_dwordx4 a[216:219], v9, s[12:15], s47 offen sc1\nbuffer_load_dwordx4 a[220:223], v9, s[12:15], s47 offen offset:1024 sc1\nis26_p0:\n"
"v_readfirstlane_b32 s93, v13\nv_lshlrev_b32 v166, 2, v9\ns_mov_b32 s52, 1064960\nbuffer_load_dword v164, v166, s[8:11], s52 offen sc1\n"
"s_add_u32 s52, s52, 4096\nbuffer_load_dword v165, v166, s[8:11], s52 offen sc1\nbuffer_load_dword v218, v11, s[8:11], 0 offen sc1\ns_mov_b64 s[94:95], 1\n"
"s_barrier\ns_waitcnt vmcnt(0)\nar_p0:\nv_xor_b32 v164, s34, v164\n"
"v_xor_b32 v165, s34, v165\nv_or_b32 v164, v164, v165\nv_cmp_ne_u32 vcc, 0, v164\ns_cbranch_vccz arok_p0\n"
"s_add_u32 s53, s53, 0\ns_add_u32 s49, s49, 1\ns_cmp_gt_u32 s49, 65536\ns_cbranch_scc1 end_p0\n"
"s_sleep 2\nv_lshlrev_b32 v166, 2, v9\ns_mov_b32 s52, 1064960\nbuffer_load_dword v164, v166, s[8:11], s52 offen sc1\n"
"s_add_u32 s52, s52, 4096\nbuffer_load_dword v165, v166, s[8:11], s52 offen sc1\ns_waitcnt vmcnt(0)\ns_branch ar_p0\n"
"arok_p0:\nfl_p0:\ns_add_u32 s47, s40, 2\ns_min_u32 s47, s47, 64\n"
"s_add_u32 s47, s47, s34\nv_subrev_u32 v218, s47, v218\nv_cmp_lt_u32 vcc, 64, v218\ns_cbranch_vccz flok_p0\n"
"s_add_u32 s53, s53, 0\ns_add_u32 s49, s49, 1\ns_cmp_gt_u32 s49, 65536\ns_cbranch_scc1 end_p0\n"
"s_sleep 2\nbuffer_load_dword v218, v11, s[8:11], 0 offen sc1\ns_waitcnt vmcnt(0)\ns_branch fl_p0\n"
"flok_p0:\nbuffer_load_dwordx4 v[100:103], v9, s[16:19], s50 offen offset:0 sc1\nbuffer_load_dwordx4 v[104:107], v9, s[16:19], s50 offen offset:1024 sc1\nbuffer_load_dwordx4 v[108:111], v9, s[16:19], s50 offen offset:2048 sc1\n"
"buffer_load_dwordx4 v[112:115], v9, s[16:19], s50 offen offset:3072 sc1\ns_add_u32 s50, s50, 16384\ns_memrealtime s[54:55]\ns_waitcnt lgkmcnt(0)\n"
"s_add_u32 s44, s54, 12\ns_mov_b32 s45, 8\ns_mov_b32 s42, 1\ns_mul_i32 s48, s45, 6\n"
"s_and_b32 s48, s48, -4\ns_getpc_b64 s[88:89]\nmpc_p0:\ns_sub_u32 s48, mgo_p0-mpc_p0, s48\n"
"s_add_u32 s88, s88, s48\ns_addc_u32 s89, s89, 0\ns_branch mf_p0\n.p2align 6\n"
"top_p0:\nmf_p0:\ns_memrealtime s[54:55]\ns_nop 2\n"
"s_memrealtime s[56:57]\ns_nop 2\ns_memrealtime s[58:59]\nmg_p0:\n"
"s_waitcnt lgkmcnt(4)\ns_sub_u32 s47, s54, s44\ns_add_u32 s48, s47, 12\ns_cmp_ge_u32 s48, 12\n"
"s_cbranch_scc1 mo_p0\ns_memrealtime s[54:55]\ns_waitcnt lgkmcnt(4)\ns_cmp_ge_u32 s56, s44\n"
"s_cbranch_scc1 mx_p0\ns_memrealtime s[56:57]\ns_waitcnt lgkmcnt(4)\ns_cmp_ge_u32 s58, s44\n"
"s_cbranch_scc1 mx_p0\ns_memrealtime s[58:59]\ns_waitcnt lgkmcnt(4)\ns_cmp_ge_u32 s54, s44\n"
"s_cbranch_scc1 mx_p0\ns_memrealtime s[54:55]\ns_waitcnt lgkmcnt(4)\ns_cmp_ge_u32 s56, s44\n"
"s_cbranch_scc1 mx_p0\ns_memrealtime s[56:57]\ns_waitcnt lgkmcnt(4)\ns_cmp_ge_u32 s58, s44\n"
"s_cbranch_scc1 mx_p0\ns_memrealtime s[58:59]\ns_waitcnt lgkmcnt(4)\ns_cmp_ge_u32 s54, s44\n"
"s_cbranch_scc1 mx_p0\ns_memrealtime s[54:55]\ns_waitcnt lgkmcnt(4)\ns_cmp_ge_u32 s56, s44\n"
"s_cbranch_scc1 mx_p0\ns_memrealtime s[56:57]\ns_waitcnt lgkmcnt(4)\ns_cmp_ge_u32 s58, s44\n"
"s_cbranch_scc1 mx_p0\ns_memrealtime s[58:59]\ns_waitcnt lgkmcnt(4)\ns_cmp_ge_u32 s54, s44\n"
"s_cbranch_scc1 mx_p0\ns_memrealtime s[54:55]\ns_waitcnt lgkmcnt(4)\ns_cmp_ge_u32 s56, s44\n"
"s_cbranch_scc1 mx_p0\ns_memrealtime s[56:57]\ns_waitcnt lgkmcnt(4)\ns_cmp_ge_u32 s58, s44\n"
"s_cbranch_scc1 mx_p0\ns_memrealtime s[58:59]\ns_waitcnt lgkmcnt(4)\ns_cmp_ge_u32 s54, s44\n"
"s_cbranch_scc1 mx_p0\ns_memrealtime s[54:55]\ns_waitcnt lgkmcnt(4)\ns_cmp_ge_u32 s56, s44\n"
"s_cbranch_scc1 mx_p0\ns_memrealtime s[56:57]\ns_waitcnt lgkmcnt(4)\ns_cmp_ge_u32 s58, s44\n"
"s_cbranch_scc1 mx_p0\ns_memrealtime s[58:59]\ns_waitcnt lgkmcnt(4)\ns_cmp_ge_u32 s54, s44\n"
"s_cbranch_scc1 mx_p0\ns_memrealtime s[54:55]\ns_waitcnt lgkmcnt(4)\ns_cmp_ge_u32 s56, s44\n"
"s_cbranch_scc1 mx_p0\ns_memrealtime s[56:57]\ns_branch mx_p0\nmo_p0:\n"
"s_waitcnt lgkmcnt(0)\ns_cmp_lt_i32 s47, 0\ns_cbranch_scc1 mf_p0\ns_branch mgo_p0\n"
"mx_p0:\ns_setpc_b64 s[88:89]\ns_nop 0\ns_nop 0\n"
"s_nop 0\ns_nop 0\ns_nop 0\ns_nop 0\n"
"s_nop 0\ns_nop 0\ns_nop 0\ns_nop 0\n"
"s_nop 0\ns_nop 0\ns_nop 0\ns_nop 0\n"
"s_nop 0\ns_nop 0\ns_nop 0\ns_nop 0\n"
"s_nop 0\ns_nop 0\ns_nop 0\ns_nop 0\n"
"s_nop 0\ns_nop 0\nmgo_p0:\ns_nop 15\n"
"s_nop 7\ntop2_p0:\nbuffer_load_dwordx4 v[40:43], v10, s[8:11], s43 offen sc1\nbuffer_load_dwordx4 v[44:47], v12, s[8:11], s43 offen sc1\n"
"v_mov_b32 v68, 0\nv_mov_b32 v69, 0\nv_mov_b32 v70, 0\nv_mov_b32 v71, 0\n"
"v_mov_b32 v72, 0\nv_mov_b32 v73, 0\nv_mov_b32 v74, 0\nv_mov_b32 v75, 0\n"
"v_mov_b32 v76, 0\nv_mov_b32 v77, 0\nv_mov_b32 v78, 0\nv_mov_b32 v79, 0\n"
"v_mov_b32 v80, 0\nv_mov_b32 v81, 0\nv_mov_b32 v82, 0\nv_mov_b32 v83, 0\n"
"v_mov_b32 v218, 0\ns_cmp_eq_u32 s42, 0\ns_cbranch_scc1 np_p0\ns_mov_b32 s42, 0\n"
"s_and_b32 s47, s46, 15\ns_mul_i32 s47, s47, 4\ns_add_u32 s47, s47, s31\ns_add_u32 s47, s47, s45\n"
"s_and_b32 s91, s47, 15\ns_lshr_b32 s47, s47, 4\ns_lshl_b32 s47, s47, 2\ns_add_u32 s90, s44, s47\n"
"s_mul_i32 s47, s91, 6\ns_add_u32 s47, s47, s93\ns_lshr_b32 s47, s47, 4\ns_mul_i32 s47, s47, 300\n"
"s_getpc_b64 s[60:61]\nep1_p0:\ns_add_u32 s47, s47, sgo_p0-ep1_p0\ns_add_u32 s60, s60, s47\n"
"s_addc_u32 s61, s61, 0\ns_mul_i32 s48, s46, 8\ns_add_u32 s45, s45, s48\ns_add_u32 s45, s45, s32\n"
"s_lshr_b32 s48, s45, 4\ns_lshl_b32 s48, s48, 2\ns_add_u32 s44, s44, s48\ns_and_b32 s45, s45, 15\n"
"s_add_u32 s52, s40, 1\ns_bitcmp1_b64 s[28:29], s52\ns_subb_u32 s46, s46, 0\ns_mul_i32 s48, s45, 6\n"
"s_and_b32 s48, s48, -4\ns_getpc_b64 s[88:89]\nmpc1_p0:\ns_sub_u32 s48, mgo_p0-mpc1_p0, s48\n"
"s_add_u32 s88, s88, s48\ns_addc_u32 s89, s89, -1\nnp_p0:\ns_waitcnt vmcnt(1)\n"
"ds_write_b128 v226, v[40:43]\nv_max3_u32 v218, v218, v40, v41\nv_max3_u32 v218, v218, v42, v43\ns_waitcnt vmcnt(0)\n"
"ds_write_b128 v227, v[44:47]\nv_max3_u32 v218, v218, v44, v45\nv_max3_u32 v218, v218, v46, v47\nv_cmp_eq_u32 vcc, -1, v218\n"
"v_smfmac_f32_16x16x64_bf16 v[68:71], v[40:43], a[0:7], v8\nv_mov_b32 v84, v100\nv_mov_b32 v85, v101\nv_mov_b32 v86, v102\n"
"v_mov_b32 v87, v103\nv_mov_b32 v88, v104\nv_smfmac_f32_16x16x64_bf16 v[72:75], v[40:43], a[56:63], v8\nv_mov_b32 v89, v105\n"
"v_mov_b32 v90, v106\nv_mov_b32 v91, v107\nv_mov_b32 v92, v108\nv_mov_b32 v93, v109\n"
"s_cbranch_vccnz retry1_p0\ns_waitcnt lgkmcnt(0)\ns_barrier\nds_read_b32 v221, v222\n"
"ds_read_b128 v[48:51], v228\nds_read_b128 v[52:55], v229\nds_read_b128 v[56:59], v230\nds_read_b128 v[60:63], v231\n"
"ds_read_b128 v[64:67], v232\nv_smfmac_f32_16x16x64_bf16 v[76:79], v[40:43], a[112:119], v8\nv_mov_b32 v94, v110\nv_smfmac_f32_16x16x64_bf16 v[80:83], v[40:43], a[168:175], v8\n"
"v_mov_b32 v95, v111\nv_smfmac_f32_16x16x64_bf16 v[68:71], v[44:47], a[8:15], v8\nv_mov_b32 v96, v112\nv_smfmac_f32_16x16x64_bf16 v[72:75], v[44:47], a[64:71], v8\n"
"v_mov_b32 v97, v113\ns_waitcnt lgkmcnt(0)\nv_readfirstlane_b32 s47, v221\ns_memrealtime s[54:55]\n"
"v_smfmac_f32_16x16x64_bf16 v[76:79], v[44:47], a[120:127], v8\nv_mov_b32 v98, v114\ns_memrealtime s[56:57]\nv_smfmac_f32_16x16x64_bf16 v[80:83], v[44:47], a[176:183], v8\n"
"v_mov_b32 v99, v115\ns_cmp_eq_u32 s40, 63\ns_cbranch_scc1 last_p0\ns_memrealtime s[58:59]\n"
"s_sub_u32 s47, s47, s40\ns_cmp_eq_u32 s47, 1\ns_cbranch_scc1 adopt_p0\ns_waitcnt lgkmcnt(4)\n"
"s_sub_u32 s47, s54, s90\ns_cmp_ge_i32 s47, 0\ns_cbranch_scc1 so_p0\ns_memrealtime s[54:55]\n"
"gr_p0:\nv_smfmac_f32_16x16x64_bf16 v[68:71], v[48:51], a[16:23], v8\ns_waitcnt lgkmcnt(4)\ns_cmp_ge_u32 s56, s90\n"
"s_cbranch_scc1 sx1_p0\ns_memrealtime s[56:57]\nv_smfmac_f32_16x16x64_bf16 v[72:75], v[48:51], a[72:79], v8\ns_waitcnt lgkmcnt(4)\n"
"s_cmp_ge_u32 s58, s90\ns_cbranch_scc1 sx2_p0\ns_memrealtime s[58:59]\nv_smfmac_f32_16x16x64_bf16 v[76:79], v[48:51], a[128:135], v8\n"
"s_waitcnt lgkmcnt(4)\ns_cmp_ge_u32 s54, s90\ns_cbranch_scc1 sx3_p0\ns_memrealtime s[54:55]\n"
"v_smfmac_f32_16x16x64_bf16 v[80:83], v[48:51], a[184:191], v8\ns_waitcnt lgkmcnt(4)\ns_cmp_ge_u32 s56, s90\ns_cbranch_scc1 sx4_p0\n"
"s_memrealtime s[56:57]\nv_smfmac_f32_16x16x64_bf16 v[68:71], v[52:55], a[24:31], v8\ns_waitcnt lgkmcnt(4)\ns_cmp_ge_u32 s58, s90\n"
"s_cbranch_scc1 sx5_p0\ns_memrealtime s[58:59]\nv_smfmac_f32_16x16x64_bf16 v[72:75], v[52:55], a[80:87], v8\ns_waitcnt lgkmcnt(4)\n"
"s_cmp_ge_u32 s54, s90\ns_cbranch_scc1 sx6_p0\ns_memrealtime s[54:55]\nv_smfmac_f32_16x16x64_bf16 v[76:79], v[52:55], a[136:143], v8\n"
"s_waitcnt lgkmcnt(4)\ns_cmp_ge_u32 s56, s90\ns_cbranch_scc1 sx7_p0\ns_memrealtime s[56:57]\n"
"v_smfmac_f32_16x16x64_bf16 v[80:83], v[52:55], a[192:199], v8\ns_waitcnt lgkmcnt(4)\ns_cmp_ge_u32 s58, s90\ns_cbranch_scc1 sx8_p0\n"
"s_memrealtime s[58:59]\nv_smfmac_f32_16x16x64_bf16 v[68:71], v[56:59], a[32:39], v8\ns_waitcnt lgkmcnt(4)\ns_cmp_ge_u32 s54, s90\n"
"s_cbranch_scc1 sx9_p0\ns_memrealtime s[54:55]\nv_smfmac_f32_16x16x64_bf16 v[72:75], v[56:59], a[88:95], v8\ns_waitcnt lgkmcnt(4)\n"
"s_cmp_ge_u32 s56, s90\ns_cbranch_scc1 sx10_p0\ns_memrealtime s[56:57]\nv_smfmac_f32_16x16x64_bf16 v[76:79], v[56:59], a[144:151], v8\n"
"s_waitcnt lgkmcnt(4)\ns_cmp_ge_u32 s58, s90\ns_cbranch_scc1 sx11_p0\ns_memrealtime s[58:59]\n"
"v_smfmac_f32_16x16x64_bf16 v[80:83], v[56:59], a[200:207], v8\ns_waitcnt lgkmcnt(4)\ns_cmp_ge_u32 s54, s90\ns_cbranch_scc1 sx12_p0\n"
"s_memrealtime s[54:55]\nv_smfmac_f32_16x16x64_bf16 v[68:71], v[60:63], a[40:47], v8\ns_waitcnt lgkmcnt(4)\ns_cmp_ge_u32 s56, s90\n"
"s_cbranch_scc1 sx13_p0\ns_memrealtime s[56:57]\nv_smfmac_f32_16x16x64_bf16 v[72:75], v[60:63], a[96:103], v8\ns_waitcnt lgkmcnt(4)\n"
"s_cmp_ge_u32 s58, s90\ns_cbranch_scc1 sx14_p0\ns_memrealtime s[58:59]\nv_smfmac_f32_16x16x64_bf16 v[76:79], v[60:63], a[152:159], v8\n"
"s_waitcnt lgkmcnt(4)\ns_cmp_ge_u32 s54, s90\ns_cbranch_scc1 sx15_p0\ns_memrealtime s[54:55]\n"
"v_smfmac_f32_16x16x64_bf16 v[80:83], v[60:63], a[208:215], v8\ns_waitcnt lgkmcnt(4)\ns_cmp_ge_u32 s56, s90\ns_cbranch_scc1 sx16_p0\n"
"s_memrealtime s[56:57]\nv_smfmac_f32_16x16x64_bf16 v[68:71], v[64:67], a[48:55], v8\ns_waitcnt lgkmcnt(4)\ns_cmp_ge_u32 s58, s90\n"
"s_cbranch_scc1 sx17_p0\ns_memrealtime s[58:59]\nv_smfmac_f32_16x16x64_bf16 v[72:75], v[64:67], a[104:111], v8\ns_waitcnt lgkmcnt(4)\n"
"s_cmp_ge_u32 s54, s90\ns_cbranch_scc1 sx18_p0\ns_memrealtime s[54:55]\nv_smfmac_f32_16x16x64_bf16 v[76:79], v[64:67], a[160:167], v8\n"
"s_waitcnt lgkmcnt(4)\ns_cmp_ge_u32 s56, s90\ns_cbranch_scc1 sx19_p0\ns_memrealtime s[56:57]\n"
"v_smfmac_f32_16x16x64_bf16 v[80:83], v[64:67], a[216:223], v8\ns_waitcnt lgkmcnt(4)\ns_cmp_ge_u32 s58, s90\ns_cbranch_scc1 sx20_p0\n"
"s_memrealtime s[58:59]\nsp_p0:\ns_waitcnt lgkmcnt(4)\ns_sub_u32 s47, s54, s90\n"
"s_cmp_ge_i32 s47, 0\ns_cbranch_scc1 sx20_p0\ns_memrealtime s[54:55]\ns_waitcnt lgkmcnt(4)\n"
"s_sub_u32 s47, s56, s90\ns_cmp_ge_i32 s47, 0\ns_cbranch_scc1 sx20_p0\ns_memrealtime s[56:57]\n"
"s_waitcnt lgkmcnt(4)\ns_sub_u32 s47, s58, s90\ns_cmp_ge_i32 s47, 0\ns_cbranch_scc1 sx20_p0\n"
"s_memrealtime s[58:59]\ns_waitcnt lgkmcnt(4)\ns_sub_u32 s47, s54, s90\ns_cmp_ge_i32 s47, 0\n"
"s_cbranch_scc1 sx20_p0\ns_memrealtime s[54:55]\ns_waitcnt lgkmcnt(4)\ns_sub_u32 s47, s56, s90\n"
"s_cmp_ge_i32 s47, 0\ns_cbranch_scc1 sx20_p0\ns_memrealtime s[56:57]\ns_waitcnt lgkmcnt(4)\n"
"s_sub_u32 s47, s58, s90\ns_cmp_ge_i32 s47, 0\ns_cbranch_scc1 sx20_p0\ns_memrealtime s[58:59]\n"
"s_cbranch_execnz sp_p0\ns_branch tl10_p0\nso_p0:\ns_cmp_gt_i32 s47, 4\n"
"s_cbranch_scc1 late_p0\ns_branch sgo_p0\ntl10_p0:\nv_smfmac_f32_16x16x64_bf16 v[76:79], v[56:59], a[144:151], v8\n"
"tl11_p0:\nv_smfmac_f32_16x16x64_bf16 v[80:83], v[56:59], a[200:207], v8\ntl12_p0:\nv_smfmac_f32_16x16x64_bf16 v[68:71], v[60:63], a[40:47], v8\n"
"tl13_p0:\nv_smfmac_f32_16x16x64_bf16 v[72:75], v[60:63], a[96:103], v8\ntl14_p0:\nv_smfmac_f32_16x16x64_bf16 v[76:79], v[60:63], a[152:159], v8\n"
"tl15_p0:\nv_smfmac_f32_16x16x64_bf16 v[80:83], v[60:63], a[208:215], v8\ntl16_p0:\nv_smfmac_f32_16x16x64_bf16 v[68:71], v[64:67], a[48:55], v8\n"
"tl17_p0:\nv_smfmac_f32_16x16x64_bf16 v[72:75], v[64:67], a[104:111], v8\ntl18_p0:\nv_smfmac_f32_16x16x64_bf16 v[76:79], v[64:67], a[160:167], v8\n"
"tl19_p0:\nv_smfmac_f32_16x16x64_bf16 v[80:83], v[64:67], a[216:223], v8\ntl20_p0:\nv_add_f32 v219, v68, v69\n"
"v_add_f32 v220, v70, v71\nv_cvt_pk_bf16_f32 v156, v219, v220\ntl21_p0:\nv_add_f32 v219, v72, v73\n"
"v_add_f32 v220, v74, v75\nv_cvt_pk_bf16_f32 v157, v219, v220\ntl22_p0:\nv_add_f32 v219, v76, v77\n"
"v_add_f32 v220, v78, v79\nv_cvt_pk_bf16_f32 v158, v219, v220\ntl23_p0:\nv_add_f32 v219, v80, v81\n"
"v_add_f32 v220, v82, v83\nv_cvt_pk_bf16_f32 v159, v219, v220\ntl24_p0:\ns_cbranch_execz warm_p0\n"
"buffer_load_dwordx4 v[100:103], v9, s[16:19], s50 offen offset:0 sc1\nbuffer_load_dwordx4 v[104:107], v9, s[16:19], s50 offen offset:1024 sc1\nbuffer_load_dwordx4 v[108:111], v9, s[16:19], s50 offen offset:2048 sc1\nbuffer_load_dwordx4 v[112:115], v9, s[16:19], s50 offen offset:3072 sc1\n"
"s_add_u32 s50, s50, 16384\nbuffer_store_dwordx4 v[156:159], v9, s[20:23], s51 offen sc1\ns_waitcnt vmcnt(8)\nv_mfma_f32_16x16x32_bf16 v[132:135], v[84:87], v[116:119], 0\n"
"v_mfma_f32_16x16x32_bf16 v[136:139], v[88:91], v[116:119], 0\nv_mfma_f32_16x16x32_bf16 v[148:151], v[176:179], v[116:119], 0\ns_waitcnt vmcnt(7)\nv_mfma_f32_16x16x32_bf16 v[140:143], v[92:95], v[120:123], 0\n"
"v_mfma_f32_16x16x32_bf16 v[144:147], v[96:99], v[120:123], 0\nv_mfma_f32_16x16x32_bf16 v[148:151], v[180:183], v[120:123], v[148:151]\nv_cndmask_b32 v132, v136, v132, s[62:63]\nv_cndmask_b32 v133, v137, v133, s[62:63]\n"
"v_cndmask_b32 v134, v138, v134, s[62:63]\nv_cndmask_b32 v135, v139, v135, s[62:63]\ns_waitcnt vmcnt(6)\nv_mfma_f32_16x16x32_f16 v[152:155], v[168:171], v[124:127], 0\n"
"s_waitcnt vmcnt(5)\nv_mfma_f32_16x16x32_f16 v[152:155], v[172:175], v[128:131], v[152:155]\nv_cndmask_b32 v140, v144, v140, s[62:63]\nv_cndmask_b32 v141, v145, v141, s[62:63]\n"
"v_cndmask_b32 v142, v146, v142, s[62:63]\nv_cndmask_b32 v143, v147, v143, s[62:63]\ns_nop 2\nv_exp_f32 v152, v152\n"
"v_exp_f32 v153, v153\nv_exp_f32 v154, v154\nv_exp_f32 v155, v155\nv_cvt_pk_bf16_f32 v200, v152, v153\n"
"v_cvt_pk_bf16_f32 v201, v154, v155\nv_and_b32 v160, v200, v223\nv_and_b32 v161, v200, v224\nv_mfma_f32_16x16x16_bf16 v[148:151], v[214:215], v[200:201], v[148:151]\n"
"v_and_b32 v162, v201, v223\nv_and_b32 v163, v201, v224\nv_mfma_f32_16x16x16_bf16 v[132:135], v[156:157], v[160:161], v[132:135]\ns_nop 0\n"
"v_mfma_f32_16x16x16_bf16 v[140:143], v[158:159], v[162:163], v[140:143]\nv_rcp_f32 v202, v148\nv_rcp_f32 v203, v149\nv_mul_f32 v132, v132, v202\n"
"v_mul_f32 v133, v133, v202\nv_mul_f32 v134, v134, v202\nv_mul_f32 v135, v135, v202\nv_fmac_f32 v132, v140, v203\n"
"v_fmac_f32 v133, v141, v203\nv_fmac_f32 v134, v142, v203\nv_fmac_f32 v135, v143, v203\nv_add_f32_dpp v204, v132, v132 row_ror:8 row_mask:0xf bank_mask:0x3\n"
"v_add_f32_dpp v205, v133, v133 row_ror:8 row_mask:0xf bank_mask:0x3\nv_add_f32_dpp v204, v134, v134 row_ror:8 row_mask:0xf bank_mask:0xc\nv_add_f32_dpp v205, v135, v135 row_ror:8 row_mask:0xf bank_mask:0xc\nv_subrev_u32 v219, 0x23800000, v148\n"
"v_subrev_u32 v220, 0x23800000, v149\nv_max_u32 v219, v219, v220\nv_cmp_le_u32 vcc, 0x38000000, v219\nds_write_b64 v22, v[204:205]\n"
"s_or_b64 s[56:57], vcc, s[94:95]\ns_cbranch_scc1 slow_p0\njoin_p0:\nv_mov_b32 v219, 0\n"
"s_nop 1\nv_mov_b32 v219, 0\ns_waitcnt lgkmcnt(0)\ns_barrier\n"
"ds_read_b64 v[206:207], v23 offset:0\nds_read_b64 v[208:209], v23 offset:1280\nds_read_b64 v[210:211], v23 offset:2560\nds_read_b64 v[212:213], v23 offset:3840\n"
"ds_read_b32 v221, v222\nv_mov_b32 v219, 0\ns_add_u32 s40, s40, 1\ns_mov_b32 s43, s41\n"
"s_add_u32 s41, s41, 131072\nv_mov_b32 v219, 0\ns_and_b32 s41, s41, 524287\ns_add_u32 s92, s92, 1\n"
"s_add_u32 s51, s51, 4096\nv_mov_b32 v219, 0\ns_mov_b32 s42, 1\ns_waitcnt lgkmcnt(3)\n"
"v_pk_add_f32 v[206:207], v[206:207], v[208:209]\ns_waitcnt lgkmcnt(1)\nv_pk_add_f32 v[210:211], v[210:211], v[212:213]\nv_pk_add_f32 v[206:207], v[206:207], v[210:211]\n"
"v_cvt_pk_bf16_f32 v220, v206, v207\nbuffer_store_dword v220, v16, s[8:11], s43 offen sc1\ns_waitcnt lgkmcnt(0)\ns_memrealtime s[54:55]\n"
"v_readfirstlane_b32 s47, v221\ns_nop 1\ns_memrealtime s[56:57]\ns_sub_u32 s47, s47, s40\n"
"s_cmp_lg_u32 s47, 1\ns_memrealtime s[58:59]\ns_cbranch_scc1 mg_p0\ns_lshl_b32 s52, s40, 26\n"
"s_cmp_le_u32 s92, 2\ns_cselect_b32 s48, s33, s38\ns_waitcnt lgkmcnt(0)\ns_sub_u32 s47, s54, s30\n"
"s_add_u32 s47, s47, s48\ns_min_u32 s47, s47, 0x3ffff\ns_lshl_b32 s47, s47, 8\ns_or_b32 s47, s47, s52\n"
"s_sub_u32 s52, s92, 2\ns_cmp_lt_u32 s52, 6\ns_cselect_b32 s48, 1, 0\ns_cmp_gt_u32 s40, 12\n"
"s_cselect_b32 s48, s48, 0\ns_add_u32 s48, s48, s46\ns_min_u32 s48, s48, 15\ns_or_b32 s47, s47, s48\n"
"v_mov_b32 v225, s47\nbuffer_store_dword v225, v19, s[8:11], 0 offen sc1\ns_cmp_lg_u32 s40, 1\ns_cbranch_scc1 rec_p0\n"
"buffer_load_dword v218, v11, s[8:11], 0 offen sc1\ns_waitcnt vmcnt(0)\nf1_p0:\ns_add_u32 s47, s40, 2\n"
"s_min_u32 s47, s47, 64\ns_add_u32 s47, s47, s34\nv_subrev_u32 v218, s47, v218\nv_cmp_lt_u32 vcc, 64, v218\n"
"s_cbranch_vccz f1ok_p0\ns_add_u32 s53, s53, 0\ns_add_u32 s49, s49, 1\ns_cmp_gt_u32 s49, 65536\n"
"s_cbranch_scc1 end_p0\ns_sleep 2\nbuffer_load_dword v218, v11, s[8:11], 0 offen sc1\ns_waitcnt vmcnt(0)\n"
"s_branch f1_p0\nf1ok_p0:\ns_branch top2_p0\nlast_p0:\n"
"v_smfmac_f32_16x16x64_bf16 v[68:71], v[48:51], a[16:23], v8\nv_smfmac_f32_16x16x64_bf16 v[72:75], v[48:51], a[72:79], v8\nv_smfmac_f32_16x16x64_bf16 v[76:79], v[48:51], a[128:135], v8\nv_smfmac_f32_16x16x64_bf16 v[80:83], v[48:51], a[184:191], v8\n"
"v_smfmac_f32_16x16x64_bf16 v[68:71], v[52:55], a[24:31], v8\nv_smfmac_f32_16x16x64_bf16 v[72:75], v[52:55], a[80:87], v8\nv_smfmac_f32_16x16x64_bf16 v[76:79], v[52:55], a[136:143], v8\nv_smfmac_f32_16x16x64_bf16 v[80:83], v[52:55], a[192:199], v8\n"
"v_smfmac_f32_16x16x64_bf16 v[68:71], v[56:59], a[32:39], v8\nv_smfmac_f32_16x16x64_bf16 v[72:75], v[56:59], a[88:95], v8\nv_smfmac_f32_16x16x64_bf16 v[76:79], v[56:59], a[144:151], v8\nv_smfmac_f32_16x16x64_bf16 v[80:83], v[56:59], a[200:207], v8\n"
"v_smfmac_f32_16x16x64_bf16 v[68:71], v[60:63], a[40:47], v8\nv_smfmac_f32_16x16x64_bf16 v[72:75], v[60:63], a[96:103], v8\nv_smfmac_f32_16x16x64_bf16 v[76:79], v[60:63], a[152:159], v8\nv_smfmac_f32_16x16x64_bf16 v[80:83], v[60:63], a[208:215], v8\n"
"v_smfmac_f32_16x16x64_bf16 v[68:71], v[64:67], a[48:55], v8\nv_smfmac_f32_16x16x64_bf16 v[72:75], v[64:67], a[104:111], v8\nv_smfmac_f32_16x16x64_bf16 v[76:79], v[64:67], a[160:167], v8\nv_smfmac_f32_16x16x64_bf16 v[80:83], v[64:67], a[216:223], v8\n"
"v_add_f32 v219, v68, v69\nv_add_f32 v220, v70, v71\nv_cvt_pk_bf16_f32 v156, v219, v220\nv_add_f32 v219, v72, v73\n"
"v_add_f32 v220, v74, v75\nv_cvt_pk_bf16_f32 v157, v219, v220\nv_add_f32 v219, v76, v77\nv_add_f32 v220, v78, v79\n"
"v_cvt_pk_bf16_f32 v158, v219, v220\nv_add_f32 v219, v80, v81\nv_add_f32 v220, v82, v83\nv_cvt_pk_bf16_f32 v159, v219, v220\n"
"buffer_store_dwordx4 v[156:159], v9, s[20:23], s51 offen sc1\ns_add_u32 s47, s34, 64\nv_mov_b32 v219, s47\ns_waitcnt vmcnt(0)\n"
"buffer_store_dword v219, v18, s[8:11], 0 offen sc1\nv_mov_b32 v220, s34\nbuffer_store_dword v220, v15, s[8:11], 0 offen sc1\ns_branch end_p0\n"
"retry1_p0:\ns_add_u32 s53, s53, 256\ns_add_u32 s49, s49, 1\ns_cmp_gt_u32 s49, 65536\n"
"s_cbranch_scc1 end_p0\ns_add_u32 s47, s40, 1\nv_mov_b32 v221, s47\nds_write_b32 v222, v221\n"
"s_sleep 2\ns_branch top2_p0\nrec_p0:\ns_sleep 6\n"
"s_branch top2_p0\nadopt_p0:\nbuffer_load_dword v164, v28, s[8:11], 0 offen sc1\nv_smfmac_f32_16x16x64_bf16 v[68:71], v[48:51], a[16:23], v8\n"
"v_smfmac_f32_16x16x64_bf16 v[72:75], v[48:51], a[72:79], v8\nv_smfmac_f32_16x16x64_bf16 v[76:79], v[48:51], a[128:135], v8\nv_smfmac_f32_16x16x64_bf16 v[80:83], v[48:51], a[184:191], v8\nv_smfmac_f32_16x16x64_bf16 v[68:71], v[52:55], a[24:31], v8\n"
"v_smfmac_f32_16x16x64_bf16 v[72:75], v[52:55], a[80:87], v8\nv_smfmac_f32_16x16x64_bf16 v[76:79], v[52:55], a[136:143], v8\nv_smfmac_f32_16x16x64_bf16 v[80:83], v[52:55], a[192:199], v8\nv_smfmac_f32_16x16x64_bf16 v[68:71], v[56:59], a[32:39], v8\n"
"v_smfmac_f32_16x16x64_bf16 v[72:75], v[56:59], a[88:95], v8\nv_smfmac_f32_16x16x64_bf16 v[76:79], v[56:59], a[144:151], v8\nv_smfmac_f32_16x16x64_bf16 v[80:83], v[56:59], a[200:207], v8\nv_smfmac_f32_16x16x64_bf16 v[68:71], v[60:63], a[40:47], v8\n"
"v_smfmac_f32_16x16x64_bf16 v[72:75], v[60:63], a[96:103], v8\nv_smfmac_f32_16x16x64_bf16 v[76:79], v[60:63], a[152:159], v8\nv_smfmac_f32_16x16x64_bf16 v[80:83], v[60:63], a[208:215], v8\nv_smfmac_f32_16x16x64_bf16 v[68:71], v[64:67], a[48:55], v8\n"
"v_smfmac_f32_16x16x64_bf16 v[72:75], v[64:67], a[104:111], v8\nv_smfmac_f32_16x16x64_bf16 v[76:79], v[64:67], a[160:167], v8\nv_smfmac_f32_16x16x64_bf16 v[80:83], v[64:67], a[216:223], v8\ns_lshl_b32 s52, s40, 26\n"
"s_or_b32 s47, s52, 0x3ffffff\ns_waitcnt vmcnt(0)\nv_cmp_ge_u32 vcc, s47, v164\ns_nop 1\n"
"v_cndmask_b32 v221, 0, v164, vcc\ns_nop 1\nv_max_u32_dpp v221, v221, v221 row_ror:8 row_mask:0xf bank_mask:0xf\ns_nop 1\n"
"v_max_u32_dpp v221, v221, v221 row_ror:4 row_mask:0xf bank_mask:0xf\ns_nop 1\nv_max_u32_dpp v221, v221, v221 row_ror:2 row_mask:0xf bank_mask:0xf\ns_nop 1\n"
"v_max_u32_dpp v221, v221, v221 row_ror:1 row_mask:0xf bank_mask:0xf\ns_nop 0\nv_readlane_b32 s47, v221, 0\nv_readlane_b32 s48, v221, 16\n"
"s_max_u32 s48, s48, s47\ns_cmp_le_u32 s48, s52\ns_cbranch_scc1 back3_p0\ns_bfe_u32 s52, s48, 1179656\n"
"s_add_u32 s52, s52, s30\ns_sub_u32 s45, s52, s44\ns_and_b32 s45, s45, 3\ns_sub_u32 s44, s52, s45\n"
"s_lshl_b32 s45, s45, 2\ns_and_b32 s46, s48, 255\ns_mov_b32 s92, 0\ns_branch back2_p0\n"
"back2_p0:\ns_and_b32 s47, s46, 15\ns_mul_i32 s47, s47, 4\ns_add_u32 s47, s47, s31\n"
"s_add_u32 s47, s47, s45\ns_and_b32 s91, s47, 15\ns_lshr_b32 s47, s47, 4\ns_lshl_b32 s47, s47, 2\n"
"s_add_u32 s90, s44, s47\ns_mul_i32 s47, s91, 6\ns_add_u32 s47, s47, s93\ns_lshr_b32 s47, s47, 4\n"
"s_mul_i32 s47, s47, 300\ns_getpc_b64 s[60:61]\nep2_p0:\ns_add_u32 s47, s47, sgo_p0-ep2_p0\n"
"s_add_u32 s60, s60, s47\ns_addc_u32 s61, s61, 0\ns_mul_i32 s48, s46, 8\ns_add_u32 s45, s45, s48\n"
"s_add_u32 s45, s45, s32\ns_lshr_b32 s48, s45, 4\ns_lshl_b32 s48, s48, 2\ns_add_u32 s44, s44, s48\n"
"s_and_b32 s45, s45, 15\ns_add_u32 s52, s40, 1\ns_bitcmp1_b64 s[28:29], s52\ns_subb_u32 s46, s46, 0\n"
"s_mul_i32 s48, s45, 6\ns_and_b32 s48, s48, -4\ns_getpc_b64 s[88:89]\nmpc2_p0:\n"
"s_sub_u32 s48, mgo_p0-mpc2_p0, s48\ns_add_u32 s88, s88, s48\ns_addc_u32 s89, s89, -1\nback3_p0:\n"
"s_waitcnt lgkmcnt(0)\ns_memrealtime s[54:55]\ns_nop 2\ns_memrealtime s[56:57]\n"
"s_nop 2\ns_memrealtime s[58:59]\ns_waitcnt lgkmcnt(0)\ns_sub_u32 s47, s58, s90\n"
"s_cmp_lt_i32 s47, 0\ns_cbranch_scc1 sp_p0\ns_cmp_le_i32 s47, 0\ns_cbranch_scc1 sgn_p0\n"
"s_add_u32 s53, s53, 16777216\ns_mov_b64 s[94:95], 1\ns_add_u32 s52, s90, 32\nlw2_p0:\n"
"s_memrealtime s[54:55]\ns_waitcnt lgkmcnt(0)\ns_sub_u32 s47, s54, s52\ns_cmp_lt_i32 s47, 0\n"
"s_cbranch_scc1 lw2_p0\ns_branch sgn_p0\nlate_p0:\ns_add_u32 s53, s53, 16777216\n"
"s_mov_b64 s[94:95], 1\ns_add_u32 s52, s90, 32\nlw_p0:\ns_memrealtime s[54:55]\n"
"s_waitcnt lgkmcnt(0)\ns_sub_u32 s47, s54, s52\ns_cmp_lt_i32 s47, 0\ns_cbranch_scc1 lw_p0\n"
"s_branch sgo_p0\nslow_p0:\ns_mov_b64 s[94:95], 0\ns_cbranch_vccnz slow2_p0\n"
"s_add_u32 s47, s40, 2\nv_mov_b32 v221, s47\nds_write_b32 v222, v221\ns_branch join_p0\n"
"slow2_p0:\nv_max3_u32 v218, v124, v125, v126\nv_max_u32 v218, v218, v127\nv_max3_u32 v218, v218, v128, v129\n"
"v_max3_u32 v218, v218, v130, v131\nv_max3_u32 v218, v218, v116, v117\nv_max3_u32 v218, v218, v118, v119\nv_max3_u32 v218, v218, v120, v121\n"
"v_max3_u32 v218, v218, v122, v123\nv_cmp_eq_u32 vcc, -1, v218\ns_cbranch_vccz exact_p0\ns_branch spin_p0\n"
"spin2_p0:\ns_sleep 3\nspin_p0:\ns_add_u32 s53, s53, 1\n"
"s_add_u32 s49, s49, 1\ns_cmp_gt_u32 s49, 65536\ns_cbranch_scc1 end_p0\nbuffer_load_dwordx4 v[116:119], v20, s[8:11], s41 offen offset:0 sc1\n"
"buffer_load_dwordx4 v[120:123], v20, s[8:11], s41 offen offset:1024 sc1\nbuffer_load_dwordx4 v[124:127], v21, s[8:11], s41 offen offset:0 sc1\nbuffer_load_dwordx4 v[128:131], v21, s[8:11], s41 offen offset:1024 sc1\ns_waitcnt vmcnt(0)\n"
"v_max3_u32 v218, v124, v125, v126\nv_max_u32 v218, v218, v127\nv_max3_u32 v218, v218, v128, v129\nv_max3_u32 v218, v218, v130, v131\n"
"v_max3_u32 v218, v218, v116, v117\nv_max3_u32 v218, v218, v118, v119\nv_max3_u32 v218, v218, v120, v121\nv_max3_u32 v218, v218, v122, v123\n"
"v_cmp_eq_u32 vcc, -1, v218\ns_cbranch_vccnz spin2_p0\ns_add_u32 s47, s40, 2\nv_mov_b32 v221, s47\n"
"ds_write_b32 v222, v221\nv_mfma_f32_16x16x32_bf16 v[132:135], v[84:87], v[116:119], 0\nv_mfma_f32_16x16x32_bf16 v[136:139], v[88:91], v[116:119], 0\nv_mfma_f32_16x16x32_bf16 v[148:151], v[176:179], v[116:119], 0\n"
"v_mfma_f32_16x16x32_bf16 v[140:143], v[92:95], v[120:123], 0\nv_mfma_f32_16x16x32_bf16 v[144:147], v[96:99], v[120:123], 0\nv_mfma_f32_16x16x32_bf16 v[148:151], v[180:183], v[120:123], v[148:151]\nv_cndmask_b32 v132, v136, v132, s[62:63]\n"
"v_cndmask_b32 v133, v137, v133, s[62:63]\nv_cndmask_b32 v134, v138, v134, s[62:63]\nv_cndmask_b32 v135, v139, v135, s[62:63]\nv_mfma_f32_16x16x32_f16 v[152:155], v[168:171], v[124:127], 0\n"
"v_mfma_f32_16x16x32_f16 v[152:155], v[172:175], v[128:131], v[152:155]\nv_cndmask_b32 v140, v144, v140, s[62:63]\nv_cndmask_b32 v141, v145, v141, s[62:63]\nv_cndmask_b32 v142, v146, v142, s[62:63]\n"
"v_cndmask_b32 v143, v147, v143, s[62:63]\ns_nop 2\nv_exp_f32 v152, v152\nv_exp_f32 v153, v153\n"
"v_exp_f32 v154, v154\nv_exp_f32 v155, v155\nv_cvt_pk_bf16_f32 v200, v152, v153\nv_cvt_pk_bf16_f32 v201, v154, v155\n"
"v_and_b32 v160, v200, v223\nv_and_b32 v161, v200, v224\nv_mfma_f32_16x16x16_bf16 v[148:151], v[214:215], v[200:201], v[148:151]\nv_and_b32 v162, v201, v223\n"
"v_and_b32 v163, v201, v224\nv_mfma_f32_16x16x16_bf16 v[132:135], v[156:157], v[160:161], v[132:135]\ns_nop 0\nv_mfma_f32_16x16x16_bf16 v[140:143], v[158:159], v[162:163], v[140:143]\n"
"v_rcp_f32 v202, v148\nv_rcp_f32 v203, v149\nv_mul_f32 v132, v132, v202\nv_mul_f32 v133, v133, v202\n"
"v_mul_f32 v134, v134, v202\nv_mul_f32 v135, v135, v202\nv_fmac_f32 v132, v140, v203\nv_fmac_f32 v133, v141, v203\n"
"v_fmac_f32 v134, v142, v203\nv_fmac_f32 v135, v143, v203\nv_add_f32_dpp v204, v132, v132 row_ror:8 row_mask:0xf bank_mask:0x3\nv_add_f32_dpp v205, v133, v133 row_ror:8 row_mask:0xf bank_mask:0x3\n"
"v_add_f32_dpp v204, v134, v134 row_ror:8 row_mask:0xf bank_mask:0xc\nv_add_f32_dpp v205, v135, v135 row_ror:8 row_mask:0xf bank_mask:0xc\nv_subrev_u32 v219, 0x23800000, v148\nv_subrev_u32 v220, 0x23800000, v149\n"
"v_max_u32 v219, v219, v220\nv_cmp_le_u32 vcc, 0x38000000, v219\nds_write_b64 v22, v[204:205]\ns_cbranch_vccz join_p0\n"
"exact_p0:\ns_add_u32 s53, s53, 65536\nex0_p0:\ns_add_u32 s47, s41, 0\n"
"buffer_load_dwordx2 v[184:185], v24, s[8:11], s47 offen offset:0 sc1\nbuffer_load_dwordx2 v[186:187], v24, s[8:11], s47 offen offset:512 sc1\nbuffer_load_dwordx2 v[188:189], v24, s[8:11], s47 offen offset:1024 sc1\nbuffer_load_dwordx2 v[190:191], v24, s[8:11], s47 offen offset:1536 sc1\n"
"buffer_load_dwordx4 v[192:195], v25, s[8:11], s47 offen sc1\nbuffer_load_dwordx4 v[196:199], v25, s[8:11], s47 offen offset:16 sc1\nv_mov_b32 v218, 0\ns_waitcnt vmcnt(0)\n"
"v_max3_u32 v218, v218, v184, v185\nv_max3_u32 v218, v218, v186, v187\nv_max3_u32 v218, v218, v188, v189\nv_max3_u32 v218, v218, v190, v191\n"
"v_max3_u32 v218, v218, v192, v193\nv_max3_u32 v218, v218, v194, v195\nv_max3_u32 v218, v218, v196, v197\nv_max3_u32 v218, v218, v198, v199\n"
"v_cmp_eq_u32 vcc, -1, v218\ns_cbranch_vccz exok0_p0\ns_add_u32 s53, s53, 0\ns_add_u32 s49, s49, 1\n"
"s_cmp_gt_u32 s49, 65536\ns_cbranch_scc1 end_p0\ns_branch ex0_p0\nexok0_p0:\n"
"v_add_f32 v184, v184, v186\nv_add_f32 v188, v188, v190\nv_add_f32 v184, v184, v188\nv_add_f32 v185, v185, v187\n"
"v_add_f32 v189, v189, v191\nv_add_f32 v185, v185, v189\nv_max_f32 v225, v184, v185\nv_max3_f32 v225, v225, v192, v193\n"
"v_max3_f32 v225, v225, v194, v195\nv_max3_f32 v225, v225, v196, v197\nv_max3_f32 v225, v225, v198, v199\nv_mov_b32 v219, v225\n"
"s_nop 1\nv_permlane16_swap_b32 v225, v219\nv_max_f32 v225, v225, v219\nv_mov_b32 v219, v225\n"
"s_nop 1\nv_permlane32_swap_b32 v225, v219\nv_max_f32 v225, v225, v219\nv_sub_f32 v184, v184, v225\n"
"v_exp_f32 v152, v184\nv_sub_f32 v185, v185, v225\nv_exp_f32 v153, v185\nv_sub_f32 v192, v192, v225\n"
"v_exp_f32 v192, v192\nv_sub_f32 v193, v193, v225\nv_exp_f32 v193, v193\nv_sub_f32 v194, v194, v225\n"
"v_exp_f32 v194, v194\nv_sub_f32 v195, v195, v225\nv_exp_f32 v195, v195\nv_sub_f32 v196, v196, v225\n"
"v_exp_f32 v196, v196\nv_sub_f32 v197, v197, v225\nv_exp_f32 v197, v197\nv_sub_f32 v198, v198, v225\n"
"v_exp_f32 v198, v198\nv_sub_f32 v199, v199, v225\nv_exp_f32 v199, v199\ns_nop 0\n"
"v_cvt_pk_bf16_f32 v116, v192, v193\nv_cvt_pk_bf16_f32 v117, v194, v195\nv_cvt_pk_bf16_f32 v118, v196, v197\nv_cvt_pk_bf16_f32 v119, v198, v199\n"
"ex1_p0:\ns_add_u32 s47, s41, 4096\nbuffer_load_dwordx2 v[184:185], v24, s[8:11], s47 offen offset:0 sc1\nbuffer_load_dwordx2 v[186:187], v24, s[8:11], s47 offen offset:512 sc1\n"
"buffer_load_dwordx2 v[188:189], v24, s[8:11], s47 offen offset:1024 sc1\nbuffer_load_dwordx2 v[190:191], v24, s[8:11], s47 offen offset:1536 sc1\nbuffer_load_dwordx4 v[192:195], v25, s[8:11], s47 offen sc1\nbuffer_load_dwordx4 v[196:199], v25, s[8:11], s47 offen offset:16 sc1\n"
"v_mov_b32 v218, 0\ns_waitcnt vmcnt(0)\nv_max3_u32 v218, v218, v184, v185\nv_max3_u32 v218, v218, v186, v187\n"
"v_max3_u32 v218, v218, v188, v189\nv_max3_u32 v218, v218, v190, v191\nv_max3_u32 v218, v218, v192, v193\nv_max3_u32 v218, v218, v194, v195\n"
"v_max3_u32 v218, v218, v196, v197\nv_max3_u32 v218, v218, v198, v199\nv_cmp_eq_u32 vcc, -1, v218\ns_cbranch_vccz exok1_p0\n"
"s_add_u32 s53, s53, 0\ns_add_u32 s49, s49, 1\ns_cmp_gt_u32 s49, 65536\ns_cbranch_scc1 end_p0\n"
"s_branch ex1_p0\nexok1_p0:\nv_add_f32 v184, v184, v186\nv_add_f32 v188, v188, v190\n"
"v_add_f32 v184, v184, v188\nv_add_f32 v185, v185, v187\nv_add_f32 v189, v189, v191\nv_add_f32 v185, v185, v189\n"
"v_max_f32 v225, v184, v185\nv_max3_f32 v225, v225, v192, v193\nv_max3_f32 v225, v225, v194, v195\nv_max3_f32 v225, v225, v196, v197\n"
"v_max3_f32 v225, v225, v198, v199\nv_mov_b32 v219, v225\ns_nop 1\nv_permlane16_swap_b32 v225, v219\n"
"v_max_f32 v225, v225, v219\nv_mov_b32 v219, v225\ns_nop 1\nv_permlane32_swap_b32 v225, v219\n"
"v_max_f32 v225, v225, v219\nv_sub_f32 v184, v184, v225\nv_exp_f32 v154, v184\nv_sub_f32 v185, v185, v225\n"
"v_exp_f32 v155, v185\nv_sub_f32 v192, v192, v225\nv_exp_f32 v192, v192\nv_sub_f32 v193, v193, v225\n"
"v_exp_f32 v193, v193\nv_sub_f32 v194, v194, v225\nv_exp_f32 v194, v194\nv_sub_f32 v195, v195, v225\n"
"v_exp_f32 v195, v195\nv_sub_f32 v196, v196, v225\nv_exp_f32 v196, v196\nv_sub_f32 v197, v197, v225\n"
"v_exp_f32 v197, v197\nv_sub_f32 v198, v198, v225\nv_exp_f32 v198, v198\nv_sub_f32 v199, v199, v225\n"
"v_exp_f32 v199, v199\ns_nop 0\nv_cvt_pk_bf16_f32 v120, v192, v193\nv_cvt_pk_bf16_f32 v121, v194, v195\n"
"v_cvt_pk_bf16_f32 v122, v196, v197\nv_cvt_pk_bf16_f32 v123, v198, v199\ns_add_u32 s47, s40, 2\nv_mov_b32 v221, s47\n"
"ds_write_b32 v222, v221\nv_mfma_f32_16x16x32_bf16 v[132:135], v[84:87], v[116:119], 0\nv_mfma_f32_16x16x32_bf16 v[136:139], v[88:91], v[116:119], 0\nv_mfma_f32_16x16x32_bf16 v[148:151], v[176:179], v[116:119], 0\n"
"v_mfma_f32_16x16x32_bf16 v[140:143], v[92:95], v[120:123], 0\nv_mfma_f32_16x16x32_bf16 v[144:147], v[96:99], v[120:123], 0\nv_mfma_f32_16x16x32_bf16 v[148:151], v[180:183], v[120:123], v[148:151]\nv_cndmask_b32 v132, v136, v132, s[62:63]\n"
"v_cndmask_b32 v133, v137, v133, s[62:63]\nv_cndmask_b32 v134, v138, v134, s[62:63]\nv_cndmask_b32 v135, v139, v135, s[62:63]\ns_nop 0\n"
"v_cndmask_b32 v140, v144, v140, s[62:63]\nv_cndmask_b32 v141, v145, v141, s[62:63]\nv_cndmask_b32 v142, v146, v142, s[62:63]\nv_cndmask_b32 v143, v147, v143, s[62:63]\n"
"v_cvt_pk_bf16_f32 v200, v152, v153\nv_cvt_pk_bf16_f32 v201, v154, v155\nv_and_b32 v160, v200, v223\nv_and_b32 v161, v200, v224\n"
"v_mfma_f32_16x16x16_bf16 v[148:151], v[214:215], v[200:201], v[148:151]\nv_and_b32 v162, v201, v223\nv_and_b32 v163, v201, v224\nv_mfma_f32_16x16x16_bf16 v[132:135], v[156:157], v[160:161], v[132:135]\n"
"s_nop 0\nv_mfma_f32_16x16x16_bf16 v[140:143], v[158:159], v[162:163], v[140:143]\nv_rcp_f32 v202, v148\nv_rcp_f32 v203, v149\n"
"v_mul_f32 v132, v132, v202\nv_mul_f32 v133, v133, v202\nv_mul_f32 v134, v134, v202\nv_mul_f32 v135, v135, v202\n"
"v_fmac_f32 v132, v140, v203\nv_fmac_f32 v133, v141, v203\nv_fmac_f32 v134, v142, v203\nv_fmac_f32 v135, v143, v203\n"
"v_add_f32_dpp v204, v132, v132 row_ror:8 row_mask:0xf bank_mask:0x3\nv_add_f32_dpp v205, v133, v133 row_ror:8 row_mask:0xf bank_mask:0x3\nv_add_f32_dpp v204, v134, v134 row_ror:8 row_mask:0xf bank_mask:0xc\nv_add_f32_dpp v205, v135, v135 row_ror:8 row_mask:0xf bank_mask:0xc\n"
"ds_write_b64 v22, v[204:205]\ns_branch join_p0\n.p2align 6\nva0_p0:\n"
"s_branch warm_p0\nsx0_p0:\ns_add_u32 s60, s60, 308\ns_addc_u32 s61, s61, 0\n"
"s_setpc_b64 s[60:61]\nvb0_p0:\ns_branch vr0_p0\ns_nop 1\n"
"s_nop 1\ns_nop 1\ns_nop 1\ns_nop 1\n"
"s_nop 1\ns_nop 1\ns_nop 1\ns_nop 1\n"
"s_nop 1\ns_nop 1\ns_nop 1\ns_nop 1\n"
"s_nop 1\nvr0_p0:\ns_nop 1\ns_nop 1\n"
"s_nop 1\ns_nop 1\ns_nop 1\ns_nop 1\n"
"s_nop 1\ns_nop 1\nsgo_p0:\ns_cbranch_execz vc0x0_p0\n"
"buffer_load_dwordx4 v[116:119], v20, s[8:11], s41 offen offset:0 sc1\nbuffer_load_dwordx4 v[120:123], v20, s[8:11], s41 offen offset:1024 sc1\nv_smfmac_f32_16x16x64_bf16 v[68:71], v[48:51], a[16:23], v8\nv_smfmac_f32_16x16x64_bf16 v[72:75], v[48:51], a[72:79], v8\n"
"v_smfmac_f32_16x16x64_bf16 v[76:79], v[48:51], a[128:135], v8\nv_smfmac_f32_16x16x64_bf16 v[80:83], v[48:51], a[184:191], v8\nv_smfmac_f32_16x16x64_bf16 v[68:71], v[52:55], a[24:31], v8\nvc0x0_p0:\n"
"s_cbranch_execz vc0x1_p0\nv_smfmac_f32_16x16x64_bf16 v[72:75], v[52:55], a[80:87], v8\nv_smfmac_f32_16x16x64_bf16 v[76:79], v[52:55], a[136:143], v8\nv_smfmac_f32_16x16x64_bf16 v[80:83], v[52:55], a[192:199], v8\n"
"v_smfmac_f32_16x16x64_bf16 v[68:71], v[56:59], a[32:39], v8\nv_smfmac_f32_16x16x64_bf16 v[72:75], v[56:59], a[88:95], v8\nvc0x1_p0:\ns_cbranch_execz vd0_p0\n"
"buffer_load_dwordx4 v[124:127], v21, s[8:11], s41 offen offset:0 sc1\nbuffer_load_dwordx4 v[128:131], v21, s[8:11], s41 offen offset:1024 sc1\nvd0_p0:\ns_cbranch_execnz tl10_p0\n"
"s_nop 0\ns_nop 0\ns_nop 0\ns_nop 0\n"
"s_nop 0\ns_nop 0\ns_nop 0\ns_nop 0\n"
"s_nop 0\ns_nop 0\ns_nop 0\ns_nop 0\n"
"s_nop 0\ns_nop 0\ns_nop 0\ns_nop 0\n"
"s_nop 0\nva1_p0:\ns_branch vb1_p0\nsx1_p0:\n"
"s_add_u32 s60, s60, 308\ns_addc_u32 s61, s61, 0\ns_setpc_b64 s[60:61]\nvb1_p0:\n"
"s_branch vr1_p0\ns_nop 1\ns_nop 1\ns_nop 1\n"
"s_nop 1\ns_nop 1\ns_nop 1\ns_nop 1\n"
"s_nop 1\ns_nop 1\ns_nop 1\ns_nop 1\n"
"s_nop 1\ns_nop 1\ns_nop 1\nvr1_p0:\n"
"s_nop 1\ns_nop 1\ns_nop 1\ns_nop 1\n"
"s_nop 1\ns_nop 1\nv_smfmac_f32_16x16x64_bf16 v[68:71], v[48:51], a[16:23], v8\ns_cbranch_execz vc1x0_p0\n"
"buffer_load_dwordx4 v[116:119], v20, s[8:11], s41 offen offset:0 sc1\nbuffer_load_dwordx4 v[120:123], v20, s[8:11], s41 offen offset:1024 sc1\nv_smfmac_f32_16x16x64_bf16 v[72:75], v[48:51], a[72:79], v8\nv_smfmac_f32_16x16x64_bf16 v[76:79], v[48:51], a[128:135], v8\n"
"v_smfmac_f32_16x16x64_bf16 v[80:83], v[48:51], a[184:191], v8\nv_smfmac_f32_16x16x64_bf16 v[68:71], v[52:55], a[24:31], v8\nv_smfmac_f32_16x16x64_bf16 v[72:75], v[52:55], a[80:87], v8\nvc1x0_p0:\n"
"s_cbranch_execz vc1x1_p0\nv_smfmac_f32_16x16x64_bf16 v[76:79], v[52:55], a[136:143], v8\nv_smfmac_f32_16x16x64_bf16 v[80:83], v[52:55], a[192:199], v8\nv_smfmac_f32_16x16x64_bf16 v[68:71], v[56:59], a[32:39], v8\n"
"v_smfmac_f32_16x16x64_bf16 v[72:75], v[56:59], a[88:95], v8\nv_smfmac_f32_16x16x64_bf16 v[76:79], v[56:59], a[144:151], v8\nvc1x1_p0:\ns_cbranch_execz vd1_p0\n"
"buffer_load_dwordx4 v[124:127], v21, s[8:11], s41 offen offset:0 sc1\nbuffer_load_dwordx4 v[128:131], v21, s[8:11], s41 offen offset:1024 sc1\nvd1_p0:\ns_cbranch_execnz tl11_p0\n"
"s_nop 0\ns_nop 0\ns_nop 0\ns_nop 0\n"
"s_nop 0\ns_nop 0\ns_nop 0\ns_nop 0\n"
"s_nop 0\ns_nop 0\ns_nop 0\ns_nop 0\n"
"s_nop 0\ns_nop 0\ns_nop 0\ns_nop 0\n"
"s_nop 0\nva2_p0:\ns_branch vb2_p0\nsx2_p0:\n"
"s_add_u32 s60, s60, 616\ns_addc_u32 s61, s61, 0\ns_setpc_b64 s[60:61]\nvb2_p0:\n"
"s_branch vr2_p0\ns_nop 1\ns_nop 1\ns_nop 1\n"
"s_nop 1\ns_nop 1\ns_nop 1\ns_nop 1\n"
"s_nop 1\ns_nop 1\ns_nop 1\ns_nop 1\n"
"s_nop 1\ns_nop 1\ns_nop 1\nvr2_p0:\n"
"s_nop 1\ns_nop 1\ns_nop 1\ns_nop 1\n"
"v_smfmac_f32_16x16x64_bf16 v[68:71], v[48:51], a[16:23], v8\nv_smfmac_f32_16x16x64_bf16 v[72:75], v[48:51], a[72:79], v8\ns_cbranch_execz vc2x0_p0\nbuffer_load_dwordx4 v[116:119], v20, s[8:11], s41 offen offset:0 sc1\n"
"buffer_load_dwordx4 v[120:123], v20, s[8:11], s41 offen offset:1024 sc1\nv_smfmac_f32_16x16x64_bf16 v[76:79], v[48:51], a[128:135], v8\nv_smfmac_f32_16x16x64_bf16 v[80:83], v[48:51], a[184:191], v8\nv_smfmac_f32_16x16x64_bf16 v[68:71], v[52:55], a[24:31], v8\n"
"v_smfmac_f32_16x16x64_bf16 v[72:75], v[52:55], a[80:87], v8\nv_smfmac_f32_16x16x64_bf16 v[76:79], v[52:55], a[136:143], v8\nvc2x0_p0:\ns_cbranch_execz vc2x1_p0\n"
"v_smfmac_f32_16x16x64_bf16 v[80:83], v[52:55], a[192:199], v8\nv_smfmac_f32_16x16x64_bf16 v[68:71], v[56:59], a[32:39], v8\nv_smfmac_f32_16x16x64_bf16 v[72:75], v[56:59], a[88:95], v8\nv_smfmac_f32_16x16x64_bf16 v[76:79], v[56:59], a[144:151], v8\n"
"v_smfmac_f32_16x16x64_bf16 v[80:83], v[56:59], a[200:207], v8\nvc2x1_p0:\ns_cbranch_execz vd2_p0\nbuffer_load_dwordx4 v[124:127], v21, s[8:11], s41 offen offset:0 sc1\n"
"buffer_load_dwordx4 v[128:131], v21, s[8:11], s41 offen offset:1024 sc1\nvd2_p0:\ns_cbranch_execnz tl12_p0\ns_nop 0\n"
"s_nop 0\ns_nop 0\ns_nop 0\ns_nop 0\n"
"s_nop 0\ns_nop 0\ns_nop 0\ns_nop 0\n"
"s_nop 0\ns_nop 0\ns_nop 0\ns_nop 0\n"
"s_nop 0\ns_nop 0\ns_nop 0\ns_nop 0\n"
"va3_p0:\ns_branch vb3_p0\nsx3_p0:\ns_add_u32 s60, s60, 924\n"
"s_addc_u32 s61, s61, 0\ns_setpc_b64 s[60:61]\nvb3_p0:\ns_branch vr3_p0\n"
"s_nop 1\ns_nop 1\ns_nop 1\ns_nop 1\n"
"s_nop 1\ns_nop 1\ns_nop 1\ns_nop 1\n"
"s_nop 1\ns_nop 1\ns_nop 1\ns_nop 1\n"
"s_nop 1\ns_nop 1\nvr3_p0:\ns_nop 1\n"
"s_nop 1\nv_smfmac_f32_16x16x64_bf16 v[68:71], v[48:51], a[16:23], v8\nv_smfmac_f32_16x16x64_bf16 v[72:75], v[48:51], a[72:79], v8\nv_smfmac_f32_16x16x64_bf16 v[76:79], v[48:51], a[128:135], v8\n"
"s_cbranch_execz vc3x0_p0\nbuffer_load_dwordx4 v[116:119], v20, s[8:11], s41 offen offset:0 sc1\nbuffer_load_dwordx4 v[120:123], v20, s[8:11], s41 offen offset:1024 sc1\nv_smfmac_f32_16x16x64_bf16 v[80:83], v[48:51], a[184:191], v8\n"
"v_smfmac_f32_16x16x64_bf16 v[68:71], v[52:55], a[24:31], v8\nv_smfmac_f32_16x16x64_bf16 v[72:75], v[52:55], a[80:87], v8\nv_smfmac_f32_16x16x64_bf16 v[76:79], v[52:55], a[136:143], v8\nv_smfmac_f32_16x16x64_bf16 v[80:83], v[52:55], a[192:199], v8\n"
"vc3x0_p0:\ns_cbranch_execz vc3x1_p0\nv_smfmac_f32_16x16x64_bf16 v[68:71], v[56:59], a[32:39], v8\nv_smfmac_f32_16x16x64_bf16 v[72:75], v[56:59], a[88:95], v8\n"
"v_smfmac_f32_16x16x64_bf16 v[76:79], v[56:59], a[144:151], v8\nv_smfmac_f32_16x16x64_bf16 v[80:83], v[56:59], a[200:207], v8\nv_smfmac_f32_16x16x64_bf16 v[68:71], v[60:63], a[40:47], v8\nvc3x1_p0:\n"
"s_cbranch_execz vd3_p0\nbuffer_load_dwordx4 v[124:127], v21, s[8:11], s41 offen offset:0 sc1\nbuffer_load_dwordx4 v[128:131], v21, s[8:11], s41 offen offset:1024 sc1\nvd3_p0:\n"
"s_cbranch_execnz tl13_p0\ns_nop 0\ns_nop 0\ns_nop 0\n"
"s_nop 0\ns_nop 0\ns_nop 0\ns_nop 0\n"
"s_nop 0\ns_nop 0\ns_nop 0\ns_nop 0\n"
"s_nop 0\ns_nop 0\ns_nop 0\ns_nop 0\n"
"s_nop 0\ns_nop 0\nva4_p0:\ns_branch vb4_p0\n"
"sx4_p0:\ns_add_u32 s60, s60, 1232\ns_addc_u32 s61, s61, 0\ns_setpc_b64 s[60:61]\n"
"vb4_p0:\ns_branch vr4_p0\ns_nop 1\ns_nop 1\n"
"s_nop 1\ns_nop 1\ns_nop 1\ns_nop 1\n"
"s_nop 1\ns_nop 1\ns_nop 1\ns_nop 1\n"
"s_nop 1\ns_nop 1\ns_nop 1\ns_nop 1\n"
"vr4_p0:\nv_smfmac_f32_16x16x64_bf16 v[68:71], v[48:51], a[16:23], v8\nv_smfmac_f32_16x16x64_bf16 v[72:75], v[48:51], a[72:79], v8\nv_smfmac_f32_16x16x64_bf16 v[76:79], v[48:51], a[128:135], v8\n"
"v_smfmac_f32_16x16x64_bf16 v[80:83], v[48:51], a[184:191], v8\ns_cbranch_execz vc4x0_p0\nbuffer_load_dwordx4 v[116:119], v20, s[8:11], s41 offen offset:0 sc1\nbuffer_load_dwordx4 v[120:123], v20, s[8:11], s41 offen offset:1024 sc1\n"
"v_smfmac_f32_16x16x64_bf16 v[68:71], v[52:55], a[24:31], v8\nv_smfmac_f32_16x16x64_bf16 v[72:75], v[52:55], a[80:87], v8\nv_smfmac_f32_16x16x64_bf16 v[76:79], v[52:55], a[136:143], v8\nv_smfmac_f32_16x16x64_bf16 v[80:83], v[52:55], a[192:199], v8\n"
"v_smfmac_f32_16x16x64_bf16 v[68:71], v[56:59], a[32:39], v8\nvc4x0_p0:\ns_cbranch_execz vc4x1_p0\nv_smfmac_f32_16x16x64_bf16 v[72:75], v[56:59], a[88:95], v8\n"
"v_smfmac_f32_16x16x64_bf16 v[76:79], v[56:59], a[144:151], v8\nv_smfmac_f32_16x16x64_bf16 v[80:83], v[56:59], a[200:207], v8\nv_smfmac_f32_16x16x64_bf16 v[68:71], v[60:63], a[40:47], v8\nv_smfmac_f32_16x16x64_bf16 v[72:75], v[60:63], a[96:103], v8\n"
"vc4x1_p0:\ns_cbranch_execz vd4_p0\nbuffer_load_dwordx4 v[124:127], v21, s[8:11], s41 offen offset:0 sc1\nbuffer_load_dwordx4 v[128:131], v21, s[8:11], s41 offen offset:1024 sc1\n"
"vd4_p0:\ns_cbranch_execnz tl14_p0\ns_nop 0\ns_nop 0\n"
"s_nop 0\ns_nop 0\ns_nop 0\ns_nop 0\n"
"s_nop 0\ns_nop 0\ns_nop 0\ns_nop 0\n"
"s_nop 0\ns_nop 0\ns_nop 0\ns_nop 0\n"
"s_nop 0\ns_nop 0\ns_nop 0\nva5_p0:\n"
"s_branch vb5_p0\nsx5_p0:\ns_add_u32 s60, s60, 1540\ns_addc_u32 s61, s61, 0\n"
"s_setpc_b64 s[60:61]\nvb5_p0:\ns_branch vr5_p0\ns_nop 1\n"
"s_nop 1\ns_nop 1\ns_nop 1\ns_nop 1\n"
"s_nop 1\ns_nop 1\ns_nop 1\ns_nop 1\n"
"s_nop 1\ns_nop 1\ns_nop 1\nv_smfmac_f32_16x16x64_bf16 v[68:71], v[48:51], a[16:23], v8\n"
"vr5_p0:\nv_smfmac_f32_16x16x64_bf16 v[72:75], v[48:51], a[72:79], v8\nv_smfmac_f32_16x16x64_bf16 v[76:79], v[48:51], a[128:135], v8\nv_smfmac_f32_16x16x64_bf16 v[80:83], v[48:51], a[184:191], v8\n"
"v_smfmac_f32_16x16x64_bf16 v[68:71], v[52:55], a[24:31], v8\ns_cbranch_execz vc5x0_p0\nbuffer_load_dwordx4 v[116:119], v20, s[8:11], s41 offen offset:0 sc1\nbuffer_load_dwordx4 v[120:123], v20, s[8:11], s41 offen offset:1024 sc1\n"
"v_smfmac_f32_16x16x64_bf16 v[72:75], v[52:55], a[80:87], v8\nv_smfmac_f32_16x16x64_bf16 v[76:79], v[52:55], a[136:143], v8\nv_smfmac_f32_16x16x64_bf16 v[80:83], v[52:55], a[192:199], v8\nv_smfmac_f32_16x16x64_bf16 v[68:71], v[56:59], a[32:39], v8\n"
"v_smfmac_f32_16x16x64_bf16 v[72:75], v[56:59], a[88:95], v8\nvc5x0_p0:\ns_cbranch_execz vc5x1_p0\nv_smfmac_f32_16x16x64_bf16 v[76:79], v[56:59], a[144:151], v8\n"
"v_smfmac_f32_16x16x64_bf16 v[80:83], v[56:59], a[200:207], v8\nv_smfmac_f32_16x16x64_bf16 v[68:71], v[60:63], a[40:47], v8\nv_smfmac_f32_16x16x64_bf16 v[72:75], v[60:63], a[96:103], v8\nv_smfmac_f32_16x16x64_bf16 v[76:79], v[60:63], a[152:159], v8\n"
"vc5x1_p0:\ns_cbranch_execz vd5_p0\nbuffer_load_dwordx4 v[124:127], v21, s[8:11], s41 offen offset:0 sc1\nbuffer_load_dwordx4 v[128:131], v21, s[8:11], s41 offen offset:1024 sc1\n"
"vd5_p0:\ns_cbranch_execnz tl15_p0\ns_nop 0\ns_nop 0\n"
"s_nop 0\ns_nop 0\ns_nop 0\ns_nop 0\n"
"s_nop 0\ns_nop 0\ns_nop 0\ns_nop 0\n"
"s_nop 0\ns_nop 0\ns_nop 0\ns_nop 0\n"
"s_nop 0\ns_nop 0\ns_nop 0\nva6_p0:\n"
"s_branch warm_p0\nsx6_p0:\ns_add_u32 s60, s60, 1848\ns_addc_u32 s61, s61, 0\n"
"s_setpc_b64 s[60:61]\nvb6_p0:\ns_branch vr6_p0\ns_nop 1\n"
"s_nop 1\ns_nop 1\ns_nop 1\ns_nop 1\n"
"s_nop 1\ns_nop 1\ns_nop 1\ns_nop 1\n"
"s_nop 1\nv_smfmac_f32_16x16x64_bf16 v[68:71], v[48:51], a[16:23], v8\nv_smfmac_f32_16x16x64_bf16 v[72:75], v[48:51], a[72:79], v8\nvr6_p0:\n"
"v_smfmac_f32_16x16x64_bf16 v[76:79], v[48:51], a[128:135], v8\nv_smfmac_f32_16x16x64_bf16 v[80:83], v[48:51], a[184:191], v8\nv_smfmac_f32_16x16x64_bf16 v[68:71], v[52:55], a[24:31], v8\nv_smfmac_f32_16x16x64_bf16 v[72:75], v[52:55], a[80:87], v8\n"
"s_cbranch_execz vc6x0_p0\nbuffer_load_dwordx4 v[116:119], v20, s[8:11], s41 offen offset:0 sc1\nbuffer_load_dwordx4 v[120:123], v20, s[8:11], s41 offen offset:1024 sc1\nv_smfmac_f32_16x16x64_bf16 v[76:79], v[52:55], a[136:143], v8\n"
"v_smfmac_f32_16x16x64_bf16 v[80:83], v[52:55], a[192:199], v8\nv_smfmac_f32_16x16x64_bf16 v[68:71], v[56:59], a[32:39], v8\nv_smfmac_f32_16x16x64_bf16 v[72:75], v[56:59], a[88:95], v8\nv_smfmac_f32_16x16x64_bf16 v[76:79], v[56:59], a[144:151], v8\n"
"vc6x0_p0:\ns_cbranch_execz vc6x1_p0\nv_smfmac_f32_16x16x64_bf16 v[80:83], v[56:59], a[200:207], v8\nv_smfmac_f32_16x16x64_bf16 v[68:71], v[60:63], a[40:47], v8\n"
"v_smfmac_f32_16x16x64_bf16 v[72:75], v[60:63], a[96:103], v8\nv_smfmac_f32_16x16x64_bf16 v[76:79], v[60:63], a[152:159], v8\nv_smfmac_f32_16x16x64_bf16 v[80:83], v[60:63], a[208:215], v8\nvc6x1_p0:\n"
"s_cbranch_execz vd6_p0\nbuffer_load_dwordx4 v[124:127], v21, s[8:11], s41 offen offset:0 sc1\nbuffer_load_dwordx4 v[128:131], v21, s[8:11], s41 offen offset:1024 sc1\ns_nop 1\n"
"vd6_p0:\ns_cbranch_execnz tl16_p0\ns_nop 0\ns_nop 0\n"
"s_nop 0\ns_nop 0\ns_nop 0\ns_nop 0\n"
"s_nop 0\ns_nop 0\ns_nop 0\ns_nop 0\n"
"s_nop 0\ns_nop 0\ns_nop 0\ns_nop 0\n"
"s_nop 0\ns_nop 0\nva7_p0:\ns_branch vb7_p0\n"
"sx7_p0:\ns_add_u32 s60, s60, 2156\ns_addc_u32 s61, s61, 0\ns_setpc_b64 s[60:61]\n"
"vb7_p0:\ns_branch vr7_p0\ns_nop 1\ns_nop 1\n"
"s_nop 1\ns_nop 1\ns_nop 1\ns_nop 1\n"
"s_nop 1\ns_nop 1\nv_smfmac_f32_16x16x64_bf16 v[68:71], v[48:51], a[16:23], v8\nv_smfmac_f32_16x16x64_bf16 v[72:75], v[48:51], a[72:79], v8\n"
"v_smfmac_f32_16x16x64_bf16 v[76:79], v[48:51], a[128:135], v8\nvr7_p0:\nv_smfmac_f32_16x16x64_bf16 v[80:83], v[48:51], a[184:191], v8\nv_smfmac_f32_16x16x64_bf16 v[68:71], v[52:55], a[24:31], v8\n"
"v_smfmac_f32_16x16x64_bf16 v[72:75], v[52:55], a[80:87], v8\nv_smfmac_f32_16x16x64_bf16 v[76:79], v[52:55], a[136:143], v8\ns_cbranch_execz vc7x0_p0\nbuffer_load_dwordx4 v[116:119], v20, s[8:11], s41 offen offset:0 sc1\n"
"buffer_load_dwordx4 v[120:123], v20, s[8:11], s41 offen offset:1024 sc1\nv_smfmac_f32_16x16x64_bf16 v[80:83], v[52:55], a[192:199], v8\nv_smfmac_f32_16x16x64_bf16 v[68:71], v[56:59], a[32:39], v8\nv_smfmac_f32_16x16x64_bf16 v[72:75], v[56:59], a[88:95], v8\n"
"v_smfmac_f32_16x16x64_bf16 v[76:79], v[56:59], a[144:151], v8\nv_smfmac_f32_16x16x64_bf16 v[80:83], v[56:59], a[200:207], v8\nvc7x0_p0:\ns_cbranch_execz vc7x1_p0\n"
"v_smfmac_f32_16x16x64_bf16 v[68:71], v[60:63], a[40:47], v8\nv_smfmac_f32_16x16x64_bf16 v[72:75], v[60:63], a[96:103], v8\nv_smfmac_f32_16x16x64_bf16 v[76:79], v[60:63], a[152:159], v8\nv_smfmac_f32_16x16x64_bf16 v[80:83], v[60:63], a[208:215], v8\n"
"v_smfmac_f32_16x16x64_bf16 v[68:71], v[64:67], a[48:55], v8\nvc7x1_p0:\ns_cbranch_execz vd7_p0\nbuffer_load_dwordx4 v[124:127], v21, s[8:11], s41 offen offset:0 sc1\n"
"buffer_load_dwordx4 v[128:131], v21, s[8:11], s41 offen offset:1024 sc1\ns_nop 5\nvd7_p0:\ns_cbranch_execnz tl17_p0\n"
"s_nop 0\ns_nop 0\ns_nop 0\ns_nop 0\n"
"s_nop 0\ns_nop 0\ns_nop 0\ns_nop 0\n"
"s_nop 0\ns_nop 0\ns_nop 0\ns_nop 0\n"
"s_nop 0\ns_nop 0\ns_nop 0\ns_nop 0\n"
"va8_p0:\ns_branch vb8_p0\nsx8_p0:\ns_add_u32 s60, s60, 2464\n"
"s_addc_u32 s61, s61, 0\ns_setpc_b64 s[60:61]\nvb8_p0:\ns_branch vr8_p0\n"
"s_nop 1\ns_nop 1\ns_nop 1\ns_nop 1\n"
"s_nop 1\ns_nop 1\nv_smfmac_f32_16x16x64_bf16 v[68:71], v[48:51], a[16:23], v8\nv_smfmac_f32_16x16x64_bf16 v[72:75], v[48:51], a[72:79], v8\n"
"v_smfmac_f32_16x16x64_bf16 v[76:79], v[48:51], a[128:135], v8\nv_smfmac_f32_16x16x64_bf16 v[80:83], v[48:51], a[184:191], v8\nvr8_p0:\nv_smfmac_f32_16x16x64_bf16 v[68:71], v[52:55], a[24:31], v8\n"
"v_smfmac_f32_16x16x64_bf16 v[72:75], v[52:55], a[80:87], v8\nv_smfmac_f32_16x16x64_bf16 v[76:79], v[52:55], a[136:143], v8\nv_smfmac_f32_16x16x64_bf16 v[80:83], v[52:55], a[192:199], v8\ns_cbranch_execz vc8x0_p0\n"
"buffer_load_dwordx4 v[116:119], v20, s[8:11], s41 offen offset:0 sc1\nbuffer_load_dwordx4 v[120:123], v20, s[8:11], s41 offen offset:1024 sc1\nv_smfmac_f32_16x16x64_bf16 v[68:71], v[56:59], a[32:39], v8\nv_smfmac_f32_16x16x64_bf16 v[72:75], v[56:59], a[88:95], v8\n"
"v_smfmac_f32_16x16x64_bf16 v[76:79], v[56:59], a[144:151], v8\nv_smfmac_f32_16x16x64_bf16 v[80:83], v[56:59], a[200:207], v8\nv_smfmac_f32_16x16x64_bf16 v[68:71], v[60:63], a[40:47], v8\nvc8x0_p0:\n"
"s_cbranch_execz vc8x1_p0\nv_smfmac_f32_16x16x64_bf16 v[72:75], v[60:63], a[96:103], v8\nv_smfmac_f32_16x16x64_bf16 v[76:79], v[60:63], a[152:159], v8\nv_smfmac_f32_16x16x64_bf16 v[80:83], v[60:63], a[208:215], v8\n"
"v_smfmac_f32_16x16x64_bf16 v[68:71], v[64:67], a[48:55], v8\nv_smfmac_f32_16x16x64_bf16 v[72:75], v[64:67], a[104:111], v8\nvc8x1_p0:\ns_cbranch_execz vd8_p0\n"
"buffer_load_dwordx4 v[124:127], v21, s[8:11], s41 offen offset:0 sc1\nbuffer_load_dwordx4 v[128:131], v21, s[8:11], s41 offen offset:1024 sc1\ns_nop 9\nvd8_p0:\n"
"s_cbranch_execnz tl18_p0\ns_nop 0\ns_nop 0\ns_nop 0\n"
"s_nop 0\ns_nop 0\ns_nop 0\ns_nop 0\n"
"s_nop 0\ns_nop 0\ns_nop 0\ns_nop 0\n"
"s_nop 0\ns_nop 0\ns_nop 0\ns_nop 0\n"
"s_nop 0\nva9_p0:\ns_branch vb9_p0\nsx9_p0:\n"
"s_add_u32 s60, s60, 2772\ns_addc_u32 s61, s61, 0\ns_setpc_b64 s[60:61]\nvb9_p0:\n"
"s_branch vr9_p0\ns_nop 1\ns_nop 1\ns_nop 1\n"
"s_nop 1\nv_smfmac_f32_16x16x64_bf16 v[68:71], v[48:51], a[16:23], v8\nv_smfmac_f32_16x16x64_bf16 v[72:75], v[48:51], a[72:79], v8\nv_smfmac_f32_16x16x64_bf16 v[76:79], v[48:51], a[128:135], v8\n"
"v_smfmac_f32_16x16x64_bf16 v[80:83], v[48:51], a[184:191], v8\nv_smfmac_f32_16x16x64_bf16 v[68:71], v[52:55], a[24:31], v8\nvr9_p0:\nv_smfmac_f32_16x16x64_bf16 v[72:75], v[52:55], a[80:87], v8\n"
"v_smfmac_f32_16x16x64_bf16 v[76:79], v[52:55], a[136:143], v8\nv_smfmac_f32_16x16x64_bf16 v[80:83], v[52:55], a[192:199], v8\nv_smfmac_f32_16x16x64_bf16 v[68:71], v[56:59], a[32:39], v8\ns_cbranch_execz vc9x0_p0\n"
"buffer_load_dwordx4 v[116:119], v20, s[8:11], s41 offen offset:0 sc1\nbuffer_load_dwordx4 v[120:123], v20, s[8:11], s41 offen offset:1024 sc1\nv_smfmac_f32_16x16x64_bf16 v[72:75], v[56:59], a[88:95], v8\nv_smfmac_f32_16x16x64_bf16 v[76:79], v[56:59], a[144:151], v8\n"
"v_smfmac_f32_16x16x64_bf16 v[80:83], v[56:59], a[200:207], v8\nv_smfmac_f32_16x16x64_bf16 v[68:71], v[60:63], a[40:47], v8\nv_smfmac_f32_16x16x64_bf16 v[72:75], v[60:63], a[96:103], v8\nvc9x0_p0:\n"
"s_cbranch_execz vc9x1_p0\nv_smfmac_f32_16x16x64_bf16 v[76:79], v[60:63], a[152:159], v8\nv_smfmac_f32_16x16x64_bf16 v[80:83], v[60:63], a[208:215], v8\nv_smfmac_f32_16x16x64_bf16 v[68:71], v[64:67], a[48:55], v8\n"
"v_smfmac_f32_16x16x64_bf16 v[72:75], v[64:67], a[104:111], v8\nv_smfmac_f32_16x16x64_bf16 v[76:79], v[64:67], a[160:167], v8\nvc9x1_p0:\ns_cbranch_execz vd9_p0\n"
"buffer_load_dwordx4 v[124:127], v21, s[8:11], s41 offen offset:0 sc1\nbuffer_load_dwordx4 v[128:131], v21, s[8:11], s41 offen offset:1024 sc1\ns_nop 13\nvd9_p0:\n"
"s_cbranch_execnz tl19_p0\ns_nop 0\ns_nop 0\ns_nop 0\n"
"s_nop 0\ns_nop 0\ns_nop 0\ns_nop 0\n"
"s_nop 0\ns_nop 0\ns_nop 0\ns_nop 0\n"
"s_nop 0\ns_nop 0\ns_nop 0\ns_nop 0\n"
"s_nop 0\nva10_p0:\ns_branch vb10_p0\nsx10_p0:\n"
"s_add_u32 s60, s60, 3080\ns_addc_u32 s61, s61, 0\ns_setpc_b64 s[60:61]\nvb10_p0:\n"
"s_branch vr10_p0\ns_nop 1\ns_nop 1\nv_smfmac_f32_16x16x64_bf16 v[68:71], v[48:51], a[16:23], v8\n"
"v_smfmac_f32_16x16x64_bf16 v[72:75], v[48:51], a[72:79], v8\nv_smfmac_f32_16x16x64_bf16 v[76:79], v[48:51], a[128:135], v8\nv_smfmac_f32_16x16x64_bf16 v[80:83], v[48:51], a[184:191], v8\nv_smfmac_f32_16x16x64_bf16 v[68:71], v[52:55], a[24:31], v8\n"
"v_smfmac_f32_16x16x64_bf16 v[72:75], v[52:55], a[80:87], v8\nvr10_p0:\nv_smfmac_f32_16x16x64_bf16 v[76:79], v[52:55], a[136:143], v8\nv_smfmac_f32_16x16x64_bf16 v[80:83], v[52:55], a[192:199], v8\n"
"v_smfmac_f32_16x16x64_bf16 v[68:71], v[56:59], a[32:39], v8\nv_smfmac_f32_16x16x64_bf16 v[72:75], v[56:59], a[88:95], v8\ns_cbranch_execz vc10x0_p0\nbuffer_load_dwordx4 v[116:119], v20, s[8:11], s41 offen offset:0 sc1\n"
"buffer_load_dwordx4 v[120:123], v20, s[8:11], s41 offen offset:1024 sc1\nv_smfmac_f32_16x16x64_bf16 v[76:79], v[56:59], a[144:151], v8\nv_smfmac_f32_16x16x64_bf16 v[80:83], v[56:59], a[200:207], v8\nv_smfmac_f32_16x16x64_bf16 v[68:71], v[60:63], a[40:47], v8\n"
"v_smfmac_f32_16x16x64_bf16 v[72:75], v[60:63], a[96:103], v8\nv_smfmac_f32_16x16x64_bf16 v[76:79], v[60:63], a[152:159], v8\nvc10x0_p0:\ns_cbranch_execz vc10x1_p0\n"
"v_smfmac_f32_16x16x64_bf16 v[80:83], v[60:63], a[208:215], v8\nv_smfmac_f32_16x16x64_bf16 v[68:71], v[64:67], a[48:55], v8\nv_smfmac_f32_16x16x64_bf16 v[72:75], v[64:67], a[104:111], v8\nv_smfmac_f32_16x16x64_bf16 v[76:79], v[64:67], a[160:167], v8\n"
"v_smfmac_f32_16x16x64_bf16 v[80:83], v[64:67], a[216:223], v8\nvc10x1_p0:\ns_cbranch_execz vd10_p0\nbuffer_load_dwordx4 v[124:127], v21, s[8:11], s41 offen offset:0 sc1\n"
"buffer_load_dwordx4 v[128:131], v21, s[8:11], s41 offen offset:1024 sc1\ns_nop 15\ns_nop 1\nvd10_p0:\n"
"s_cbranch_execnz tl20_p0\ns_nop 0\ns_nop 0\ns_nop 0\n"
"s_nop 0\ns_nop 0\ns_nop 0\ns_nop 0\n"
"s_nop 0\ns_nop 0\ns_nop 0\ns_nop 0\n"
"s_nop 0\ns_nop 0\ns_nop 0\ns_nop 0\n"
"va11_p0:\ns_branch vb11_p0\nsx11_p0:\ns_add_u32 s60, s60, 3388\n"
"s_addc_u32 s61, s61, 0\ns_setpc_b64 s[60:61]\nvb11_p0:\ns_branch vr11_p0\n"
"v_smfmac_f32_16x16x64_bf16 v[68:71], v[48:51], a[16:23], v8\nv_smfmac_f32_16x16x64_bf16 v[72:75], v[48:51], a[72:79], v8\nv_smfmac_f32_16x16x64_bf16 v[76:79], v[48:51], a[128:135], v8\nv_smfmac_f32_16x16x64_bf16 v[80:83], v[48:51], a[184:191], v8\n"
"v_smfmac_f32_16x16x64_bf16 v[68:71], v[52:55], a[24:31], v8\nv_smfmac_f32_16x16x64_bf16 v[72:75], v[52:55], a[80:87], v8\nv_smfmac_f32_16x16x64_bf16 v[76:79], v[52:55], a[136:143], v8\nvr11_p0:\n"
"v_smfmac_f32_16x16x64_bf16 v[80:83], v[52:55], a[192:199], v8\nv_smfmac_f32_16x16x64_bf16 v[68:71], v[56:59], a[32:39], v8\nv_smfmac_f32_16x16x64_bf16 v[72:75], v[56:59], a[88:95], v8\nv_smfmac_f32_16x16x64_bf16 v[76:79], v[56:59], a[144:151], v8\n"
"s_cbranch_execz vc11x0_p0\nbuffer_load_dwordx4 v[116:119], v20, s[8:11], s41 offen offset:0 sc1\nbuffer_load_dwordx4 v[120:123], v20, s[8:11], s41 offen offset:1024 sc1\nv_smfmac_f32_16x16x64_bf16 v[80:83], v[56:59], a[200:207], v8\n"
"v_smfmac_f32_16x16x64_bf16 v[68:71], v[60:63], a[40:47], v8\nv_smfmac_f32_16x16x64_bf16 v[72:75], v[60:63], a[96:103], v8\nv_smfmac_f32_16x16x64_bf16 v[76:79], v[60:63], a[152:159], v8\nv_smfmac_f32_16x16x64_bf16 v[80:83], v[60:63], a[208:215], v8\n"
"vc11x0_p0:\ns_cbranch_execz vc11x1_p0\nv_smfmac_f32_16x16x64_bf16 v[68:71], v[64:67], a[48:55], v8\nv_smfmac_f32_16x16x64_bf16 v[72:75], v[64:67], a[104:111], v8\n"
"v_smfmac_f32_16x16x64_bf16 v[76:79], v[64:67], a[160:167], v8\nv_smfmac_f32_16x16x64_bf16 v[80:83], v[64:67], a[216:223], v8\nv_add_f32 v219, v68, v69\nv_add_f32 v220, v70, v71\n"
"v_cvt_pk_bf16_f32 v156, v219, v220\ns_nop 0\nvc11x1_p0:\ns_cbranch_execz vd11_p0\n"
"buffer_load_dwordx4 v[124:127], v21, s[8:11], s41 offen offset:0 sc1\nbuffer_load_dwordx4 v[128:131], v21, s[8:11], s41 offen offset:1024 sc1\ns_nop 15\ns_nop 4\n"
"vd11_p0:\ns_cbranch_execnz tl21_p0\ns_nop 0\ns_nop 0\n"
"s_nop 0\ns_nop 0\ns_nop 0\ns_nop 0\n"
"s_nop 0\ns_nop 0\ns_nop 0\ns_nop 0\n"
"s_nop 0\ns_nop 0\nva12_p0:\ns_branch warm_p0\n"
"sx12_p0:\ns_add_u32 s60, s60, 3696\ns_addc_u32 s61, s61, 0\ns_setpc_b64 s[60:61]\n"
"vb12_p0:\ns_branch vr12_p0\nv_smfmac_f32_16x16x64_bf16 v[72:75], v[48:51], a[72:79], v8\nv_smfmac_f32_16x16x64_bf16 v[76:79], v[48:51], a[128:135], v8\n"
"v_smfmac_f32_16x16x64_bf16 v[80:83], v[48:51], a[184:191], v8\nv_smfmac_f32_16x16x64_bf16 v[68:71], v[52:55], a[24:31], v8\nv_smfmac_f32_16x16x64_bf16 v[72:75], v[52:55], a[80:87], v8\nv_smfmac_f32_16x16x64_bf16 v[76:79], v[52:55], a[136:143], v8\n"
"v_smfmac_f32_16x16x64_bf16 v[80:83], v[52:55], a[192:199], v8\nvr12_p0:\nv_smfmac_f32_16x16x64_bf16 v[68:71], v[56:59], a[32:39], v8\nv_smfmac_f32_16x16x64_bf16 v[72:75], v[56:59], a[88:95], v8\n"
"v_smfmac_f32_16x16x64_bf16 v[76:79], v[56:59], a[144:151], v8\nv_smfmac_f32_16x16x64_bf16 v[80:83], v[56:59], a[200:207], v8\ns_cbranch_execz vc12x0_p0\nbuffer_load_dwordx4 v[116:119], v20, s[8:11], s41 offen offset:0 sc1\n"
"buffer_load_dwordx4 v[120:123], v20, s[8:11], s41 offen offset:1024 sc1\nv_smfmac_f32_16x16x64_bf16 v[68:71], v[60:63], a[40:47], v8\nv_smfmac_f32_16x16x64_bf16 v[72:75], v[60:63], a[96:103], v8\nv_smfmac_f32_16x16x64_bf16 v[76:79], v[60:63], a[152:159], v8\n"
"v_smfmac_f32_16x16x64_bf16 v[80:83], v[60:63], a[208:215], v8\nv_smfmac_f32_16x16x64_bf16 v[68:71], v[64:67], a[48:55], v8\nvc12x0_p0:\ns_cbranch_execz vc12x1_p0\n"
"v_smfmac_f32_16x16x64_bf16 v[72:75], v[64:67], a[104:111], v8\nv_smfmac_f32_16x16x64_bf16 v[76:79], v[64:67], a[160:167], v8\nv_smfmac_f32_16x16x64_bf16 v[80:83], v[64:67], a[216:223], v8\nv_add_f32 v219, v68, v69\n"
"v_add_f32 v220, v70, v71\nv_cvt_pk_bf16_f32 v156, v219, v220\ns_nop 0\nvc12x1_p0:\n"
"s_cbranch_execz vc12x2_p0\nv_add_f32 v219, v72, v73\nv_add_f32 v220, v74, v75\nv_cvt_pk_bf16_f32 v157, v219, v220\n"
"s_nop 0\nvc12x2_p0:\ns_cbranch_execz vd12_p0\nbuffer_load_dwordx4 v[124:127], v21, s[8:11], s41 offen offset:0 sc1\n"
"buffer_load_dwordx4 v[128:131], v21, s[8:11], s41 offen offset:1024 sc1\ns_nop 15\ns_nop 7\nvd12_p0:\n"
"s_cbranch_execnz tl22_p0\ns_nop 0\ns_nop 0\ns_nop 0\n"
"s_nop 0\ns_nop 0\ns_nop 0\ns_nop 0\n"
"s_nop 0\nva13_p0:\ns_branch vb13_p0\nsx13_p0:\n"
"s_add_u32 s60, s60, 4004\ns_addc_u32 s61, s61, 0\ns_setpc_b64 s[60:61]\nvb13_p0:\n"
"s_branch vr13_p0\nv_smfmac_f32_16x16x64_bf16 v[76:79], v[48:51], a[128:135], v8\nv_smfmac_f32_16x16x64_bf16 v[80:83], v[48:51], a[184:191], v8\nv_smfmac_f32_16x16x64_bf16 v[68:71], v[52:55], a[24:31], v8\n"
"v_smfmac_f32_16x16x64_bf16 v[72:75], v[52:55], a[80:87], v8\nv_smfmac_f32_16x16x64_bf16 v[76:79], v[52:55], a[136:143], v8\nv_smfmac_f32_16x16x64_bf16 v[80:83], v[52:55], a[192:199], v8\nv_smfmac_f32_16x16x64_bf16 v[68:71], v[56:59], a[32:39], v8\n"
"vr13_p0:\nv_smfmac_f32_16x16x64_bf16 v[72:75], v[56:59], a[88:95], v8\nv_smfmac_f32_16x16x64_bf16 v[76:79], v[56:59], a[144:151], v8\nv_smfmac_f32_16x16x64_bf16 v[80:83], v[56:59], a[200:207], v8\n"
"v_smfmac_f32_16x16x64_bf16 v[68:71], v[60:63], a[40:47], v8\ns_cbranch_execz vc13x0_p0\nbuffer_load_dwordx4 v[116:119], v20, s[8:11], s41 offen offset:0 sc1\nbuffer_load_dwordx4 v[120:123], v20, s[8:11], s41 offen offset:1024 sc1\n"
"v_smfmac_f32_16x16x64_bf16 v[72:75], v[60:63], a[96:103], v8\nv_smfmac_f32_16x16x64_bf16 v[76:79], v[60:63], a[152:159], v8\nv_smfmac_f32_16x16x64_bf16 v[80:83], v[60:63], a[208:215], v8\nv_smfmac_f32_16x16x64_bf16 v[68:71], v[64:67], a[48:55], v8\n"
"v_smfmac_f32_16x16x64_bf16 v[72:75], v[64:67], a[104:111], v8\nvc13x0_p0:\ns_cbranch_execz vc13x1_p0\nv_smfmac_f32_16x16x64_bf16 v[76:79], v[64:67], a[160:167], v8\n"
"v_smfmac_f32_16x16x64_bf16 v[80:83], v[64:67], a[216:223], v8\nv_add_f32 v219, v68, v69\nv_add_f32 v220, v70, v71\nv_cvt_pk_bf16_f32 v156, v219, v220\n"
"s_nop 0\nv_add_f32 v219, v72, v73\nv_add_f32 v220, v74, v75\nv_cvt_pk_bf16_f32 v157, v219, v220\n"
"s_nop 0\nvc13x1_p0:\ns_cbranch_execz vc13x2_p0\nv_add_f32 v219, v76, v77\n"
"v_add_f32 v220, v78, v79\nv_cvt_pk_bf16_f32 v158, v219, v220\ns_nop 0\nvc13x2_p0:\n"
"s_cbranch_execz vd13_p0\nbuffer_load_dwordx4 v[124:127], v21, s[8:11], s41 offen offset:0 sc1\nbuffer_load_dwordx4 v[128:131], v21, s[8:11], s41 offen offset:1024 sc1\ns_nop 15\n"
"s_nop 10\nvd13_p0:\ns_cbranch_execnz tl23_p0\ns_nop 0\n"
"s_nop 0\ns_nop 0\ns_nop 0\ns_nop 0\n"
"va14_p0:\ns_branch vb14_p0\nsx14_p0:\ns_add_u32 s60, s60, 4312\n"
"s_addc_u32 s61, s61, 0\ns_setpc_b64 s[60:61]\nvb14_p0:\ns_branch vr14_p0\n"
"v_smfmac_f32_16x16x64_bf16 v[80:83], v[48:51], a[184:191], v8\nv_smfmac_f32_16x16x64_bf16 v[68:71], v[52:55], a[24:31], v8\nv_smfmac_f32_16x16x64_bf16 v[72:75], v[52:55], a[80:87], v8\nv_smfmac_f32_16x16x64_bf16 v[76:79], v[52:55], a[136:143], v8\n"
"v_smfmac_f32_16x16x64_bf16 v[80:83], v[52:55], a[192:199], v8\nv_smfmac_f32_16x16x64_bf16 v[68:71], v[56:59], a[32:39], v8\nv_smfmac_f32_16x16x64_bf16 v[72:75], v[56:59], a[88:95], v8\nvr14_p0:\n"
"v_smfmac_f32_16x16x64_bf16 v[76:79], v[56:59], a[144:151], v8\nv_smfmac_f32_16x16x64_bf16 v[80:83], v[56:59], a[200:207], v8\nv_smfmac_f32_16x16x64_bf16 v[68:71], v[60:63], a[40:47], v8\nv_smfmac_f32_16x16x64_bf16 v[72:75], v[60:63], a[96:103], v8\n"
"s_cbranch_execz vc14x0_p0\nbuffer_load_dwordx4 v[116:119], v20, s[8:11], s41 offen offset:0 sc1\nbuffer_load_dwordx4 v[120:123], v20, s[8:11], s41 offen offset:1024 sc1\nv_smfmac_f32_16x16x64_bf16 v[76:79], v[60:63], a[152:159], v8\n"
"v_smfmac_f32_16x16x64_bf16 v[80:83], v[60:63], a[208:215], v8\nv_smfmac_f32_16x16x64_bf16 v[68:71], v[64:67], a[48:55], v8\nv_smfmac_f32_16x16x64_bf16 v[72:75], v[64:67], a[104:111], v8\nv_smfmac_f32_16x16x64_bf16 v[76:79], v[64:67], a[160:167], v8\n"
"vc14x0_p0:\ns_cbranch_execz vc14x1_p0\nv_smfmac_f32_16x16x64_bf16 v[80:83], v[64:67], a[216:223], v8\nv_add_f32 v219, v68, v69\n"
"v_add_f32 v220, v70, v71\nv_cvt_pk_bf16_f32 v156, v219, v220\ns_nop 0\nv_add_f32 v219, v72, v73\n"
"v_add_f32 v220, v74, v75\nv_cvt_pk_bf16_f32 v157, v219, v220\ns_nop 0\nvc14x1_p0:\n"
"s_cbranch_execz vc14x2_p0\nv_add_f32 v219, v76, v77\nv_add_f32 v220, v78, v79\nv_cvt_pk_bf16_f32 v158, v219, v220\n"
"s_nop 0\nv_add_f32 v219, v80, v81\nv_add_f32 v220, v82, v83\nv_cvt_pk_bf16_f32 v159, v219, v220\n"
"s_nop 0\nvc14x2_p0:\ns_cbranch_execz vd14_p0\nbuffer_load_dwordx4 v[124:127], v21, s[8:11], s41 offen offset:0 sc1\n"
"buffer_load_dwordx4 v[128:131], v21, s[8:11], s41 offen offset:1024 sc1\ns_nop 15\ns_nop 13\nvd14_p0:\n"
"s_cbranch_execnz tl24_p0\ns_nop 0\ns_nop 0\nva15_p0:\n"
"s_branch vb15_p0\nsx15_p0:\ns_add_u32 s60, s60, 4620\ns_addc_u32 s61, s61, 0\n"
"s_setpc_b64 s[60:61]\nvb15_p0:\ns_branch vr15_p0\nv_smfmac_f32_16x16x64_bf16 v[68:71], v[52:55], a[24:31], v8\n"
"v_smfmac_f32_16x16x64_bf16 v[72:75], v[52:55], a[80:87], v8\nv_smfmac_f32_16x16x64_bf16 v[76:79], v[52:55], a[136:143], v8\nv_smfmac_f32_16x16x64_bf16 v[80:83], v[52:55], a[192:199], v8\nv_smfmac_f32_16x16x64_bf16 v[68:71], v[56:59], a[32:39], v8\n"
"v_smfmac_f32_16x16x64_bf16 v[72:75], v[56:59], a[88:95], v8\nv_smfmac_f32_16x16x64_bf16 v[76:79], v[56:59], a[144:151], v8\nvr15_p0:\nv_smfmac_f32_16x16x64_bf16 v[80:83], v[56:59], a[200:207], v8\n"
"v_smfmac_f32_16x16x64_bf16 v[68:71], v[60:63], a[40:47], v8\nv_smfmac_f32_16x16x64_bf16 v[72:75], v[60:63], a[96:103], v8\nv_smfmac_f32_16x16x64_bf16 v[76:79], v[60:63], a[152:159], v8\ns_cbranch_execz vc15x0_p0\n"
"buffer_load_dwordx4 v[116:119], v20, s[8:11], s41 offen offset:0 sc1\nbuffer_load_dwordx4 v[120:123], v20, s[8:11], s41 offen offset:1024 sc1\nv_smfmac_f32_16x16x64_bf16 v[80:83], v[60:63], a[208:215], v8\nv_smfmac_f32_16x16x64_bf16 v[68:71], v[64:67], a[48:55], v8\n"
"v_smfmac_f32_16x16x64_bf16 v[72:75], v[64:67], a[104:111], v8\nv_smfmac_f32_16x16x64_bf16 v[76:79], v[64:67], a[160:167], v8\nv_smfmac_f32_16x16x64_bf16 v[80:83], v[64:67], a[216:223], v8\nvc15x0_p0:\n"
"s_cbranch_execz vc15x1_p0\nv_add_f32 v219, v68, v69\nv_add_f32 v220, v70, v71\nv_cvt_pk_bf16_f32 v156, v219, v220\n"
"s_nop 0\nv_add_f32 v219, v72, v73\nv_add_f32 v220, v74, v75\nv_cvt_pk_bf16_f32 v157, v219, v220\n"
"s_nop 0\nvc15x1_p0:\ns_cbranch_execz vc15x2_p0\nv_add_f32 v219, v76, v77\n"
"v_add_f32 v220, v78, v79\nv_cvt_pk_bf16_f32 v158, v219, v220\ns_nop 0\nv_add_f32 v219, v80, v81\n"
"v_add_f32 v220, v82, v83\nv_cvt_pk_bf16_f32 v159, v219, v220\ns_nop 0\ns_nop 1\n"
"s_nop 1\nvc15x2_p0:\ns_cbranch_execz vd15_p0\nbuffer_load_dwordx4 v[124:127], v21, s[8:11], s41 offen offset:0 sc1\n"
"buffer_load_dwordx4 v[128:131], v21, s[8:11], s41 offen offset:1024 sc1\ns_nop 15\ns_nop 13\nvd15_p0:\n"
"s_cbranch_execnz tl24_p0\ns_nop 0\ns_nop 0\nva16_p0:\n"
"s_branch vb16_p0\nsx16_p0:\ns_add_u32 s60, s60, 4928\ns_addc_u32 s61, s61, 0\n"
"s_setpc_b64 s[60:61]\nvb16_p0:\ns_branch vr16_p0\nv_smfmac_f32_16x16x64_bf16 v[72:75], v[52:55], a[80:87], v8\n"
"v_smfmac_f32_16x16x64_bf16 v[76:79], v[52:55], a[136:143], v8\nv_smfmac_f32_16x16x64_bf16 v[80:83], v[52:55], a[192:199], v8\nv_smfmac_f32_16x16x64_bf16 v[68:71], v[56:59], a[32:39], v8\nv_smfmac_f32_16x16x64_bf16 v[72:75], v[56:59], a[88:95], v8\n"
"v_smfmac_f32_16x16x64_bf16 v[76:79], v[56:59], a[144:151], v8\nv_smfmac_f32_16x16x64_bf16 v[80:83], v[56:59], a[200:207], v8\nvr16_p0:\nv_smfmac_f32_16x16x64_bf16 v[68:71], v[60:63], a[40:47], v8\n"
"v_smfmac_f32_16x16x64_bf16 v[72:75], v[60:63], a[96:103], v8\nv_smfmac_f32_16x16x64_bf16 v[76:79], v[60:63], a[152:159], v8\nv_smfmac_f32_16x16x64_bf16 v[80:83], v[60:63], a[208:215], v8\ns_cbranch_execz vc16x0_p0\n"
"buffer_load_dwordx4 v[116:119], v20, s[8:11], s41 offen offset:0 sc1\nbuffer_load_dwordx4 v[120:123], v20, s[8:11], s41 offen offset:1024 sc1\nv_smfmac_f32_16x16x64_bf16 v[68:71], v[64:67], a[48:55], v8\nv_smfmac_f32_16x16x64_bf16 v[72:75], v[64:67], a[104:111], v8\n"
"v_smfmac_f32_16x16x64_bf16 v[76:79], v[64:67], a[160:167], v8\nv_smfmac_f32_16x16x64_bf16 v[80:83], v[64:67], a[216:223], v8\nvc16x0_p0:\ns_cbranch_execz vc16x1_p0\n"
"v_add_f32 v219, v68, v69\nv_add_f32 v220, v70, v71\nv_cvt_pk_bf16_f32 v156, v219, v220\ns_nop 0\n"
"v_add_f32 v219, v72, v73\nv_add_f32 v220, v74, v75\nv_cvt_pk_bf16_f32 v157, v219, v220\ns_nop 0\n"
"vc16x1_p0:\ns_cbranch_execz vc16x2_p0\nv_add_f32 v219, v76, v77\nv_add_f32 v220, v78, v79\n"
"v_cvt_pk_bf16_f32 v158, v219, v220\ns_nop 0\nv_add_f32 v219, v80, v81\nv_add_f32 v220, v82, v83\n"
"v_cvt_pk_bf16_f32 v159, v219, v220\ns_nop 0\ns_nop 1\ns_nop 1\n"
"s_nop 1\ns_nop 1\nvc16x2_p0:\ns_cbranch_execz vd16_p0\n"
"buffer_load_dwordx4 v[124:127], v21, s[8:11], s41 offen offset:0 sc1\nbuffer_load_dwordx4 v[128:131], v21, s[8:11], s41 offen offset:1024 sc1\ns_nop 15\ns_nop 13\n"
"vd16_p0:\ns_cbranch_execnz tl24_p0\ns_nop 0\ns_nop 0\n"
"va17_p0:\ns_branch vb17_p0\nsx17_p0:\ns_add_u32 s60, s60, 5236\n"
"s_addc_u32 s61, s61, 0\ns_setpc_b64 s[60:61]\nvb17_p0:\ns_branch vr17_p0\n"
"v_smfmac_f32_16x16x64_bf16 v[76:79], v[52:55], a[136:143], v8\nv_smfmac_f32_16x16x64_bf16 v[80:83], v[52:55], a[192:199], v8\nv_smfmac_f32_16x16x64_bf16 v[68:71], v[56:59], a[32:39], v8\nv_smfmac_f32_16x16x64_bf16 v[72:75], v[56:59], a[88:95], v8\n"
"v_smfmac_f32_16x16x64_bf16 v[76:79], v[56:59], a[144:151], v8\nv_smfmac_f32_16x16x64_bf16 v[80:83], v[56:59], a[200:207], v8\nv_smfmac_f32_16x16x64_bf16 v[68:71], v[60:63], a[40:47], v8\nvr17_p0:\n"
"v_smfmac_f32_16x16x64_bf16 v[72:75], v[60:63], a[96:103], v8\nv_smfmac_f32_16x16x64_bf16 v[76:79], v[60:63], a[152:159], v8\nv_smfmac_f32_16x16x64_bf16 v[80:83], v[60:63], a[208:215], v8\nv_smfmac_f32_16x16x64_bf16 v[68:71], v[64:67], a[48:55], v8\n"
"s_cbranch_execz vc17x0_p0\nbuffer_load_dwordx4 v[116:119], v20, s[8:11], s41 offen offset:0 sc1\nbuffer_load_dwordx4 v[120:123], v20, s[8:11], s41 offen offset:1024 sc1\nv_smfmac_f32_16x16x64_bf16 v[72:75], v[64:67], a[104:111], v8\n"
"v_smfmac_f32_16x16x64_bf16 v[76:79], v[64:67], a[160:167], v8\nv_smfmac_f32_16x16x64_bf16 v[80:83], v[64:67], a[216:223], v8\nvc17x0_p0:\ns_cbranch_execz vc17x1_p0\n"
"v_add_f32 v219, v68, v69\nv_add_f32 v220, v70, v71\nv_cvt_pk_bf16_f32 v156, v219, v220\ns_nop 0\n"
"v_add_f32 v219, v72, v73\nv_add_f32 v220, v74, v75\nv_cvt_pk_bf16_f32 v157, v219, v220\ns_nop 0\n"
"vc17x1_p0:\ns_cbranch_execz vc17x2_p0\nv_add_f32 v219, v76, v77\nv_add_f32 v220, v78, v79\n"
"v_cvt_pk_bf16_f32 v158, v219, v220\ns_nop 0\nv_add_f32 v219, v80, v81\nv_add_f32 v220, v82, v83\n"
"v_cvt_pk_bf16_f32 v159, v219, v220\ns_nop 0\ns_nop 1\ns_nop 1\n"
"s_nop 1\ns_nop 1\nvc17x2_p0:\ns_cbranch_execz vc17x3_p0\n"
"s_nop 1\ns_nop 1\nvc17x3_p0:\ns_cbranch_execz vd17_p0\n"
"buffer_load_dwordx4 v[124:127], v21, s[8:11], s41 offen offset:0 sc1\nbuffer_load_dwordx4 v[128:131], v21, s[8:11], s41 offen offset:1024 sc1\ns_nop 15\ns_nop 13\n"
"vd17_p0:\ns_cbranch_execnz tl24_p0\ns_nop 0\nva18_p0:\n"
"s_branch warm_p0\nsx18_p0:\ns_add_u32 s60, s60, 5544\ns_addc_u32 s61, s61, 0\n"
"s_setpc_b64 s[60:61]\nvb18_p0:\ns_branch vr18_p0\nv_smfmac_f32_16x16x64_bf16 v[80:83], v[52:55], a[192:199], v8\n"
"v_smfmac_f32_16x16x64_bf16 v[68:71], v[56:59], a[32:39], v8\nv_smfmac_f32_16x16x64_bf16 v[72:75], v[56:59], a[88:95], v8\nv_smfmac_f32_16x16x64_bf16 v[76:79], v[56:59], a[144:151], v8\nv_smfmac_f32_16x16x64_bf16 v[80:83], v[56:59], a[200:207], v8\n"
"v_smfmac_f32_16x16x64_bf16 v[68:71], v[60:63], a[40:47], v8\nv_smfmac_f32_16x16x64_bf16 v[72:75], v[60:63], a[96:103], v8\nvr18_p0:\nv_smfmac_f32_16x16x64_bf16 v[76:79], v[60:63], a[152:159], v8\n"
"v_smfmac_f32_16x16x64_bf16 v[80:83], v[60:63], a[208:215], v8\nv_smfmac_f32_16x16x64_bf16 v[68:71], v[64:67], a[48:55], v8\nv_smfmac_f32_16x16x64_bf16 v[72:75], v[64:67], a[104:111], v8\ns_cbranch_execz vc18x0_p0\n"
"buffer_load_dwordx4 v[116:119], v20, s[8:11], s41 offen offset:0 sc1\nbuffer_load_dwordx4 v[120:123], v20, s[8:11], s41 offen offset:1024 sc1\nv_smfmac_f32_16x16x64_bf16 v[76:79], v[64:67], a[160:167], v8\nv_smfmac_f32_16x16x64_bf16 v[80:83], v[64:67], a[216:223], v8\n"
"v_add_f32 v219, v68, v69\nv_add_f32 v220, v70, v71\nv_cvt_pk_bf16_f32 v156, v219, v220\ns_nop 0\n"
"vc18x0_p0:\ns_cbranch_execz vc18x1_p0\nv_add_f32 v219, v72, v73\nv_add_f32 v220, v74, v75\n"
"v_cvt_pk_bf16_f32 v157, v219, v220\ns_nop 0\nv_add_f32 v219, v76, v77\nv_add_f32 v220, v78, v79\n"
"v_cvt_pk_bf16_f32 v158, v219, v220\ns_nop 0\nvc18x1_p0:\ns_cbranch_execz vc18x2_p0\n"
"v_add_f32 v219, v80, v81\nv_add_f32 v220, v82, v83\nv_cvt_pk_bf16_f32 v159, v219, v220\ns_nop 0\n"
"s_nop 1\ns_nop 1\ns_nop 1\ns_nop 1\n"
"s_nop 1\ns_nop 1\ns_nop 1\ns_nop 1\n"
"vc18x2_p0:\ns_cbranch_execz vd18_p0\nbuffer_load_dwordx4 v[124:127], v21, s[8:11], s41 offen offset:0 sc1\nbuffer_load_dwordx4 v[128:131], v21, s[8:11], s41 offen offset:1024 sc1\n"
"s_nop 15\ns_nop 13\nvd18_p0:\ns_cbranch_execnz tl24_p0\n"
"s_nop 0\ns_nop 0\nva19_p0:\ns_branch vb19_p0\n"
"sx19_p0:\ns_add_u32 s60, s60, 5852\ns_addc_u32 s61, s61, 0\ns_setpc_b64 s[60:61]\n"
"vb19_p0:\ns_branch vr19_p0\nv_smfmac_f32_16x16x64_bf16 v[68:71], v[56:59], a[32:39], v8\nv_smfmac_f32_16x16x64_bf16 v[72:75], v[56:59], a[88:95], v8\n"
"v_smfmac_f32_16x16x64_bf16 v[76:79], v[56:59], a[144:151], v8\nv_smfmac_f32_16x16x64_bf16 v[80:83], v[56:59], a[200:207], v8\nv_smfmac_f32_16x16x64_bf16 v[68:71], v[60:63], a[40:47], v8\nv_smfmac_f32_16x16x64_bf16 v[72:75], v[60:63], a[96:103], v8\n"
"v_smfmac_f32_16x16x64_bf16 v[76:79], v[60:63], a[152:159], v8\nvr19_p0:\nv_smfmac_f32_16x16x64_bf16 v[80:83], v[60:63], a[208:215], v8\nv_smfmac_f32_16x16x64_bf16 v[68:71], v[64:67], a[48:55], v8\n"
"v_smfmac_f32_16x16x64_bf16 v[72:75], v[64:67], a[104:111], v8\nv_smfmac_f32_16x16x64_bf16 v[76:79], v[64:67], a[160:167], v8\ns_cbranch_execz vc19x0_p0\nbuffer_load_dwordx4 v[116:119], v20, s[8:11], s41 offen offset:0 sc1\n"
"buffer_load_dwordx4 v[120:123], v20, s[8:11], s41 offen offset:1024 sc1\nv_smfmac_f32_16x16x64_bf16 v[80:83], v[64:67], a[216:223], v8\nv_add_f32 v219, v68, v69\nv_add_f32 v220, v70, v71\n"
"v_cvt_pk_bf16_f32 v156, v219, v220\ns_nop 0\nvc19x0_p0:\ns_cbranch_execz vc19x1_p0\n"
"v_add_f32 v219, v72, v73\nv_add_f32 v220, v74, v75\nv_cvt_pk_bf16_f32 v157, v219, v220\ns_nop 0\n"
"v_add_f32 v219, v76, v77\nv_add_f32 v220, v78, v79\nv_cvt_pk_bf16_f32 v158, v219, v220\ns_nop 0\n"
"vc19x1_p0:\ns_cbranch_execz vc19x2_p0\nv_add_f32 v219, v80, v81\nv_add_f32 v220, v82, v83\n"
"v_cvt_pk_bf16_f32 v159, v219, v220\ns_nop 0\ns_nop 1\ns_nop 1\n"
"s_nop 1\ns_nop 1\ns_nop 1\ns_nop 1\n"
"s_nop 1\ns_nop 1\nvc19x2_p0:\ns_cbranch_execz vc19x3_p0\n"
"s_nop 1\ns_nop 1\nvc19x3_p0:\ns_cbranch_execz vd19_p0\n"
"buffer_load_dwordx4 v[124:127], v21, s[8:11], s41 offen offset:0 sc1\nbuffer_load_dwordx4 v[128:131], v21, s[8:11], s41 offen offset:1024 sc1\ns_nop 15\ns_nop 13\n"
"vd19_p0:\ns_cbranch_execnz tl24_p0\ns_nop 0\nva20_p0:\n"
"s_branch vb20_p0\nsx20_p0:\ns_add_u32 s60, s60, 6160\ns_addc_u32 s61, s61, 0\n"
"s_setpc_b64 s[60:61]\nvb20_p0:\ns_branch vr20_p0\nv_smfmac_f32_16x16x64_bf16 v[72:75], v[56:59], a[88:95], v8\n"
"v_smfmac_f32_16x16x64_bf16 v[76:79], v[56:59], a[144:151], v8\nv_smfmac_f32_16x16x64_bf16 v[80:83], v[56:59], a[200:207], v8\nv_smfmac_f32_16x16x64_bf16 v[68:71], v[60:63], a[40:47], v8\nv_smfmac_f32_16x16x64_bf16 v[72:75], v[60:63], a[96:103], v8\n"
"v_smfmac_f32_16x16x64_bf16 v[76:79], v[60:63], a[152:159], v8\nv_smfmac_f32_16x16x64_bf16 v[80:83], v[60:63], a[208:215], v8\nvr20_p0:\nv_smfmac_f32_16x16x64_bf16 v[68:71], v[64:67], a[48:55], v8\n"
"v_smfmac_f32_16x16x64_bf16 v[72:75], v[64:67], a[104:111], v8\nv_smfmac_f32_16x16x64_bf16 v[76:79], v[64:67], a[160:167], v8\nv_smfmac_f32_16x16x64_bf16 v[80:83], v[64:67], a[216:223], v8\nsgn_p0:\n"
"s_cbranch_execz vc20x0_p0\nbuffer_load_dwordx4 v[116:119], v20, s[8:11], s41 offen offset:0 sc1\nbuffer_load_dwordx4 v[120:123], v20, s[8:11], s41 offen offset:1024 sc1\nv_add_f32 v219, v68, v69\n"
"v_add_f32 v220, v70, v71\nv_cvt_pk_bf16_f32 v156, v219, v220\ns_nop 0\nv_add_f32 v219, v72, v73\n"
"v_add_f32 v220, v74, v75\nv_cvt_pk_bf16_f32 v157, v219, v220\ns_nop 0\nvc20x0_p0:\n"
"s_cbranch_execz vc20x1_p0\nv_add_f32 v219, v76, v77\nv_add_f32 v220, v78, v79\nv_cvt_pk_bf16_f32 v158, v219, v220\n"
"s_nop 0\nv_add_f32 v219, v80, v81\nv_add_f32 v220, v82, v83\nv_cvt_pk_bf16_f32 v159, v219, v220\n"
"s_nop 0\ns_nop 1\ns_nop 1\ns_nop 1\n"
"s_nop 1\nvc20x1_p0:\ns_cbranch_execz vc20x2_p0\ns_nop 1\n"
"s_nop 1\ns_nop 1\ns_nop 1\ns_nop 1\n"
"s_nop 1\ns_nop 1\ns_nop 1\nvc20x2_p0:\n"
"s_cbranch_execz vd20_p0\nbuffer_load_dwordx4 v[124:127], v21, s[8:11], s41 offen offset:0 sc1\nbuffer_load_dwordx4 v[128:131], v21, s[8:11], s41 offen offset:1024 sc1\ns_nop 15\n"
"s_nop 13\nvd20_p0:\ns_cbranch_execnz tl24_p0\ns_nop 0\n"
"s_nop 0\nva21_p0:\ns_branch vb21_p0\nsx21_p0:\n"
"s_add_u32 s60, s60, 6468\ns_addc_u32 s61, s61, 0\ns_setpc_b64 s[60:61]\nvb21_p0:\n"
"s_branch vr21_p0\nv_smfmac_f32_16x16x64_bf16 v[76:79], v[56:59], a[144:151], v8\nv_smfmac_f32_16x16x64_bf16 v[80:83], v[56:59], a[200:207], v8\nv_smfmac_f32_16x16x64_bf16 v[68:71], v[60:63], a[40:47], v8\n"
"v_smfmac_f32_16x16x64_bf16 v[72:75], v[60:63], a[96:103], v8\nv_smfmac_f32_16x16x64_bf16 v[76:79], v[60:63], a[152:159], v8\nv_smfmac_f32_16x16x64_bf16 v[80:83], v[60:63], a[208:215], v8\nv_smfmac_f32_16x16x64_bf16 v[68:71], v[64:67], a[48:55], v8\n"
"vr21_p0:\nv_smfmac_f32_16x16x64_bf16 v[72:75], v[64:67], a[104:111], v8\nv_smfmac_f32_16x16x64_bf16 v[76:79], v[64:67], a[160:167], v8\nv_smfmac_f32_16x16x64_bf16 v[80:83], v[64:67], a[216:223], v8\n"
"s_nop 1\ns_nop 1\ns_cbranch_execz vc21x0_p0\nbuffer_load_dwordx4 v[116:119], v20, s[8:11], s41 offen offset:0 sc1\n"
"buffer_load_dwordx4 v[120:123], v20, s[8:11], s41 offen offset:1024 sc1\nv_add_f32 v219, v68, v69\nv_add_f32 v220, v70, v71\nv_cvt_pk_bf16_f32 v156, v219, v220\n"
"s_nop 0\nv_add_f32 v219, v72, v73\nv_add_f32 v220, v74, v75\nv_cvt_pk_bf16_f32 v157, v219, v220\n"
"s_nop 0\nvc21x0_p0:\ns_cbranch_execz vc21x1_p0\nv_add_f32 v219, v76, v77\n"
"v_add_f32 v220, v78, v79\nv_cvt_pk_bf16_f32 v158, v219, v220\ns_nop 0\nv_add_f32 v219, v80, v81\n"
"v_add_f32 v220, v82, v83\nv_cvt_pk_bf16_f32 v159, v219, v220\ns_nop 0\ns_nop 1\n"
"s_nop 1\ns_nop 1\ns_nop 1\nvc21x1_p0:\n"
"s_cbranch_execz vc21x2_p0\ns_nop 1\ns_nop 1\ns_nop 1\n"
"s_nop 1\ns_nop 1\ns_nop 1\ns_nop 1\n"
"s_nop 1\nvc21x2_p0:\ns_cbranch_execz vd21_p0\nbuffer_load_dwordx4 v[124:127], v21, s[8:11], s41 offen offset:0 sc1\n"
"buffer_load_dwordx4 v[128:131], v21, s[8:11], s41 offen offset:1024 sc1\ns_nop 15\ns_nop 13\nvd21_p0:\n"
"s_cbranch_execnz tl24_p0\ns_nop 0\ns_nop 0\nva22_p0:\n"
"s_branch vb22_p0\nsx22_p0:\ns_add_u32 s60, s60, 6776\ns_addc_u32 s61, s61, 0\n"
"s_setpc_b64 s[60:61]\nvb22_p0:\ns_branch vr22_p0\nv_smfmac_f32_16x16x64_bf16 v[80:83], v[56:59], a[200:207], v8\n"
"v_smfmac_f32_16x16x64_bf16 v[68:71], v[60:63], a[40:47], v8\nv_smfmac_f32_16x16x64_bf16 v[72:75], v[60:63], a[96:103], v8\nv_smfmac_f32_16x16x64_bf16 v[76:79], v[60:63], a[152:159], v8\nv_smfmac_f32_16x16x64_bf16 v[80:83], v[60:63], a[208:215], v8\n"
"v_smfmac_f32_16x16x64_bf16 v[68:71], v[64:67], a[48:55], v8\nv_smfmac_f32_16x16x64_bf16 v[72:75], v[64:67], a[104:111], v8\nvr22_p0:\nv_smfmac_f32_16x16x64_bf16 v[76:79], v[64:67], a[160:167], v8\n"
"v_smfmac_f32_16x16x64_bf16 v[80:83], v[64:67], a[216:223], v8\ns_nop 1\ns_nop 1\ns_nop 1\n"
"s_nop 1\ns_cbranch_execz vc22x0_p0\nbuffer_load_dwordx4 v[116:119], v20, s[8:11], s41 offen offset:0 sc1\nbuffer_load_dwordx4 v[120:123], v20, s[8:11], s41 offen offset:1024 sc1\n"
"v_add_f32 v219, v68, v69\nv_add_f32 v220, v70, v71\nv_cvt_pk_bf16_f32 v156, v219, v220\ns_nop 0\n"
"v_add_f32 v219, v72, v73\nv_add_f32 v220, v74, v75\nv_cvt_pk_bf16_f32 v157, v219, v220\ns_nop 0\n"
"vc22x0_p0:\ns_cbranch_execz vc22x1_p0\nv_add_f32 v219, v76, v77\nv_add_f32 v220, v78, v79\n"
"v_cvt_pk_bf16_f32 v158, v219, v220\ns_nop 0\nv_add_f32 v219, v80, v81\nv_add_f32 v220, v82, v83\n"
"v_cvt_pk_bf16_f32 v159, v219, v220\ns_nop 0\ns_nop 1\ns_nop 1\n"
"s_nop 1\ns_nop 1\nvc22x1_p0:\ns_cbranch_execz vc22x2_p0\n"
"s_nop 1\ns_nop 1\ns_nop 1\ns_nop 1\n"
"s_nop 1\ns_nop 1\ns_nop 1\ns_nop 1\n"
"vc22x2_p0:\ns_cbranch_execz vd22_p0\nbuffer_load_dwordx4 v[124:127], v21, s[8:11], s41 offen offset:0 sc1\nbuffer_load_dwordx4 v[128:131], v21, s[8:11], s41 offen offset:1024 sc1\n"
"s_nop 15\ns_nop 13\nvd22_p0:\ns_cbranch_execnz tl24_p0\n"
"s_nop 0\ns_nop 0\nva23_p0:\ns_branch vb23_p0\n"
"sx23_p0:\ns_add_u32 s60, s60, 7084\ns_addc_u32 s61, s61, 0\ns_setpc_b64 s[60:61]\n"
"vb23_p0:\ns_branch vr23_p0\nv_smfmac_f32_16x16x64_bf16 v[68:71], v[60:63], a[40:47], v8\nv_smfmac_f32_16x16x64_bf16 v[72:75], v[60:63], a[96:103], v8\n"
"v_smfmac_f32_16x16x64_bf16 v[76:79], v[60:63], a[152:159], v8\nv_smfmac_f32_16x16x64_bf16 v[80:83], v[60:63], a[208:215], v8\nv_smfmac_f32_16x16x64_bf16 v[68:71], v[64:67], a[48:55], v8\nv_smfmac_f32_16x16x64_bf16 v[72:75], v[64:67], a[104:111], v8\n"
"v_smfmac_f32_16x16x64_bf16 v[76:79], v[64:67], a[160:167], v8\nvr23_p0:\nv_smfmac_f32_16x16x64_bf16 v[80:83], v[64:67], a[216:223], v8\ns_nop 1\n"
"s_nop 1\ns_nop 1\ns_nop 1\ns_nop 1\n"
"s_nop 1\ns_cbranch_execz vc23x0_p0\nbuffer_load_dwordx4 v[116:119], v20, s[8:11], s41 offen offset:0 sc1\nbuffer_load_dwordx4 v[120:123], v20, s[8:11], s41 offen offset:1024 sc1\n"
"v_add_f32 v219, v68, v69\nv_add_f32 v220, v70, v71\nv_cvt_pk_bf16_f32 v156, v219, v220\ns_nop 0\n"
"v_add_f32 v219, v72, v73\nv_add_f32 v220, v74, v75\nv_cvt_pk_bf16_f32 v157, v219, v220\ns_nop 0\n"
"vc23x0_p0:\ns_cbranch_execz vc23x1_p0\nv_add_f32 v219, v76, v77\nv_add_f32 v220, v78, v79\n"
"v_cvt_pk_bf16_f32 v158, v219, v220\ns_nop 0\nv_add_f32 v219, v80, v81\nv_add_f32 v220, v82, v83\n"
"v_cvt_pk_bf16_f32 v159, v219, v220\ns_nop 0\ns_nop 1\ns_nop 1\n"
"s_nop 1\ns_nop 1\nvc23x1_p0:\ns_cbranch_execz vc23x2_p0\n"
"s_nop 1\ns_nop 1\ns_nop 1\ns_nop 1\n"
"s_nop 1\ns_nop 1\ns_nop 1\ns_nop 1\n"
"vc23x2_p0:\ns_cbranch_execz vd23_p0\nbuffer_load_dwordx4 v[124:127], v21, s[8:11], s41 offen offset:0 sc1\nbuffer_load_dwordx4 v[128:131], v21, s[8:11], s41 offen offset:1024 sc1\n"
"s_nop 15\ns_nop 13\nvd23_p0:\ns_cbranch_execnz tl24_p0\n"
"s_nop 0\ns_nop 0\nva24_p0:\ns_branch warm_p0\n"
"sx24_p0:\ns_add_u32 s60, s60, 7392\ns_addc_u32 s61, s61, 0\ns_setpc_b64 s[60:61]\n"
"vb24_p0:\ns_branch vr24_p0\nv_smfmac_f32_16x16x64_bf16 v[72:75], v[60:63], a[96:103], v8\nv_smfmac_f32_16x16x64_bf16 v[76:79], v[60:63], a[152:159], v8\n"
"v_smfmac_f32_16x16x64_bf16 v[80:83], v[60:63], a[208:215], v8\nv_smfmac_f32_16x16x64_bf16 v[68:71], v[64:67], a[48:55], v8\nv_smfmac_f32_16x16x64_bf16 v[72:75], v[64:67], a[104:111], v8\nv_smfmac_f32_16x16x64_bf16 v[76:79], v[64:67], a[160:167], v8\n"
"v_smfmac_f32_16x16x64_bf16 v[80:83], v[64:67], a[216:223], v8\nvr24_p0:\ns_nop 1\ns_nop 1\n"
"s_nop 1\ns_nop 1\ns_nop 1\ns_nop 1\n"
"s_nop 1\ns_nop 1\ns_cbranch_execz vc24x0_p0\nbuffer_load_dwordx4 v[116:119], v20, s[8:11], s41 offen offset:0 sc1\n"
"buffer_load_dwordx4 v[120:123], v20, s[8:11], s41 offen offset:1024 sc1\nv_add_f32 v219, v68, v69\nv_add_f32 v220, v70, v71\nv_cvt_pk_bf16_f32 v156, v219, v220\n"
"s_nop 0\nv_add_f32 v219, v72, v73\nv_add_f32 v220, v74, v75\nv_cvt_pk_bf16_f32 v157, v219, v220\n"
"s_nop 0\nvc24x0_p0:\ns_cbranch_execz vc24x1_p0\nv_add_f32 v219, v76, v77\n"
"v_add_f32 v220, v78, v79\nv_cvt_pk_bf16_f32 v158, v219, v220\ns_nop 0\nv_add_f32 v219, v80, v81\n"
"v_add_f32 v220, v82, v83\nv_cvt_pk_bf16_f32 v159, v219, v220\ns_nop 0\ns_nop 1\n"
"s_nop 1\ns_nop 1\ns_nop 1\nvc24x1_p0:\n"
"s_cbranch_execz vc24x2_p0\ns_nop 1\ns_nop 1\ns_nop 1\n"
"s_nop 1\ns_nop 1\ns_nop 1\ns_nop 1\n"
"s_nop 1\nvc24x2_p0:\ns_cbranch_execz vd24_p0\nbuffer_load_dwordx4 v[124:127], v21, s[8:11], s41 offen offset:0 sc1\n"
"buffer_load_dwordx4 v[128:131], v21, s[8:11], s41 offen offset:1024 sc1\ns_nop 15\ns_nop 13\nvd24_p0:\n"
"s_cbranch_execnz tl24_p0\ns_nop 0\ns_nop 0\nva25_p0:\n"
"s_branch vb25_p0\nsx25_p0:\ns_add_u32 s60, s60, 7700\ns_addc_u32 s61, s61, 0\n"
"s_setpc_b64 s[60:61]\nvb25_p0:\ns_branch vr25_p0\nv_smfmac_f32_16x16x64_bf16 v[76:79], v[60:63], a[152:159], v8\n"
"v_smfmac_f32_16x16x64_bf16 v[80:83], v[60:63], a[208:215], v8\nv_smfmac_f32_16x16x64_bf16 v[68:71], v[64:67], a[48:55], v8\nv_smfmac_f32_16x16x64_bf16 v[72:75], v[64:67], a[104:111], v8\nv_smfmac_f32_16x16x64_bf16 v[76:79], v[64:67], a[160:167], v8\n"
"v_smfmac_f32_16x16x64_bf16 v[80:83], v[64:67], a[216:223], v8\ns_nop 1\ns_nop 1\nvr25_p0:\n"
"s_nop 1\ns_nop 1\ns_nop 1\ns_nop 1\n"
"s_nop 1\ns_nop 1\ns_nop 1\ns_nop 1\n"
"s_cbranch_execz vc25x0_p0\nbuffer_load_dwordx4 v[116:119], v20, s[8:11], s41 offen offset:0 sc1\nbuffer_load_dwordx4 v[120:123], v20, s[8:11], s41 offen offset:1024 sc1\nv_add_f32 v219, v68, v69\n"
"v_add_f32 v220, v70, v71\nv_cvt_pk_bf16_f32 v156, v219, v220\ns_nop 0\nv_add_f32 v219, v72, v73\n"
"v_add_f32 v220, v74, v75\nv_cvt_pk_bf16_f32 v157, v219, v220\ns_nop 0\nvc25x0_p0:\n"
"s_cbranch_execz vc25x1_p0\nv_add_f32 v219, v76, v77\nv_add_f32 v220, v78, v79\nv_cvt_pk_bf16_f32 v158, v219, v220\n"
"s_nop 0\nv_add_f32 v219, v80, v81\nv_add_f32 v220, v82, v83\nv_cvt_pk_bf16_f32 v159, v219, v220\n"
"s_nop 0\ns_nop 1\ns_nop 1\ns_nop 1\n"
"s_nop 1\nvc25x1_p0:\ns_cbranch_execz vc25x2_p0\ns_nop 1\n"
"s_nop 1\ns_nop 1\ns_nop 1\ns_nop 1\n"
"s_nop 1\ns_nop 1\ns_nop 1\nvc25x2_p0:\n"
"s_cbranch_execz vd25_p0\nbuffer_load_dwordx4 v[124:127], v21, s[8:11], s41 offen offset:0 sc1\nbuffer_load_dwordx4 v[128:131], v21, s[8:11], s41 offen offset:1024 sc1\ns_nop 15\n"
"s_nop 13\nvd25_p0:\ns_cbranch_execnz tl24_p0\ns_nop 0\n"
"s_nop 0\nva26_p0:\ns_branch vb26_p0\nsx26_p0:\n"
"s_add_u32 s60, s60, 8008\ns_addc_u32 s61, s61, 0\ns_setpc_b64 s[60:61]\nvb26_p0:\n"
"s_branch vr26_p0\nv_smfmac_f32_16x16x64_bf16 v[80:83], v[60:63], a[208:215], v8\nv_smfmac_f32_16x16x64_bf16 v[68:71], v[64:67], a[48:55], v8\nv_smfmac_f32_16x16x64_bf16 v[72:75], v[64:67], a[104:111], v8\n"
"v_smfmac_f32_16x16x64_bf16 v[76:79], v[64:67], a[160:167], v8\nv_smfmac_f32_16x16x64_bf16 v[80:83], v[64:67], a[216:223], v8\ns_nop 1\ns_nop 1\n"
"s_nop 1\ns_nop 1\nvr26_p0:\ns_nop 1\n"
"s_nop 1\ns_nop 1\ns_nop 1\ns_nop 1\n"
"s_nop 1\ns_nop 1\ns_nop 1\ns_cbranch_execz vc26x0_p0\n"
"buffer_load_dwordx4 v[116:119], v20, s[8:11], s41 offen offset:0 sc1\nbuffer_load_dwordx4 v[120:123], v20, s[8:11], s41 offen offset:1024 sc1\nv_add_f32 v219, v68, v69\nv_add_f32 v220, v70, v71\n"
"v_cvt_pk_bf16_f32 v156, v219, v220\ns_nop 0\nv_add_f32 v219, v72, v73\nv_add_f32 v220, v74, v75\n"
"v_cvt_pk_bf16_f32 v157, v219, v220\ns_nop 0\nvc26x0_p0:\ns_cbranch_execz vc26x1_p0\n"
"v_add_f32 v219, v76, v77\nv_add_f32 v220, v78, v79\nv_cvt_pk_bf16_f32 v158, v219, v220\ns_nop 0\n"
"v_add_f32 v219, v80, v81\nv_add_f32 v220, v82, v83\nv_cvt_pk_bf16_f32 v159, v219, v220\ns_nop 0\n"
"s_nop 1\ns_nop 1\ns_nop 1\ns_nop 1\n"
"vc26x1_p0:\ns_cbranch_execz vc26x2_p0\ns_nop 1\ns_nop 1\n"
"s_nop 1\ns_nop 1\ns_nop 1\ns_nop 1\n"
"s_nop 1\ns_nop 1\nvc26x2_p0:\ns_cbranch_execz vd26_p0\n"
"buffer_load_dwordx4 v[124:127], v21, s[8:11], s41 offen offset:0 sc1\nbuffer_load_dwordx4 v[128:131], v21, s[8:11], s41 offen offset:1024 sc1\ns_nop 15\ns_nop 13\n"
"vd26_p0:\ns_cbranch_execnz tl24_p0\ns_nop 0\ns_nop 0\n"
"va27_p0:\ns_branch vb27_p0\nsx27_p0:\ns_add_u32 s60, s60, 8316\n"
"s_addc_u32 s61, s61, 0\ns_setpc_b64 s[60:61]\nvb27_p0:\ns_branch vr27_p0\n"
"v_smfmac_f32_16x16x64_bf16 v[68:71], v[64:67], a[48:55], v8\nv_smfmac_f32_16x16x64_bf16 v[72:75], v[64:67], a[104:111], v8\nv_smfmac_f32_16x16x64_bf16 v[76:79], v[64:67], a[160:167], v8\nv_smfmac_f32_16x16x64_bf16 v[80:83], v[64:67], a[216:223], v8\n"
"s_nop 1\ns_nop 1\ns_nop 1\ns_nop 1\n"
"s_nop 1\ns_nop 1\nvr27_p0:\ns_nop 1\n"
"s_nop 1\ns_nop 1\ns_nop 1\ns_nop 1\n"
"s_nop 1\ns_nop 1\ns_nop 1\ns_cbranch_execz vc27x0_p0\n"
"buffer_load_dwordx4 v[116:119], v20, s[8:11], s41 offen offset:0 sc1\nbuffer_load_dwordx4 v[120:123], v20, s[8:11], s41 offen offset:1024 sc1\nv_add_f32 v219, v68, v69\nv_add_f32 v220, v70, v71\n"
"v_cvt_pk_bf16_f32 v156, v219, v220\ns_nop 0\nv_add_f32 v219, v72, v73\nv_add_f32 v220, v74, v75\n"
"v_cvt_pk_bf16_f32 v157, v219, v220\ns_nop 0\nvc27x0_p0:\ns_cbranch_execz vc27x1_p0\n"
"v_add_f32 v219, v76, v77\nv_add_f32 v220, v78, v79\nv_cvt_pk_bf16_f32 v158, v219, v220\ns_nop 0\n"
"v_add_f32 v219, v80, v81\nv_add_f32 v220, v82, v83\nv_cvt_pk_bf16_f32 v159, v219, v220\ns_nop 0\n"
"s_nop 1\ns_nop 1\ns_nop 1\ns_nop 1\n"
"vc27x1_p0:\ns_cbranch_execz vc27x2_p0\ns_nop 1\ns_nop 1\n"
"s_nop 1\ns_nop 1\ns_nop 1\ns_nop 1\n"
"s_nop 1\ns_nop 1\nvc27x2_p0:\ns_cbranch_execz vd27_p0\n"
"buffer_load_dwordx4 v[124:127], v21, s[8:11], s41 offen offset:0 sc1\nbuffer_load_dwordx4 v[128:131], v21, s[8:11], s41 offen offset:1024 sc1\ns_nop 15\ns_nop 13\n"
"vd27_p0:\ns_cbranch_execnz tl24_p0\ns_nop 0\ns_nop 0\n"
"va28_p0:\ns_branch vb28_p0\nsx28_p0:\ns_add_u32 s60, s60, 8624\n"
"s_addc_u32 s61, s61, 0\ns_setpc_b64 s[60:61]\nvb28_p0:\ns_branch vr28_p0\n"
"v_smfmac_f32_16x16x64_bf16 v[72:75], v[64:67], a[104:111], v8\nv_smfmac_f32_16x16x64_bf16 v[76:79], v[64:67], a[160:167], v8\nv_smfmac_f32_16x16x64_bf16 v[80:83], v[64:67], a[216:223], v8\ns_nop 1\n"
"s_nop 1\ns_nop 1\ns_nop 1\ns_nop 1\n"
"s_nop 1\ns_nop 1\ns_nop 1\nvr28_p0:\n"
"s_nop 1\ns_nop 1\ns_nop 1\ns_nop 1\n"
"s_nop 1\ns_nop 1\ns_nop 1\ns_nop 1\n"
"s_cbranch_execz vc28x0_p0\nbuffer_load_dwordx4 v[116:119], v20, s[8:11], s41 offen offset:0 sc1\nbuffer_load_dwordx4 v[120:123], v20, s[8:11], s41 offen offset:1024 sc1\nv_add_f32 v219, v68, v69\n"
"v_add_f32 v220, v70, v71\nv_cvt_pk_bf16_f32 v156, v219, v220\ns_nop 0\nv_add_f32 v219, v72, v73\n"
"v_add_f32 v220, v74, v75\nv_cvt_pk_bf16_f32 v157, v219, v220\ns_nop 0\nvc28x0_p0:\n"
"s_cbranch_execz vc28x1_p0\nv_add_f32 v219, v76, v77\nv_add_f32 v220, v78, v79\nv_cvt_pk_bf16_f32 v158, v219, v220\n"
"s_nop 0\nv_add_f32 v219, v80, v81\nv_add_f32 v220, v82, v83\nv_cvt_pk_bf16_f32 v159, v219, v220\n"
"s_nop 0\ns_nop 1\ns_nop 1\ns_nop 1\n"
"s_nop 1\nvc28x1_p0:\ns_cbranch_execz vc28x2_p0\ns_nop 1\n"
"s_nop 1\ns_nop 1\ns_nop 1\ns_nop 1\n"
"s_nop 1\ns_nop 1\ns_nop 1\nvc28x2_p0:\n"
"s_cbranch_execz vd28_p0\nbuffer_load_dwordx4 v[124:127], v21, s[8:11], s41 offen offset:0 sc1\nbuffer_load_dwordx4 v[128:131], v21, s[8:11], s41 offen offset:1024 sc1\ns_nop 15\n"
"s_nop 13\nvd28_p0:\ns_cbranch_execnz tl24_p0\ns_nop 0\n"
"s_nop 0\nva29_p0:\ns_branch vb29_p0\nsx29_p0:\n"
"s_add_u32 s60, s60, 8932\ns_addc_u32 s61, s61, 0\ns_setpc_b64 s[60:61]\nvb29_p0:\n"
"s_branch vr29_p0\nv_smfmac_f32_16x16x64_bf16 v[76:79], v[64:67], a[160:167], v8\nv_smfmac_f32_16x16x64_bf16 v[80:83], v[64:67], a[216:223], v8\ns_nop 1\n"
"s_nop 1\ns_nop 1\ns_nop 1\ns_nop 1\n"
"s_nop 1\ns_nop 1\ns_nop 1\ns_nop 1\n"
"s_nop 1\nvr29_p0:\ns_nop 1\ns_nop 1\n"
"s_nop 1\ns_nop 1\ns_nop 1\ns_nop 1\n"
"s_nop 1\ns_nop 1\ns_cbranch_execz vc29x0_p0\nbuffer_load_dwordx4 v[116:119], v20, s[8:11], s41 offen offset:0 sc1\n"
"buffer_load_dwordx4 v[120:123], v20, s[8:11], s41 offen offset:1024 sc1\nv_add_f32 v219, v68, v69\nv_add_f32 v220, v70, v71\nv_cvt_pk_bf16_f32 v156, v219, v220\n"
"s_nop 0\nv_add_f32 v219, v72, v73\nv_add_f32 v220, v74, v75\nv_cvt_pk_bf16_f32 v157, v219, v220\n"
"s_nop 0\nvc29x0_p0:\ns_cbranch_execz vc29x1_p0\nv_add_f32 v219, v76, v77\n"
"v_add_f32 v220, v78, v79\nv_cvt_pk_bf16_f32 v158, v219, v220\ns_nop 0\nv_add_f32 v219, v80, v81\n"
"v_add_f32 v220, v82, v83\nv_cvt_pk_bf16_f32 v159, v219, v220\ns_nop 0\ns_nop 1\n"
"s_nop 1\ns_nop 1\ns_nop 1\nvc29x1_p0:\n"
"s_cbranch_execz vc29x2_p0\ns_nop 1\ns_nop 1\ns_nop 1\n"
"s_nop 1\ns_nop 1\ns_nop 1\ns_nop 1\n"
"s_nop 1\nvc29x2_p0:\ns_cbranch_execz vd29_p0\nbuffer_load_dwordx4 v[124:127], v21, s[8:11], s41 offen offset:0 sc1\n"
"buffer_load_dwordx4 v[128:131], v21, s[8:11], s41 offen offset:1024 sc1\ns_nop 15\ns_nop 13\nvd29_p0:\n"
"s_cbranch_execnz tl24_p0\ns_nop 0\ns_nop 0\nva30_p0:\n"
"s_branch warm_p0\nsx30_p0:\ns_add_u32 s60, s60, 9240\ns_addc_u32 s61, s61, 0\n"
"s_setpc_b64 s[60:61]\nvb30_p0:\ns_branch vr30_p0\nv_smfmac_f32_16x16x64_bf16 v[80:83], v[64:67], a[216:223], v8\n"
"s_nop 1\ns_nop 1\ns_nop 1\ns_nop 1\n"
"s_nop 1\ns_nop 1\ns_nop 1\ns_nop 1\n"
"s_nop 1\ns_nop 1\ns_nop 1\ns_nop 1\n"
"vr30_p0:\ns_nop 1\ns_nop 1\ns_nop 1\n"
"s_nop 1\ns_nop 1\ns_nop 1\ns_nop 1\n"
"s_nop 1\ns_cbranch_execz vc30x0_p0\nbuffer_load_dwordx4 v[116:119], v20, s[8:11], s41 offen offset:0 sc1\nbuffer_load_dwordx4 v[120:123], v20, s[8:11], s41 offen offset:1024 sc1\n"
"v_add_f32 v219, v68, v69\nv_add_f32 v220, v70, v71\nv_cvt_pk_bf16_f32 v156, v219, v220\ns_nop 0\n"
"v_add_f32 v219, v72, v73\nv_add_f32 v220, v74, v75\nv_cvt_pk_bf16_f32 v157, v219, v220\ns_nop 0\n"
"vc30x0_p0:\ns_cbranch_execz vc30x1_p0\nv_add_f32 v219, v76, v77\nv_add_f32 v220, v78, v79\n"
"v_cvt_pk_bf16_f32 v158, v219, v220\ns_nop 0\nv_add_f32 v219, v80, v81\nv_add_f32 v220, v82, v83\n"
"v_cvt_pk_bf16_f32 v159, v219, v220\ns_nop 0\ns_nop 1\ns_nop 1\n"
"s_nop 1\ns_nop 1\nvc30x1_p0:\ns_cbranch_execz vc30x2_p0\n"
"s_nop 1\ns_nop 1\ns_nop 1\ns_nop 1\n"
"s_nop 1\ns_nop 1\ns_nop 1\ns_nop 1\n"
"vc30x2_p0:\ns_cbranch_execz vd30_p0\nbuffer_load_dwordx4 v[124:127], v21, s[8:11], s41 offen offset:0 sc1\nbuffer_load_dwordx4 v[128:131], v21, s[8:11], s41 offen offset:1024 sc1\n"
"s_nop 15\ns_nop 13\nvd30_p0:\ns_cbranch_execnz tl24_p0\n"
"s_nop 0\ns_nop 0\nva31_p0:\ns_branch vb31_p0\n"
"sx31_p0:\ns_add_u32 s60, s60, 9548\ns_addc_u32 s61, s61, 0\ns_setpc_b64 s[60:61]\n"
"vb31_p0:\ns_branch vr31_p0\ns_nop 1\ns_nop 1\n"
"s_nop 1\ns_nop 1\ns_nop 1\ns_nop 1\n"
"s_nop 1\ns_nop 1\ns_nop 1\ns_nop 1\n"
"s_nop 1\ns_nop 1\ns_nop 1\ns_nop 1\n"
"vr31_p0:\ns_nop 1\ns_nop 1\ns_nop 1\n"
"s_nop 1\ns_nop 1\ns_nop 1\ns_nop 1\n"
"s_nop 1\ns_cbranch_execz vc31x0_p0\nbuffer_load_dwordx4 v[116:119], v20, s[8:11], s41 offen offset:0 sc1\nbuffer_load_dwordx4 v[120:123], v20, s[8:11], s41 offen offset:1024 sc1\n"
"v_add_f32 v219, v68, v69\nv_add_f32 v220, v70, v71\nv_cvt_pk_bf16_f32 v156, v219, v220\ns_nop 0\n"
"v_add_f32 v219, v72, v73\nv_add_f32 v220, v74, v75\nv_cvt_pk_bf16_f32 v157, v219, v220\ns_nop 0\n"
"vc31x0_p0:\ns_cbranch_execz vc31x1_p0\nv_add_f32 v219, v76, v77\nv_add_f32 v220, v78, v79\n"
"v_cvt_pk_bf16_f32 v158, v219, v220\ns_nop 0\nv_add_f32 v219, v80, v81\nv_add_f32 v220, v82, v83\n"
"v_cvt_pk_bf16_f32 v159, v219, v220\ns_nop 0\ns_nop 1\ns_nop 1\n"
"s_nop 1\ns_nop 1\nvc31x1_p0:\ns_cbranch_execz vc31x2_p0\n"
"s_nop 1\ns_nop 1\ns_nop 1\ns_nop 1\n"
"s_nop 1\ns_nop 1\ns_nop 1\ns_nop 1\n"
"vc31x2_p0:\ns_cbranch_execz vd31_p0\nbuffer_load_dwordx4 v[124:127], v21, s[8:11], s41 offen offset:0 sc1\nbuffer_load_dwordx4 v[128:131], v21, s[8:11], s41 offen offset:1024 sc1\n"
"s_nop 15\ns_nop 13\nvd31_p0:\ns_cbranch_execnz tl24_p0\n"
"s_nop 0\ns_nop 0\nva32_p0:\ns_branch vb32_p0\n"
"sx32_p0:\ns_add_u32 s60, s60, 9856\ns_addc_u32 s61, s61, 0\ns_setpc_b64 s[60:61]\n"
"vb32_p0:\ns_branch vr32_p0\ns_nop 1\ns_nop 1\n"
"s_nop 1\ns_nop 1\ns_nop 1\ns_nop 1\n"
"s_nop 1\ns_nop 1\ns_nop 1\ns_nop 1\n"
"s_nop 1\ns_nop 1\ns_nop 1\ns_nop 1\n"
"vr32_p0:\ns_nop 1\ns_nop 1\ns_nop 1\n"
"s_nop 1\ns_nop 1\ns_nop 1\ns_nop 1\n"
"s_nop 1\ns_cbranch_execz vc32x0_p0\nbuffer_load_dwordx4 v[116:119], v20, s[8:11], s41 offen offset:0 sc1\nbuffer_load_dwordx4 v[120:123], v20, s[8:11], s41 offen offset:1024 sc1\n"
"v_add_f32 v219, v68, v69\nv_add_f32 v220, v70, v71\nv_cvt_pk_bf16_f32 v156, v219, v220\ns_nop 0\n"
"v_add_f32 v219, v72, v73\nv_add_f32 v220, v74, v75\nv_cvt_pk_bf16_f32 v157, v219, v220\ns_nop 0\n"
"vc32x0_p0:\ns_cbranch_execz vc32x1_p0\nv_add_f32 v219, v76, v77\nv_add_f32 v220, v78, v79\n"
"v_cvt_pk_bf16_f32 v158, v219, v220\ns_nop 0\nv_add_f32 v219, v80, v81\nv_add_f32 v220, v82, v83\n"
"v_cvt_pk_bf16_f32 v159, v219, v220\ns_nop 0\ns_nop 1\ns_nop 1\n"
"s_nop 1\ns_nop 1\nvc32x1_p0:\ns_cbranch_execz vc32x2_p0\n"
"s_nop 1\ns_nop 1\ns_nop 1\ns_nop 1\n"
"s_nop 1\ns_nop 1\ns_nop 1\ns_nop 1\n"
"vc32x2_p0:\ns_cbranch_execz vd32_p0\nbuffer_load_dwordx4 v[124:127], v21, s[8:11], s41 offen offset:0 sc1\nbuffer_load_dwordx4 v[128:131], v21, s[8:11], s41 offen offset:1024 sc1\n"
"s_nop 15\ns_nop 13\nvd32_p0:\ns_cbranch_execnz tl24_p0\n"
"s_nop 0\ns_nop 0\nva33_p0:\ns_branch vb33_p0\n"
"sx33_p0:\ns_add_u32 s60, s60, 10164\ns_addc_u32 s61, s61, 0\ns_setpc_b64 s[60:61]\n"
"vb33_p0:\ns_branch vr33_p0\ns_nop 1\ns_nop 1\n"
"s_nop 1\ns_nop 1\ns_nop 1\ns_nop 1\n"
"s_nop 1\ns_nop 1\ns_nop 1\ns_nop 1\n"
"s_nop 1\ns_nop 1\ns_nop 1\ns_nop 1\n"
"vr33_p0:\ns_nop 1\ns_nop 1\ns_nop 1\n"
"s_nop 1\ns_nop 1\ns_nop 1\ns_nop 1\n"
"s_nop 1\ns_cbranch_execz vc33x0_p0\nbuffer_load_dwordx4 v[116:119], v20, s[8:11], s41 offen offset:0 sc1\nbuffer_load_dwordx4 v[120:123], v20, s[8:11], s41 offen offset:1024 sc1\n"
"v_add_f32 v219, v68, v69\nv_add_f32 v220, v70, v71\nv_cvt_pk_bf16_f32 v156, v219, v220\ns_nop 0\n"
"v_add_f32 v219, v72, v73\nv_add_f32 v220, v74, v75\nv_cvt_pk_bf16_f32 v157, v219, v220\ns_nop 0\n"
"vc33x0_p0:\ns_cbranch_execz vc33x1_p0\nv_add_f32 v219, v76, v77\nv_add_f32 v220, v78, v79\n"
"v_cvt_pk_bf16_f32 v158, v219, v220\ns_nop 0\nv_add_f32 v219, v80, v81\nv_add_f32 v220, v82, v83\n"
"v_cvt_pk_bf16_f32 v159, v219, v220\ns_nop 0\ns_nop 1\ns_nop 1\n"
"s_nop 1\ns_nop 1\nvc33x1_p0:\ns_cbranch_execz vc33x2_p0\n"
"s_nop 1\ns_nop 1\ns_nop 1\ns_nop 1\n"
"s_nop 1\ns_nop 1\ns_nop 1\ns_nop 1\n"
"vc33x2_p0:\ns_cbranch_execz vd33_p0\nbuffer_load_dwordx4 v[124:127], v21, s[8:11], s41 offen offset:0 sc1\nbuffer_load_dwordx4 v[128:131], v21, s[8:11], s41 offen offset:1024 sc1\n"
"s_nop 15\ns_nop 13\nvd33_p0:\ns_cbranch_execnz tl24_p0\n"
"s_nop 0\ns_nop 0\nva34_p0:\ns_branch vb34_p0\n"
"sx34_p0:\ns_add_u32 s60, s60, 10472\ns_addc_u32 s61, s61, 0\ns_setpc_b64 s[60:61]\n"
"vb34_p0:\ns_branch vr34_p0\ns_nop 1\ns_nop 1\n"
"s_nop 1\ns_nop 1\ns_nop 1\ns_nop 1\n"
"s_nop 1\ns_nop 1\ns_nop 1\ns_nop 1\n"
"s_nop 1\ns_nop 1\ns_nop 1\ns_nop 1\n"
"vr34_p0:\ns_nop 1\ns_nop 1\ns_nop 1\n"
"s_nop 1\ns_nop 1\ns_nop 1\ns_nop 1\n"
"s_nop 1\ns_cbranch_execz vc34x0_p0\nbuffer_load_dwordx4 v[116:119], v20, s[8:11], s41 offen offset:0 sc1\nbuffer_load_dwordx4 v[120:123], v20, s[8:11], s41 offen offset:1024 sc1\n"
"v_add_f32 v219, v68, v69\nv_add_f32 v220, v70, v71\nv_cvt_pk_bf16_f32 v156, v219, v220\ns_nop 0\n"
"v_add_f32 v219, v72, v73\nv_add_f32 v220, v74, v75\nv_cvt_pk_bf16_f32 v157, v219, v220\ns_nop 0\n"
"vc34x0_p0:\ns_cbranch_execz vc34x1_p0\nv_add_f32 v219, v76, v77\nv_add_f32 v220, v78, v79\n"
"v_cvt_pk_bf16_f32 v158, v219, v220\ns_nop 0\nv_add_f32 v219, v80, v81\nv_add_f32 v220, v82, v83\n"
"v_cvt_pk_bf16_f32 v159, v219, v220\ns_nop 0\ns_nop 1\ns_nop 1\n"
"s_nop 1\ns_nop 1\nvc34x1_p0:\ns_cbranch_execz vc34x2_p0\n"
"s_nop 1\ns_nop 1\ns_nop 1\ns_nop 1\n"
"s_nop 1\ns_nop 1\ns_nop 1\ns_nop 1\n"
"vc34x2_p0:\ns_cbranch_execz vd34_p0\nbuffer_load_dwordx4 v[124:127], v21, s[8:11], s41 offen offset:0 sc1\nbuffer_load_dwordx4 v[128:131], v21, s[8:11], s41 offen offset:1024 sc1\n"
"s_nop 15\ns_nop 13\nvd34_p0:\ns_cbranch_execnz tl24_p0\n"
"s_nop 0\ns_nop 0\nva35_p0:\ns_branch vb35_p0\n"
"sx35_p0:\ns_add_u32 s60, s60, 10780\ns_addc_u32 s61, s61, 0\ns_setpc_b64 s[60:61]\n"
"vb35_p0:\ns_branch vr35_p0\ns_nop 1\ns_nop 1\n"
"s_nop 1\ns_nop 1\ns_nop 1\ns_nop 1\n"
"s_nop 1\ns_nop 1\ns_nop 1\ns_nop 1\n"
"s_nop 1\ns_nop 1\ns_nop 1\ns_nop 1\n"
"vr35_p0:\ns_nop 1\ns_nop 1\ns_nop 1\n"
"s_nop 1\ns_nop 1\ns_nop 1\ns_nop 1\n"
"s_nop 1\ns_cbranch_execz vc35x0_p0\nbuffer_load_dwordx4 v[116:119], v20, s[8:11], s41 offen offset:0 sc1\nbuffer_load_dwordx4 v[120:123], v20, s[8:11], s41 offen offset:1024 sc1\n"
"v_add_f32 v219, v68, v69\nv_add_f32 v220, v70, v71\nv_cvt_pk_bf16_f32 v156, v219, v220\ns_nop 0\n"
"v_add_f32 v219, v72, v73\nv_add_f32 v220, v74, v75\nv_cvt_pk_bf16_f32 v157, v219, v220\ns_nop 0\n"
"vc35x0_p0:\ns_cbranch_execz vc35x1_p0\nv_add_f32 v219, v76, v77\nv_add_f32 v220, v78, v79\n"
"v_cvt_pk_bf16_f32 v158, v219, v220\ns_nop 0\nv_add_f32 v219, v80, v81\nv_add_f32 v220, v82, v83\n"
"v_cvt_pk_bf16_f32 v159, v219, v220\ns_nop 0\ns_nop 1\ns_nop 1\n"
"s_nop 1\ns_nop 1\nvc35x1_p0:\ns_cbranch_execz vc35x2_p0\n"
"s_nop 1\ns_nop 1\ns_nop 1\ns_nop 1\n"
"s_nop 1\ns_nop 1\ns_nop 1\ns_nop 1\n"
"vc35x2_p0:\ns_cbranch_execz vd35_p0\nbuffer_load_dwordx4 v[124:127], v21, s[8:11], s41 offen offset:0 sc1\nbuffer_load_dwordx4 v[128:131], v21, s[8:11], s41 offen offset:1024 sc1\n"
"s_nop 15\ns_nop 13\nvd35_p0:\ns_cbranch_execnz tl24_p0\n"
"s_nop 0\ns_nop 0\nva36_p0:\nwarm_p0:\n"
"s_setpc_b64 s[88:89]\ns_nop 0\ns_nop 0\ns_nop 0\n"
"s_nop 0\ns_memrealtime s[54:55]\ns_waitcnt lgkmcnt(0)\ns_add_u32 s90, s54, 0x1000\n"
"s_cselect_b32 s90, -1, s90\ns_mov_b32 s56, s54\ns_mov_b32 s58, s54\ns_getpc_b64 s[60:61]\n"
"dq_p0:\ns_sub_u32 s60, s60, dq_p0-vb0_p0\ns_subb_u32 s61, s61, 0\ns_branch gr_p0\n"
"end_p0:\ns_branch fk_done\nfk_done:\ns_endpgm\n"
    :: "{s[8:23]}"(s0), "{s[24:39]}"(s1), "{v[8:23]}"(v0), "{v[24:39]}"(v1) : "v40", "v41", "v42", "v43", "v44", "v45", "v46", "v47", "v48", "v49", "v50", "v51", "v52", "v53", "v54", "v55", "v56", "v57", "v58", "v59", "v60", "v61", "v62", "v63", "v64", "v65", "v66", "v67", "v68", "v69", "v70", "v71", "v72", "v73", "v74", "v75", "v76", "v77", "v78", "v79", "v80", "v81", "v82", "v83", "v84", "v85", "v86", "v87", "v88", "v89", "v90", "v91", "v92", "v93", "v94", "v95", "v96", "v97", "v98", "v99", "v100", "v101", "v102", "v103", "v104", "v105", "v106", "v107", "v108", "v109", "v110", "v111", "v112", "v113", "v114", "v115", "v116", "v117", "v118", "v119", "v120", "v121", "v122", "v123", "v124", "v125", "v126", "v127", "v128", "v129", "v130", "v131", "v132", "v133", "v134", "v135", "v136", "v137", "v138", "v139", "v140", "v141", "v142", "v143", "v144", "v145", "v146", "v147", "v148", "v149", "v150", "v151", "v152", "v153", "v154", "v155", "v156", "v157", "v158", "v159", "v160", "v161", "v162", "v163", "v164", "v165", "v166", "v167", "v168", "v169", "v170", "v171", "v172", "v173", "v174", "v175", "v176", "v177", "v178", "v179", "v180", "v181", "v182", "v183", "v184", "v185", "v186", "v187", "v188", "v189", "v190", "v191", "v192", "v193", "v194", "v195", "v196", "v197", "v198", "v199", "v200", "v201", "v202", "v203", "v204", "v205", "v206", "v207", "v208", "v209", "v210", "v211", "v212", "v213", "v214", "v215", "v216", "v217", "v218", "v219", "v220", "v221", "v222", "v223", "v224", "v225", "v226", "v227", "v228", "v229", "v230", "v231", "v232", "v233", "v234", "v235", "v236", "v237", "v238", "v239", "v240", "v241", "v242", "v243", "v244", "v245", "v246", "v247", "v248", "v249", "v250", "v251", "v252", "v253", "v254", "v255", "a0", "a1", "a2", "a3", "a4", "a5", "a6", "a7", "a8", "a9", "a10", "a11", "a12", "a13", "a14", "a15", "a16", "a17", "a18", "a19", "a20", "a21", "a22", "a23", "a24", "a25", "a26", "a27", "a28", "a29", "a30", "a31", "a32", "a33", "a34", "a35", "a36", "a37", "a38", "a39", "a40", "a41", "a42", "a43", "a44", "a45", "a46", "a47", "a48", "a49", "a50", "a51", "a52", "a53", "a54", "a55", "a56", "a57", "a58", "a59", "a60", "a61", "a62", "a63", "a64", "a65", "a66", "a67", "a68", "a69", "a70", "a71", "a72", "a73", "a74", "a75", "a76", "a77", "a78", "a79", "a80", "a81", "a82", "a83", "a84", "a85", "a86", "a87", "a88", "a89", "a90", "a91", "a92", "a93", "a94", "a95", "a96", "a97", "a98", "a99", "a100", "a101", "a102", "a103", "a104", "a105", "a106", "a107", "a108", "a109", "a110", "a111", "a112", "a113", "a114", "a115", "a116", "a117", "a118", "a119", "a120", "a121", "a122", "a123", "a124", "a125", "a126", "a127", "a128", "a129", "a130", "a131", "a132", "a133", "a134", "a135", "a136", "a137", "a138", "a139", "a140", "a141", "a142", "a143", "a144", "a145", "a146", "a147", "a148", "a149", "a150", "a151", "a152", "a153", "a154", "a155", "a156", "a157", "a158", "a159", "a160", "a161", "a162", "a163", "a164", "a165", "a166", "a167", "a168", "a169", "a170", "a171", "a172", "a173", "a174", "a175", "a176", "a177", "a178", "a179", "a180", "a181", "a182", "a183", "a184", "a185", "a186", "a187", "a188", "a189", "a190", "a191", "a192", "a193", "a194", "a195", "a196", "a197", "a198", "a199", "a200", "a201", "a202", "a203", "a204", "a205", "a206", "a207", "a208", "a209", "a210", "a211", "a212", "a213", "a214", "a215", "a216", "a217", "a218", "a219", "a220", "a221", "a222", "a223", "a224", "a225", "a226", "a227", "a228", "a229", "a230", "a231", "a232", "a233", "a234", "a235", "a236", "a237", "a238", "a239", "a240", "a241", "a242", "a243", "a244", "a245", "a246", "a247", "a248", "a249", "a250", "a251", "a252", "a253", "a254", "a255", "s40", "s41", "s42", "s43", "s44", "s45", "s46", "s47", "s48", "s49", "s50", "s51", "s52", "s53", "s54", "s55", "s56", "s57", "s58", "s59", "s60", "s61", "s62", "s63", "s64", "s65", "s66", "s67", "s68", "s69", "s70", "s71", "s72", "s73", "s74", "s75", "s76", "s77", "s78", "s79", "s80", "s81", "s82", "s83", "s84", "s85", "s86", "s87", "s88", "s89", "s90", "s91", "s92", "s93", "s94", "s95", "vcc", "scc", "memory");
    __builtin_unreachable();
}
struct GCtx { u4 rw1, rx, rexp, rout, rhb; unsigned tag, rot, sw1, mw, mx, sfg, stay, csc, ostr, kb, p1, p2; unsigned vr0, vr1, vx, wa, xa, vexp, vimp, vo0, vo1, vo2, vo3, vfl, vpf, vpo, vk, p5; };
DI unsigned gemm_asm0(const GCtx& c, u8v& w) {
    const u16v s0 = {RFL(c.rw1[0]), RFL(c.rw1[1]), RFL(c.rw1[2]), RFL(c.rw1[3]), RFL(c.rx[0]), RFL(c.rx[1]), RFL(c.rx[2]), RFL(c.rx[3]), RFL(c.rexp[0]), RFL(c.rexp[1]), RFL(c.rexp[2]), RFL(c.rexp[3]), RFL(c.rout[0]), RFL(c.rout[1]), RFL(c.rout[2]), RFL(c.rout[3])}, s1 = {RFL(c.rhb[0]), RFL(c.rhb[1]), RFL(c.rhb[2]), RFL(c.rhb[3]), RFL(c.tag), RFL(c.rot), RFL(c.sw1), RFL(c.mw), RFL(c.mx), RFL(c.sfg), RFL(c.stay), RFL(c.csc), RFL(c.ostr), RFL(c.kb), RFL(c.p1), RFL(c.p2)}, v0 = {c.vr0, c.vr1, c.vx, c.wa, c.xa, c.vexp, c.vimp, c.vo0, c.vo1, c.vo2, c.vo3, c.vfl, c.vpf, c.vpo, c.vk, c.p5};
    unsigned o = 0;
    asm volatile(
"s_nop 4\ns_add_u32 s40, s29, 0\ns_add_u32 s41, s29, 1\ns_add_u32 s42, s29, 2\n"
"s_add_u32 s43, s29, 3\nv_xor_b32 v24, 0, v11\nv_xor_b32 v25, 64, v11\nv_xor_b32 v26, 128, v11\n"
"v_xor_b32 v27, 192, v11\nv_xor_b32 v28, 2064, v11\nv_xor_b32 v29, 2128, v11\nv_xor_b32 v30, 2192, v11\n"
"v_xor_b32 v31, 2256, v11\nv_add_u32 v40, s37, v24\nv_add_u32 v41, s37, v25\nv_add_u32 v42, s37, v26\n"
"v_add_u32 v43, s37, v27\nv_add_u32 v44, s37, v28\nv_add_u32 v45, s37, v29\nv_add_u32 v46, s37, v30\n"
"v_add_u32 v47, s37, v31\nds_read_b128 a[128:131], v40 offset:0\nds_read_b128 a[132:135], v41 offset:0\nds_read_b128 a[136:139], v42 offset:0\n"
"ds_read_b128 a[140:143], v43 offset:0\ns_lshl_b32 s53, s41, 19\ns_add_u32 s53, s53, s30\ns_add_u32 m0, s31, 65536\n"
"s_add_u32 s53, s53, 0\nbuffer_load_dwordx4 v8, s[8:11], s53 offen lds\nbuffer_load_dwordx4 v9, s[8:11], s53 offen offset:1024 lds\nds_read_b128 a[144:147], v40 offset:256\n"
"ds_read_b128 a[148:151], v41 offset:256\nds_read_b128 a[152:155], v42 offset:256\nds_read_b128 a[156:159], v43 offset:256\ns_lshl_b32 s53, s41, 19\n"
"s_add_u32 s53, s53, s30\ns_add_u32 m0, s31, 73728\ns_add_u32 s53, s53, 131072\nbuffer_load_dwordx4 v8, s[8:11], s53 offen lds\n"
"buffer_load_dwordx4 v9, s[8:11], s53 offen offset:1024 lds\ns_waitcnt lgkmcnt(0)\nds_read_b128 a[160:163], v44 offset:0\nds_read_b128 a[164:167], v45 offset:0\n"
"ds_read_b128 a[168:171], v46 offset:0\nds_read_b128 a[172:175], v47 offset:0\ns_lshl_b32 s53, s41, 19\ns_add_u32 s53, s53, s30\n"
"s_add_u32 m0, s31, 81920\ns_add_u32 s53, s53, 262144\nbuffer_load_dwordx4 v8, s[8:11], s53 offen lds\nbuffer_load_dwordx4 v9, s[8:11], s53 offen offset:1024 lds\n"
"ds_read_b128 a[176:179], v44 offset:256\nds_read_b128 a[180:183], v45 offset:256\nds_read_b128 a[184:187], v46 offset:256\nds_read_b128 a[188:191], v47 offset:256\n"
"s_lshl_b32 s53, s41, 19\ns_add_u32 s53, s53, s30\ns_add_u32 m0, s31, 90112\ns_add_u32 s53, s53, 393216\n"
"buffer_load_dwordx4 v8, s[8:11], s53 offen lds\nbuffer_load_dwordx4 v9, s[8:11], s53 offen offset:1024 lds\ns_waitcnt lgkmcnt(0)\nv_add_u32 v40, 65536, v24\n"
"v_add_u32 v41, 65536, v25\nv_add_u32 v42, 65536, v26\nv_add_u32 v43, 65536, v27\nv_add_u32 v44, 65536, v28\n"
"v_add_u32 v45, 65536, v29\nv_add_u32 v46, 65536, v30\nv_add_u32 v47, 65536, v31\nv_xor_b32 v32, 0, v12\n"
"v_xor_b32 v33, 2048, v12\nv_xor_b32 v34, 64, v12\nv_xor_b32 v35, 2112, v12\ns_mov_b32 s55, 0\n"
"s_waitcnt vmcnt(11)\ns_barrier\nds_read_b128 v[96:99], v24 offset:0\nds_read_b128 v[100:103], v28 offset:0\n"
"ds_read_b128 v[104:107], v25 offset:0\nds_read_b128 v[108:111], v29 offset:0\nds_read_b128 v[112:115], v26 offset:0\nds_read_b128 v[116:119], v30 offset:0\n"
"ds_read_b128 v[120:123], v27 offset:0\nds_read_b128 v[124:127], v31 offset:0\ns_waitcnt lgkmcnt(6)\nv_mfma_f32_16x16x32_bf16 v[64:67], v[96:99], a[128:131], 0\n"
"s_lshl_b32 s53, s42, 19\ns_add_u32 s53, s53, s30\nv_mfma_f32_16x16x32_bf16 v[68:71], v[96:99], a[160:163], 0\nds_read_b128 v[96:99], v24 offset:256\n"
"s_add_u32 m0, s31, 98304\ns_add_u32 s53, s53, 0\nv_mfma_f32_16x16x32_bf16 v[72:75], v[100:103], a[128:131], 0\nbuffer_load_dwordx4 v8, s[8:11], s53 offen lds\n"
"v_mfma_f32_16x16x32_bf16 v[76:79], v[100:103], a[160:163], 0\nds_read_b128 v[100:103], v28 offset:256\nbuffer_load_dwordx4 v9, s[8:11], s53 offen offset:1024 lds\ns_waitcnt lgkmcnt(6)\n"
"v_mfma_f32_16x16x32_bf16 v[64:67], v[104:107], a[132:135], v[64:67]\ns_lshl_b32 s53, s42, 19\ns_add_u32 s53, s53, s30\nv_mfma_f32_16x16x32_bf16 v[68:71], v[104:107], a[164:167], v[68:71]\n"
"ds_read_b128 v[104:107], v25 offset:256\ns_add_u32 m0, s31, 106496\ns_add_u32 s53, s53, 131072\nv_mfma_f32_16x16x32_bf16 v[72:75], v[108:111], a[132:135], v[72:75]\n"
"buffer_load_dwordx4 v8, s[8:11], s53 offen lds\nv_mfma_f32_16x16x32_bf16 v[76:79], v[108:111], a[164:167], v[76:79]\nds_read_b128 v[108:111], v29 offset:256\nbuffer_load_dwordx4 v9, s[8:11], s53 offen offset:1024 lds\n"
"s_waitcnt lgkmcnt(6)\nv_mfma_f32_16x16x32_bf16 v[64:67], v[112:115], a[136:139], v[64:67]\nv_mfma_f32_16x16x32_bf16 v[68:71], v[112:115], a[168:171], v[68:71]\nds_read_b128 v[112:115], v26 offset:256\n"
"v_mfma_f32_16x16x32_bf16 v[72:75], v[116:119], a[136:139], v[72:75]\nv_mfma_f32_16x16x32_bf16 v[76:79], v[116:119], a[168:171], v[76:79]\nds_read_b128 v[116:119], v30 offset:256\ns_waitcnt lgkmcnt(6)\n"
"v_mfma_f32_16x16x32_bf16 v[64:67], v[120:123], a[140:143], v[64:67]\nv_mfma_f32_16x16x32_bf16 v[68:71], v[120:123], a[172:175], v[68:71]\nds_read_b128 v[120:123], v27 offset:256\nv_mfma_f32_16x16x32_bf16 v[72:75], v[124:127], a[140:143], v[72:75]\n"
"v_mfma_f32_16x16x32_bf16 v[76:79], v[124:127], a[172:175], v[76:79]\nds_read_b128 v[124:127], v31 offset:256\ns_waitcnt lgkmcnt(6)\nv_mfma_f32_16x16x32_bf16 v[64:67], v[96:99], a[144:147], v[64:67]\n"
"v_mfma_f32_16x16x32_bf16 v[68:71], v[96:99], a[176:179], v[68:71]\nds_read_b128 v[96:99], v24 offset:16384\nv_mfma_f32_16x16x32_bf16 v[72:75], v[100:103], a[144:147], v[72:75]\nv_mfma_f32_16x16x32_bf16 v[76:79], v[100:103], a[176:179], v[76:79]\n"
"ds_read_b128 v[100:103], v28 offset:16384\ns_waitcnt lgkmcnt(6)\nv_mfma_f32_16x16x32_bf16 v[64:67], v[104:107], a[148:151], v[64:67]\nv_mfma_f32_16x16x32_bf16 v[68:71], v[104:107], a[180:183], v[68:71]\n"
"ds_read_b128 v[104:107], v25 offset:16384\nv_mfma_f32_16x16x32_bf16 v[72:75], v[108:111], a[148:151], v[72:75]\nv_mfma_f32_16x16x32_bf16 v[76:79], v[108:111], a[180:183], v[76:79]\nds_read_b128 v[108:111], v29 offset:16384\n"
"s_waitcnt lgkmcnt(6)\nv_mfma_f32_16x16x32_bf16 v[64:67], v[112:115], a[152:155], v[64:67]\nv_mfma_f32_16x16x32_bf16 v[68:71], v[112:115], a[184:187], v[68:71]\nds_read_b128 v[112:115], v26 offset:16384\n"
"v_mfma_f32_16x16x32_bf16 v[72:75], v[116:119], a[152:155], v[72:75]\nv_mfma_f32_16x16x32_bf16 v[76:79], v[116:119], a[184:187], v[76:79]\nds_read_b128 v[116:119], v30 offset:16384\ns_waitcnt lgkmcnt(6)\n"
"v_mfma_f32_16x16x32_bf16 v[64:67], v[120:123], a[156:159], v[64:67]\nv_mfma_f32_16x16x32_bf16 v[68:71], v[120:123], a[188:191], v[68:71]\nds_read_b128 v[120:123], v27 offset:16384\nv_mfma_f32_16x16x32_bf16 v[72:75], v[124:127], a[156:159], v[72:75]\n"
"v_mfma_f32_16x16x32_bf16 v[76:79], v[124:127], a[188:191], v[76:79]\nds_read_b128 v[124:127], v31 offset:16384\ns_waitcnt lgkmcnt(6)\nv_mfma_f32_16x16x32_bf16 v[80:83], v[96:99], a[128:131], 0\n"
"s_lshl_b32 s53, s42, 19\ns_add_u32 s53, s53, s30\nv_mfma_f32_16x16x32_bf16 v[84:87], v[96:99], a[160:163], 0\nds_read_b128 v[96:99], v24 offset:16640\n"
"s_add_u32 m0, s31, 114688\ns_add_u32 s53, s53, 262144\nv_mfma_f32_16x16x32_bf16 v[88:91], v[100:103], a[128:131], 0\nbuffer_load_dwordx4 v8, s[8:11], s53 offen lds\n"
"v_mfma_f32_16x16x32_bf16 v[92:95], v[100:103], a[160:163], 0\nds_read_b128 v[100:103], v28 offset:16640\nbuffer_load_dwordx4 v9, s[8:11], s53 offen offset:1024 lds\ns_waitcnt lgkmcnt(6)\n"
"v_mfma_f32_16x16x32_bf16 v[80:83], v[104:107], a[132:135], v[80:83]\nv_mul_f32 v64, s35, v64\nv_mul_f32 v65, s35, v65\nv_mfma_f32_16x16x32_bf16 v[84:87], v[104:107], a[164:167], v[84:87]\n"
"ds_read_b128 v[104:107], v25 offset:16640\nv_mul_f32 v66, s35, v66\nv_mul_f32 v67, s35, v67\nv_mfma_f32_16x16x32_bf16 v[88:91], v[108:111], a[132:135], v[88:91]\n"
"v_cvt_pk_bf16_f32 v128, v64, v65\nv_cvt_pk_bf16_f32 v129, v66, v67\nv_mfma_f32_16x16x32_bf16 v[92:95], v[108:111], a[164:167], v[92:95]\nds_read_b128 v[108:111], v29 offset:16640\n"
"v_mul_f32 v72, s35, v72\nv_mul_f32 v73, s35, v73\ns_waitcnt lgkmcnt(6)\nv_mfma_f32_16x16x32_bf16 v[80:83], v[112:115], a[136:139], v[80:83]\n"
"v_mul_f32 v74, s35, v74\nv_mul_f32 v75, s35, v75\nv_mfma_f32_16x16x32_bf16 v[84:87], v[112:115], a[168:171], v[84:87]\nds_read_b128 v[112:115], v26 offset:16640\n"
"v_cvt_pk_bf16_f32 v130, v72, v73\nv_cvt_pk_bf16_f32 v131, v74, v75\nv_mfma_f32_16x16x32_bf16 v[88:91], v[116:119], a[136:139], v[88:91]\nv_mul_f32 v68, s35, v68\n"
"v_mul_f32 v69, s35, v69\nv_mfma_f32_16x16x32_bf16 v[92:95], v[116:119], a[168:171], v[92:95]\nds_read_b128 v[116:119], v30 offset:16640\nv_mul_f32 v70, s35, v70\n"
"v_mul_f32 v71, s35, v71\ns_waitcnt lgkmcnt(6)\nv_mfma_f32_16x16x32_bf16 v[80:83], v[120:123], a[140:143], v[80:83]\nv_cvt_pk_bf16_f32 v132, v68, v69\n"
"v_cvt_pk_bf16_f32 v133, v70, v71\nv_mfma_f32_16x16x32_bf16 v[84:87], v[120:123], a[172:175], v[84:87]\nds_read_b128 v[120:123], v27 offset:16640\nv_mul_f32 v76, s35, v76\n"
"v_mul_f32 v77, s35, v77\nv_mfma_f32_16x16x32_bf16 v[88:91], v[124:127], a[140:143], v[88:91]\nv_mul_f32 v78, s35, v78\nv_mul_f32 v79, s35, v79\n"
"v_mfma_f32_16x16x32_bf16 v[92:95], v[124:127], a[172:175], v[92:95]\nds_read_b128 v[124:127], v31 offset:16640\nv_cvt_pk_bf16_f32 v134, v76, v77\nv_cvt_pk_bf16_f32 v135, v78, v79\n"
"s_waitcnt vmcnt(6)\ns_barrier\ns_waitcnt lgkmcnt(6)\nv_mfma_f32_16x16x32_bf16 v[80:83], v[96:99], a[144:147], v[80:83]\n"
"s_lshl_b32 s54, s40, 11\ns_add_u32 s54, s54, 0\nv_mfma_f32_16x16x32_bf16 v[84:87], v[96:99], a[176:179], v[84:87]\nds_read_b128 v[96:99], v24 offset:32768\n"
"buffer_store_dwordx4 v[128:131], v13, s[16:19], s54 offen sc1\nv_mfma_f32_16x16x32_bf16 v[88:91], v[100:103], a[144:147], v[88:91]\ns_add_u32 s54, s54, 14336\nv_mfma_f32_16x16x32_bf16 v[92:95], v[100:103], a[176:179], v[92:95]\n"
"ds_read_b128 v[100:103], v28 offset:32768\nbuffer_store_dwordx4 v[132:135], v13, s[16:19], s54 offen sc1\ns_waitcnt lgkmcnt(6)\nv_mfma_f32_16x16x32_bf16 v[80:83], v[104:107], a[148:151], v[80:83]\n"
"s_lshl_b32 s53, s42, 19\ns_add_u32 s53, s53, s30\nv_mfma_f32_16x16x32_bf16 v[84:87], v[104:107], a[180:183], v[84:87]\nds_read_b128 v[104:107], v25 offset:32768\n"
"s_add_u32 m0, s31, 122880\ns_add_u32 s53, s53, 393216\nv_mfma_f32_16x16x32_bf16 v[88:91], v[108:111], a[148:151], v[88:91]\nbuffer_load_dwordx4 v8, s[8:11], s53 offen lds\n"
"v_mfma_f32_16x16x32_bf16 v[92:95], v[108:111], a[180:183], v[92:95]\nds_read_b128 v[108:111], v29 offset:32768\nbuffer_load_dwordx4 v9, s[8:11], s53 offen offset:1024 lds\ns_waitcnt lgkmcnt(6)\n"
"v_mfma_f32_16x16x32_bf16 v[80:83], v[112:115], a[152:155], v[80:83]\nv_mfma_f32_16x16x32_bf16 v[84:87], v[112:115], a[184:187], v[84:87]\nds_read_b128 v[112:115], v26 offset:32768\nv_mfma_f32_16x16x32_bf16 v[88:91], v[116:119], a[152:155], v[88:91]\n"
"v_mfma_f32_16x16x32_bf16 v[92:95], v[116:119], a[184:187], v[92:95]\nds_read_b128 v[116:119], v30 offset:32768\ns_waitcnt lgkmcnt(6)\nv_mfma_f32_16x16x32_bf16 v[80:83], v[120:123], a[156:159], v[80:83]\n"
"v_mfma_f32_16x16x32_bf16 v[84:87], v[120:123], a[188:191], v[84:87]\nds_read_b128 v[120:123], v27 offset:32768\nv_mfma_f32_16x16x32_bf16 v[88:91], v[124:127], a[156:159], v[88:91]\nv_mfma_f32_16x16x32_bf16 v[92:95], v[124:127], a[188:191], v[92:95]\n"
"ds_read_b128 v[124:127], v31 offset:32768\ns_waitcnt lgkmcnt(6)\nv_mfma_f32_16x16x32_bf16 v[64:67], v[96:99], a[128:131], 0\ns_lshl_b32 s53, s43, 19\n"
"s_add_u32 s53, s53, s30\nv_mfma_f32_16x16x32_bf16 v[68:71], v[96:99], a[160:163], 0\nds_read_b128 v[96:99], v24 offset:33024\ns_add_u32 m0, s31, 131072\n"
"s_add_u32 s53, s53, 0\nv_mfma_f32_16x16x32_bf16 v[72:75], v[100:103], a[128:131], 0\nbuffer_load_dwordx4 v8, s[8:11], s53 offen lds\nv_mfma_f32_16x16x32_bf16 v[76:79], v[100:103], a[160:163], 0\n"
"ds_read_b128 v[100:103], v28 offset:33024\nbuffer_load_dwordx4 v9, s[8:11], s53 offen offset:1024 lds\ns_waitcnt lgkmcnt(6)\nv_mfma_f32_16x16x32_bf16 v[64:67], v[104:107], a[132:135], v[64:67]\n"
"v_mul_f32 v80, s35, v80\nv_mul_f32 v81, s35, v81\nv_mfma_f32_16x16x32_bf16 v[68:71], v[104:107], a[164:167], v[68:71]\nds_read_b128 v[104:107], v25 offset:33024\n"
"v_mul_f32 v82, s35, v82\nv_mul_f32 v83, s35, v83\nv_mfma_f32_16x16x32_bf16 v[72:75], v[108:111], a[132:135], v[72:75]\nv_cvt_pk_bf16_f32 v136, v80, v81\n"
"v_cvt_pk_bf16_f32 v137, v82, v83\nv_mfma_f32_16x16x32_bf16 v[76:79], v[108:111], a[164:167], v[76:79]\nds_read_b128 v[108:111], v29 offset:33024\nv_mul_f32 v88, s35, v88\n"
"v_mul_f32 v89, s35, v89\ns_waitcnt lgkmcnt(6)\nv_mfma_f32_16x16x32_bf16 v[64:67], v[112:115], a[136:139], v[64:67]\nv_mul_f32 v90, s35, v90\n"
"v_mul_f32 v91, s35, v91\nv_mfma_f32_16x16x32_bf16 v[68:71], v[112:115], a[168:171], v[68:71]\nds_read_b128 v[112:115], v26 offset:33024\nv_cvt_pk_bf16_f32 v138, v88, v89\n"
"v_cvt_pk_bf16_f32 v139, v90, v91\nv_mfma_f32_16x16x32_bf16 v[72:75], v[116:119], a[136:139], v[72:75]\nv_mul_f32 v84, s35, v84\nv_mul_f32 v85, s35, v85\n"
"v_mfma_f32_16x16x32_bf16 v[76:79], v[116:119], a[168:171], v[76:79]\nds_read_b128 v[116:119], v30 offset:33024\nv_mul_f32 v86, s35, v86\nv_mul_f32 v87, s35, v87\n"
"s_waitcnt lgkmcnt(6)\nv_mfma_f32_16x16x32_bf16 v[64:67], v[120:123], a[140:143], v[64:67]\nv_cvt_pk_bf16_f32 v140, v84, v85\nv_cvt_pk_bf16_f32 v141, v86, v87\n"
"v_mfma_f32_16x16x32_bf16 v[68:71], v[120:123], a[172:175], v[68:71]\nds_read_b128 v[120:123], v27 offset:33024\nv_mul_f32 v92, s35, v92\nv_mul_f32 v93, s35, v93\n"
"v_mfma_f32_16x16x32_bf16 v[72:75], v[124:127], a[140:143], v[72:75]\nv_mul_f32 v94, s35, v94\nv_mul_f32 v95, s35, v95\nv_mfma_f32_16x16x32_bf16 v[76:79], v[124:127], a[172:175], v[76:79]\n"
"ds_read_b128 v[124:127], v31 offset:33024\nv_cvt_pk_bf16_f32 v142, v92, v93\nv_cvt_pk_bf16_f32 v143, v94, v95\ns_waitcnt lgkmcnt(6)\n"
"v_mfma_f32_16x16x32_bf16 v[64:67], v[96:99], a[144:147], v[64:67]\ns_lshl_b32 s54, s40, 11\ns_add_u32 s54, s54, 1024\nv_mfma_f32_16x16x32_bf16 v[68:71], v[96:99], a[176:179], v[68:71]\n"
"ds_read_b128 v[96:99], v24 offset:49152\nbuffer_store_dwordx4 v[136:139], v13, s[16:19], s54 offen sc1\nv_mfma_f32_16x16x32_bf16 v[72:75], v[100:103], a[144:147], v[72:75]\ns_add_u32 s54, s54, 14336\n"
"v_mfma_f32_16x16x32_bf16 v[76:79], v[100:103], a[176:179], v[76:79]\nds_read_b128 v[100:103], v28 offset:49152\nbuffer_store_dwordx4 v[140:143], v13, s[16:19], s54 offen sc1\ns_waitcnt lgkmcnt(6)\n"
"v_mfma_f32_16x16x32_bf16 v[64:67], v[104:107], a[148:151], v[64:67]\ns_lshl_b32 s53, s43, 19\ns_add_u32 s53, s53, s30\nv_mfma_f32_16x16x32_bf16 v[68:71], v[104:107], a[180:183], v[68:71]\n"
"ds_read_b128 v[104:107], v25 offset:49152\ns_add_u32 m0, s31, 139264\ns_add_u32 s53, s53, 131072\nv_mfma_f32_16x16x32_bf16 v[72:75], v[108:111], a[148:151], v[72:75]\n"
"buffer_load_dwordx4 v8, s[8:11], s53 offen lds\nv_mfma_f32_16x16x32_bf16 v[76:79], v[108:111], a[180:183], v[76:79]\nds_read_b128 v[108:111], v29 offset:49152\nbuffer_load_dwordx4 v9, s[8:11], s53 offen offset:1024 lds\n"
"s_waitcnt lgkmcnt(6)\nv_mfma_f32_16x16x32_bf16 v[64:67], v[112:115], a[152:155], v[64:67]\nv_mfma_f32_16x16x32_bf16 v[68:71], v[112:115], a[184:187], v[68:71]\nds_read_b128 v[112:115], v26 offset:49152\n"
"v_mfma_f32_16x16x32_bf16 v[72:75], v[116:119], a[152:155], v[72:75]\nv_mfma_f32_16x16x32_bf16 v[76:79], v[116:119], a[184:187], v[76:79]\nds_read_b128 v[116:119], v30 offset:49152\ns_waitcnt lgkmcnt(6)\n"
"v_mfma_f32_16x16x32_bf16 v[64:67], v[120:123], a[156:159], v[64:67]\nv_mfma_f32_16x16x32_bf16 v[68:71], v[120:123], a[188:191], v[68:71]\nds_read_b128 v[120:123], v27 offset:49152\nv_mfma_f32_16x16x32_bf16 v[72:75], v[124:127], a[156:159], v[72:75]\n"
"v_mfma_f32_16x16x32_bf16 v[76:79], v[124:127], a[188:191], v[76:79]\nds_read_b128 v[124:127], v31 offset:49152\ns_waitcnt lgkmcnt(6)\nv_mfma_f32_16x16x32_bf16 v[80:83], v[96:99], a[128:131], 0\n"
"s_lshl_b32 s53, s43, 19\ns_add_u32 s53, s53, s30\nv_mfma_f32_16x16x32_bf16 v[84:87], v[96:99], a[160:163], 0\nds_read_b128 v[96:99], v24 offset:49408\n"
"s_add_u32 m0, s31, 147456\ns_add_u32 s53, s53, 262144\nv_mfma_f32_16x16x32_bf16 v[88:91], v[100:103], a[128:131], 0\nbuffer_load_dwordx4 v8, s[8:11], s53 offen lds\n"
"v_mfma_f32_16x16x32_bf16 v[92:95], v[100:103], a[160:163], 0\nds_read_b128 v[100:103], v28 offset:49408\nbuffer_load_dwordx4 v9, s[8:11], s53 offen offset:1024 lds\ns_waitcnt lgkmcnt(6)\n"
"v_mfma_f32_16x16x32_bf16 v[80:83], v[104:107], a[132:135], v[80:83]\nv_mul_f32 v64, s35, v64\nv_mul_f32 v65, s35, v65\nv_mfma_f32_16x16x32_bf16 v[84:87], v[104:107], a[164:167], v[84:87]\n"
"ds_read_b128 v[104:107], v25 offset:49408\nv_mul_f32 v66, s35, v66\nv_mul_f32 v67, s35, v67\nv_mfma_f32_16x16x32_bf16 v[88:91], v[108:111], a[132:135], v[88:91]\n"
"v_cvt_pk_bf16_f32 v144, v64, v65\nv_cvt_pk_bf16_f32 v145, v66, v67\nv_mfma_f32_16x16x32_bf16 v[92:95], v[108:111], a[164:167], v[92:95]\nds_read_b128 v[108:111], v29 offset:49408\n"
"v_mul_f32 v72, s35, v72\nv_mul_f32 v73, s35, v73\ns_waitcnt lgkmcnt(6)\nv_mfma_f32_16x16x32_bf16 v[80:83], v[112:115], a[136:139], v[80:83]\n"
"v_mul_f32 v74, s35, v74\nv_mul_f32 v75, s35, v75\nv_mfma_f32_16x16x32_bf16 v[84:87], v[112:115], a[168:171], v[84:87]\nds_read_b128 v[112:115], v26 offset:49408\n"
"v_cvt_pk_bf16_f32 v146, v72, v73\nv_cvt_pk_bf16_f32 v147, v74, v75\nv_mfma_f32_16x16x32_bf16 v[88:91], v[116:119], a[136:139], v[88:91]\nv_mul_f32 v68, s35, v68\n"
"v_mul_f32 v69, s35, v69\nv_mfma_f32_16x16x32_bf16 v[92:95], v[116:119], a[168:171], v[92:95]\nds_read_b128 v[116:119], v30 offset:49408\nv_mul_f32 v70, s35, v70\n"
"v_mul_f32 v71, s35, v71\ns_waitcnt lgkmcnt(6)\nv_mfma_f32_16x16x32_bf16 v[80:83], v[120:123], a[140:143], v[80:83]\nv_cvt_pk_bf16_f32 v148, v68, v69\n"
"v_cvt_pk_bf16_f32 v149, v70, v71\nv_mfma_f32_16x16x32_bf16 v[84:87], v[120:123], a[172:175], v[84:87]\nds_read_b128 v[120:123], v27 offset:49408\nv_mul_f32 v76, s35, v76\n"
"v_mul_f32 v77, s35, v77\nv_mfma_f32_16x16x32_bf16 v[88:91], v[124:127], a[140:143], v[88:91]\nv_mul_f32 v78, s35, v78\nv_mul_f32 v79, s35, v79\n"
"v_mfma_f32_16x16x32_bf16 v[92:95], v[124:127], a[172:175], v[92:95]\nds_read_b128 v[124:127], v31 offset:49408\nv_cvt_pk_bf16_f32 v150, v76, v77\nv_cvt_pk_bf16_f32 v151, v78, v79\n"
"s_waitcnt vmcnt(8)\ns_barrier\ns_waitcnt lgkmcnt(6)\nv_mfma_f32_16x16x32_bf16 v[80:83], v[96:99], a[144:147], v[80:83]\n"
"s_lshl_b32 s54, s41, 11\ns_add_u32 s54, s54, 0\nv_mfma_f32_16x16x32_bf16 v[84:87], v[96:99], a[176:179], v[84:87]\nds_read_b128 v[96:99], v40 offset:0\n"
"buffer_store_dwordx4 v[144:147], v13, s[16:19], s54 offen sc1\nv_mfma_f32_16x16x32_bf16 v[88:91], v[100:103], a[144:147], v[88:91]\ns_add_u32 s54, s54, 14336\nv_mfma_f32_16x16x32_bf16 v[92:95], v[100:103], a[176:179], v[92:95]\n"
"ds_read_b128 v[100:103], v44 offset:0\nbuffer_store_dwordx4 v[148:151], v13, s[16:19], s54 offen sc1\ns_waitcnt lgkmcnt(6)\nv_mfma_f32_16x16x32_bf16 v[80:83], v[104:107], a[148:151], v[80:83]\n"
"s_lshl_b32 s53, s43, 19\ns_add_u32 s53, s53, s30\nv_mfma_f32_16x16x32_bf16 v[84:87], v[104:107], a[180:183], v[84:87]\nds_read_b128 v[104:107], v41 offset:0\n"
"s_add_u32 m0, s31, 155648\ns_add_u32 s53, s53, 393216\nv_mfma_f32_16x16x32_bf16 v[88:91], v[108:111], a[148:151], v[88:91]\nbuffer_load_dwordx4 v8, s[8:11], s53 offen lds\n"
"v_mfma_f32_16x16x32_bf16 v[92:95], v[108:111], a[180:183], v[92:95]\nds_read_b128 v[108:111], v45 offset:0\nbuffer_load_dwordx4 v9, s[8:11], s53 offen offset:1024 lds\ns_waitcnt lgkmcnt(6)\n"
"v_mfma_f32_16x16x32_bf16 v[80:83], v[112:115], a[152:155], v[80:83]\nv_mfma_f32_16x16x32_bf16 v[84:87], v[112:115], a[184:187], v[84:87]\nds_read_b128 v[112:115], v42 offset:0\nv_mfma_f32_16x16x32_bf16 v[88:91], v[116:119], a[152:155], v[88:91]\n"
"v_mfma_f32_16x16x32_bf16 v[92:95], v[116:119], a[184:187], v[92:95]\nds_read_b128 v[116:119], v46 offset:0\ns_waitcnt lgkmcnt(6)\nv_mfma_f32_16x16x32_bf16 v[80:83], v[120:123], a[156:159], v[80:83]\n"
"v_mfma_f32_16x16x32_bf16 v[84:87], v[120:123], a[188:191], v[84:87]\nds_read_b128 v[120:123], v43 offset:0\nv_mfma_f32_16x16x32_bf16 v[88:91], v[124:127], a[156:159], v[88:91]\nv_mfma_f32_16x16x32_bf16 v[92:95], v[124:127], a[188:191], v[92:95]\n"
"ds_read_b128 v[124:127], v47 offset:0\ns_waitcnt lgkmcnt(6)\nv_mfma_f32_16x16x32_bf16 v[64:67], v[96:99], a[128:131], 0\ns_add_u32 m0, s32, 0\n"
"v_mfma_f32_16x16x32_bf16 v[68:71], v[96:99], a[160:163], 0\nds_read_b128 v[96:99], v40 offset:256\ns_mov_b32 s53, 0\nv_mfma_f32_16x16x32_bf16 v[72:75], v[100:103], a[128:131], 0\n"
"buffer_load_dwordx4 v10, s[12:15], s53 offen lds\nv_mfma_f32_16x16x32_bf16 v[76:79], v[100:103], a[160:163], 0\nds_read_b128 v[100:103], v44 offset:256\nv_mul_f32 v80, s35, v80\n"
"v_mul_f32 v81, s35, v81\ns_waitcnt lgkmcnt(6)\nv_mfma_f32_16x16x32_bf16 v[64:67], v[104:107], a[132:135], v[64:67]\nv_mul_f32 v82, s35, v82\n"
"v_mul_f32 v83, s35, v83\nv_mfma_f32_16x16x32_bf16 v[68:71], v[104:107], a[164:167], v[68:71]\nds_read_b128 v[104:107], v41 offset:256\nv_cvt_pk_bf16_f32 v152, v80, v81\n"
"v_cvt_pk_bf16_f32 v153, v82, v83\nv_mfma_f32_16x16x32_bf16 v[72:75], v[108:111], a[132:135], v[72:75]\nv_mul_f32 v88, s35, v88\nv_mul_f32 v89, s35, v89\n"
"v_mfma_f32_16x16x32_bf16 v[76:79], v[108:111], a[164:167], v[76:79]\nds_read_b128 v[108:111], v45 offset:256\nv_mul_f32 v90, s35, v90\nv_mul_f32 v91, s35, v91\n"
"s_waitcnt lgkmcnt(6)\nv_mfma_f32_16x16x32_bf16 v[64:67], v[112:115], a[136:139], v[64:67]\nv_cvt_pk_bf16_f32 v154, v88, v89\nv_cvt_pk_bf16_f32 v155, v90, v91\n"
"v_mfma_f32_16x16x32_bf16 v[68:71], v[112:115], a[168:171], v[68:71]\nds_read_b128 v[112:115], v42 offset:256\nv_mul_f32 v84, s35, v84\nv_mul_f32 v85, s35, v85\n"
"v_mfma_f32_16x16x32_bf16 v[72:75], v[116:119], a[136:139], v[72:75]\nv_mul_f32 v86, s35, v86\nv_mul_f32 v87, s35, v87\nv_mfma_f32_16x16x32_bf16 v[76:79], v[116:119], a[168:171], v[76:79]\n"
"ds_read_b128 v[116:119], v46 offset:256\nv_cvt_pk_bf16_f32 v156, v84, v85\nv_cvt_pk_bf16_f32 v157, v86, v87\ns_waitcnt lgkmcnt(6)\n"
"v_mfma_f32_16x16x32_bf16 v[64:67], v[120:123], a[140:143], v[64:67]\nv_mul_f32 v92, s35, v92\nv_mul_f32 v93, s35, v93\nv_mfma_f32_16x16x32_bf16 v[68:71], v[120:123], a[172:175], v[68:71]\n"
"ds_read_b128 v[120:123], v43 offset:256\nv_mul_f32 v94, s35, v94\nv_mul_f32 v95, s35, v95\nv_mfma_f32_16x16x32_bf16 v[72:75], v[124:127], a[140:143], v[72:75]\n"
"v_cvt_pk_bf16_f32 v158, v92, v93\nv_cvt_pk_bf16_f32 v159, v94, v95\nv_mfma_f32_16x16x32_bf16 v[76:79], v[124:127], a[172:175], v[76:79]\nds_read_b128 v[124:127], v47 offset:256\n"
"s_lshl_b32 s54, s41, 11\ns_add_u32 s54, s54, 1024\ns_waitcnt lgkmcnt(6)\nv_mfma_f32_16x16x32_bf16 v[64:67], v[96:99], a[144:147], v[64:67]\n"
"buffer_store_dwordx4 v[152:155], v13, s[16:19], s54 offen sc1\nv_mfma_f32_16x16x32_bf16 v[68:71], v[96:99], a[176:179], v[68:71]\nds_read_b128 v[96:99], v40 offset:16384\ns_add_u32 s54, s54, 14336\n"
"v_mfma_f32_16x16x32_bf16 v[72:75], v[100:103], a[144:147], v[72:75]\nbuffer_store_dwordx4 v[156:159], v13, s[16:19], s54 offen sc1\nv_mfma_f32_16x16x32_bf16 v[76:79], v[100:103], a[176:179], v[76:79]\nds_read_b128 v[100:103], v44 offset:16384\n"
"s_add_u32 m0, s32, 4096\ns_waitcnt lgkmcnt(6)\nv_mfma_f32_16x16x32_bf16 v[64:67], v[104:107], a[148:151], v[64:67]\ns_mov_b32 s53, 128\n"
"v_mfma_f32_16x16x32_bf16 v[68:71], v[104:107], a[180:183], v[68:71]\nds_read_b128 v[104:107], v41 offset:16384\nbuffer_load_dwordx4 v10, s[12:15], s53 offen lds\nv_mfma_f32_16x16x32_bf16 v[72:75], v[108:111], a[148:151], v[72:75]\n"
"s_add_u32 m0, s32, 8192\nv_mfma_f32_16x16x32_bf16 v[76:79], v[108:111], a[180:183], v[76:79]\nds_read_b128 v[108:111], v45 offset:16384\ns_mov_b32 s53, 256\n"
"s_waitcnt lgkmcnt(6)\nv_mfma_f32_16x16x32_bf16 v[64:67], v[112:115], a[152:155], v[64:67]\nbuffer_load_dwordx4 v10, s[12:15], s53 offen lds\nv_mfma_f32_16x16x32_bf16 v[68:71], v[112:115], a[184:187], v[68:71]\n"
"ds_read_b128 v[112:115], v42 offset:16384\ns_add_u32 m0, s32, 12288\nv_mfma_f32_16x16x32_bf16 v[72:75], v[116:119], a[152:155], v[72:75]\ns_mov_b32 s53, 384\n"
"v_mfma_f32_16x16x32_bf16 v[76:79], v[116:119], a[184:187], v[76:79]\nds_read_b128 v[116:119], v46 offset:16384\nbuffer_load_dwordx4 v10, s[12:15], s53 offen lds\ns_waitcnt lgkmcnt(6)\n"
"v_mfma_f32_16x16x32_bf16 v[64:67], v[120:123], a[156:159], v[64:67]\nv_mfma_f32_16x16x32_bf16 v[68:71], v[120:123], a[188:191], v[68:71]\nds_read_b128 v[120:123], v43 offset:16384\nv_mfma_f32_16x16x32_bf16 v[72:75], v[124:127], a[156:159], v[72:75]\n"
"v_mfma_f32_16x16x32_bf16 v[76:79], v[124:127], a[188:191], v[76:79]\nds_read_b128 v[124:127], v47 offset:16384\ns_waitcnt lgkmcnt(6)\nv_mfma_f32_16x16x32_bf16 v[80:83], v[96:99], a[128:131], 0\n"
"s_add_u32 m0, s32, 16384\nv_mfma_f32_16x16x32_bf16 v[84:87], v[96:99], a[160:163], 0\nds_read_b128 v[96:99], v40 offset:16640\ns_mov_b32 s53, 512\n"
"v_mfma_f32_16x16x32_bf16 v[88:91], v[100:103], a[128:131], 0\nbuffer_load_dwordx4 v10, s[12:15], s53 offen lds\nv_mfma_f32_16x16x32_bf16 v[92:95], v[100:103], a[160:163], 0\nds_read_b128 v[100:103], v44 offset:16640\n"
"v_mul_f32 v64, s35, v64\nv_mul_f32 v65, s35, v65\ns_waitcnt lgkmcnt(6)\nv_mfma_f32_16x16x32_bf16 v[80:83], v[104:107], a[132:135], v[80:83]\n"
"v_mul_f32 v66, s35, v66\nv_mul_f32 v67, s35, v67\nv_mfma_f32_16x16x32_bf16 v[84:87], v[104:107], a[164:167], v[84:87]\nds_read_b128 v[104:107], v41 offset:16640\n"
"v_cvt_pk_bf16_f32 v160, v64, v65\nv_cvt_pk_bf16_f32 v161, v66, v67\nv_mfma_f32_16x16x32_bf16 v[88:91], v[108:111], a[132:135], v[88:91]\nv_mul_f32 v72, s35, v72\n"
"v_mul_f32 v73, s35, v73\nv_mfma_f32_16x16x32_bf16 v[92:95], v[108:111], a[164:167], v[92:95]\nds_read_b128 v[108:111], v45 offset:16640\nv_mul_f32 v74, s35, v74\n"
"v_mul_f32 v75, s35, v75\ns_waitcnt lgkmcnt(6)\nv_mfma_f32_16x16x32_bf16 v[80:83], v[112:115], a[136:139], v[80:83]\nv_cvt_pk_bf16_f32 v162, v72, v73\n"
"v_cvt_pk_bf16_f32 v163, v74, v75\nv_mfma_f32_16x16x32_bf16 v[84:87], v[112:115], a[168:171], v[84:87]\nds_read_b128 v[112:115], v42 offset:16640\nv_mul_f32 v68, s35, v68\n"
"v_mul_f32 v69, s35, v69\nv_mfma_f32_16x16x32_bf16 v[88:91], v[116:119], a[136:139], v[88:91]\nv_mul_f32 v70, s35, v70\nv_mul_f32 v71, s35, v71\n"
"v_mfma_f32_16x16x32_bf16 v[92:95], v[116:119], a[168:171], v[92:95]\nds_read_b128 v[116:119], v46 offset:16640\nv_cvt_pk_bf16_f32 v164, v68, v69\nv_cvt_pk_bf16_f32 v165, v70, v71\n"
"s_waitcnt lgkmcnt(6)\nv_mfma_f32_16x16x32_bf16 v[80:83], v[120:123], a[140:143], v[80:83]\nv_mul_f32 v76, s35, v76\nv_mul_f32 v77, s35, v77\n"
"v_mfma_f32_16x16x32_bf16 v[84:87], v[120:123], a[172:175], v[84:87]\nds_read_b128 v[120:123], v43 offset:16640\nv_mul_f32 v78, s35, v78\nv_mul_f32 v79, s35, v79\n"
"v_mfma_f32_16x16x32_bf16 v[88:91], v[124:127], a[140:143], v[88:91]\nv_cvt_pk_bf16_f32 v166, v76, v77\nv_cvt_pk_bf16_f32 v167, v78, v79\nv_mfma_f32_16x16x32_bf16 v[92:95], v[124:127], a[172:175], v[92:95]\n"
"ds_read_b128 v[124:127], v47 offset:16640\ns_lshl_b32 s54, s42, 11\ns_add_u32 s54, s54, 0\ns_waitcnt vmcnt(7)\n"
"s_barrier\ns_waitcnt lgkmcnt(6)\nv_mfma_f32_16x16x32_bf16 v[80:83], v[96:99], a[144:147], v[80:83]\nbuffer_store_dwordx4 v[160:163], v13, s[16:19], s54 offen sc1\n"
"v_mfma_f32_16x16x32_bf16 v[84:87], v[96:99], a[176:179], v[84:87]\nds_read_b128 v[96:99], v40 offset:32768\ns_add_u32 s54, s54, 14336\nv_mfma_f32_16x16x32_bf16 v[88:91], v[100:103], a[144:147], v[88:91]\n"
"buffer_store_dwordx4 v[164:167], v13, s[16:19], s54 offen sc1\nv_mfma_f32_16x16x32_bf16 v[92:95], v[100:103], a[176:179], v[92:95]\nds_read_b128 v[100:103], v44 offset:32768\ns_add_u32 m0, s32, 20480\n"
"s_waitcnt lgkmcnt(6)\nv_mfma_f32_16x16x32_bf16 v[80:83], v[104:107], a[148:151], v[80:83]\ns_mov_b32 s53, 640\nv_mfma_f32_16x16x32_bf16 v[84:87], v[104:107], a[180:183], v[84:87]\n"
"ds_read_b128 v[104:107], v41 offset:32768\nbuffer_load_dwordx4 v10, s[12:15], s53 offen lds\nv_mfma_f32_16x16x32_bf16 v[88:91], v[108:111], a[148:151], v[88:91]\ns_add_u32 m0, s32, 24576\n"
"v_mfma_f32_16x16x32_bf16 v[92:95], v[108:111], a[180:183], v[92:95]\nds_read_b128 v[108:111], v45 offset:32768\ns_mov_b32 s53, 768\ns_waitcnt lgkmcnt(6)\n"
"v_mfma_f32_16x16x32_bf16 v[80:83], v[112:115], a[152:155], v[80:83]\nbuffer_load_dwordx4 v10, s[12:15], s53 offen lds\nv_mfma_f32_16x16x32_bf16 v[84:87], v[112:115], a[184:187], v[84:87]\nds_read_b128 v[112:115], v42 offset:32768\n"
"v_mfma_f32_16x16x32_bf16 v[88:91], v[116:119], a[152:155], v[88:91]\nv_mfma_f32_16x16x32_bf16 v[92:95], v[116:119], a[184:187], v[92:95]\nds_read_b128 v[116:119], v46 offset:32768\ns_waitcnt lgkmcnt(6)\n"
"v_mfma_f32_16x16x32_bf16 v[80:83], v[120:123], a[156:159], v[80:83]\nv_mfma_f32_16x16x32_bf16 v[84:87], v[120:123], a[188:191], v[84:87]\nds_read_b128 v[120:123], v43 offset:32768\nv_mfma_f32_16x16x32_bf16 v[88:91], v[124:127], a[156:159], v[88:91]\n"
"v_mfma_f32_16x16x32_bf16 v[92:95], v[124:127], a[188:191], v[92:95]\nds_read_b128 v[124:127], v47 offset:32768\ns_cmp_lt_u32 s34, 2\ns_cbranch_scc1 fg0_main\n"
"s_nop 7\nv_mul_f32 v80, s35, v80\nv_mul_f32 v81, s35, v81\nv_mul_f32 v82, s35, v82\n"
"v_mul_f32 v83, s35, v83\nv_cvt_pk_bf16_f32 v168, v80, v81\nv_cvt_pk_bf16_f32 v169, v82, v83\nv_mul_f32 v88, s35, v88\n"
"v_mul_f32 v89, s35, v89\nv_mul_f32 v90, s35, v90\nv_mul_f32 v91, s35, v91\nv_cvt_pk_bf16_f32 v170, v88, v89\n"
"v_cvt_pk_bf16_f32 v171, v90, v91\nv_mul_f32 v84, s35, v84\nv_mul_f32 v85, s35, v85\nv_mul_f32 v86, s35, v86\n"
"v_mul_f32 v87, s35, v87\nv_cvt_pk_bf16_f32 v172, v84, v85\nv_cvt_pk_bf16_f32 v173, v86, v87\nv_mul_f32 v92, s35, v92\n"
"v_mul_f32 v93, s35, v93\nv_mul_f32 v94, s35, v94\nv_mul_f32 v95, s35, v95\nv_cvt_pk_bf16_f32 v174, v92, v93\n"
"v_cvt_pk_bf16_f32 v175, v94, v95\ns_lshl_b32 s54, s42, 11\ns_add_u32 s54, s54, 1024\nbuffer_store_dwordx4 v[168:171], v13, s[16:19], s54 offen sc1\n"
"s_add_u32 s54, s54, 14336\nbuffer_store_dwordx4 v[172:175], v13, s[16:19], s54 offen sc1\nbuffer_load_dword v246, v21, s[24:27], 0 offen sc1\nbuffer_load_dwordx2 v[244:245], v20, s[24:27], 0 offen sc1\n"
"buffer_load_dwordx2 v[248:249], v15, s[24:27], 0 offen sc1\nv_mov_b32 v36, s28\ns_waitcnt vmcnt(3) lgkmcnt(0)\ns_barrier\n"
"buffer_store_dword v36, v19, s[24:27], 0 offen sc1\ns_waitcnt vmcnt(1)\ns_branch fg0_end\nfg0_main:\n"
"s_waitcnt lgkmcnt(6)\nv_mfma_f32_16x16x32_bf16 v[64:67], v[96:99], a[128:131], 0\ns_add_u32 m0, s32, 32768\nv_mfma_f32_16x16x32_bf16 v[68:71], v[96:99], a[160:163], 0\n"
"ds_read_b128 v[96:99], v40 offset:33024\ns_mov_b32 s53, 28672\nv_mfma_f32_16x16x32_bf16 v[72:75], v[100:103], a[128:131], 0\nbuffer_load_dwordx4 v10, s[12:15], s53 offen lds\n"
"v_mfma_f32_16x16x32_bf16 v[76:79], v[100:103], a[160:163], 0\nds_read_b128 v[100:103], v44 offset:33024\nv_mul_f32 v80, s35, v80\nv_mul_f32 v81, s35, v81\n"
"s_waitcnt lgkmcnt(6)\nv_mfma_f32_16x16x32_bf16 v[64:67], v[104:107], a[132:135], v[64:67]\nv_mul_f32 v82, s35, v82\nv_mul_f32 v83, s35, v83\n"
"v_mfma_f32_16x16x32_bf16 v[68:71], v[104:107], a[164:167], v[68:71]\nds_read_b128 v[104:107], v41 offset:33024\nv_cvt_pk_bf16_f32 v168, v80, v81\nv_cvt_pk_bf16_f32 v169, v82, v83\n"
"v_mfma_f32_16x16x32_bf16 v[72:75], v[108:111], a[132:135], v[72:75]\nv_mul_f32 v88, s35, v88\nv_mul_f32 v89, s35, v89\nv_mfma_f32_16x16x32_bf16 v[76:79], v[108:111], a[164:167], v[76:79]\n"
"ds_read_b128 v[108:111], v45 offset:33024\nv_mul_f32 v90, s35, v90\nv_mul_f32 v91, s35, v91\ns_waitcnt lgkmcnt(6)\n"
"v_mfma_f32_16x16x32_bf16 v[64:67], v[112:115], a[136:139], v[64:67]\nv_cvt_pk_bf16_f32 v170, v88, v89\nv_cvt_pk_bf16_f32 v171, v90, v91\nv_mfma_f32_16x16x32_bf16 v[68:71], v[112:115], a[168:171], v[68:71]\n"
"ds_read_b128 v[112:115], v42 offset:33024\nv_mul_f32 v84, s35, v84\nv_mul_f32 v85, s35, v85\nv_mfma_f32_16x16x32_bf16 v[72:75], v[116:119], a[136:139], v[72:75]\n"
"v_mul_f32 v86, s35, v86\nv_mul_f32 v87, s35, v87\nv_mfma_f32_16x16x32_bf16 v[76:79], v[116:119], a[168:171], v[76:79]\nds_read_b128 v[116:119], v46 offset:33024\n"
"v_cvt_pk_bf16_f32 v172, v84, v85\nv_cvt_pk_bf16_f32 v173, v86, v87\ns_waitcnt lgkmcnt(6)\nv_mfma_f32_16x16x32_bf16 v[64:67], v[120:123], a[140:143], v[64:67]\n"
"v_mul_f32 v92, s35, v92\nv_mul_f32 v93, s35, v93\nv_mfma_f32_16x16x32_bf16 v[68:71], v[120:123], a[172:175], v[68:71]\nds_read_b128 v[120:123], v43 offset:33024\n"
"v_mul_f32 v94, s35, v94\nv_mul_f32 v95, s35, v95\nv_mfma_f32_16x16x32_bf16 v[72:75], v[124:127], a[140:143], v[72:75]\nv_cvt_pk_bf16_f32 v174, v92, v93\n"
"v_cvt_pk_bf16_f32 v175, v94, v95\nv_mfma_f32_16x16x32_bf16 v[76:79], v[124:127], a[172:175], v[76:79]\nds_read_b128 v[124:127], v47 offset:33024\ns_lshl_b32 s54, s42, 11\n"
"s_add_u32 s54, s54, 1024\ns_waitcnt lgkmcnt(6)\nv_mfma_f32_16x16x32_bf16 v[64:67], v[96:99], a[144:147], v[64:67]\nbuffer_store_dwordx4 v[168:171], v13, s[16:19], s54 offen sc1\n"
"v_mfma_f32_16x16x32_bf16 v[68:71], v[96:99], a[176:179], v[68:71]\nds_read_b128 v[96:99], v40 offset:49152\ns_add_u32 s54, s54, 14336\nv_mfma_f32_16x16x32_bf16 v[72:75], v[100:103], a[144:147], v[72:75]\n"
"buffer_store_dwordx4 v[172:175], v13, s[16:19], s54 offen sc1\nv_mfma_f32_16x16x32_bf16 v[76:79], v[100:103], a[176:179], v[76:79]\nds_read_b128 v[100:103], v44 offset:49152\ns_add_u32 m0, s32, 36864\n"
"s_waitcnt lgkmcnt(6)\nv_mfma_f32_16x16x32_bf16 v[64:67], v[104:107], a[148:151], v[64:67]\ns_mov_b32 s53, 28800\nv_mfma_f32_16x16x32_bf16 v[68:71], v[104:107], a[180:183], v[68:71]\n"
"ds_read_b128 v[104:107], v41 offset:49152\nbuffer_load_dwordx4 v10, s[12:15], s53 offen lds\nv_mfma_f32_16x16x32_bf16 v[72:75], v[108:111], a[148:151], v[72:75]\ns_add_u32 m0, s32, 40960\n"
"v_mfma_f32_16x16x32_bf16 v[76:79], v[108:111], a[180:183], v[76:79]\nds_read_b128 v[108:111], v45 offset:49152\ns_mov_b32 s53, 28928\ns_waitcnt lgkmcnt(6)\n"
"v_mfma_f32_16x16x32_bf16 v[64:67], v[112:115], a[152:155], v[64:67]\nbuffer_load_dwordx4 v10, s[12:15], s53 offen lds\nv_mfma_f32_16x16x32_bf16 v[68:71], v[112:115], a[184:187], v[68:71]\nds_read_b128 v[112:115], v42 offset:49152\n"
"s_add_u32 m0, s32, 45056\nv_mfma_f32_16x16x32_bf16 v[72:75], v[116:119], a[152:155], v[72:75]\ns_mov_b32 s53, 29056\nv_mfma_f32_16x16x32_bf16 v[76:79], v[116:119], a[184:187], v[76:79]\n"
"ds_read_b128 v[116:119], v46 offset:49152\nbuffer_load_dwordx4 v10, s[12:15], s53 offen lds\ns_waitcnt lgkmcnt(6)\nv_mfma_f32_16x16x32_bf16 v[64:67], v[120:123], a[156:159], v[64:67]\n"
"v_mfma_f32_16x16x32_bf16 v[68:71], v[120:123], a[188:191], v[68:71]\nds_read_b128 v[120:123], v43 offset:49152\nv_mfma_f32_16x16x32_bf16 v[72:75], v[124:127], a[156:159], v[72:75]\nv_mfma_f32_16x16x32_bf16 v[76:79], v[124:127], a[188:191], v[76:79]\n"
"ds_read_b128 v[124:127], v47 offset:49152\ns_waitcnt lgkmcnt(6)\nv_mfma_f32_16x16x32_bf16 v[80:83], v[96:99], a[128:131], 0\ns_add_u32 m0, s32, 49152\n"
"v_mfma_f32_16x16x32_bf16 v[84:87], v[96:99], a[160:163], 0\nds_read_b128 v[96:99], v40 offset:49408\ns_mov_b32 s53, 29184\nv_mfma_f32_16x16x32_bf16 v[88:91], v[100:103], a[128:131], 0\n"
"buffer_load_dwordx4 v10, s[12:15], s53 offen lds\nv_mfma_f32_16x16x32_bf16 v[92:95], v[100:103], a[160:163], 0\nds_read_b128 v[100:103], v44 offset:49408\nv_mul_f32 v64, s35, v64\n"
"v_mul_f32 v65, s35, v65\ns_waitcnt lgkmcnt(6)\nv_mfma_f32_16x16x32_bf16 v[80:83], v[104:107], a[132:135], v[80:83]\nv_mul_f32 v66, s35, v66\n"
"v_mul_f32 v67, s35, v67\nv_mfma_f32_16x16x32_bf16 v[84:87], v[104:107], a[164:167], v[84:87]\nds_read_b128 v[104:107], v41 offset:49408\nv_cvt_pk_bf16_f32 v176, v64, v65\n"
"v_cvt_pk_bf16_f32 v177, v66, v67\nv_mfma_f32_16x16x32_bf16 v[88:91], v[108:111], a[132:135], v[88:91]\nv_mul_f32 v72, s35, v72\nv_mul_f32 v73, s35, v73\n"
"v_mfma_f32_16x16x32_bf16 v[92:95], v[108:111], a[164:167], v[92:95]\nds_read_b128 v[108:111], v45 offset:49408\nv_mul_f32 v74, s35, v74\nv_mul_f32 v75, s35, v75\n"
"s_waitcnt lgkmcnt(6)\nv_mfma_f32_16x16x32_bf16 v[80:83], v[112:115], a[136:139], v[80:83]\nv_cvt_pk_bf16_f32 v178, v72, v73\nv_cvt_pk_bf16_f32 v179, v74, v75\n"
"v_mfma_f32_16x16x32_bf16 v[84:87], v[112:115], a[168:171], v[84:87]\nds_read_b128 v[112:115], v42 offset:49408\nv_mul_f32 v68, s35, v68\nv_mul_f32 v69, s35, v69\n"
"v_mfma_f32_16x16x32_bf16 v[88:91], v[116:119], a[136:139], v[88:91]\nv_mul_f32 v70, s35, v70\nv_mul_f32 v71, s35, v71\nv_mfma_f32_16x16x32_bf16 v[92:95], v[116:119], a[168:171], v[92:95]\n"
"ds_read_b128 v[116:119], v46 offset:49408\nv_cvt_pk_bf16_f32 v180, v68, v69\nv_cvt_pk_bf16_f32 v181, v70, v71\ns_waitcnt lgkmcnt(6)\n"
"v_mfma_f32_16x16x32_bf16 v[80:83], v[120:123], a[140:143], v[80:83]\nv_mul_f32 v76, s35, v76\nv_mul_f32 v77, s35, v77\nv_mfma_f32_16x16x32_bf16 v[84:87], v[120:123], a[172:175], v[84:87]\n"
"ds_read_b128 v[120:123], v43 offset:49408\nv_mul_f32 v78, s35, v78\nv_mul_f32 v79, s35, v79\nv_mfma_f32_16x16x32_bf16 v[88:91], v[124:127], a[140:143], v[88:91]\n"
"v_cvt_pk_bf16_f32 v182, v76, v77\nv_cvt_pk_bf16_f32 v183, v78, v79\nv_mfma_f32_16x16x32_bf16 v[92:95], v[124:127], a[172:175], v[92:95]\nds_read_b128 v[124:127], v47 offset:49408\n"
"s_lshl_b32 s54, s43, 11\ns_add_u32 s54, s54, 0\ns_waitcnt lgkmcnt(6)\nv_mfma_f32_16x16x32_bf16 v[80:83], v[96:99], a[144:147], v[80:83]\n"
"buffer_store_dwordx4 v[176:179], v13, s[16:19], s54 offen sc1\nv_mfma_f32_16x16x32_bf16 v[84:87], v[96:99], a[176:179], v[84:87]\ns_add_u32 s54, s54, 14336\nv_mfma_f32_16x16x32_bf16 v[88:91], v[100:103], a[144:147], v[88:91]\n"
"buffer_store_dwordx4 v[180:183], v13, s[16:19], s54 offen sc1\nv_mfma_f32_16x16x32_bf16 v[92:95], v[100:103], a[176:179], v[92:95]\ns_add_u32 m0, s32, 53248\ns_waitcnt lgkmcnt(4)\n"
"v_mfma_f32_16x16x32_bf16 v[80:83], v[104:107], a[148:151], v[80:83]\ns_mov_b32 s53, 29312\nv_mfma_f32_16x16x32_bf16 v[84:87], v[104:107], a[180:183], v[84:87]\nbuffer_load_dwordx4 v10, s[12:15], s53 offen lds\n"
"v_mfma_f32_16x16x32_bf16 v[88:91], v[108:111], a[148:151], v[88:91]\ns_add_u32 m0, s32, 57344\nv_mfma_f32_16x16x32_bf16 v[92:95], v[108:111], a[180:183], v[92:95]\ns_mov_b32 s53, 29440\n"
"s_waitcnt lgkmcnt(2)\nv_mfma_f32_16x16x32_bf16 v[80:83], v[112:115], a[152:155], v[80:83]\nbuffer_load_dwordx4 v10, s[12:15], s53 offen lds\nv_mfma_f32_16x16x32_bf16 v[84:87], v[112:115], a[184:187], v[84:87]\n"
"buffer_load_dword v37, v21, s[24:27], 0 offen sc1\nv_mfma_f32_16x16x32_bf16 v[88:91], v[116:119], a[152:155], v[88:91]\nv_mfma_f32_16x16x32_bf16 v[92:95], v[116:119], a[184:187], v[92:95]\ns_waitcnt lgkmcnt(0)\n"
"v_mfma_f32_16x16x32_bf16 v[80:83], v[120:123], a[156:159], v[80:83]\nv_mfma_f32_16x16x32_bf16 v[84:87], v[120:123], a[188:191], v[84:87]\nv_mfma_f32_16x16x32_bf16 v[88:91], v[124:127], a[156:159], v[88:91]\nv_mfma_f32_16x16x32_bf16 v[92:95], v[124:127], a[188:191], v[92:95]\n"
"s_nop 7\nv_mul_f32 v80, s35, v80\nv_mul_f32 v81, s35, v81\nv_mul_f32 v82, s35, v82\n"
"v_mul_f32 v83, s35, v83\nv_cvt_pk_bf16_f32 v184, v80, v81\nv_cvt_pk_bf16_f32 v185, v82, v83\nv_mul_f32 v88, s35, v88\n"
"v_mul_f32 v89, s35, v89\nv_mul_f32 v90, s35, v90\nv_mul_f32 v91, s35, v91\nv_cvt_pk_bf16_f32 v186, v88, v89\n"
"v_cvt_pk_bf16_f32 v187, v90, v91\nv_mul_f32 v84, s35, v84\nv_mul_f32 v85, s35, v85\nv_mul_f32 v86, s35, v86\n"
"v_mul_f32 v87, s35, v87\nv_cvt_pk_bf16_f32 v188, v84, v85\nv_cvt_pk_bf16_f32 v189, v86, v87\nv_mul_f32 v92, s35, v92\n"
"v_mul_f32 v93, s35, v93\nv_mul_f32 v94, s35, v94\nv_mul_f32 v95, s35, v95\nv_cvt_pk_bf16_f32 v190, v92, v93\n"
"v_cvt_pk_bf16_f32 v191, v94, v95\ns_lshl_b32 s54, s43, 11\ns_add_u32 s54, s54, 1024\nbuffer_store_dwordx4 v[184:187], v13, s[16:19], s54 offen sc1\n"
"s_add_u32 s54, s54, 14336\nbuffer_store_dwordx4 v[188:191], v13, s[16:19], s54 offen sc1\ns_waitcnt vmcnt(2)\nv_mov_b32 v36, s28\n"
"v_cmp_ne_u32 vcc, s28, v37\ns_cbranch_vccz fg0_e1\ns_waitcnt vmcnt(0)\ns_barrier\n"
"buffer_store_dword v36, v19, s[24:27], 0 offen sc1\nfg0_po:\nv_cmp_ne_u32 vcc, s28, v37\ns_cbranch_vccz fg0_pook\n"
"s_add_u32 s55, s55, 1\ns_cmp_gt_u32 s55, 262144\ns_cbranch_scc0 fg0_po2\ns_endpgm\n"
"fg0_po2:\ns_sleep 1\nbuffer_load_dword v37, v21, s[24:27], 0 offen sc1\ns_waitcnt vmcnt(0)\n"
"s_branch fg0_po\nfg0_pook:\ns_mov_b32 s53, 8192\nbuffer_load_dwordx4 v[192:195], v13, s[16:19], s53 offen sc1\n"
"s_mov_b32 s53, 22528\nbuffer_load_dwordx4 v[196:199], v13, s[16:19], s53 offen sc1\ns_mov_b32 s53, 9216\nbuffer_load_dwordx4 v[200:203], v13, s[16:19], s53 offen sc1\n"
"s_mov_b32 s53, 23552\nbuffer_load_dwordx4 v[204:207], v13, s[16:19], s53 offen sc1\ns_mov_b32 s53, 10240\nbuffer_load_dwordx4 v[208:211], v13, s[16:19], s53 offen sc1\n"
"s_mov_b32 s53, 24576\nbuffer_load_dwordx4 v[212:215], v13, s[16:19], s53 offen sc1\ns_mov_b32 s53, 11264\nbuffer_load_dwordx4 v[216:219], v13, s[16:19], s53 offen sc1\n"
"s_mov_b32 s53, 25600\nbuffer_load_dwordx4 v[220:223], v13, s[16:19], s53 offen sc1\ns_mov_b32 s53, 12288\nbuffer_load_dwordx4 v[224:227], v13, s[16:19], s53 offen sc1\n"
"s_mov_b32 s53, 26624\nbuffer_load_dwordx4 v[228:231], v13, s[16:19], s53 offen sc1\ns_mov_b32 s53, 13312\nbuffer_load_dwordx4 v[232:235], v13, s[16:19], s53 offen sc1\n"
"s_mov_b32 s53, 27648\nbuffer_load_dwordx4 v[236:239], v13, s[16:19], s53 offen sc1\nbuffer_store_dword v36, v19, s[24:27], 0 offen sc1\ns_branch fg0_e2\n"
"fg0_e1:\ns_mov_b32 s53, 8192\nbuffer_load_dwordx4 v[192:195], v13, s[16:19], s53 offen sc1\ns_mov_b32 s53, 22528\n"
"buffer_load_dwordx4 v[196:199], v13, s[16:19], s53 offen sc1\ns_mov_b32 s53, 9216\nbuffer_load_dwordx4 v[200:203], v13, s[16:19], s53 offen sc1\ns_mov_b32 s53, 23552\n"
"buffer_load_dwordx4 v[204:207], v13, s[16:19], s53 offen sc1\ns_mov_b32 s53, 10240\nbuffer_load_dwordx4 v[208:211], v13, s[16:19], s53 offen sc1\ns_mov_b32 s53, 24576\n"
"buffer_load_dwordx4 v[212:215], v13, s[16:19], s53 offen sc1\ns_mov_b32 s53, 11264\nbuffer_load_dwordx4 v[216:219], v13, s[16:19], s53 offen sc1\ns_mov_b32 s53, 25600\n"
"buffer_load_dwordx4 v[220:223], v13, s[16:19], s53 offen sc1\ns_mov_b32 s53, 12288\nbuffer_load_dwordx4 v[224:227], v13, s[16:19], s53 offen sc1\ns_mov_b32 s53, 26624\n"
"buffer_load_dwordx4 v[228:231], v13, s[16:19], s53 offen sc1\ns_mov_b32 s53, 13312\nbuffer_load_dwordx4 v[232:235], v13, s[16:19], s53 offen sc1\ns_mov_b32 s53, 27648\n"
"buffer_load_dwordx4 v[236:239], v13, s[16:19], s53 offen sc1\ns_waitcnt vmcnt(12)\ns_barrier\nbuffer_store_dword v36, v19, s[24:27], 0 offen sc1\n"
"fg0_e2:\ns_add_u32 m0, s32, 65536\ns_mov_b32 s53, 57344\nbuffer_load_dwordx4 v10, s[12:15], s53 offen lds\n"
"s_add_u32 m0, s32, 69632\ns_mov_b32 s53, 57472\nbuffer_load_dwordx4 v10, s[12:15], s53 offen lds\ns_add_u32 m0, s32, 73728\n"
"s_mov_b32 s53, 57600\nbuffer_load_dwordx4 v10, s[12:15], s53 offen lds\ns_add_u32 m0, s32, 77824\ns_mov_b32 s53, 57728\n"
"buffer_load_dwordx4 v10, s[12:15], s53 offen lds\ns_add_u32 m0, s32, 81920\ns_mov_b32 s53, 57856\nbuffer_load_dwordx4 v10, s[12:15], s53 offen lds\n"
"s_add_u32 m0, s32, 86016\ns_mov_b32 s53, 57984\nbuffer_load_dwordx4 v10, s[12:15], s53 offen lds\ns_add_u32 m0, s32, 90112\n"
"s_mov_b32 s53, 58112\nbuffer_load_dwordx4 v10, s[12:15], s53 offen lds\ns_sub_u32 s48, s28, 1\ns_mov_b32 s49, 86016\n"
"s_mov_b32 s50, 0\ns_mov_b32 s51, 0\ns_add_u32 s52, s32, 98304\ns_add_u32 s47, s32, 131072\n"
"ds_read_b128 v[40:43], v32 offset:0\nds_read_b128 v[44:47], v33 offset:0\nds_read_b128 v[48:51], v34 offset:0\nds_read_b128 v[52:55], v35 offset:0\n"
"s_barrier\ns_waitcnt lgkmcnt(3)\nv_mfma_f32_16x16x32_bf16 v[64:67], v[128:131], v[40:43], 0\nv_mov_b32 v36, s48\n"
"s_add_u32 s48, s48, 1\ns_nop 0\nbuffer_store_dword v36, v19, s[24:27], s33 offen sc1\nv_mfma_f32_16x16x32_bf16 v[68:71], v[132:135], v[40:43], 0\n"
"ds_read_b128 v[40:43], v32 offset:4096\ns_waitcnt lgkmcnt(3)\nv_mfma_f32_16x16x32_bf16 v[72:75], v[128:131], v[44:47], 0\nv_mfma_f32_16x16x32_bf16 v[76:79], v[132:135], v[44:47], 0\n"
"ds_read_b128 v[44:47], v33 offset:4096\ns_waitcnt lgkmcnt(3)\nv_mfma_f32_16x16x32_bf16 v[64:67], v[136:139], v[48:51], v[64:67]\ns_add_u32 m0, s52, 0\n"
"v_mfma_f32_16x16x32_bf16 v[68:71], v[140:143], v[48:51], v[68:71]\nds_read_b128 v[48:51], v34 offset:4096\ns_add_u32 s53, s49, 0\ns_waitcnt lgkmcnt(3)\n"
"v_mfma_f32_16x16x32_bf16 v[72:75], v[136:139], v[52:55], v[72:75]\nbuffer_load_dwordx4 v10, s[12:15], s53 offen lds\nv_mfma_f32_16x16x32_bf16 v[76:79], v[140:143], v[52:55], v[76:79]\nds_read_b128 v[52:55], v35 offset:4096\n"
"s_add_u32 m0, s52, 4096\ns_waitcnt lgkmcnt(3)\nv_mfma_f32_16x16x32_bf16 v[64:67], v[144:147], v[40:43], v[64:67]\ns_add_u32 s53, s49, 128\n"
"v_mfma_f32_16x16x32_bf16 v[68:71], v[148:151], v[40:43], v[68:71]\nds_read_b128 v[40:43], v32 offset:8192\nbuffer_load_dwordx4 v10, s[12:15], s53 offen lds\ns_waitcnt lgkmcnt(3)\n"
"v_mfma_f32_16x16x32_bf16 v[72:75], v[144:147], v[44:47], v[72:75]\ns_add_u32 m0, s52, 8192\nv_mfma_f32_16x16x32_bf16 v[76:79], v[148:151], v[44:47], v[76:79]\nds_read_b128 v[44:47], v33 offset:8192\n"
"s_add_u32 s53, s49, 256\ns_waitcnt lgkmcnt(3)\nv_mfma_f32_16x16x32_bf16 v[64:67], v[152:155], v[48:51], v[64:67]\nbuffer_load_dwordx4 v10, s[12:15], s53 offen lds\n"
"v_mfma_f32_16x16x32_bf16 v[68:71], v[156:159], v[48:51], v[68:71]\nds_read_b128 v[48:51], v34 offset:8192\ns_add_u32 m0, s52, 12288\ns_waitcnt lgkmcnt(3)\n"
"v_mfma_f32_16x16x32_bf16 v[72:75], v[152:155], v[52:55], v[72:75]\ns_add_u32 s53, s49, 384\nv_mfma_f32_16x16x32_bf16 v[76:79], v[156:159], v[52:55], v[76:79]\nds_read_b128 v[52:55], v35 offset:8192\n"
"buffer_load_dwordx4 v10, s[12:15], s53 offen lds\ns_waitcnt lgkmcnt(3)\nv_mfma_f32_16x16x32_bf16 v[64:67], v[160:163], v[40:43], v[64:67]\ns_add_u32 m0, s52, 16384\n"
"v_mfma_f32_16x16x32_bf16 v[68:71], v[164:167], v[40:43], v[68:71]\nds_read_b128 v[40:43], v32 offset:12288\ns_add_u32 s53, s49, 512\ns_waitcnt lgkmcnt(3)\n"
"v_mfma_f32_16x16x32_bf16 v[72:75], v[160:163], v[44:47], v[72:75]\nbuffer_load_dwordx4 v10, s[12:15], s53 offen lds\nv_mfma_f32_16x16x32_bf16 v[76:79], v[164:167], v[44:47], v[76:79]\nds_read_b128 v[44:47], v33 offset:12288\n"
"s_add_u32 m0, s52, 20480\ns_waitcnt lgkmcnt(3)\nv_mfma_f32_16x16x32_bf16 v[64:67], v[168:171], v[48:51], v[64:67]\ns_add_u32 s53, s49, 640\n"
"v_mfma_f32_16x16x32_bf16 v[68:71], v[172:175], v[48:51], v[68:71]\nds_read_b128 v[48:51], v34 offset:12288\nbuffer_load_dwordx4 v10, s[12:15], s53 offen lds\ns_waitcnt lgkmcnt(3)\n"
"v_mfma_f32_16x16x32_bf16 v[72:75], v[168:171], v[52:55], v[72:75]\ns_add_u32 m0, s52, 24576\nv_mfma_f32_16x16x32_bf16 v[76:79], v[172:175], v[52:55], v[76:79]\nds_read_b128 v[52:55], v35 offset:12288\n"
"s_add_u32 s53, s49, 768\ns_waitcnt lgkmcnt(3)\nv_mfma_f32_16x16x32_bf16 v[64:67], v[176:179], v[40:43], v[64:67]\nbuffer_load_dwordx4 v10, s[12:15], s53 offen lds\n"
"v_mfma_f32_16x16x32_bf16 v[68:71], v[180:183], v[40:43], v[68:71]\nds_read_b128 v[40:43], v32 offset:16384\ns_waitcnt lgkmcnt(3)\nv_mfma_f32_16x16x32_bf16 v[72:75], v[176:179], v[44:47], v[72:75]\n"
"v_mfma_f32_16x16x32_bf16 v[76:79], v[180:183], v[44:47], v[76:79]\nds_read_b128 v[44:47], v33 offset:16384\ns_waitcnt lgkmcnt(3)\nv_mfma_f32_16x16x32_bf16 v[64:67], v[184:187], v[48:51], v[64:67]\n"
"v_mfma_f32_16x16x32_bf16 v[68:71], v[188:191], v[48:51], v[68:71]\nds_read_b128 v[48:51], v34 offset:16384\ns_waitcnt lgkmcnt(3)\nv_mfma_f32_16x16x32_bf16 v[72:75], v[184:187], v[52:55], v[72:75]\n"
"v_mfma_f32_16x16x32_bf16 v[76:79], v[188:191], v[52:55], v[76:79]\nds_read_b128 v[52:55], v35 offset:16384\ns_waitcnt vmcnt(16)\ns_waitcnt lgkmcnt(3)\n"
"v_mfma_f32_16x16x32_bf16 v[64:67], v[192:195], v[40:43], v[64:67]\nv_mfma_f32_16x16x32_bf16 v[68:71], v[196:199], v[40:43], v[68:71]\nds_read_b128 v[40:43], v32 offset:20480\ns_waitcnt lgkmcnt(3)\n"
"v_mfma_f32_16x16x32_bf16 v[72:75], v[192:195], v[44:47], v[72:75]\nv_mfma_f32_16x16x32_bf16 v[76:79], v[196:199], v[44:47], v[76:79]\nds_read_b128 v[44:47], v33 offset:20480\ns_waitcnt lgkmcnt(3)\n"
"v_mfma_f32_16x16x32_bf16 v[64:67], v[200:203], v[48:51], v[64:67]\nv_mfma_f32_16x16x32_bf16 v[68:71], v[204:207], v[48:51], v[68:71]\nds_read_b128 v[48:51], v34 offset:20480\ns_waitcnt lgkmcnt(3)\n"
"v_mfma_f32_16x16x32_bf16 v[72:75], v[200:203], v[52:55], v[72:75]\nv_mfma_f32_16x16x32_bf16 v[76:79], v[204:207], v[52:55], v[76:79]\nds_read_b128 v[52:55], v35 offset:20480\ns_waitcnt lgkmcnt(3)\n"
"v_mfma_f32_16x16x32_bf16 v[64:67], v[208:211], v[40:43], v[64:67]\nv_mfma_f32_16x16x32_bf16 v[68:71], v[212:215], v[40:43], v[68:71]\nds_read_b128 v[40:43], v32 offset:24576\ns_waitcnt lgkmcnt(3)\n"
"v_mfma_f32_16x16x32_bf16 v[72:75], v[208:211], v[44:47], v[72:75]\nv_mfma_f32_16x16x32_bf16 v[76:79], v[212:215], v[44:47], v[76:79]\nds_read_b128 v[44:47], v33 offset:24576\ns_waitcnt lgkmcnt(3)\n"
"v_mfma_f32_16x16x32_bf16 v[64:67], v[216:219], v[48:51], v[64:67]\ns_add_u32 s54, s51, 32768\ns_cmp_eq_u32 s54, 163840\nv_mfma_f32_16x16x32_bf16 v[68:71], v[220:223], v[48:51], v[68:71]\n"
"s_cselect_b32 s54, 0, s54\ns_sub_u32 s54, s54, s51\nds_read_b128 v[48:51], v34 offset:24576\ns_waitcnt lgkmcnt(3)\n"
"v_mfma_f32_16x16x32_bf16 v[72:75], v[216:219], v[52:55], v[72:75]\nv_mfma_f32_16x16x32_bf16 v[76:79], v[220:223], v[52:55], v[76:79]\nds_read_b128 v[52:55], v35 offset:24576\ns_waitcnt lgkmcnt(3)\n"
"v_mfma_f32_16x16x32_bf16 v[64:67], v[224:227], v[40:43], v[64:67]\nv_add_u32 v32, s54, v32\nv_mfma_f32_16x16x32_bf16 v[68:71], v[228:231], v[40:43], v[68:71]\nds_read_b128 v[40:43], v32 offset:0\n"
"s_waitcnt lgkmcnt(3)\nv_mfma_f32_16x16x32_bf16 v[72:75], v[224:227], v[44:47], v[72:75]\nv_add_u32 v33, s54, v33\nv_mfma_f32_16x16x32_bf16 v[76:79], v[228:231], v[44:47], v[76:79]\n"
"ds_read_b128 v[44:47], v33 offset:0\ns_waitcnt lgkmcnt(3)\nv_mfma_f32_16x16x32_bf16 v[64:67], v[232:235], v[48:51], v[64:67]\nv_add_u32 v34, s54, v34\n"
"v_mfma_f32_16x16x32_bf16 v[68:71], v[236:239], v[48:51], v[68:71]\nds_read_b128 v[48:51], v34 offset:0\ns_waitcnt lgkmcnt(3)\nv_mfma_f32_16x16x32_bf16 v[72:75], v[232:235], v[52:55], v[72:75]\n"
"v_add_u32 v35, s54, v35\nv_mfma_f32_16x16x32_bf16 v[76:79], v[236:239], v[52:55], v[76:79]\nds_read_b128 v[52:55], v35 offset:0\ns_mov_b32 s52, s47\n"
"s_add_u32 s47, s51, s32\ns_add_u32 s51, s51, s54\ns_add_u32 s49, s49, 28672\ns_waitcnt vmcnt(8)\n"
"s_barrier\ns_waitcnt lgkmcnt(3)\nv_mfma_f32_16x16x32_bf16 v[96:99], v[128:131], v[40:43], 0\nv_mov_b32 v36, s48\n"
"s_add_u32 s48, s48, 1\ns_nop 0\nbuffer_store_dword v36, v19, s[24:27], s33 offen sc1\nv_mfma_f32_16x16x32_bf16 v[100:103], v[132:135], v[40:43], 0\n"
"ds_read_b128 v[40:43], v32 offset:4096\ns_waitcnt lgkmcnt(3)\nv_mfma_f32_16x16x32_bf16 v[104:107], v[128:131], v[44:47], 0\nv_mfma_f32_16x16x32_bf16 v[108:111], v[132:135], v[44:47], 0\n"
"ds_read_b128 v[44:47], v33 offset:4096\nv_cvt_pk_bf16_f32 v56, v64, v65\ns_waitcnt lgkmcnt(3)\nv_mfma_f32_16x16x32_bf16 v[96:99], v[136:139], v[48:51], v[96:99]\n"
"v_cvt_pk_bf16_f32 v57, v66, v67\nv_mfma_f32_16x16x32_bf16 v[100:103], v[140:143], v[48:51], v[100:103]\nds_read_b128 v[48:51], v34 offset:4096\nv_cvt_pk_bf16_f32 v58, v68, v69\n"
"s_waitcnt lgkmcnt(3)\nv_mfma_f32_16x16x32_bf16 v[104:107], v[136:139], v[52:55], v[104:107]\nv_cvt_pk_bf16_f32 v59, v70, v71\nv_mfma_f32_16x16x32_bf16 v[108:111], v[140:143], v[52:55], v[108:111]\n"
"ds_read_b128 v[52:55], v35 offset:4096\ns_waitcnt lgkmcnt(3)\nv_mfma_f32_16x16x32_bf16 v[96:99], v[144:147], v[40:43], v[96:99]\nv_mfma_f32_16x16x32_bf16 v[100:103], v[148:151], v[40:43], v[100:103]\n"
"ds_read_b128 v[40:43], v32 offset:8192\nbuffer_store_dwordx4 v[56:59], v15, s[20:23], s50 offen sc1\ns_waitcnt lgkmcnt(3)\nv_mfma_f32_16x16x32_bf16 v[104:107], v[144:147], v[44:47], v[104:107]\n"
"v_cvt_pk_bf16_f32 v60, v72, v73\nv_mfma_f32_16x16x32_bf16 v[108:111], v[148:151], v[44:47], v[108:111]\nds_read_b128 v[44:47], v33 offset:8192\nv_cvt_pk_bf16_f32 v61, v74, v75\n"
"s_waitcnt lgkmcnt(3)\nv_mfma_f32_16x16x32_bf16 v[96:99], v[152:155], v[48:51], v[96:99]\nv_cvt_pk_bf16_f32 v62, v76, v77\nv_mfma_f32_16x16x32_bf16 v[100:103], v[156:159], v[48:51], v[100:103]\n"
"ds_read_b128 v[48:51], v34 offset:8192\nv_cvt_pk_bf16_f32 v63, v78, v79\ns_waitcnt lgkmcnt(3)\nv_mfma_f32_16x16x32_bf16 v[104:107], v[152:155], v[52:55], v[104:107]\n"
"v_mfma_f32_16x16x32_bf16 v[108:111], v[156:159], v[52:55], v[108:111]\nds_read_b128 v[52:55], v35 offset:8192\ns_waitcnt lgkmcnt(3)\nv_mfma_f32_16x16x32_bf16 v[96:99], v[160:163], v[40:43], v[96:99]\n"
"buffer_store_dwordx4 v[60:63], v16, s[20:23], s50 offen sc1\nv_mfma_f32_16x16x32_bf16 v[100:103], v[164:167], v[40:43], v[100:103]\nds_read_b128 v[40:43], v32 offset:12288\ns_add_u32 s50, s50, s36\n"
"s_waitcnt lgkmcnt(3)\nv_mfma_f32_16x16x32_bf16 v[104:107], v[160:163], v[44:47], v[104:107]\ns_add_u32 m0, s52, 0\nv_mfma_f32_16x16x32_bf16 v[108:111], v[164:167], v[44:47], v[108:111]\n"
"ds_read_b128 v[44:47], v33 offset:12288\ns_add_u32 s53, s49, 0\ns_waitcnt lgkmcnt(3)\nv_mfma_f32_16x16x32_bf16 v[96:99], v[168:171], v[48:51], v[96:99]\n"
"buffer_load_dwordx4 v10, s[12:15], s53 offen lds\nv_mfma_f32_16x16x32_bf16 v[100:103], v[172:175], v[48:51], v[100:103]\nds_read_b128 v[48:51], v34 offset:12288\ns_add_u32 m0, s52, 4096\n"
"s_waitcnt lgkmcnt(3)\nv_mfma_f32_16x16x32_bf16 v[104:107], v[168:171], v[52:55], v[104:107]\ns_add_u32 s53, s49, 128\nv_mfma_f32_16x16x32_bf16 v[108:111], v[172:175], v[52:55], v[108:111]\n"
"ds_read_b128 v[52:55], v35 offset:12288\nbuffer_load_dwordx4 v10, s[12:15], s53 offen lds\ns_waitcnt lgkmcnt(3)\nv_mfma_f32_16x16x32_bf16 v[96:99], v[176:179], v[40:43], v[96:99]\n"
"s_add_u32 m0, s52, 8192\nv_mfma_f32_16x16x32_bf16 v[100:103], v[180:183], v[40:43], v[100:103]\nds_read_b128 v[40:43], v32 offset:16384\ns_add_u32 s53, s49, 256\n"
"s_waitcnt lgkmcnt(3)\nv_mfma_f32_16x16x32_bf16 v[104:107], v[176:179], v[44:47], v[104:107]\nbuffer_load_dwordx4 v10, s[12:15], s53 offen lds\nv_mfma_f32_16x16x32_bf16 v[108:111], v[180:183], v[44:47], v[108:111]\n"
"ds_read_b128 v[44:47], v33 offset:16384\ns_add_u32 m0, s52, 12288\ns_waitcnt lgkmcnt(3)\nv_mfma_f32_16x16x32_bf16 v[96:99], v[184:187], v[48:51], v[96:99]\n"
"s_add_u32 s53, s49, 384\nv_mfma_f32_16x16x32_bf16 v[100:103], v[188:191], v[48:51], v[100:103]\nds_read_b128 v[48:51], v34 offset:16384\nbuffer_load_dwordx4 v10, s[12:15], s53 offen lds\n"
"s_waitcnt lgkmcnt(3)\nv_mfma_f32_16x16x32_bf16 v[104:107], v[184:187], v[52:55], v[104:107]\ns_add_u32 m0, s52, 16384\nv_mfma_f32_16x16x32_bf16 v[108:111], v[188:191], v[52:55], v[108:111]\n"
"ds_read_b128 v[52:55], v35 offset:16384\ns_add_u32 s53, s49, 512\ns_waitcnt lgkmcnt(3)\nv_mfma_f32_16x16x32_bf16 v[96:99], v[192:195], v[40:43], v[96:99]\n"
"buffer_load_dwordx4 v10, s[12:15], s53 offen lds\nv_mfma_f32_16x16x32_bf16 v[100:103], v[196:199], v[40:43], v[100:103]\nds_read_b128 v[40:43], v32 offset:20480\ns_add_u32 m0, s52, 20480\n"
"s_waitcnt lgkmcnt(3)\nv_mfma_f32_16x16x32_bf16 v[104:107], v[192:195], v[44:47], v[104:107]\ns_add_u32 s53, s49, 640\nv_mfma_f32_16x16x32_bf16 v[108:111], v[196:199], v[44:47], v[108:111]\n"
"ds_read_b128 v[44:47], v33 offset:20480\nbuffer_load_dwordx4 v10, s[12:15], s53 offen lds\ns_waitcnt lgkmcnt(3)\nv_mfma_f32_16x16x32_bf16 v[96:99], v[200:203], v[48:51], v[96:99]\n"
"s_add_u32 m0, s52, 24576\nv_mfma_f32_16x16x32_bf16 v[100:103], v[204:207], v[48:51], v[100:103]\nds_read_b128 v[48:51], v34 offset:20480\ns_add_u32 s53, s49, 768\n"
"s_waitcnt lgkmcnt(3)\nv_mfma_f32_16x16x32_bf16 v[104:107], v[200:203], v[52:55], v[104:107]\nbuffer_load_dwordx4 v10, s[12:15], s53 offen lds\nv_mfma_f32_16x16x32_bf16 v[108:111], v[204:207], v[52:55], v[108:111]\n"
"ds_read_b128 v[52:55], v35 offset:20480\ns_waitcnt lgkmcnt(3)\nv_mfma_f32_16x16x32_bf16 v[96:99], v[208:211], v[40:43], v[96:99]\nv_mfma_f32_16x16x32_bf16 v[100:103], v[212:215], v[40:43], v[100:103]\n"
"ds_read_b128 v[40:43], v32 offset:24576\ns_waitcnt lgkmcnt(3)\nv_mfma_f32_16x16x32_bf16 v[104:107], v[208:211], v[44:47], v[104:107]\nv_mfma_f32_16x16x32_bf16 v[108:111], v[212:215], v[44:47], v[108:111]\n"
"ds_read_b128 v[44:47], v33 offset:24576\ns_waitcnt lgkmcnt(3)\nv_mfma_f32_16x16x32_bf16 v[96:99], v[216:219], v[48:51], v[96:99]\ns_add_u32 s54, s51, 32768\n"
"s_cmp_eq_u32 s54, 163840\nv_mfma_f32_16x16x32_bf16 v[100:103], v[220:223], v[48:51], v[100:103]\ns_cselect_b32 s54, 0, s54\ns_sub_u32 s54, s54, s51\n"
"ds_read_b128 v[48:51], v34 offset:24576\ns_waitcnt lgkmcnt(3)\nv_mfma_f32_16x16x32_bf16 v[104:107], v[216:219], v[52:55], v[104:107]\nv_mfma_f32_16x16x32_bf16 v[108:111], v[220:223], v[52:55], v[108:111]\n"
"ds_read_b128 v[52:55], v35 offset:24576\ns_waitcnt lgkmcnt(3)\nv_mfma_f32_16x16x32_bf16 v[96:99], v[224:227], v[40:43], v[96:99]\nv_add_u32 v32, s54, v32\n"
"v_mfma_f32_16x16x32_bf16 v[100:103], v[228:231], v[40:43], v[100:103]\nds_read_b128 v[40:43], v32 offset:0\ns_waitcnt lgkmcnt(3)\nv_mfma_f32_16x16x32_bf16 v[104:107], v[224:227], v[44:47], v[104:107]\n"
"v_add_u32 v33, s54, v33\nv_mfma_f32_16x16x32_bf16 v[108:111], v[228:231], v[44:47], v[108:111]\nds_read_b128 v[44:47], v33 offset:0\ns_waitcnt lgkmcnt(3)\n"
"v_mfma_f32_16x16x32_bf16 v[96:99], v[232:235], v[48:51], v[96:99]\nv_add_u32 v34, s54, v34\nv_mfma_f32_16x16x32_bf16 v[100:103], v[236:239], v[48:51], v[100:103]\nds_read_b128 v[48:51], v34 offset:0\n"
"s_waitcnt lgkmcnt(3)\nv_mfma_f32_16x16x32_bf16 v[104:107], v[232:235], v[52:55], v[104:107]\nv_add_u32 v35, s54, v35\nv_mfma_f32_16x16x32_bf16 v[108:111], v[236:239], v[52:55], v[108:111]\n"
"ds_read_b128 v[52:55], v35 offset:0\ns_mov_b32 s52, s47\ns_add_u32 s47, s51, s32\ns_add_u32 s51, s51, s54\n"
"s_add_u32 s49, s49, 28672\ns_waitcnt vmcnt(7)\ns_barrier\ns_waitcnt lgkmcnt(3)\n"
"v_mfma_f32_16x16x32_bf16 v[64:67], v[128:131], v[40:43], 0\nv_mov_b32 v36, s48\ns_add_u32 s48, s48, 1\ns_nop 0\n"
"buffer_store_dword v36, v19, s[24:27], s33 offen sc1\nv_mfma_f32_16x16x32_bf16 v[68:71], v[132:135], v[40:43], 0\nds_read_b128 v[40:43], v32 offset:4096\ns_waitcnt lgkmcnt(3)\n"
"v_mfma_f32_16x16x32_bf16 v[72:75], v[128:131], v[44:47], 0\nv_mfma_f32_16x16x32_bf16 v[76:79], v[132:135], v[44:47], 0\nds_read_b128 v[44:47], v33 offset:4096\nv_cvt_pk_bf16_f32 v56, v96, v97\n"
"s_waitcnt lgkmcnt(3)\nv_mfma_f32_16x16x32_bf16 v[64:67], v[136:139], v[48:51], v[64:67]\nv_cvt_pk_bf16_f32 v57, v98, v99\nv_mfma_f32_16x16x32_bf16 v[68:71], v[140:143], v[48:51], v[68:71]\n"
"ds_read_b128 v[48:51], v34 offset:4096\nv_cvt_pk_bf16_f32 v58, v100, v101\ns_waitcnt lgkmcnt(3)\nv_mfma_f32_16x16x32_bf16 v[72:75], v[136:139], v[52:55], v[72:75]\n"
"v_cvt_pk_bf16_f32 v59, v102, v103\nv_mfma_f32_16x16x32_bf16 v[76:79], v[140:143], v[52:55], v[76:79]\nds_read_b128 v[52:55], v35 offset:4096\ns_waitcnt lgkmcnt(3)\n"
"v_mfma_f32_16x16x32_bf16 v[64:67], v[144:147], v[40:43], v[64:67]\nv_mfma_f32_16x16x32_bf16 v[68:71], v[148:151], v[40:43], v[68:71]\nds_read_b128 v[40:43], v32 offset:8192\nbuffer_store_dwordx4 v[56:59], v15, s[20:23], s50 offen sc1\n"
"s_waitcnt lgkmcnt(3)\nv_mfma_f32_16x16x32_bf16 v[72:75], v[144:147], v[44:47], v[72:75]\nv_cvt_pk_bf16_f32 v60, v104, v105\nv_mfma_f32_16x16x32_bf16 v[76:79], v[148:151], v[44:47], v[76:79]\n"
"ds_read_b128 v[44:47], v33 offset:8192\nv_cvt_pk_bf16_f32 v61, v106, v107\ns_waitcnt lgkmcnt(3)\nv_mfma_f32_16x16x32_bf16 v[64:67], v[152:155], v[48:51], v[64:67]\n"
"v_cvt_pk_bf16_f32 v62, v108, v109\nv_mfma_f32_16x16x32_bf16 v[68:71], v[156:159], v[48:51], v[68:71]\nds_read_b128 v[48:51], v34 offset:8192\nv_cvt_pk_bf16_f32 v63, v110, v111\n"
"s_waitcnt lgkmcnt(3)\nv_mfma_f32_16x16x32_bf16 v[72:75], v[152:155], v[52:55], v[72:75]\nv_mfma_f32_16x16x32_bf16 v[76:79], v[156:159], v[52:55], v[76:79]\nds_read_b128 v[52:55], v35 offset:8192\n"
"s_waitcnt lgkmcnt(3)\nv_mfma_f32_16x16x32_bf16 v[64:67], v[160:163], v[40:43], v[64:67]\nbuffer_store_dwordx4 v[60:63], v16, s[20:23], s50 offen sc1\nv_mfma_f32_16x16x32_bf16 v[68:71], v[164:167], v[40:43], v[68:71]\n"
"ds_read_b128 v[40:43], v32 offset:12288\ns_add_u32 s50, s50, s36\ns_waitcnt lgkmcnt(3)\nv_mfma_f32_16x16x32_bf16 v[72:75], v[160:163], v[44:47], v[72:75]\n"
"s_add_u32 m0, s52, 0\nv_mfma_f32_16x16x32_bf16 v[76:79], v[164:167], v[44:47], v[76:79]\nds_read_b128 v[44:47], v33 offset:12288\ns_add_u32 s53, s49, 0\n"
"s_waitcnt lgkmcnt(3)\nv_mfma_f32_16x16x32_bf16 v[64:67], v[168:171], v[48:51], v[64:67]\nbuffer_load_dwordx4 v10, s[12:15], s53 offen lds\nv_mfma_f32_16x16x32_bf16 v[68:71], v[172:175], v[48:51], v[68:71]\n"
"ds_read_b128 v[48:51], v34 offset:12288\ns_add_u32 m0, s52, 4096\ns_waitcnt lgkmcnt(3)\nv_mfma_f32_16x16x32_bf16 v[72:75], v[168:171], v[52:55], v[72:75]\n"
"s_add_u32 s53, s49, 128\nv_mfma_f32_16x16x32_bf16 v[76:79], v[172:175], v[52:55], v[76:79]\nds_read_b128 v[52:55], v35 offset:12288\nbuffer_load_dwordx4 v10, s[12:15], s53 offen lds\n"
"s_waitcnt lgkmcnt(3)\nv_mfma_f32_16x16x32_bf16 v[64:67], v[176:179], v[40:43], v[64:67]\ns_add_u32 m0, s52, 8192\nv_mfma_f32_16x16x32_bf16 v[68:71], v[180:183], v[40:43], v[68:71]\n"
"ds_read_b128 v[40:43], v32 offset:16384\ns_add_u32 s53, s49, 256\ns_waitcnt lgkmcnt(3)\nv_mfma_f32_16x16x32_bf16 v[72:75], v[176:179], v[44:47], v[72:75]\n"
"buffer_load_dwordx4 v10, s[12:15], s53 offen lds\nv_mfma_f32_16x16x32_bf16 v[76:79], v[180:183], v[44:47], v[76:79]\nds_read_b128 v[44:47], v33 offset:16384\ns_add_u32 m0, s52, 12288\n"
"s_waitcnt lgkmcnt(3)\nv_mfma_f32_16x16x32_bf16 v[64:67], v[184:187], v[48:51], v[64:67]\ns_add_u32 s53, s49, 384\nv_mfma_f32_16x16x32_bf16 v[68:71], v[188:191], v[48:51], v[68:71]\n"
"ds_read_b128 v[48:51], v34 offset:16384\nbuffer_load_dwordx4 v10, s[12:15], s53 offen lds\ns_waitcnt lgkmcnt(3)\nv_mfma_f32_16x16x32_bf16 v[72:75], v[184:187], v[52:55], v[72:75]\n"
"s_add_u32 m0, s52, 16384\nv_mfma_f32_16x16x32_bf16 v[76:79], v[188:191], v[52:55], v[76:79]\nds_read_b128 v[52:55], v35 offset:16384\ns_add_u32 s53, s49, 512\n"
"s_waitcnt lgkmcnt(3)\nv_mfma_f32_16x16x32_bf16 v[64:67], v[192:195], v[40:43], v[64:67]\nbuffer_load_dwordx4 v10, s[12:15], s53 offen lds\nv_mfma_f32_16x16x32_bf16 v[68:71], v[196:199], v[40:43], v[68:71]\n"
"ds_read_b128 v[40:43], v32 offset:20480\ns_add_u32 m0, s52, 20480\ns_waitcnt lgkmcnt(3)\nv_mfma_f32_16x16x32_bf16 v[72:75], v[192:195], v[44:47], v[72:75]\n"
"s_add_u32 s53, s49, 640\nv_mfma_f32_16x16x32_bf16 v[76:79], v[196:199], v[44:47], v[76:79]\nds_read_b128 v[44:47], v33 offset:20480\nbuffer_load_dwordx4 v10, s[12:15], s53 offen lds\n"
"s_waitcnt lgkmcnt(3)\nv_mfma_f32_16x16x32_bf16 v[64:67], v[200:203], v[48:51], v[64:67]\ns_add_u32 m0, s52, 24576\nv_mfma_f32_16x16x32_bf16 v[68:71], v[204:207], v[48:51], v[68:71]\n"
"ds_read_b128 v[48:51], v34 offset:20480\ns_add_u32 s53, s49, 768\ns_waitcnt lgkmcnt(3)\nv_mfma_f32_16x16x32_bf16 v[72:75], v[200:203], v[52:55], v[72:75]\n"
"buffer_load_dwordx4 v10, s[12:15], s53 offen lds\nv_mfma_f32_16x16x32_bf16 v[76:79], v[204:207], v[52:55], v[76:79]\nds_read_b128 v[52:55], v35 offset:20480\ns_waitcnt lgkmcnt(3)\n"
"v_mfma_f32_16x16x32_bf16 v[64:67], v[208:211], v[40:43], v[64:67]\nv_mfma_f32_16x16x32_bf16 v[68:71], v[212:215], v[40:43], v[68:71]\nds_read_b128 v[40:43], v32 offset:24576\ns_waitcnt lgkmcnt(3)\n"
"v_mfma_f32_16x16x32_bf16 v[72:75], v[208:211], v[44:47], v[72:75]\nv_mfma_f32_16x16x32_bf16 v[76:79], v[212:215], v[44:47], v[76:79]\nds_read_b128 v[44:47], v33 offset:24576\ns_waitcnt lgkmcnt(3)\n"
"v_mfma_f32_16x16x32_bf16 v[64:67], v[216:219], v[48:51], v[64:67]\ns_add_u32 s54, s51, 32768\ns_cmp_eq_u32 s54, 163840\nv_mfma_f32_16x16x32_bf16 v[68:71], v[220:223], v[48:51], v[68:71]\n"
"s_cselect_b32 s54, 0, s54\ns_sub_u32 s54, s54, s51\nds_read_b128 v[48:51], v34 offset:24576\ns_waitcnt lgkmcnt(3)\n"
"v_mfma_f32_16x16x32_bf16 v[72:75], v[216:219], v[52:55], v[72:75]\nv_mfma_f32_16x16x32_bf16 v[76:79], v[220:223], v[52:55], v[76:79]\nds_read_b128 v[52:55], v35 offset:24576\ns_waitcnt lgkmcnt(3)\n"
"v_mfma_f32_16x16x32_bf16 v[64:67], v[224:227], v[40:43], v[64:67]\nv_add_u32 v32, s54, v32\nv_mfma_f32_16x16x32_bf16 v[68:71], v[228:231], v[40:43], v[68:71]\nds_read_b128 v[40:43], v32 offset:0\n"
"s_waitcnt lgkmcnt(3)\nv_mfma_f32_16x16x32_bf16 v[72:75], v[224:227], v[44:47], v[72:75]\nv_add_u32 v33, s54, v33\nv_mfma_f32_16x16x32_bf16 v[76:79], v[228:231], v[44:47], v[76:79]\n"
"ds_read_b128 v[44:47], v33 offset:0\ns_waitcnt lgkmcnt(3)\nv_mfma_f32_16x16x32_bf16 v[64:67], v[232:235], v[48:51], v[64:67]\nv_add_u32 v34, s54, v34\n"
"v_mfma_f32_16x16x32_bf16 v[68:71], v[236:239], v[48:51], v[68:71]\nds_read_b128 v[48:51], v34 offset:0\ns_waitcnt lgkmcnt(3)\nv_mfma_f32_16x16x32_bf16 v[72:75], v[232:235], v[52:55], v[72:75]\n"
"v_add_u32 v35, s54, v35\nv_mfma_f32_16x16x32_bf16 v[76:79], v[236:239], v[52:55], v[76:79]\nds_read_b128 v[52:55], v35 offset:0\ns_mov_b32 s52, s47\n"
"s_add_u32 s47, s51, s32\ns_add_u32 s51, s51, s54\ns_add_u32 s49, s49, 28672\ns_waitcnt vmcnt(7)\n"
"s_barrier\ns_waitcnt lgkmcnt(3)\nv_mfma_f32_16x16x32_bf16 v[96:99], v[128:131], v[40:43], 0\nv_mov_b32 v36, s48\n"
"s_add_u32 s48, s48, 1\ns_nop 0\nbuffer_store_dword v36, v19, s[24:27], s33 offen sc1\nv_mfma_f32_16x16x32_bf16 v[100:103], v[132:135], v[40:43], 0\n"
"ds_read_b128 v[40:43], v32 offset:4096\ns_waitcnt lgkmcnt(3)\nv_mfma_f32_16x16x32_bf16 v[104:107], v[128:131], v[44:47], 0\nv_mfma_f32_16x16x32_bf16 v[108:111], v[132:135], v[44:47], 0\n"
"ds_read_b128 v[44:47], v33 offset:4096\nv_cvt_pk_bf16_f32 v56, v64, v65\ns_waitcnt lgkmcnt(3)\nv_mfma_f32_16x16x32_bf16 v[96:99], v[136:139], v[48:51], v[96:99]\n"
"v_cvt_pk_bf16_f32 v57, v66, v67\nv_mfma_f32_16x16x32_bf16 v[100:103], v[140:143], v[48:51], v[100:103]\nds_read_b128 v[48:51], v34 offset:4096\nv_cvt_pk_bf16_f32 v58, v68, v69\n"
"s_waitcnt lgkmcnt(3)\nv_mfma_f32_16x16x32_bf16 v[104:107], v[136:139], v[52:55], v[104:107]\nv_cvt_pk_bf16_f32 v59, v70, v71\nv_mfma_f32_16x16x32_bf16 v[108:111], v[140:143], v[52:55], v[108:111]\n"
"ds_read_b128 v[52:55], v35 offset:4096\ns_waitcnt lgkmcnt(3)\nv_mfma_f32_16x16x32_bf16 v[96:99], v[144:147], v[40:43], v[96:99]\nv_mfma_f32_16x16x32_bf16 v[100:103], v[148:151], v[40:43], v[100:103]\n"
"ds_read_b128 v[40:43], v32 offset:8192\nbuffer_store_dwordx4 v[56:59], v15, s[20:23], s50 offen sc1\ns_waitcnt lgkmcnt(3)\nv_mfma_f32_16x16x32_bf16 v[104:107], v[144:147], v[44:47], v[104:107]\n"
"v_cvt_pk_bf16_f32 v60, v72, v73\nv_mfma_f32_16x16x32_bf16 v[108:111], v[148:151], v[44:47], v[108:111]\nds_read_b128 v[44:47], v33 offset:8192\nv_cvt_pk_bf16_f32 v61, v74, v75\n"
"s_waitcnt lgkmcnt(3)\nv_mfma_f32_16x16x32_bf16 v[96:99], v[152:155], v[48:51], v[96:99]\nv_cvt_pk_bf16_f32 v62, v76, v77\nv_mfma_f32_16x16x32_bf16 v[100:103], v[156:159], v[48:51], v[100:103]\n"
"ds_read_b128 v[48:51], v34 offset:8192\nv_cvt_pk_bf16_f32 v63, v78, v79\ns_waitcnt lgkmcnt(3)\nv_mfma_f32_16x16x32_bf16 v[104:107], v[152:155], v[52:55], v[104:107]\n"
"v_mfma_f32_16x16x32_bf16 v[108:111], v[156:159], v[52:55], v[108:111]\nds_read_b128 v[52:55], v35 offset:8192\ns_waitcnt lgkmcnt(3)\nv_mfma_f32_16x16x32_bf16 v[96:99], v[160:163], v[40:43], v[96:99]\n"
"buffer_store_dwordx4 v[60:63], v16, s[20:23], s50 offen sc1\nv_mfma_f32_16x16x32_bf16 v[100:103], v[164:167], v[40:43], v[100:103]\nds_read_b128 v[40:43], v32 offset:12288\ns_add_u32 s50, s50, s36\n"
"s_waitcnt lgkmcnt(3)\nv_mfma_f32_16x16x32_bf16 v[104:107], v[160:163], v[44:47], v[104:107]\ns_add_u32 m0, s52, 0\nv_mfma_f32_16x16x32_bf16 v[108:111], v[164:167], v[44:47], v[108:111]\n"
"ds_read_b128 v[44:47], v33 offset:12288\ns_add_u32 s53, s49, 0\ns_waitcnt lgkmcnt(3)\nv_mfma_f32_16x16x32_bf16 v[96:99], v[168:171], v[48:51], v[96:99]\n"
"buffer_load_dwordx4 v10, s[12:15], s53 offen lds\nv_mfma_f32_16x16x32_bf16 v[100:103], v[172:175], v[48:51], v[100:103]\nds_read_b128 v[48:51], v34 offset:12288\ns_add_u32 m0, s52, 4096\n"
"s_waitcnt lgkmcnt(3)\nv_mfma_f32_16x16x32_bf16 v[104:107], v[168:171], v[52:55], v[104:107]\ns_add_u32 s53, s49, 128\nv_mfma_f32_16x16x32_bf16 v[108:111], v[172:175], v[52:55], v[108:111]\n"
"ds_read_b128 v[52:55], v35 offset:12288\nbuffer_load_dwordx4 v10, s[12:15], s53 offen lds\ns_waitcnt lgkmcnt(3)\nv_mfma_f32_16x16x32_bf16 v[96:99], v[176:179], v[40:43], v[96:99]\n"
"s_add_u32 m0, s52, 8192\nv_mfma_f32_16x16x32_bf16 v[100:103], v[180:183], v[40:43], v[100:103]\nds_read_b128 v[40:43], v32 offset:16384\ns_add_u32 s53, s49, 256\n"
"s_waitcnt lgkmcnt(3)\nv_mfma_f32_16x16x32_bf16 v[104:107], v[176:179], v[44:47], v[104:107]\nbuffer_load_dwordx4 v10, s[12:15], s53 offen lds\nv_mfma_f32_16x16x32_bf16 v[108:111], v[180:183], v[44:47], v[108:111]\n"
"ds_read_b128 v[44:47], v33 offset:16384\ns_add_u32 m0, s52, 12288\ns_waitcnt lgkmcnt(3)\nv_mfma_f32_16x16x32_bf16 v[96:99], v[184:187], v[48:51], v[96:99]\n"
"s_add_u32 s53, s49, 384\nv_mfma_f32_16x16x32_bf16 v[100:103], v[188:191], v[48:51], v[100:103]\nds_read_b128 v[48:51], v34 offset:16384\nbuffer_load_dwordx4 v10, s[12:15], s53 offen lds\n"
"s_waitcnt lgkmcnt(3)\nv_mfma_f32_16x16x32_bf16 v[104:107], v[184:187], v[52:55], v[104:107]\ns_add_u32 m0, s52, 16384\nv_mfma_f32_16x16x32_bf16 v[108:111], v[188:191], v[52:55], v[108:111]\n"
"ds_read_b128 v[52:55], v35 offset:16384\ns_add_u32 s53, s49, 512\ns_waitcnt lgkmcnt(3)\nv_mfma_f32_16x16x32_bf16 v[96:99], v[192:195], v[40:43], v[96:99]\n"
"buffer_load_dwordx4 v10, s[12:15], s53 offen lds\nv_mfma_f32_16x16x32_bf16 v[100:103], v[196:199], v[40:43], v[100:103]\nds_read_b128 v[40:43], v32 offset:20480\ns_add_u32 m0, s52, 20480\n"
"s_waitcnt lgkmcnt(3)\nv_mfma_f32_16x16x32_bf16 v[104:107], v[192:195], v[44:47], v[104:107]\ns_add_u32 s53, s49, 640\nv_mfma_f32_16x16x32_bf16 v[108:111], v[196:199], v[44:47], v[108:111]\n"
"ds_read_b128 v[44:47], v33 offset:20480\nbuffer_load_dwordx4 v10, s[12:15], s53 offen lds\ns_waitcnt lgkmcnt(3)\nv_mfma_f32_16x16x32_bf16 v[96:99], v[200:203], v[48:51], v[96:99]\n"
"s_add_u32 m0, s52, 24576\nv_mfma_f32_16x16x32_bf16 v[100:103], v[204:207], v[48:51], v[100:103]\nds_read_b128 v[48:51], v34 offset:20480\ns_add_u32 s53, s49, 768\n"
"s_waitcnt lgkmcnt(3)\nv_mfma_f32_16x16x32_bf16 v[104:107], v[200:203], v[52:55], v[104:107]\nbuffer_load_dwordx4 v10, s[12:15], s53 offen lds\nv_mfma_f32_16x16x32_bf16 v[108:111], v[204:207], v[52:55], v[108:111]\n"
"ds_read_b128 v[52:55], v35 offset:20480\ns_waitcnt lgkmcnt(3)\nv_mfma_f32_16x16x32_bf16 v[96:99], v[208:211], v[40:43], v[96:99]\nv_mfma_f32_16x16x32_bf16 v[100:103], v[212:215], v[40:43], v[100:103]\n"
"ds_read_b128 v[40:43], v32 offset:24576\ns_waitcnt lgkmcnt(3)\nv_mfma_f32_16x16x32_bf16 v[104:107], v[208:211], v[44:47], v[104:107]\nv_mfma_f32_16x16x32_bf16 v[108:111], v[212:215], v[44:47], v[108:111]\n"
"ds_read_b128 v[44:47], v33 offset:24576\ns_waitcnt lgkmcnt(3)\nv_mfma_f32_16x16x32_bf16 v[96:99], v[216:219], v[48:51], v[96:99]\ns_add_u32 s54, s51, 32768\n"
"s_cmp_eq_u32 s54, 163840\nv_mfma_f32_16x16x32_bf16 v[100:103], v[220:223], v[48:51], v[100:103]\ns_cselect_b32 s54, 0, s54\ns_sub_u32 s54, s54, s51\n"
"ds_read_b128 v[48:51], v34 offset:24576\ns_waitcnt lgkmcnt(3)\nv_mfma_f32_16x16x32_bf16 v[104:107], v[216:219], v[52:55], v[104:107]\nv_mfma_f32_16x16x32_bf16 v[108:111], v[220:223], v[52:55], v[108:111]\n"
"ds_read_b128 v[52:55], v35 offset:24576\ns_waitcnt lgkmcnt(3)\nv_mfma_f32_16x16x32_bf16 v[96:99], v[224:227], v[40:43], v[96:99]\nv_add_u32 v32, s54, v32\n"
"v_mfma_f32_16x16x32_bf16 v[100:103], v[228:231], v[40:43], v[100:103]\nds_read_b128 v[40:43], v32 offset:0\ns_waitcnt lgkmcnt(3)\nv_mfma_f32_16x16x32_bf16 v[104:107], v[224:227], v[44:47], v[104:107]\n"
"v_add_u32 v33, s54, v33\nv_mfma_f32_16x16x32_bf16 v[108:111], v[228:231], v[44:47], v[108:111]\nds_read_b128 v[44:47], v33 offset:0\ns_waitcnt lgkmcnt(3)\n"
"v_mfma_f32_16x16x32_bf16 v[96:99], v[232:235], v[48:51], v[96:99]\nv_add_u32 v34, s54, v34\nv_mfma_f32_16x16x32_bf16 v[100:103], v[236:239], v[48:51], v[100:103]\nds_read_b128 v[48:51], v34 offset:0\n"
"s_waitcnt lgkmcnt(3)\nv_mfma_f32_16x16x32_bf16 v[104:107], v[232:235], v[52:55], v[104:107]\nv_add_u32 v35, s54, v35\nv_mfma_f32_16x16x32_bf16 v[108:111], v[236:239], v[52:55], v[108:111]\n"
"ds_read_b128 v[52:55], v35 offset:0\ns_mov_b32 s52, s47\ns_add_u32 s47, s51, s32\ns_add_u32 s51, s51, s54\n"
"s_add_u32 s49, s49, 28672\ns_waitcnt vmcnt(7)\ns_barrier\ns_waitcnt lgkmcnt(3)\n"
"v_mfma_f32_16x16x32_bf16 v[64:67], v[128:131], v[40:43], 0\nv_mov_b32 v36, s48\ns_add_u32 s48, s48, 1\ns_nop 0\n"
"buffer_store_dword v36, v19, s[24:27], s33 offen sc1\nv_mfma_f32_16x16x32_bf16 v[68:71], v[132:135], v[40:43], 0\nds_read_b128 v[40:43], v32 offset:4096\ns_waitcnt lgkmcnt(3)\n"
"v_mfma_f32_16x16x32_bf16 v[72:75], v[128:131], v[44:47], 0\nv_mfma_f32_16x16x32_bf16 v[76:79], v[132:135], v[44:47], 0\nds_read_b128 v[44:47], v33 offset:4096\nv_cvt_pk_bf16_f32 v56, v96, v97\n"
"s_waitcnt lgkmcnt(3)\nv_mfma_f32_16x16x32_bf16 v[64:67], v[136:139], v[48:51], v[64:67]\nv_cvt_pk_bf16_f32 v57, v98, v99\nv_mfma_f32_16x16x32_bf16 v[68:71], v[140:143], v[48:51], v[68:71]\n"
"ds_read_b128 v[48:51], v34 offset:4096\nv_cvt_pk_bf16_f32 v58, v100, v101\ns_waitcnt lgkmcnt(3)\nv_mfma_f32_16x16x32_bf16 v[72:75], v[136:139], v[52:55], v[72:75]\n"
"v_cvt_pk_bf16_f32 v59, v102, v103\nv_mfma_f32_16x16x32_bf16 v[76:79], v[140:143], v[52:55], v[76:79]\nds_read_b128 v[52:55], v35 offset:4096\ns_waitcnt lgkmcnt(3)\n"
"v_mfma_f32_16x16x32_bf16 v[64:67], v[144:147], v[40:43], v[64:67]\nv_mfma_f32_16x16x32_bf16 v[68:71], v[148:151], v[40:43], v[68:71]\nds_read_b128 v[40:43], v32 offset:8192\nbuffer_store_dwordx4 v[56:59], v15, s[20:23], s50 offen sc1\n"
"s_waitcnt lgkmcnt(3)\nv_mfma_f32_16x16x32_bf16 v[72:75], v[144:147], v[44:47], v[72:75]\nv_cvt_pk_bf16_f32 v60, v104, v105\nv_mfma_f32_16x16x32_bf16 v[76:79], v[148:151], v[44:47], v[76:79]\n"
"ds_read_b128 v[44:47], v33 offset:8192\nv_cvt_pk_bf16_f32 v61, v106, v107\ns_waitcnt lgkmcnt(3)\nv_mfma_f32_16x16x32_bf16 v[64:67], v[152:155], v[48:51], v[64:67]\n"
"v_cvt_pk_bf16_f32 v62, v108, v109\nv_mfma_f32_16x16x32_bf16 v[68:71], v[156:159], v[48:51], v[68:71]\nds_read_b128 v[48:51], v34 offset:8192\nv_cvt_pk_bf16_f32 v63, v110, v111\n"
"s_waitcnt lgkmcnt(3)\nv_mfma_f32_16x16x32_bf16 v[72:75], v[152:155], v[52:55], v[72:75]\nv_mfma_f32_16x16x32_bf16 v[76:79], v[156:159], v[52:55], v[76:79]\nds_read_b128 v[52:55], v35 offset:8192\n"
"s_waitcnt lgkmcnt(3)\nv_mfma_f32_16x16x32_bf16 v[64:67], v[160:163], v[40:43], v[64:67]\nbuffer_store_dwordx4 v[60:63], v16, s[20:23], s50 offen sc1\nv_mfma_f32_16x16x32_bf16 v[68:71], v[164:167], v[40:43], v[68:71]\n"
"ds_read_b128 v[40:43], v32 offset:12288\ns_add_u32 s50, s50, s36\ns_waitcnt lgkmcnt(3)\nv_mfma_f32_16x16x32_bf16 v[72:75], v[160:163], v[44:47], v[72:75]\n"
"s_add_u32 m0, s52, 0\nv_mfma_f32_16x16x32_bf16 v[76:79], v[164:167], v[44:47], v[76:79]\nds_read_b128 v[44:47], v33 offset:12288\ns_add_u32 s53, s49, 0\n"
"s_waitcnt lgkmcnt(3)\nv_mfma_f32_16x16x32_bf16 v[64:67], v[168:171], v[48:51], v[64:67]\nbuffer_load_dwordx4 v10, s[12:15], s53 offen lds\nv_mfma_f32_16x16x32_bf16 v[68:71], v[172:175], v[48:51], v[68:71]\n"
"ds_read_b128 v[48:51], v34 offset:12288\ns_add_u32 m0, s52, 4096\ns_waitcnt lgkmcnt(3)\nv_mfma_f32_16x16x32_bf16 v[72:75], v[168:171], v[52:55], v[72:75]\n"
"s_add_u32 s53, s49, 128\nv_mfma_f32_16x16x32_bf16 v[76:79], v[172:175], v[52:55], v[76:79]\nds_read_b128 v[52:55], v35 offset:12288\nbuffer_load_dwordx4 v10, s[12:15], s53 offen lds\n"
"s_waitcnt lgkmcnt(3)\nv_mfma_f32_16x16x32_bf16 v[64:67], v[176:179], v[40:43], v[64:67]\ns_add_u32 m0, s52, 8192\nv_mfma_f32_16x16x32_bf16 v[68:71], v[180:183], v[40:43], v[68:71]\n"
"ds_read_b128 v[40:43], v32 offset:16384\ns_add_u32 s53, s49, 256\ns_waitcnt lgkmcnt(3)\nv_mfma_f32_16x16x32_bf16 v[72:75], v[176:179], v[44:47], v[72:75]\n"
"buffer_load_dwordx4 v10, s[12:15], s53 offen lds\nv_mfma_f32_16x16x32_bf16 v[76:79], v[180:183], v[44:47], v[76:79]\nds_read_b128 v[44:47], v33 offset:16384\ns_add_u32 m0, s52, 12288\n"
"s_waitcnt lgkmcnt(3)\nv_mfma_f32_16x16x32_bf16 v[64:67], v[184:187], v[48:51], v[64:67]\ns_add_u32 s53, s49, 384\nv_mfma_f32_16x16x32_bf16 v[68:71], v[188:191], v[48:51], v[68:71]\n"
"ds_read_b128 v[48:51], v34 offset:16384\nbuffer_load_dwordx4 v10, s[12:15], s53 offen lds\ns_waitcnt lgkmcnt(3)\nv_mfma_f32_16x16x32_bf16 v[72:75], v[184:187], v[52:55], v[72:75]\n"
"s_add_u32 m0, s52, 16384\nv_mfma_f32_16x16x32_bf16 v[76:79], v[188:191], v[52:55], v[76:79]\nds_read_b128 v[52:55], v35 offset:16384\ns_add_u32 s53, s49, 512\n"
"s_waitcnt lgkmcnt(3)\nv_mfma_f32_16x16x32_bf16 v[64:67], v[192:195], v[40:43], v[64:67]\nbuffer_load_dwordx4 v10, s[12:15], s53 offen lds\nv_mfma_f32_16x16x32_bf16 v[68:71], v[196:199], v[40:43], v[68:71]\n"
"ds_read_b128 v[40:43], v32 offset:20480\ns_add_u32 m0, s52, 20480\ns_waitcnt lgkmcnt(3)\nv_mfma_f32_16x16x32_bf16 v[72:75], v[192:195], v[44:47], v[72:75]\n"
"s_add_u32 s53, s49, 640\nv_mfma_f32_16x16x32_bf16 v[76:79], v[196:199], v[44:47], v[76:79]\nds_read_b128 v[44:47], v33 offset:20480\nbuffer_load_dwordx4 v10, s[12:15], s53 offen lds\n"
"s_waitcnt lgkmcnt(3)\nv_mfma_f32_16x16x32_bf16 v[64:67], v[200:203], v[48:51], v[64:67]\ns_add_u32 m0, s52, 24576\nv_mfma_f32_16x16x32_bf16 v[68:71], v[204:207], v[48:51], v[68:71]\n"
"ds_read_b128 v[48:51], v34 offset:20480\ns_add_u32 s53, s49, 768\ns_waitcnt lgkmcnt(3)\nv_mfma_f32_16x16x32_bf16 v[72:75], v[200:203], v[52:55], v[72:75]\n"
"buffer_load_dwordx4 v10, s[12:15], s53 offen lds\nv_mfma_f32_16x16x32_bf16 v[76:79], v[204:207], v[52:55], v[76:79]\nds_read_b128 v[52:55], v35 offset:20480\ns_waitcnt lgkmcnt(3)\n"
"v_mfma_f32_16x16x32_bf16 v[64:67], v[208:211], v[40:43], v[64:67]\nv_mfma_f32_16x16x32_bf16 v[68:71], v[212:215], v[40:43], v[68:71]\nds_read_b128 v[40:43], v32 offset:24576\ns_waitcnt lgkmcnt(3)\n"
"v_mfma_f32_16x16x32_bf16 v[72:75], v[208:211], v[44:47], v[72:75]\nv_mfma_f32_16x16x32_bf16 v[76:79], v[212:215], v[44:47], v[76:79]\nds_read_b128 v[44:47], v33 offset:24576\ns_waitcnt lgkmcnt(3)\n"
"v_mfma_f32_16x16x32_bf16 v[64:67], v[216:219], v[48:51], v[64:67]\ns_add_u32 s54, s51, 32768\ns_cmp_eq_u32 s54, 163840\nv_mfma_f32_16x16x32_bf16 v[68:71], v[220:223], v[48:51], v[68:71]\n"
"s_cselect_b32 s54, 0, s54\ns_sub_u32 s54, s54, s51\nds_read_b128 v[48:51], v34 offset:24576\ns_waitcnt lgkmcnt(3)\n"
"v_mfma_f32_16x16x32_bf16 v[72:75], v[216:219], v[52:55], v[72:75]\nv_mfma_f32_16x16x32_bf16 v[76:79], v[220:223], v[52:55], v[76:79]\nds_read_b128 v[52:55], v35 offset:24576\ns_waitcnt lgkmcnt(3)\n"
"v_mfma_f32_16x16x32_bf16 v[64:67], v[224:227], v[40:43], v[64:67]\nv_add_u32 v32, s54, v32\nv_mfma_f32_16x16x32_bf16 v[68:71], v[228:231], v[40:43], v[68:71]\nds_read_b128 v[40:43], v32 offset:0\n"
"s_waitcnt lgkmcnt(3)\nv_mfma_f32_16x16x32_bf16 v[72:75], v[224:227], v[44:47], v[72:75]\nv_add_u32 v33, s54, v33\nv_mfma_f32_16x16x32_bf16 v[76:79], v[228:231], v[44:47], v[76:79]\n"
"ds_read_b128 v[44:47], v33 offset:0\ns_waitcnt lgkmcnt(3)\nv_mfma_f32_16x16x32_bf16 v[64:67], v[232:235], v[48:51], v[64:67]\nv_add_u32 v34, s54, v34\n"
"v_mfma_f32_16x16x32_bf16 v[68:71], v[236:239], v[48:51], v[68:71]\nds_read_b128 v[48:51], v34 offset:0\ns_waitcnt lgkmcnt(3)\nv_mfma_f32_16x16x32_bf16 v[72:75], v[232:235], v[52:55], v[72:75]\n"
"v_add_u32 v35, s54, v35\nv_mfma_f32_16x16x32_bf16 v[76:79], v[236:239], v[52:55], v[76:79]\nds_read_b128 v[52:55], v35 offset:0\ns_mov_b32 s52, s47\n"
"s_add_u32 s47, s51, s32\ns_add_u32 s51, s51, s54\ns_add_u32 s49, s49, 28672\ns_waitcnt vmcnt(7)\n"
"s_barrier\ns_waitcnt lgkmcnt(3)\nv_mfma_f32_16x16x32_bf16 v[96:99], v[128:131], v[40:43], 0\nv_mov_b32 v36, s48\n"
"s_add_u32 s48, s48, 1\ns_nop 0\nbuffer_store_dword v36, v19, s[24:27], s33 offen sc1\nbuffer_load_dword v37, v20, s[24:27], 0 offen sc1\n"
"v_mfma_f32_16x16x32_bf16 v[100:103], v[132:135], v[40:43], 0\nds_read_b128 v[40:43], v32 offset:4096\ns_waitcnt lgkmcnt(3)\nv_mfma_f32_16x16x32_bf16 v[104:107], v[128:131], v[44:47], 0\n"
"v_mfma_f32_16x16x32_bf16 v[108:111], v[132:135], v[44:47], 0\nds_read_b128 v[44:47], v33 offset:4096\nv_cvt_pk_bf16_f32 v56, v64, v65\ns_waitcnt lgkmcnt(3)\n"
"v_mfma_f32_16x16x32_bf16 v[96:99], v[136:139], v[48:51], v[96:99]\nv_cvt_pk_bf16_f32 v57, v66, v67\nv_mfma_f32_16x16x32_bf16 v[100:103], v[140:143], v[48:51], v[100:103]\nds_read_b128 v[48:51], v34 offset:4096\n"
"v_cvt_pk_bf16_f32 v58, v68, v69\ns_waitcnt lgkmcnt(3)\nv_mfma_f32_16x16x32_bf16 v[104:107], v[136:139], v[52:55], v[104:107]\nv_cvt_pk_bf16_f32 v59, v70, v71\n"
"v_mfma_f32_16x16x32_bf16 v[108:111], v[140:143], v[52:55], v[108:111]\nds_read_b128 v[52:55], v35 offset:4096\ns_waitcnt lgkmcnt(3)\nv_mfma_f32_16x16x32_bf16 v[96:99], v[144:147], v[40:43], v[96:99]\n"
"v_mfma_f32_16x16x32_bf16 v[100:103], v[148:151], v[40:43], v[100:103]\nds_read_b128 v[40:43], v32 offset:8192\nbuffer_store_dwordx4 v[56:59], v15, s[20:23], s50 offen sc1\ns_waitcnt lgkmcnt(3)\n"
"v_mfma_f32_16x16x32_bf16 v[104:107], v[144:147], v[44:47], v[104:107]\nv_cvt_pk_bf16_f32 v60, v72, v73\nv_mfma_f32_16x16x32_bf16 v[108:111], v[148:151], v[44:47], v[108:111]\nds_read_b128 v[44:47], v33 offset:8192\n"
"v_cvt_pk_bf16_f32 v61, v74, v75\ns_waitcnt lgkmcnt(3)\nv_mfma_f32_16x16x32_bf16 v[96:99], v[152:155], v[48:51], v[96:99]\nv_cvt_pk_bf16_f32 v62, v76, v77\n"
"v_mfma_f32_16x16x32_bf16 v[100:103], v[156:159], v[48:51], v[100:103]\nds_read_b128 v[48:51], v34 offset:8192\nv_cvt_pk_bf16_f32 v63, v78, v79\ns_waitcnt lgkmcnt(3)\n"
"v_mfma_f32_16x16x32_bf16 v[104:107], v[152:155], v[52:55], v[104:107]\nv_mfma_f32_16x16x32_bf16 v[108:111], v[156:159], v[52:55], v[108:111]\nds_read_b128 v[52:55], v35 offset:8192\ns_waitcnt lgkmcnt(3)\n"
"v_mfma_f32_16x16x32_bf16 v[96:99], v[160:163], v[40:43], v[96:99]\nbuffer_store_dwordx4 v[60:63], v16, s[20:23], s50 offen sc1\nv_mfma_f32_16x16x32_bf16 v[100:103], v[164:167], v[40:43], v[100:103]\nds_read_b128 v[40:43], v32 offset:12288\n"
"s_add_u32 s50, s50, s36\ns_waitcnt lgkmcnt(3)\nv_mfma_f32_16x16x32_bf16 v[104:107], v[160:163], v[44:47], v[104:107]\ns_add_u32 m0, s52, 0\n"
"v_mfma_f32_16x16x32_bf16 v[108:111], v[164:167], v[44:47], v[108:111]\nds_read_b128 v[44:47], v33 offset:12288\ns_add_u32 s53, s49, 0\ns_waitcnt lgkmcnt(3)\n"
"v_mfma_f32_16x16x32_bf16 v[96:99], v[168:171], v[48:51], v[96:99]\nbuffer_load_dwordx4 v10, s[12:15], s53 offen lds\nv_mfma_f32_16x16x32_bf16 v[100:103], v[172:175], v[48:51], v[100:103]\nds_read_b128 v[48:51], v34 offset:12288\n"
"s_add_u32 m0, s52, 4096\ns_waitcnt lgkmcnt(3)\nv_mfma_f32_16x16x32_bf16 v[104:107], v[168:171], v[52:55], v[104:107]\ns_add_u32 s53, s49, 128\n"
"v_mfma_f32_16x16x32_bf16 v[108:111], v[172:175], v[52:55], v[108:111]\nds_read_b128 v[52:55], v35 offset:12288\nbuffer_load_dwordx4 v10, s[12:15], s53 offen lds\ns_waitcnt lgkmcnt(3)\n"
"v_mfma_f32_16x16x32_bf16 v[96:99], v[176:179], v[40:43], v[96:99]\ns_add_u32 m0, s52, 8192\nv_mfma_f32_16x16x32_bf16 v[100:103], v[180:183], v[40:43], v[100:103]\nds_read_b128 v[40:43], v32 offset:16384\n"
"s_add_u32 s53, s49, 256\ns_waitcnt lgkmcnt(3)\nv_mfma_f32_16x16x32_bf16 v[104:107], v[176:179], v[44:47], v[104:107]\nbuffer_load_dwordx4 v10, s[12:15], s53 offen lds\n"
"v_mfma_f32_16x16x32_bf16 v[108:111], v[180:183], v[44:47], v[108:111]\nds_read_b128 v[44:47], v33 offset:16384\ns_add_u32 m0, s52, 12288\ns_waitcnt lgkmcnt(3)\n"
"v_mfma_f32_16x16x32_bf16 v[96:99], v[184:187], v[48:51], v[96:99]\ns_add_u32 s53, s49, 384\nv_mfma_f32_16x16x32_bf16 v[100:103], v[188:191], v[48:51], v[100:103]\nds_read_b128 v[48:51], v34 offset:16384\n"
"buffer_load_dwordx4 v10, s[12:15], s53 offen lds\ns_waitcnt lgkmcnt(3)\nv_mfma_f32_16x16x32_bf16 v[104:107], v[184:187], v[52:55], v[104:107]\ns_add_u32 m0, s52, 16384\n"
"v_mfma_f32_16x16x32_bf16 v[108:111], v[188:191], v[52:55], v[108:111]\nds_read_b128 v[52:55], v35 offset:16384\ns_add_u32 s53, s49, 512\ns_waitcnt lgkmcnt(3)\n"
"v_mfma_f32_16x16x32_bf16 v[96:99], v[192:195], v[40:43], v[96:99]\nbuffer_load_dwordx4 v10, s[12:15], s53 offen lds\nv_mfma_f32_16x16x32_bf16 v[100:103], v[196:199], v[40:43], v[100:103]\nds_read_b128 v[40:43], v32 offset:20480\n"
"s_add_u32 m0, s52, 20480\ns_waitcnt lgkmcnt(3)\nv_mfma_f32_16x16x32_bf16 v[104:107], v[192:195], v[44:47], v[104:107]\ns_add_u32 s53, s49, 640\n"
"v_mfma_f32_16x16x32_bf16 v[108:111], v[196:199], v[44:47], v[108:111]\nds_read_b128 v[44:47], v33 offset:20480\nbuffer_load_dwordx4 v10, s[12:15], s53 offen lds\ns_waitcnt lgkmcnt(3)\n"
"v_mfma_f32_16x16x32_bf16 v[96:99], v[200:203], v[48:51], v[96:99]\ns_add_u32 m0, s52, 24576\nv_mfma_f32_16x16x32_bf16 v[100:103], v[204:207], v[48:51], v[100:103]\nds_read_b128 v[48:51], v34 offset:20480\n"
"s_add_u32 s53, s49, 768\ns_waitcnt lgkmcnt(3)\nv_mfma_f32_16x16x32_bf16 v[104:107], v[200:203], v[52:55], v[104:107]\nbuffer_load_dwordx4 v10, s[12:15], s53 offen lds\n"
"v_mfma_f32_16x16x32_bf16 v[108:111], v[204:207], v[52:55], v[108:111]\nds_read_b128 v[52:55], v35 offset:20480\ns_waitcnt lgkmcnt(3)\nv_mfma_f32_16x16x32_bf16 v[96:99], v[208:211], v[40:43], v[96:99]\n"
"v_mfma_f32_16x16x32_bf16 v[100:103], v[212:215], v[40:43], v[100:103]\nds_read_b128 v[40:43], v32 offset:24576\ns_waitcnt lgkmcnt(3)\nv_mfma_f32_16x16x32_bf16 v[104:107], v[208:211], v[44:47], v[104:107]\n"
"v_mfma_f32_16x16x32_bf16 v[108:111], v[212:215], v[44:47], v[108:111]\nds_read_b128 v[44:47], v33 offset:24576\ns_waitcnt lgkmcnt(3)\nv_mfma_f32_16x16x32_bf16 v[96:99], v[216:219], v[48:51], v[96:99]\n"
"s_add_u32 s54, s51, 32768\ns_cmp_eq_u32 s54, 163840\nv_mfma_f32_16x16x32_bf16 v[100:103], v[220:223], v[48:51], v[100:103]\ns_cselect_b32 s54, 0, s54\n"
"s_sub_u32 s54, s54, s51\nds_read_b128 v[48:51], v34 offset:24576\ns_waitcnt lgkmcnt(3)\nv_mfma_f32_16x16x32_bf16 v[104:107], v[216:219], v[52:55], v[104:107]\n"
"v_mfma_f32_16x16x32_bf16 v[108:111], v[220:223], v[52:55], v[108:111]\nds_read_b128 v[52:55], v35 offset:24576\ns_waitcnt lgkmcnt(3)\nv_mfma_f32_16x16x32_bf16 v[96:99], v[224:227], v[40:43], v[96:99]\n"
"v_add_u32 v32, s54, v32\nv_mfma_f32_16x16x32_bf16 v[100:103], v[228:231], v[40:43], v[100:103]\nds_read_b128 v[40:43], v32 offset:0\ns_waitcnt lgkmcnt(3)\n"
"v_mfma_f32_16x16x32_bf16 v[104:107], v[224:227], v[44:47], v[104:107]\nv_add_u32 v33, s54, v33\nv_mfma_f32_16x16x32_bf16 v[108:111], v[228:231], v[44:47], v[108:111]\nds_read_b128 v[44:47], v33 offset:0\n"
"s_waitcnt lgkmcnt(3)\nv_mfma_f32_16x16x32_bf16 v[96:99], v[232:235], v[48:51], v[96:99]\nv_add_u32 v34, s54, v34\nv_mfma_f32_16x16x32_bf16 v[100:103], v[236:239], v[48:51], v[100:103]\n"
"ds_read_b128 v[48:51], v34 offset:0\ns_waitcnt lgkmcnt(3)\nv_mfma_f32_16x16x32_bf16 v[104:107], v[232:235], v[52:55], v[104:107]\nv_add_u32 v35, s54, v35\n"
"v_mfma_f32_16x16x32_bf16 v[108:111], v[236:239], v[52:55], v[108:111]\nds_read_b128 v[52:55], v35 offset:0\ns_mov_b32 s52, s47\ns_add_u32 s47, s51, s32\n"
"s_add_u32 s51, s51, s54\ns_add_u32 s49, s49, 28672\ns_waitcnt vmcnt(7)\nfg0_pf:\n"
"v_cmp_ne_u32 vcc, s28, v37\ns_cbranch_vccz fg0_pfok\ns_add_u32 s55, s55, 1\ns_cmp_gt_u32 s55, 262144\n"
"s_cbranch_scc0 fg0_pf2\ns_endpgm\nfg0_pf2:\ns_sleep 1\n"
"buffer_load_dword v37, v20, s[24:27], 0 offen sc1\ns_waitcnt vmcnt(0)\ns_branch fg0_pf\nfg0_pfok:\n"
"s_barrier\ns_waitcnt lgkmcnt(3)\nv_mfma_f32_16x16x32_bf16 v[64:67], v[128:131], v[40:43], 0\nv_mov_b32 v36, s48\n"
"s_add_u32 s48, s48, 1\ns_nop 0\nbuffer_store_dword v36, v19, s[24:27], s33 offen sc1\nv_mfma_f32_16x16x32_bf16 v[68:71], v[132:135], v[40:43], 0\n"
"ds_read_b128 v[40:43], v32 offset:4096\ns_waitcnt lgkmcnt(3)\nv_mfma_f32_16x16x32_bf16 v[72:75], v[128:131], v[44:47], 0\nv_mfma_f32_16x16x32_bf16 v[76:79], v[132:135], v[44:47], 0\n"
"ds_read_b128 v[44:47], v33 offset:4096\nv_cvt_pk_bf16_f32 v56, v96, v97\ns_waitcnt lgkmcnt(3)\nv_mfma_f32_16x16x32_bf16 v[64:67], v[136:139], v[48:51], v[64:67]\n"
"v_cvt_pk_bf16_f32 v57, v98, v99\nv_mfma_f32_16x16x32_bf16 v[68:71], v[140:143], v[48:51], v[68:71]\nds_read_b128 v[48:51], v34 offset:4096\nv_cvt_pk_bf16_f32 v58, v100, v101\n"
"s_waitcnt lgkmcnt(3)\nv_mfma_f32_16x16x32_bf16 v[72:75], v[136:139], v[52:55], v[72:75]\nv_cvt_pk_bf16_f32 v59, v102, v103\nv_mfma_f32_16x16x32_bf16 v[76:79], v[140:143], v[52:55], v[76:79]\n"
"ds_read_b128 v[52:55], v35 offset:4096\ns_waitcnt lgkmcnt(3)\nv_mfma_f32_16x16x32_bf16 v[64:67], v[144:147], v[40:43], v[64:67]\nv_mfma_f32_16x16x32_bf16 v[68:71], v[148:151], v[40:43], v[68:71]\n"
"ds_read_b128 v[40:43], v32 offset:8192\nbuffer_store_dwordx4 v[56:59], v15, s[20:23], s50 offen sc1\ns_waitcnt lgkmcnt(3)\nv_mfma_f32_16x16x32_bf16 v[72:75], v[144:147], v[44:47], v[72:75]\n"
"v_cvt_pk_bf16_f32 v60, v104, v105\nv_mfma_f32_16x16x32_bf16 v[76:79], v[148:151], v[44:47], v[76:79]\nds_read_b128 v[44:47], v33 offset:8192\nv_cvt_pk_bf16_f32 v61, v106, v107\n"
"s_waitcnt lgkmcnt(3)\nv_mfma_f32_16x16x32_bf16 v[64:67], v[152:155], v[48:51], v[64:67]\nv_cvt_pk_bf16_f32 v62, v108, v109\nv_mfma_f32_16x16x32_bf16 v[68:71], v[156:159], v[48:51], v[68:71]\n"
"ds_read_b128 v[48:51], v34 offset:8192\nv_cvt_pk_bf16_f32 v63, v110, v111\ns_waitcnt lgkmcnt(3)\nv_mfma_f32_16x16x32_bf16 v[72:75], v[152:155], v[52:55], v[72:75]\n"
"v_mfma_f32_16x16x32_bf16 v[76:79], v[156:159], v[52:55], v[76:79]\nds_read_b128 v[52:55], v35 offset:8192\ns_waitcnt lgkmcnt(3)\nv_mfma_f32_16x16x32_bf16 v[64:67], v[160:163], v[40:43], v[64:67]\n"
"buffer_store_dwordx4 v[60:63], v16, s[20:23], s50 offen sc1\nv_mfma_f32_16x16x32_bf16 v[68:71], v[164:167], v[40:43], v[68:71]\nds_read_b128 v[40:43], v32 offset:12288\ns_add_u32 s50, s50, s36\n"
"s_waitcnt lgkmcnt(3)\nv_mfma_f32_16x16x32_bf16 v[72:75], v[160:163], v[44:47], v[72:75]\ns_add_u32 m0, s52, 0\nv_mfma_f32_16x16x32_bf16 v[76:79], v[164:167], v[44:47], v[76:79]\n"
"ds_read_b128 v[44:47], v33 offset:12288\ns_add_u32 s53, s49, 0\ns_waitcnt lgkmcnt(3)\nv_mfma_f32_16x16x32_bf16 v[64:67], v[168:171], v[48:51], v[64:67]\n"
"buffer_load_dwordx4 v10, s[12:15], s53 offen lds\nv_mfma_f32_16x16x32_bf16 v[68:71], v[172:175], v[48:51], v[68:71]\nds_read_b128 v[48:51], v34 offset:12288\ns_add_u32 m0, s52, 4096\n"
"s_waitcnt lgkmcnt(3)\nv_mfma_f32_16x16x32_bf16 v[72:75], v[168:171], v[52:55], v[72:75]\ns_add_u32 s53, s49, 128\nv_mfma_f32_16x16x32_bf16 v[76:79], v[172:175], v[52:55], v[76:79]\n"
"ds_read_b128 v[52:55], v35 offset:12288\nbuffer_load_dwordx4 v10, s[12:15], s53 offen lds\ns_waitcnt lgkmcnt(3)\nv_mfma_f32_16x16x32_bf16 v[64:67], v[176:179], v[40:43], v[64:67]\n"
"s_add_u32 m0, s52, 8192\nv_mfma_f32_16x16x32_bf16 v[68:71], v[180:183], v[40:43], v[68:71]\nds_read_b128 v[40:43], v32 offset:16384\ns_add_u32 s53, s49, 256\n"
"s_waitcnt lgkmcnt(3)\nv_mfma_f32_16x16x32_bf16 v[72:75], v[176:179], v[44:47], v[72:75]\nbuffer_load_dwordx4 v10, s[12:15], s53 offen lds\nv_mfma_f32_16x16x32_bf16 v[76:79], v[180:183], v[44:47], v[76:79]\n"
"ds_read_b128 v[44:47], v33 offset:16384\ns_add_u32 m0, s52, 12288\ns_waitcnt lgkmcnt(3)\nv_mfma_f32_16x16x32_bf16 v[64:67], v[184:187], v[48:51], v[64:67]\n"
"s_add_u32 s53, s49, 384\nv_mfma_f32_16x16x32_bf16 v[68:71], v[188:191], v[48:51], v[68:71]\nds_read_b128 v[48:51], v34 offset:16384\nbuffer_load_dwordx4 v10, s[12:15], s53 offen lds\n"
"s_waitcnt lgkmcnt(3)\nv_mfma_f32_16x16x32_bf16 v[72:75], v[184:187], v[52:55], v[72:75]\ns_add_u32 m0, s52, 16384\nv_mfma_f32_16x16x32_bf16 v[76:79], v[188:191], v[52:55], v[76:79]\n"
"ds_read_b128 v[52:55], v35 offset:16384\ns_add_u32 s53, s49, 512\ns_waitcnt lgkmcnt(3)\nv_mfma_f32_16x16x32_bf16 v[64:67], v[192:195], v[40:43], v[64:67]\n"
"buffer_load_dwordx4 v10, s[12:15], s53 offen lds\nv_mfma_f32_16x16x32_bf16 v[68:71], v[196:199], v[40:43], v[68:71]\nds_read_b128 v[40:43], v32 offset:20480\ns_add_u32 m0, s52, 20480\n"
"s_waitcnt lgkmcnt(3)\nv_mfma_f32_16x16x32_bf16 v[72:75], v[192:195], v[44:47], v[72:75]\ns_add_u32 s53, s49, 640\nv_mfma_f32_16x16x32_bf16 v[76:79], v[196:199], v[44:47], v[76:79]\n"
"ds_read_b128 v[44:47], v33 offset:20480\nbuffer_load_dwordx4 v10, s[12:15], s53 offen lds\ns_waitcnt lgkmcnt(3)\nv_mfma_f32_16x16x32_bf16 v[64:67], v[200:203], v[48:51], v[64:67]\n"
"s_add_u32 m0, s52, 24576\nv_mfma_f32_16x16x32_bf16 v[68:71], v[204:207], v[48:51], v[68:71]\nds_read_b128 v[48:51], v34 offset:20480\ns_add_u32 s53, s49, 768\n"
"s_waitcnt lgkmcnt(3)\nv_mfma_f32_16x16x32_bf16 v[72:75], v[200:203], v[52:55], v[72:75]\nbuffer_load_dwordx4 v10, s[12:15], s53 offen lds\nv_mfma_f32_16x16x32_bf16 v[76:79], v[204:207], v[52:55], v[76:79]\n"
"ds_read_b128 v[52:55], v35 offset:20480\ns_mov_b32 s53, 0\nbuffer_load_dwordx4 a[0:3], v14, s[16:19], s53 offen sc1\ns_waitcnt lgkmcnt(3)\n"
"v_mfma_f32_16x16x32_bf16 v[64:67], v[208:211], v[40:43], v[64:67]\ns_mov_b32 s53, 14336\nbuffer_load_dwordx4 a[4:7], v14, s[16:19], s53 offen sc1\nv_mfma_f32_16x16x32_bf16 v[68:71], v[212:215], v[40:43], v[68:71]\n"
"ds_read_b128 v[40:43], v32 offset:24576\ns_mov_b32 s53, 1024\nbuffer_load_dwordx4 a[8:11], v14, s[16:19], s53 offen sc1\ns_waitcnt lgkmcnt(3)\n"
"v_mfma_f32_16x16x32_bf16 v[72:75], v[208:211], v[44:47], v[72:75]\ns_mov_b32 s53, 15360\nbuffer_load_dwordx4 a[12:15], v14, s[16:19], s53 offen sc1\nv_mfma_f32_16x16x32_bf16 v[76:79], v[212:215], v[44:47], v[76:79]\n"
"ds_read_b128 v[44:47], v33 offset:24576\ns_mov_b32 s53, 2048\nbuffer_load_dwordx4 a[16:19], v14, s[16:19], s53 offen sc1\ns_waitcnt lgkmcnt(3)\n"
"v_mfma_f32_16x16x32_bf16 v[64:67], v[216:219], v[48:51], v[64:67]\ns_add_u32 s54, s51, 32768\ns_cmp_eq_u32 s54, 163840\nv_mfma_f32_16x16x32_bf16 v[68:71], v[220:223], v[48:51], v[68:71]\n"
"s_cselect_b32 s54, 0, s54\ns_sub_u32 s54, s54, s51\nds_read_b128 v[48:51], v34 offset:24576\ns_mov_b32 s53, 16384\n"
"buffer_load_dwordx4 a[20:23], v14, s[16:19], s53 offen sc1\ns_waitcnt lgkmcnt(3)\nv_mfma_f32_16x16x32_bf16 v[72:75], v[216:219], v[52:55], v[72:75]\ns_mov_b32 s53, 3072\n"
"buffer_load_dwordx4 a[24:27], v14, s[16:19], s53 offen sc1\nv_mfma_f32_16x16x32_bf16 v[76:79], v[220:223], v[52:55], v[76:79]\nds_read_b128 v[52:55], v35 offset:24576\ns_waitcnt lgkmcnt(3)\n"
"v_mfma_f32_16x16x32_bf16 v[64:67], v[224:227], v[40:43], v[64:67]\nv_add_u32 v32, s54, v32\nv_mfma_f32_16x16x32_bf16 v[68:71], v[228:231], v[40:43], v[68:71]\nds_read_b128 v[40:43], v32 offset:0\n"
"s_waitcnt lgkmcnt(3)\nv_mfma_f32_16x16x32_bf16 v[72:75], v[224:227], v[44:47], v[72:75]\nv_add_u32 v33, s54, v33\nv_mfma_f32_16x16x32_bf16 v[76:79], v[228:231], v[44:47], v[76:79]\n"
"ds_read_b128 v[44:47], v33 offset:0\ns_waitcnt lgkmcnt(3)\nv_mfma_f32_16x16x32_bf16 v[64:67], v[232:235], v[48:51], v[64:67]\nv_add_u32 v34, s54, v34\n"
"v_mfma_f32_16x16x32_bf16 v[68:71], v[236:239], v[48:51], v[68:71]\nds_read_b128 v[48:51], v34 offset:0\ns_waitcnt lgkmcnt(3)\nv_mfma_f32_16x16x32_bf16 v[72:75], v[232:235], v[52:55], v[72:75]\n"
"v_add_u32 v35, s54, v35\nv_mfma_f32_16x16x32_bf16 v[76:79], v[236:239], v[52:55], v[76:79]\nds_read_b128 v[52:55], v35 offset:0\ns_mov_b32 s52, s47\n"
"s_add_u32 s47, s51, s32\ns_add_u32 s51, s51, s54\ns_add_u32 s49, s49, 28672\ns_waitcnt vmcnt(14)\n"
"s_barrier\ns_waitcnt lgkmcnt(3)\nv_mfma_f32_16x16x32_bf16 v[96:99], v[128:131], v[40:43], 0\nv_mov_b32 v36, s48\n"
"s_add_u32 s48, s48, 1\ns_nop 0\nbuffer_store_dword v36, v19, s[24:27], s33 offen sc1\nv_mfma_f32_16x16x32_bf16 v[100:103], v[132:135], v[40:43], 0\n"
"ds_read_b128 v[40:43], v32 offset:4096\ns_waitcnt lgkmcnt(3)\nv_mfma_f32_16x16x32_bf16 v[104:107], v[128:131], v[44:47], 0\nv_mfma_f32_16x16x32_bf16 v[108:111], v[132:135], v[44:47], 0\n"
"ds_read_b128 v[44:47], v33 offset:4096\nv_cvt_pk_bf16_f32 v56, v64, v65\ns_waitcnt lgkmcnt(3)\nv_mfma_f32_16x16x32_bf16 v[96:99], v[136:139], v[48:51], v[96:99]\n"
"v_cvt_pk_bf16_f32 v57, v66, v67\nv_mfma_f32_16x16x32_bf16 v[100:103], v[140:143], v[48:51], v[100:103]\nds_read_b128 v[48:51], v34 offset:4096\nv_cvt_pk_bf16_f32 v58, v68, v69\n"
"s_waitcnt lgkmcnt(3)\nv_mfma_f32_16x16x32_bf16 v[104:107], v[136:139], v[52:55], v[104:107]\nv_cvt_pk_bf16_f32 v59, v70, v71\nv_mfma_f32_16x16x32_bf16 v[108:111], v[140:143], v[52:55], v[108:111]\n"
"ds_read_b128 v[52:55], v35 offset:4096\ns_waitcnt lgkmcnt(3)\nv_mfma_f32_16x16x32_bf16 v[96:99], v[144:147], v[40:43], v[96:99]\nv_mfma_f32_16x16x32_bf16 v[100:103], v[148:151], v[40:43], v[100:103]\n"
"ds_read_b128 v[40:43], v32 offset:8192\nbuffer_store_dwordx4 v[56:59], v15, s[20:23], s50 offen sc1\ns_waitcnt lgkmcnt(3)\nv_mfma_f32_16x16x32_bf16 v[104:107], v[144:147], v[44:47], v[104:107]\n"
"v_cvt_pk_bf16_f32 v60, v72, v73\nv_mfma_f32_16x16x32_bf16 v[108:111], v[148:151], v[44:47], v[108:111]\nds_read_b128 v[44:47], v33 offset:8192\nv_cvt_pk_bf16_f32 v61, v74, v75\n"
"s_waitcnt lgkmcnt(3)\nv_mfma_f32_16x16x32_bf16 v[96:99], v[152:155], v[48:51], v[96:99]\nv_cvt_pk_bf16_f32 v62, v76, v77\nv_mfma_f32_16x16x32_bf16 v[100:103], v[156:159], v[48:51], v[100:103]\n"
"ds_read_b128 v[48:51], v34 offset:8192\nv_cvt_pk_bf16_f32 v63, v78, v79\ns_waitcnt lgkmcnt(3)\nv_mfma_f32_16x16x32_bf16 v[104:107], v[152:155], v[52:55], v[104:107]\n"
"v_mfma_f32_16x16x32_bf16 v[108:111], v[156:159], v[52:55], v[108:111]\nds_read_b128 v[52:55], v35 offset:8192\ns_waitcnt lgkmcnt(3)\nv_mfma_f32_16x16x32_bf16 v[96:99], v[160:163], v[40:43], v[96:99]\n"
"buffer_store_dwordx4 v[60:63], v16, s[20:23], s50 offen sc1\nv_mfma_f32_16x16x32_bf16 v[100:103], v[164:167], v[40:43], v[100:103]\nds_read_b128 v[40:43], v32 offset:12288\ns_add_u32 s50, s50, s36\n"
"s_waitcnt lgkmcnt(3)\nv_mfma_f32_16x16x32_bf16 v[104:107], v[160:163], v[44:47], v[104:107]\ns_add_u32 m0, s52, 0\nv_mfma_f32_16x16x32_bf16 v[108:111], v[164:167], v[44:47], v[108:111]\n"
"ds_read_b128 v[44:47], v33 offset:12288\ns_add_u32 s53, s49, 0\ns_waitcnt lgkmcnt(3)\nv_mfma_f32_16x16x32_bf16 v[96:99], v[168:171], v[48:51], v[96:99]\n"
"buffer_load_dwordx4 v10, s[12:15], s53 offen lds\nv_mfma_f32_16x16x32_bf16 v[100:103], v[172:175], v[48:51], v[100:103]\nds_read_b128 v[48:51], v34 offset:12288\ns_add_u32 m0, s52, 4096\n"
"s_waitcnt lgkmcnt(3)\nv_mfma_f32_16x16x32_bf16 v[104:107], v[168:171], v[52:55], v[104:107]\ns_add_u32 s53, s49, 128\nv_mfma_f32_16x16x32_bf16 v[108:111], v[172:175], v[52:55], v[108:111]\n"
"ds_read_b128 v[52:55], v35 offset:12288\nbuffer_load_dwordx4 v10, s[12:15], s53 offen lds\ns_waitcnt lgkmcnt(3)\nv_mfma_f32_16x16x32_bf16 v[96:99], v[176:179], v[40:43], v[96:99]\n"
"s_add_u32 m0, s52, 8192\nv_mfma_f32_16x16x32_bf16 v[100:103], v[180:183], v[40:43], v[100:103]\nds_read_b128 v[40:43], v32 offset:16384\ns_add_u32 s53, s49, 256\n"
"s_waitcnt lgkmcnt(3)\nv_mfma_f32_16x16x32_bf16 v[104:107], v[176:179], v[44:47], v[104:107]\nbuffer_load_dwordx4 v10, s[12:15], s53 offen lds\nv_mfma_f32_16x16x32_bf16 v[108:111], v[180:183], v[44:47], v[108:111]\n"
"ds_read_b128 v[44:47], v33 offset:16384\ns_add_u32 m0, s52, 12288\ns_waitcnt lgkmcnt(3)\nv_mfma_f32_16x16x32_bf16 v[96:99], v[184:187], v[48:51], v[96:99]\n"
"s_add_u32 s53, s49, 384\nv_mfma_f32_16x16x32_bf16 v[100:103], v[188:191], v[48:51], v[100:103]\nds_read_b128 v[48:51], v34 offset:16384\nbuffer_load_dwordx4 v10, s[12:15], s53 offen lds\n"
"s_waitcnt lgkmcnt(3)\nv_mfma_f32_16x16x32_bf16 v[104:107], v[184:187], v[52:55], v[104:107]\ns_add_u32 m0, s52, 16384\nv_mfma_f32_16x16x32_bf16 v[108:111], v[188:191], v[52:55], v[108:111]\n"
"ds_read_b128 v[52:55], v35 offset:16384\ns_add_u32 s53, s49, 512\ns_waitcnt lgkmcnt(3)\nv_mfma_f32_16x16x32_bf16 v[96:99], v[192:195], v[40:43], v[96:99]\n"
"buffer_load_dwordx4 v10, s[12:15], s53 offen lds\nv_mfma_f32_16x16x32_bf16 v[100:103], v[196:199], v[40:43], v[100:103]\nds_read_b128 v[40:43], v32 offset:20480\ns_add_u32 m0, s52, 20480\n"
"s_waitcnt lgkmcnt(3)\nv_mfma_f32_16x16x32_bf16 v[104:107], v[192:195], v[44:47], v[104:107]\ns_add_u32 s53, s49, 640\nv_mfma_f32_16x16x32_bf16 v[108:111], v[196:199], v[44:47], v[108:111]\n"
"ds_read_b128 v[44:47], v33 offset:20480\nbuffer_load_dwordx4 v10, s[12:15], s53 offen lds\ns_waitcnt lgkmcnt(3)\nv_mfma_f32_16x16x32_bf16 v[96:99], v[200:203], v[48:51], v[96:99]\n"
"s_add_u32 m0, s52, 24576\nv_mfma_f32_16x16x32_bf16 v[100:103], v[204:207], v[48:51], v[100:103]\nds_read_b128 v[48:51], v34 offset:20480\ns_add_u32 s53, s49, 768\n"
"s_waitcnt lgkmcnt(3)\nv_mfma_f32_16x16x32_bf16 v[104:107], v[200:203], v[52:55], v[104:107]\nbuffer_load_dwordx4 v10, s[12:15], s53 offen lds\nv_mfma_f32_16x16x32_bf16 v[108:111], v[204:207], v[52:55], v[108:111]\n"
"ds_read_b128 v[52:55], v35 offset:20480\ns_mov_b32 s53, 17408\nbuffer_load_dwordx4 a[28:31], v14, s[16:19], s53 offen sc1\ns_waitcnt lgkmcnt(3)\n"
"v_mfma_f32_16x16x32_bf16 v[96:99], v[208:211], v[40:43], v[96:99]\ns_mov_b32 s53, 4096\nbuffer_load_dwordx4 a[32:35], v14, s[16:19], s53 offen sc1\nv_mfma_f32_16x16x32_bf16 v[100:103], v[212:215], v[40:43], v[100:103]\n"
"ds_read_b128 v[40:43], v32 offset:24576\ns_mov_b32 s53, 18432\nbuffer_load_dwordx4 a[36:39], v14, s[16:19], s53 offen sc1\ns_waitcnt lgkmcnt(3)\n"
"v_mfma_f32_16x16x32_bf16 v[104:107], v[208:211], v[44:47], v[104:107]\ns_mov_b32 s53, 5120\nbuffer_load_dwordx4 a[40:43], v14, s[16:19], s53 offen sc1\nv_mfma_f32_16x16x32_bf16 v[108:111], v[212:215], v[44:47], v[108:111]\n"
"ds_read_b128 v[44:47], v33 offset:24576\ns_mov_b32 s53, 19456\nbuffer_load_dwordx4 a[44:47], v14, s[16:19], s53 offen sc1\ns_waitcnt lgkmcnt(3)\n"
"v_mfma_f32_16x16x32_bf16 v[96:99], v[216:219], v[48:51], v[96:99]\ns_add_u32 s54, s51, 32768\ns_cmp_eq_u32 s54, 163840\nv_mfma_f32_16x16x32_bf16 v[100:103], v[220:223], v[48:51], v[100:103]\n"
"s_cselect_b32 s54, 0, s54\ns_sub_u32 s54, s54, s51\nds_read_b128 v[48:51], v34 offset:24576\ns_mov_b32 s53, 6144\n"
"buffer_load_dwordx4 a[48:51], v14, s[16:19], s53 offen sc1\ns_waitcnt lgkmcnt(3)\nv_mfma_f32_16x16x32_bf16 v[104:107], v[216:219], v[52:55], v[104:107]\ns_mov_b32 s53, 20480\n"
"buffer_load_dwordx4 a[52:55], v14, s[16:19], s53 offen sc1\nv_mfma_f32_16x16x32_bf16 v[108:111], v[220:223], v[52:55], v[108:111]\nds_read_b128 v[52:55], v35 offset:24576\ns_waitcnt lgkmcnt(3)\n"
"v_mfma_f32_16x16x32_bf16 v[96:99], v[224:227], v[40:43], v[96:99]\nv_add_u32 v32, s54, v32\nv_mfma_f32_16x16x32_bf16 v[100:103], v[228:231], v[40:43], v[100:103]\nds_read_b128 v[40:43], v32 offset:0\n"
"s_waitcnt lgkmcnt(3)\nv_mfma_f32_16x16x32_bf16 v[104:107], v[224:227], v[44:47], v[104:107]\nv_add_u32 v33, s54, v33\nv_mfma_f32_16x16x32_bf16 v[108:111], v[228:231], v[44:47], v[108:111]\n"
"ds_read_b128 v[44:47], v33 offset:0\ns_waitcnt lgkmcnt(3)\nv_mfma_f32_16x16x32_bf16 v[96:99], v[232:235], v[48:51], v[96:99]\nv_add_u32 v34, s54, v34\n"
"v_mfma_f32_16x16x32_bf16 v[100:103], v[236:239], v[48:51], v[100:103]\nds_read_b128 v[48:51], v34 offset:0\ns_waitcnt lgkmcnt(3)\nv_mfma_f32_16x16x32_bf16 v[104:107], v[232:235], v[52:55], v[104:107]\n"
"v_add_u32 v35, s54, v35\nv_mfma_f32_16x16x32_bf16 v[108:111], v[236:239], v[52:55], v[108:111]\nds_read_b128 v[52:55], v35 offset:0\ns_mov_b32 s52, s47\n"
"s_add_u32 s47, s51, s32\ns_add_u32 s51, s51, s54\ns_add_u32 s49, s49, 28672\ns_waitcnt vmcnt(14)\n"
"s_barrier\ns_waitcnt lgkmcnt(3)\nv_mfma_f32_16x16x32_bf16 v[64:67], v[128:131], v[40:43], 0\nv_mov_b32 v36, s48\n"
"s_add_u32 s48, s48, 1\ns_nop 0\nbuffer_store_dword v36, v19, s[24:27], s33 offen sc1\nv_mfma_f32_16x16x32_bf16 v[68:71], v[132:135], v[40:43], 0\n"
"ds_read_b128 v[40:43], v32 offset:4096\ns_waitcnt lgkmcnt(3)\nv_mfma_f32_16x16x32_bf16 v[72:75], v[128:131], v[44:47], 0\nv_mfma_f32_16x16x32_bf16 v[76:79], v[132:135], v[44:47], 0\n"
"ds_read_b128 v[44:47], v33 offset:4096\nv_cvt_pk_bf16_f32 v56, v96, v97\ns_waitcnt lgkmcnt(3)\nv_mfma_f32_16x16x32_bf16 v[64:67], v[136:139], v[48:51], v[64:67]\n"
"v_cvt_pk_bf16_f32 v57, v98, v99\nv_mfma_f32_16x16x32_bf16 v[68:71], v[140:143], v[48:51], v[68:71]\nds_read_b128 v[48:51], v34 offset:4096\nv_cvt_pk_bf16_f32 v58, v100, v101\n"
"s_waitcnt lgkmcnt(3)\nv_mfma_f32_16x16x32_bf16 v[72:75], v[136:139], v[52:55], v[72:75]\nv_cvt_pk_bf16_f32 v59, v102, v103\nv_mfma_f32_16x16x32_bf16 v[76:79], v[140:143], v[52:55], v[76:79]\n"
"ds_read_b128 v[52:55], v35 offset:4096\ns_waitcnt lgkmcnt(3)\nv_mfma_f32_16x16x32_bf16 v[64:67], v[144:147], v[40:43], v[64:67]\nv_mfma_f32_16x16x32_bf16 v[68:71], v[148:151], v[40:43], v[68:71]\n"
"ds_read_b128 v[40:43], v32 offset:8192\nbuffer_store_dwordx4 v[56:59], v15, s[20:23], s50 offen sc1\ns_waitcnt lgkmcnt(3)\nv_mfma_f32_16x16x32_bf16 v[72:75], v[144:147], v[44:47], v[72:75]\n"
"v_cvt_pk_bf16_f32 v60, v104, v105\nv_mfma_f32_16x16x32_bf16 v[76:79], v[148:151], v[44:47], v[76:79]\nds_read_b128 v[44:47], v33 offset:8192\nv_cvt_pk_bf16_f32 v61, v106, v107\n"
"s_waitcnt lgkmcnt(3)\nv_mfma_f32_16x16x32_bf16 v[64:67], v[152:155], v[48:51], v[64:67]\nv_cvt_pk_bf16_f32 v62, v108, v109\nv_mfma_f32_16x16x32_bf16 v[68:71], v[156:159], v[48:51], v[68:71]\n"
"ds_read_b128 v[48:51], v34 offset:8192\nv_cvt_pk_bf16_f32 v63, v110, v111\ns_waitcnt lgkmcnt(3)\nv_mfma_f32_16x16x32_bf16 v[72:75], v[152:155], v[52:55], v[72:75]\n"
"v_mfma_f32_16x16x32_bf16 v[76:79], v[156:159], v[52:55], v[76:79]\nds_read_b128 v[52:55], v35 offset:8192\ns_waitcnt lgkmcnt(3)\nv_mfma_f32_16x16x32_bf16 v[64:67], v[160:163], v[40:43], v[64:67]\n"
"buffer_store_dwordx4 v[60:63], v16, s[20:23], s50 offen sc1\nv_mfma_f32_16x16x32_bf16 v[68:71], v[164:167], v[40:43], v[68:71]\nds_read_b128 v[40:43], v32 offset:12288\ns_add_u32 s50, s50, s36\n"
"s_waitcnt lgkmcnt(3)\nv_mfma_f32_16x16x32_bf16 v[72:75], v[160:163], v[44:47], v[72:75]\ns_add_u32 m0, s52, 0\nv_mfma_f32_16x16x32_bf16 v[76:79], v[164:167], v[44:47], v[76:79]\n"
"ds_read_b128 v[44:47], v33 offset:12288\ns_add_u32 s53, s49, 0\ns_waitcnt lgkmcnt(3)\nv_mfma_f32_16x16x32_bf16 v[64:67], v[168:171], v[48:51], v[64:67]\n"
"buffer_load_dwordx4 v10, s[12:15], s53 offen lds\nv_mfma_f32_16x16x32_bf16 v[68:71], v[172:175], v[48:51], v[68:71]\nds_read_b128 v[48:51], v34 offset:12288\ns_add_u32 m0, s52, 4096\n"
"s_waitcnt lgkmcnt(3)\nv_mfma_f32_16x16x32_bf16 v[72:75], v[168:171], v[52:55], v[72:75]\ns_add_u32 s53, s49, 128\nv_mfma_f32_16x16x32_bf16 v[76:79], v[172:175], v[52:55], v[76:79]\n"
"ds_read_b128 v[52:55], v35 offset:12288\nbuffer_load_dwordx4 v10, s[12:15], s53 offen lds\ns_waitcnt lgkmcnt(3)\nv_mfma_f32_16x16x32_bf16 v[64:67], v[176:179], v[40:43], v[64:67]\n"
"s_add_u32 m0, s52, 8192\nv_mfma_f32_16x16x32_bf16 v[68:71], v[180:183], v[40:43], v[68:71]\nds_read_b128 v[40:43], v32 offset:16384\ns_add_u32 s53, s49, 256\n"
"s_waitcnt lgkmcnt(3)\nv_mfma_f32_16x16x32_bf16 v[72:75], v[176:179], v[44:47], v[72:75]\nbuffer_load_dwordx4 v10, s[12:15], s53 offen lds\nv_mfma_f32_16x16x32_bf16 v[76:79], v[180:183], v[44:47], v[76:79]\n"
"ds_read_b128 v[44:47], v33 offset:16384\ns_add_u32 m0, s52, 12288\ns_waitcnt lgkmcnt(3)\nv_mfma_f32_16x16x32_bf16 v[64:67], v[184:187], v[48:51], v[64:67]\n"
"s_add_u32 s53, s49, 384\nv_mfma_f32_16x16x32_bf16 v[68:71], v[188:191], v[48:51], v[68:71]\nds_read_b128 v[48:51], v34 offset:16384\nbuffer_load_dwordx4 v10, s[12:15], s53 offen lds\n"
"s_waitcnt lgkmcnt(3)\nv_mfma_f32_16x16x32_bf16 v[72:75], v[184:187], v[52:55], v[72:75]\ns_add_u32 m0, s52, 16384\nv_mfma_f32_16x16x32_bf16 v[76:79], v[188:191], v[52:55], v[76:79]\n"
"ds_read_b128 v[52:55], v35 offset:16384\ns_add_u32 s53, s49, 512\ns_waitcnt lgkmcnt(3)\nv_mfma_f32_16x16x32_bf16 v[64:67], v[192:195], v[40:43], v[64:67]\n"
"buffer_load_dwordx4 v10, s[12:15], s53 offen lds\nv_mfma_f32_16x16x32_bf16 v[68:71], v[196:199], v[40:43], v[68:71]\nds_read_b128 v[40:43], v32 offset:20480\ns_add_u32 m0, s52, 20480\n"
"s_waitcnt lgkmcnt(3)\nv_mfma_f32_16x16x32_bf16 v[72:75], v[192:195], v[44:47], v[72:75]\ns_add_u32 s53, s49, 640\nv_mfma_f32_16x16x32_bf16 v[76:79], v[196:199], v[44:47], v[76:79]\n"
"ds_read_b128 v[44:47], v33 offset:20480\nbuffer_load_dwordx4 v10, s[12:15], s53 offen lds\ns_waitcnt lgkmcnt(3)\nv_mfma_f32_16x16x32_bf16 v[64:67], v[200:203], v[48:51], v[64:67]\n"
"s_add_u32 m0, s52, 24576\nv_mfma_f32_16x16x32_bf16 v[68:71], v[204:207], v[48:51], v[68:71]\nds_read_b128 v[48:51], v34 offset:20480\ns_add_u32 s53, s49, 768\n"
"s_waitcnt lgkmcnt(3)\nv_mfma_f32_16x16x32_bf16 v[72:75], v[200:203], v[52:55], v[72:75]\nbuffer_load_dwordx4 v10, s[12:15], s53 offen lds\nv_mfma_f32_16x16x32_bf16 v[76:79], v[204:207], v[52:55], v[76:79]\n"
"ds_read_b128 v[52:55], v35 offset:20480\ns_mov_b32 s53, 7168\nbuffer_load_dwordx4 a[56:59], v14, s[16:19], s53 offen sc1\ns_waitcnt lgkmcnt(3)\n"
"v_mfma_f32_16x16x32_bf16 v[64:67], v[208:211], v[40:43], v[64:67]\ns_mov_b32 s53, 21504\nbuffer_load_dwordx4 a[60:63], v14, s[16:19], s53 offen sc1\nv_mfma_f32_16x16x32_bf16 v[68:71], v[212:215], v[40:43], v[68:71]\n"
"ds_read_b128 v[40:43], v32 offset:24576\ns_mov_b32 s53, 8192\nbuffer_load_dwordx4 a[64:67], v14, s[16:19], s53 offen sc1\ns_waitcnt lgkmcnt(3)\n"
"v_mfma_f32_16x16x32_bf16 v[72:75], v[208:211], v[44:47], v[72:75]\ns_mov_b32 s53, 22528\nbuffer_load_dwordx4 a[68:71], v14, s[16:19], s53 offen sc1\nv_mfma_f32_16x16x32_bf16 v[76:79], v[212:215], v[44:47], v[76:79]\n"
"ds_read_b128 v[44:47], v33 offset:24576\ns_mov_b32 s53, 9216\nbuffer_load_dwordx4 a[72:75], v14, s[16:19], s53 offen sc1\ns_waitcnt lgkmcnt(3)\n"
"v_mfma_f32_16x16x32_bf16 v[64:67], v[216:219], v[48:51], v[64:67]\ns_add_u32 s54, s51, 32768\ns_cmp_eq_u32 s54, 163840\nv_mfma_f32_16x16x32_bf16 v[68:71], v[220:223], v[48:51], v[68:71]\n"
"s_cselect_b32 s54, 0, s54\ns_sub_u32 s54, s54, s51\nds_read_b128 v[48:51], v34 offset:24576\ns_mov_b32 s53, 23552\n"
"buffer_load_dwordx4 a[76:79], v14, s[16:19], s53 offen sc1\ns_waitcnt lgkmcnt(3)\nv_mfma_f32_16x16x32_bf16 v[72:75], v[216:219], v[52:55], v[72:75]\ns_mov_b32 s53, 10240\n"
"buffer_load_dwordx4 a[80:83], v14, s[16:19], s53 offen sc1\nv_mfma_f32_16x16x32_bf16 v[76:79], v[220:223], v[52:55], v[76:79]\nds_read_b128 v[52:55], v35 offset:24576\ns_waitcnt lgkmcnt(3)\n"
"v_mfma_f32_16x16x32_bf16 v[64:67], v[224:227], v[40:43], v[64:67]\nv_add_u32 v32, s54, v32\nv_mfma_f32_16x16x32_bf16 v[68:71], v[228:231], v[40:43], v[68:71]\nds_read_b128 v[40:43], v32 offset:0\n"
"s_waitcnt lgkmcnt(3)\nv_mfma_f32_16x16x32_bf16 v[72:75], v[224:227], v[44:47], v[72:75]\nv_add_u32 v33, s54, v33\nv_mfma_f32_16x16x32_bf16 v[76:79], v[228:231], v[44:47], v[76:79]\n"
"ds_read_b128 v[44:47], v33 offset:0\ns_waitcnt lgkmcnt(3)\nv_mfma_f32_16x16x32_bf16 v[64:67], v[232:235], v[48:51], v[64:67]\nv_add_u32 v34, s54, v34\n"
"v_mfma_f32_16x16x32_bf16 v[68:71], v[236:239], v[48:51], v[68:71]\nds_read_b128 v[48:51], v34 offset:0\ns_waitcnt lgkmcnt(3)\nv_mfma_f32_16x16x32_bf16 v[72:75], v[232:235], v[52:55], v[72:75]\n"
"v_add_u32 v35, s54, v35\nv_mfma_f32_16x16x32_bf16 v[76:79], v[236:239], v[52:55], v[76:79]\nds_read_b128 v[52:55], v35 offset:0\ns_mov_b32 s52, s47\n"
"s_add_u32 s47, s51, s32\ns_add_u32 s51, s51, s54\ns_add_u32 s49, s49, 28672\ns_waitcnt vmcnt(14)\n"
"s_barrier\ns_waitcnt lgkmcnt(3)\nv_mfma_f32_16x16x32_bf16 v[96:99], v[128:131], v[40:43], 0\nv_mov_b32 v36, s48\n"
"s_add_u32 s48, s48, 1\ns_nop 0\nbuffer_store_dword v36, v19, s[24:27], s33 offen sc1\nv_mfma_f32_16x16x32_bf16 v[100:103], v[132:135], v[40:43], 0\n"
"ds_read_b128 v[40:43], v32 offset:4096\ns_waitcnt lgkmcnt(3)\nv_mfma_f32_16x16x32_bf16 v[104:107], v[128:131], v[44:47], 0\nv_mfma_f32_16x16x32_bf16 v[108:111], v[132:135], v[44:47], 0\n"
"ds_read_b128 v[44:47], v33 offset:4096\nv_cvt_pk_bf16_f32 v56, v64, v65\ns_waitcnt lgkmcnt(3)\nv_mfma_f32_16x16x32_bf16 v[96:99], v[136:139], v[48:51], v[96:99]\n"
"v_cvt_pk_bf16_f32 v57, v66, v67\nv_mfma_f32_16x16x32_bf16 v[100:103], v[140:143], v[48:51], v[100:103]\nds_read_b128 v[48:51], v34 offset:4096\nv_cvt_pk_bf16_f32 v58, v68, v69\n"
"s_waitcnt lgkmcnt(3)\nv_mfma_f32_16x16x32_bf16 v[104:107], v[136:139], v[52:55], v[104:107]\nv_cvt_pk_bf16_f32 v59, v70, v71\nv_mfma_f32_16x16x32_bf16 v[108:111], v[140:143], v[52:55], v[108:111]\n"
"ds_read_b128 v[52:55], v35 offset:4096\ns_waitcnt lgkmcnt(3)\nv_mfma_f32_16x16x32_bf16 v[96:99], v[144:147], v[40:43], v[96:99]\nv_mfma_f32_16x16x32_bf16 v[100:103], v[148:151], v[40:43], v[100:103]\n"
"ds_read_b128 v[40:43], v32 offset:8192\nbuffer_store_dwordx4 v[56:59], v15, s[20:23], s50 offen sc1\ns_waitcnt lgkmcnt(3)\nv_mfma_f32_16x16x32_bf16 v[104:107], v[144:147], v[44:47], v[104:107]\n"
"v_cvt_pk_bf16_f32 v60, v72, v73\nv_mfma_f32_16x16x32_bf16 v[108:111], v[148:151], v[44:47], v[108:111]\nds_read_b128 v[44:47], v33 offset:8192\nv_cvt_pk_bf16_f32 v61, v74, v75\n"
"s_waitcnt lgkmcnt(3)\nv_mfma_f32_16x16x32_bf16 v[96:99], v[152:155], v[48:51], v[96:99]\nv_cvt_pk_bf16_f32 v62, v76, v77\nv_mfma_f32_16x16x32_bf16 v[100:103], v[156:159], v[48:51], v[100:103]\n"
"ds_read_b128 v[48:51], v34 offset:8192\nv_cvt_pk_bf16_f32 v63, v78, v79\ns_waitcnt lgkmcnt(3)\nv_mfma_f32_16x16x32_bf16 v[104:107], v[152:155], v[52:55], v[104:107]\n"
"v_mfma_f32_16x16x32_bf16 v[108:111], v[156:159], v[52:55], v[108:111]\nds_read_b128 v[52:55], v35 offset:8192\ns_waitcnt lgkmcnt(3)\nv_mfma_f32_16x16x32_bf16 v[96:99], v[160:163], v[40:43], v[96:99]\n"
"buffer_store_dwordx4 v[60:63], v16, s[20:23], s50 offen sc1\nv_mfma_f32_16x16x32_bf16 v[100:103], v[164:167], v[40:43], v[100:103]\nds_read_b128 v[40:43], v32 offset:12288\ns_add_u32 s50, s50, s36\n"
"s_waitcnt lgkmcnt(3)\nv_mfma_f32_16x16x32_bf16 v[104:107], v[160:163], v[44:47], v[104:107]\ns_add_u32 m0, s52, 0\nv_mfma_f32_16x16x32_bf16 v[108:111], v[164:167], v[44:47], v[108:111]\n"
"ds_read_b128 v[44:47], v33 offset:12288\ns_add_u32 s53, s49, 0\ns_waitcnt lgkmcnt(3)\nv_mfma_f32_16x16x32_bf16 v[96:99], v[168:171], v[48:51], v[96:99]\n"
"buffer_load_dwordx4 v10, s[12:15], s53 offen lds\nv_mfma_f32_16x16x32_bf16 v[100:103], v[172:175], v[48:51], v[100:103]\nds_read_b128 v[48:51], v34 offset:12288\ns_add_u32 m0, s52, 4096\n"
"s_waitcnt lgkmcnt(3)\nv_mfma_f32_16x16x32_bf16 v[104:107], v[168:171], v[52:55], v[104:107]\ns_add_u32 s53, s49, 128\nv_mfma_f32_16x16x32_bf16 v[108:111], v[172:175], v[52:55], v[108:111]\n"
"ds_read_b128 v[52:55], v35 offset:12288\nbuffer_load_dwordx4 v10, s[12:15], s53 offen lds\ns_waitcnt lgkmcnt(3)\nv_mfma_f32_16x16x32_bf16 v[96:99], v[176:179], v[40:43], v[96:99]\n"
"s_add_u32 m0, s52, 8192\nv_mfma_f32_16x16x32_bf16 v[100:103], v[180:183], v[40:43], v[100:103]\nds_read_b128 v[40:43], v32 offset:16384\ns_add_u32 s53, s49, 256\n"
"s_waitcnt lgkmcnt(3)\nv_mfma_f32_16x16x32_bf16 v[104:107], v[176:179], v[44:47], v[104:107]\nbuffer_load_dwordx4 v10, s[12:15], s53 offen lds\nv_mfma_f32_16x16x32_bf16 v[108:111], v[180:183], v[44:47], v[108:111]\n"
"ds_read_b128 v[44:47], v33 offset:16384\ns_add_u32 m0, s52, 12288\ns_waitcnt lgkmcnt(3)\nv_mfma_f32_16x16x32_bf16 v[96:99], v[184:187], v[48:51], v[96:99]\n"
"s_add_u32 s53, s49, 384\nv_mfma_f32_16x16x32_bf16 v[100:103], v[188:191], v[48:51], v[100:103]\nds_read_b128 v[48:51], v34 offset:16384\nbuffer_load_dwordx4 v10, s[12:15], s53 offen lds\n"
"s_waitcnt lgkmcnt(3)\nv_mfma_f32_16x16x32_bf16 v[104:107], v[184:187], v[52:55], v[104:107]\ns_add_u32 m0, s52, 16384\nv_mfma_f32_16x16x32_bf16 v[108:111], v[188:191], v[52:55], v[108:111]\n"
"ds_read_b128 v[52:55], v35 offset:16384\ns_add_u32 s53, s49, 512\ns_waitcnt lgkmcnt(3)\nv_mfma_f32_16x16x32_bf16 v[96:99], v[192:195], v[40:43], v[96:99]\n"
"buffer_load_dwordx4 v10, s[12:15], s53 offen lds\nv_mfma_f32_16x16x32_bf16 v[100:103], v[196:199], v[40:43], v[100:103]\nds_read_b128 v[40:43], v32 offset:20480\ns_add_u32 m0, s52, 20480\n"
"s_waitcnt lgkmcnt(3)\nv_mfma_f32_16x16x32_bf16 v[104:107], v[192:195], v[44:47], v[104:107]\ns_add_u32 s53, s49, 640\nv_mfma_f32_16x16x32_bf16 v[108:111], v[196:199], v[44:47], v[108:111]\n"
"ds_read_b128 v[44:47], v33 offset:20480\nbuffer_load_dwordx4 v10, s[12:15], s53 offen lds\ns_waitcnt lgkmcnt(3)\nv_mfma_f32_16x16x32_bf16 v[96:99], v[200:203], v[48:51], v[96:99]\n"
"s_add_u32 m0, s52, 24576\nv_mfma_f32_16x16x32_bf16 v[100:103], v[204:207], v[48:51], v[100:103]\nds_read_b128 v[48:51], v34 offset:20480\ns_add_u32 s53, s49, 768\n"
"s_waitcnt lgkmcnt(3)\nv_mfma_f32_16x16x32_bf16 v[104:107], v[200:203], v[52:55], v[104:107]\nbuffer_load_dwordx4 v10, s[12:15], s53 offen lds\nv_mfma_f32_16x16x32_bf16 v[108:111], v[204:207], v[52:55], v[108:111]\n"
"ds_read_b128 v[52:55], v35 offset:20480\ns_mov_b32 s53, 24576\nbuffer_load_dwordx4 a[84:87], v14, s[16:19], s53 offen sc1\ns_waitcnt lgkmcnt(3)\n"
"v_mfma_f32_16x16x32_bf16 v[96:99], v[208:211], v[40:43], v[96:99]\ns_mov_b32 s53, 11264\nbuffer_load_dwordx4 a[88:91], v14, s[16:19], s53 offen sc1\nv_mfma_f32_16x16x32_bf16 v[100:103], v[212:215], v[40:43], v[100:103]\n"
"ds_read_b128 v[40:43], v32 offset:24576\ns_mov_b32 s53, 25600\nbuffer_load_dwordx4 a[92:95], v14, s[16:19], s53 offen sc1\ns_waitcnt lgkmcnt(3)\n"
"v_mfma_f32_16x16x32_bf16 v[104:107], v[208:211], v[44:47], v[104:107]\ns_mov_b32 s53, 12288\nbuffer_load_dwordx4 a[96:99], v14, s[16:19], s53 offen sc1\nv_mfma_f32_16x16x32_bf16 v[108:111], v[212:215], v[44:47], v[108:111]\n"
"ds_read_b128 v[44:47], v33 offset:24576\ns_mov_b32 s53, 26624\nbuffer_load_dwordx4 a[100:103], v14, s[16:19], s53 offen sc1\ns_waitcnt lgkmcnt(3)\n"
"v_mfma_f32_16x16x32_bf16 v[96:99], v[216:219], v[48:51], v[96:99]\ns_add_u32 s54, s51, 32768\ns_cmp_eq_u32 s54, 163840\nv_mfma_f32_16x16x32_bf16 v[100:103], v[220:223], v[48:51], v[100:103]\n"
"s_cselect_b32 s54, 0, s54\ns_sub_u32 s54, s54, s51\nds_read_b128 v[48:51], v34 offset:24576\ns_mov_b32 s53, 13312\n"
"buffer_load_dwordx4 a[104:107], v14, s[16:19], s53 offen sc1\ns_waitcnt lgkmcnt(3)\nv_mfma_f32_16x16x32_bf16 v[104:107], v[216:219], v[52:55], v[104:107]\ns_mov_b32 s53, 27648\n"
"buffer_load_dwordx4 a[108:111], v14, s[16:19], s53 offen sc1\nv_mfma_f32_16x16x32_bf16 v[108:111], v[220:223], v[52:55], v[108:111]\nds_read_b128 v[52:55], v35 offset:24576\ns_waitcnt lgkmcnt(3)\n"
"v_mfma_f32_16x16x32_bf16 v[96:99], v[224:227], v[40:43], v[96:99]\nv_add_u32 v32, s54, v32\nv_mfma_f32_16x16x32_bf16 v[100:103], v[228:231], v[40:43], v[100:103]\nds_read_b128 v[40:43], v32 offset:0\n"
"s_waitcnt lgkmcnt(3)\nv_mfma_f32_16x16x32_bf16 v[104:107], v[224:227], v[44:47], v[104:107]\nv_add_u32 v33, s54, v33\nv_mfma_f32_16x16x32_bf16 v[108:111], v[228:231], v[44:47], v[108:111]\n"
"ds_read_b128 v[44:47], v33 offset:0\ns_waitcnt lgkmcnt(3)\nv_mfma_f32_16x16x32_bf16 v[96:99], v[232:235], v[48:51], v[96:99]\nv_add_u32 v34, s54, v34\n"
"v_mfma_f32_16x16x32_bf16 v[100:103], v[236:239], v[48:51], v[100:103]\nds_read_b128 v[48:51], v34 offset:0\ns_waitcnt lgkmcnt(3)\nv_mfma_f32_16x16x32_bf16 v[104:107], v[232:235], v[52:55], v[104:107]\n"
"v_add_u32 v35, s54, v35\nv_mfma_f32_16x16x32_bf16 v[108:111], v[236:239], v[52:55], v[108:111]\nds_read_b128 v[52:55], v35 offset:0\ns_mov_b32 s52, s47\n"
"s_add_u32 s47, s51, s32\ns_add_u32 s51, s51, s54\ns_add_u32 s49, s49, 28672\ns_cmp_lg_u32 s34, 0\n"
"s_cbranch_scc1 fg0_stay\ns_nop 7\nv_cvt_pk_bf16_f32 v56, v96, v97\nv_cvt_pk_bf16_f32 v57, v98, v99\n"
"v_cvt_pk_bf16_f32 v58, v100, v101\nv_cvt_pk_bf16_f32 v59, v102, v103\ns_nop 0\ns_nop 0\n"
"buffer_store_dwordx4 v[56:59], v15, s[20:23], s50 offen sc1\nv_cvt_pk_bf16_f32 v60, v104, v105\nv_cvt_pk_bf16_f32 v61, v106, v107\nv_cvt_pk_bf16_f32 v62, v108, v109\n"
"v_cvt_pk_bf16_f32 v63, v110, v111\ns_nop 0\ns_nop 0\nbuffer_store_dwordx4 v[60:63], v16, s[20:23], s50 offen sc1\n"
"s_add_u32 s48, s28, 64\nv_mov_b32 v36, s48\ns_waitcnt vmcnt(0) lgkmcnt(0)\ns_barrier\n"
"buffer_store_dword v36, v19, s[24:27], s33 offen sc1\ns_branch fg0_end\nfg0_stay:\ns_waitcnt vmcnt(0)\n"
"s_barrier\ns_waitcnt lgkmcnt(3)\nv_mfma_f32_16x16x32_bf16 v[64:67], v[128:131], v[40:43], 0\nv_mov_b32 v36, s48\n"
"s_add_u32 s48, s48, 1\ns_nop 0\nbuffer_store_dword v36, v19, s[24:27], s33 offen sc1\nv_mfma_f32_16x16x32_bf16 v[68:71], v[132:135], v[40:43], 0\n"
"v_mfma_f32_16x16x32_bf16 v[72:75], a[0:3], v[40:43], 0\nv_mfma_f32_16x16x32_bf16 v[76:79], a[4:7], v[40:43], 0\nds_read_b128 v[40:43], v32 offset:4096\nv_cvt_pk_bf16_f32 v56, v96, v97\n"
"s_waitcnt lgkmcnt(3)\nv_mfma_f32_16x16x32_bf16 v[80:83], v[128:131], v[44:47], 0\nv_cvt_pk_bf16_f32 v57, v98, v99\nv_mfma_f32_16x16x32_bf16 v[84:87], v[132:135], v[44:47], 0\n"
"v_cvt_pk_bf16_f32 v58, v100, v101\nv_mfma_f32_16x16x32_bf16 v[88:91], a[0:3], v[44:47], 0\nv_cvt_pk_bf16_f32 v59, v102, v103\nv_mfma_f32_16x16x32_bf16 v[92:95], a[4:7], v[44:47], 0\n"
"ds_read_b128 v[44:47], v33 offset:4096\ns_waitcnt lgkmcnt(3)\nv_mfma_f32_16x16x32_bf16 v[64:67], v[136:139], v[48:51], v[64:67]\nv_mfma_f32_16x16x32_bf16 v[68:71], v[140:143], v[48:51], v[68:71]\n"
"buffer_store_dwordx4 v[56:59], v15, s[20:23], s50 offen sc1\nv_mfma_f32_16x16x32_bf16 v[72:75], a[8:11], v[48:51], v[72:75]\nv_cvt_pk_bf16_f32 v60, v104, v105\nv_mfma_f32_16x16x32_bf16 v[76:79], a[12:15], v[48:51], v[76:79]\n"
"ds_read_b128 v[48:51], v34 offset:4096\nv_cvt_pk_bf16_f32 v61, v106, v107\ns_waitcnt lgkmcnt(3)\nv_mfma_f32_16x16x32_bf16 v[80:83], v[136:139], v[52:55], v[80:83]\n"
"v_cvt_pk_bf16_f32 v62, v108, v109\nv_mfma_f32_16x16x32_bf16 v[84:87], v[140:143], v[52:55], v[84:87]\nv_cvt_pk_bf16_f32 v63, v110, v111\nv_mfma_f32_16x16x32_bf16 v[88:91], a[8:11], v[52:55], v[88:91]\n"
"v_mfma_f32_16x16x32_bf16 v[92:95], a[12:15], v[52:55], v[92:95]\nds_read_b128 v[52:55], v35 offset:4096\ns_waitcnt lgkmcnt(3)\nv_mfma_f32_16x16x32_bf16 v[64:67], v[144:147], v[40:43], v[64:67]\n"
"buffer_store_dwordx4 v[60:63], v16, s[20:23], s50 offen sc1\nv_mfma_f32_16x16x32_bf16 v[68:71], v[148:151], v[40:43], v[68:71]\ns_add_u32 s50, s50, s36\nv_mfma_f32_16x16x32_bf16 v[72:75], a[16:19], v[40:43], v[72:75]\n"
"s_add_u32 m0, s52, 0\nv_mfma_f32_16x16x32_bf16 v[76:79], a[20:23], v[40:43], v[76:79]\nds_read_b128 v[40:43], v32 offset:8192\ns_add_u32 s53, s49, 0\n"
"s_waitcnt lgkmcnt(3)\nv_mfma_f32_16x16x32_bf16 v[80:83], v[144:147], v[44:47], v[80:83]\nbuffer_load_dwordx4 v10, s[12:15], s53 offen lds\nv_mfma_f32_16x16x32_bf16 v[84:87], v[148:151], v[44:47], v[84:87]\n"
"v_mfma_f32_16x16x32_bf16 v[88:91], a[16:19], v[44:47], v[88:91]\nv_mfma_f32_16x16x32_bf16 v[92:95], a[20:23], v[44:47], v[92:95]\nds_read_b128 v[44:47], v33 offset:8192\ns_waitcnt lgkmcnt(3)\n"
"v_mfma_f32_16x16x32_bf16 v[64:67], v[152:155], v[48:51], v[64:67]\nv_mfma_f32_16x16x32_bf16 v[68:71], v[156:159], v[48:51], v[68:71]\nv_mfma_f32_16x16x32_bf16 v[72:75], a[24:27], v[48:51], v[72:75]\nv_mfma_f32_16x16x32_bf16 v[76:79], a[28:31], v[48:51], v[76:79]\n"
"ds_read_b128 v[48:51], v34 offset:8192\ns_waitcnt lgkmcnt(3)\nv_mfma_f32_16x16x32_bf16 v[80:83], v[152:155], v[52:55], v[80:83]\nv_mfma_f32_16x16x32_bf16 v[84:87], v[156:159], v[52:55], v[84:87]\n"
"s_add_u32 m0, s52, 4096\nv_mfma_f32_16x16x32_bf16 v[88:91], a[24:27], v[52:55], v[88:91]\ns_add_u32 s53, s49, 128\nv_mfma_f32_16x16x32_bf16 v[92:95], a[28:31], v[52:55], v[92:95]\n"
"ds_read_b128 v[52:55], v35 offset:8192\nbuffer_load_dwordx4 v10, s[12:15], s53 offen lds\ns_waitcnt lgkmcnt(3)\nv_mfma_f32_16x16x32_bf16 v[64:67], v[160:163], v[40:43], v[64:67]\n"
"v_mfma_f32_16x16x32_bf16 v[68:71], v[164:167], v[40:43], v[68:71]\nv_mfma_f32_16x16x32_bf16 v[72:75], a[32:35], v[40:43], v[72:75]\nv_mfma_f32_16x16x32_bf16 v[76:79], a[36:39], v[40:43], v[76:79]\nds_read_b128 v[40:43], v32 offset:12288\n"
"s_waitcnt lgkmcnt(3)\nv_mfma_f32_16x16x32_bf16 v[80:83], v[160:163], v[44:47], v[80:83]\nv_mfma_f32_16x16x32_bf16 v[84:87], v[164:167], v[44:47], v[84:87]\nv_mfma_f32_16x16x32_bf16 v[88:91], a[32:35], v[44:47], v[88:91]\n"
"v_mfma_f32_16x16x32_bf16 v[92:95], a[36:39], v[44:47], v[92:95]\nds_read_b128 v[44:47], v33 offset:12288\ns_waitcnt lgkmcnt(3)\nv_mfma_f32_16x16x32_bf16 v[64:67], v[168:171], v[48:51], v[64:67]\n"
"s_add_u32 m0, s52, 8192\nv_mfma_f32_16x16x32_bf16 v[68:71], v[172:175], v[48:51], v[68:71]\ns_add_u32 s53, s49, 256\nv_mfma_f32_16x16x32_bf16 v[72:75], a[40:43], v[48:51], v[72:75]\n"
"buffer_load_dwordx4 v10, s[12:15], s53 offen lds\nv_mfma_f32_16x16x32_bf16 v[76:79], a[44:47], v[48:51], v[76:79]\nds_read_b128 v[48:51], v34 offset:12288\ns_waitcnt lgkmcnt(3)\n"
"v_mfma_f32_16x16x32_bf16 v[80:83], v[168:171], v[52:55], v[80:83]\nv_mfma_f32_16x16x32_bf16 v[84:87], v[172:175], v[52:55], v[84:87]\nv_mfma_f32_16x16x32_bf16 v[88:91], a[40:43], v[52:55], v[88:91]\nv_mfma_f32_16x16x32_bf16 v[92:95], a[44:47], v[52:55], v[92:95]\n"
"ds_read_b128 v[52:55], v35 offset:12288\ns_waitcnt lgkmcnt(3)\nv_mfma_f32_16x16x32_bf16 v[64:67], v[176:179], v[40:43], v[64:67]\nv_mfma_f32_16x16x32_bf16 v[68:71], v[180:183], v[40:43], v[68:71]\n"
"v_mfma_f32_16x16x32_bf16 v[72:75], a[48:51], v[40:43], v[72:75]\nv_mfma_f32_16x16x32_bf16 v[76:79], a[52:55], v[40:43], v[76:79]\nds_read_b128 v[40:43], v32 offset:16384\ns_add_u32 m0, s52, 12288\n"
"s_waitcnt lgkmcnt(3)\nv_mfma_f32_16x16x32_bf16 v[80:83], v[176:179], v[44:47], v[80:83]\ns_add_u32 s53, s49, 384\nv_mfma_f32_16x16x32_bf16 v[84:87], v[180:183], v[44:47], v[84:87]\n"
"buffer_load_dwordx4 v10, s[12:15], s53 offen lds\nv_mfma_f32_16x16x32_bf16 v[88:91], a[48:51], v[44:47], v[88:91]\nv_mfma_f32_16x16x32_bf16 v[92:95], a[52:55], v[44:47], v[92:95]\nds_read_b128 v[44:47], v33 offset:16384\n"
"s_waitcnt lgkmcnt(3)\nv_mfma_f32_16x16x32_bf16 v[64:67], v[184:187], v[48:51], v[64:67]\nv_mfma_f32_16x16x32_bf16 v[68:71], v[188:191], v[48:51], v[68:71]\nv_mfma_f32_16x16x32_bf16 v[72:75], a[56:59], v[48:51], v[72:75]\n"
"v_mfma_f32_16x16x32_bf16 v[76:79], a[60:63], v[48:51], v[76:79]\nds_read_b128 v[48:51], v34 offset:16384\ns_waitcnt lgkmcnt(3)\nv_mfma_f32_16x16x32_bf16 v[80:83], v[184:187], v[52:55], v[80:83]\n"
"v_mfma_f32_16x16x32_bf16 v[84:87], v[188:191], v[52:55], v[84:87]\nv_mfma_f32_16x16x32_bf16 v[88:91], a[56:59], v[52:55], v[88:91]\ns_add_u32 m0, s52, 16384\nv_mfma_f32_16x16x32_bf16 v[92:95], a[60:63], v[52:55], v[92:95]\n"
"ds_read_b128 v[52:55], v35 offset:16384\ns_add_u32 s53, s49, 512\ns_waitcnt lgkmcnt(3)\nv_mfma_f32_16x16x32_bf16 v[64:67], v[192:195], v[40:43], v[64:67]\n"
"buffer_load_dwordx4 v10, s[12:15], s53 offen lds\nv_mfma_f32_16x16x32_bf16 v[68:71], v[196:199], v[40:43], v[68:71]\nv_mfma_f32_16x16x32_bf16 v[72:75], a[64:67], v[40:43], v[72:75]\nv_mfma_f32_16x16x32_bf16 v[76:79], a[68:71], v[40:43], v[76:79]\n"
"ds_read_b128 v[40:43], v32 offset:20480\ns_waitcnt lgkmcnt(3)\nv_mfma_f32_16x16x32_bf16 v[80:83], v[192:195], v[44:47], v[80:83]\nv_mfma_f32_16x16x32_bf16 v[84:87], v[196:199], v[44:47], v[84:87]\n"
"v_mfma_f32_16x16x32_bf16 v[88:91], a[64:67], v[44:47], v[88:91]\nv_mfma_f32_16x16x32_bf16 v[92:95], a[68:71], v[44:47], v[92:95]\nds_read_b128 v[44:47], v33 offset:20480\ns_waitcnt lgkmcnt(3)\n"
"v_mfma_f32_16x16x32_bf16 v[64:67], v[200:203], v[48:51], v[64:67]\nv_mfma_f32_16x16x32_bf16 v[68:71], v[204:207], v[48:51], v[68:71]\ns_add_u32 m0, s52, 20480\nv_mfma_f32_16x16x32_bf16 v[72:75], a[72:75], v[48:51], v[72:75]\n"
"s_add_u32 s53, s49, 640\nv_mfma_f32_16x16x32_bf16 v[76:79], a[76:79], v[48:51], v[76:79]\nds_read_b128 v[48:51], v34 offset:20480\nbuffer_load_dwordx4 v10, s[12:15], s53 offen lds\n"
"s_waitcnt lgkmcnt(3)\nv_mfma_f32_16x16x32_bf16 v[80:83], v[200:203], v[52:55], v[80:83]\nv_mfma_f32_16x16x32_bf16 v[84:87], v[204:207], v[52:55], v[84:87]\nv_mfma_f32_16x16x32_bf16 v[88:91], a[72:75], v[52:55], v[88:91]\n"
"v_mfma_f32_16x16x32_bf16 v[92:95], a[76:79], v[52:55], v[92:95]\nds_read_b128 v[52:55], v35 offset:20480\ns_waitcnt lgkmcnt(3)\nv_mfma_f32_16x16x32_bf16 v[64:67], v[208:211], v[40:43], v[64:67]\n"
"v_mfma_f32_16x16x32_bf16 v[68:71], v[212:215], v[40:43], v[68:71]\nv_mfma_f32_16x16x32_bf16 v[72:75], a[80:83], v[40:43], v[72:75]\nv_mfma_f32_16x16x32_bf16 v[76:79], a[84:87], v[40:43], v[76:79]\nds_read_b128 v[40:43], v32 offset:24576\n"
"s_waitcnt lgkmcnt(3)\nv_mfma_f32_16x16x32_bf16 v[80:83], v[208:211], v[44:47], v[80:83]\ns_add_u32 m0, s52, 24576\nv_mfma_f32_16x16x32_bf16 v[84:87], v[212:215], v[44:47], v[84:87]\n"
"s_add_u32 s53, s49, 768\nv_mfma_f32_16x16x32_bf16 v[88:91], a[80:83], v[44:47], v[88:91]\nbuffer_load_dwordx4 v10, s[12:15], s53 offen lds\nv_mfma_f32_16x16x32_bf16 v[92:95], a[84:87], v[44:47], v[92:95]\n"
"ds_read_b128 v[44:47], v33 offset:24576\ns_waitcnt lgkmcnt(3)\nv_mfma_f32_16x16x32_bf16 v[64:67], v[216:219], v[48:51], v[64:67]\ns_add_u32 s54, s51, 32768\n"
"s_cmp_eq_u32 s54, 163840\nv_mfma_f32_16x16x32_bf16 v[68:71], v[220:223], v[48:51], v[68:71]\nv_mfma_f32_16x16x32_bf16 v[72:75], a[88:91], v[48:51], v[72:75]\nv_mfma_f32_16x16x32_bf16 v[76:79], a[92:95], v[48:51], v[76:79]\n"
"s_cselect_b32 s54, 0, s54\ns_sub_u32 s54, s54, s51\nds_read_b128 v[48:51], v34 offset:24576\ns_waitcnt lgkmcnt(3)\n"
"v_mfma_f32_16x16x32_bf16 v[80:83], v[216:219], v[52:55], v[80:83]\nv_mfma_f32_16x16x32_bf16 v[84:87], v[220:223], v[52:55], v[84:87]\nv_mfma_f32_16x16x32_bf16 v[88:91], a[88:91], v[52:55], v[88:91]\nv_mfma_f32_16x16x32_bf16 v[92:95], a[92:95], v[52:55], v[92:95]\n"
"ds_read_b128 v[52:55], v35 offset:24576\ns_waitcnt lgkmcnt(3)\nv_mfma_f32_16x16x32_bf16 v[64:67], v[224:227], v[40:43], v[64:67]\nv_add_u32 v32, s54, v32\n"
"v_mfma_f32_16x16x32_bf16 v[68:71], v[228:231], v[40:43], v[68:71]\nv_mfma_f32_16x16x32_bf16 v[72:75], a[96:99], v[40:43], v[72:75]\nv_mfma_f32_16x16x32_bf16 v[76:79], a[100:103], v[40:43], v[76:79]\nds_read_b128 v[40:43], v32 offset:0\n"
"s_waitcnt lgkmcnt(3)\nv_mfma_f32_16x16x32_bf16 v[80:83], v[224:227], v[44:47], v[80:83]\nv_add_u32 v33, s54, v33\nv_mfma_f32_16x16x32_bf16 v[84:87], v[228:231], v[44:47], v[84:87]\n"
"v_mfma_f32_16x16x32_bf16 v[88:91], a[96:99], v[44:47], v[88:91]\nv_mfma_f32_16x16x32_bf16 v[92:95], a[100:103], v[44:47], v[92:95]\nds_read_b128 v[44:47], v33 offset:0\ns_waitcnt lgkmcnt(3)\n"
"v_mfma_f32_16x16x32_bf16 v[64:67], v[232:235], v[48:51], v[64:67]\nv_add_u32 v34, s54, v34\nv_mfma_f32_16x16x32_bf16 v[68:71], v[236:239], v[48:51], v[68:71]\nv_mfma_f32_16x16x32_bf16 v[72:75], a[104:107], v[48:51], v[72:75]\n"
"v_mfma_f32_16x16x32_bf16 v[76:79], a[108:111], v[48:51], v[76:79]\nds_read_b128 v[48:51], v34 offset:0\ns_waitcnt lgkmcnt(3)\nv_mfma_f32_16x16x32_bf16 v[80:83], v[232:235], v[52:55], v[80:83]\n"
"v_add_u32 v35, s54, v35\nv_mfma_f32_16x16x32_bf16 v[84:87], v[236:239], v[52:55], v[84:87]\nv_mfma_f32_16x16x32_bf16 v[88:91], a[104:107], v[52:55], v[88:91]\nv_mfma_f32_16x16x32_bf16 v[92:95], a[108:111], v[52:55], v[92:95]\n"
"ds_read_b128 v[52:55], v35 offset:0\ns_mov_b32 s52, s47\ns_add_u32 s47, s51, s32\ns_add_u32 s51, s51, s54\n"
"s_add_u32 s49, s49, 28672\ns_waitcnt vmcnt(7)\ns_barrier\ns_waitcnt lgkmcnt(3)\n"
"v_mfma_f32_16x16x32_bf16 v[96:99], v[128:131], v[40:43], 0\nv_mov_b32 v36, s48\ns_add_u32 s48, s48, 1\ns_nop 0\n"
"buffer_store_dword v36, v19, s[24:27], s33 offen sc1\nv_mfma_f32_16x16x32_bf16 v[100:103], v[132:135], v[40:43], 0\nv_mfma_f32_16x16x32_bf16 v[104:107], a[0:3], v[40:43], 0\nv_mfma_f32_16x16x32_bf16 v[108:111], a[4:7], v[40:43], 0\n"
"ds_read_b128 v[40:43], v32 offset:4096\nv_cvt_pk_bf16_f32 v56, v64, v65\ns_waitcnt lgkmcnt(3)\nv_mfma_f32_16x16x32_bf16 v[112:115], v[128:131], v[44:47], 0\n"
"v_cvt_pk_bf16_f32 v57, v66, v67\nv_mfma_f32_16x16x32_bf16 v[116:119], v[132:135], v[44:47], 0\nv_cvt_pk_bf16_f32 v58, v68, v69\nv_mfma_f32_16x16x32_bf16 v[120:123], a[0:3], v[44:47], 0\n"
"v_cvt_pk_bf16_f32 v59, v70, v71\nv_mfma_f32_16x16x32_bf16 v[124:127], a[4:7], v[44:47], 0\nds_read_b128 v[44:47], v33 offset:4096\ns_waitcnt lgkmcnt(3)\n"
"v_mfma_f32_16x16x32_bf16 v[96:99], v[136:139], v[48:51], v[96:99]\nv_mfma_f32_16x16x32_bf16 v[100:103], v[140:143], v[48:51], v[100:103]\nbuffer_store_dwordx4 v[56:59], v15, s[20:23], s50 offen sc1\nv_mfma_f32_16x16x32_bf16 v[104:107], a[8:11], v[48:51], v[104:107]\n"
"v_cvt_pk_bf16_f32 v60, v80, v81\nv_mfma_f32_16x16x32_bf16 v[108:111], a[12:15], v[48:51], v[108:111]\nds_read_b128 v[48:51], v34 offset:4096\nv_cvt_pk_bf16_f32 v61, v82, v83\n"
"s_waitcnt lgkmcnt(3)\nv_mfma_f32_16x16x32_bf16 v[112:115], v[136:139], v[52:55], v[112:115]\nv_cvt_pk_bf16_f32 v62, v84, v85\nv_mfma_f32_16x16x32_bf16 v[116:119], v[140:143], v[52:55], v[116:119]\n"
"v_cvt_pk_bf16_f32 v63, v86, v87\nv_mfma_f32_16x16x32_bf16 v[120:123], a[8:11], v[52:55], v[120:123]\nv_mfma_f32_16x16x32_bf16 v[124:127], a[12:15], v[52:55], v[124:127]\nds_read_b128 v[52:55], v35 offset:4096\n"
"s_waitcnt lgkmcnt(3)\nv_mfma_f32_16x16x32_bf16 v[96:99], v[144:147], v[40:43], v[96:99]\nbuffer_store_dwordx4 v[60:63], v16, s[20:23], s50 offen sc1\nv_mfma_f32_16x16x32_bf16 v[100:103], v[148:151], v[40:43], v[100:103]\n"
"v_cvt_pk_bf16_f32 v56, v72, v73\nv_mfma_f32_16x16x32_bf16 v[104:107], a[16:19], v[40:43], v[104:107]\nv_cvt_pk_bf16_f32 v57, v74, v75\nv_mfma_f32_16x16x32_bf16 v[108:111], a[20:23], v[40:43], v[108:111]\n"
"ds_read_b128 v[40:43], v32 offset:8192\nv_cvt_pk_bf16_f32 v58, v76, v77\ns_waitcnt lgkmcnt(3)\nv_mfma_f32_16x16x32_bf16 v[112:115], v[144:147], v[44:47], v[112:115]\n"
"v_cvt_pk_bf16_f32 v59, v78, v79\nv_mfma_f32_16x16x32_bf16 v[116:119], v[148:151], v[44:47], v[116:119]\nv_mfma_f32_16x16x32_bf16 v[120:123], a[16:19], v[44:47], v[120:123]\nv_mfma_f32_16x16x32_bf16 v[124:127], a[20:23], v[44:47], v[124:127]\n"
"ds_read_b128 v[44:47], v33 offset:8192\nbuffer_store_dwordx4 v[56:59], v17, s[20:23], s50 offen sc1\ns_waitcnt lgkmcnt(3)\nv_mfma_f32_16x16x32_bf16 v[96:99], v[152:155], v[48:51], v[96:99]\n"
"v_cvt_pk_bf16_f32 v60, v88, v89\nv_mfma_f32_16x16x32_bf16 v[100:103], v[156:159], v[48:51], v[100:103]\nv_cvt_pk_bf16_f32 v61, v90, v91\nv_mfma_f32_16x16x32_bf16 v[104:107], a[24:27], v[48:51], v[104:107]\n"
"v_cvt_pk_bf16_f32 v62, v92, v93\nv_mfma_f32_16x16x32_bf16 v[108:111], a[28:31], v[48:51], v[108:111]\nds_read_b128 v[48:51], v34 offset:8192\nv_cvt_pk_bf16_f32 v63, v94, v95\n"
"s_waitcnt lgkmcnt(3)\nv_mfma_f32_16x16x32_bf16 v[112:115], v[152:155], v[52:55], v[112:115]\nv_mfma_f32_16x16x32_bf16 v[116:119], v[156:159], v[52:55], v[116:119]\nv_mfma_f32_16x16x32_bf16 v[120:123], a[24:27], v[52:55], v[120:123]\n"
"buffer_store_dwordx4 v[60:63], v18, s[20:23], s50 offen sc1\nv_mfma_f32_16x16x32_bf16 v[124:127], a[28:31], v[52:55], v[124:127]\nds_read_b128 v[52:55], v35 offset:8192\ns_add_u32 s50, s50, s36\n"
"s_waitcnt lgkmcnt(3)\nv_mfma_f32_16x16x32_bf16 v[96:99], v[160:163], v[40:43], v[96:99]\ns_add_u32 m0, s52, 0\nv_mfma_f32_16x16x32_bf16 v[100:103], v[164:167], v[40:43], v[100:103]\n"
"s_add_u32 s53, s49, 0\nv_mfma_f32_16x16x32_bf16 v[104:107], a[32:35], v[40:43], v[104:107]\nbuffer_load_dwordx4 v10, s[12:15], s53 offen lds\nv_mfma_f32_16x16x32_bf16 v[108:111], a[36:39], v[40:43], v[108:111]\n"
"ds_read_b128 v[40:43], v32 offset:12288\ns_waitcnt lgkmcnt(3)\nv_mfma_f32_16x16x32_bf16 v[112:115], v[160:163], v[44:47], v[112:115]\nv_mfma_f32_16x16x32_bf16 v[116:119], v[164:167], v[44:47], v[116:119]\n"
"v_mfma_f32_16x16x32_bf16 v[120:123], a[32:35], v[44:47], v[120:123]\nv_mfma_f32_16x16x32_bf16 v[124:127], a[36:39], v[44:47], v[124:127]\nds_read_b128 v[44:47], v33 offset:12288\ns_waitcnt lgkmcnt(3)\n"
"v_mfma_f32_16x16x32_bf16 v[96:99], v[168:171], v[48:51], v[96:99]\nv_mfma_f32_16x16x32_bf16 v[100:103], v[172:175], v[48:51], v[100:103]\nv_mfma_f32_16x16x32_bf16 v[104:107], a[40:43], v[48:51], v[104:107]\nv_mfma_f32_16x16x32_bf16 v[108:111], a[44:47], v[48:51], v[108:111]\n"
"ds_read_b128 v[48:51], v34 offset:12288\ns_add_u32 m0, s52, 4096\ns_waitcnt lgkmcnt(3)\nv_mfma_f32_16x16x32_bf16 v[112:115], v[168:171], v[52:55], v[112:115]\n"
"s_add_u32 s53, s49, 128\nv_mfma_f32_16x16x32_bf16 v[116:119], v[172:175], v[52:55], v[116:119]\nbuffer_load_dwordx4 v10, s[12:15], s53 offen lds\nv_mfma_f32_16x16x32_bf16 v[120:123], a[40:43], v[52:55], v[120:123]\n"
"v_mfma_f32_16x16x32_bf16 v[124:127], a[44:47], v[52:55], v[124:127]\nds_read_b128 v[52:55], v35 offset:12288\ns_waitcnt lgkmcnt(3)\nv_mfma_f32_16x16x32_bf16 v[96:99], v[176:179], v[40:43], v[96:99]\n"
"v_mfma_f32_16x16x32_bf16 v[100:103], v[180:183], v[40:43], v[100:103]\nv_mfma_f32_16x16x32_bf16 v[104:107], a[48:51], v[40:43], v[104:107]\nv_mfma_f32_16x16x32_bf16 v[108:111], a[52:55], v[40:43], v[108:111]\nds_read_b128 v[40:43], v32 offset:16384\n"
"s_waitcnt lgkmcnt(3)\nv_mfma_f32_16x16x32_bf16 v[112:115], v[176:179], v[44:47], v[112:115]\nv_mfma_f32_16x16x32_bf16 v[116:119], v[180:183], v[44:47], v[116:119]\nv_mfma_f32_16x16x32_bf16 v[120:123], a[48:51], v[44:47], v[120:123]\n"
"s_add_u32 m0, s52, 8192\nv_mfma_f32_16x16x32_bf16 v[124:127], a[52:55], v[44:47], v[124:127]\nds_read_b128 v[44:47], v33 offset:16384\ns_add_u32 s53, s49, 256\n"
"s_waitcnt lgkmcnt(3)\nv_mfma_f32_16x16x32_bf16 v[96:99], v[184:187], v[48:51], v[96:99]\nbuffer_load_dwordx4 v10, s[12:15], s53 offen lds\nv_mfma_f32_16x16x32_bf16 v[100:103], v[188:191], v[48:51], v[100:103]\n"
"v_mfma_f32_16x16x32_bf16 v[104:107], a[56:59], v[48:51], v[104:107]\nv_mfma_f32_16x16x32_bf16 v[108:111], a[60:63], v[48:51], v[108:111]\nds_read_b128 v[48:51], v34 offset:16384\ns_waitcnt lgkmcnt(3)\n"
"v_mfma_f32_16x16x32_bf16 v[112:115], v[184:187], v[52:55], v[112:115]\nv_mfma_f32_16x16x32_bf16 v[116:119], v[188:191], v[52:55], v[116:119]\nv_mfma_f32_16x16x32_bf16 v[120:123], a[56:59], v[52:55], v[120:123]\nv_mfma_f32_16x16x32_bf16 v[124:127], a[60:63], v[52:55], v[124:127]\n"
"ds_read_b128 v[52:55], v35 offset:16384\ns_waitcnt lgkmcnt(3)\nv_mfma_f32_16x16x32_bf16 v[96:99], v[192:195], v[40:43], v[96:99]\nv_mfma_f32_16x16x32_bf16 v[100:103], v[196:199], v[40:43], v[100:103]\n"
"s_add_u32 m0, s52, 12288\nv_mfma_f32_16x16x32_bf16 v[104:107], a[64:67], v[40:43], v[104:107]\ns_add_u32 s53, s49, 384\nv_mfma_f32_16x16x32_bf16 v[108:111], a[68:71], v[40:43], v[108:111]\n"
"ds_read_b128 v[40:43], v32 offset:20480\nbuffer_load_dwordx4 v10, s[12:15], s53 offen lds\ns_waitcnt lgkmcnt(3)\nv_mfma_f32_16x16x32_bf16 v[112:115], v[192:195], v[44:47], v[112:115]\n"
"v_mfma_f32_16x16x32_bf16 v[116:119], v[196:199], v[44:47], v[116:119]\nv_mfma_f32_16x16x32_bf16 v[120:123], a[64:67], v[44:47], v[120:123]\nv_mfma_f32_16x16x32_bf16 v[124:127], a[68:71], v[44:47], v[124:127]\nds_read_b128 v[44:47], v33 offset:20480\n"
"s_waitcnt lgkmcnt(3)\nv_mfma_f32_16x16x32_bf16 v[96:99], v[200:203], v[48:51], v[96:99]\nv_mfma_f32_16x16x32_bf16 v[100:103], v[204:207], v[48:51], v[100:103]\nv_mfma_f32_16x16x32_bf16 v[104:107], a[72:75], v[48:51], v[104:107]\n"
"v_mfma_f32_16x16x32_bf16 v[108:111], a[76:79], v[48:51], v[108:111]\nds_read_b128 v[48:51], v34 offset:20480\ns_waitcnt lgkmcnt(3)\nv_mfma_f32_16x16x32_bf16 v[112:115], v[200:203], v[52:55], v[112:115]\n"
"s_add_u32 m0, s52, 16384\nv_mfma_f32_16x16x32_bf16 v[116:119], v[204:207], v[52:55], v[116:119]\ns_add_u32 s53, s49, 512\nv_mfma_f32_16x16x32_bf16 v[120:123], a[72:75], v[52:55], v[120:123]\n"
"buffer_load_dwordx4 v10, s[12:15], s53 offen lds\nv_mfma_f32_16x16x32_bf16 v[124:127], a[76:79], v[52:55], v[124:127]\nds_read_b128 v[52:55], v35 offset:20480\ns_waitcnt lgkmcnt(3)\n"
"v_mfma_f32_16x16x32_bf16 v[96:99], v[208:211], v[40:43], v[96:99]\nv_mfma_f32_16x16x32_bf16 v[100:103], v[212:215], v[40:43], v[100:103]\nv_mfma_f32_16x16x32_bf16 v[104:107], a[80:83], v[40:43], v[104:107]\nv_mfma_f32_16x16x32_bf16 v[108:111], a[84:87], v[40:43], v[108:111]\n"
"ds_read_b128 v[40:43], v32 offset:24576\ns_waitcnt lgkmcnt(3)\nv_mfma_f32_16x16x32_bf16 v[112:115], v[208:211], v[44:47], v[112:115]\nv_mfma_f32_16x16x32_bf16 v[116:119], v[212:215], v[44:47], v[116:119]\n"
"v_mfma_f32_16x16x32_bf16 v[120:123], a[80:83], v[44:47], v[120:123]\nv_mfma_f32_16x16x32_bf16 v[124:127], a[84:87], v[44:47], v[124:127]\nds_read_b128 v[44:47], v33 offset:24576\ns_add_u32 m0, s52, 20480\n"
"s_waitcnt lgkmcnt(3)\nv_mfma_f32_16x16x32_bf16 v[96:99], v[216:219], v[48:51], v[96:99]\ns_add_u32 s54, s51, 32768\ns_cmp_eq_u32 s54, 163840\n"
"v_mfma_f32_16x16x32_bf16 v[100:103], v[220:223], v[48:51], v[100:103]\nv_mfma_f32_16x16x32_bf16 v[104:107], a[88:91], v[48:51], v[104:107]\nv_mfma_f32_16x16x32_bf16 v[108:111], a[92:95], v[48:51], v[108:111]\ns_cselect_b32 s54, 0, s54\n"
"s_sub_u32 s54, s54, s51\nds_read_b128 v[48:51], v34 offset:24576\ns_add_u32 s53, s49, 640\ns_waitcnt lgkmcnt(3)\n"
"v_mfma_f32_16x16x32_bf16 v[112:115], v[216:219], v[52:55], v[112:115]\nbuffer_load_dwordx4 v10, s[12:15], s53 offen lds\nv_mfma_f32_16x16x32_bf16 v[116:119], v[220:223], v[52:55], v[116:119]\nv_mfma_f32_16x16x32_bf16 v[120:123], a[88:91], v[52:55], v[120:123]\n"
"v_mfma_f32_16x16x32_bf16 v[124:127], a[92:95], v[52:55], v[124:127]\nds_read_b128 v[52:55], v35 offset:24576\ns_waitcnt lgkmcnt(3)\nv_mfma_f32_16x16x32_bf16 v[96:99], v[224:227], v[40:43], v[96:99]\n"
"v_add_u32 v32, s54, v32\nv_mfma_f32_16x16x32_bf16 v[100:103], v[228:231], v[40:43], v[100:103]\nv_mfma_f32_16x16x32_bf16 v[104:107], a[96:99], v[40:43], v[104:107]\nv_mfma_f32_16x16x32_bf16 v[108:111], a[100:103], v[40:43], v[108:111]\n"
"ds_read_b128 v[40:43], v32 offset:0\ns_waitcnt lgkmcnt(3)\nv_mfma_f32_16x16x32_bf16 v[112:115], v[224:227], v[44:47], v[112:115]\nv_add_u32 v33, s54, v33\n"
"v_mfma_f32_16x16x32_bf16 v[116:119], v[228:231], v[44:47], v[116:119]\ns_add_u32 m0, s52, 24576\nv_mfma_f32_16x16x32_bf16 v[120:123], a[96:99], v[44:47], v[120:123]\ns_add_u32 s53, s49, 768\n"
"v_mfma_f32_16x16x32_bf16 v[124:127], a[100:103], v[44:47], v[124:127]\nds_read_b128 v[44:47], v33 offset:0\nbuffer_load_dwordx4 v10, s[12:15], s53 offen lds\ns_waitcnt lgkmcnt(3)\n"
"v_mfma_f32_16x16x32_bf16 v[96:99], v[232:235], v[48:51], v[96:99]\nv_add_u32 v34, s54, v34\nv_mfma_f32_16x16x32_bf16 v[100:103], v[236:239], v[48:51], v[100:103]\nv_mfma_f32_16x16x32_bf16 v[104:107], a[104:107], v[48:51], v[104:107]\n"
"v_mfma_f32_16x16x32_bf16 v[108:111], a[108:111], v[48:51], v[108:111]\nds_read_b128 v[48:51], v34 offset:0\ns_waitcnt lgkmcnt(3)\nv_mfma_f32_16x16x32_bf16 v[112:115], v[232:235], v[52:55], v[112:115]\n"
"v_add_u32 v35, s54, v35\nv_mfma_f32_16x16x32_bf16 v[116:119], v[236:239], v[52:55], v[116:119]\nv_mfma_f32_16x16x32_bf16 v[120:123], a[104:107], v[52:55], v[120:123]\nv_mfma_f32_16x16x32_bf16 v[124:127], a[108:111], v[52:55], v[124:127]\n"
"ds_read_b128 v[52:55], v35 offset:0\ns_mov_b32 s52, s47\ns_add_u32 s47, s51, s32\ns_add_u32 s51, s51, s54\n"
"s_add_u32 s49, s49, 28672\n.p2align 6\nfg0_loop:\ns_waitcnt vmcnt(7)\n"
"s_barrier\ns_waitcnt lgkmcnt(3)\nv_mfma_f32_16x16x32_bf16 v[64:67], v[128:131], v[40:43], 0\nv_mov_b32 v36, s48\n"
"s_add_u32 s48, s48, 1\ns_nop 0\nbuffer_store_dword v36, v19, s[24:27], s33 offen sc1\nv_mfma_f32_16x16x32_bf16 v[68:71], v[132:135], v[40:43], 0\n"
"v_mfma_f32_16x16x32_bf16 v[72:75], a[0:3], v[40:43], 0\nv_mfma_f32_16x16x32_bf16 v[76:79], a[4:7], v[40:43], 0\nds_read_b128 v[40:43], v32 offset:4096\nv_cvt_pk_bf16_f32 v56, v96, v97\n"
"s_waitcnt lgkmcnt(3)\nv_mfma_f32_16x16x32_bf16 v[80:83], v[128:131], v[44:47], 0\nv_cvt_pk_bf16_f32 v57, v98, v99\nv_mfma_f32_16x16x32_bf16 v[84:87], v[132:135], v[44:47], 0\n"
"v_cvt_pk_bf16_f32 v58, v100, v101\nv_mfma_f32_16x16x32_bf16 v[88:91], a[0:3], v[44:47], 0\nv_cvt_pk_bf16_f32 v59, v102, v103\nv_mfma_f32_16x16x32_bf16 v[92:95], a[4:7], v[44:47], 0\n"
"ds_read_b128 v[44:47], v33 offset:4096\ns_waitcnt lgkmcnt(3)\nv_mfma_f32_16x16x32_bf16 v[64:67], v[136:139], v[48:51], v[64:67]\nv_mfma_f32_16x16x32_bf16 v[68:71], v[140:143], v[48:51], v[68:71]\n"
"buffer_store_dwordx4 v[56:59], v15, s[20:23], s50 offen sc1\nv_mfma_f32_16x16x32_bf16 v[72:75], a[8:11], v[48:51], v[72:75]\nv_cvt_pk_bf16_f32 v60, v112, v113\nv_mfma_f32_16x16x32_bf16 v[76:79], a[12:15], v[48:51], v[76:79]\n"
"ds_read_b128 v[48:51], v34 offset:4096\nv_cvt_pk_bf16_f32 v61, v114, v115\ns_waitcnt lgkmcnt(3)\nv_mfma_f32_16x16x32_bf16 v[80:83], v[136:139], v[52:55], v[80:83]\n"
"v_cvt_pk_bf16_f32 v62, v116, v117\nv_mfma_f32_16x16x32_bf16 v[84:87], v[140:143], v[52:55], v[84:87]\nv_cvt_pk_bf16_f32 v63, v118, v119\nv_mfma_f32_16x16x32_bf16 v[88:91], a[8:11], v[52:55], v[88:91]\n"
"v_mfma_f32_16x16x32_bf16 v[92:95], a[12:15], v[52:55], v[92:95]\nds_read_b128 v[52:55], v35 offset:4096\ns_waitcnt lgkmcnt(3)\nv_mfma_f32_16x16x32_bf16 v[64:67], v[144:147], v[40:43], v[64:67]\n"
"buffer_store_dwordx4 v[60:63], v16, s[20:23], s50 offen sc1\nv_mfma_f32_16x16x32_bf16 v[68:71], v[148:151], v[40:43], v[68:71]\nv_cvt_pk_bf16_f32 v56, v104, v105\nv_mfma_f32_16x16x32_bf16 v[72:75], a[16:19], v[40:43], v[72:75]\n"
"v_cvt_pk_bf16_f32 v57, v106, v107\nv_mfma_f32_16x16x32_bf16 v[76:79], a[20:23], v[40:43], v[76:79]\nds_read_b128 v[40:43], v32 offset:8192\nv_cvt_pk_bf16_f32 v58, v108, v109\n"
"s_waitcnt lgkmcnt(3)\nv_mfma_f32_16x16x32_bf16 v[80:83], v[144:147], v[44:47], v[80:83]\nv_cvt_pk_bf16_f32 v59, v110, v111\nv_mfma_f32_16x16x32_bf16 v[84:87], v[148:151], v[44:47], v[84:87]\n"
"v_mfma_f32_16x16x32_bf16 v[88:91], a[16:19], v[44:47], v[88:91]\nv_mfma_f32_16x16x32_bf16 v[92:95], a[20:23], v[44:47], v[92:95]\nds_read_b128 v[44:47], v33 offset:8192\nbuffer_store_dwordx4 v[56:59], v17, s[20:23], s50 offen sc1\n"
"s_waitcnt lgkmcnt(3)\nv_mfma_f32_16x16x32_bf16 v[64:67], v[152:155], v[48:51], v[64:67]\nv_cvt_pk_bf16_f32 v60, v120, v121\nv_mfma_f32_16x16x32_bf16 v[68:71], v[156:159], v[48:51], v[68:71]\n"
"v_cvt_pk_bf16_f32 v61, v122, v123\nv_mfma_f32_16x16x32_bf16 v[72:75], a[24:27], v[48:51], v[72:75]\nv_cvt_pk_bf16_f32 v62, v124, v125\nv_mfma_f32_16x16x32_bf16 v[76:79], a[28:31], v[48:51], v[76:79]\n"
"ds_read_b128 v[48:51], v34 offset:8192\nv_cvt_pk_bf16_f32 v63, v126, v127\ns_waitcnt lgkmcnt(3)\nv_mfma_f32_16x16x32_bf16 v[80:83], v[152:155], v[52:55], v[80:83]\n"
"v_mfma_f32_16x16x32_bf16 v[84:87], v[156:159], v[52:55], v[84:87]\nv_mfma_f32_16x16x32_bf16 v[88:91], a[24:27], v[52:55], v[88:91]\nbuffer_store_dwordx4 v[60:63], v18, s[20:23], s50 offen sc1\nv_mfma_f32_16x16x32_bf16 v[92:95], a[28:31], v[52:55], v[92:95]\n"
"ds_read_b128 v[52:55], v35 offset:8192\ns_add_u32 s50, s50, s36\ns_waitcnt lgkmcnt(3)\nv_mfma_f32_16x16x32_bf16 v[64:67], v[160:163], v[40:43], v[64:67]\n"
"s_add_u32 m0, s52, 0\nv_mfma_f32_16x16x32_bf16 v[68:71], v[164:167], v[40:43], v[68:71]\ns_add_u32 s53, s49, 0\nv_mfma_f32_16x16x32_bf16 v[72:75], a[32:35], v[40:43], v[72:75]\n"
"buffer_load_dwordx4 v10, s[12:15], s53 offen lds\nv_mfma_f32_16x16x32_bf16 v[76:79], a[36:39], v[40:43], v[76:79]\nds_read_b128 v[40:43], v32 offset:12288\ns_waitcnt lgkmcnt(3)\n"
"v_mfma_f32_16x16x32_bf16 v[80:83], v[160:163], v[44:47], v[80:83]\nv_mfma_f32_16x16x32_bf16 v[84:87], v[164:167], v[44:47], v[84:87]\nv_mfma_f32_16x16x32_bf16 v[88:91], a[32:35], v[44:47], v[88:91]\nv_mfma_f32_16x16x32_bf16 v[92:95], a[36:39], v[44:47], v[92:95]\n"
"ds_read_b128 v[44:47], v33 offset:12288\ns_waitcnt lgkmcnt(3)\nv_mfma_f32_16x16x32_bf16 v[64:67], v[168:171], v[48:51], v[64:67]\nv_mfma_f32_16x16x32_bf16 v[68:71], v[172:175], v[48:51], v[68:71]\n"
"v_mfma_f32_16x16x32_bf16 v[72:75], a[40:43], v[48:51], v[72:75]\nv_mfma_f32_16x16x32_bf16 v[76:79], a[44:47], v[48:51], v[76:79]\nds_read_b128 v[48:51], v34 offset:12288\ns_add_u32 m0, s52, 4096\n"
"s_waitcnt lgkmcnt(3)\nv_mfma_f32_16x16x32_bf16 v[80:83], v[168:171], v[52:55], v[80:83]\ns_add_u32 s53, s49, 128\nv_mfma_f32_16x16x32_bf16 v[84:87], v[172:175], v[52:55], v[84:87]\n"
"buffer_load_dwordx4 v10, s[12:15], s53 offen lds\nv_mfma_f32_16x16x32_bf16 v[88:91], a[40:43], v[52:55], v[88:91]\nv_mfma_f32_16x16x32_bf16 v[92:95], a[44:47], v[52:55], v[92:95]\nds_read_b128 v[52:55], v35 offset:12288\n"
"s_waitcnt lgkmcnt(3)\nv_mfma_f32_16x16x32_bf16 v[64:67], v[176:179], v[40:43], v[64:67]\nv_mfma_f32_16x16x32_bf16 v[68:71], v[180:183], v[40:43], v[68:71]\nv_mfma_f32_16x16x32_bf16 v[72:75], a[48:51], v[40:43], v[72:75]\n"
"v_mfma_f32_16x16x32_bf16 v[76:79], a[52:55], v[40:43], v[76:79]\nds_read_b128 v[40:43], v32 offset:16384\ns_waitcnt lgkmcnt(3)\nv_mfma_f32_16x16x32_bf16 v[80:83], v[176:179], v[44:47], v[80:83]\n"
"v_mfma_f32_16x16x32_bf16 v[84:87], v[180:183], v[44:47], v[84:87]\nv_mfma_f32_16x16x32_bf16 v[88:91], a[48:51], v[44:47], v[88:91]\ns_add_u32 m0, s52, 8192\nv_mfma_f32_16x16x32_bf16 v[92:95], a[52:55], v[44:47], v[92:95]\n"
"ds_read_b128 v[44:47], v33 offset:16384\ns_add_u32 s53, s49, 256\ns_waitcnt lgkmcnt(3)\nv_mfma_f32_16x16x32_bf16 v[64:67], v[184:187], v[48:51], v[64:67]\n"
"buffer_load_dwordx4 v10, s[12:15], s53 offen lds\nv_mfma_f32_16x16x32_bf16 v[68:71], v[188:191], v[48:51], v[68:71]\nv_mfma_f32_16x16x32_bf16 v[72:75], a[56:59], v[48:51], v[72:75]\nv_mfma_f32_16x16x32_bf16 v[76:79], a[60:63], v[48:51], v[76:79]\n"
"ds_read_b128 v[48:51], v34 offset:16384\ns_waitcnt lgkmcnt(3)\nv_mfma_f32_16x16x32_bf16 v[80:83], v[184:187], v[52:55], v[80:83]\nv_mfma_f32_16x16x32_bf16 v[84:87], v[188:191], v[52:55], v[84:87]\n"
"v_mfma_f32_16x16x32_bf16 v[88:91], a[56:59], v[52:55], v[88:91]\nv_mfma_f32_16x16x32_bf16 v[92:95], a[60:63], v[52:55], v[92:95]\nds_read_b128 v[52:55], v35 offset:16384\ns_waitcnt lgkmcnt(3)\n"
"v_mfma_f32_16x16x32_bf16 v[64:67], v[192:195], v[40:43], v[64:67]\nv_mfma_f32_16x16x32_bf16 v[68:71], v[196:199], v[40:43], v[68:71]\ns_add_u32 m0, s52, 12288\nv_mfma_f32_16x16x32_bf16 v[72:75], a[64:67], v[40:43], v[72:75]\n"
"s_add_u32 s53, s49, 384\nv_mfma_f32_16x16x32_bf16 v[76:79], a[68:71], v[40:43], v[76:79]\nds_read_b128 v[40:43], v32 offset:20480\nbuffer_load_dwordx4 v10, s[12:15], s53 offen lds\n"
"s_waitcnt lgkmcnt(3)\nv_mfma_f32_16x16x32_bf16 v[80:83], v[192:195], v[44:47], v[80:83]\nv_mfma_f32_16x16x32_bf16 v[84:87], v[196:199], v[44:47], v[84:87]\nv_mfma_f32_16x16x32_bf16 v[88:91], a[64:67], v[44:47], v[88:91]\n"
"v_mfma_f32_16x16x32_bf16 v[92:95], a[68:71], v[44:47], v[92:95]\nds_read_b128 v[44:47], v33 offset:20480\ns_waitcnt lgkmcnt(3)\nv_mfma_f32_16x16x32_bf16 v[64:67], v[200:203], v[48:51], v[64:67]\n"
"v_mfma_f32_16x16x32_bf16 v[68:71], v[204:207], v[48:51], v[68:71]\nv_mfma_f32_16x16x32_bf16 v[72:75], a[72:75], v[48:51], v[72:75]\nv_mfma_f32_16x16x32_bf16 v[76:79], a[76:79], v[48:51], v[76:79]\nds_read_b128 v[48:51], v34 offset:20480\n"
"s_waitcnt lgkmcnt(3)\nv_mfma_f32_16x16x32_bf16 v[80:83], v[200:203], v[52:55], v[80:83]\ns_add_u32 m0, s52, 16384\nv_mfma_f32_16x16x32_bf16 v[84:87], v[204:207], v[52:55], v[84:87]\n"
"s_add_u32 s53, s49, 512\nv_mfma_f32_16x16x32_bf16 v[88:91], a[72:75], v[52:55], v[88:91]\nbuffer_load_dwordx4 v10, s[12:15], s53 offen lds\nv_mfma_f32_16x16x32_bf16 v[92:95], a[76:79], v[52:55], v[92:95]\n"
"ds_read_b128 v[52:55], v35 offset:20480\ns_waitcnt lgkmcnt(3)\nv_mfma_f32_16x16x32_bf16 v[64:67], v[208:211], v[40:43], v[64:67]\nv_mfma_f32_16x16x32_bf16 v[68:71], v[212:215], v[40:43], v[68:71]\n"
"v_mfma_f32_16x16x32_bf16 v[72:75], a[80:83], v[40:43], v[72:75]\nv_mfma_f32_16x16x32_bf16 v[76:79], a[84:87], v[40:43], v[76:79]\nds_read_b128 v[40:43], v32 offset:24576\ns_waitcnt lgkmcnt(3)\n"
"v_mfma_f32_16x16x32_bf16 v[80:83], v[208:211], v[44:47], v[80:83]\nv_mfma_f32_16x16x32_bf16 v[84:87], v[212:215], v[44:47], v[84:87]\nv_mfma_f32_16x16x32_bf16 v[88:91], a[80:83], v[44:47], v[88:91]\nv_mfma_f32_16x16x32_bf16 v[92:95], a[84:87], v[44:47], v[92:95]\n"
"ds_read_b128 v[44:47], v33 offset:24576\ns_add_u32 m0, s52, 20480\ns_waitcnt lgkmcnt(3)\nv_mfma_f32_16x16x32_bf16 v[64:67], v[216:219], v[48:51], v[64:67]\n"
"s_add_u32 s54, s51, 32768\ns_cmp_eq_u32 s54, 163840\nv_mfma_f32_16x16x32_bf16 v[68:71], v[220:223], v[48:51], v[68:71]\nv_mfma_f32_16x16x32_bf16 v[72:75], a[88:91], v[48:51], v[72:75]\n"
"v_mfma_f32_16x16x32_bf16 v[76:79], a[92:95], v[48:51], v[76:79]\ns_cselect_b32 s54, 0, s54\ns_sub_u32 s54, s54, s51\nds_read_b128 v[48:51], v34 offset:24576\n"
"s_add_u32 s53, s49, 640\ns_waitcnt lgkmcnt(3)\nv_mfma_f32_16x16x32_bf16 v[80:83], v[216:219], v[52:55], v[80:83]\nbuffer_load_dwordx4 v10, s[12:15], s53 offen lds\n"
"v_mfma_f32_16x16x32_bf16 v[84:87], v[220:223], v[52:55], v[84:87]\nv_mfma_f32_16x16x32_bf16 v[88:91], a[88:91], v[52:55], v[88:91]\nv_mfma_f32_16x16x32_bf16 v[92:95], a[92:95], v[52:55], v[92:95]\nds_read_b128 v[52:55], v35 offset:24576\n"
"s_waitcnt lgkmcnt(3)\nv_mfma_f32_16x16x32_bf16 v[64:67], v[224:227], v[40:43], v[64:67]\nv_add_u32 v32, s54, v32\nv_mfma_f32_16x16x32_bf16 v[68:71], v[228:231], v[40:43], v[68:71]\n"
"v_mfma_f32_16x16x32_bf16 v[72:75], a[96:99], v[40:43], v[72:75]\nv_mfma_f32_16x16x32_bf16 v[76:79], a[100:103], v[40:43], v[76:79]\nds_read_b128 v[40:43], v32 offset:0\ns_waitcnt lgkmcnt(3)\n"
"v_mfma_f32_16x16x32_bf16 v[80:83], v[224:227], v[44:47], v[80:83]\nv_add_u32 v33, s54, v33\nv_mfma_f32_16x16x32_bf16 v[84:87], v[228:231], v[44:47], v[84:87]\ns_add_u32 m0, s52, 24576\n"
"v_mfma_f32_16x16x32_bf16 v[88:91], a[96:99], v[44:47], v[88:91]\ns_add_u32 s53, s49, 768\nv_mfma_f32_16x16x32_bf16 v[92:95], a[100:103], v[44:47], v[92:95]\nds_read_b128 v[44:47], v33 offset:0\n"
"buffer_load_dwordx4 v10, s[12:15], s53 offen lds\ns_waitcnt lgkmcnt(3)\nv_mfma_f32_16x16x32_bf16 v[64:67], v[232:235], v[48:51], v[64:67]\nv_add_u32 v34, s54, v34\n"
"v_mfma_f32_16x16x32_bf16 v[68:71], v[236:239], v[48:51], v[68:71]\nv_mfma_f32_16x16x32_bf16 v[72:75], a[104:107], v[48:51], v[72:75]\nv_mfma_f32_16x16x32_bf16 v[76:79], a[108:111], v[48:51], v[76:79]\nds_read_b128 v[48:51], v34 offset:0\n"
"s_waitcnt lgkmcnt(3)\nv_mfma_f32_16x16x32_bf16 v[80:83], v[232:235], v[52:55], v[80:83]\nv_add_u32 v35, s54, v35\nv_mfma_f32_16x16x32_bf16 v[84:87], v[236:239], v[52:55], v[84:87]\n"
"v_mfma_f32_16x16x32_bf16 v[88:91], a[104:107], v[52:55], v[88:91]\nv_mfma_f32_16x16x32_bf16 v[92:95], a[108:111], v[52:55], v[92:95]\nds_read_b128 v[52:55], v35 offset:0\ns_mov_b32 s52, s47\n"
"s_add_u32 s47, s51, s32\ns_add_u32 s51, s51, s54\ns_add_u32 s49, s49, 28672\ns_waitcnt vmcnt(7)\n"
"s_barrier\ns_waitcnt lgkmcnt(3)\nv_mfma_f32_16x16x32_bf16 v[96:99], v[128:131], v[40:43], 0\ns_add_u32 s48, s48, 1\n"
"v_mfma_f32_16x16x32_bf16 v[100:103], v[132:135], v[40:43], 0\nv_mfma_f32_16x16x32_bf16 v[104:107], a[0:3], v[40:43], 0\nv_mfma_f32_16x16x32_bf16 v[108:111], a[4:7], v[40:43], 0\nds_read_b128 v[40:43], v32 offset:4096\n"
"v_cvt_pk_bf16_f32 v56, v64, v65\ns_waitcnt lgkmcnt(3)\nv_mfma_f32_16x16x32_bf16 v[112:115], v[128:131], v[44:47], 0\nv_cvt_pk_bf16_f32 v57, v66, v67\n"
"v_mfma_f32_16x16x32_bf16 v[116:119], v[132:135], v[44:47], 0\nv_cvt_pk_bf16_f32 v58, v68, v69\nv_mfma_f32_16x16x32_bf16 v[120:123], a[0:3], v[44:47], 0\nv_cvt_pk_bf16_f32 v59, v70, v71\n"
"v_mfma_f32_16x16x32_bf16 v[124:127], a[4:7], v[44:47], 0\nds_read_b128 v[44:47], v33 offset:4096\ns_waitcnt lgkmcnt(3)\nv_mfma_f32_16x16x32_bf16 v[96:99], v[136:139], v[48:51], v[96:99]\n"
"v_mfma_f32_16x16x32_bf16 v[100:103], v[140:143], v[48:51], v[100:103]\nbuffer_store_dwordx4 v[56:59], v15, s[20:23], s50 offen sc1\nv_mfma_f32_16x16x32_bf16 v[104:107], a[8:11], v[48:51], v[104:107]\nv_cvt_pk_bf16_f32 v60, v80, v81\n"
"v_mfma_f32_16x16x32_bf16 v[108:111], a[12:15], v[48:51], v[108:111]\nds_read_b128 v[48:51], v34 offset:4096\nv_cvt_pk_bf16_f32 v61, v82, v83\ns_waitcnt lgkmcnt(3)\n"
"v_mfma_f32_16x16x32_bf16 v[112:115], v[136:139], v[52:55], v[112:115]\nv_cvt_pk_bf16_f32 v62, v84, v85\nv_mfma_f32_16x16x32_bf16 v[116:119], v[140:143], v[52:55], v[116:119]\nv_cvt_pk_bf16_f32 v63, v86, v87\n"
"v_mfma_f32_16x16x32_bf16 v[120:123], a[8:11], v[52:55], v[120:123]\nv_mfma_f32_16x16x32_bf16 v[124:127], a[12:15], v[52:55], v[124:127]\nds_read_b128 v[52:55], v35 offset:4096\ns_waitcnt lgkmcnt(3)\n"
"v_mfma_f32_16x16x32_bf16 v[96:99], v[144:147], v[40:43], v[96:99]\nbuffer_store_dwordx4 v[60:63], v16, s[20:23], s50 offen sc1\nv_mfma_f32_16x16x32_bf16 v[100:103], v[148:151], v[40:43], v[100:103]\nv_cvt_pk_bf16_f32 v56, v72, v73\n"
"v_mfma_f32_16x16x32_bf16 v[104:107], a[16:19], v[40:43], v[104:107]\nv_cvt_pk_bf16_f32 v57, v74, v75\nv_mfma_f32_16x16x32_bf16 v[108:111], a[20:23], v[40:43], v[108:111]\nds_read_b128 v[40:43], v32 offset:8192\n"
"v_cvt_pk_bf16_f32 v58, v76, v77\ns_waitcnt lgkmcnt(3)\nv_mfma_f32_16x16x32_bf16 v[112:115], v[144:147], v[44:47], v[112:115]\nv_cvt_pk_bf16_f32 v59, v78, v79\n"
"v_mfma_f32_16x16x32_bf16 v[116:119], v[148:151], v[44:47], v[116:119]\nv_mfma_f32_16x16x32_bf16 v[120:123], a[16:19], v[44:47], v[120:123]\nv_mfma_f32_16x16x32_bf16 v[124:127], a[20:23], v[44:47], v[124:127]\nds_read_b128 v[44:47], v33 offset:8192\n"
"buffer_store_dwordx4 v[56:59], v17, s[20:23], s50 offen sc1\ns_waitcnt lgkmcnt(3)\nv_mfma_f32_16x16x32_bf16 v[96:99], v[152:155], v[48:51], v[96:99]\nv_cvt_pk_bf16_f32 v60, v88, v89\n"
"v_mfma_f32_16x16x32_bf16 v[100:103], v[156:159], v[48:51], v[100:103]\nv_cvt_pk_bf16_f32 v61, v90, v91\nv_mfma_f32_16x16x32_bf16 v[104:107], a[24:27], v[48:51], v[104:107]\nv_cvt_pk_bf16_f32 v62, v92, v93\n"
"v_mfma_f32_16x16x32_bf16 v[108:111], a[28:31], v[48:51], v[108:111]\nds_read_b128 v[48:51], v34 offset:8192\nv_cvt_pk_bf16_f32 v63, v94, v95\ns_waitcnt lgkmcnt(3)\n"
"v_mfma_f32_16x16x32_bf16 v[112:115], v[152:155], v[52:55], v[112:115]\nv_mfma_f32_16x16x32_bf16 v[116:119], v[156:159], v[52:55], v[116:119]\nv_mfma_f32_16x16x32_bf16 v[120:123], a[24:27], v[52:55], v[120:123]\nbuffer_store_dwordx4 v[60:63], v18, s[20:23], s50 offen sc1\n"
"v_mfma_f32_16x16x32_bf16 v[124:127], a[28:31], v[52:55], v[124:127]\nds_read_b128 v[52:55], v35 offset:8192\ns_add_u32 s50, s50, s36\ns_waitcnt lgkmcnt(3)\n"
"v_mfma_f32_16x16x32_bf16 v[96:99], v[160:163], v[40:43], v[96:99]\ns_add_u32 m0, s52, 0\nv_mfma_f32_16x16x32_bf16 v[100:103], v[164:167], v[40:43], v[100:103]\ns_add_u32 s53, s49, 0\n"
"v_mfma_f32_16x16x32_bf16 v[104:107], a[32:35], v[40:43], v[104:107]\nbuffer_load_dwordx4 v10, s[12:15], s53 offen lds\nv_mfma_f32_16x16x32_bf16 v[108:111], a[36:39], v[40:43], v[108:111]\nds_read_b128 v[40:43], v32 offset:12288\n"
"s_waitcnt lgkmcnt(3)\nv_mfma_f32_16x16x32_bf16 v[112:115], v[160:163], v[44:47], v[112:115]\nv_mfma_f32_16x16x32_bf16 v[116:119], v[164:167], v[44:47], v[116:119]\nv_mfma_f32_16x16x32_bf16 v[120:123], a[32:35], v[44:47], v[120:123]\n"
"v_mfma_f32_16x16x32_bf16 v[124:127], a[36:39], v[44:47], v[124:127]\nds_read_b128 v[44:47], v33 offset:12288\ns_waitcnt lgkmcnt(3)\nv_mfma_f32_16x16x32_bf16 v[96:99], v[168:171], v[48:51], v[96:99]\n"
"v_mfma_f32_16x16x32_bf16 v[100:103], v[172:175], v[48:51], v[100:103]\nv_mfma_f32_16x16x32_bf16 v[104:107], a[40:43], v[48:51], v[104:107]\nv_mfma_f32_16x16x32_bf16 v[108:111], a[44:47], v[48:51], v[108:111]\nds_read_b128 v[48:51], v34 offset:12288\n"
"s_add_u32 m0, s52, 4096\ns_waitcnt lgkmcnt(3)\nv_mfma_f32_16x16x32_bf16 v[112:115], v[168:171], v[52:55], v[112:115]\ns_add_u32 s53, s49, 128\n"
"v_mfma_f32_16x16x32_bf16 v[116:119], v[172:175], v[52:55], v[116:119]\nbuffer_load_dwordx4 v10, s[12:15], s53 offen lds\nv_mfma_f32_16x16x32_bf16 v[120:123], a[40:43], v[52:55], v[120:123]\nv_mfma_f32_16x16x32_bf16 v[124:127], a[44:47], v[52:55], v[124:127]\n"
"ds_read_b128 v[52:55], v35 offset:12288\ns_waitcnt lgkmcnt(3)\nv_mfma_f32_16x16x32_bf16 v[96:99], v[176:179], v[40:43], v[96:99]\nv_mfma_f32_16x16x32_bf16 v[100:103], v[180:183], v[40:43], v[100:103]\n"
"v_mfma_f32_16x16x32_bf16 v[104:107], a[48:51], v[40:43], v[104:107]\nv_mfma_f32_16x16x32_bf16 v[108:111], a[52:55], v[40:43], v[108:111]\nds_read_b128 v[40:43], v32 offset:16384\ns_waitcnt lgkmcnt(3)\n"
"v_mfma_f32_16x16x32_bf16 v[112:115], v[176:179], v[44:47], v[112:115]\nv_mfma_f32_16x16x32_bf16 v[116:119], v[180:183], v[44:47], v[116:119]\nv_mfma_f32_16x16x32_bf16 v[120:123], a[48:51], v[44:47], v[120:123]\ns_add_u32 m0, s52, 8192\n"
"v_mfma_f32_16x16x32_bf16 v[124:127], a[52:55], v[44:47], v[124:127]\nds_read_b128 v[44:47], v33 offset:16384\ns_add_u32 s53, s49, 256\ns_waitcnt lgkmcnt(3)\n"
"v_mfma_f32_16x16x32_bf16 v[96:99], v[184:187], v[48:51], v[96:99]\nbuffer_load_dwordx4 v10, s[12:15], s53 offen lds\nv_mfma_f32_16x16x32_bf16 v[100:103], v[188:191], v[48:51], v[100:103]\nv_mfma_f32_16x16x32_bf16 v[104:107], a[56:59], v[48:51], v[104:107]\n"
"v_mfma_f32_16x16x32_bf16 v[108:111], a[60:63], v[48:51], v[108:111]\nds_read_b128 v[48:51], v34 offset:16384\ns_waitcnt lgkmcnt(3)\nv_mfma_f32_16x16x32_bf16 v[112:115], v[184:187], v[52:55], v[112:115]\n"
"v_mfma_f32_16x16x32_bf16 v[116:119], v[188:191], v[52:55], v[116:119]\nv_mfma_f32_16x16x32_bf16 v[120:123], a[56:59], v[52:55], v[120:123]\nv_mfma_f32_16x16x32_bf16 v[124:127], a[60:63], v[52:55], v[124:127]\nds_read_b128 v[52:55], v35 offset:16384\n"
"s_waitcnt lgkmcnt(3)\nv_mfma_f32_16x16x32_bf16 v[96:99], v[192:195], v[40:43], v[96:99]\nv_mfma_f32_16x16x32_bf16 v[100:103], v[196:199], v[40:43], v[100:103]\ns_add_u32 m0, s52, 12288\n"
"v_mfma_f32_16x16x32_bf16 v[104:107], a[64:67], v[40:43], v[104:107]\ns_add_u32 s53, s49, 384\nv_mfma_f32_16x16x32_bf16 v[108:111], a[68:71], v[40:43], v[108:111]\nds_read_b128 v[40:43], v32 offset:20480\n"
"buffer_load_dwordx4 v10, s[12:15], s53 offen lds\ns_waitcnt lgkmcnt(3)\nv_mfma_f32_16x16x32_bf16 v[112:115], v[192:195], v[44:47], v[112:115]\nv_mfma_f32_16x16x32_bf16 v[116:119], v[196:199], v[44:47], v[116:119]\n"
"v_mfma_f32_16x16x32_bf16 v[120:123], a[64:67], v[44:47], v[120:123]\nv_mfma_f32_16x16x32_bf16 v[124:127], a[68:71], v[44:47], v[124:127]\nds_read_b128 v[44:47], v33 offset:20480\ns_waitcnt lgkmcnt(3)\n"
"v_mfma_f32_16x16x32_bf16 v[96:99], v[200:203], v[48:51], v[96:99]\nv_mfma_f32_16x16x32_bf16 v[100:103], v[204:207], v[48:51], v[100:103]\nv_mfma_f32_16x16x32_bf16 v[104:107], a[72:75], v[48:51], v[104:107]\nv_mfma_f32_16x16x32_bf16 v[108:111], a[76:79], v[48:51], v[108:111]\n"
"ds_read_b128 v[48:51], v34 offset:20480\ns_waitcnt lgkmcnt(3)\nv_mfma_f32_16x16x32_bf16 v[112:115], v[200:203], v[52:55], v[112:115]\ns_add_u32 m0, s52, 16384\n"
"v_mfma_f32_16x16x32_bf16 v[116:119], v[204:207], v[52:55], v[116:119]\ns_add_u32 s53, s49, 512\nv_mfma_f32_16x16x32_bf16 v[120:123], a[72:75], v[52:55], v[120:123]\nbuffer_load_dwordx4 v10, s[12:15], s53 offen lds\n"
"v_mfma_f32_16x16x32_bf16 v[124:127], a[76:79], v[52:55], v[124:127]\nds_read_b128 v[52:55], v35 offset:20480\ns_waitcnt lgkmcnt(3)\nv_mfma_f32_16x16x32_bf16 v[96:99], v[208:211], v[40:43], v[96:99]\n"
"v_mfma_f32_16x16x32_bf16 v[100:103], v[212:215], v[40:43], v[100:103]\nv_mfma_f32_16x16x32_bf16 v[104:107], a[80:83], v[40:43], v[104:107]\nv_mfma_f32_16x16x32_bf16 v[108:111], a[84:87], v[40:43], v[108:111]\nds_read_b128 v[40:43], v32 offset:24576\n"
"s_waitcnt lgkmcnt(3)\nv_mfma_f32_16x16x32_bf16 v[112:115], v[208:211], v[44:47], v[112:115]\nv_mfma_f32_16x16x32_bf16 v[116:119], v[212:215], v[44:47], v[116:119]\nv_mfma_f32_16x16x32_bf16 v[120:123], a[80:83], v[44:47], v[120:123]\n"
"v_mfma_f32_16x16x32_bf16 v[124:127], a[84:87], v[44:47], v[124:127]\nds_read_b128 v[44:47], v33 offset:24576\ns_add_u32 m0, s52, 20480\ns_waitcnt lgkmcnt(3)\n"
"v_mfma_f32_16x16x32_bf16 v[96:99], v[216:219], v[48:51], v[96:99]\ns_add_u32 s54, s51, 32768\ns_cmp_eq_u32 s54, 163840\nv_mfma_f32_16x16x32_bf16 v[100:103], v[220:223], v[48:51], v[100:103]\n"
"v_mfma_f32_16x16x32_bf16 v[104:107], a[88:91], v[48:51], v[104:107]\nv_mfma_f32_16x16x32_bf16 v[108:111], a[92:95], v[48:51], v[108:111]\ns_cselect_b32 s54, 0, s54\ns_sub_u32 s54, s54, s51\n"
"ds_read_b128 v[48:51], v34 offset:24576\ns_add_u32 s53, s49, 640\ns_waitcnt lgkmcnt(3)\nv_mfma_f32_16x16x32_bf16 v[112:115], v[216:219], v[52:55], v[112:115]\n"
"buffer_load_dwordx4 v10, s[12:15], s53 offen lds\nv_mfma_f32_16x16x32_bf16 v[116:119], v[220:223], v[52:55], v[116:119]\nv_mfma_f32_16x16x32_bf16 v[120:123], a[88:91], v[52:55], v[120:123]\nv_mfma_f32_16x16x32_bf16 v[124:127], a[92:95], v[52:55], v[124:127]\n"
"ds_read_b128 v[52:55], v35 offset:24576\ns_waitcnt lgkmcnt(3)\nv_mfma_f32_16x16x32_bf16 v[96:99], v[224:227], v[40:43], v[96:99]\nv_add_u32 v32, s54, v32\n"
"v_mfma_f32_16x16x32_bf16 v[100:103], v[228:231], v[40:43], v[100:103]\nv_mfma_f32_16x16x32_bf16 v[104:107], a[96:99], v[40:43], v[104:107]\nv_mfma_f32_16x16x32_bf16 v[108:111], a[100:103], v[40:43], v[108:111]\nds_read_b128 v[40:43], v32 offset:0\n"
"s_waitcnt lgkmcnt(3)\nv_mfma_f32_16x16x32_bf16 v[112:115], v[224:227], v[44:47], v[112:115]\nv_add_u32 v33, s54, v33\nv_mfma_f32_16x16x32_bf16 v[116:119], v[228:231], v[44:47], v[116:119]\n"
"s_add_u32 m0, s52, 24576\nv_mfma_f32_16x16x32_bf16 v[120:123], a[96:99], v[44:47], v[120:123]\ns_add_u32 s53, s49, 768\nv_mfma_f32_16x16x32_bf16 v[124:127], a[100:103], v[44:47], v[124:127]\n"
"ds_read_b128 v[44:47], v33 offset:0\nbuffer_load_dwordx4 v10, s[12:15], s53 offen lds\ns_waitcnt lgkmcnt(3)\nv_mfma_f32_16x16x32_bf16 v[96:99], v[232:235], v[48:51], v[96:99]\n"
"v_add_u32 v34, s54, v34\nv_mfma_f32_16x16x32_bf16 v[100:103], v[236:239], v[48:51], v[100:103]\nv_mfma_f32_16x16x32_bf16 v[104:107], a[104:107], v[48:51], v[104:107]\nv_mfma_f32_16x16x32_bf16 v[108:111], a[108:111], v[48:51], v[108:111]\n"
"ds_read_b128 v[48:51], v34 offset:0\ns_waitcnt lgkmcnt(3)\nv_mfma_f32_16x16x32_bf16 v[112:115], v[232:235], v[52:55], v[112:115]\nv_add_u32 v35, s54, v35\n"
"v_mfma_f32_16x16x32_bf16 v[116:119], v[236:239], v[52:55], v[116:119]\nv_mfma_f32_16x16x32_bf16 v[120:123], a[104:107], v[52:55], v[120:123]\nv_mfma_f32_16x16x32_bf16 v[124:127], a[108:111], v[52:55], v[124:127]\nds_read_b128 v[52:55], v35 offset:0\n"
"s_mov_b32 s52, s47\ns_add_u32 s47, s51, s32\ns_add_u32 s51, s51, s54\ns_add_u32 s49, s49, 28672\n"
"s_cmp_lt_u32 s49, 1921024\ns_cbranch_scc1 fg0_loop\ns_nop 7\nv_cvt_pk_bf16_f32 v56, v96, v97\n"
"v_cvt_pk_bf16_f32 v57, v98, v99\nv_cvt_pk_bf16_f32 v58, v100, v101\nv_cvt_pk_bf16_f32 v59, v102, v103\ns_nop 0\n"
"s_nop 0\nbuffer_store_dwordx4 v[56:59], v15, s[20:23], s50 offen sc1\nv_cvt_pk_bf16_f32 v60, v112, v113\nv_cvt_pk_bf16_f32 v61, v114, v115\n"
"v_cvt_pk_bf16_f32 v62, v116, v117\nv_cvt_pk_bf16_f32 v63, v118, v119\ns_nop 0\ns_nop 0\n"
"buffer_store_dwordx4 v[60:63], v16, s[20:23], s50 offen sc1\nv_cvt_pk_bf16_f32 v56, v104, v105\nv_cvt_pk_bf16_f32 v57, v106, v107\nv_cvt_pk_bf16_f32 v58, v108, v109\n"
"v_cvt_pk_bf16_f32 v59, v110, v111\ns_nop 0\ns_nop 0\nbuffer_store_dwordx4 v[56:59], v17, s[20:23], s50 offen sc1\n"
"v_cvt_pk_bf16_f32 v60, v120, v121\nv_cvt_pk_bf16_f32 v61, v122, v123\nv_cvt_pk_bf16_f32 v62, v124, v125\nv_cvt_pk_bf16_f32 v63, v126, v127\n"
"s_nop 0\ns_nop 0\nbuffer_store_dwordx4 v[60:63], v18, s[20:23], s50 offen sc1\ns_add_u32 s48, s28, 64\n"
"v_mov_b32 v36, s48\ns_waitcnt vmcnt(0) lgkmcnt(0)\ns_barrier\nbuffer_store_dword v36, v19, s[24:27], s33 offen sc1\n"
"fg0_end:\n"
    : "+{v[244:251]}"(w) : "{s[8:23]}"(s0), "{s[24:39]}"(s1), "{v[8:23]}"(v0) : "v24", "v25", "v26", "v27", "v28", "v29", "v30", "v31", "v32", "v33", "v34", "v35", "v36", "v37", "v38", "v39", "v40", "v41", "v42", "v43", "v44", "v45", "v46", "v47", "v48", "v49", "v50", "v51", "v52", "v53", "v54", "v55", "v56", "v57", "v58", "v59", "v60", "v61", "v62", "v63", "v64", "v65", "v66", "v67", "v68", "v69", "v70", "v71", "v72", "v73", "v74", "v75", "v76", "v77", "v78", "v79", "v80", "v81", "v82", "v83", "v84", "v85", "v86", "v87", "v88", "v89", "v90", "v91", "v92", "v93", "v94", "v95", "v96", "v97", "v98", "v99", "v100", "v101", "v102", "v103", "v104", "v105", "v106", "v107", "v108", "v109", "v110", "v111", "v112", "v113", "v114", "v115", "v116", "v117", "v118", "v119", "v120", "v121", "v122", "v123", "v124", "v125", "v126", "v127", "v128", "v129", "v130", "v131", "v132", "v133", "v134", "v135", "v136", "v137", "v138", "v139", "v140", "v141", "v142", "v143", "v144", "v145", "v146", "v147", "v148", "v149", "v150", "v151", "v152", "v153", "v154", "v155", "v156", "v157", "v158", "v159", "v160", "v161", "v162", "v163", "v164", "v165", "v166", "v167", "v168", "v169", "v170", "v171", "v172", "v173", "v174", "v175", "v176", "v177", "v178", "v179", "v180", "v181", "v182", "v183", "v184", "v185", "v186", "v187", "v188", "v189", "v190", "v191", "v192", "v193", "v194", "v195", "v196", "v197", "v198", "v199", "v200", "v201", "v202", "v203", "v204", "v205", "v206", "v207", "v208", "v209", "v210", "v211", "v212", "v213", "v214", "v215", "v216", "v217", "v218", "v219", "v220", "v221", "v222", "v223", "v224", "v225", "v226", "v227", "v228", "v229", "v230", "v231", "v232", "v233", "v234", "v235", "v236", "v237", "v238", "v239", "v240", "v241", "v242", "v243", "a0", "a1", "a2", "a3", "a4", "a5", "a6", "a7", "a8", "a9", "a10", "a11", "a12", "a13", "a14", "a15", "a16", "a17", "a18", "a19", "a20", "a21", "a22", "a23", "a24", "a25", "a26", "a27", "a28", "a29", "a30", "a31", "a32", "a33", "a34", "a35", "a36", "a37", "a38", "a39", "a40", "a41", "a42", "a43", "a44", "a45", "a46", "a47", "a48", "a49", "a50", "a51", "a52", "a53", "a54", "a55", "a56", "a57", "a58", "a59", "a60", "a61", "a62", "a63", "a64", "a65", "a66", "a67", "a68", "a69", "a70", "a71", "a72", "a73", "a74", "a75", "a76", "a77", "a78", "a79", "a80", "a81", "a82", "a83", "a84", "a85", "a86", "a87", "a88", "a89", "a90", "a91", "a92", "a93", "a94", "a95", "a96", "a97", "a98", "a99", "a100", "a101", "a102", "a103", "a104", "a105", "a106", "a107", "a108", "a109", "a110", "a111", "a112", "a113", "a114", "a115", "a116", "a117", "a118", "a119", "a120", "a121", "a122", "a123", "a124", "a125", "a126", "a127", "a128", "a129", "a130", "a131", "a132", "a133", "a134", "a135", "a136", "a137", "a138", "a139", "a140", "a141", "a142", "a143", "a144", "a145", "a146", "a147", "a148", "a149", "a150", "a151", "a152", "a153", "a154", "a155", "a156", "a157", "a158", "a159", "a160", "a161", "a162", "a163", "a164", "a165", "a166", "a167", "a168", "a169", "a170", "a171", "a172", "a173", "a174", "a175", "a176", "a177", "a178", "a179", "a180", "a181", "a182", "a183", "a184", "a185", "a186", "a187", "a188", "a189", "a190", "a191", "a192", "a193", "a194", "a195", "a196", "a197", "a198", "a199", "a200", "a201", "a202", "a203", "a204", "a205", "a206", "a207", "a208", "a209", "a210", "a211", "a212", "a213", "a214", "a215", "a216", "a217", "a218", "a219", "a220", "a221", "a222", "a223", "s40", "s41", "s42", "s43", "s44", "s45", "s46", "s47", "s48", "s49", "s50", "s51", "s52", "s53", "s54", "s55", "s56", "s57", "s58", "s59", "s60", "s61", "vcc", "scc", "memory");
    return o;
}
DI unsigned gemm_asm1(const GCtx& c, u8v& w) {
    const u16v s0 = {RFL(c.rw1[0]), RFL(c.rw1[1]), RFL(c.rw1[2]), RFL(c.rw1[3]), RFL(c.rx[0]), RFL(c.rx[1]), RFL(c.rx[2]), RFL(c.rx[3]), RFL(c.rexp[0]), RFL(c.rexp[1]), RFL(c.rexp[2]), RFL(c.rexp[3]), RFL(c.rout[0]), RFL(c.rout[1]), RFL(c.rout[2]), RFL(c.rout[3])}, s1 = {RFL(c.rhb[0]), RFL(c.rhb[1]), RFL(c.rhb[2]), RFL(c.rhb[3]), RFL(c.tag), RFL(c.rot), RFL(c.sw1), RFL(c.mw), RFL(c.mx), RFL(c.sfg), RFL(c.stay), RFL(c.csc), RFL(c.ostr), RFL(c.kb), RFL(c.p1), RFL(c.p2)}, v0 = {c.vr0, c.vr1, c.vx, c.wa, c.xa, c.vexp, c.vimp, c.vo0, c.vo1, c.vo2, c.vo3, c.vfl, c.vpf, c.vpo, c.vk, c.p5};
    unsigned o = 0;
    asm volatile(
"s_nop 4\ns_add_u32 s40, s29, 0\ns_add_u32 s41, s29, 1\ns_add_u32 s42, s29, 2\n"
"s_add_u32 s43, s29, 3\nv_xor_b32 v24, 0, v11\nv_xor_b32 v25, 64, v11\nv_xor_b32 v26, 128, v11\n"
"v_xor_b32 v27, 192, v11\nv_xor_b32 v28, 2064, v11\nv_xor_b32 v29, 2128, v11\nv_xor_b32 v30, 2192, v11\n"
"v_xor_b32 v31, 2256, v11\nv_xor_b32 v40, 32, v22\nds_read_b64_tr_b16 a[128:129], v22 offset:0\nds_read_b64_tr_b16 a[130:131], v22 offset:512\n"
"ds_read_b64_tr_b16 a[132:133], v22 offset:4096\nds_read_b64_tr_b16 a[134:135], v22 offset:4608\nds_read_b64_tr_b16 a[136:137], v22 offset:8192\nds_read_b64_tr_b16 a[138:139], v22 offset:8704\n"
"ds_read_b64_tr_b16 a[140:141], v22 offset:12288\nds_read_b64_tr_b16 a[142:143], v22 offset:12800\ns_lshl_b32 s53, s41, 19\ns_add_u32 s53, s53, s30\n"
"s_add_u32 m0, s31, 65536\ns_add_u32 s53, s53, 0\nbuffer_load_dwordx4 v8, s[8:11], s53 offen lds\nbuffer_load_dwordx4 v9, s[8:11], s53 offen offset:1024 lds\n"
"s_waitcnt lgkmcnt(0)\nds_read_b64_tr_b16 a[144:145], v22 offset:16384\nds_read_b64_tr_b16 a[146:147], v22 offset:16896\nds_read_b64_tr_b16 a[148:149], v22 offset:20480\n"
"ds_read_b64_tr_b16 a[150:151], v22 offset:20992\nds_read_b64_tr_b16 a[152:153], v22 offset:24576\nds_read_b64_tr_b16 a[154:155], v22 offset:25088\nds_read_b64_tr_b16 a[156:157], v22 offset:28672\n"
"ds_read_b64_tr_b16 a[158:159], v22 offset:29184\ns_lshl_b32 s53, s41, 19\ns_add_u32 s53, s53, s30\ns_add_u32 m0, s31, 73728\n"
"s_add_u32 s53, s53, 131072\nbuffer_load_dwordx4 v8, s[8:11], s53 offen lds\nbuffer_load_dwordx4 v9, s[8:11], s53 offen offset:1024 lds\ns_waitcnt lgkmcnt(0)\n"
"ds_read_b64_tr_b16 a[160:161], v40 offset:0\nds_read_b64_tr_b16 a[162:163], v40 offset:512\nds_read_b64_tr_b16 a[164:165], v40 offset:4096\nds_read_b64_tr_b16 a[166:167], v40 offset:4608\n"
"ds_read_b64_tr_b16 a[168:169], v40 offset:8192\nds_read_b64_tr_b16 a[170:171], v40 offset:8704\nds_read_b64_tr_b16 a[172:173], v40 offset:12288\nds_read_b64_tr_b16 a[174:175], v40 offset:12800\n"
"s_lshl_b32 s53, s41, 19\ns_add_u32 s53, s53, s30\ns_add_u32 m0, s31, 81920\ns_add_u32 s53, s53, 262144\n"
"buffer_load_dwordx4 v8, s[8:11], s53 offen lds\nbuffer_load_dwordx4 v9, s[8:11], s53 offen offset:1024 lds\ns_waitcnt lgkmcnt(0)\nds_read_b64_tr_b16 a[176:177], v40 offset:16384\n"
"ds_read_b64_tr_b16 a[178:179], v40 offset:16896\nds_read_b64_tr_b16 a[180:181], v40 offset:20480\nds_read_b64_tr_b16 a[182:183], v40 offset:20992\nds_read_b64_tr_b16 a[184:185], v40 offset:24576\n"
"ds_read_b64_tr_b16 a[186:187], v40 offset:25088\nds_read_b64_tr_b16 a[188:189], v40 offset:28672\nds_read_b64_tr_b16 a[190:191], v40 offset:29184\ns_lshl_b32 s53, s41, 19\n"
"s_add_u32 s53, s53, s30\ns_add_u32 m0, s31, 90112\ns_add_u32 s53, s53, 393216\nbuffer_load_dwordx4 v8, s[8:11], s53 offen lds\n"
"buffer_load_dwordx4 v9, s[8:11], s53 offen offset:1024 lds\ns_waitcnt lgkmcnt(0)\ns_waitcnt lgkmcnt(0)\nv_add_u32 v40, 65536, v24\n"
"v_add_u32 v41, 65536, v25\nv_add_u32 v42, 65536, v26\nv_add_u32 v43, 65536, v27\nv_add_u32 v44, 65536, v28\n"
"v_add_u32 v45, 65536, v29\nv_add_u32 v46, 65536, v30\nv_add_u32 v47, 65536, v31\nv_xor_b32 v32, 0, v12\n"
"v_xor_b32 v33, 512, v12\nv_xor_b32 v34, 64, v12\nv_xor_b32 v35, 576, v12\ns_mov_b32 s55, 0\n"
"s_waitcnt vmcnt(11)\ns_barrier\nds_read_b128 v[96:99], v24 offset:0\nds_read_b128 v[100:103], v28 offset:0\n"
"ds_read_b128 v[104:107], v25 offset:0\nds_read_b128 v[108:111], v29 offset:0\nds_read_b128 v[112:115], v26 offset:0\nds_read_b128 v[116:119], v30 offset:0\n"
"ds_read_b128 v[120:123], v27 offset:0\nds_read_b128 v[124:127], v31 offset:0\ns_waitcnt lgkmcnt(6)\nv_mfma_f32_16x16x32_bf16 v[64:67], v[96:99], a[128:131], 0\n"
"s_lshl_b32 s53, s42, 19\ns_add_u32 s53, s53, s30\nv_mfma_f32_16x16x32_bf16 v[68:71], v[96:99], a[160:163], 0\nds_read_b128 v[96:99], v24 offset:256\n"
"s_add_u32 m0, s31, 98304\ns_add_u32 s53, s53, 0\nv_mfma_f32_16x16x32_bf16 v[72:75], v[100:103], a[128:131], 0\nbuffer_load_dwordx4 v8, s[8:11], s53 offen lds\n"
"v_mfma_f32_16x16x32_bf16 v[76:79], v[100:103], a[160:163], 0\nds_read_b128 v[100:103], v28 offset:256\nbuffer_load_dwordx4 v9, s[8:11], s53 offen offset:1024 lds\ns_waitcnt lgkmcnt(6)\n"
"v_mfma_f32_16x16x32_bf16 v[64:67], v[104:107], a[132:135], v[64:67]\ns_lshl_b32 s53, s42, 19\ns_add_u32 s53, s53, s30\nv_mfma_f32_16x16x32_bf16 v[68:71], v[104:107], a[164:167], v[68:71]\n"
"ds_read_b128 v[104:107], v25 offset:256\ns_add_u32 m0, s31, 106496\ns_add_u32 s53, s53, 131072\nv_mfma_f32_16x16x32_bf16 v[72:75], v[108:111], a[132:135], v[72:75]\n"
"buffer_load_dwordx4 v8, s[8:11], s53 offen lds\nv_mfma_f32_16x16x32_bf16 v[76:79], v[108:111], a[164:167], v[76:79]\nds_read_b128 v[108:111], v29 offset:256\nbuffer_load_dwordx4 v9, s[8:11], s53 offen offset:1024 lds\n"
"s_waitcnt lgkmcnt(6)\nv_mfma_f32_16x16x32_bf16 v[64:67], v[112:115], a[136:139], v[64:67]\nv_mfma_f32_16x16x32_bf16 v[68:71], v[112:115], a[168:171], v[68:71]\nds_read_b128 v[112:115], v26 offset:256\n"
"v_mfma_f32_16x16x32_bf16 v[72:75], v[116:119], a[136:139], v[72:75]\nv_mfma_f32_16x16x32_bf16 v[76:79], v[116:119], a[168:171], v[76:79]\nds_read_b128 v[116:119], v30 offset:256\ns_waitcnt lgkmcnt(6)\n"
"v_mfma_f32_16x16x32_bf16 v[64:67], v[120:123], a[140:143], v[64:67]\nv_mfma_f32_16x16x32_bf16 v[68:71], v[120:123], a[172:175], v[68:71]\nds_read_b128 v[120:123], v27 offset:256\nv_mfma_f32_16x16x32_bf16 v[72:75], v[124:127], a[140:143], v[72:75]\n"
"v_mfma_f32_16x16x32_bf16 v[76:79], v[124:127], a[172:175], v[76:79]\nds_read_b128 v[124:127], v31 offset:256\ns_waitcnt lgkmcnt(6)\nv_mfma_f32_16x16x32_bf16 v[64:67], v[96:99], a[144:147], v[64:67]\n"
"v_mfma_f32_16x16x32_bf16 v[68:71], v[96:99], a[176:179], v[68:71]\nds_read_b128 v[96:99], v24 offset:16384\nv_mfma_f32_16x16x32_bf16 v[72:75], v[100:103], a[144:147], v[72:75]\nv_mfma_f32_16x16x32_bf16 v[76:79], v[100:103], a[176:179], v[76:79]\n"
"ds_read_b128 v[100:103], v28 offset:16384\ns_waitcnt lgkmcnt(6)\nv_mfma_f32_16x16x32_bf16 v[64:67], v[104:107], a[148:151], v[64:67]\nv_mfma_f32_16x16x32_bf16 v[68:71], v[104:107], a[180:183], v[68:71]\n"
"ds_read_b128 v[104:107], v25 offset:16384\nv_mfma_f32_16x16x32_bf16 v[72:75], v[108:111], a[148:151], v[72:75]\nv_mfma_f32_16x16x32_bf16 v[76:79], v[108:111], a[180:183], v[76:79]\nds_read_b128 v[108:111], v29 offset:16384\n"
"s_waitcnt lgkmcnt(6)\nv_mfma_f32_16x16x32_bf16 v[64:67], v[112:115], a[152:155], v[64:67]\nv_mfma_f32_16x16x32_bf16 v[68:71], v[112:115], a[184:187], v[68:71]\nds_read_b128 v[112:115], v26 offset:16384\n"
"v_mfma_f32_16x16x32_bf16 v[72:75], v[116:119], a[152:155], v[72:75]\nv_mfma_f32_16x16x32_bf16 v[76:79], v[116:119], a[184:187], v[76:79]\nds_read_b128 v[116:119], v30 offset:16384\ns_waitcnt lgkmcnt(6)\n"
"v_mfma_f32_16x16x32_bf16 v[64:67], v[120:123], a[156:159], v[64:67]\nv_mfma_f32_16x16x32_bf16 v[68:71], v[120:123], a[188:191], v[68:71]\nds_read_b128 v[120:123], v27 offset:16384\nv_mfma_f32_16x16x32_bf16 v[72:75], v[124:127], a[156:159], v[72:75]\n"
"v_mfma_f32_16x16x32_bf16 v[76:79], v[124:127], a[188:191], v[76:79]\nds_read_b128 v[124:127], v31 offset:16384\ns_waitcnt lgkmcnt(6)\nv_mfma_f32_16x16x32_bf16 v[80:83], v[96:99], a[128:131], 0\n"
"s_lshl_b32 s53, s42, 19\ns_add_u32 s53, s53, s30\nv_mfma_f32_16x16x32_bf16 v[84:87], v[96:99], a[160:163], 0\nds_read_b128 v[96:99], v24 offset:16640\n"
"s_add_u32 m0, s31, 114688\ns_add_u32 s53, s53, 262144\nv_mfma_f32_16x16x32_bf16 v[88:91], v[100:103], a[128:131], 0\nbuffer_load_dwordx4 v8, s[8:11], s53 offen lds\n"
"v_mfma_f32_16x16x32_bf16 v[92:95], v[100:103], a[160:163], 0\nds_read_b128 v[100:103], v28 offset:16640\nbuffer_load_dwordx4 v9, s[8:11], s53 offen offset:1024 lds\ns_waitcnt lgkmcnt(6)\n"
"v_mfma_f32_16x16x32_bf16 v[80:83], v[104:107], a[132:135], v[80:83]\nv_cvt_pk_bf16_f32 v128, v64, v65\nv_cvt_pk_bf16_f32 v129, v66, v67\nv_mfma_f32_16x16x32_bf16 v[84:87], v[104:107], a[164:167], v[84:87]\n"
"ds_read_b128 v[104:107], v25 offset:16640\nv_cvt_pk_bf16_f32 v130, v72, v73\nv_cvt_pk_bf16_f32 v131, v74, v75\nv_mfma_f32_16x16x32_bf16 v[88:91], v[108:111], a[132:135], v[88:91]\n"
"v_cvt_pk_bf16_f32 v132, v68, v69\nv_cvt_pk_bf16_f32 v133, v70, v71\nv_mfma_f32_16x16x32_bf16 v[92:95], v[108:111], a[164:167], v[92:95]\nds_read_b128 v[108:111], v29 offset:16640\n"
"v_cvt_pk_bf16_f32 v134, v76, v77\nv_cvt_pk_bf16_f32 v135, v78, v79\ns_waitcnt lgkmcnt(6)\nv_mfma_f32_16x16x32_bf16 v[80:83], v[112:115], a[136:139], v[80:83]\n"
"s_lshl_b32 s54, s40, 11\ns_add_u32 s54, s54, 0\nv_mfma_f32_16x16x32_bf16 v[84:87], v[112:115], a[168:171], v[84:87]\nds_read_b128 v[112:115], v26 offset:16640\n"
"buffer_store_dwordx4 v[128:131], v13, s[16:19], s54 offen sc1\nv_mfma_f32_16x16x32_bf16 v[88:91], v[116:119], a[136:139], v[88:91]\ns_add_u32 s54, s54, 262144\nv_mfma_f32_16x16x32_bf16 v[92:95], v[116:119], a[168:171], v[92:95]\n"
"ds_read_b128 v[116:119], v30 offset:16640\nbuffer_store_dwordx4 v[132:135], v13, s[16:19], s54 offen sc1\ns_waitcnt lgkmcnt(6)\nv_mfma_f32_16x16x32_bf16 v[80:83], v[120:123], a[140:143], v[80:83]\n"
"s_lshl_b32 s53, s42, 19\ns_add_u32 s53, s53, s30\nv_mfma_f32_16x16x32_bf16 v[84:87], v[120:123], a[172:175], v[84:87]\nds_read_b128 v[120:123], v27 offset:16640\n"
"s_add_u32 m0, s31, 122880\ns_add_u32 s53, s53, 393216\nv_mfma_f32_16x16x32_bf16 v[88:91], v[124:127], a[140:143], v[88:91]\nbuffer_load_dwordx4 v8, s[8:11], s53 offen lds\n"
"v_mfma_f32_16x16x32_bf16 v[92:95], v[124:127], a[172:175], v[92:95]\nds_read_b128 v[124:127], v31 offset:16640\nbuffer_load_dwordx4 v9, s[8:11], s53 offen offset:1024 lds\ns_waitcnt vmcnt(10)\n"
"s_barrier\ns_waitcnt lgkmcnt(6)\nv_mfma_f32_16x16x32_bf16 v[80:83], v[96:99], a[144:147], v[80:83]\nv_mfma_f32_16x16x32_bf16 v[84:87], v[96:99], a[176:179], v[84:87]\n"
"ds_read_b128 v[96:99], v24 offset:32768\nv_mfma_f32_16x16x32_bf16 v[88:91], v[100:103], a[144:147], v[88:91]\nv_mfma_f32_16x16x32_bf16 v[92:95], v[100:103], a[176:179], v[92:95]\nds_read_b128 v[100:103], v28 offset:32768\n"
"s_waitcnt lgkmcnt(6)\nv_mfma_f32_16x16x32_bf16 v[80:83], v[104:107], a[148:151], v[80:83]\nv_mfma_f32_16x16x32_bf16 v[84:87], v[104:107], a[180:183], v[84:87]\nds_read_b128 v[104:107], v25 offset:32768\n"
"v_mfma_f32_16x16x32_bf16 v[88:91], v[108:111], a[148:151], v[88:91]\nv_mfma_f32_16x16x32_bf16 v[92:95], v[108:111], a[180:183], v[92:95]\nds_read_b128 v[108:111], v29 offset:32768\ns_waitcnt lgkmcnt(6)\n"
"v_mfma_f32_16x16x32_bf16 v[80:83], v[112:115], a[152:155], v[80:83]\nv_mfma_f32_16x16x32_bf16 v[84:87], v[112:115], a[184:187], v[84:87]\nds_read_b128 v[112:115], v26 offset:32768\nv_mfma_f32_16x16x32_bf16 v[88:91], v[116:119], a[152:155], v[88:91]\n"
"v_mfma_f32_16x16x32_bf16 v[92:95], v[116:119], a[184:187], v[92:95]\nds_read_b128 v[116:119], v30 offset:32768\ns_waitcnt lgkmcnt(6)\nv_mfma_f32_16x16x32_bf16 v[80:83], v[120:123], a[156:159], v[80:83]\n"
"v_mfma_f32_16x16x32_bf16 v[84:87], v[120:123], a[188:191], v[84:87]\nds_read_b128 v[120:123], v27 offset:32768\nv_mfma_f32_16x16x32_bf16 v[88:91], v[124:127], a[156:159], v[88:91]\nv_mfma_f32_16x16x32_bf16 v[92:95], v[124:127], a[188:191], v[92:95]\n"
"ds_read_b128 v[124:127], v31 offset:32768\ns_waitcnt lgkmcnt(6)\nv_mfma_f32_16x16x32_bf16 v[64:67], v[96:99], a[128:131], 0\ns_lshl_b32 s53, s43, 19\n"
"s_add_u32 s53, s53, s30\nv_mfma_f32_16x16x32_bf16 v[68:71], v[96:99], a[160:163], 0\nds_read_b128 v[96:99], v24 offset:33024\ns_add_u32 m0, s31, 131072\n"
"s_add_u32 s53, s53, 0\nv_mfma_f32_16x16x32_bf16 v[72:75], v[100:103], a[128:131], 0\nbuffer_load_dwordx4 v8, s[8:11], s53 offen lds\nv_mfma_f32_16x16x32_bf16 v[76:79], v[100:103], a[160:163], 0\n"
"ds_read_b128 v[100:103], v28 offset:33024\nbuffer_load_dwordx4 v9, s[8:11], s53 offen offset:1024 lds\ns_waitcnt lgkmcnt(6)\nv_mfma_f32_16x16x32_bf16 v[64:67], v[104:107], a[132:135], v[64:67]\n"
"v_cvt_pk_bf16_f32 v136, v80, v81\nv_cvt_pk_bf16_f32 v137, v82, v83\nv_mfma_f32_16x16x32_bf16 v[68:71], v[104:107], a[164:167], v[68:71]\nds_read_b128 v[104:107], v25 offset:33024\n"
"v_cvt_pk_bf16_f32 v138, v88, v89\nv_cvt_pk_bf16_f32 v139, v90, v91\nv_mfma_f32_16x16x32_bf16 v[72:75], v[108:111], a[132:135], v[72:75]\nv_cvt_pk_bf16_f32 v140, v84, v85\n"
"v_cvt_pk_bf16_f32 v141, v86, v87\nv_mfma_f32_16x16x32_bf16 v[76:79], v[108:111], a[164:167], v[76:79]\nds_read_b128 v[108:111], v29 offset:33024\nv_cvt_pk_bf16_f32 v142, v92, v93\n"
"v_cvt_pk_bf16_f32 v143, v94, v95\ns_waitcnt lgkmcnt(6)\nv_mfma_f32_16x16x32_bf16 v[64:67], v[112:115], a[136:139], v[64:67]\ns_lshl_b32 s54, s40, 11\n"
"s_add_u32 s54, s54, 1024\nv_mfma_f32_16x16x32_bf16 v[68:71], v[112:115], a[168:171], v[68:71]\nds_read_b128 v[112:115], v26 offset:33024\nbuffer_store_dwordx4 v[136:139], v13, s[16:19], s54 offen sc1\n"
"v_mfma_f32_16x16x32_bf16 v[72:75], v[116:119], a[136:139], v[72:75]\ns_add_u32 s54, s54, 262144\nv_mfma_f32_16x16x32_bf16 v[76:79], v[116:119], a[168:171], v[76:79]\nds_read_b128 v[116:119], v30 offset:33024\n"
"buffer_store_dwordx4 v[140:143], v13, s[16:19], s54 offen sc1\ns_waitcnt lgkmcnt(6)\nv_mfma_f32_16x16x32_bf16 v[64:67], v[120:123], a[140:143], v[64:67]\ns_lshl_b32 s53, s43, 19\n"
"s_add_u32 s53, s53, s30\nv_mfma_f32_16x16x32_bf16 v[68:71], v[120:123], a[172:175], v[68:71]\nds_read_b128 v[120:123], v27 offset:33024\ns_add_u32 m0, s31, 139264\n"
"s_add_u32 s53, s53, 131072\nv_mfma_f32_16x16x32_bf16 v[72:75], v[124:127], a[140:143], v[72:75]\nbuffer_load_dwordx4 v8, s[8:11], s53 offen lds\nv_mfma_f32_16x16x32_bf16 v[76:79], v[124:127], a[172:175], v[76:79]\n"
"ds_read_b128 v[124:127], v31 offset:33024\nbuffer_load_dwordx4 v9, s[8:11], s53 offen offset:1024 lds\ns_waitcnt lgkmcnt(6)\nv_mfma_f32_16x16x32_bf16 v[64:67], v[96:99], a[144:147], v[64:67]\n"
"v_mfma_f32_16x16x32_bf16 v[68:71], v[96:99], a[176:179], v[68:71]\nds_read_b128 v[96:99], v24 offset:49152\nv_mfma_f32_16x16x32_bf16 v[72:75], v[100:103], a[144:147], v[72:75]\nv_mfma_f32_16x16x32_bf16 v[76:79], v[100:103], a[176:179], v[76:79]\n"
"ds_read_b128 v[100:103], v28 offset:49152\ns_waitcnt lgkmcnt(6)\nv_mfma_f32_16x16x32_bf16 v[64:67], v[104:107], a[148:151], v[64:67]\nv_mfma_f32_16x16x32_bf16 v[68:71], v[104:107], a[180:183], v[68:71]\n"
"ds_read_b128 v[104:107], v25 offset:49152\nv_mfma_f32_16x16x32_bf16 v[72:75], v[108:111], a[148:151], v[72:75]\nv_mfma_f32_16x16x32_bf16 v[76:79], v[108:111], a[180:183], v[76:79]\nds_read_b128 v[108:111], v29 offset:49152\n"
"s_waitcnt lgkmcnt(6)\nv_mfma_f32_16x16x32_bf16 v[64:67], v[112:115], a[152:155], v[64:67]\nv_mfma_f32_16x16x32_bf16 v[68:71], v[112:115], a[184:187], v[68:71]\nds_read_b128 v[112:115], v26 offset:49152\n"
"v_mfma_f32_16x16x32_bf16 v[72:75], v[116:119], a[152:155], v[72:75]\nv_mfma_f32_16x16x32_bf16 v[76:79], v[116:119], a[184:187], v[76:79]\nds_read_b128 v[116:119], v30 offset:49152\ns_waitcnt lgkmcnt(6)\n"
"v_mfma_f32_16x16x32_bf16 v[64:67], v[120:123], a[156:159], v[64:67]\nv_mfma_f32_16x16x32_bf16 v[68:71], v[120:123], a[188:191], v[68:71]\nds_read_b128 v[120:123], v27 offset:49152\nv_mfma_f32_16x16x32_bf16 v[72:75], v[124:127], a[156:159], v[72:75]\n"
"v_mfma_f32_16x16x32_bf16 v[76:79], v[124:127], a[188:191], v[76:79]\nds_read_b128 v[124:127], v31 offset:49152\ns_waitcnt lgkmcnt(6)\nv_mfma_f32_16x16x32_bf16 v[80:83], v[96:99], a[128:131], 0\n"
"s_lshl_b32 s53, s43, 19\ns_add_u32 s53, s53, s30\nv_mfma_f32_16x16x32_bf16 v[84:87], v[96:99], a[160:163], 0\nds_read_b128 v[96:99], v24 offset:49408\n"
"s_add_u32 m0, s31, 147456\ns_add_u32 s53, s53, 262144\nv_mfma_f32_16x16x32_bf16 v[88:91], v[100:103], a[128:131], 0\nbuffer_load_dwordx4 v8, s[8:11], s53 offen lds\n"
"v_mfma_f32_16x16x32_bf16 v[92:95], v[100:103], a[160:163], 0\nds_read_b128 v[100:103], v28 offset:49408\nbuffer_load_dwordx4 v9, s[8:11], s53 offen offset:1024 lds\ns_waitcnt lgkmcnt(6)\n"
"v_mfma_f32_16x16x32_bf16 v[80:83], v[104:107], a[132:135], v[80:83]\nv_cvt_pk_bf16_f32 v144, v64, v65\nv_cvt_pk_bf16_f32 v145, v66, v67\nv_mfma_f32_16x16x32_bf16 v[84:87], v[104:107], a[164:167], v[84:87]\n"
"ds_read_b128 v[104:107], v25 offset:49408\nv_cvt_pk_bf16_f32 v146, v72, v73\nv_cvt_pk_bf16_f32 v147, v74, v75\nv_mfma_f32_16x16x32_bf16 v[88:91], v[108:111], a[132:135], v[88:91]\n"
"v_cvt_pk_bf16_f32 v148, v68, v69\nv_cvt_pk_bf16_f32 v149, v70, v71\nv_mfma_f32_16x16x32_bf16 v[92:95], v[108:111], a[164:167], v[92:95]\nds_read_b128 v[108:111], v29 offset:49408\n"
"v_cvt_pk_bf16_f32 v150, v76, v77\nv_cvt_pk_bf16_f32 v151, v78, v79\ns_waitcnt lgkmcnt(6)\nv_mfma_f32_16x16x32_bf16 v[80:83], v[112:115], a[136:139], v[80:83]\n"
"s_lshl_b32 s54, s41, 11\ns_add_u32 s54, s54, 0\nv_mfma_f32_16x16x32_bf16 v[84:87], v[112:115], a[168:171], v[84:87]\nds_read_b128 v[112:115], v26 offset:49408\n"
"buffer_store_dwordx4 v[144:147], v13, s[16:19], s54 offen sc1\nv_mfma_f32_16x16x32_bf16 v[88:91], v[116:119], a[136:139], v[88:91]\ns_add_u32 s54, s54, 262144\nv_mfma_f32_16x16x32_bf16 v[92:95], v[116:119], a[168:171], v[92:95]\n"
"ds_read_b128 v[116:119], v30 offset:49408\nbuffer_store_dwordx4 v[148:151], v13, s[16:19], s54 offen sc1\ns_waitcnt lgkmcnt(6)\nv_mfma_f32_16x16x32_bf16 v[80:83], v[120:123], a[140:143], v[80:83]\n"
"s_lshl_b32 s53, s43, 19\ns_add_u32 s53, s53, s30\nv_mfma_f32_16x16x32_bf16 v[84:87], v[120:123], a[172:175], v[84:87]\nds_read_b128 v[120:123], v27 offset:49408\n"
"s_add_u32 m0, s31, 155648\ns_add_u32 s53, s53, 393216\nv_mfma_f32_16x16x32_bf16 v[88:91], v[124:127], a[140:143], v[88:91]\nbuffer_load_dwordx4 v8, s[8:11], s53 offen lds\n"
"v_mfma_f32_16x16x32_bf16 v[92:95], v[124:127], a[172:175], v[92:95]\nds_read_b128 v[124:127], v31 offset:49408\nbuffer_load_dwordx4 v9, s[8:11], s53 offen offset:1024 lds\ns_waitcnt vmcnt(12)\n"
"s_barrier\ns_waitcnt lgkmcnt(6)\nv_mfma_f32_16x16x32_bf16 v[80:83], v[96:99], a[144:147], v[80:83]\nv_mfma_f32_16x16x32_bf16 v[84:87], v[96:99], a[176:179], v[84:87]\n"
"ds_read_b128 v[96:99], v40 offset:0\nv_mfma_f32_16x16x32_bf16 v[88:91], v[100:103], a[144:147], v[88:91]\nv_mfma_f32_16x16x32_bf16 v[92:95], v[100:103], a[176:179], v[92:95]\nds_read_b128 v[100:103], v44 offset:0\n"
"s_waitcnt lgkmcnt(6)\nv_mfma_f32_16x16x32_bf16 v[80:83], v[104:107], a[148:151], v[80:83]\nv_mfma_f32_16x16x32_bf16 v[84:87], v[104:107], a[180:183], v[84:87]\nds_read_b128 v[104:107], v41 offset:0\n"
"v_mfma_f32_16x16x32_bf16 v[88:91], v[108:111], a[148:151], v[88:91]\nv_mfma_f32_16x16x32_bf16 v[92:95], v[108:111], a[180:183], v[92:95]\nds_read_b128 v[108:111], v45 offset:0\ns_waitcnt lgkmcnt(6)\n"
"v_mfma_f32_16x16x32_bf16 v[80:83], v[112:115], a[152:155], v[80:83]\nv_mfma_f32_16x16x32_bf16 v[84:87], v[112:115], a[184:187], v[84:87]\nds_read_b128 v[112:115], v42 offset:0\nv_mfma_f32_16x16x32_bf16 v[88:91], v[116:119], a[152:155], v[88:91]\n"
"v_mfma_f32_16x16x32_bf16 v[92:95], v[116:119], a[184:187], v[92:95]\nds_read_b128 v[116:119], v46 offset:0\ns_waitcnt lgkmcnt(6)\nv_mfma_f32_16x16x32_bf16 v[80:83], v[120:123], a[156:159], v[80:83]\n"
"v_mfma_f32_16x16x32_bf16 v[84:87], v[120:123], a[188:191], v[84:87]\nds_read_b128 v[120:123], v43 offset:0\nv_mfma_f32_16x16x32_bf16 v[88:91], v[124:127], a[156:159], v[88:91]\nv_mfma_f32_16x16x32_bf16 v[92:95], v[124:127], a[188:191], v[92:95]\n"
"ds_read_b128 v[124:127], v47 offset:0\ns_waitcnt lgkmcnt(6)\nv_mfma_f32_16x16x32_bf16 v[64:67], v[96:99], a[128:131], 0\ns_add_u32 m0, s32, 0\n"
"v_mfma_f32_16x16x32_bf16 v[68:71], v[96:99], a[160:163], 0\nds_read_b128 v[96:99], v40 offset:256\ns_mov_b32 s53, 0\nv_mfma_f32_16x16x32_bf16 v[72:75], v[100:103], a[128:131], 0\n"
"buffer_load_dwordx4 v10, s[12:15], s53 offen lds\nv_mfma_f32_16x16x32_bf16 v[76:79], v[100:103], a[160:163], 0\nds_read_b128 v[100:103], v44 offset:256\nv_cvt_pk_bf16_f32 v152, v80, v81\n"
"v_cvt_pk_bf16_f32 v153, v82, v83\ns_waitcnt lgkmcnt(6)\nv_mfma_f32_16x16x32_bf16 v[64:67], v[104:107], a[132:135], v[64:67]\nv_cvt_pk_bf16_f32 v154, v88, v89\n"
"v_cvt_pk_bf16_f32 v155, v90, v91\nv_mfma_f32_16x16x32_bf16 v[68:71], v[104:107], a[164:167], v[68:71]\nds_read_b128 v[104:107], v41 offset:256\nv_cvt_pk_bf16_f32 v156, v84, v85\n"
"v_cvt_pk_bf16_f32 v157, v86, v87\nv_mfma_f32_16x16x32_bf16 v[72:75], v[108:111], a[132:135], v[72:75]\nv_cvt_pk_bf16_f32 v158, v92, v93\nv_cvt_pk_bf16_f32 v159, v94, v95\n"
"v_mfma_f32_16x16x32_bf16 v[76:79], v[108:111], a[164:167], v[76:79]\nds_read_b128 v[108:111], v45 offset:256\ns_lshl_b32 s54, s41, 11\ns_add_u32 s54, s54, 1024\n"
"s_waitcnt lgkmcnt(6)\nv_mfma_f32_16x16x32_bf16 v[64:67], v[112:115], a[136:139], v[64:67]\nbuffer_store_dwordx4 v[152:155], v13, s[16:19], s54 offen sc1\nv_mfma_f32_16x16x32_bf16 v[68:71], v[112:115], a[168:171], v[68:71]\n"
"ds_read_b128 v[112:115], v42 offset:256\ns_add_u32 s54, s54, 262144\nv_mfma_f32_16x16x32_bf16 v[72:75], v[116:119], a[136:139], v[72:75]\nbuffer_store_dwordx4 v[156:159], v13, s[16:19], s54 offen sc1\n"
"v_mfma_f32_16x16x32_bf16 v[76:79], v[116:119], a[168:171], v[76:79]\nds_read_b128 v[116:119], v46 offset:256\ns_add_u32 m0, s32, 4096\ns_waitcnt lgkmcnt(6)\n"
"v_mfma_f32_16x16x32_bf16 v[64:67], v[120:123], a[140:143], v[64:67]\ns_mov_b32 s53, 128\nv_mfma_f32_16x16x32_bf16 v[68:71], v[120:123], a[172:175], v[68:71]\nds_read_b128 v[120:123], v43 offset:256\n"
"buffer_load_dwordx4 v10, s[12:15], s53 offen lds\nv_mfma_f32_16x16x32_bf16 v[72:75], v[124:127], a[140:143], v[72:75]\ns_add_u32 m0, s32, 8192\nv_mfma_f32_16x16x32_bf16 v[76:79], v[124:127], a[172:175], v[76:79]\n"
"ds_read_b128 v[124:127], v47 offset:256\ns_mov_b32 s53, 256\ns_waitcnt lgkmcnt(6)\nv_mfma_f32_16x16x32_bf16 v[64:67], v[96:99], a[144:147], v[64:67]\n"
"buffer_load_dwordx4 v10, s[12:15], s53 offen lds\nv_mfma_f32_16x16x32_bf16 v[68:71], v[96:99], a[176:179], v[68:71]\nds_read_b128 v[96:99], v40 offset:16384\ns_add_u32 m0, s32, 12288\n"
"v_mfma_f32_16x16x32_bf16 v[72:75], v[100:103], a[144:147], v[72:75]\ns_mov_b32 s53, 384\nv_mfma_f32_16x16x32_bf16 v[76:79], v[100:103], a[176:179], v[76:79]\nds_read_b128 v[100:103], v44 offset:16384\n"
"buffer_load_dwordx4 v10, s[12:15], s53 offen lds\ns_waitcnt lgkmcnt(6)\nv_mfma_f32_16x16x32_bf16 v[64:67], v[104:107], a[148:151], v[64:67]\nv_mfma_f32_16x16x32_bf16 v[68:71], v[104:107], a[180:183], v[68:71]\n"
"ds_read_b128 v[104:107], v41 offset:16384\nv_mfma_f32_16x16x32_bf16 v[72:75], v[108:111], a[148:151], v[72:75]\nv_mfma_f32_16x16x32_bf16 v[76:79], v[108:111], a[180:183], v[76:79]\nds_read_b128 v[108:111], v45 offset:16384\n"
"s_waitcnt lgkmcnt(6)\nv_mfma_f32_16x16x32_bf16 v[64:67], v[112:115], a[152:155], v[64:67]\nv_mfma_f32_16x16x32_bf16 v[68:71], v[112:115], a[184:187], v[68:71]\nds_read_b128 v[112:115], v42 offset:16384\n"
"v_mfma_f32_16x16x32_bf16 v[72:75], v[116:119], a[152:155], v[72:75]\nv_mfma_f32_16x16x32_bf16 v[76:79], v[116:119], a[184:187], v[76:79]\nds_read_b128 v[116:119], v46 offset:16384\ns_waitcnt lgkmcnt(6)\n"
"v_mfma_f32_16x16x32_bf16 v[64:67], v[120:123], a[156:159], v[64:67]\nv_mfma_f32_16x16x32_bf16 v[68:71], v[120:123], a[188:191], v[68:71]\nds_read_b128 v[120:123], v43 offset:16384\nv_mfma_f32_16x16x32_bf16 v[72:75], v[124:127], a[156:159], v[72:75]\n"
"v_mfma_f32_16x16x32_bf16 v[76:79], v[124:127], a[188:191], v[76:79]\nds_read_b128 v[124:127], v47 offset:16384\ns_waitcnt lgkmcnt(6)\nv_mfma_f32_16x16x32_bf16 v[80:83], v[96:99], a[128:131], 0\n"
"s_add_u32 m0, s32, 16384\nv_mfma_f32_16x16x32_bf16 v[84:87], v[96:99], a[160:163], 0\nds_read_b128 v[96:99], v40 offset:16640\ns_mov_b32 s53, 512\n"
"v_mfma_f32_16x16x32_bf16 v[88:91], v[100:103], a[128:131], 0\nbuffer_load_dwordx4 v10, s[12:15], s53 offen lds\nv_mfma_f32_16x16x32_bf16 v[92:95], v[100:103], a[160:163], 0\nds_read_b128 v[100:103], v44 offset:16640\n"
"v_cvt_pk_bf16_f32 v160, v64, v65\nv_cvt_pk_bf16_f32 v161, v66, v67\ns_waitcnt lgkmcnt(6)\nv_mfma_f32_16x16x32_bf16 v[80:83], v[104:107], a[132:135], v[80:83]\n"
"v_cvt_pk_bf16_f32 v162, v72, v73\nv_cvt_pk_bf16_f32 v163, v74, v75\nv_mfma_f32_16x16x32_bf16 v[84:87], v[104:107], a[164:167], v[84:87]\nds_read_b128 v[104:107], v41 offset:16640\n"
"v_cvt_pk_bf16_f32 v164, v68, v69\nv_cvt_pk_bf16_f32 v165, v70, v71\nv_mfma_f32_16x16x32_bf16 v[88:91], v[108:111], a[132:135], v[88:91]\nv_cvt_pk_bf16_f32 v166, v76, v77\n"
"v_cvt_pk_bf16_f32 v167, v78, v79\nv_mfma_f32_16x16x32_bf16 v[92:95], v[108:111], a[164:167], v[92:95]\nds_read_b128 v[108:111], v45 offset:16640\ns_lshl_b32 s54, s42, 11\n"
"s_add_u32 s54, s54, 0\ns_waitcnt lgkmcnt(6)\nv_mfma_f32_16x16x32_bf16 v[80:83], v[112:115], a[136:139], v[80:83]\nbuffer_store_dwordx4 v[160:163], v13, s[16:19], s54 offen sc1\n"
"v_mfma_f32_16x16x32_bf16 v[84:87], v[112:115], a[168:171], v[84:87]\nds_read_b128 v[112:115], v42 offset:16640\ns_add_u32 s54, s54, 262144\nv_mfma_f32_16x16x32_bf16 v[88:91], v[116:119], a[136:139], v[88:91]\n"
"buffer_store_dwordx4 v[164:167], v13, s[16:19], s54 offen sc1\nv_mfma_f32_16x16x32_bf16 v[92:95], v[116:119], a[168:171], v[92:95]\nds_read_b128 v[116:119], v46 offset:16640\ns_add_u32 m0, s32, 20480\n"
"s_waitcnt lgkmcnt(6)\nv_mfma_f32_16x16x32_bf16 v[80:83], v[120:123], a[140:143], v[80:83]\ns_mov_b32 s53, 640\nv_mfma_f32_16x16x32_bf16 v[84:87], v[120:123], a[172:175], v[84:87]\n"
"ds_read_b128 v[120:123], v43 offset:16640\nbuffer_load_dwordx4 v10, s[12:15], s53 offen lds\nv_mfma_f32_16x16x32_bf16 v[88:91], v[124:127], a[140:143], v[88:91]\ns_add_u32 m0, s32, 24576\n"
"v_mfma_f32_16x16x32_bf16 v[92:95], v[124:127], a[172:175], v[92:95]\nds_read_b128 v[124:127], v47 offset:16640\ns_mov_b32 s53, 768\ns_waitcnt vmcnt(10)\n"
"s_barrier\ns_waitcnt lgkmcnt(6)\nv_mfma_f32_16x16x32_bf16 v[80:83], v[96:99], a[144:147], v[80:83]\nbuffer_load_dwordx4 v10, s[12:15], s53 offen lds\n"
"v_mfma_f32_16x16x32_bf16 v[84:87], v[96:99], a[176:179], v[84:87]\nds_read_b128 v[96:99], v40 offset:32768\nv_mfma_f32_16x16x32_bf16 v[88:91], v[100:103], a[144:147], v[88:91]\nv_mfma_f32_16x16x32_bf16 v[92:95], v[100:103], a[176:179], v[92:95]\n"
"ds_read_b128 v[100:103], v44 offset:32768\ns_waitcnt lgkmcnt(6)\nv_mfma_f32_16x16x32_bf16 v[80:83], v[104:107], a[148:151], v[80:83]\nv_mfma_f32_16x16x32_bf16 v[84:87], v[104:107], a[180:183], v[84:87]\n"
"ds_read_b128 v[104:107], v41 offset:32768\nv_mfma_f32_16x16x32_bf16 v[88:91], v[108:111], a[148:151], v[88:91]\nv_mfma_f32_16x16x32_bf16 v[92:95], v[108:111], a[180:183], v[92:95]\nds_read_b128 v[108:111], v45 offset:32768\n"
"s_waitcnt lgkmcnt(6)\nv_mfma_f32_16x16x32_bf16 v[80:83], v[112:115], a[152:155], v[80:83]\nv_mfma_f32_16x16x32_bf16 v[84:87], v[112:115], a[184:187], v[84:87]\nds_read_b128 v[112:115], v42 offset:32768\n"
"v_mfma_f32_16x16x32_bf16 v[88:91], v[116:119], a[152:155], v[88:91]\nv_mfma_f32_16x16x32_bf16 v[92:95], v[116:119], a[184:187], v[92:95]\nds_read_b128 v[116:119], v46 offset:32768\ns_waitcnt lgkmcnt(6)\n"
"v_mfma_f32_16x16x32_bf16 v[80:83], v[120:123], a[156:159], v[80:83]\nv_mfma_f32_16x16x32_bf16 v[84:87], v[120:123], a[188:191], v[84:87]\nds_read_b128 v[120:123], v43 offset:32768\nv_mfma_f32_16x16x32_bf16 v[88:91], v[124:127], a[156:159], v[88:91]\n"
"v_mfma_f32_16x16x32_bf16 v[92:95], v[124:127], a[188:191], v[92:95]\nds_read_b128 v[124:127], v47 offset:32768\ns_cmp_lt_u32 s34, 2\ns_cbranch_scc1 fg1_main\n"
"s_nop 7\nv_cvt_pk_bf16_f32 v168, v80, v81\nv_cvt_pk_bf16_f32 v169, v82, v83\nv_cvt_pk_bf16_f32 v170, v88, v89\n"
"v_cvt_pk_bf16_f32 v171, v90, v91\nv_cvt_pk_bf16_f32 v172, v84, v85\nv_cvt_pk_bf16_f32 v173, v86, v87\nv_cvt_pk_bf16_f32 v174, v92, v93\n"
"v_cvt_pk_bf16_f32 v175, v94, v95\ns_lshl_b32 s54, s42, 11\ns_add_u32 s54, s54, 1024\nbuffer_store_dwordx4 v[168:171], v13, s[16:19], s54 offen sc1\n"
"s_add_u32 s54, s54, 262144\nbuffer_store_dwordx4 v[172:175], v13, s[16:19], s54 offen sc1\nbuffer_load_dword v246, v21, s[24:27], 0 offen sc1\nbuffer_load_dwordx2 v[244:245], v20, s[24:27], 0 offen sc1\n"
"buffer_load_dwordx2 v[248:249], v15, s[24:27], 0 offen sc1\nv_mov_b32 v36, s28\ns_waitcnt vmcnt(3) lgkmcnt(0)\ns_barrier\n"
"buffer_store_dword v36, v19, s[24:27], 0 offen sc1\ns_waitcnt vmcnt(1)\ns_cmp_eq_u32 s34, 2\ns_cbranch_scc1 fg1_end\n"
"fg1_pp:\nv_cmp_ne_u32 vcc, s28, v246\ns_cbranch_vccz fg1_ppok\ns_add_u32 s55, s55, 1\n"
"s_cmp_gt_u32 s55, 262144\ns_cbranch_scc0 fg1_pp2\ns_endpgm\nfg1_pp2:\n"
"s_sleep 1\nbuffer_load_dword v246, v21, s[24:27], 0 offen sc1\ns_waitcnt vmcnt(0)\ns_branch fg1_pp\n"
"fg1_ppok:\ns_and_b32 s54, s31, 0x1800\ns_lshl_b32 s54, s54, 1\ns_cmp_ge_u32 s54, 8192\n"
"s_cbranch_scc1 fg1_k0\ns_add_u32 s53, s54, s36\nbuffer_load_dwordx4 a[0:3], v14, s[16:19], s53 offen sc1\nbuffer_load_dwordx4 a[4:7], v14, s[16:19], s53 offen offset:1024 sc1\n"
"s_add_u32 s53, s53, 14336\nbuffer_load_dwordx4 a[56:59], v14, s[16:19], s53 offen sc1\nbuffer_load_dwordx4 a[60:63], v14, s[16:19], s53 offen offset:1024 sc1\ns_add_u32 s53, s53, 14336\n"
"fg1_k0:\ns_add_u32 s54, s54, 2048\ns_cmp_eq_u32 s54, 14336\ns_cselect_b32 s54, 0, s54\n"
"s_cmp_ge_u32 s54, 8192\ns_cbranch_scc1 fg1_k1\ns_add_u32 s53, s54, s36\nbuffer_load_dwordx4 a[8:11], v14, s[16:19], s53 offen sc1\n"
"buffer_load_dwordx4 a[12:15], v14, s[16:19], s53 offen offset:1024 sc1\ns_add_u32 s53, s53, 14336\nbuffer_load_dwordx4 a[64:67], v14, s[16:19], s53 offen sc1\nbuffer_load_dwordx4 a[68:71], v14, s[16:19], s53 offen offset:1024 sc1\n"
"s_add_u32 s53, s53, 14336\nfg1_k1:\ns_add_u32 s54, s54, 2048\ns_cmp_eq_u32 s54, 14336\n"
"s_cselect_b32 s54, 0, s54\ns_cmp_ge_u32 s54, 8192\ns_cbranch_scc1 fg1_k2\ns_add_u32 s53, s54, s36\n"
"buffer_load_dwordx4 a[16:19], v14, s[16:19], s53 offen sc1\nbuffer_load_dwordx4 a[20:23], v14, s[16:19], s53 offen offset:1024 sc1\ns_add_u32 s53, s53, 14336\nbuffer_load_dwordx4 a[72:75], v14, s[16:19], s53 offen sc1\n"
"buffer_load_dwordx4 a[76:79], v14, s[16:19], s53 offen offset:1024 sc1\ns_add_u32 s53, s53, 14336\nfg1_k2:\ns_add_u32 s54, s54, 2048\n"
"s_cmp_eq_u32 s54, 14336\ns_cselect_b32 s54, 0, s54\ns_cmp_ge_u32 s54, 8192\ns_cbranch_scc1 fg1_k3\n"
"s_add_u32 s53, s54, s36\nbuffer_load_dwordx4 a[24:27], v14, s[16:19], s53 offen sc1\nbuffer_load_dwordx4 a[28:31], v14, s[16:19], s53 offen offset:1024 sc1\ns_add_u32 s53, s53, 14336\n"
"buffer_load_dwordx4 a[80:83], v14, s[16:19], s53 offen sc1\nbuffer_load_dwordx4 a[84:87], v14, s[16:19], s53 offen offset:1024 sc1\ns_add_u32 s53, s53, 14336\nfg1_k3:\n"
"s_add_u32 s54, s54, 2048\ns_cmp_eq_u32 s54, 14336\ns_cselect_b32 s54, 0, s54\ns_cmp_ge_u32 s54, 8192\n"
"s_cbranch_scc1 fg1_k4\ns_add_u32 s53, s54, s36\nbuffer_load_dwordx4 a[32:35], v14, s[16:19], s53 offen sc1\nbuffer_load_dwordx4 a[36:39], v14, s[16:19], s53 offen offset:1024 sc1\n"
"s_add_u32 s53, s53, 14336\nbuffer_load_dwordx4 a[88:91], v14, s[16:19], s53 offen sc1\nbuffer_load_dwordx4 a[92:95], v14, s[16:19], s53 offen offset:1024 sc1\ns_add_u32 s53, s53, 14336\n"
"fg1_k4:\ns_add_u32 s54, s54, 2048\ns_cmp_eq_u32 s54, 14336\ns_cselect_b32 s54, 0, s54\n"
"s_cmp_ge_u32 s54, 8192\ns_cbranch_scc1 fg1_k5\ns_add_u32 s53, s54, s36\nbuffer_load_dwordx4 a[40:43], v14, s[16:19], s53 offen sc1\n"
"buffer_load_dwordx4 a[44:47], v14, s[16:19], s53 offen offset:1024 sc1\ns_add_u32 s53, s53, 14336\nbuffer_load_dwordx4 a[96:99], v14, s[16:19], s53 offen sc1\nbuffer_load_dwordx4 a[100:103], v14, s[16:19], s53 offen offset:1024 sc1\n"
"s_add_u32 s53, s53, 14336\nfg1_k5:\ns_add_u32 s54, s54, 2048\ns_cmp_eq_u32 s54, 14336\n"
"s_cselect_b32 s54, 0, s54\ns_cmp_ge_u32 s54, 8192\ns_cbranch_scc1 fg1_k6\ns_add_u32 s53, s54, s36\n"
"buffer_load_dwordx4 a[48:51], v14, s[16:19], s53 offen sc1\nbuffer_load_dwordx4 a[52:55], v14, s[16:19], s53 offen offset:1024 sc1\ns_add_u32 s53, s53, 14336\nbuffer_load_dwordx4 a[104:107], v14, s[16:19], s53 offen sc1\n"
"buffer_load_dwordx4 a[108:111], v14, s[16:19], s53 offen offset:1024 sc1\ns_add_u32 s53, s53, 14336\nfg1_k6:\ns_add_u32 s54, s54, 2048\n"
"s_cmp_eq_u32 s54, 14336\ns_cselect_b32 s54, 0, s54\ns_branch fg1_end\nfg1_main:\n"
"s_waitcnt lgkmcnt(6)\nv_mfma_f32_16x16x32_bf16 v[64:67], v[96:99], a[128:131], 0\ns_add_u32 m0, s32, 32768\nv_mfma_f32_16x16x32_bf16 v[68:71], v[96:99], a[160:163], 0\n"
"ds_read_b128 v[96:99], v40 offset:33024\ns_mov_b32 s53, 28672\nv_mfma_f32_16x16x32_bf16 v[72:75], v[100:103], a[128:131], 0\nbuffer_load_dwordx4 v10, s[12:15], s53 offen lds\n"
"v_mfma_f32_16x16x32_bf16 v[76:79], v[100:103], a[160:163], 0\nds_read_b128 v[100:103], v44 offset:33024\nv_cvt_pk_bf16_f32 v168, v80, v81\nv_cvt_pk_bf16_f32 v169, v82, v83\n"
"s_waitcnt lgkmcnt(6)\nv_mfma_f32_16x16x32_bf16 v[64:67], v[104:107], a[132:135], v[64:67]\nv_cvt_pk_bf16_f32 v170, v88, v89\nv_cvt_pk_bf16_f32 v171, v90, v91\n"
"v_mfma_f32_16x16x32_bf16 v[68:71], v[104:107], a[164:167], v[68:71]\nds_read_b128 v[104:107], v41 offset:33024\nv_cvt_pk_bf16_f32 v172, v84, v85\nv_cvt_pk_bf16_f32 v173, v86, v87\n"
"v_mfma_f32_16x16x32_bf16 v[72:75], v[108:111], a[132:135], v[72:75]\nv_cvt_pk_bf16_f32 v174, v92, v93\nv_cvt_pk_bf16_f32 v175, v94, v95\nv_mfma_f32_16x16x32_bf16 v[76:79], v[108:111], a[164:167], v[76:79]\n"
"ds_read_b128 v[108:111], v45 offset:33024\ns_lshl_b32 s54, s42, 11\ns_add_u32 s54, s54, 1024\ns_waitcnt lgkmcnt(6)\n"
"v_mfma_f32_16x16x32_bf16 v[64:67], v[112:115], a[136:139], v[64:67]\nbuffer_store_dwordx4 v[168:171], v13, s[16:19], s54 offen sc1\nv_mfma_f32_16x16x32_bf16 v[68:71], v[112:115], a[168:171], v[68:71]\nds_read_b128 v[112:115], v42 offset:33024\n"
"s_add_u32 s54, s54, 262144\nv_mfma_f32_16x16x32_bf16 v[72:75], v[116:119], a[136:139], v[72:75]\nbuffer_store_dwordx4 v[172:175], v13, s[16:19], s54 offen sc1\nv_mfma_f32_16x16x32_bf16 v[76:79], v[116:119], a[168:171], v[76:79]\n"
"ds_read_b128 v[116:119], v46 offset:33024\ns_add_u32 m0, s32, 36864\ns_waitcnt lgkmcnt(6)\nv_mfma_f32_16x16x32_bf16 v[64:67], v[120:123], a[140:143], v[64:67]\n"
"s_mov_b32 s53, 28800\nv_mfma_f32_16x16x32_bf16 v[68:71], v[120:123], a[172:175], v[68:71]\nds_read_b128 v[120:123], v43 offset:33024\nbuffer_load_dwordx4 v10, s[12:15], s53 offen lds\n"
"v_mfma_f32_16x16x32_bf16 v[72:75], v[124:127], a[140:143], v[72:75]\ns_add_u32 m0, s32, 40960\nv_mfma_f32_16x16x32_bf16 v[76:79], v[124:127], a[172:175], v[76:79]\nds_read_b128 v[124:127], v47 offset:33024\n"
"s_mov_b32 s53, 28928\ns_waitcnt lgkmcnt(6)\nv_mfma_f32_16x16x32_bf16 v[64:67], v[96:99], a[144:147], v[64:67]\nbuffer_load_dwordx4 v10, s[12:15], s53 offen lds\n"
"v_mfma_f32_16x16x32_bf16 v[68:71], v[96:99], a[176:179], v[68:71]\nds_read_b128 v[96:99], v40 offset:49152\ns_add_u32 m0, s32, 45056\nv_mfma_f32_16x16x32_bf16 v[72:75], v[100:103], a[144:147], v[72:75]\n"
"s_mov_b32 s53, 29056\nv_mfma_f32_16x16x32_bf16 v[76:79], v[100:103], a[176:179], v[76:79]\nds_read_b128 v[100:103], v44 offset:49152\nbuffer_load_dwordx4 v10, s[12:15], s53 offen lds\n"
"s_waitcnt lgkmcnt(6)\nv_mfma_f32_16x16x32_bf16 v[64:67], v[104:107], a[148:151], v[64:67]\nv_mfma_f32_16x16x32_bf16 v[68:71], v[104:107], a[180:183], v[68:71]\nds_read_b128 v[104:107], v41 offset:49152\n"
"v_mfma_f32_16x16x32_bf16 v[72:75], v[108:111], a[148:151], v[72:75]\nv_mfma_f32_16x16x32_bf16 v[76:79], v[108:111], a[180:183], v[76:79]\nds_read_b128 v[108:111], v45 offset:49152\ns_waitcnt lgkmcnt(6)\n"
"v_mfma_f32_16x16x32_bf16 v[64:67], v[112:115], a[152:155], v[64:67]\nv_mfma_f32_16x16x32_bf16 v[68:71], v[112:115], a[184:187], v[68:71]\nds_read_b128 v[112:115], v42 offset:49152\nv_mfma_f32_16x16x32_bf16 v[72:75], v[116:119], a[152:155], v[72:75]\n"
"v_mfma_f32_16x16x32_bf16 v[76:79], v[116:119], a[184:187], v[76:79]\nds_read_b128 v[116:119], v46 offset:49152\ns_waitcnt lgkmcnt(6)\nv_mfma_f32_16x16x32_bf16 v[64:67], v[120:123], a[156:159], v[64:67]\n"
"v_mfma_f32_16x16x32_bf16 v[68:71], v[120:123], a[188:191], v[68:71]\nds_read_b128 v[120:123], v43 offset:49152\nv_mfma_f32_16x16x32_bf16 v[72:75], v[124:127], a[156:159], v[72:75]\nv_mfma_f32_16x16x32_bf16 v[76:79], v[124:127], a[188:191], v[76:79]\n"
"ds_read_b128 v[124:127], v47 offset:49152\ns_waitcnt lgkmcnt(6)\nv_mfma_f32_16x16x32_bf16 v[80:83], v[96:99], a[128:131], 0\ns_add_u32 m0, s32, 49152\n"
"v_mfma_f32_16x16x32_bf16 v[84:87], v[96:99], a[160:163], 0\nds_read_b128 v[96:99], v40 offset:49408\ns_mov_b32 s53, 29184\nv_mfma_f32_16x16x32_bf16 v[88:91], v[100:103], a[128:131], 0\n"
"buffer_load_dwordx4 v10, s[12:15], s53 offen lds\nv_mfma_f32_16x16x32_bf16 v[92:95], v[100:103], a[160:163], 0\nds_read_b128 v[100:103], v44 offset:49408\nv_cvt_pk_bf16_f32 v176, v64, v65\n"
"v_cvt_pk_bf16_f32 v177, v66, v67\ns_waitcnt lgkmcnt(6)\nv_mfma_f32_16x16x32_bf16 v[80:83], v[104:107], a[132:135], v[80:83]\nv_cvt_pk_bf16_f32 v178, v72, v73\n"
"v_cvt_pk_bf16_f32 v179, v74, v75\nv_mfma_f32_16x16x32_bf16 v[84:87], v[104:107], a[164:167], v[84:87]\nds_read_b128 v[104:107], v41 offset:49408\nv_cvt_pk_bf16_f32 v180, v68, v69\n"
"v_cvt_pk_bf16_f32 v181, v70, v71\nv_mfma_f32_16x16x32_bf16 v[88:91], v[108:111], a[132:135], v[88:91]\nv_cvt_pk_bf16_f32 v182, v76, v77\nv_cvt_pk_bf16_f32 v183, v78, v79\n"
"v_mfma_f32_16x16x32_bf16 v[92:95], v[108:111], a[164:167], v[92:95]\nds_read_b128 v[108:111], v45 offset:49408\ns_lshl_b32 s54, s43, 11\ns_add_u32 s54, s54, 0\n"
"s_waitcnt lgkmcnt(6)\nv_mfma_f32_16x16x32_bf16 v[80:83], v[112:115], a[136:139], v[80:83]\nbuffer_store_dwordx4 v[176:179], v13, s[16:19], s54 offen sc1\nv_mfma_f32_16x16x32_bf16 v[84:87], v[112:115], a[168:171], v[84:87]\n"
"ds_read_b128 v[112:115], v42 offset:49408\ns_add_u32 s54, s54, 262144\nv_mfma_f32_16x16x32_bf16 v[88:91], v[116:119], a[136:139], v[88:91]\nbuffer_store_dwordx4 v[180:183], v13, s[16:19], s54 offen sc1\n"
"v_mfma_f32_16x16x32_bf16 v[92:95], v[116:119], a[168:171], v[92:95]\nds_read_b128 v[116:119], v46 offset:49408\ns_add_u32 m0, s32, 53248\ns_waitcnt lgkmcnt(6)\n"
"v_mfma_f32_16x16x32_bf16 v[80:83], v[120:123], a[140:143], v[80:83]\ns_mov_b32 s53, 29312\nv_mfma_f32_16x16x32_bf16 v[84:87], v[120:123], a[172:175], v[84:87]\nds_read_b128 v[120:123], v43 offset:49408\n"
"buffer_load_dwordx4 v10, s[12:15], s53 offen lds\nv_mfma_f32_16x16x32_bf16 v[88:91], v[124:127], a[140:143], v[88:91]\ns_add_u32 m0, s32, 57344\nv_mfma_f32_16x16x32_bf16 v[92:95], v[124:127], a[172:175], v[92:95]\n"
"ds_read_b128 v[124:127], v47 offset:49408\ns_mov_b32 s53, 29440\ns_waitcnt lgkmcnt(6)\nv_mfma_f32_16x16x32_bf16 v[80:83], v[96:99], a[144:147], v[80:83]\n"
"buffer_load_dwordx4 v10, s[12:15], s53 offen lds\nv_mfma_f32_16x16x32_bf16 v[84:87], v[96:99], a[176:179], v[84:87]\nbuffer_load_dword v37, v21, s[24:27], 0 offen sc1\nv_mfma_f32_16x16x32_bf16 v[88:91], v[100:103], a[144:147], v[88:91]\n"
"v_mfma_f32_16x16x32_bf16 v[92:95], v[100:103], a[176:179], v[92:95]\ns_waitcnt lgkmcnt(4)\nv_mfma_f32_16x16x32_bf16 v[80:83], v[104:107], a[148:151], v[80:83]\nv_mfma_f32_16x16x32_bf16 v[84:87], v[104:107], a[180:183], v[84:87]\n"
"v_mfma_f32_16x16x32_bf16 v[88:91], v[108:111], a[148:151], v[88:91]\nv_mfma_f32_16x16x32_bf16 v[92:95], v[108:111], a[180:183], v[92:95]\ns_waitcnt lgkmcnt(2)\nv_mfma_f32_16x16x32_bf16 v[80:83], v[112:115], a[152:155], v[80:83]\n"
"v_mfma_f32_16x16x32_bf16 v[84:87], v[112:115], a[184:187], v[84:87]\nv_mfma_f32_16x16x32_bf16 v[88:91], v[116:119], a[152:155], v[88:91]\nv_mfma_f32_16x16x32_bf16 v[92:95], v[116:119], a[184:187], v[92:95]\ns_waitcnt lgkmcnt(0)\n"
"v_mfma_f32_16x16x32_bf16 v[80:83], v[120:123], a[156:159], v[80:83]\nv_mfma_f32_16x16x32_bf16 v[84:87], v[120:123], a[188:191], v[84:87]\nv_mfma_f32_16x16x32_bf16 v[88:91], v[124:127], a[156:159], v[88:91]\nv_mfma_f32_16x16x32_bf16 v[92:95], v[124:127], a[188:191], v[92:95]\n"
"s_nop 7\nv_cvt_pk_bf16_f32 v184, v80, v81\nv_cvt_pk_bf16_f32 v185, v82, v83\nv_cvt_pk_bf16_f32 v186, v88, v89\n"
"v_cvt_pk_bf16_f32 v187, v90, v91\nv_cvt_pk_bf16_f32 v188, v84, v85\nv_cvt_pk_bf16_f32 v189, v86, v87\nv_cvt_pk_bf16_f32 v190, v92, v93\n"
"v_cvt_pk_bf16_f32 v191, v94, v95\ns_lshl_b32 s54, s43, 11\ns_add_u32 s54, s54, 1024\nbuffer_store_dwordx4 v[184:187], v13, s[16:19], s54 offen sc1\n"
"s_add_u32 s54, s54, 262144\nbuffer_store_dwordx4 v[188:191], v13, s[16:19], s54 offen sc1\ns_waitcnt vmcnt(2)\nv_mov_b32 v36, s28\n"
"v_cmp_ne_u32 vcc, s28, v37\ns_cbranch_vccz fg1_e1\ns_waitcnt vmcnt(0)\ns_barrier\n"
"buffer_store_dword v36, v19, s[24:27], 0 offen sc1\nfg1_po:\nv_cmp_ne_u32 vcc, s28, v37\ns_cbranch_vccz fg1_pook\n"
"s_add_u32 s55, s55, 1\ns_cmp_gt_u32 s55, 262144\ns_cbranch_scc0 fg1_po2\ns_endpgm\n"
"fg1_po2:\ns_sleep 1\nbuffer_load_dword v37, v21, s[24:27], 0 offen sc1\ns_waitcnt vmcnt(0)\n"
"s_branch fg1_po\nfg1_pook:\ns_mov_b32 s53, 8192\nbuffer_load_dwordx4 v[192:195], v13, s[16:19], s53 offen sc1\n"
"s_mov_b32 s53, 270336\nbuffer_load_dwordx4 v[196:199], v13, s[16:19], s53 offen sc1\ns_mov_b32 s53, 9216\nbuffer_load_dwordx4 v[200:203], v13, s[16:19], s53 offen sc1\n"
"s_mov_b32 s53, 271360\nbuffer_load_dwordx4 v[204:207], v13, s[16:19], s53 offen sc1\ns_mov_b32 s53, 10240\nbuffer_load_dwordx4 v[208:211], v13, s[16:19], s53 offen sc1\n"
"s_mov_b32 s53, 272384\nbuffer_load_dwordx4 v[212:215], v13, s[16:19], s53 offen sc1\ns_mov_b32 s53, 11264\nbuffer_load_dwordx4 v[216:219], v13, s[16:19], s53 offen sc1\n"
"s_mov_b32 s53, 273408\nbuffer_load_dwordx4 v[220:223], v13, s[16:19], s53 offen sc1\ns_mov_b32 s53, 12288\nbuffer_load_dwordx4 v[224:227], v13, s[16:19], s53 offen sc1\n"
"s_mov_b32 s53, 274432\nbuffer_load_dwordx4 v[228:231], v13, s[16:19], s53 offen sc1\ns_mov_b32 s53, 13312\nbuffer_load_dwordx4 v[232:235], v13, s[16:19], s53 offen sc1\n"
"s_mov_b32 s53, 275456\nbuffer_load_dwordx4 v[236:239], v13, s[16:19], s53 offen sc1\nbuffer_store_dword v36, v19, s[24:27], 0 offen sc1\ns_branch fg1_e2\n"
"fg1_e1:\ns_mov_b32 s53, 8192\nbuffer_load_dwordx4 v[192:195], v13, s[16:19], s53 offen sc1\ns_mov_b32 s53, 270336\n"
"buffer_load_dwordx4 v[196:199], v13, s[16:19], s53 offen sc1\ns_mov_b32 s53, 9216\nbuffer_load_dwordx4 v[200:203], v13, s[16:19], s53 offen sc1\ns_mov_b32 s53, 271360\n"
"buffer_load_dwordx4 v[204:207], v13, s[16:19], s53 offen sc1\ns_mov_b32 s53, 10240\nbuffer_load_dwordx4 v[208:211], v13, s[16:19], s53 offen sc1\ns_mov_b32 s53, 272384\n"
"buffer_load_dwordx4 v[212:215], v13, s[16:19], s53 offen sc1\ns_mov_b32 s53, 11264\nbuffer_load_dwordx4 v[216:219], v13, s[16:19], s53 offen sc1\ns_mov_b32 s53, 273408\n"
"buffer_load_dwordx4 v[220:223], v13, s[16:19], s53 offen sc1\ns_mov_b32 s53, 12288\nbuffer_load_dwordx4 v[224:227], v13, s[16:19], s53 offen sc1\ns_mov_b32 s53, 274432\n"
"buffer_load_dwordx4 v[228:231], v13, s[16:19], s53 offen sc1\ns_mov_b32 s53, 13312\nbuffer_load_dwordx4 v[232:235], v13, s[16:19], s53 offen sc1\ns_mov_b32 s53, 275456\n"
"buffer_load_dwordx4 v[236:239], v13, s[16:19], s53 offen sc1\ns_waitcnt vmcnt(12)\ns_barrier\nbuffer_store_dword v36, v19, s[24:27], 0 offen sc1\n"
"fg1_e2:\ns_add_u32 m0, s32, 65536\ns_mov_b32 s53, 57344\nbuffer_load_dwordx4 v10, s[12:15], s53 offen lds\n"
"s_add_u32 m0, s32, 69632\ns_mov_b32 s53, 57472\nbuffer_load_dwordx4 v10, s[12:15], s53 offen lds\ns_add_u32 m0, s32, 73728\n"
"s_mov_b32 s53, 57600\nbuffer_load_dwordx4 v10, s[12:15], s53 offen lds\ns_add_u32 m0, s32, 77824\ns_mov_b32 s53, 57728\n"
"buffer_load_dwordx4 v10, s[12:15], s53 offen lds\ns_add_u32 m0, s32, 81920\ns_mov_b32 s53, 57856\nbuffer_load_dwordx4 v10, s[12:15], s53 offen lds\n"
"s_add_u32 m0, s32, 86016\ns_mov_b32 s53, 57984\nbuffer_load_dwordx4 v10, s[12:15], s53 offen lds\ns_add_u32 m0, s32, 90112\n"
"s_mov_b32 s53, 58112\nbuffer_load_dwordx4 v10, s[12:15], s53 offen lds\ns_sub_u32 s48, s28, 1\ns_mov_b32 s49, 86016\n"
"s_mov_b32 s50, 0\ns_mov_b32 s51, 0\ns_add_u32 s52, s32, 98304\ns_add_u32 s47, s32, 131072\n"
"ds_read_b128 v[40:43], v32 offset:0\nds_read_b128 v[44:47], v33 offset:0\nds_read_b128 v[48:51], v34 offset:0\nds_read_b128 v[52:55], v35 offset:0\n"
"s_barrier\ns_waitcnt lgkmcnt(3)\nv_mfma_f32_16x16x32_bf16 v[64:67], v[40:43], v[128:131], 0\nv_mov_b32 v36, s48\n"
"s_add_u32 s48, s48, 1\ns_nop 0\nbuffer_store_dword v36, v19, s[24:27], s33 offen sc1\nv_mfma_f32_16x16x32_bf16 v[68:71], v[40:43], v[132:135], 0\n"
"ds_read_b128 v[40:43], v32 offset:4096\ns_waitcnt lgkmcnt(3)\nv_mfma_f32_16x16x32_bf16 v[72:75], v[44:47], v[128:131], 0\nv_mfma_f32_16x16x32_bf16 v[76:79], v[44:47], v[132:135], 0\n"
"ds_read_b128 v[44:47], v33 offset:4096\ns_waitcnt lgkmcnt(3)\nv_mfma_f32_16x16x32_bf16 v[64:67], v[48:51], v[136:139], v[64:67]\ns_add_u32 m0, s52, 0\n"
"v_mfma_f32_16x16x32_bf16 v[68:71], v[48:51], v[140:143], v[68:71]\nds_read_b128 v[48:51], v34 offset:4096\ns_add_u32 s53, s49, 0\ns_waitcnt lgkmcnt(3)\n"
"v_mfma_f32_16x16x32_bf16 v[72:75], v[52:55], v[136:139], v[72:75]\nbuffer_load_dwordx4 v10, s[12:15], s53 offen lds\nv_mfma_f32_16x16x32_bf16 v[76:79], v[52:55], v[140:143], v[76:79]\nds_read_b128 v[52:55], v35 offset:4096\n"
"s_add_u32 m0, s52, 4096\ns_waitcnt lgkmcnt(3)\nv_mfma_f32_16x16x32_bf16 v[64:67], v[40:43], v[144:147], v[64:67]\ns_add_u32 s53, s49, 128\n"
"v_mfma_f32_16x16x32_bf16 v[68:71], v[40:43], v[148:151], v[68:71]\nds_read_b128 v[40:43], v32 offset:8192\nbuffer_load_dwordx4 v10, s[12:15], s53 offen lds\ns_waitcnt lgkmcnt(3)\n"
"v_mfma_f32_16x16x32_bf16 v[72:75], v[44:47], v[144:147], v[72:75]\ns_add_u32 m0, s52, 8192\nv_mfma_f32_16x16x32_bf16 v[76:79], v[44:47], v[148:151], v[76:79]\nds_read_b128 v[44:47], v33 offset:8192\n"
"s_add_u32 s53, s49, 256\ns_waitcnt lgkmcnt(3)\nv_mfma_f32_16x16x32_bf16 v[64:67], v[48:51], v[152:155], v[64:67]\nbuffer_load_dwordx4 v10, s[12:15], s53 offen lds\n"
"v_mfma_f32_16x16x32_bf16 v[68:71], v[48:51], v[156:159], v[68:71]\nds_read_b128 v[48:51], v34 offset:8192\ns_add_u32 m0, s52, 12288\ns_waitcnt lgkmcnt(3)\n"
"v_mfma_f32_16x16x32_bf16 v[72:75], v[52:55], v[152:155], v[72:75]\ns_add_u32 s53, s49, 384\nv_mfma_f32_16x16x32_bf16 v[76:79], v[52:55], v[156:159], v[76:79]\nds_read_b128 v[52:55], v35 offset:8192\n"
"buffer_load_dwordx4 v10, s[12:15], s53 offen lds\ns_waitcnt lgkmcnt(3)\nv_mfma_f32_16x16x32_bf16 v[64:67], v[40:43], v[160:163], v[64:67]\ns_add_u32 m0, s52, 16384\n"
"v_mfma_f32_16x16x32_bf16 v[68:71], v[40:43], v[164:167], v[68:71]\nds_read_b128 v[40:43], v32 offset:12288\ns_add_u32 s53, s49, 512\ns_waitcnt lgkmcnt(3)\n"
"v_mfma_f32_16x16x32_bf16 v[72:75], v[44:47], v[160:163], v[72:75]\nbuffer_load_dwordx4 v10, s[12:15], s53 offen lds\nv_mfma_f32_16x16x32_bf16 v[76:79], v[44:47], v[164:167], v[76:79]\nds_read_b128 v[44:47], v33 offset:12288\n"
"s_add_u32 m0, s52, 20480\ns_waitcnt lgkmcnt(3)\nv_mfma_f32_16x16x32_bf16 v[64:67], v[48:51], v[168:171], v[64:67]\ns_add_u32 s53, s49, 640\n"
"v_mfma_f32_16x16x32_bf16 v[68:71], v[48:51], v[172:175], v[68:71]\nds_read_b128 v[48:51], v34 offset:12288\nbuffer_load_dwordx4 v10, s[12:15], s53 offen lds\ns_waitcnt lgkmcnt(3)\n"
"v_mfma_f32_16x16x32_bf16 v[72:75], v[52:55], v[168:171], v[72:75]\ns_add_u32 m0, s52, 24576\nv_mfma_f32_16x16x32_bf16 v[76:79], v[52:55], v[172:175], v[76:79]\nds_read_b128 v[52:55], v35 offset:12288\n"
"s_add_u32 s53, s49, 768\ns_waitcnt lgkmcnt(3)\nv_mfma_f32_16x16x32_bf16 v[64:67], v[40:43], v[176:179], v[64:67]\nbuffer_load_dwordx4 v10, s[12:15], s53 offen lds\n"
"v_mfma_f32_16x16x32_bf16 v[68:71], v[40:43], v[180:183], v[68:71]\nds_read_b128 v[40:43], v32 offset:16384\ns_waitcnt lgkmcnt(3)\nv_mfma_f32_16x16x32_bf16 v[72:75], v[44:47], v[176:179], v[72:75]\n"
"v_mfma_f32_16x16x32_bf16 v[76:79], v[44:47], v[180:183], v[76:79]\nds_read_b128 v[44:47], v33 offset:16384\ns_waitcnt lgkmcnt(3)\nv_mfma_f32_16x16x32_bf16 v[64:67], v[48:51], v[184:187], v[64:67]\n"
"v_mfma_f32_16x16x32_bf16 v[68:71], v[48:51], v[188:191], v[68:71]\nds_read_b128 v[48:51], v34 offset:16384\ns_waitcnt lgkmcnt(3)\nv_mfma_f32_16x16x32_bf16 v[72:75], v[52:55], v[184:187], v[72:75]\n"
"v_mfma_f32_16x16x32_bf16 v[76:79], v[52:55], v[188:191], v[76:79]\nds_read_b128 v[52:55], v35 offset:16384\ns_waitcnt vmcnt(16)\ns_waitcnt lgkmcnt(3)\n"
"v_mfma_f32_16x16x32_bf16 v[64:67], v[40:43], v[192:195], v[64:67]\nv_mfma_f32_16x16x32_bf16 v[68:71], v[40:43], v[196:199], v[68:71]\nds_read_b128 v[40:43], v32 offset:20480\ns_waitcnt lgkmcnt(3)\n"
"v_mfma_f32_16x16x32_bf16 v[72:75], v[44:47], v[192:195], v[72:75]\nv_mfma_f32_16x16x32_bf16 v[76:79], v[44:47], v[196:199], v[76:79]\nds_read_b128 v[44:47], v33 offset:20480\ns_waitcnt lgkmcnt(3)\n"
"v_mfma_f32_16x16x32_bf16 v[64:67], v[48:51], v[200:203], v[64:67]\nv_mfma_f32_16x16x32_bf16 v[68:71], v[48:51], v[204:207], v[68:71]\nds_read_b128 v[48:51], v34 offset:20480\ns_waitcnt lgkmcnt(3)\n"
"v_mfma_f32_16x16x32_bf16 v[72:75], v[52:55], v[200:203], v[72:75]\nv_mfma_f32_16x16x32_bf16 v[76:79], v[52:55], v[204:207], v[76:79]\nds_read_b128 v[52:55], v35 offset:20480\ns_waitcnt lgkmcnt(3)\n"
"v_mfma_f32_16x16x32_bf16 v[64:67], v[40:43], v[208:211], v[64:67]\nv_mfma_f32_16x16x32_bf16 v[68:71], v[40:43], v[212:215], v[68:71]\nds_read_b128 v[40:43], v32 offset:24576\ns_waitcnt lgkmcnt(3)\n"
"v_mfma_f32_16x16x32_bf16 v[72:75], v[44:47], v[208:211], v[72:75]\nv_mfma_f32_16x16x32_bf16 v[76:79], v[44:47], v[212:215], v[76:79]\nds_read_b128 v[44:47], v33 offset:24576\ns_waitcnt lgkmcnt(3)\n"
"v_mfma_f32_16x16x32_bf16 v[64:67], v[48:51], v[216:219], v[64:67]\ns_add_u32 s54, s51, 32768\ns_cmp_eq_u32 s54, 163840\nv_mfma_f32_16x16x32_bf16 v[68:71], v[48:51], v[220:223], v[68:71]\n"
"s_cselect_b32 s54, 0, s54\ns_sub_u32 s54, s54, s51\nds_read_b128 v[48:51], v34 offset:24576\ns_waitcnt lgkmcnt(3)\n"
"v_mfma_f32_16x16x32_bf16 v[72:75], v[52:55], v[216:219], v[72:75]\nv_mfma_f32_16x16x32_bf16 v[76:79], v[52:55], v[220:223], v[76:79]\nds_read_b128 v[52:55], v35 offset:24576\ns_waitcnt lgkmcnt(3)\n"
"v_mfma_f32_16x16x32_bf16 v[64:67], v[40:43], v[224:227], v[64:67]\nv_add_u32 v32, s54, v32\nv_mfma_f32_16x16x32_bf16 v[68:71], v[40:43], v[228:231], v[68:71]\nds_read_b128 v[40:43], v32 offset:0\n"
"s_waitcnt lgkmcnt(3)\nv_mfma_f32_16x16x32_bf16 v[72:75], v[44:47], v[224:227], v[72:75]\nv_add_u32 v33, s54, v33\nv_mfma_f32_16x16x32_bf16 v[76:79], v[44:47], v[228:231], v[76:79]\n"
"ds_read_b128 v[44:47], v33 offset:0\ns_waitcnt lgkmcnt(3)\nv_mfma_f32_16x16x32_bf16 v[64:67], v[48:51], v[232:235], v[64:67]\nv_add_u32 v34, s54, v34\n"
"v_mfma_f32_16x16x32_bf16 v[68:71], v[48:51], v[236:239], v[68:71]\nds_read_b128 v[48:51], v34 offset:0\ns_waitcnt lgkmcnt(3)\nv_mfma_f32_16x16x32_bf16 v[72:75], v[52:55], v[232:235], v[72:75]\n"
"v_add_u32 v35, s54, v35\nv_mfma_f32_16x16x32_bf16 v[76:79], v[52:55], v[236:239], v[76:79]\nds_read_b128 v[52:55], v35 offset:0\ns_mov_b32 s52, s47\n"
"s_add_u32 s47, s51, s32\ns_add_u32 s51, s51, s54\ns_add_u32 s49, s49, 28672\ns_waitcnt vmcnt(8)\n"
"s_barrier\ns_waitcnt lgkmcnt(3)\nv_mfma_f32_16x16x32_bf16 v[96:99], v[40:43], v[128:131], 0\nv_mov_b32 v36, s48\n"
"s_add_u32 s48, s48, 1\ns_nop 0\nbuffer_store_dword v36, v19, s[24:27], s33 offen sc1\nv_mfma_f32_16x16x32_bf16 v[100:103], v[40:43], v[132:135], 0\n"
"ds_read_b128 v[40:43], v32 offset:4096\ns_waitcnt lgkmcnt(3)\nv_mfma_f32_16x16x32_bf16 v[104:107], v[44:47], v[128:131], 0\nv_mfma_f32_16x16x32_bf16 v[108:111], v[44:47], v[132:135], 0\n"
"ds_read_b128 v[44:47], v33 offset:4096\nv_cvt_pk_bf16_f32 v56, v64, v65\ns_waitcnt lgkmcnt(3)\nv_mfma_f32_16x16x32_bf16 v[96:99], v[48:51], v[136:139], v[96:99]\n"
"v_cvt_pk_bf16_f32 v57, v66, v67\nv_mfma_f32_16x16x32_bf16 v[100:103], v[48:51], v[140:143], v[100:103]\nds_read_b128 v[48:51], v34 offset:4096\nv_cvt_pk_bf16_f32 v58, v72, v73\n"
"s_waitcnt lgkmcnt(3)\nv_mfma_f32_16x16x32_bf16 v[104:107], v[52:55], v[136:139], v[104:107]\nv_cvt_pk_bf16_f32 v59, v74, v75\nv_mfma_f32_16x16x32_bf16 v[108:111], v[52:55], v[140:143], v[108:111]\n"
"ds_read_b128 v[52:55], v35 offset:4096\ns_waitcnt lgkmcnt(3)\nv_mfma_f32_16x16x32_bf16 v[96:99], v[40:43], v[144:147], v[96:99]\nv_mfma_f32_16x16x32_bf16 v[100:103], v[40:43], v[148:151], v[100:103]\n"
"ds_read_b128 v[40:43], v32 offset:8192\nbuffer_store_dwordx4 v[56:59], v15, s[20:23], s50 offen sc1\ns_waitcnt lgkmcnt(3)\nv_mfma_f32_16x16x32_bf16 v[104:107], v[44:47], v[144:147], v[104:107]\n"
"v_cvt_pk_bf16_f32 v60, v68, v69\nv_mfma_f32_16x16x32_bf16 v[108:111], v[44:47], v[148:151], v[108:111]\nds_read_b128 v[44:47], v33 offset:8192\nv_cvt_pk_bf16_f32 v61, v70, v71\n"
"s_waitcnt lgkmcnt(3)\nv_mfma_f32_16x16x32_bf16 v[96:99], v[48:51], v[152:155], v[96:99]\nv_cvt_pk_bf16_f32 v62, v76, v77\nv_mfma_f32_16x16x32_bf16 v[100:103], v[48:51], v[156:159], v[100:103]\n"
"ds_read_b128 v[48:51], v34 offset:8192\nv_cvt_pk_bf16_f32 v63, v78, v79\ns_waitcnt lgkmcnt(3)\nv_mfma_f32_16x16x32_bf16 v[104:107], v[52:55], v[152:155], v[104:107]\n"
"v_mfma_f32_16x16x32_bf16 v[108:111], v[52:55], v[156:159], v[108:111]\nds_read_b128 v[52:55], v35 offset:8192\ns_waitcnt lgkmcnt(3)\nv_mfma_f32_16x16x32_bf16 v[96:99], v[40:43], v[160:163], v[96:99]\n"
"buffer_store_dwordx4 v[60:63], v16, s[20:23], s50 offen sc1\nv_mfma_f32_16x16x32_bf16 v[100:103], v[40:43], v[164:167], v[100:103]\nds_read_b128 v[40:43], v32 offset:12288\ns_add_u32 s50, s50, s36\n"
"s_waitcnt lgkmcnt(3)\nv_mfma_f32_16x16x32_bf16 v[104:107], v[44:47], v[160:163], v[104:107]\ns_add_u32 m0, s52, 0\nv_mfma_f32_16x16x32_bf16 v[108:111], v[44:47], v[164:167], v[108:111]\n"
"ds_read_b128 v[44:47], v33 offset:12288\ns_add_u32 s53, s49, 0\ns_waitcnt lgkmcnt(3)\nv_mfma_f32_16x16x32_bf16 v[96:99], v[48:51], v[168:171], v[96:99]\n"
"buffer_load_dwordx4 v10, s[12:15], s53 offen lds\nv_mfma_f32_16x16x32_bf16 v[100:103], v[48:51], v[172:175], v[100:103]\nds_read_b128 v[48:51], v34 offset:12288\ns_add_u32 m0, s52, 4096\n"
"s_waitcnt lgkmcnt(3)\nv_mfma_f32_16x16x32_bf16 v[104:107], v[52:55], v[168:171], v[104:107]\ns_add_u32 s53, s49, 128\nv_mfma_f32_16x16x32_bf16 v[108:111], v[52:55], v[172:175], v[108:111]\n"
"ds_read_b128 v[52:55], v35 offset:12288\nbuffer_load_dwordx4 v10, s[12:15], s53 offen lds\ns_waitcnt lgkmcnt(3)\nv_mfma_f32_16x16x32_bf16 v[96:99], v[40:43], v[176:179], v[96:99]\n"
"s_add_u32 m0, s52, 8192\nv_mfma_f32_16x16x32_bf16 v[100:103], v[40:43], v[180:183], v[100:103]\nds_read_b128 v[40:43], v32 offset:16384\ns_add_u32 s53, s49, 256\n"
"s_waitcnt lgkmcnt(3)\nv_mfma_f32_16x16x32_bf16 v[104:107], v[44:47], v[176:179], v[104:107]\nbuffer_load_dwordx4 v10, s[12:15], s53 offen lds\nv_mfma_f32_16x16x32_bf16 v[108:111], v[44:47], v[180:183], v[108:111]\n"
"ds_read_b128 v[44:47], v33 offset:16384\ns_add_u32 m0, s52, 12288\ns_waitcnt lgkmcnt(3)\nv_mfma_f32_16x16x32_bf16 v[96:99], v[48:51], v[184:187], v[96:99]\n"
"s_add_u32 s53, s49, 384\nv_mfma_f32_16x16x32_bf16 v[100:103], v[48:51], v[188:191], v[100:103]\nds_read_b128 v[48:51], v34 offset:16384\nbuffer_load_dwordx4 v10, s[12:15], s53 offen lds\n"
"s_waitcnt lgkmcnt(3)\nv_mfma_f32_16x16x32_bf16 v[104:107], v[52:55], v[184:187], v[104:107]\ns_add_u32 m0, s52, 16384\nv_mfma_f32_16x16x32_bf16 v[108:111], v[52:55], v[188:191], v[108:111]\n"
"ds_read_b128 v[52:55], v35 offset:16384\ns_add_u32 s53, s49, 512\ns_waitcnt lgkmcnt(3)\nv_mfma_f32_16x16x32_bf16 v[96:99], v[40:43], v[192:195], v[96:99]\n"
"buffer_load_dwordx4 v10, s[12:15], s53 offen lds\nv_mfma_f32_16x16x32_bf16 v[100:103], v[40:43], v[196:199], v[100:103]\nds_read_b128 v[40:43], v32 offset:20480\ns_add_u32 m0, s52, 20480\n"
"s_waitcnt lgkmcnt(3)\nv_mfma_f32_16x16x32_bf16 v[104:107], v[44:47], v[192:195], v[104:107]\ns_add_u32 s53, s49, 640\nv_mfma_f32_16x16x32_bf16 v[108:111], v[44:47], v[196:199], v[108:111]\n"
"ds_read_b128 v[44:47], v33 offset:20480\nbuffer_load_dwordx4 v10, s[12:15], s53 offen lds\ns_waitcnt lgkmcnt(3)\nv_mfma_f32_16x16x32_bf16 v[96:99], v[48:51], v[200:203], v[96:99]\n"
"s_add_u32 m0, s52, 24576\nv_mfma_f32_16x16x32_bf16 v[100:103], v[48:51], v[204:207], v[100:103]\nds_read_b128 v[48:51], v34 offset:20480\ns_add_u32 s53, s49, 768\n"
"s_waitcnt lgkmcnt(3)\nv_mfma_f32_16x16x32_bf16 v[104:107], v[52:55], v[200:203], v[104:107]\nbuffer_load_dwordx4 v10, s[12:15], s53 offen lds\nv_mfma_f32_16x16x32_bf16 v[108:111], v[52:55], v[204:207], v[108:111]\n"
"ds_read_b128 v[52:55], v35 offset:20480\ns_waitcnt lgkmcnt(3)\nv_mfma_f32_16x16x32_bf16 v[96:99], v[40:43], v[208:211], v[96:99]\nv_mfma_f32_16x16x32_bf16 v[100:103], v[40:43], v[212:215], v[100:103]\n"
"ds_read_b128 v[40:43], v32 offset:24576\ns_waitcnt lgkmcnt(3)\nv_mfma_f32_16x16x32_bf16 v[104:107], v[44:47], v[208:211], v[104:107]\nv_mfma_f32_16x16x32_bf16 v[108:111], v[44:47], v[212:215], v[108:111]\n"
"ds_read_b128 v[44:47], v33 offset:24576\ns_waitcnt lgkmcnt(3)\nv_mfma_f32_16x16x32_bf16 v[96:99], v[48:51], v[216:219], v[96:99]\ns_add_u32 s54, s51, 32768\n"
"s_cmp_eq_u32 s54, 163840\nv_mfma_f32_16x16x32_bf16 v[100:103], v[48:51], v[220:223], v[100:103]\ns_cselect_b32 s54, 0, s54\ns_sub_u32 s54, s54, s51\n"
"ds_read_b128 v[48:51], v34 offset:24576\ns_waitcnt lgkmcnt(3)\nv_mfma_f32_16x16x32_bf16 v[104:107], v[52:55], v[216:219], v[104:107]\nv_mfma_f32_16x16x32_bf16 v[108:111], v[52:55], v[220:223], v[108:111]\n"
"ds_read_b128 v[52:55], v35 offset:24576\ns_waitcnt lgkmcnt(3)\nv_mfma_f32_16x16x32_bf16 v[96:99], v[40:43], v[224:227], v[96:99]\nv_add_u32 v32, s54, v32\n"
"v_mfma_f32_16x16x32_bf16 v[100:103], v[40:43], v[228:231], v[100:103]\nds_read_b128 v[40:43], v32 offset:0\ns_waitcnt lgkmcnt(3)\nv_mfma_f32_16x16x32_bf16 v[104:107], v[44:47], v[224:227], v[104:107]\n"
"v_add_u32 v33, s54, v33\nv_mfma_f32_16x16x32_bf16 v[108:111], v[44:47], v[228:231], v[108:111]\nds_read_b128 v[44:47], v33 offset:0\ns_waitcnt lgkmcnt(3)\n"
"v_mfma_f32_16x16x32_bf16 v[96:99], v[48:51], v[232:235], v[96:99]\nv_add_u32 v34, s54, v34\nv_mfma_f32_16x16x32_bf16 v[100:103], v[48:51], v[236:239], v[100:103]\nds_read_b128 v[48:51], v34 offset:0\n"
"s_waitcnt lgkmcnt(3)\nv_mfma_f32_16x16x32_bf16 v[104:107], v[52:55], v[232:235], v[104:107]\nv_add_u32 v35, s54, v35\nv_mfma_f32_16x16x32_bf16 v[108:111], v[52:55], v[236:239], v[108:111]\n"
"ds_read_b128 v[52:55], v35 offset:0\ns_mov_b32 s52, s47\ns_add_u32 s47, s51, s32\ns_add_u32 s51, s51, s54\n"
"s_add_u32 s49, s49, 28672\ns_waitcnt vmcnt(7)\ns_barrier\ns_waitcnt lgkmcnt(3)\n"
"v_mfma_f32_16x16x32_bf16 v[64:67], v[40:43], v[128:131], 0\nv_mov_b32 v36, s48\ns_add_u32 s48, s48, 1\ns_nop 0\n"
"buffer_store_dword v36, v19, s[24:27], s33 offen sc1\nv_mfma_f32_16x16x32_bf16 v[68:71], v[40:43], v[132:135], 0\nds_read_b128 v[40:43], v32 offset:4096\ns_waitcnt lgkmcnt(3)\n"
"v_mfma_f32_16x16x32_bf16 v[72:75], v[44:47], v[128:131], 0\nv_mfma_f32_16x16x32_bf16 v[76:79], v[44:47], v[132:135], 0\nds_read_b128 v[44:47], v33 offset:4096\nv_cvt_pk_bf16_f32 v56, v96, v97\n"
"s_waitcnt lgkmcnt(3)\nv_mfma_f32_16x16x32_bf16 v[64:67], v[48:51], v[136:139], v[64:67]\nv_cvt_pk_bf16_f32 v57, v98, v99\nv_mfma_f32_16x16x32_bf16 v[68:71], v[48:51], v[140:143], v[68:71]\n"
"ds_read_b128 v[48:51], v34 offset:4096\nv_cvt_pk_bf16_f32 v58, v104, v105\ns_waitcnt lgkmcnt(3)\nv_mfma_f32_16x16x32_bf16 v[72:75], v[52:55], v[136:139], v[72:75]\n"
"v_cvt_pk_bf16_f32 v59, v106, v107\nv_mfma_f32_16x16x32_bf16 v[76:79], v[52:55], v[140:143], v[76:79]\nds_read_b128 v[52:55], v35 offset:4096\ns_waitcnt lgkmcnt(3)\n"
"v_mfma_f32_16x16x32_bf16 v[64:67], v[40:43], v[144:147], v[64:67]\nv_mfma_f32_16x16x32_bf16 v[68:71], v[40:43], v[148:151], v[68:71]\nds_read_b128 v[40:43], v32 offset:8192\nbuffer_store_dwordx4 v[56:59], v15, s[20:23], s50 offen sc1\n"
"s_waitcnt lgkmcnt(3)\nv_mfma_f32_16x16x32_bf16 v[72:75], v[44:47], v[144:147], v[72:75]\nv_cvt_pk_bf16_f32 v60, v100, v101\nv_mfma_f32_16x16x32_bf16 v[76:79], v[44:47], v[148:151], v[76:79]\n"
"ds_read_b128 v[44:47], v33 offset:8192\nv_cvt_pk_bf16_f32 v61, v102, v103\ns_waitcnt lgkmcnt(3)\nv_mfma_f32_16x16x32_bf16 v[64:67], v[48:51], v[152:155], v[64:67]\n"
"v_cvt_pk_bf16_f32 v62, v108, v109\nv_mfma_f32_16x16x32_bf16 v[68:71], v[48:51], v[156:159], v[68:71]\nds_read_b128 v[48:51], v34 offset:8192\nv_cvt_pk_bf16_f32 v63, v110, v111\n"
"s_waitcnt lgkmcnt(3)\nv_mfma_f32_16x16x32_bf16 v[72:75], v[52:55], v[152:155], v[72:75]\nv_mfma_f32_16x16x32_bf16 v[76:79], v[52:55], v[156:159], v[76:79]\nds_read_b128 v[52:55], v35 offset:8192\n"
"s_waitcnt lgkmcnt(3)\nv_mfma_f32_16x16x32_bf16 v[64:67], v[40:43], v[160:163], v[64:67]\nbuffer_store_dwordx4 v[60:63], v16, s[20:23], s50 offen sc1\nv_mfma_f32_16x16x32_bf16 v[68:71], v[40:43], v[164:167], v[68:71]\n"
"ds_read_b128 v[40:43], v32 offset:12288\ns_add_u32 s50, s50, s36\ns_waitcnt lgkmcnt(3)\nv_mfma_f32_16x16x32_bf16 v[72:75], v[44:47], v[160:163], v[72:75]\n"
"s_add_u32 m0, s52, 0\nv_mfma_f32_16x16x32_bf16 v[76:79], v[44:47], v[164:167], v[76:79]\nds_read_b128 v[44:47], v33 offset:12288\ns_add_u32 s53, s49, 0\n"
"s_waitcnt lgkmcnt(3)\nv_mfma_f32_16x16x32_bf16 v[64:67], v[48:51], v[168:171], v[64:67]\nbuffer_load_dwordx4 v10, s[12:15], s53 offen lds\nv_mfma_f32_16x16x32_bf16 v[68:71], v[48:51], v[172:175], v[68:71]\n"
"ds_read_b128 v[48:51], v34 offset:12288\ns_add_u32 m0, s52, 4096\ns_waitcnt lgkmcnt(3)\nv_mfma_f32_16x16x32_bf16 v[72:75], v[52:55], v[168:171], v[72:75]\n"
"s_add_u32 s53, s49, 128\nv_mfma_f32_16x16x32_bf16 v[76:79], v[52:55], v[172:175], v[76:79]\nds_read_b128 v[52:55], v35 offset:12288\nbuffer_load_dwordx4 v10, s[12:15], s53 offen lds\n"
"s_waitcnt lgkmcnt(3)\nv_mfma_f32_16x16x32_bf16 v[64:67], v[40:43], v[176:179], v[64:67]\ns_add_u32 m0, s52, 8192\nv_mfma_f32_16x16x32_bf16 v[68:71], v[40:43], v[180:183], v[68:71]\n"
"ds_read_b128 v[40:43], v32 offset:16384\ns_add_u32 s53, s49, 256\ns_waitcnt lgkmcnt(3)\nv_mfma_f32_16x16x32_bf16 v[72:75], v[44:47], v[176:179], v[72:75]\n"
"buffer_load_dwordx4 v10, s[12:15], s53 offen lds\nv_mfma_f32_16x16x32_bf16 v[76:79], v[44:47], v[180:183], v[76:79]\nds_read_b128 v[44:47], v33 offset:16384\ns_add_u32 m0, s52, 12288\n"
"s_waitcnt lgkmcnt(3)\nv_mfma_f32_16x16x32_bf16 v[64:67], v[48:51], v[184:187], v[64:67]\ns_add_u32 s53, s49, 384\nv_mfma_f32_16x16x32_bf16 v[68:71], v[48:51], v[188:191], v[68:71]\n"
"ds_read_b128 v[48:51], v34 offset:16384\nbuffer_load_dwordx4 v10, s[12:15], s53 offen lds\ns_waitcnt lgkmcnt(3)\nv_mfma_f32_16x16x32_bf16 v[72:75], v[52:55], v[184:187], v[72:75]\n"
"s_add_u32 m0, s52, 16384\nv_mfma_f32_16x16x32_bf16 v[76:79], v[52:55], v[188:191], v[76:79]\nds_read_b128 v[52:55], v35 offset:16384\ns_add_u32 s53, s49, 512\n"
"s_waitcnt lgkmcnt(3)\nv_mfma_f32_16x16x32_bf16 v[64:67], v[40:43], v[192:195], v[64:67]\nbuffer_load_dwordx4 v10, s[12:15], s53 offen lds\nv_mfma_f32_16x16x32_bf16 v[68:71], v[40:43], v[196:199], v[68:71]\n"
"ds_read_b128 v[40:43], v32 offset:20480\ns_add_u32 m0, s52, 20480\ns_waitcnt lgkmcnt(3)\nv_mfma_f32_16x16x32_bf16 v[72:75], v[44:47], v[192:195], v[72:75]\n"
"s_add_u32 s53, s49, 640\nv_mfma_f32_16x16x32_bf16 v[76:79], v[44:47], v[196:199], v[76:79]\nds_read_b128 v[44:47], v33 offset:20480\nbuffer_load_dwordx4 v10, s[12:15], s53 offen lds\n"
"s_waitcnt lgkmcnt(3)\nv_mfma_f32_16x16x32_bf16 v[64:67], v[48:51], v[200:203], v[64:67]\ns_add_u32 m0, s52, 24576\nv_mfma_f32_16x16x32_bf16 v[68:71], v[48:51], v[204:207], v[68:71]\n"
"ds_read_b128 v[48:51], v34 offset:20480\ns_add_u32 s53, s49, 768\ns_waitcnt lgkmcnt(3)\nv_mfma_f32_16x16x32_bf16 v[72:75], v[52:55], v[200:203], v[72:75]\n"
"buffer_load_dwordx4 v10, s[12:15], s53 offen lds\nv_mfma_f32_16x16x32_bf16 v[76:79], v[52:55], v[204:207], v[76:79]\nds_read_b128 v[52:55], v35 offset:20480\ns_waitcnt lgkmcnt(3)\n"
"v_mfma_f32_16x16x32_bf16 v[64:67], v[40:43], v[208:211], v[64:67]\nv_mfma_f32_16x16x32_bf16 v[68:71], v[40:43], v[212:215], v[68:71]\nds_read_b128 v[40:43], v32 offset:24576\ns_waitcnt lgkmcnt(3)\n"
"v_mfma_f32_16x16x32_bf16 v[72:75], v[44:47], v[208:211], v[72:75]\nv_mfma_f32_16x16x32_bf16 v[76:79], v[44:47], v[212:215], v[76:79]\nds_read_b128 v[44:47], v33 offset:24576\ns_waitcnt lgkmcnt(3)\n"
"v_mfma_f32_16x16x32_bf16 v[64:67], v[48:51], v[216:219], v[64:67]\ns_add_u32 s54, s51, 32768\ns_cmp_eq_u32 s54, 163840\nv_mfma_f32_16x16x32_bf16 v[68:71], v[48:51], v[220:223], v[68:71]\n"
"s_cselect_b32 s54, 0, s54\ns_sub_u32 s54, s54, s51\nds_read_b128 v[48:51], v34 offset:24576\ns_waitcnt lgkmcnt(3)\n"
"v_mfma_f32_16x16x32_bf16 v[72:75], v[52:55], v[216:219], v[72:75]\nv_mfma_f32_16x16x32_bf16 v[76:79], v[52:55], v[220:223], v[76:79]\nds_read_b128 v[52:55], v35 offset:24576\ns_waitcnt lgkmcnt(3)\n"
"v_mfma_f32_16x16x32_bf16 v[64:67], v[40:43], v[224:227], v[64:67]\nv_add_u32 v32, s54, v32\nv_mfma_f32_16x16x32_bf16 v[68:71], v[40:43], v[228:231], v[68:71]\nds_read_b128 v[40:43], v32 offset:0\n"
"s_waitcnt lgkmcnt(3)\nv_mfma_f32_16x16x32_bf16 v[72:75], v[44:47], v[224:227], v[72:75]\nv_add_u32 v33, s54, v33\nv_mfma_f32_16x16x32_bf16 v[76:79], v[44:47], v[228:231], v[76:79]\n"
"ds_read_b128 v[44:47], v33 offset:0\ns_waitcnt lgkmcnt(3)\nv_mfma_f32_16x16x32_bf16 v[64:67], v[48:51], v[232:235], v[64:67]\nv_add_u32 v34, s54, v34\n"
"v_mfma_f32_16x16x32_bf16 v[68:71], v[48:51], v[236:239], v[68:71]\nds_read_b128 v[48:51], v34 offset:0\ns_waitcnt lgkmcnt(3)\nv_mfma_f32_16x16x32_bf16 v[72:75], v[52:55], v[232:235], v[72:75]\n"
"v_add_u32 v35, s54, v35\nv_mfma_f32_16x16x32_bf16 v[76:79], v[52:55], v[236:239], v[76:79]\nds_read_b128 v[52:55], v35 offset:0\ns_mov_b32 s52, s47\n"
"s_add_u32 s47, s51, s32\ns_add_u32 s51, s51, s54\ns_add_u32 s49, s49, 28672\ns_waitcnt vmcnt(7)\n"
"s_barrier\ns_waitcnt lgkmcnt(3)\nv_mfma_f32_16x16x32_bf16 v[96:99], v[40:43], v[128:131], 0\nv_mov_b32 v36, s48\n"
"s_add_u32 s48, s48, 1\ns_nop 0\nbuffer_store_dword v36, v19, s[24:27], s33 offen sc1\nv_mfma_f32_16x16x32_bf16 v[100:103], v[40:43], v[132:135], 0\n"
"ds_read_b128 v[40:43], v32 offset:4096\ns_waitcnt lgkmcnt(3)\nv_mfma_f32_16x16x32_bf16 v[104:107], v[44:47], v[128:131], 0\nv_mfma_f32_16x16x32_bf16 v[108:111], v[44:47], v[132:135], 0\n"
"ds_read_b128 v[44:47], v33 offset:4096\nv_cvt_pk_bf16_f32 v56, v64, v65\ns_waitcnt lgkmcnt(3)\nv_mfma_f32_16x16x32_bf16 v[96:99], v[48:51], v[136:139], v[96:99]\n"
"v_cvt_pk_bf16_f32 v57, v66, v67\nv_mfma_f32_16x16x32_bf16 v[100:103], v[48:51], v[140:143], v[100:103]\nds_read_b128 v[48:51], v34 offset:4096\nv_cvt_pk_bf16_f32 v58, v72, v73\n"
"s_waitcnt lgkmcnt(3)\nv_mfma_f32_16x16x32_bf16 v[104:107], v[52:55], v[136:139], v[104:107]\nv_cvt_pk_bf16_f32 v59, v74, v75\nv_mfma_f32_16x16x32_bf16 v[108:111], v[52:55], v[140:143], v[108:111]\n"
"ds_read_b128 v[52:55], v35 offset:4096\ns_waitcnt lgkmcnt(3)\nv_mfma_f32_16x16x32_bf16 v[96:99], v[40:43], v[144:147], v[96:99]\nv_mfma_f32_16x16x32_bf16 v[100:103], v[40:43], v[148:151], v[100:103]\n"
"ds_read_b128 v[40:43], v32 offset:8192\nbuffer_store_dwordx4 v[56:59], v15, s[20:23], s50 offen sc1\ns_waitcnt lgkmcnt(3)\nv_mfma_f32_16x16x32_bf16 v[104:107], v[44:47], v[144:147], v[104:107]\n"
"v_cvt_pk_bf16_f32 v60, v68, v69\nv_mfma_f32_16x16x32_bf16 v[108:111], v[44:47], v[148:151], v[108:111]\nds_read_b128 v[44:47], v33 offset:8192\nv_cvt_pk_bf16_f32 v61, v70, v71\n"
"s_waitcnt lgkmcnt(3)\nv_mfma_f32_16x16x32_bf16 v[96:99], v[48:51], v[152:155], v[96:99]\nv_cvt_pk_bf16_f32 v62, v76, v77\nv_mfma_f32_16x16x32_bf16 v[100:103], v[48:51], v[156:159], v[100:103]\n"
"ds_read_b128 v[48:51], v34 offset:8192\nv_cvt_pk_bf16_f32 v63, v78, v79\ns_waitcnt lgkmcnt(3)\nv_mfma_f32_16x16x32_bf16 v[104:107], v[52:55], v[152:155], v[104:107]\n"
"v_mfma_f32_16x16x32_bf16 v[108:111], v[52:55], v[156:159], v[108:111]\nds_read_b128 v[52:55], v35 offset:8192\ns_waitcnt lgkmcnt(3)\nv_mfma_f32_16x16x32_bf16 v[96:99], v[40:43], v[160:163], v[96:99]\n"
"buffer_store_dwordx4 v[60:63], v16, s[20:23], s50 offen sc1\nv_mfma_f32_16x16x32_bf16 v[100:103], v[40:43], v[164:167], v[100:103]\nds_read_b128 v[40:43], v32 offset:12288\ns_add_u32 s50, s50, s36\n"
"s_waitcnt lgkmcnt(3)\nv_mfma_f32_16x16x32_bf16 v[104:107], v[44:47], v[160:163], v[104:107]\ns_add_u32 m0, s52, 0\nv_mfma_f32_16x16x32_bf16 v[108:111], v[44:47], v[164:167], v[108:111]\n"
"ds_read_b128 v[44:47], v33 offset:12288\ns_add_u32 s53, s49, 0\ns_waitcnt lgkmcnt(3)\nv_mfma_f32_16x16x32_bf16 v[96:99], v[48:51], v[168:171], v[96:99]\n"
"buffer_load_dwordx4 v10, s[12:15], s53 offen lds\nv_mfma_f32_16x16x32_bf16 v[100:103], v[48:51], v[172:175], v[100:103]\nds_read_b128 v[48:51], v34 offset:12288\ns_add_u32 m0, s52, 4096\n"
"s_waitcnt lgkmcnt(3)\nv_mfma_f32_16x16x32_bf16 v[104:107], v[52:55], v[168:171], v[104:107]\ns_add_u32 s53, s49, 128\nv_mfma_f32_16x16x32_bf16 v[108:111], v[52:55], v[172:175], v[108:111]\n"
"ds_read_b128 v[52:55], v35 offset:12288\nbuffer_load_dwordx4 v10, s[12:15], s53 offen lds\ns_waitcnt lgkmcnt(3)\nv_mfma_f32_16x16x32_bf16 v[96:99], v[40:43], v[176:179], v[96:99]\n"
"s_add_u32 m0, s52, 8192\nv_mfma_f32_16x16x32_bf16 v[100:103], v[40:43], v[180:183], v[100:103]\nds_read_b128 v[40:43], v32 offset:16384\ns_add_u32 s53, s49, 256\n"
"s_waitcnt lgkmcnt(3)\nv_mfma_f32_16x16x32_bf16 v[104:107], v[44:47], v[176:179], v[104:107]\nbuffer_load_dwordx4 v10, s[12:15], s53 offen lds\nv_mfma_f32_16x16x32_bf16 v[108:111], v[44:47], v[180:183], v[108:111]\n"
"ds_read_b128 v[44:47], v33 offset:16384\ns_add_u32 m0, s52, 12288\ns_waitcnt lgkmcnt(3)\nv_mfma_f32_16x16x32_bf16 v[96:99], v[48:51], v[184:187], v[96:99]\n"
"s_add_u32 s53, s49, 384\nv_mfma_f32_16x16x32_bf16 v[100:103], v[48:51], v[188:191], v[100:103]\nds_read_b128 v[48:51], v34 offset:16384\nbuffer_load_dwordx4 v10, s[12:15], s53 offen lds\n"
"s_waitcnt lgkmcnt(3)\nv_mfma_f32_16x16x32_bf16 v[104:107], v[52:55], v[184:187], v[104:107]\ns_add_u32 m0, s52, 16384\nv_mfma_f32_16x16x32_bf16 v[108:111], v[52:55], v[188:191], v[108:111]\n"
"ds_read_b128 v[52:55], v35 offset:16384\ns_add_u32 s53, s49, 512\ns_waitcnt lgkmcnt(3)\nv_mfma_f32_16x16x32_bf16 v[96:99], v[40:43], v[192:195], v[96:99]\n"
"buffer_load_dwordx4 v10, s[12:15], s53 offen lds\nv_mfma_f32_16x16x32_bf16 v[100:103], v[40:43], v[196:199], v[100:103]\nds_read_b128 v[40:43], v32 offset:20480\ns_add_u32 m0, s52, 20480\n"
"s_waitcnt lgkmcnt(3)\nv_mfma_f32_16x16x32_bf16 v[104:107], v[44:47], v[192:195], v[104:107]\ns_add_u32 s53, s49, 640\nv_mfma_f32_16x16x32_bf16 v[108:111], v[44:47], v[196:199], v[108:111]\n"
"ds_read_b128 v[44:47], v33 offset:20480\nbuffer_load_dwordx4 v10, s[12:15], s53 offen lds\ns_waitcnt lgkmcnt(3)\nv_mfma_f32_16x16x32_bf16 v[96:99], v[48:51], v[200:203], v[96:99]\n"
"s_add_u32 m0, s52, 24576\nv_mfma_f32_16x16x32_bf16 v[100:103], v[48:51], v[204:207], v[100:103]\nds_read_b128 v[48:51], v34 offset:20480\ns_add_u32 s53, s49, 768\n"
"s_waitcnt lgkmcnt(3)\nv_mfma_f32_16x16x32_bf16 v[104:107], v[52:55], v[200:203], v[104:107]\nbuffer_load_dwordx4 v10, s[12:15], s53 offen lds\nv_mfma_f32_16x16x32_bf16 v[108:111], v[52:55], v[204:207], v[108:111]\n"
"ds_read_b128 v[52:55], v35 offset:20480\ns_waitcnt lgkmcnt(3)\nv_mfma_f32_16x16x32_bf16 v[96:99], v[40:43], v[208:211], v[96:99]\nv_mfma_f32_16x16x32_bf16 v[100:103], v[40:43], v[212:215], v[100:103]\n"
"ds_read_b128 v[40:43], v32 offset:24576\ns_waitcnt lgkmcnt(3)\nv_mfma_f32_16x16x32_bf16 v[104:107], v[44:47], v[208:211], v[104:107]\nv_mfma_f32_16x16x32_bf16 v[108:111], v[44:47], v[212:215], v[108:111]\n"
"ds_read_b128 v[44:47], v33 offset:24576\ns_waitcnt lgkmcnt(3)\nv_mfma_f32_16x16x32_bf16 v[96:99], v[48:51], v[216:219], v[96:99]\ns_add_u32 s54, s51, 32768\n"
"s_cmp_eq_u32 s54, 163840\nv_mfma_f32_16x16x32_bf16 v[100:103], v[48:51], v[220:223], v[100:103]\ns_cselect_b32 s54, 0, s54\ns_sub_u32 s54, s54, s51\n"
"ds_read_b128 v[48:51], v34 offset:24576\ns_waitcnt lgkmcnt(3)\nv_mfma_f32_16x16x32_bf16 v[104:107], v[52:55], v[216:219], v[104:107]\nv_mfma_f32_16x16x32_bf16 v[108:111], v[52:55], v[220:223], v[108:111]\n"
"ds_read_b128 v[52:55], v35 offset:24576\ns_waitcnt lgkmcnt(3)\nv_mfma_f32_16x16x32_bf16 v[96:99], v[40:43], v[224:227], v[96:99]\nv_add_u32 v32, s54, v32\n"
"v_mfma_f32_16x16x32_bf16 v[100:103], v[40:43], v[228:231], v[100:103]\nds_read_b128 v[40:43], v32 offset:0\ns_waitcnt lgkmcnt(3)\nv_mfma_f32_16x16x32_bf16 v[104:107], v[44:47], v[224:227], v[104:107]\n"
"v_add_u32 v33, s54, v33\nv_mfma_f32_16x16x32_bf16 v[108:111], v[44:47], v[228:231], v[108:111]\nds_read_b128 v[44:47], v33 offset:0\ns_waitcnt lgkmcnt(3)\n"
"v_mfma_f32_16x16x32_bf16 v[96:99], v[48:51], v[232:235], v[96:99]\nv_add_u32 v34, s54, v34\nv_mfma_f32_16x16x32_bf16 v[100:103], v[48:51], v[236:239], v[100:103]\nds_read_b128 v[48:51], v34 offset:0\n"
"s_waitcnt lgkmcnt(3)\nv_mfma_f32_16x16x32_bf16 v[104:107], v[52:55], v[232:235], v[104:107]\nv_add_u32 v35, s54, v35\nv_mfma_f32_16x16x32_bf16 v[108:111], v[52:55], v[236:239], v[108:111]\n"
"ds_read_b128 v[52:55], v35 offset:0\ns_mov_b32 s52, s47\ns_add_u32 s47, s51, s32\ns_add_u32 s51, s51, s54\n"
"s_add_u32 s49, s49, 28672\ns_waitcnt vmcnt(7)\ns_barrier\ns_waitcnt lgkmcnt(3)\n"
"v_mfma_f32_16x16x32_bf16 v[64:67], v[40:43], v[128:131], 0\nv_mov_b32 v36, s48\ns_add_u32 s48, s48, 1\ns_nop 0\n"
"buffer_store_dword v36, v19, s[24:27], s33 offen sc1\nv_mfma_f32_16x16x32_bf16 v[68:71], v[40:43], v[132:135], 0\nds_read_b128 v[40:43], v32 offset:4096\ns_waitcnt lgkmcnt(3)\n"
"v_mfma_f32_16x16x32_bf16 v[72:75], v[44:47], v[128:131], 0\nv_mfma_f32_16x16x32_bf16 v[76:79], v[44:47], v[132:135], 0\nds_read_b128 v[44:47], v33 offset:4096\nv_cvt_pk_bf16_f32 v56, v96, v97\n"
"s_waitcnt lgkmcnt(3)\nv_mfma_f32_16x16x32_bf16 v[64:67], v[48:51], v[136:139], v[64:67]\nv_cvt_pk_bf16_f32 v57, v98, v99\nv_mfma_f32_16x16x32_bf16 v[68:71], v[48:51], v[140:143], v[68:71]\n"
"ds_read_b128 v[48:51], v34 offset:4096\nv_cvt_pk_bf16_f32 v58, v104, v105\ns_waitcnt lgkmcnt(3)\nv_mfma_f32_16x16x32_bf16 v[72:75], v[52:55], v[136:139], v[72:75]\n"
"v_cvt_pk_bf16_f32 v59, v106, v107\nv_mfma_f32_16x16x32_bf16 v[76:79], v[52:55], v[140:143], v[76:79]\nds_read_b128 v[52:55], v35 offset:4096\ns_waitcnt lgkmcnt(3)\n"
"v_mfma_f32_16x16x32_bf16 v[64:67], v[40:43], v[144:147], v[64:67]\nv_mfma_f32_16x16x32_bf16 v[68:71], v[40:43], v[148:151], v[68:71]\nds_read_b128 v[40:43], v32 offset:8192\nbuffer_store_dwordx4 v[56:59], v15, s[20:23], s50 offen sc1\n"
"s_waitcnt lgkmcnt(3)\nv_mfma_f32_16x16x32_bf16 v[72:75], v[44:47], v[144:147], v[72:75]\nv_cvt_pk_bf16_f32 v60, v100, v101\nv_mfma_f32_16x16x32_bf16 v[76:79], v[44:47], v[148:151], v[76:79]\n"
"ds_read_b128 v[44:47], v33 offset:8192\nv_cvt_pk_bf16_f32 v61, v102, v103\ns_waitcnt lgkmcnt(3)\nv_mfma_f32_16x16x32_bf16 v[64:67], v[48:51], v[152:155], v[64:67]\n"
"v_cvt_pk_bf16_f32 v62, v108, v109\nv_mfma_f32_16x16x32_bf16 v[68:71], v[48:51], v[156:159], v[68:71]\nds_read_b128 v[48:51], v34 offset:8192\nv_cvt_pk_bf16_f32 v63, v110, v111\n"
"s_waitcnt lgkmcnt(3)\nv_mfma_f32_16x16x32_bf16 v[72:75], v[52:55], v[152:155], v[72:75]\nv_mfma_f32_16x16x32_bf16 v[76:79], v[52:55], v[156:159], v[76:79]\nds_read_b128 v[52:55], v35 offset:8192\n"
"s_waitcnt lgkmcnt(3)\nv_mfma_f32_16x16x32_bf16 v[64:67], v[40:43], v[160:163], v[64:67]\nbuffer_store_dwordx4 v[60:63], v16, s[20:23], s50 offen sc1\nv_mfma_f32_16x16x32_bf16 v[68:71], v[40:43], v[164:167], v[68:71]\n"
"ds_read_b128 v[40:43], v32 offset:12288\ns_add_u32 s50, s50, s36\ns_waitcnt lgkmcnt(3)\nv_mfma_f32_16x16x32_bf16 v[72:75], v[44:47], v[160:163], v[72:75]\n"
"s_add_u32 m0, s52, 0\nv_mfma_f32_16x16x32_bf16 v[76:79], v[44:47], v[164:167], v[76:79]\nds_read_b128 v[44:47], v33 offset:12288\ns_add_u32 s53, s49, 0\n"
"s_waitcnt lgkmcnt(3)\nv_mfma_f32_16x16x32_bf16 v[64:67], v[48:51], v[168:171], v[64:67]\nbuffer_load_dwordx4 v10, s[12:15], s53 offen lds\nv_mfma_f32_16x16x32_bf16 v[68:71], v[48:51], v[172:175], v[68:71]\n"
"ds_read_b128 v[48:51], v34 offset:12288\ns_add_u32 m0, s52, 4096\ns_waitcnt lgkmcnt(3)\nv_mfma_f32_16x16x32_bf16 v[72:75], v[52:55], v[168:171], v[72:75]\n"
"s_add_u32 s53, s49, 128\nv_mfma_f32_16x16x32_bf16 v[76:79], v[52:55], v[172:175], v[76:79]\nds_read_b128 v[52:55], v35 offset:12288\nbuffer_load_dwordx4 v10, s[12:15], s53 offen lds\n"
"s_waitcnt lgkmcnt(3)\nv_mfma_f32_16x16x32_bf16 v[64:67], v[40:43], v[176:179], v[64:67]\ns_add_u32 m0, s52, 8192\nv_mfma_f32_16x16x32_bf16 v[68:71], v[40:43], v[180:183], v[68:71]\n"
"ds_read_b128 v[40:43], v32 offset:16384\ns_add_u32 s53, s49, 256\ns_waitcnt lgkmcnt(3)\nv_mfma_f32_16x16x32_bf16 v[72:75], v[44:47], v[176:179], v[72:75]\n"
"buffer_load_dwordx4 v10, s[12:15], s53 offen lds\nv_mfma_f32_16x16x32_bf16 v[76:79], v[44:47], v[180:183], v[76:79]\nds_read_b128 v[44:47], v33 offset:16384\ns_add_u32 m0, s52, 12288\n"
"s_waitcnt lgkmcnt(3)\nv_mfma_f32_16x16x32_bf16 v[64:67], v[48:51], v[184:187], v[64:67]\ns_add_u32 s53, s49, 384\nv_mfma_f32_16x16x32_bf16 v[68:71], v[48:51], v[188:191], v[68:71]\n"
"ds_read_b128 v[48:51], v34 offset:16384\nbuffer_load_dwordx4 v10, s[12:15], s53 offen lds\ns_waitcnt lgkmcnt(3)\nv_mfma_f32_16x16x32_bf16 v[72:75], v[52:55], v[184:187], v[72:75]\n"
"s_add_u32 m0, s52, 16384\nv_mfma_f32_16x16x32_bf16 v[76:79], v[52:55], v[188:191], v[76:79]\nds_read_b128 v[52:55], v35 offset:16384\ns_add_u32 s53, s49, 512\n"
"s_waitcnt lgkmcnt(3)\nv_mfma_f32_16x16x32_bf16 v[64:67], v[40:43], v[192:195], v[64:67]\nbuffer_load_dwordx4 v10, s[12:15], s53 offen lds\nv_mfma_f32_16x16x32_bf16 v[68:71], v[40:43], v[196:199], v[68:71]\n"
"ds_read_b128 v[40:43], v32 offset:20480\ns_add_u32 m0, s52, 20480\ns_waitcnt lgkmcnt(3)\nv_mfma_f32_16x16x32_bf16 v[72:75], v[44:47], v[192:195], v[72:75]\n"
"s_add_u32 s53, s49, 640\nv_mfma_f32_16x16x32_bf16 v[76:79], v[44:47], v[196:199], v[76:79]\nds_read_b128 v[44:47], v33 offset:20480\nbuffer_load_dwordx4 v10, s[12:15], s53 offen lds\n"
"s_waitcnt lgkmcnt(3)\nv_mfma_f32_16x16x32_bf16 v[64:67], v[48:51], v[200:203], v[64:67]\ns_add_u32 m0, s52, 24576\nv_mfma_f32_16x16x32_bf16 v[68:71], v[48:51], v[204:207], v[68:71]\n"
"ds_read_b128 v[48:51], v34 offset:20480\ns_add_u32 s53, s49, 768\ns_waitcnt lgkmcnt(3)\nv_mfma_f32_16x16x32_bf16 v[72:75], v[52:55], v[200:203], v[72:75]\n"
"buffer_load_dwordx4 v10, s[12:15], s53 offen lds\nv_mfma_f32_16x16x32_bf16 v[76:79], v[52:55], v[204:207], v[76:79]\nds_read_b128 v[52:55], v35 offset:20480\ns_waitcnt lgkmcnt(3)\n"
"v_mfma_f32_16x16x32_bf16 v[64:67], v[40:43], v[208:211], v[64:67]\nv_mfma_f32_16x16x32_bf16 v[68:71], v[40:43], v[212:215], v[68:71]\nds_read_b128 v[40:43], v32 offset:24576\ns_waitcnt lgkmcnt(3)\n"
"v_mfma_f32_16x16x32_bf16 v[72:75], v[44:47], v[208:211], v[72:75]\nv_mfma_f32_16x16x32_bf16 v[76:79], v[44:47], v[212:215], v[76:79]\nds_read_b128 v[44:47], v33 offset:24576\ns_waitcnt lgkmcnt(3)\n"
"v_mfma_f32_16x16x32_bf16 v[64:67], v[48:51], v[216:219], v[64:67]\ns_add_u32 s54, s51, 32768\ns_cmp_eq_u32 s54, 163840\nv_mfma_f32_16x16x32_bf16 v[68:71], v[48:51], v[220:223], v[68:71]\n"
"s_cselect_b32 s54, 0, s54\ns_sub_u32 s54, s54, s51\nds_read_b128 v[48:51], v34 offset:24576\ns_waitcnt lgkmcnt(3)\n"
"v_mfma_f32_16x16x32_bf16 v[72:75], v[52:55], v[216:219], v[72:75]\nv_mfma_f32_16x16x32_bf16 v[76:79], v[52:55], v[220:223], v[76:79]\nds_read_b128 v[52:55], v35 offset:24576\ns_waitcnt lgkmcnt(3)\n"
"v_mfma_f32_16x16x32_bf16 v[64:67], v[40:43], v[224:227], v[64:67]\nv_add_u32 v32, s54, v32\nv_mfma_f32_16x16x32_bf16 v[68:71], v[40:43], v[228:231], v[68:71]\nds_read_b128 v[40:43], v32 offset:0\n"
"s_waitcnt lgkmcnt(3)\nv_mfma_f32_16x16x32_bf16 v[72:75], v[44:47], v[224:227], v[72:75]\nv_add_u32 v33, s54, v33\nv_mfma_f32_16x16x32_bf16 v[76:79], v[44:47], v[228:231], v[76:79]\n"
"ds_read_b128 v[44:47], v33 offset:0\ns_waitcnt lgkmcnt(3)\nv_mfma_f32_16x16x32_bf16 v[64:67], v[48:51], v[232:235], v[64:67]\nv_add_u32 v34, s54, v34\n"
"v_mfma_f32_16x16x32_bf16 v[68:71], v[48:51], v[236:239], v[68:71]\nds_read_b128 v[48:51], v34 offset:0\ns_waitcnt lgkmcnt(3)\nv_mfma_f32_16x16x32_bf16 v[72:75], v[52:55], v[232:235], v[72:75]\n"
"v_add_u32 v35, s54, v35\nv_mfma_f32_16x16x32_bf16 v[76:79], v[52:55], v[236:239], v[76:79]\nds_read_b128 v[52:55], v35 offset:0\ns_mov_b32 s52, s47\n"
"s_add_u32 s47, s51, s32\ns_add_u32 s51, s51, s54\ns_add_u32 s49, s49, 28672\ns_waitcnt vmcnt(7)\n"
"s_barrier\ns_waitcnt lgkmcnt(3)\nv_mfma_f32_16x16x32_bf16 v[96:99], v[40:43], v[128:131], 0\nv_mov_b32 v36, s48\n"
"s_add_u32 s48, s48, 1\ns_nop 0\nbuffer_store_dword v36, v19, s[24:27], s33 offen sc1\nbuffer_load_dword v37, v20, s[24:27], 0 offen sc1\n"
"v_mfma_f32_16x16x32_bf16 v[100:103], v[40:43], v[132:135], 0\nds_read_b128 v[40:43], v32 offset:4096\ns_waitcnt lgkmcnt(3)\nv_mfma_f32_16x16x32_bf16 v[104:107], v[44:47], v[128:131], 0\n"
"v_mfma_f32_16x16x32_bf16 v[108:111], v[44:47], v[132:135], 0\nds_read_b128 v[44:47], v33 offset:4096\nv_cvt_pk_bf16_f32 v56, v64, v65\ns_waitcnt lgkmcnt(3)\n"
"v_mfma_f32_16x16x32_bf16 v[96:99], v[48:51], v[136:139], v[96:99]\nv_cvt_pk_bf16_f32 v57, v66, v67\nv_mfma_f32_16x16x32_bf16 v[100:103], v[48:51], v[140:143], v[100:103]\nds_read_b128 v[48:51], v34 offset:4096\n"
"v_cvt_pk_bf16_f32 v58, v72, v73\ns_waitcnt lgkmcnt(3)\nv_mfma_f32_16x16x32_bf16 v[104:107], v[52:55], v[136:139], v[104:107]\nv_cvt_pk_bf16_f32 v59, v74, v75\n"
"v_mfma_f32_16x16x32_bf16 v[108:111], v[52:55], v[140:143], v[108:111]\nds_read_b128 v[52:55], v35 offset:4096\ns_waitcnt lgkmcnt(3)\nv_mfma_f32_16x16x32_bf16 v[96:99], v[40:43], v[144:147], v[96:99]\n"
"v_mfma_f32_16x16x32_bf16 v[100:103], v[40:43], v[148:151], v[100:103]\nds_read_b128 v[40:43], v32 offset:8192\nbuffer_store_dwordx4 v[56:59], v15, s[20:23], s50 offen sc1\ns_waitcnt lgkmcnt(3)\n"
"v_mfma_f32_16x16x32_bf16 v[104:107], v[44:47], v[144:147], v[104:107]\nv_cvt_pk_bf16_f32 v60, v68, v69\nv_mfma_f32_16x16x32_bf16 v[108:111], v[44:47], v[148:151], v[108:111]\nds_read_b128 v[44:47], v33 offset:8192\n"
"v_cvt_pk_bf16_f32 v61, v70, v71\ns_waitcnt lgkmcnt(3)\nv_mfma_f32_16x16x32_bf16 v[96:99], v[48:51], v[152:155], v[96:99]\nv_cvt_pk_bf16_f32 v62, v76, v77\n"
"v_mfma_f32_16x16x32_bf16 v[100:103], v[48:51], v[156:159], v[100:103]\nds_read_b128 v[48:51], v34 offset:8192\nv_cvt_pk_bf16_f32 v63, v78, v79\ns_waitcnt lgkmcnt(3)\n"
"v_mfma_f32_16x16x32_bf16 v[104:107], v[52:55], v[152:155], v[104:107]\nv_mfma_f32_16x16x32_bf16 v[108:111], v[52:55], v[156:159], v[108:111]\nds_read_b128 v[52:55], v35 offset:8192\ns_waitcnt lgkmcnt(3)\n"
"v_mfma_f32_16x16x32_bf16 v[96:99], v[40:43], v[160:163], v[96:99]\nbuffer_store_dwordx4 v[60:63], v16, s[20:23], s50 offen sc1\nv_mfma_f32_16x16x32_bf16 v[100:103], v[40:43], v[164:167], v[100:103]\nds_read_b128 v[40:43], v32 offset:12288\n"
"s_add_u32 s50, s50, s36\ns_waitcnt lgkmcnt(3)\nv_mfma_f32_16x16x32_bf16 v[104:107], v[44:47], v[160:163], v[104:107]\ns_add_u32 m0, s52, 0\n"
"v_mfma_f32_16x16x32_bf16 v[108:111], v[44:47], v[164:167], v[108:111]\nds_read_b128 v[44:47], v33 offset:12288\ns_add_u32 s53, s49, 0\ns_waitcnt lgkmcnt(3)\n"
"v_mfma_f32_16x16x32_bf16 v[96:99], v[48:51], v[168:171], v[96:99]\nbuffer_load_dwordx4 v10, s[12:15], s53 offen lds\nv_mfma_f32_16x16x32_bf16 v[100:103], v[48:51], v[172:175], v[100:103]\nds_read_b128 v[48:51], v34 offset:12288\n"
"s_add_u32 m0, s52, 4096\ns_waitcnt lgkmcnt(3)\nv_mfma_f32_16x16x32_bf16 v[104:107], v[52:55], v[168:171], v[104:107]\ns_add_u32 s53, s49, 128\n"
"v_mfma_f32_16x16x32_bf16 v[108:111], v[52:55], v[172:175], v[108:111]\nds_read_b128 v[52:55], v35 offset:12288\nbuffer_load_dwordx4 v10, s[12:15], s53 offen lds\ns_waitcnt lgkmcnt(3)\n"
"v_mfma_f32_16x16x32_bf16 v[96:99], v[40:43], v[176:179], v[96:99]\ns_add_u32 m0, s52, 8192\nv_mfma_f32_16x16x32_bf16 v[100:103], v[40:43], v[180:183], v[100:103]\nds_read_b128 v[40:43], v32 offset:16384\n"
"s_add_u32 s53, s49, 256\ns_waitcnt lgkmcnt(3)\nv_mfma_f32_16x16x32_bf16 v[104:107], v[44:47], v[176:179], v[104:107]\nbuffer_load_dwordx4 v10, s[12:15], s53 offen lds\n"
"v_mfma_f32_16x16x32_bf16 v[108:111], v[44:47], v[180:183], v[108:111]\nds_read_b128 v[44:47], v33 offset:16384\ns_add_u32 m0, s52, 12288\ns_waitcnt lgkmcnt(3)\n"
"v_mfma_f32_16x16x32_bf16 v[96:99], v[48:51], v[184:187], v[96:99]\ns_add_u32 s53, s49, 384\nv_mfma_f32_16x16x32_bf16 v[100:103], v[48:51], v[188:191], v[100:103]\nds_read_b128 v[48:51], v34 offset:16384\n"
"buffer_load_dwordx4 v10, s[12:15], s53 offen lds\ns_waitcnt lgkmcnt(3)\nv_mfma_f32_16x16x32_bf16 v[104:107], v[52:55], v[184:187], v[104:107]\ns_add_u32 m0, s52, 16384\n"
"v_mfma_f32_16x16x32_bf16 v[108:111], v[52:55], v[188:191], v[108:111]\nds_read_b128 v[52:55], v35 offset:16384\ns_add_u32 s53, s49, 512\ns_waitcnt lgkmcnt(3)\n"
"v_mfma_f32_16x16x32_bf16 v[96:99], v[40:43], v[192:195], v[96:99]\nbuffer_load_dwordx4 v10, s[12:15], s53 offen lds\nv_mfma_f32_16x16x32_bf16 v[100:103], v[40:43], v[196:199], v[100:103]\nds_read_b128 v[40:43], v32 offset:20480\n"
"s_add_u32 m0, s52, 20480\ns_waitcnt lgkmcnt(3)\nv_mfma_f32_16x16x32_bf16 v[104:107], v[44:47], v[192:195], v[104:107]\ns_add_u32 s53, s49, 640\n"
"v_mfma_f32_16x16x32_bf16 v[108:111], v[44:47], v[196:199], v[108:111]\nds_read_b128 v[44:47], v33 offset:20480\nbuffer_load_dwordx4 v10, s[12:15], s53 offen lds\ns_waitcnt lgkmcnt(3)\n"
"v_mfma_f32_16x16x32_bf16 v[96:99], v[48:51], v[200:203], v[96:99]\ns_add_u32 m0, s52, 24576\nv_mfma_f32_16x16x32_bf16 v[100:103], v[48:51], v[204:207], v[100:103]\nds_read_b128 v[48:51], v34 offset:20480\n"
"s_add_u32 s53, s49, 768\ns_waitcnt lgkmcnt(3)\nv_mfma_f32_16x16x32_bf16 v[104:107], v[52:55], v[200:203], v[104:107]\nbuffer_load_dwordx4 v10, s[12:15], s53 offen lds\n"
"v_mfma_f32_16x16x32_bf16 v[108:111], v[52:55], v[204:207], v[108:111]\nds_read_b128 v[52:55], v35 offset:20480\ns_waitcnt lgkmcnt(3)\nv_mfma_f32_16x16x32_bf16 v[96:99], v[40:43], v[208:211], v[96:99]\n"
"v_mfma_f32_16x16x32_bf16 v[100:103], v[40:43], v[212:215], v[100:103]\nds_read_b128 v[40:43], v32 offset:24576\ns_waitcnt lgkmcnt(3)\nv_mfma_f32_16x16x32_bf16 v[104:107], v[44:47], v[208:211], v[104:107]\n"
"v_mfma_f32_16x16x32_bf16 v[108:111], v[44:47], v[212:215], v[108:111]\nds_read_b128 v[44:47], v33 offset:24576\ns_waitcnt lgkmcnt(3)\nv_mfma_f32_16x16x32_bf16 v[96:99], v[48:51], v[216:219], v[96:99]\n"
"s_add_u32 s54, s51, 32768\ns_cmp_eq_u32 s54, 163840\nv_mfma_f32_16x16x32_bf16 v[100:103], v[48:51], v[220:223], v[100:103]\ns_cselect_b32 s54, 0, s54\n"
"s_sub_u32 s54, s54, s51\nds_read_b128 v[48:51], v34 offset:24576\ns_waitcnt lgkmcnt(3)\nv_mfma_f32_16x16x32_bf16 v[104:107], v[52:55], v[216:219], v[104:107]\n"
"v_mfma_f32_16x16x32_bf16 v[108:111], v[52:55], v[220:223], v[108:111]\nds_read_b128 v[52:55], v35 offset:24576\ns_waitcnt lgkmcnt(3)\nv_mfma_f32_16x16x32_bf16 v[96:99], v[40:43], v[224:227], v[96:99]\n"
"v_add_u32 v32, s54, v32\nv_mfma_f32_16x16x32_bf16 v[100:103], v[40:43], v[228:231], v[100:103]\nds_read_b128 v[40:43], v32 offset:0\ns_waitcnt lgkmcnt(3)\n"
"v_mfma_f32_16x16x32_bf16 v[104:107], v[44:47], v[224:227], v[104:107]\nv_add_u32 v33, s54, v33\nv_mfma_f32_16x16x32_bf16 v[108:111], v[44:47], v[228:231], v[108:111]\nds_read_b128 v[44:47], v33 offset:0\n"
"s_waitcnt lgkmcnt(3)\nv_mfma_f32_16x16x32_bf16 v[96:99], v[48:51], v[232:235], v[96:99]\nv_add_u32 v34, s54, v34\nv_mfma_f32_16x16x32_bf16 v[100:103], v[48:51], v[236:239], v[100:103]\n"
"ds_read_b128 v[48:51], v34 offset:0\ns_waitcnt lgkmcnt(3)\nv_mfma_f32_16x16x32_bf16 v[104:107], v[52:55], v[232:235], v[104:107]\nv_add_u32 v35, s54, v35\n"
"v_mfma_f32_16x16x32_bf16 v[108:111], v[52:55], v[236:239], v[108:111]\nds_read_b128 v[52:55], v35 offset:0\ns_mov_b32 s52, s47\ns_add_u32 s47, s51, s32\n"
"s_add_u32 s51, s51, s54\ns_add_u32 s49, s49, 28672\ns_waitcnt vmcnt(7)\nfg1_pf:\n"
"v_cmp_ne_u32 vcc, s28, v37\ns_cbranch_vccz fg1_pfok\ns_add_u32 s55, s55, 1\ns_cmp_gt_u32 s55, 262144\n"
"s_cbranch_scc0 fg1_pf2\ns_endpgm\nfg1_pf2:\ns_sleep 1\n"
"buffer_load_dword v37, v20, s[24:27], 0 offen sc1\ns_waitcnt vmcnt(0)\ns_branch fg1_pf\nfg1_pfok:\n"
"s_barrier\ns_waitcnt lgkmcnt(3)\nv_mfma_f32_16x16x32_bf16 v[64:67], v[40:43], v[128:131], 0\nv_mov_b32 v36, s48\n"
"s_add_u32 s48, s48, 1\ns_nop 0\nbuffer_store_dword v36, v19, s[24:27], s33 offen sc1\nv_mfma_f32_16x16x32_bf16 v[68:71], v[40:43], v[132:135], 0\n"
"ds_read_b128 v[40:43], v32 offset:4096\ns_waitcnt lgkmcnt(3)\nv_mfma_f32_16x16x32_bf16 v[72:75], v[44:47], v[128:131], 0\nv_mfma_f32_16x16x32_bf16 v[76:79], v[44:47], v[132:135], 0\n"
"ds_read_b128 v[44:47], v33 offset:4096\nv_cvt_pk_bf16_f32 v56, v96, v97\ns_waitcnt lgkmcnt(3)\nv_mfma_f32_16x16x32_bf16 v[64:67], v[48:51], v[136:139], v[64:67]\n"
"v_cvt_pk_bf16_f32 v57, v98, v99\nv_mfma_f32_16x16x32_bf16 v[68:71], v[48:51], v[140:143], v[68:71]\nds_read_b128 v[48:51], v34 offset:4096\nv_cvt_pk_bf16_f32 v58, v104, v105\n"
"s_waitcnt lgkmcnt(3)\nv_mfma_f32_16x16x32_bf16 v[72:75], v[52:55], v[136:139], v[72:75]\nv_cvt_pk_bf16_f32 v59, v106, v107\nv_mfma_f32_16x16x32_bf16 v[76:79], v[52:55], v[140:143], v[76:79]\n"
"ds_read_b128 v[52:55], v35 offset:4096\ns_waitcnt lgkmcnt(3)\nv_mfma_f32_16x16x32_bf16 v[64:67], v[40:43], v[144:147], v[64:67]\nv_mfma_f32_16x16x32_bf16 v[68:71], v[40:43], v[148:151], v[68:71]\n"
"ds_read_b128 v[40:43], v32 offset:8192\nbuffer_store_dwordx4 v[56:59], v15, s[20:23], s50 offen sc1\ns_waitcnt lgkmcnt(3)\nv_mfma_f32_16x16x32_bf16 v[72:75], v[44:47], v[144:147], v[72:75]\n"
"v_cvt_pk_bf16_f32 v60, v100, v101\nv_mfma_f32_16x16x32_bf16 v[76:79], v[44:47], v[148:151], v[76:79]\nds_read_b128 v[44:47], v33 offset:8192\nv_cvt_pk_bf16_f32 v61, v102, v103\n"
"s_waitcnt lgkmcnt(3)\nv_mfma_f32_16x16x32_bf16 v[64:67], v[48:51], v[152:155], v[64:67]\nv_cvt_pk_bf16_f32 v62, v108, v109\nv_mfma_f32_16x16x32_bf16 v[68:71], v[48:51], v[156:159], v[68:71]\n"
"ds_read_b128 v[48:51], v34 offset:8192\nv_cvt_pk_bf16_f32 v63, v110, v111\ns_waitcnt lgkmcnt(3)\nv_mfma_f32_16x16x32_bf16 v[72:75], v[52:55], v[152:155], v[72:75]\n"
"v_mfma_f32_16x16x32_bf16 v[76:79], v[52:55], v[156:159], v[76:79]\nds_read_b128 v[52:55], v35 offset:8192\ns_waitcnt lgkmcnt(3)\nv_mfma_f32_16x16x32_bf16 v[64:67], v[40:43], v[160:163], v[64:67]\n"
"buffer_store_dwordx4 v[60:63], v16, s[20:23], s50 offen sc1\nv_mfma_f32_16x16x32_bf16 v[68:71], v[40:43], v[164:167], v[68:71]\nds_read_b128 v[40:43], v32 offset:12288\ns_add_u32 s50, s50, s36\n"
"s_waitcnt lgkmcnt(3)\nv_mfma_f32_16x16x32_bf16 v[72:75], v[44:47], v[160:163], v[72:75]\ns_add_u32 m0, s52, 0\nv_mfma_f32_16x16x32_bf16 v[76:79], v[44:47], v[164:167], v[76:79]\n"
"ds_read_b128 v[44:47], v33 offset:12288\ns_add_u32 s53, s49, 0\ns_waitcnt lgkmcnt(3)\nv_mfma_f32_16x16x32_bf16 v[64:67], v[48:51], v[168:171], v[64:67]\n"
"buffer_load_dwordx4 v10, s[12:15], s53 offen lds\nv_mfma_f32_16x16x32_bf16 v[68:71], v[48:51], v[172:175], v[68:71]\nds_read_b128 v[48:51], v34 offset:12288\ns_add_u32 m0, s52, 4096\n"
"s_waitcnt lgkmcnt(3)\nv_mfma_f32_16x16x32_bf16 v[72:75], v[52:55], v[168:171], v[72:75]\ns_add_u32 s53, s49, 128\nv_mfma_f32_16x16x32_bf16 v[76:79], v[52:55], v[172:175], v[76:79]\n"
"ds_read_b128 v[52:55], v35 offset:12288\nbuffer_load_dwordx4 v10, s[12:15], s53 offen lds\ns_waitcnt lgkmcnt(3)\nv_mfma_f32_16x16x32_bf16 v[64:67], v[40:43], v[176:179], v[64:67]\n"
"s_add_u32 m0, s52, 8192\nv_mfma_f32_16x16x32_bf16 v[68:71], v[40:43], v[180:183], v[68:71]\nds_read_b128 v[40:43], v32 offset:16384\ns_add_u32 s53, s49, 256\n"
"s_waitcnt lgkmcnt(3)\nv_mfma_f32_16x16x32_bf16 v[72:75], v[44:47], v[176:179], v[72:75]\nbuffer_load_dwordx4 v10, s[12:15], s53 offen lds\nv_mfma_f32_16x16x32_bf16 v[76:79], v[44:47], v[180:183], v[76:79]\n"
"ds_read_b128 v[44:47], v33 offset:16384\ns_add_u32 m0, s52, 12288\ns_waitcnt lgkmcnt(3)\nv_mfma_f32_16x16x32_bf16 v[64:67], v[48:51], v[184:187], v[64:67]\n"
"s_add_u32 s53, s49, 384\nv_mfma_f32_16x16x32_bf16 v[68:71], v[48:51], v[188:191], v[68:71]\nds_read_b128 v[48:51], v34 offset:16384\nbuffer_load_dwordx4 v10, s[12:15], s53 offen lds\n"
"s_waitcnt lgkmcnt(3)\nv_mfma_f32_16x16x32_bf16 v[72:75], v[52:55], v[184:187], v[72:75]\ns_add_u32 m0, s52, 16384\nv_mfma_f32_16x16x32_bf16 v[76:79], v[52:55], v[188:191], v[76:79]\n"
"ds_read_b128 v[52:55], v35 offset:16384\ns_add_u32 s53, s49, 512\ns_waitcnt lgkmcnt(3)\nv_mfma_f32_16x16x32_bf16 v[64:67], v[40:43], v[192:195], v[64:67]\n"
"buffer_load_dwordx4 v10, s[12:15], s53 offen lds\nv_mfma_f32_16x16x32_bf16 v[68:71], v[40:43], v[196:199], v[68:71]\nds_read_b128 v[40:43], v32 offset:20480\ns_add_u32 m0, s52, 20480\n"
"s_waitcnt lgkmcnt(3)\nv_mfma_f32_16x16x32_bf16 v[72:75], v[44:47], v[192:195], v[72:75]\ns_add_u32 s53, s49, 640\nv_mfma_f32_16x16x32_bf16 v[76:79], v[44:47], v[196:199], v[76:79]\n"
"ds_read_b128 v[44:47], v33 offset:20480\nbuffer_load_dwordx4 v10, s[12:15], s53 offen lds\ns_waitcnt lgkmcnt(3)\nv_mfma_f32_16x16x32_bf16 v[64:67], v[48:51], v[200:203], v[64:67]\n"
"s_add_u32 m0, s52, 24576\nv_mfma_f32_16x16x32_bf16 v[68:71], v[48:51], v[204:207], v[68:71]\nds_read_b128 v[48:51], v34 offset:20480\ns_add_u32 s53, s49, 768\n"
"s_waitcnt lgkmcnt(3)\nv_mfma_f32_16x16x32_bf16 v[72:75], v[52:55], v[200:203], v[72:75]\nbuffer_load_dwordx4 v10, s[12:15], s53 offen lds\nv_mfma_f32_16x16x32_bf16 v[76:79], v[52:55], v[204:207], v[76:79]\n"
"ds_read_b128 v[52:55], v35 offset:20480\ns_mov_b32 s53, 0\nbuffer_load_dwordx4 a[0:3], v14, s[16:19], s53 offen sc1\ns_waitcnt lgkmcnt(3)\n"
"v_mfma_f32_16x16x32_bf16 v[64:67], v[40:43], v[208:211], v[64:67]\ns_mov_b32 s53, 262144\nbuffer_load_dwordx4 a[4:7], v14, s[16:19], s53 offen sc1\nv_mfma_f32_16x16x32_bf16 v[68:71], v[40:43], v[212:215], v[68:71]\n"
"ds_read_b128 v[40:43], v32 offset:24576\ns_mov_b32 s53, 1024\nbuffer_load_dwordx4 a[8:11], v14, s[16:19], s53 offen sc1\ns_waitcnt lgkmcnt(3)\n"
"v_mfma_f32_16x16x32_bf16 v[72:75], v[44:47], v[208:211], v[72:75]\ns_mov_b32 s53, 263168\nbuffer_load_dwordx4 a[12:15], v14, s[16:19], s53 offen sc1\nv_mfma_f32_16x16x32_bf16 v[76:79], v[44:47], v[212:215], v[76:79]\n"
"ds_read_b128 v[44:47], v33 offset:24576\ns_mov_b32 s53, 2048\nbuffer_load_dwordx4 a[16:19], v14, s[16:19], s53 offen sc1\ns_waitcnt lgkmcnt(3)\n"
"v_mfma_f32_16x16x32_bf16 v[64:67], v[48:51], v[216:219], v[64:67]\ns_add_u32 s54, s51, 32768\ns_cmp_eq_u32 s54, 163840\nv_mfma_f32_16x16x32_bf16 v[68:71], v[48:51], v[220:223], v[68:71]\n"
"s_cselect_b32 s54, 0, s54\ns_sub_u32 s54, s54, s51\nds_read_b128 v[48:51], v34 offset:24576\ns_mov_b32 s53, 264192\n"
"buffer_load_dwordx4 a[20:23], v14, s[16:19], s53 offen sc1\ns_waitcnt lgkmcnt(3)\nv_mfma_f32_16x16x32_bf16 v[72:75], v[52:55], v[216:219], v[72:75]\ns_mov_b32 s53, 3072\n"
"buffer_load_dwordx4 a[24:27], v14, s[16:19], s53 offen sc1\nv_mfma_f32_16x16x32_bf16 v[76:79], v[52:55], v[220:223], v[76:79]\nds_read_b128 v[52:55], v35 offset:24576\ns_waitcnt lgkmcnt(3)\n"
"v_mfma_f32_16x16x32_bf16 v[64:67], v[40:43], v[224:227], v[64:67]\nv_add_u32 v32, s54, v32\nv_mfma_f32_16x16x32_bf16 v[68:71], v[40:43], v[228:231], v[68:71]\nds_read_b128 v[40:43], v32 offset:0\n"
"s_waitcnt lgkmcnt(3)\nv_mfma_f32_16x16x32_bf16 v[72:75], v[44:47], v[224:227], v[72:75]\nv_add_u32 v33, s54, v33\nv_mfma_f32_16x16x32_bf16 v[76:79], v[44:47], v[228:231], v[76:79]\n"
"ds_read_b128 v[44:47], v33 offset:0\ns_waitcnt lgkmcnt(3)\nv_mfma_f32_16x16x32_bf16 v[64:67], v[48:51], v[232:235], v[64:67]\nv_add_u32 v34, s54, v34\n"
"v_mfma_f32_16x16x32_bf16 v[68:71], v[48:51], v[236:239], v[68:71]\nds_read_b128 v[48:51], v34 offset:0\ns_waitcnt lgkmcnt(3)\nv_mfma_f32_16x16x32_bf16 v[72:75], v[52:55], v[232:235], v[72:75]\n"
"v_add_u32 v35, s54, v35\nv_mfma_f32_16x16x32_bf16 v[76:79], v[52:55], v[236:239], v[76:79]\nds_read_b128 v[52:55], v35 offset:0\ns_mov_b32 s52, s47\n"
"s_add_u32 s47, s51, s32\ns_add_u32 s51, s51, s54\ns_add_u32 s49, s49, 28672\ns_waitcnt vmcnt(14)\n"
"s_barrier\ns_waitcnt lgkmcnt(3)\nv_mfma_f32_16x16x32_bf16 v[96:99], v[40:43], v[128:131], 0\nv_mov_b32 v36, s48\n"
"s_add_u32 s48, s48, 1\ns_nop 0\nbuffer_store_dword v36, v19, s[24:27], s33 offen sc1\nv_mfma_f32_16x16x32_bf16 v[100:103], v[40:43], v[132:135], 0\n"
"ds_read_b128 v[40:43], v32 offset:4096\ns_waitcnt lgkmcnt(3)\nv_mfma_f32_16x16x32_bf16 v[104:107], v[44:47], v[128:131], 0\nv_mfma_f32_16x16x32_bf16 v[108:111], v[44:47], v[132:135], 0\n"
"ds_read_b128 v[44:47], v33 offset:4096\nv_cvt_pk_bf16_f32 v56, v64, v65\ns_waitcnt lgkmcnt(3)\nv_mfma_f32_16x16x32_bf16 v[96:99], v[48:51], v[136:139], v[96:99]\n"
"v_cvt_pk_bf16_f32 v57, v66, v67\nv_mfma_f32_16x16x32_bf16 v[100:103], v[48:51], v[140:143], v[100:103]\nds_read_b128 v[48:51], v34 offset:4096\nv_cvt_pk_bf16_f32 v58, v72, v73\n"
"s_waitcnt lgkmcnt(3)\nv_mfma_f32_16x16x32_bf16 v[104:107], v[52:55], v[136:139], v[104:107]\nv_cvt_pk_bf16_f32 v59, v74, v75\nv_mfma_f32_16x16x32_bf16 v[108:111], v[52:55], v[140:143], v[108:111]\n"
"ds_read_b128 v[52:55], v35 offset:4096\ns_waitcnt lgkmcnt(3)\nv_mfma_f32_16x16x32_bf16 v[96:99], v[40:43], v[144:147], v[96:99]\nv_mfma_f32_16x16x32_bf16 v[100:103], v[40:43], v[148:151], v[100:103]\n"
"ds_read_b128 v[40:43], v32 offset:8192\nbuffer_store_dwordx4 v[56:59], v15, s[20:23], s50 offen sc1\ns_waitcnt lgkmcnt(3)\nv_mfma_f32_16x16x32_bf16 v[104:107], v[44:47], v[144:147], v[104:107]\n"
"v_cvt_pk_bf16_f32 v60, v68, v69\nv_mfma_f32_16x16x32_bf16 v[108:111], v[44:47], v[148:151], v[108:111]\nds_read_b128 v[44:47], v33 offset:8192\nv_cvt_pk_bf16_f32 v61, v70, v71\n"
"s_waitcnt lgkmcnt(3)\nv_mfma_f32_16x16x32_bf16 v[96:99], v[48:51], v[152:155], v[96:99]\nv_cvt_pk_bf16_f32 v62, v76, v77\nv_mfma_f32_16x16x32_bf16 v[100:103], v[48:51], v[156:159], v[100:103]\n"
"ds_read_b128 v[48:51], v34 offset:8192\nv_cvt_pk_bf16_f32 v63, v78, v79\ns_waitcnt lgkmcnt(3)\nv_mfma_f32_16x16x32_bf16 v[104:107], v[52:55], v[152:155], v[104:107]\n"
"v_mfma_f32_16x16x32_bf16 v[108:111], v[52:55], v[156:159], v[108:111]\nds_read_b128 v[52:55], v35 offset:8192\ns_waitcnt lgkmcnt(3)\nv_mfma_f32_16x16x32_bf16 v[96:99], v[40:43], v[160:163], v[96:99]\n"
"buffer_store_dwordx4 v[60:63], v16, s[20:23], s50 offen sc1\nv_mfma_f32_16x16x32_bf16 v[100:103], v[40:43], v[164:167], v[100:103]\nds_read_b128 v[40:43], v32 offset:12288\ns_add_u32 s50, s50, s36\n"
"s_waitcnt lgkmcnt(3)\nv_mfma_f32_16x16x32_bf16 v[104:107], v[44:47], v[160:163], v[104:107]\ns_add_u32 m0, s52, 0\nv_mfma_f32_16x16x32_bf16 v[108:111], v[44:47], v[164:167], v[108:111]\n"
"ds_read_b128 v[44:47], v33 offset:12288\ns_add_u32 s53, s49, 0\ns_waitcnt lgkmcnt(3)\nv_mfma_f32_16x16x32_bf16 v[96:99], v[48:51], v[168:171], v[96:99]\n"
"buffer_load_dwordx4 v10, s[12:15], s53 offen lds\nv_mfma_f32_16x16x32_bf16 v[100:103], v[48:51], v[172:175], v[100:103]\nds_read_b128 v[48:51], v34 offset:12288\ns_add_u32 m0, s52, 4096\n"
"s_waitcnt lgkmcnt(3)\nv_mfma_f32_16x16x32_bf16 v[104:107], v[52:55], v[168:171], v[104:107]\ns_add_u32 s53, s49, 128\nv_mfma_f32_16x16x32_bf16 v[108:111], v[52:55], v[172:175], v[108:111]\n"
"ds_read_b128 v[52:55], v35 offset:12288\nbuffer_load_dwordx4 v10, s[12:15], s53 offen lds\ns_waitcnt lgkmcnt(3)\nv_mfma_f32_16x16x32_bf16 v[96:99], v[40:43], v[176:179], v[96:99]\n"
"s_add_u32 m0, s52, 8192\nv_mfma_f32_16x16x32_bf16 v[100:103], v[40:43], v[180:183], v[100:103]\nds_read_b128 v[40:43], v32 offset:16384\ns_add_u32 s53, s49, 256\n"
"s_waitcnt lgkmcnt(3)\nv_mfma_f32_16x16x32_bf16 v[104:107], v[44:47], v[176:179], v[104:107]\nbuffer_load_dwordx4 v10, s[12:15], s53 offen lds\nv_mfma_f32_16x16x32_bf16 v[108:111], v[44:47], v[180:183], v[108:111]\n"
"ds_read_b128 v[44:47], v33 offset:16384\ns_add_u32 m0, s52, 12288\ns_waitcnt lgkmcnt(3)\nv_mfma_f32_16x16x32_bf16 v[96:99], v[48:51], v[184:187], v[96:99]\n"
"s_add_u32 s53, s49, 384\nv_mfma_f32_16x16x32_bf16 v[100:103], v[48:51], v[188:191], v[100:103]\nds_read_b128 v[48:51], v34 offset:16384\nbuffer_load_dwordx4 v10, s[12:15], s53 offen lds\n"
"s_waitcnt lgkmcnt(3)\nv_mfma_f32_16x16x32_bf16 v[104:107], v[52:55], v[184:187], v[104:107]\ns_add_u32 m0, s52, 16384\nv_mfma_f32_16x16x32_bf16 v[108:111], v[52:55], v[188:191], v[108:111]\n"
"ds_read_b128 v[52:55], v35 offset:16384\ns_add_u32 s53, s49, 512\ns_waitcnt lgkmcnt(3)\nv_mfma_f32_16x16x32_bf16 v[96:99], v[40:43], v[192:195], v[96:99]\n"
"buffer_load_dwordx4 v10, s[12:15], s53 offen lds\nv_mfma_f32_16x16x32_bf16 v[100:103], v[40:43], v[196:199], v[100:103]\nds_read_b128 v[40:43], v32 offset:20480\ns_add_u32 m0, s52, 20480\n"
"s_waitcnt lgkmcnt(3)\nv_mfma_f32_16x16x32_bf16 v[104:107], v[44:47], v[192:195], v[104:107]\ns_add_u32 s53, s49, 640\nv_mfma_f32_16x16x32_bf16 v[108:111], v[44:47], v[196:199], v[108:111]\n"
"ds_read_b128 v[44:47], v33 offset:20480\nbuffer_load_dwordx4 v10, s[12:15], s53 offen lds\ns_waitcnt lgkmcnt(3)\nv_mfma_f32_16x16x32_bf16 v[96:99], v[48:51], v[200:203], v[96:99]\n"
"s_add_u32 m0, s52, 24576\nv_mfma_f32_16x16x32_bf16 v[100:103], v[48:51], v[204:207], v[100:103]\nds_read_b128 v[48:51], v34 offset:20480\ns_add_u32 s53, s49, 768\n"
"s_waitcnt lgkmcnt(3)\nv_mfma_f32_16x16x32_bf16 v[104:107], v[52:55], v[200:203], v[104:107]\nbuffer_load_dwordx4 v10, s[12:15], s53 offen lds\nv_mfma_f32_16x16x32_bf16 v[108:111], v[52:55], v[204:207], v[108:111]\n"
"ds_read_b128 v[52:55], v35 offset:20480\ns_mov_b32 s53, 265216\nbuffer_load_dwordx4 a[28:31], v14, s[16:19], s53 offen sc1\ns_waitcnt lgkmcnt(3)\n"
"v_mfma_f32_16x16x32_bf16 v[96:99], v[40:43], v[208:211], v[96:99]\ns_mov_b32 s53, 4096\nbuffer_load_dwordx4 a[32:35], v14, s[16:19], s53 offen sc1\nv_mfma_f32_16x16x32_bf16 v[100:103], v[40:43], v[212:215], v[100:103]\n"
"ds_read_b128 v[40:43], v32 offset:24576\ns_mov_b32 s53, 266240\nbuffer_load_dwordx4 a[36:39], v14, s[16:19], s53 offen sc1\ns_waitcnt lgkmcnt(3)\n"
"v_mfma_f32_16x16x32_bf16 v[104:107], v[44:47], v[208:211], v[104:107]\ns_mov_b32 s53, 5120\nbuffer_load_dwordx4 a[40:43], v14, s[16:19], s53 offen sc1\nv_mfma_f32_16x16x32_bf16 v[108:111], v[44:47], v[212:215], v[108:111]\n"
"ds_read_b128 v[44:47], v33 offset:24576\ns_mov_b32 s53, 267264\nbuffer_load_dwordx4 a[44:47], v14, s[16:19], s53 offen sc1\ns_waitcnt lgkmcnt(3)\n"
"v_mfma_f32_16x16x32_bf16 v[96:99], v[48:51], v[216:219], v[96:99]\ns_add_u32 s54, s51, 32768\ns_cmp_eq_u32 s54, 163840\nv_mfma_f32_16x16x32_bf16 v[100:103], v[48:51], v[220:223], v[100:103]\n"
"s_cselect_b32 s54, 0, s54\ns_sub_u32 s54, s54, s51\nds_read_b128 v[48:51], v34 offset:24576\ns_mov_b32 s53, 6144\n"
"buffer_load_dwordx4 a[48:51], v14, s[16:19], s53 offen sc1\ns_waitcnt lgkmcnt(3)\nv_mfma_f32_16x16x32_bf16 v[104:107], v[52:55], v[216:219], v[104:107]\ns_mov_b32 s53, 268288\n"
"buffer_load_dwordx4 a[52:55], v14, s[16:19], s53 offen sc1\nv_mfma_f32_16x16x32_bf16 v[108:111], v[52:55], v[220:223], v[108:111]\nds_read_b128 v[52:55], v35 offset:24576\ns_waitcnt lgkmcnt(3)\n"
"v_mfma_f32_16x16x32_bf16 v[96:99], v[40:43], v[224:227], v[96:99]\nv_add_u32 v32, s54, v32\nv_mfma_f32_16x16x32_bf16 v[100:103], v[40:43], v[228:231], v[100:103]\nds_read_b128 v[40:43], v32 offset:0\n"
"s_waitcnt lgkmcnt(3)\nv_mfma_f32_16x16x32_bf16 v[104:107], v[44:47], v[224:227], v[104:107]\nv_add_u32 v33, s54, v33\nv_mfma_f32_16x16x32_bf16 v[108:111], v[44:47], v[228:231], v[108:111]\n"
"ds_read_b128 v[44:47], v33 offset:0\ns_waitcnt lgkmcnt(3)\nv_mfma_f32_16x16x32_bf16 v[96:99], v[48:51], v[232:235], v[96:99]\nv_add_u32 v34, s54, v34\n"
"v_mfma_f32_16x16x32_bf16 v[100:103], v[48:51], v[236:239], v[100:103]\nds_read_b128 v[48:51], v34 offset:0\ns_waitcnt lgkmcnt(3)\nv_mfma_f32_16x16x32_bf16 v[104:107], v[52:55], v[232:235], v[104:107]\n"
"v_add_u32 v35, s54, v35\nv_mfma_f32_16x16x32_bf16 v[108:111], v[52:55], v[236:239], v[108:111]\nds_read_b128 v[52:55], v35 offset:0\ns_mov_b32 s52, s47\n"
"s_add_u32 s47, s51, s32\ns_add_u32 s51, s51, s54\ns_add_u32 s49, s49, 28672\ns_waitcnt vmcnt(14)\n"
"s_barrier\ns_waitcnt lgkmcnt(3)\nv_mfma_f32_16x16x32_bf16 v[64:67], v[40:43], v[128:131], 0\nv_mov_b32 v36, s48\n"
"s_add_u32 s48, s48, 1\ns_nop 0\nbuffer_store_dword v36, v19, s[24:27], s33 offen sc1\nv_mfma_f32_16x16x32_bf16 v[68:71], v[40:43], v[132:135], 0\n"
"ds_read_b128 v[40:43], v32 offset:4096\ns_waitcnt lgkmcnt(3)\nv_mfma_f32_16x16x32_bf16 v[72:75], v[44:47], v[128:131], 0\nv_mfma_f32_16x16x32_bf16 v[76:79], v[44:47], v[132:135], 0\n"
"ds_read_b128 v[44:47], v33 offset:4096\nv_cvt_pk_bf16_f32 v56, v96, v97\ns_waitcnt lgkmcnt(3)\nv_mfma_f32_16x16x32_bf16 v[64:67], v[48:51], v[136:139], v[64:67]\n"
"v_cvt_pk_bf16_f32 v57, v98, v99\nv_mfma_f32_16x16x32_bf16 v[68:71], v[48:51], v[140:143], v[68:71]\nds_read_b128 v[48:51], v34 offset:4096\nv_cvt_pk_bf16_f32 v58, v104, v105\n"
"s_waitcnt lgkmcnt(3)\nv_mfma_f32_16x16x32_bf16 v[72:75], v[52:55], v[136:139], v[72:75]\nv_cvt_pk_bf16_f32 v59, v106, v107\nv_mfma_f32_16x16x32_bf16 v[76:79], v[52:55], v[140:143], v[76:79]\n"
"ds_read_b128 v[52:55], v35 offset:4096\ns_waitcnt lgkmcnt(3)\nv_mfma_f32_16x16x32_bf16 v[64:67], v[40:43], v[144:147], v[64:67]\nv_mfma_f32_16x16x32_bf16 v[68:71], v[40:43], v[148:151], v[68:71]\n"
"ds_read_b128 v[40:43], v32 offset:8192\nbuffer_store_dwordx4 v[56:59], v15, s[20:23], s50 offen sc1\ns_waitcnt lgkmcnt(3)\nv_mfma_f32_16x16x32_bf16 v[72:75], v[44:47], v[144:147], v[72:75]\n"
"v_cvt_pk_bf16_f32 v60, v100, v101\nv_mfma_f32_16x16x32_bf16 v[76:79], v[44:47], v[148:151], v[76:79]\nds_read_b128 v[44:47], v33 offset:8192\nv_cvt_pk_bf16_f32 v61, v102, v103\n"
"s_waitcnt lgkmcnt(3)\nv_mfma_f32_16x16x32_bf16 v[64:67], v[48:51], v[152:155], v[64:67]\nv_cvt_pk_bf16_f32 v62, v108, v109\nv_mfma_f32_16x16x32_bf16 v[68:71], v[48:51], v[156:159], v[68:71]\n"
"ds_read_b128 v[48:51], v34 offset:8192\nv_cvt_pk_bf16_f32 v63, v110, v111\ns_waitcnt lgkmcnt(3)\nv_mfma_f32_16x16x32_bf16 v[72:75], v[52:55], v[152:155], v[72:75]\n"
"v_mfma_f32_16x16x32_bf16 v[76:79], v[52:55], v[156:159], v[76:79]\nds_read_b128 v[52:55], v35 offset:8192\ns_waitcnt lgkmcnt(3)\nv_mfma_f32_16x16x32_bf16 v[64:67], v[40:43], v[160:163], v[64:67]\n"
"buffer_store_dwordx4 v[60:63], v16, s[20:23], s50 offen sc1\nv_mfma_f32_16x16x32_bf16 v[68:71], v[40:43], v[164:167], v[68:71]\nds_read_b128 v[40:43], v32 offset:12288\ns_add_u32 s50, s50, s36\n"
"s_waitcnt lgkmcnt(3)\nv_mfma_f32_16x16x32_bf16 v[72:75], v[44:47], v[160:163], v[72:75]\ns_add_u32 m0, s52, 0\nv_mfma_f32_16x16x32_bf16 v[76:79], v[44:47], v[164:167], v[76:79]\n"
"ds_read_b128 v[44:47], v33 offset:12288\ns_add_u32 s53, s49, 0\ns_waitcnt lgkmcnt(3)\nv_mfma_f32_16x16x32_bf16 v[64:67], v[48:51], v[168:171], v[64:67]\n"
"buffer_load_dwordx4 v10, s[12:15], s53 offen lds\nv_mfma_f32_16x16x32_bf16 v[68:71], v[48:51], v[172:175], v[68:71]\nds_read_b128 v[48:51], v34 offset:12288\ns_add_u32 m0, s52, 4096\n"
"s_waitcnt lgkmcnt(3)\nv_mfma_f32_16x16x32_bf16 v[72:75], v[52:55], v[168:171], v[72:75]\ns_add_u32 s53, s49, 128\nv_mfma_f32_16x16x32_bf16 v[76:79], v[52:55], v[172:175], v[76:79]\n"
"ds_read_b128 v[52:55], v35 offset:12288\nbuffer_load_dwordx4 v10, s[12:15], s53 offen lds\ns_waitcnt lgkmcnt(3)\nv_mfma_f32_16x16x32_bf16 v[64:67], v[40:43], v[176:179], v[64:67]\n"
"s_add_u32 m0, s52, 8192\nv_mfma_f32_16x16x32_bf16 v[68:71], v[40:43], v[180:183], v[68:71]\nds_read_b128 v[40:43], v32 offset:16384\ns_add_u32 s53, s49, 256\n"
"s_waitcnt lgkmcnt(3)\nv_mfma_f32_16x16x32_bf16 v[72:75], v[44:47], v[176:179], v[72:75]\nbuffer_load_dwordx4 v10, s[12:15], s53 offen lds\nv_mfma_f32_16x16x32_bf16 v[76:79], v[44:47], v[180:183], v[76:79]\n"
"ds_read_b128 v[44:47], v33 offset:16384\ns_add_u32 m0, s52, 12288\ns_waitcnt lgkmcnt(3)\nv_mfma_f32_16x16x32_bf16 v[64:67], v[48:51], v[184:187], v[64:67]\n"
"s_add_u32 s53, s49, 384\nv_mfma_f32_16x16x32_bf16 v[68:71], v[48:51], v[188:191], v[68:71]\nds_read_b128 v[48:51], v34 offset:16384\nbuffer_load_dwordx4 v10, s[12:15], s53 offen lds\n"
"s_waitcnt lgkmcnt(3)\nv_mfma_f32_16x16x32_bf16 v[72:75], v[52:55], v[184:187], v[72:75]\ns_add_u32 m0, s52, 16384\nv_mfma_f32_16x16x32_bf16 v[76:79], v[52:55], v[188:191], v[76:79]\n"
"ds_read_b128 v[52:55], v35 offset:16384\ns_add_u32 s53, s49, 512\ns_waitcnt lgkmcnt(3)\nv_mfma_f32_16x16x32_bf16 v[64:67], v[40:43], v[192:195], v[64:67]\n"
"buffer_load_dwordx4 v10, s[12:15], s53 offen lds\nv_mfma_f32_16x16x32_bf16 v[68:71], v[40:43], v[196:199], v[68:71]\nds_read_b128 v[40:43], v32 offset:20480\ns_add_u32 m0, s52, 20480\n"
"s_waitcnt lgkmcnt(3)\nv_mfma_f32_16x16x32_bf16 v[72:75], v[44:47], v[192:195], v[72:75]\ns_add_u32 s53, s49, 640\nv_mfma_f32_16x16x32_bf16 v[76:79], v[44:47], v[196:199], v[76:79]\n"
"ds_read_b128 v[44:47], v33 offset:20480\nbuffer_load_dwordx4 v10, s[12:15], s53 offen lds\ns_waitcnt lgkmcnt(3)\nv_mfma_f32_16x16x32_bf16 v[64:67], v[48:51], v[200:203], v[64:67]\n"
"s_add_u32 m0, s52, 24576\nv_mfma_f32_16x16x32_bf16 v[68:71], v[48:51], v[204:207], v[68:71]\nds_read_b128 v[48:51], v34 offset:20480\ns_add_u32 s53, s49, 768\n"
"s_waitcnt lgkmcnt(3)\nv_mfma_f32_16x16x32_bf16 v[72:75], v[52:55], v[200:203], v[72:75]\nbuffer_load_dwordx4 v10, s[12:15], s53 offen lds\nv_mfma_f32_16x16x32_bf16 v[76:79], v[52:55], v[204:207], v[76:79]\n"
"ds_read_b128 v[52:55], v35 offset:20480\ns_mov_b32 s53, 7168\nbuffer_load_dwordx4 a[56:59], v14, s[16:19], s53 offen sc1\ns_waitcnt lgkmcnt(3)\n"
"v_mfma_f32_16x16x32_bf16 v[64:67], v[40:43], v[208:211], v[64:67]\ns_mov_b32 s53, 269312\nbuffer_load_dwordx4 a[60:63], v14, s[16:19], s53 offen sc1\nv_mfma_f32_16x16x32_bf16 v[68:71], v[40:43], v[212:215], v[68:71]\n"
"ds_read_b128 v[40:43], v32 offset:24576\ns_mov_b32 s53, 8192\nbuffer_load_dwordx4 a[64:67], v14, s[16:19], s53 offen sc1\ns_waitcnt lgkmcnt(3)\n"
"v_mfma_f32_16x16x32_bf16 v[72:75], v[44:47], v[208:211], v[72:75]\ns_mov_b32 s53, 270336\nbuffer_load_dwordx4 a[68:71], v14, s[16:19], s53 offen sc1\nv_mfma_f32_16x16x32_bf16 v[76:79], v[44:47], v[212:215], v[76:79]\n"
"ds_read_b128 v[44:47], v33 offset:24576\ns_mov_b32 s53, 9216\nbuffer_load_dwordx4 a[72:75], v14, s[16:19], s53 offen sc1\ns_waitcnt lgkmcnt(3)\n"
"v_mfma_f32_16x16x32_bf16 v[64:67], v[48:51], v[216:219], v[64:67]\ns_add_u32 s54, s51, 32768\ns_cmp_eq_u32 s54, 163840\nv_mfma_f32_16x16x32_bf16 v[68:71], v[48:51], v[220:223], v[68:71]\n"
"s_cselect_b32 s54, 0, s54\ns_sub_u32 s54, s54, s51\nds_read_b128 v[48:51], v34 offset:24576\ns_mov_b32 s53, 271360\n"
"buffer_load_dwordx4 a[76:79], v14, s[16:19], s53 offen sc1\ns_waitcnt lgkmcnt(3)\nv_mfma_f32_16x16x32_bf16 v[72:75], v[52:55], v[216:219], v[72:75]\ns_mov_b32 s53, 10240\n"
"buffer_load_dwordx4 a[80:83], v14, s[16:19], s53 offen sc1\nv_mfma_f32_16x16x32_bf16 v[76:79], v[52:55], v[220:223], v[76:79]\nds_read_b128 v[52:55], v35 offset:24576\ns_waitcnt lgkmcnt(3)\n"
"v_mfma_f32_16x16x32_bf16 v[64:67], v[40:43], v[224:227], v[64:67]\nv_add_u32 v32, s54, v32\nv_mfma_f32_16x16x32_bf16 v[68:71], v[40:43], v[228:231], v[68:71]\nds_read_b128 v[40:43], v32 offset:0\n"
"s_waitcnt lgkmcnt(3)\nv_mfma_f32_16x16x32_bf16 v[72:75], v[44:47], v[224:227], v[72:75]\nv_add_u32 v33, s54, v33\nv_mfma_f32_16x16x32_bf16 v[76:79], v[44:47], v[228:231], v[76:79]\n"
"ds_read_b128 v[44:47], v33 offset:0\ns_waitcnt lgkmcnt(3)\nv_mfma_f32_16x16x32_bf16 v[64:67], v[48:51], v[232:235], v[64:67]\nv_add_u32 v34, s54, v34\n"
"v_mfma_f32_16x16x32_bf16 v[68:71], v[48:51], v[236:239], v[68:71]\nds_read_b128 v[48:51], v34 offset:0\ns_waitcnt lgkmcnt(3)\nv_mfma_f32_16x16x32_bf16 v[72:75], v[52:55], v[232:235], v[72:75]\n"
"v_add_u32 v35, s54, v35\nv_mfma_f32_16x16x32_bf16 v[76:79], v[52:55], v[236:239], v[76:79]\nds_read_b128 v[52:55], v35 offset:0\ns_mov_b32 s52, s47\n"
"s_add_u32 s47, s51, s32\ns_add_u32 s51, s51, s54\ns_add_u32 s49, s49, 28672\ns_waitcnt vmcnt(14)\n"
"s_barrier\ns_waitcnt lgkmcnt(3)\nv_mfma_f32_16x16x32_bf16 v[96:99], v[40:43], v[128:131], 0\nv_mov_b32 v36, s48\n"
"s_add_u32 s48, s48, 1\ns_nop 0\nbuffer_store_dword v36, v19, s[24:27], s33 offen sc1\nv_mfma_f32_16x16x32_bf16 v[100:103], v[40:43], v[132:135], 0\n"
"ds_read_b128 v[40:43], v32 offset:4096\ns_waitcnt lgkmcnt(3)\nv_mfma_f32_16x16x32_bf16 v[104:107], v[44:47], v[128:131], 0\nv_mfma_f32_16x16x32_bf16 v[108:111], v[44:47], v[132:135], 0\n"
"ds_read_b128 v[44:47], v33 offset:4096\nv_cvt_pk_bf16_f32 v56, v64, v65\ns_waitcnt lgkmcnt(3)\nv_mfma_f32_16x16x32_bf16 v[96:99], v[48:51], v[136:139], v[96:99]\n"
"v_cvt_pk_bf16_f32 v57, v66, v67\nv_mfma_f32_16x16x32_bf16 v[100:103], v[48:51], v[140:143], v[100:103]\nds_read_b128 v[48:51], v34 offset:4096\nv_cvt_pk_bf16_f32 v58, v72, v73\n"
"s_waitcnt lgkmcnt(3)\nv_mfma_f32_16x16x32_bf16 v[104:107], v[52:55], v[136:139], v[104:107]\nv_cvt_pk_bf16_f32 v59, v74, v75\nv_mfma_f32_16x16x32_bf16 v[108:111], v[52:55], v[140:143], v[108:111]\n"
"ds_read_b128 v[52:55], v35 offset:4096\ns_waitcnt lgkmcnt(3)\nv_mfma_f32_16x16x32_bf16 v[96:99], v[40:43], v[144:147], v[96:99]\nv_mfma_f32_16x16x32_bf16 v[100:103], v[40:43], v[148:151], v[100:103]\n"
"ds_read_b128 v[40:43], v32 offset:8192\nbuffer_store_dwordx4 v[56:59], v15, s[20:23], s50 offen sc1\ns_waitcnt lgkmcnt(3)\nv_mfma_f32_16x16x32_bf16 v[104:107], v[44:47], v[144:147], v[104:107]\n"
"v_cvt_pk_bf16_f32 v60, v68, v69\nv_mfma_f32_16x16x32_bf16 v[108:111], v[44:47], v[148:151], v[108:111]\nds_read_b128 v[44:47], v33 offset:8192\nv_cvt_pk_bf16_f32 v61, v70, v71\n"
"s_waitcnt lgkmcnt(3)\nv_mfma_f32_16x16x32_bf16 v[96:99], v[48:51], v[152:155], v[96:99]\nv_cvt_pk_bf16_f32 v62, v76, v77\nv_mfma_f32_16x16x32_bf16 v[100:103], v[48:51], v[156:159], v[100:103]\n"
"ds_read_b128 v[48:51], v34 offset:8192\nv_cvt_pk_bf16_f32 v63, v78, v79\ns_waitcnt lgkmcnt(3)\nv_mfma_f32_16x16x32_bf16 v[104:107], v[52:55], v[152:155], v[104:107]\n"
"v_mfma_f32_16x16x32_bf16 v[108:111], v[52:55], v[156:159], v[108:111]\nds_read_b128 v[52:55], v35 offset:8192\ns_waitcnt lgkmcnt(3)\nv_mfma_f32_16x16x32_bf16 v[96:99], v[40:43], v[160:163], v[96:99]\n"
"buffer_store_dwordx4 v[60:63], v16, s[20:23], s50 offen sc1\nv_mfma_f32_16x16x32_bf16 v[100:103], v[40:43], v[164:167], v[100:103]\nds_read_b128 v[40:43], v32 offset:12288\ns_add_u32 s50, s50, s36\n"
"s_waitcnt lgkmcnt(3)\nv_mfma_f32_16x16x32_bf16 v[104:107], v[44:47], v[160:163], v[104:107]\ns_add_u32 m0, s52, 0\nv_mfma_f32_16x16x32_bf16 v[108:111], v[44:47], v[164:167], v[108:111]\n"
"ds_read_b128 v[44:47], v33 offset:12288\ns_add_u32 s53, s49, 0\ns_waitcnt lgkmcnt(3)\nv_mfma_f32_16x16x32_bf16 v[96:99], v[48:51], v[168:171], v[96:99]\n"
"buffer_load_dwordx4 v10, s[12:15], s53 offen lds\nv_mfma_f32_16x16x32_bf16 v[100:103], v[48:51], v[172:175], v[100:103]\nds_read_b128 v[48:51], v34 offset:12288\ns_add_u32 m0, s52, 4096\n"
"s_waitcnt lgkmcnt(3)\nv_mfma_f32_16x16x32_bf16 v[104:107], v[52:55], v[168:171], v[104:107]\ns_add_u32 s53, s49, 128\nv_mfma_f32_16x16x32_bf16 v[108:111], v[52:55], v[172:175], v[108:111]\n"
"ds_read_b128 v[52:55], v35 offset:12288\nbuffer_load_dwordx4 v10, s[12:15], s53 offen lds\ns_waitcnt lgkmcnt(3)\nv_mfma_f32_16x16x32_bf16 v[96:99], v[40:43], v[176:179], v[96:99]\n"
"s_add_u32 m0, s52, 8192\nv_mfma_f32_16x16x32_bf16 v[100:103], v[40:43], v[180:183], v[100:103]\nds_read_b128 v[40:43], v32 offset:16384\ns_add_u32 s53, s49, 256\n"
"s_waitcnt lgkmcnt(3)\nv_mfma_f32_16x16x32_bf16 v[104:107], v[44:47], v[176:179], v[104:107]\nbuffer_load_dwordx4 v10, s[12:15], s53 offen lds\nv_mfma_f32_16x16x32_bf16 v[108:111], v[44:47], v[180:183], v[108:111]\n"
"ds_read_b128 v[44:47], v33 offset:16384\ns_add_u32 m0, s52, 12288\ns_waitcnt lgkmcnt(3)\nv_mfma_f32_16x16x32_bf16 v[96:99], v[48:51], v[184:187], v[96:99]\n"
"s_add_u32 s53, s49, 384\nv_mfma_f32_16x16x32_bf16 v[100:103], v[48:51], v[188:191], v[100:103]\nds_read_b128 v[48:51], v34 offset:16384\nbuffer_load_dwordx4 v10, s[12:15], s53 offen lds\n"
"s_waitcnt lgkmcnt(3)\nv_mfma_f32_16x16x32_bf16 v[104:107], v[52:55], v[184:187], v[104:107]\ns_add_u32 m0, s52, 16384\nv_mfma_f32_16x16x32_bf16 v[108:111], v[52:55], v[188:191], v[108:111]\n"
"ds_read_b128 v[52:55], v35 offset:16384\ns_add_u32 s53, s49, 512\ns_waitcnt lgkmcnt(3)\nv_mfma_f32_16x16x32_bf16 v[96:99], v[40:43], v[192:195], v[96:99]\n"
"buffer_load_dwordx4 v10, s[12:15], s53 offen lds\nv_mfma_f32_16x16x32_bf16 v[100:103], v[40:43], v[196:199], v[100:103]\nds_read_b128 v[40:43], v32 offset:20480\ns_add_u32 m0, s52, 20480\n"
"s_waitcnt lgkmcnt(3)\nv_mfma_f32_16x16x32_bf16 v[104:107], v[44:47], v[192:195], v[104:107]\ns_add_u32 s53, s49, 640\nv_mfma_f32_16x16x32_bf16 v[108:111], v[44:47], v[196:199], v[108:111]\n"
"ds_read_b128 v[44:47], v33 offset:20480\nbuffer_load_dwordx4 v10, s[12:15], s53 offen lds\ns_waitcnt lgkmcnt(3)\nv_mfma_f32_16x16x32_bf16 v[96:99], v[48:51], v[200:203], v[96:99]\n"
"s_add_u32 m0, s52, 24576\nv_mfma_f32_16x16x32_bf16 v[100:103], v[48:51], v[204:207], v[100:103]\nds_read_b128 v[48:51], v34 offset:20480\ns_add_u32 s53, s49, 768\n"
"s_waitcnt lgkmcnt(3)\nv_mfma_f32_16x16x32_bf16 v[104:107], v[52:55], v[200:203], v[104:107]\nbuffer_load_dwordx4 v10, s[12:15], s53 offen lds\nv_mfma_f32_16x16x32_bf16 v[108:111], v[52:55], v[204:207], v[108:111]\n"
"ds_read_b128 v[52:55], v35 offset:20480\ns_mov_b32 s53, 272384\nbuffer_load_dwordx4 a[84:87], v14, s[16:19], s53 offen sc1\ns_waitcnt lgkmcnt(3)\n"
"v_mfma_f32_16x16x32_bf16 v[96:99], v[40:43], v[208:211], v[96:99]\ns_mov_b32 s53, 11264\nbuffer_load_dwordx4 a[88:91], v14, s[16:19], s53 offen sc1\nv_mfma_f32_16x16x32_bf16 v[100:103], v[40:43], v[212:215], v[100:103]\n"
"ds_read_b128 v[40:43], v32 offset:24576\ns_mov_b32 s53, 273408\nbuffer_load_dwordx4 a[92:95], v14, s[16:19], s53 offen sc1\ns_waitcnt lgkmcnt(3)\n"
"v_mfma_f32_16x16x32_bf16 v[104:107], v[44:47], v[208:211], v[104:107]\ns_mov_b32 s53, 12288\nbuffer_load_dwordx4 a[96:99], v14, s[16:19], s53 offen sc1\nv_mfma_f32_16x16x32_bf16 v[108:111], v[44:47], v[212:215], v[108:111]\n"
"ds_read_b128 v[44:47], v33 offset:24576\ns_mov_b32 s53, 274432\nbuffer_load_dwordx4 a[100:103], v14, s[16:19], s53 offen sc1\ns_waitcnt lgkmcnt(3)\n"
"v_mfma_f32_16x16x32_bf16 v[96:99], v[48:51], v[216:219], v[96:99]\ns_add_u32 s54, s51, 32768\ns_cmp_eq_u32 s54, 163840\nv_mfma_f32_16x16x32_bf16 v[100:103], v[48:51], v[220:223], v[100:103]\n"
"s_cselect_b32 s54, 0, s54\ns_sub_u32 s54, s54, s51\nds_read_b128 v[48:51], v34 offset:24576\ns_mov_b32 s53, 13312\n"
"buffer_load_dwordx4 a[104:107], v14, s[16:19], s53 offen sc1\ns_waitcnt lgkmcnt(3)\nv_mfma_f32_16x16x32_bf16 v[104:107], v[52:55], v[216:219], v[104:107]\ns_mov_b32 s53, 275456\n"
"buffer_load_dwordx4 a[108:111], v14, s[16:19], s53 offen sc1\nv_mfma_f32_16x16x32_bf16 v[108:111], v[52:55], v[220:223], v[108:111]\nds_read_b128 v[52:55], v35 offset:24576\ns_waitcnt lgkmcnt(3)\n"
"v_mfma_f32_16x16x32_bf16 v[96:99], v[40:43], v[224:227], v[96:99]\nv_add_u32 v32, s54, v32\nv_mfma_f32_16x16x32_bf16 v[100:103], v[40:43], v[228:231], v[100:103]\nds_read_b128 v[40:43], v32 offset:0\n"
"s_waitcnt lgkmcnt(3)\nv_mfma_f32_16x16x32_bf16 v[104:107], v[44:47], v[224:227], v[104:107]\nv_add_u32 v33, s54, v33\nv_mfma_f32_16x16x32_bf16 v[108:111], v[44:47], v[228:231], v[108:111]\n"
"ds_read_b128 v[44:47], v33 offset:0\ns_waitcnt lgkmcnt(3)\nv_mfma_f32_16x16x32_bf16 v[96:99], v[48:51], v[232:235], v[96:99]\nv_add_u32 v34, s54, v34\n"
"v_mfma_f32_16x16x32_bf16 v[100:103], v[48:51], v[236:239], v[100:103]\nds_read_b128 v[48:51], v34 offset:0\ns_waitcnt lgkmcnt(3)\nv_mfma_f32_16x16x32_bf16 v[104:107], v[52:55], v[232:235], v[104:107]\n"
"v_add_u32 v35, s54, v35\nv_mfma_f32_16x16x32_bf16 v[108:111], v[52:55], v[236:239], v[108:111]\nds_read_b128 v[52:55], v35 offset:0\ns_mov_b32 s52, s47\n"
"s_add_u32 s47, s51, s32\ns_add_u32 s51, s51, s54\ns_add_u32 s49, s49, 28672\ns_cmp_lg_u32 s34, 0\n"
"s_cbranch_scc1 fg1_stay\ns_nop 7\nv_cvt_pk_bf16_f32 v56, v96, v97\nv_cvt_pk_bf16_f32 v57, v98, v99\n"
"v_cvt_pk_bf16_f32 v58, v104, v105\nv_cvt_pk_bf16_f32 v59, v106, v107\ns_nop 0\ns_nop 0\n"
"buffer_store_dwordx4 v[56:59], v15, s[20:23], s50 offen sc1\nv_cvt_pk_bf16_f32 v60, v100, v101\nv_cvt_pk_bf16_f32 v61, v102, v103\nv_cvt_pk_bf16_f32 v62, v108, v109\n"
"v_cvt_pk_bf16_f32 v63, v110, v111\ns_nop 0\ns_nop 0\nbuffer_store_dwordx4 v[60:63], v16, s[20:23], s50 offen sc1\n"
"s_add_u32 s48, s28, 64\nv_mov_b32 v36, s48\ns_waitcnt vmcnt(0) lgkmcnt(0)\ns_barrier\n"
"buffer_store_dword v36, v19, s[24:27], s33 offen sc1\ns_branch fg1_end\nfg1_stay:\ns_waitcnt vmcnt(0)\n"
"s_barrier\ns_waitcnt lgkmcnt(3)\nv_mfma_f32_16x16x32_bf16 v[64:67], v[40:43], v[128:131], 0\nv_mov_b32 v36, s48\n"
"s_add_u32 s48, s48, 1\ns_nop 0\nbuffer_store_dword v36, v19, s[24:27], s33 offen sc1\nv_mfma_f32_16x16x32_bf16 v[68:71], v[40:43], v[132:135], 0\n"
"v_mfma_f32_16x16x32_bf16 v[72:75], v[40:43], a[0:3], 0\nv_mfma_f32_16x16x32_bf16 v[76:79], v[40:43], a[4:7], 0\nds_read_b128 v[40:43], v32 offset:4096\nv_cvt_pk_bf16_f32 v56, v96, v97\n"
"s_waitcnt lgkmcnt(3)\nv_mfma_f32_16x16x32_bf16 v[80:83], v[44:47], v[128:131], 0\nv_cvt_pk_bf16_f32 v57, v98, v99\nv_mfma_f32_16x16x32_bf16 v[84:87], v[44:47], v[132:135], 0\n"
"v_cvt_pk_bf16_f32 v58, v104, v105\nv_mfma_f32_16x16x32_bf16 v[88:91], v[44:47], a[0:3], 0\nv_cvt_pk_bf16_f32 v59, v106, v107\nv_mfma_f32_16x16x32_bf16 v[92:95], v[44:47], a[4:7], 0\n"
"ds_read_b128 v[44:47], v33 offset:4096\ns_waitcnt lgkmcnt(3)\nv_mfma_f32_16x16x32_bf16 v[64:67], v[48:51], v[136:139], v[64:67]\nv_mfma_f32_16x16x32_bf16 v[68:71], v[48:51], v[140:143], v[68:71]\n"
"buffer_store_dwordx4 v[56:59], v15, s[20:23], s50 offen sc1\nv_mfma_f32_16x16x32_bf16 v[72:75], v[48:51], a[8:11], v[72:75]\nv_cvt_pk_bf16_f32 v60, v100, v101\nv_mfma_f32_16x16x32_bf16 v[76:79], v[48:51], a[12:15], v[76:79]\n"
"ds_read_b128 v[48:51], v34 offset:4096\nv_cvt_pk_bf16_f32 v61, v102, v103\ns_waitcnt lgkmcnt(3)\nv_mfma_f32_16x16x32_bf16 v[80:83], v[52:55], v[136:139], v[80:83]\n"
"v_cvt_pk_bf16_f32 v62, v108, v109\nv_mfma_f32_16x16x32_bf16 v[84:87], v[52:55], v[140:143], v[84:87]\nv_cvt_pk_bf16_f32 v63, v110, v111\nv_mfma_f32_16x16x32_bf16 v[88:91], v[52:55], a[8:11], v[88:91]\n"
"v_mfma_f32_16x16x32_bf16 v[92:95], v[52:55], a[12:15], v[92:95]\nds_read_b128 v[52:55], v35 offset:4096\ns_waitcnt lgkmcnt(3)\nv_mfma_f32_16x16x32_bf16 v[64:67], v[40:43], v[144:147], v[64:67]\n"
"buffer_store_dwordx4 v[60:63], v16, s[20:23], s50 offen sc1\nv_mfma_f32_16x16x32_bf16 v[68:71], v[40:43], v[148:151], v[68:71]\ns_add_u32 s50, s50, s36\nv_mfma_f32_16x16x32_bf16 v[72:75], v[40:43], a[16:19], v[72:75]\n"
"s_add_u32 m0, s52, 0\nv_mfma_f32_16x16x32_bf16 v[76:79], v[40:43], a[20:23], v[76:79]\nds_read_b128 v[40:43], v32 offset:8192\ns_add_u32 s53, s49, 0\n"
"s_waitcnt lgkmcnt(3)\nv_mfma_f32_16x16x32_bf16 v[80:83], v[44:47], v[144:147], v[80:83]\nbuffer_load_dwordx4 v10, s[12:15], s53 offen lds\nv_mfma_f32_16x16x32_bf16 v[84:87], v[44:47], v[148:151], v[84:87]\n"
"v_mfma_f32_16x16x32_bf16 v[88:91], v[44:47], a[16:19], v[88:91]\nv_mfma_f32_16x16x32_bf16 v[92:95], v[44:47], a[20:23], v[92:95]\nds_read_b128 v[44:47], v33 offset:8192\ns_waitcnt lgkmcnt(3)\n"
"v_mfma_f32_16x16x32_bf16 v[64:67], v[48:51], v[152:155], v[64:67]\nv_mfma_f32_16x16x32_bf16 v[68:71], v[48:51], v[156:159], v[68:71]\nv_mfma_f32_16x16x32_bf16 v[72:75], v[48:51], a[24:27], v[72:75]\nv_mfma_f32_16x16x32_bf16 v[76:79], v[48:51], a[28:31], v[76:79]\n"
"ds_read_b128 v[48:51], v34 offset:8192\ns_waitcnt lgkmcnt(3)\nv_mfma_f32_16x16x32_bf16 v[80:83], v[52:55], v[152:155], v[80:83]\nv_mfma_f32_16x16x32_bf16 v[84:87], v[52:55], v[156:159], v[84:87]\n"
"s_add_u32 m0, s52, 4096\nv_mfma_f32_16x16x32_bf16 v[88:91], v[52:55], a[24:27], v[88:91]\ns_add_u32 s53, s49, 128\nv_mfma_f32_16x16x32_bf16 v[92:95], v[52:55], a[28:31], v[92:95]\n"
"ds_read_b128 v[52:55], v35 offset:8192\nbuffer_load_dwordx4 v10, s[12:15], s53 offen lds\ns_waitcnt lgkmcnt(3)\nv_mfma_f32_16x16x32_bf16 v[64:67], v[40:43], v[160:163], v[64:67]\n"
"v_mfma_f32_16x16x32_bf16 v[68:71], v[40:43], v[164:167], v[68:71]\nv_mfma_f32_16x16x32_bf16 v[72:75], v[40:43], a[32:35], v[72:75]\nv_mfma_f32_16x16x32_bf16 v[76:79], v[40:43], a[36:39], v[76:79]\nds_read_b128 v[40:43], v32 offset:12288\n"
"s_waitcnt lgkmcnt(3)\nv_mfma_f32_16x16x32_bf16 v[80:83], v[44:47], v[160:163], v[80:83]\nv_mfma_f32_16x16x32_bf16 v[84:87], v[44:47], v[164:167], v[84:87]\nv_mfma_f32_16x16x32_bf16 v[88:91], v[44:47], a[32:35], v[88:91]\n"
"v_mfma_f32_16x16x32_bf16 v[92:95], v[44:47], a[36:39], v[92:95]\nds_read_b128 v[44:47], v33 offset:12288\ns_waitcnt lgkmcnt(3)\nv_mfma_f32_16x16x32_bf16 v[64:67], v[48:51], v[168:171], v[64:67]\n"
"s_add_u32 m0, s52, 8192\nv_mfma_f32_16x16x32_bf16 v[68:71], v[48:51], v[172:175], v[68:71]\ns_add_u32 s53, s49, 256\nv_mfma_f32_16x16x32_bf16 v[72:75], v[48:51], a[40:43], v[72:75]\n"
"buffer_load_dwordx4 v10, s[12:15], s53 offen lds\nv_mfma_f32_16x16x32_bf16 v[76:79], v[48:51], a[44:47], v[76:79]\nds_read_b128 v[48:51], v34 offset:12288\ns_waitcnt lgkmcnt(3)\n"
"v_mfma_f32_16x16x32_bf16 v[80:83], v[52:55], v[168:171], v[80:83]\nv_mfma_f32_16x16x32_bf16 v[84:87], v[52:55], v[172:175], v[84:87]\nv_mfma_f32_16x16x32_bf16 v[88:91], v[52:55], a[40:43], v[88:91]\nv_mfma_f32_16x16x32_bf16 v[92:95], v[52:55], a[44:47], v[92:95]\n"
"ds_read_b128 v[52:55], v35 offset:12288\ns_waitcnt lgkmcnt(3)\nv_mfma_f32_16x16x32_bf16 v[64:67], v[40:43], v[176:179], v[64:67]\nv_mfma_f32_16x16x32_bf16 v[68:71], v[40:43], v[180:183], v[68:71]\n"
"v_mfma_f32_16x16x32_bf16 v[72:75], v[40:43], a[48:51], v[72:75]\nv_mfma_f32_16x16x32_bf16 v[76:79], v[40:43], a[52:55], v[76:79]\nds_read_b128 v[40:43], v32 offset:16384\ns_add_u32 m0, s52, 12288\n"
"s_waitcnt lgkmcnt(3)\nv_mfma_f32_16x16x32_bf16 v[80:83], v[44:47], v[176:179], v[80:83]\ns_add_u32 s53, s49, 384\nv_mfma_f32_16x16x32_bf16 v[84:87], v[44:47], v[180:183], v[84:87]\n"
"buffer_load_dwordx4 v10, s[12:15], s53 offen lds\nv_mfma_f32_16x16x32_bf16 v[88:91], v[44:47], a[48:51], v[88:91]\nv_mfma_f32_16x16x32_bf16 v[92:95], v[44:47], a[52:55], v[92:95]\nds_read_b128 v[44:47], v33 offset:16384\n"
"s_waitcnt lgkmcnt(3)\nv_mfma_f32_16x16x32_bf16 v[64:67], v[48:51], v[184:187], v[64:67]\nv_mfma_f32_16x16x32_bf16 v[68:71], v[48:51], v[188:191], v[68:71]\nv_mfma_f32_16x16x32_bf16 v[72:75], v[48:51], a[56:59], v[72:75]\n"
"v_mfma_f32_16x16x32_bf16 v[76:79], v[48:51], a[60:63], v[76:79]\nds_read_b128 v[48:51], v34 offset:16384\ns_waitcnt lgkmcnt(3)\nv_mfma_f32_16x16x32_bf16 v[80:83], v[52:55], v[184:187], v[80:83]\n"
"v_mfma_f32_16x16x32_bf16 v[84:87], v[52:55], v[188:191], v[84:87]\nv_mfma_f32_16x16x32_bf16 v[88:91], v[52:55], a[56:59], v[88:91]\ns_add_u32 m0, s52, 16384\nv_mfma_f32_16x16x32_bf16 v[92:95], v[52:55], a[60:63], v[92:95]\n"
"ds_read_b128 v[52:55], v35 offset:16384\ns_add_u32 s53, s49, 512\ns_waitcnt lgkmcnt(3)\nv_mfma_f32_16x16x32_bf16 v[64:67], v[40:43], v[192:195], v[64:67]\n"
"buffer_load_dwordx4 v10, s[12:15], s53 offen lds\nv_mfma_f32_16x16x32_bf16 v[68:71], v[40:43], v[196:199], v[68:71]\nv_mfma_f32_16x16x32_bf16 v[72:75], v[40:43], a[64:67], v[72:75]\nv_mfma_f32_16x16x32_bf16 v[76:79], v[40:43], a[68:71], v[76:79]\n"
"ds_read_b128 v[40:43], v32 offset:20480\ns_waitcnt lgkmcnt(3)\nv_mfma_f32_16x16x32_bf16 v[80:83], v[44:47], v[192:195], v[80:83]\nv_mfma_f32_16x16x32_bf16 v[84:87], v[44:47], v[196:199], v[84:87]\n"
"v_mfma_f32_16x16x32_bf16 v[88:91], v[44:47], a[64:67], v[88:91]\nv_mfma_f32_16x16x32_bf16 v[92:95], v[44:47], a[68:71], v[92:95]\nds_read_b128 v[44:47], v33 offset:20480\ns_waitcnt lgkmcnt(3)\n"
"v_mfma_f32_16x16x32_bf16 v[64:67], v[48:51], v[200:203], v[64:67]\nv_mfma_f32_16x16x32_bf16 v[68:71], v[48:51], v[204:207], v[68:71]\ns_add_u32 m0, s52, 20480\nv_mfma_f32_16x16x32_bf16 v[72:75], v[48:51], a[72:75], v[72:75]\n"
"s_add_u32 s53, s49, 640\nv_mfma_f32_16x16x32_bf16 v[76:79], v[48:51], a[76:79], v[76:79]\nds_read_b128 v[48:51], v34 offset:20480\nbuffer_load_dwordx4 v10, s[12:15], s53 offen lds\n"
"s_waitcnt lgkmcnt(3)\nv_mfma_f32_16x16x32_bf16 v[80:83], v[52:55], v[200:203], v[80:83]\nv_mfma_f32_16x16x32_bf16 v[84:87], v[52:55], v[204:207], v[84:87]\nv_mfma_f32_16x16x32_bf16 v[88:91], v[52:55], a[72:75], v[88:91]\n"
"v_mfma_f32_16x16x32_bf16 v[92:95], v[52:55], a[76:79], v[92:95]\nds_read_b128 v[52:55], v35 offset:20480\ns_waitcnt lgkmcnt(3)\nv_mfma_f32_16x16x32_bf16 v[64:67], v[40:43], v[208:211], v[64:67]\n"
"v_mfma_f32_16x16x32_bf16 v[68:71], v[40:43], v[212:215], v[68:71]\nv_mfma_f32_16x16x32_bf16 v[72:75], v[40:43], a[80:83], v[72:75]\nv_mfma_f32_16x16x32_bf16 v[76:79], v[40:43], a[84:87], v[76:79]\nds_read_b128 v[40:43], v32 offset:24576\n"
"s_waitcnt lgkmcnt(3)\nv_mfma_f32_16x16x32_bf16 v[80:83], v[44:47], v[208:211], v[80:83]\ns_add_u32 m0, s52, 24576\nv_mfma_f32_16x16x32_bf16 v[84:87], v[44:47], v[212:215], v[84:87]\n"
"s_add_u32 s53, s49, 768\nv_mfma_f32_16x16x32_bf16 v[88:91], v[44:47], a[80:83], v[88:91]\nbuffer_load_dwordx4 v10, s[12:15], s53 offen lds\nv_mfma_f32_16x16x32_bf16 v[92:95], v[44:47], a[84:87], v[92:95]\n"
"ds_read_b128 v[44:47], v33 offset:24576\ns_waitcnt lgkmcnt(3)\nv_mfma_f32_16x16x32_bf16 v[64:67], v[48:51], v[216:219], v[64:67]\ns_add_u32 s54, s51, 32768\n"
"s_cmp_eq_u32 s54, 163840\nv_mfma_f32_16x16x32_bf16 v[68:71], v[48:51], v[220:223], v[68:71]\nv_mfma_f32_16x16x32_bf16 v[72:75], v[48:51], a[88:91], v[72:75]\nv_mfma_f32_16x16x32_bf16 v[76:79], v[48:51], a[92:95], v[76:79]\n"
"s_cselect_b32 s54, 0, s54\ns_sub_u32 s54, s54, s51\nds_read_b128 v[48:51], v34 offset:24576\ns_waitcnt lgkmcnt(3)\n"
"v_mfma_f32_16x16x32_bf16 v[80:83], v[52:55], v[216:219], v[80:83]\nv_mfma_f32_16x16x32_bf16 v[84:87], v[52:55], v[220:223], v[84:87]\nv_mfma_f32_16x16x32_bf16 v[88:91], v[52:55], a[88:91], v[88:91]\nv_mfma_f32_16x16x32_bf16 v[92:95], v[52:55], a[92:95], v[92:95]\n"
"ds_read_b128 v[52:55], v35 offset:24576\ns_waitcnt lgkmcnt(3)\nv_mfma_f32_16x16x32_bf16 v[64:67], v[40:43], v[224:227], v[64:67]\nv_add_u32 v32, s54, v32\n"
"v_mfma_f32_16x16x32_bf16 v[68:71], v[40:43], v[228:231], v[68:71]\nv_mfma_f32_16x16x32_bf16 v[72:75], v[40:43], a[96:99], v[72:75]\nv_mfma_f32_16x16x32_bf16 v[76:79], v[40:43], a[100:103], v[76:79]\nds_read_b128 v[40:43], v32 offset:0\n"
"s_waitcnt lgkmcnt(3)\nv_mfma_f32_16x16x32_bf16 v[80:83], v[44:47], v[224:227], v[80:83]\nv_add_u32 v33, s54, v33\nv_mfma_f32_16x16x32_bf16 v[84:87], v[44:47], v[228:231], v[84:87]\n"
"v_mfma_f32_16x16x32_bf16 v[88:91], v[44:47], a[96:99], v[88:91]\nv_mfma_f32_16x16x32_bf16 v[92:95], v[44:47], a[100:103], v[92:95]\nds_read_b128 v[44:47], v33 offset:0\ns_waitcnt lgkmcnt(3)\n"
"v_mfma_f32_16x16x32_bf16 v[64:67], v[48:51], v[232:235], v[64:67]\nv_add_u32 v34, s54, v34\nv_mfma_f32_16x16x32_bf16 v[68:71], v[48:51], v[236:239], v[68:71]\nv_mfma_f32_16x16x32_bf16 v[72:75], v[48:51], a[104:107], v[72:75]\n"
"v_mfma_f32_16x16x32_bf16 v[76:79], v[48:51], a[108:111], v[76:79]\nds_read_b128 v[48:51], v34 offset:0\ns_waitcnt lgkmcnt(3)\nv_mfma_f32_16x16x32_bf16 v[80:83], v[52:55], v[232:235], v[80:83]\n"
"v_add_u32 v35, s54, v35\nv_mfma_f32_16x16x32_bf16 v[84:87], v[52:55], v[236:239], v[84:87]\nv_mfma_f32_16x16x32_bf16 v[88:91], v[52:55], a[104:107], v[88:91]\nv_mfma_f32_16x16x32_bf16 v[92:95], v[52:55], a[108:111], v[92:95]\n"
"ds_read_b128 v[52:55], v35 offset:0\ns_mov_b32 s52, s47\ns_add_u32 s47, s51, s32\ns_add_u32 s51, s51, s54\n"
"s_add_u32 s49, s49, 28672\ns_waitcnt vmcnt(7)\ns_barrier\ns_waitcnt lgkmcnt(3)\n"
"v_mfma_f32_16x16x32_bf16 v[96:99], v[40:43], v[128:131], 0\nv_mov_b32 v36, s48\ns_add_u32 s48, s48, 1\ns_nop 0\n"
"buffer_store_dword v36, v19, s[24:27], s33 offen sc1\nv_mfma_f32_16x16x32_bf16 v[100:103], v[40:43], v[132:135], 0\nv_mfma_f32_16x16x32_bf16 v[104:107], v[40:43], a[0:3], 0\nv_mfma_f32_16x16x32_bf16 v[108:111], v[40:43], a[4:7], 0\n"
"ds_read_b128 v[40:43], v32 offset:4096\nv_cvt_pk_bf16_f32 v56, v64, v65\ns_waitcnt lgkmcnt(3)\nv_mfma_f32_16x16x32_bf16 v[112:115], v[44:47], v[128:131], 0\n"
"v_cvt_pk_bf16_f32 v57, v66, v67\nv_mfma_f32_16x16x32_bf16 v[116:119], v[44:47], v[132:135], 0\nv_cvt_pk_bf16_f32 v58, v80, v81\nv_mfma_f32_16x16x32_bf16 v[120:123], v[44:47], a[0:3], 0\n"
"v_cvt_pk_bf16_f32 v59, v82, v83\nv_mfma_f32_16x16x32_bf16 v[124:127], v[44:47], a[4:7], 0\nds_read_b128 v[44:47], v33 offset:4096\ns_waitcnt lgkmcnt(3)\n"
"v_mfma_f32_16x16x32_bf16 v[96:99], v[48:51], v[136:139], v[96:99]\nv_mfma_f32_16x16x32_bf16 v[100:103], v[48:51], v[140:143], v[100:103]\nbuffer_store_dwordx4 v[56:59], v15, s[20:23], s50 offen sc1\nv_mfma_f32_16x16x32_bf16 v[104:107], v[48:51], a[8:11], v[104:107]\n"
"v_cvt_pk_bf16_f32 v60, v68, v69\nv_mfma_f32_16x16x32_bf16 v[108:111], v[48:51], a[12:15], v[108:111]\nds_read_b128 v[48:51], v34 offset:4096\nv_cvt_pk_bf16_f32 v61, v70, v71\n"
"s_waitcnt lgkmcnt(3)\nv_mfma_f32_16x16x32_bf16 v[112:115], v[52:55], v[136:139], v[112:115]\nv_cvt_pk_bf16_f32 v62, v84, v85\nv_mfma_f32_16x16x32_bf16 v[116:119], v[52:55], v[140:143], v[116:119]\n"
"v_cvt_pk_bf16_f32 v63, v86, v87\nv_mfma_f32_16x16x32_bf16 v[120:123], v[52:55], a[8:11], v[120:123]\nv_mfma_f32_16x16x32_bf16 v[124:127], v[52:55], a[12:15], v[124:127]\nds_read_b128 v[52:55], v35 offset:4096\n"
"s_waitcnt lgkmcnt(3)\nv_mfma_f32_16x16x32_bf16 v[96:99], v[40:43], v[144:147], v[96:99]\nbuffer_store_dwordx4 v[60:63], v16, s[20:23], s50 offen sc1\nv_mfma_f32_16x16x32_bf16 v[100:103], v[40:43], v[148:151], v[100:103]\n"
"v_cvt_pk_bf16_f32 v56, v72, v73\nv_mfma_f32_16x16x32_bf16 v[104:107], v[40:43], a[16:19], v[104:107]\nv_cvt_pk_bf16_f32 v57, v74, v75\nv_mfma_f32_16x16x32_bf16 v[108:111], v[40:43], a[20:23], v[108:111]\n"
"ds_read_b128 v[40:43], v32 offset:8192\nv_cvt_pk_bf16_f32 v58, v88, v89\ns_waitcnt lgkmcnt(3)\nv_mfma_f32_16x16x32_bf16 v[112:115], v[44:47], v[144:147], v[112:115]\n"
"v_cvt_pk_bf16_f32 v59, v90, v91\nv_mfma_f32_16x16x32_bf16 v[116:119], v[44:47], v[148:151], v[116:119]\nv_mfma_f32_16x16x32_bf16 v[120:123], v[44:47], a[16:19], v[120:123]\nv_mfma_f32_16x16x32_bf16 v[124:127], v[44:47], a[20:23], v[124:127]\n"
"ds_read_b128 v[44:47], v33 offset:8192\nbuffer_store_dwordx4 v[56:59], v17, s[20:23], s50 offen sc1\ns_waitcnt lgkmcnt(3)\nv_mfma_f32_16x16x32_bf16 v[96:99], v[48:51], v[152:155], v[96:99]\n"
"v_cvt_pk_bf16_f32 v60, v76, v77\nv_mfma_f32_16x16x32_bf16 v[100:103], v[48:51], v[156:159], v[100:103]\nv_cvt_pk_bf16_f32 v61, v78, v79\nv_mfma_f32_16x16x32_bf16 v[104:107], v[48:51], a[24:27], v[104:107]\n"
"v_cvt_pk_bf16_f32 v62, v92, v93\nv_mfma_f32_16x16x32_bf16 v[108:111], v[48:51], a[28:31], v[108:111]\nds_read_b128 v[48:51], v34 offset:8192\nv_cvt_pk_bf16_f32 v63, v94, v95\n"
"s_waitcnt lgkmcnt(3)\nv_mfma_f32_16x16x32_bf16 v[112:115], v[52:55], v[152:155], v[112:115]\nv_mfma_f32_16x16x32_bf16 v[116:119], v[52:55], v[156:159], v[116:119]\nv_mfma_f32_16x16x32_bf16 v[120:123], v[52:55], a[24:27], v[120:123]\n"
"buffer_store_dwordx4 v[60:63], v18, s[20:23], s50 offen sc1\nv_mfma_f32_16x16x32_bf16 v[124:127], v[52:55], a[28:31], v[124:127]\nds_read_b128 v[52:55], v35 offset:8192\ns_add_u32 s50, s50, s36\n"
"s_waitcnt lgkmcnt(3)\nv_mfma_f32_16x16x32_bf16 v[96:99], v[40:43], v[160:163], v[96:99]\ns_add_u32 m0, s52, 0\nv_mfma_f32_16x16x32_bf16 v[100:103], v[40:43], v[164:167], v[100:103]\n"
"s_add_u32 s53, s49, 0\nv_mfma_f32_16x16x32_bf16 v[104:107], v[40:43], a[32:35], v[104:107]\nbuffer_load_dwordx4 v10, s[12:15], s53 offen lds\nv_mfma_f32_16x16x32_bf16 v[108:111], v[40:43], a[36:39], v[108:111]\n"
"ds_read_b128 v[40:43], v32 offset:12288\ns_waitcnt lgkmcnt(3)\nv_mfma_f32_16x16x32_bf16 v[112:115], v[44:47], v[160:163], v[112:115]\nv_mfma_f32_16x16x32_bf16 v[116:119], v[44:47], v[164:167], v[116:119]\n"
"v_mfma_f32_16x16x32_bf16 v[120:123], v[44:47], a[32:35], v[120:123]\nv_mfma_f32_16x16x32_bf16 v[124:127], v[44:47], a[36:39], v[124:127]\nds_read_b128 v[44:47], v33 offset:12288\ns_waitcnt lgkmcnt(3)\n"
"v_mfma_f32_16x16x32_bf16 v[96:99], v[48:51], v[168:171], v[96:99]\nv_mfma_f32_16x16x32_bf16 v[100:103], v[48:51], v[172:175], v[100:103]\nv_mfma_f32_16x16x32_bf16 v[104:107], v[48:51], a[40:43], v[104:107]\nv_mfma_f32_16x16x32_bf16 v[108:111], v[48:51], a[44:47], v[108:111]\n"
"ds_read_b128 v[48:51], v34 offset:12288\ns_add_u32 m0, s52, 4096\ns_waitcnt lgkmcnt(3)\nv_mfma_f32_16x16x32_bf16 v[112:115], v[52:55], v[168:171], v[112:115]\n"
"s_add_u32 s53, s49, 128\nv_mfma_f32_16x16x32_bf16 v[116:119], v[52:55], v[172:175], v[116:119]\nbuffer_load_dwordx4 v10, s[12:15], s53 offen lds\nv_mfma_f32_16x16x32_bf16 v[120:123], v[52:55], a[40:43], v[120:123]\n"
"v_mfma_f32_16x16x32_bf16 v[124:127], v[52:55], a[44:47], v[124:127]\nds_read_b128 v[52:55], v35 offset:12288\ns_waitcnt lgkmcnt(3)\nv_mfma_f32_16x16x32_bf16 v[96:99], v[40:43], v[176:179], v[96:99]\n"
"v_mfma_f32_16x16x32_bf16 v[100:103], v[40:43], v[180:183], v[100:103]\nv_mfma_f32_16x16x32_bf16 v[104:107], v[40:43], a[48:51], v[104:107]\nv_mfma_f32_16x16x32_bf16 v[108:111], v[40:43], a[52:55], v[108:111]\nds_read_b128 v[40:43], v32 offset:16384\n"
"s_waitcnt lgkmcnt(3)\nv_mfma_f32_16x16x32_bf16 v[112:115], v[44:47], v[176:179], v[112:115]\nv_mfma_f32_16x16x32_bf16 v[116:119], v[44:47], v[180:183], v[116:119]\nv_mfma_f32_16x16x32_bf16 v[120:123], v[44:47], a[48:51], v[120:123]\n"
"s_add_u32 m0, s52, 8192\nv_mfma_f32_16x16x32_bf16 v[124:127], v[44:47], a[52:55], v[124:127]\nds_read_b128 v[44:47], v33 offset:16384\ns_add_u32 s53, s49, 256\n"
"s_waitcnt lgkmcnt(3)\nv_mfma_f32_16x16x32_bf16 v[96:99], v[48:51], v[184:187], v[96:99]\nbuffer_load_dwordx4 v10, s[12:15], s53 offen lds\nv_mfma_f32_16x16x32_bf16 v[100:103], v[48:51], v[188:191], v[100:103]\n"
"v_mfma_f32_16x16x32_bf16 v[104:107], v[48:51], a[56:59], v[104:107]\nv_mfma_f32_16x16x32_bf16 v[108:111], v[48:51], a[60:63], v[108:111]\nds_read_b128 v[48:51], v34 offset:16384\ns_waitcnt lgkmcnt(3)\n"
"v_mfma_f32_16x16x32_bf16 v[112:115], v[52:55], v[184:187], v[112:115]\nv_mfma_f32_16x16x32_bf16 v[116:119], v[52:55], v[188:191], v[116:119]\nv_mfma_f32_16x16x32_bf16 v[120:123], v[52:55], a[56:59], v[120:123]\nv_mfma_f32_16x16x32_bf16 v[124:127], v[52:55], a[60:63], v[124:127]\n"
"ds_read_b128 v[52:55], v35 offset:16384\ns_waitcnt lgkmcnt(3)\nv_mfma_f32_16x16x32_bf16 v[96:99], v[40:43], v[192:195], v[96:99]\nv_mfma_f32_16x16x32_bf16 v[100:103], v[40:43], v[196:199], v[100:103]\n"
"s_add_u32 m0, s52, 12288\nv_mfma_f32_16x16x32_bf16 v[104:107], v[40:43], a[64:67], v[104:107]\ns_add_u32 s53, s49, 384\nv_mfma_f32_16x16x32_bf16 v[108:111], v[40:43], a[68:71], v[108:111]\n"
"ds_read_b128 v[40:43], v32 offset:20480\nbuffer_load_dwordx4 v10, s[12:15], s53 offen lds\ns_waitcnt lgkmcnt(3)\nv_mfma_f32_16x16x32_bf16 v[112:115], v[44:47], v[192:195], v[112:115]\n"
"v_mfma_f32_16x16x32_bf16 v[116:119], v[44:47], v[196:199], v[116:119]\nv_mfma_f32_16x16x32_bf16 v[120:123], v[44:47], a[64:67], v[120:123]\nv_mfma_f32_16x16x32_bf16 v[124:127], v[44:47], a[68:71], v[124:127]\nds_read_b128 v[44:47], v33 offset:20480\n"
"s_waitcnt lgkmcnt(3)\nv_mfma_f32_16x16x32_bf16 v[96:99], v[48:51], v[200:203], v[96:99]\nv_mfma_f32_16x16x32_bf16 v[100:103], v[48:51], v[204:207], v[100:103]\nv_mfma_f32_16x16x32_bf16 v[104:107], v[48:51], a[72:75], v[104:107]\n"
"v_mfma_f32_16x16x32_bf16 v[108:111], v[48:51], a[76:79], v[108:111]\nds_read_b128 v[48:51], v34 offset:20480\ns_waitcnt lgkmcnt(3)\nv_mfma_f32_16x16x32_bf16 v[112:115], v[52:55], v[200:203], v[112:115]\n"
"s_add_u32 m0, s52, 16384\nv_mfma_f32_16x16x32_bf16 v[116:119], v[52:55], v[204:207], v[116:119]\ns_add_u32 s53, s49, 512\nv_mfma_f32_16x16x32_bf16 v[120:123], v[52:55], a[72:75], v[120:123]\n"
"buffer_load_dwordx4 v10, s[12:15], s53 offen lds\nv_mfma_f32_16x16x32_bf16 v[124:127], v[52:55], a[76:79], v[124:127]\nds_read_b128 v[52:55], v35 offset:20480\ns_waitcnt lgkmcnt(3)\n"
"v_mfma_f32_16x16x32_bf16 v[96:99], v[40:43], v[208:211], v[96:99]\nv_mfma_f32_16x16x32_bf16 v[100:103], v[40:43], v[212:215], v[100:103]\nv_mfma_f32_16x16x32_bf16 v[104:107], v[40:43], a[80:83], v[104:107]\nv_mfma_f32_16x16x32_bf16 v[108:111], v[40:43], a[84:87], v[108:111]\n"
"ds_read_b128 v[40:43], v32 offset:24576\ns_waitcnt lgkmcnt(3)\nv_mfma_f32_16x16x32_bf16 v[112:115], v[44:47], v[208:211], v[112:115]\nv_mfma_f32_16x16x32_bf16 v[116:119], v[44:47], v[212:215], v[116:119]\n"
"v_mfma_f32_16x16x32_bf16 v[120:123], v[44:47], a[80:83], v[120:123]\nv_mfma_f32_16x16x32_bf16 v[124:127], v[44:47], a[84:87], v[124:127]\nds_read_b128 v[44:47], v33 offset:24576\ns_add_u32 m0, s52, 20480\n"
"s_waitcnt lgkmcnt(3)\nv_mfma_f32_16x16x32_bf16 v[96:99], v[48:51], v[216:219], v[96:99]\ns_add_u32 s54, s51, 32768\ns_cmp_eq_u32 s54, 163840\n"
"v_mfma_f32_16x16x32_bf16 v[100:103], v[48:51], v[220:223], v[100:103]\nv_mfma_f32_16x16x32_bf16 v[104:107], v[48:51], a[88:91], v[104:107]\nv_mfma_f32_16x16x32_bf16 v[108:111], v[48:51], a[92:95], v[108:111]\ns_cselect_b32 s54, 0, s54\n"
"s_sub_u32 s54, s54, s51\nds_read_b128 v[48:51], v34 offset:24576\ns_add_u32 s53, s49, 640\ns_waitcnt lgkmcnt(3)\n"
"v_mfma_f32_16x16x32_bf16 v[112:115], v[52:55], v[216:219], v[112:115]\nbuffer_load_dwordx4 v10, s[12:15], s53 offen lds\nv_mfma_f32_16x16x32_bf16 v[116:119], v[52:55], v[220:223], v[116:119]\nv_mfma_f32_16x16x32_bf16 v[120:123], v[52:55], a[88:91], v[120:123]\n"
"v_mfma_f32_16x16x32_bf16 v[124:127], v[52:55], a[92:95], v[124:127]\nds_read_b128 v[52:55], v35 offset:24576\ns_waitcnt lgkmcnt(3)\nv_mfma_f32_16x16x32_bf16 v[96:99], v[40:43], v[224:227], v[96:99]\n"
"v_add_u32 v32, s54, v32\nv_mfma_f32_16x16x32_bf16 v[100:103], v[40:43], v[228:231], v[100:103]\nv_mfma_f32_16x16x32_bf16 v[104:107], v[40:43], a[96:99], v[104:107]\nv_mfma_f32_16x16x32_bf16 v[108:111], v[40:43], a[100:103], v[108:111]\n"
"ds_read_b128 v[40:43], v32 offset:0\ns_waitcnt lgkmcnt(3)\nv_mfma_f32_16x16x32_bf16 v[112:115], v[44:47], v[224:227], v[112:115]\nv_add_u32 v33, s54, v33\n"
"v_mfma_f32_16x16x32_bf16 v[116:119], v[44:47], v[228:231], v[116:119]\ns_add_u32 m0, s52, 24576\nv_mfma_f32_16x16x32_bf16 v[120:123], v[44:47], a[96:99], v[120:123]\ns_add_u32 s53, s49, 768\n"
"v_mfma_f32_16x16x32_bf16 v[124:127], v[44:47], a[100:103], v[124:127]\nds_read_b128 v[44:47], v33 offset:0\nbuffer_load_dwordx4 v10, s[12:15], s53 offen lds\ns_waitcnt lgkmcnt(3)\n"
"v_mfma_f32_16x16x32_bf16 v[96:99], v[48:51], v[232:235], v[96:99]\nv_add_u32 v34, s54, v34\nv_mfma_f32_16x16x32_bf16 v[100:103], v[48:51], v[236:239], v[100:103]\nv_mfma_f32_16x16x32_bf16 v[104:107], v[48:51], a[104:107], v[104:107]\n"
"v_mfma_f32_16x16x32_bf16 v[108:111], v[48:51], a[108:111], v[108:111]\nds_read_b128 v[48:51], v34 offset:0\ns_waitcnt lgkmcnt(3)\nv_mfma_f32_16x16x32_bf16 v[112:115], v[52:55], v[232:235], v[112:115]\n"
"v_add_u32 v35, s54, v35\nv_mfma_f32_16x16x32_bf16 v[116:119], v[52:55], v[236:239], v[116:119]\nv_mfma_f32_16x16x32_bf16 v[120:123], v[52:55], a[104:107], v[120:123]\nv_mfma_f32_16x16x32_bf16 v[124:127], v[52:55], a[108:111], v[124:127]\n"
"ds_read_b128 v[52:55], v35 offset:0\ns_mov_b32 s52, s47\ns_add_u32 s47, s51, s32\ns_add_u32 s51, s51, s54\n"
"s_add_u32 s49, s49, 28672\n.p2align 6\nfg1_loop:\ns_waitcnt vmcnt(7)\n"
"s_barrier\ns_waitcnt lgkmcnt(3)\nv_mfma_f32_16x16x32_bf16 v[64:67], v[40:43], v[128:131], 0\nv_mov_b32 v36, s48\n"
"s_add_u32 s48, s48, 1\ns_nop 0\nbuffer_store_dword v36, v19, s[24:27], s33 offen sc1\nv_mfma_f32_16x16x32_bf16 v[68:71], v[40:43], v[132:135], 0\n"
"v_mfma_f32_16x16x32_bf16 v[72:75], v[40:43], a[0:3], 0\nv_mfma_f32_16x16x32_bf16 v[76:79], v[40:43], a[4:7], 0\nds_read_b128 v[40:43], v32 offset:4096\nv_cvt_pk_bf16_f32 v56, v96, v97\n"
"s_waitcnt lgkmcnt(3)\nv_mfma_f32_16x16x32_bf16 v[80:83], v[44:47], v[128:131], 0\nv_cvt_pk_bf16_f32 v57, v98, v99\nv_mfma_f32_16x16x32_bf16 v[84:87], v[44:47], v[132:135], 0\n"
"v_cvt_pk_bf16_f32 v58, v112, v113\nv_mfma_f32_16x16x32_bf16 v[88:91], v[44:47], a[0:3], 0\nv_cvt_pk_bf16_f32 v59, v114, v115\nv_mfma_f32_16x16x32_bf16 v[92:95], v[44:47], a[4:7], 0\n"
"ds_read_b128 v[44:47], v33 offset:4096\ns_waitcnt lgkmcnt(3)\nv_mfma_f32_16x16x32_bf16 v[64:67], v[48:51], v[136:139], v[64:67]\nv_mfma_f32_16x16x32_bf16 v[68:71], v[48:51], v[140:143], v[68:71]\n"
"buffer_store_dwordx4 v[56:59], v15, s[20:23], s50 offen sc1\nv_mfma_f32_16x16x32_bf16 v[72:75], v[48:51], a[8:11], v[72:75]\nv_cvt_pk_bf16_f32 v60, v100, v101\nv_mfma_f32_16x16x32_bf16 v[76:79], v[48:51], a[12:15], v[76:79]\n"
"ds_read_b128 v[48:51], v34 offset:4096\nv_cvt_pk_bf16_f32 v61, v102, v103\ns_waitcnt lgkmcnt(3)\nv_mfma_f32_16x16x32_bf16 v[80:83], v[52:55], v[136:139], v[80:83]\n"
"v_cvt_pk_bf16_f32 v62, v116, v117\nv_mfma_f32_16x16x32_bf16 v[84:87], v[52:55], v[140:143], v[84:87]\nv_cvt_pk_bf16_f32 v63, v118, v119\nv_mfma_f32_16x16x32_bf16 v[88:91], v[52:55], a[8:11], v[88:91]\n"
"v_mfma_f32_16x16x32_bf16 v[92:95], v[52:55], a[12:15], v[92:95]\nds_read_b128 v[52:55], v35 offset:4096\ns_waitcnt lgkmcnt(3)\nv_mfma_f32_16x16x32_bf16 v[64:67], v[40:43], v[144:147], v[64:67]\n"
"buffer_store_dwordx4 v[60:63], v16, s[20:23], s50 offen sc1\nv_mfma_f32_16x16x32_bf16 v[68:71], v[40:43], v[148:151], v[68:71]\nv_cvt_pk_bf16_f32 v56, v104, v105\nv_mfma_f32_16x16x32_bf16 v[72:75], v[40:43], a[16:19], v[72:75]\n"
"v_cvt_pk_bf16_f32 v57, v106, v107\nv_mfma_f32_16x16x32_bf16 v[76:79], v[40:43], a[20:23], v[76:79]\nds_read_b128 v[40:43], v32 offset:8192\nv_cvt_pk_bf16_f32 v58, v120, v121\n"
"s_waitcnt lgkmcnt(3)\nv_mfma_f32_16x16x32_bf16 v[80:83], v[44:47], v[144:147], v[80:83]\nv_cvt_pk_bf16_f32 v59, v122, v123\nv_mfma_f32_16x16x32_bf16 v[84:87], v[44:47], v[148:151], v[84:87]\n"
"v_mfma_f32_16x16x32_bf16 v[88:91], v[44:47], a[16:19], v[88:91]\nv_mfma_f32_16x16x32_bf16 v[92:95], v[44:47], a[20:23], v[92:95]\nds_read_b128 v[44:47], v33 offset:8192\nbuffer_store_dwordx4 v[56:59], v17, s[20:23], s50 offen sc1\n"
"s_waitcnt lgkmcnt(3)\nv_mfma_f32_16x16x32_bf16 v[64:67], v[48:51], v[152:155], v[64:67]\nv_cvt_pk_bf16_f32 v60, v108, v109\nv_mfma_f32_16x16x32_bf16 v[68:71], v[48:51], v[156:159], v[68:71]\n"
"v_cvt_pk_bf16_f32 v61, v110, v111\nv_mfma_f32_16x16x32_bf16 v[72:75], v[48:51], a[24:27], v[72:75]\nv_cvt_pk_bf16_f32 v62, v124, v125\nv_mfma_f32_16x16x32_bf16 v[76:79], v[48:51], a[28:31], v[76:79]\n"
"ds_read_b128 v[48:51], v34 offset:8192\nv_cvt_pk_bf16_f32 v63, v126, v127\ns_waitcnt lgkmcnt(3)\nv_mfma_f32_16x16x32_bf16 v[80:83], v[52:55], v[152:155], v[80:83]\n"
"v_mfma_f32_16x16x32_bf16 v[84:87], v[52:55], v[156:159], v[84:87]\nv_mfma_f32_16x16x32_bf16 v[88:91], v[52:55], a[24:27], v[88:91]\nbuffer_store_dwordx4 v[60:63], v18, s[20:23], s50 offen sc1\nv_mfma_f32_16x16x32_bf16 v[92:95], v[52:55], a[28:31], v[92:95]\n"
"ds_read_b128 v[52:55], v35 offset:8192\ns_add_u32 s50, s50, s36\ns_waitcnt lgkmcnt(3)\nv_mfma_f32_16x16x32_bf16 v[64:67], v[40:43], v[160:163], v[64:67]\n"
"s_add_u32 m0, s52, 0\nv_mfma_f32_16x16x32_bf16 v[68:71], v[40:43], v[164:167], v[68:71]\ns_add_u32 s53, s49, 0\nv_mfma_f32_16x16x32_bf16 v[72:75], v[40:43], a[32:35], v[72:75]\n"
"buffer_load_dwordx4 v10, s[12:15], s53 offen lds\nv_mfma_f32_16x16x32_bf16 v[76:79], v[40:43], a[36:39], v[76:79]\nds_read_b128 v[40:43], v32 offset:12288\ns_waitcnt lgkmcnt(3)\n"
"v_mfma_f32_16x16x32_bf16 v[80:83], v[44:47], v[160:163], v[80:83]\nv_mfma_f32_16x16x32_bf16 v[84:87], v[44:47], v[164:167], v[84:87]\nv_mfma_f32_16x16x32_bf16 v[88:91], v[44:47], a[32:35], v[88:91]\nv_mfma_f32_16x16x32_bf16 v[92:95], v[44:47], a[36:39], v[92:95]\n"
"ds_read_b128 v[44:47], v33 offset:12288\ns_waitcnt lgkmcnt(3)\nv_mfma_f32_16x16x32_bf16 v[64:67], v[48:51], v[168:171], v[64:67]\nv_mfma_f32_16x16x32_bf16 v[68:71], v[48:51], v[172:175], v[68:71]\n"
"v_mfma_f32_16x16x32_bf16 v[72:75], v[48:51], a[40:43], v[72:75]\nv_mfma_f32_16x16x32_bf16 v[76:79], v[48:51], a[44:47], v[76:79]\nds_read_b128 v[48:51], v34 offset:12288\ns_add_u32 m0, s52, 4096\n"
"s_waitcnt lgkmcnt(3)\nv_mfma_f32_16x16x32_bf16 v[80:83], v[52:55], v[168:171], v[80:83]\ns_add_u32 s53, s49, 128\nv_mfma_f32_16x16x32_bf16 v[84:87], v[52:55], v[172:175], v[84:87]\n"
"buffer_load_dwordx4 v10, s[12:15], s53 offen lds\nv_mfma_f32_16x16x32_bf16 v[88:91], v[52:55], a[40:43], v[88:91]\nv_mfma_f32_16x16x32_bf16 v[92:95], v[52:55], a[44:47], v[92:95]\nds_read_b128 v[52:55], v35 offset:12288\n"
"s_waitcnt lgkmcnt(3)\nv_mfma_f32_16x16x32_bf16 v[64:67], v[40:43], v[176:179], v[64:67]\nv_mfma_f32_16x16x32_bf16 v[68:71], v[40:43], v[180:183], v[68:71]\nv_mfma_f32_16x16x32_bf16 v[72:75], v[40:43], a[48:51], v[72:75]\n"
"v_mfma_f32_16x16x32_bf16 v[76:79], v[40:43], a[52:55], v[76:79]\nds_read_b128 v[40:43], v32 offset:16384\ns_waitcnt lgkmcnt(3)\nv_mfma_f32_16x16x32_bf16 v[80:83], v[44:47], v[176:179], v[80:83]\n"
"v_mfma_f32_16x16x32_bf16 v[84:87], v[44:47], v[180:183], v[84:87]\nv_mfma_f32_16x16x32_bf16 v[88:91], v[44:47], a[48:51], v[88:91]\ns_add_u32 m0, s52, 8192\nv_mfma_f32_16x16x32_bf16 v[92:95], v[44:47], a[52:55], v[92:95]\n"
"ds_read_b128 v[44:47], v33 offset:16384\ns_add_u32 s53, s49, 256\ns_waitcnt lgkmcnt(3)\nv_mfma_f32_16x16x32_bf16 v[64:67], v[48:51], v[184:187], v[64:67]\n"
"buffer_load_dwordx4 v10, s[12:15], s53 offen lds\nv_mfma_f32_16x16x32_bf16 v[68:71], v[48:51], v[188:191], v[68:71]\nv_mfma_f32_16x16x32_bf16 v[72:75], v[48:51], a[56:59], v[72:75]\nv_mfma_f32_16x16x32_bf16 v[76:79], v[48:51], a[60:63], v[76:79]\n"
"ds_read_b128 v[48:51], v34 offset:16384\ns_waitcnt lgkmcnt(3)\nv_mfma_f32_16x16x32_bf16 v[80:83], v[52:55], v[184:187], v[80:83]\nv_mfma_f32_16x16x32_bf16 v[84:87], v[52:55], v[188:191], v[84:87]\n"
"v_mfma_f32_16x16x32_bf16 v[88:91], v[52:55], a[56:59], v[88:91]\nv_mfma_f32_16x16x32_bf16 v[92:95], v[52:55], a[60:63], v[92:95]\nds_read_b128 v[52:55], v35 offset:16384\ns_waitcnt lgkmcnt(3)\n"
"v_mfma_f32_16x16x32_bf16 v[64:67], v[40:43], v[192:195], v[64:67]\nv_mfma_f32_16x16x32_bf16 v[68:71], v[40:43], v[196:199], v[68:71]\ns_add_u32 m0, s52, 12288\nv_mfma_f32_16x16x32_bf16 v[72:75], v[40:43], a[64:67], v[72:75]\n"
"s_add_u32 s53, s49, 384\nv_mfma_f32_16x16x32_bf16 v[76:79], v[40:43], a[68:71], v[76:79]\nds_read_b128 v[40:43], v32 offset:20480\nbuffer_load_dwordx4 v10, s[12:15], s53 offen lds\n"
"s_waitcnt lgkmcnt(3)\nv_mfma_f32_16x16x32_bf16 v[80:83], v[44:47], v[192:195], v[80:83]\nv_mfma_f32_16x16x32_bf16 v[84:87], v[44:47], v[196:199], v[84:87]\nv_mfma_f32_16x16x32_bf16 v[88:91], v[44:47], a[64:67], v[88:91]\n"
"v_mfma_f32_16x16x32_bf16 v[92:95], v[44:47], a[68:71], v[92:95]\nds_read_b128 v[44:47], v33 offset:20480\ns_waitcnt lgkmcnt(3)\nv_mfma_f32_16x16x32_bf16 v[64:67], v[48:51], v[200:203], v[64:67]\n"
"v_mfma_f32_16x16x32_bf16 v[68:71], v[48:51], v[204:207], v[68:71]\nv_mfma_f32_16x16x32_bf16 v[72:75], v[48:51], a[72:75], v[72:75]\nv_mfma_f32_16x16x32_bf16 v[76:79], v[48:51], a[76:79], v[76:79]\nds_read_b128 v[48:51], v34 offset:20480\n"
"s_waitcnt lgkmcnt(3)\nv_mfma_f32_16x16x32_bf16 v[80:83], v[52:55], v[200:203], v[80:83]\ns_add_u32 m0, s52, 16384\nv_mfma_f32_16x16x32_bf16 v[84:87], v[52:55], v[204:207], v[84:87]\n"
"s_add_u32 s53, s49, 512\nv_mfma_f32_16x16x32_bf16 v[88:91], v[52:55], a[72:75], v[88:91]\nbuffer_load_dwordx4 v10, s[12:15], s53 offen lds\nv_mfma_f32_16x16x32_bf16 v[92:95], v[52:55], a[76:79], v[92:95]\n"
"ds_read_b128 v[52:55], v35 offset:20480\ns_waitcnt lgkmcnt(3)\nv_mfma_f32_16x16x32_bf16 v[64:67], v[40:43], v[208:211], v[64:67]\nv_mfma_f32_16x16x32_bf16 v[68:71], v[40:43], v[212:215], v[68:71]\n"
"v_mfma_f32_16x16x32_bf16 v[72:75], v[40:43], a[80:83], v[72:75]\nv_mfma_f32_16x16x32_bf16 v[76:79], v[40:43], a[84:87], v[76:79]\nds_read_b128 v[40:43], v32 offset:24576\ns_waitcnt lgkmcnt(3)\n"
"v_mfma_f32_16x16x32_bf16 v[80:83], v[44:47], v[208:211], v[80:83]\nv_mfma_f32_16x16x32_bf16 v[84:87], v[44:47], v[212:215], v[84:87]\nv_mfma_f32_16x16x32_bf16 v[88:91], v[44:47], a[80:83], v[88:91]\nv_mfma_f32_16x16x32_bf16 v[92:95], v[44:47], a[84:87], v[92:95]\n"
"ds_read_b128 v[44:47], v33 offset:24576\ns_add_u32 m0, s52, 20480\ns_waitcnt lgkmcnt(3)\nv_mfma_f32_16x16x32_bf16 v[64:67], v[48:51], v[216:219], v[64:67]\n"
"s_add_u32 s54, s51, 32768\ns_cmp_eq_u32 s54, 163840\nv_mfma_f32_16x16x32_bf16 v[68:71], v[48:51], v[220:223], v[68:71]\nv_mfma_f32_16x16x32_bf16 v[72:75], v[48:51], a[88:91], v[72:75]\n"
"v_mfma_f32_16x16x32_bf16 v[76:79], v[48:51], a[92:95], v[76:79]\ns_cselect_b32 s54, 0, s54\ns_sub_u32 s54, s54, s51\nds_read_b128 v[48:51], v34 offset:24576\n"
"s_add_u32 s53, s49, 640\ns_waitcnt lgkmcnt(3)\nv_mfma_f32_16x16x32_bf16 v[80:83], v[52:55], v[216:219], v[80:83]\nbuffer_load_dwordx4 v10, s[12:15], s53 offen lds\n"
"v_mfma_f32_16x16x32_bf16 v[84:87], v[52:55], v[220:223], v[84:87]\nv_mfma_f32_16x16x32_bf16 v[88:91], v[52:55], a[88:91], v[88:91]\nv_mfma_f32_16x16x32_bf16 v[92:95], v[52:55], a[92:95], v[92:95]\nds_read_b128 v[52:55], v35 offset:24576\n"
"s_waitcnt lgkmcnt(3)\nv_mfma_f32_16x16x32_bf16 v[64:67], v[40:43], v[224:227], v[64:67]\nv_add_u32 v32, s54, v32\nv_mfma_f32_16x16x32_bf16 v[68:71], v[40:43], v[228:231], v[68:71]\n"
"v_mfma_f32_16x16x32_bf16 v[72:75], v[40:43], a[96:99], v[72:75]\nv_mfma_f32_16x16x32_bf16 v[76:79], v[40:43], a[100:103], v[76:79]\nds_read_b128 v[40:43], v32 offset:0\ns_waitcnt lgkmcnt(3)\n"
"v_mfma_f32_16x16x32_bf16 v[80:83], v[44:47], v[224:227], v[80:83]\nv_add_u32 v33, s54, v33\nv_mfma_f32_16x16x32_bf16 v[84:87], v[44:47], v[228:231], v[84:87]\ns_add_u32 m0, s52, 24576\n"
"v_mfma_f32_16x16x32_bf16 v[88:91], v[44:47], a[96:99], v[88:91]\ns_add_u32 s53, s49, 768\nv_mfma_f32_16x16x32_bf16 v[92:95], v[44:47], a[100:103], v[92:95]\nds_read_b128 v[44:47], v33 offset:0\n"
"buffer_load_dwordx4 v10, s[12:15], s53 offen lds\ns_waitcnt lgkmcnt(3)\nv_mfma_f32_16x16x32_bf16 v[64:67], v[48:51], v[232:235], v[64:67]\nv_add_u32 v34, s54, v34\n"
"v_mfma_f32_16x16x32_bf16 v[68:71], v[48:51], v[236:239], v[68:71]\nv_mfma_f32_16x16x32_bf16 v[72:75], v[48:51], a[104:107], v[72:75]\nv_mfma_f32_16x16x32_bf16 v[76:79], v[48:51], a[108:111], v[76:79]\nds_read_b128 v[48:51], v34 offset:0\n"
"s_waitcnt lgkmcnt(3)\nv_mfma_f32_16x16x32_bf16 v[80:83], v[52:55], v[232:235], v[80:83]\nv_add_u32 v35, s54, v35\nv_mfma_f32_16x16x32_bf16 v[84:87], v[52:55], v[236:239], v[84:87]\n"
"v_mfma_f32_16x16x32_bf16 v[88:91], v[52:55], a[104:107], v[88:91]\nv_mfma_f32_16x16x32_bf16 v[92:95], v[52:55], a[108:111], v[92:95]\nds_read_b128 v[52:55], v35 offset:0\ns_mov_b32 s52, s47\n"
"s_add_u32 s47, s51, s32\ns_add_u32 s51, s51, s54\ns_add_u32 s49, s49, 28672\ns_waitcnt vmcnt(7)\n"
"s_barrier\ns_waitcnt lgkmcnt(3)\nv_mfma_f32_16x16x32_bf16 v[96:99], v[40:43], v[128:131], 0\ns_add_u32 s48, s48, 1\n"
"v_mfma_f32_16x16x32_bf16 v[100:103], v[40:43], v[132:135], 0\nv_mfma_f32_16x16x32_bf16 v[104:107], v[40:43], a[0:3], 0\nv_mfma_f32_16x16x32_bf16 v[108:111], v[40:43], a[4:7], 0\nds_read_b128 v[40:43], v32 offset:4096\n"
"v_cvt_pk_bf16_f32 v56, v64, v65\ns_waitcnt lgkmcnt(3)\nv_mfma_f32_16x16x32_bf16 v[112:115], v[44:47], v[128:131], 0\nv_cvt_pk_bf16_f32 v57, v66, v67\n"
"v_mfma_f32_16x16x32_bf16 v[116:119], v[44:47], v[132:135], 0\nv_cvt_pk_bf16_f32 v58, v80, v81\nv_mfma_f32_16x16x32_bf16 v[120:123], v[44:47], a[0:3], 0\nv_cvt_pk_bf16_f32 v59, v82, v83\n"
"v_mfma_f32_16x16x32_bf16 v[124:127], v[44:47], a[4:7], 0\nds_read_b128 v[44:47], v33 offset:4096\ns_waitcnt lgkmcnt(3)\nv_mfma_f32_16x16x32_bf16 v[96:99], v[48:51], v[136:139], v[96:99]\n"
"v_mfma_f32_16x16x32_bf16 v[100:103], v[48:51], v[140:143], v[100:103]\nbuffer_store_dwordx4 v[56:59], v15, s[20:23], s50 offen sc1\nv_mfma_f32_16x16x32_bf16 v[104:107], v[48:51], a[8:11], v[104:107]\nv_cvt_pk_bf16_f32 v60, v68, v69\n"
"v_mfma_f32_16x16x32_bf16 v[108:111], v[48:51], a[12:15], v[108:111]\nds_read_b128 v[48:51], v34 offset:4096\nv_cvt_pk_bf16_f32 v61, v70, v71\ns_waitcnt lgkmcnt(3)\n"
"v_mfma_f32_16x16x32_bf16 v[112:115], v[52:55], v[136:139], v[112:115]\nv_cvt_pk_bf16_f32 v62, v84, v85\nv_mfma_f32_16x16x32_bf16 v[116:119], v[52:55], v[140:143], v[116:119]\nv_cvt_pk_bf16_f32 v63, v86, v87\n"
"v_mfma_f32_16x16x32_bf16 v[120:123], v[52:55], a[8:11], v[120:123]\nv_mfma_f32_16x16x32_bf16 v[124:127], v[52:55], a[12:15], v[124:127]\nds_read_b128 v[52:55], v35 offset:4096\ns_waitcnt lgkmcnt(3)\n"
"v_mfma_f32_16x16x32_bf16 v[96:99], v[40:43], v[144:147], v[96:99]\nbuffer_store_dwordx4 v[60:63], v16, s[20:23], s50 offen sc1\nv_mfma_f32_16x16x32_bf16 v[100:103], v[40:43], v[148:151], v[100:103]\nv_cvt_pk_bf16_f32 v56, v72, v73\n"
"v_mfma_f32_16x16x32_bf16 v[104:107], v[40:43], a[16:19], v[104:107]\nv_cvt_pk_bf16_f32 v57, v74, v75\nv_mfma_f32_16x16x32_bf16 v[108:111], v[40:43], a[20:23], v[108:111]\nds_read_b128 v[40:43], v32 offset:8192\n"
"v_cvt_pk_bf16_f32 v58, v88, v89\ns_waitcnt lgkmcnt(3)\nv_mfma_f32_16x16x32_bf16 v[112:115], v[44:47], v[144:147], v[112:115]\nv_cvt_pk_bf16_f32 v59, v90, v91\n"
"v_mfma_f32_16x16x32_bf16 v[116:119], v[44:47], v[148:151], v[116:119]\nv_mfma_f32_16x16x32_bf16 v[120:123], v[44:47], a[16:19], v[120:123]\nv_mfma_f32_16x16x32_bf16 v[124:127], v[44:47], a[20:23], v[124:127]\nds_read_b128 v[44:47], v33 offset:8192\n"
"buffer_store_dwordx4 v[56:59], v17, s[20:23], s50 offen sc1\ns_waitcnt lgkmcnt(3)\nv_mfma_f32_16x16x32_bf16 v[96:99], v[48:51], v[152:155], v[96:99]\nv_cvt_pk_bf16_f32 v60, v76, v77\n"
"v_mfma_f32_16x16x32_bf16 v[100:103], v[48:51], v[156:159], v[100:103]\nv_cvt_pk_bf16_f32 v61, v78, v79\nv_mfma_f32_16x16x32_bf16 v[104:107], v[48:51], a[24:27], v[104:107]\nv_cvt_pk_bf16_f32 v62, v92, v93\n"
"v_mfma_f32_16x16x32_bf16 v[108:111], v[48:51], a[28:31], v[108:111]\nds_read_b128 v[48:51], v34 offset:8192\nv_cvt_pk_bf16_f32 v63, v94, v95\ns_waitcnt lgkmcnt(3)\n"
"v_mfma_f32_16x16x32_bf16 v[112:115], v[52:55], v[152:155], v[112:115]\nv_mfma_f32_16x16x32_bf16 v[116:119], v[52:55], v[156:159], v[116:119]\nv_mfma_f32_16x16x32_bf16 v[120:123], v[52:55], a[24:27], v[120:123]\nbuffer_store_dwordx4 v[60:63], v18, s[20:23], s50 offen sc1\n"
"v_mfma_f32_16x16x32_bf16 v[124:127], v[52:55], a[28:31], v[124:127]\nds_read_b128 v[52:55], v35 offset:8192\ns_add_u32 s50, s50, s36\ns_waitcnt lgkmcnt(3)\n"
"v_mfma_f32_16x16x32_bf16 v[96:99], v[40:43], v[160:163], v[96:99]\ns_add_u32 m0, s52, 0\nv_mfma_f32_16x16x32_bf16 v[100:103], v[40:43], v[164:167], v[100:103]\ns_add_u32 s53, s49, 0\n"
"v_mfma_f32_16x16x32_bf16 v[104:107], v[40:43], a[32:35], v[104:107]\nbuffer_load_dwordx4 v10, s[12:15], s53 offen lds\nv_mfma_f32_16x16x32_bf16 v[108:111], v[40:43], a[36:39], v[108:111]\nds_read_b128 v[40:43], v32 offset:12288\n"
"s_waitcnt lgkmcnt(3)\nv_mfma_f32_16x16x32_bf16 v[112:115], v[44:47], v[160:163], v[112:115]\nv_mfma_f32_16x16x32_bf16 v[116:119], v[44:47], v[164:167], v[116:119]\nv_mfma_f32_16x16x32_bf16 v[120:123], v[44:47], a[32:35], v[120:123]\n"
"v_mfma_f32_16x16x32_bf16 v[124:127], v[44:47], a[36:39], v[124:127]\nds_read_b128 v[44:47], v33 offset:12288\ns_waitcnt lgkmcnt(3)\nv_mfma_f32_16x16x32_bf16 v[96:99], v[48:51], v[168:171], v[96:99]\n"
"v_mfma_f32_16x16x32_bf16 v[100:103], v[48:51], v[172:175], v[100:103]\nv_mfma_f32_16x16x32_bf16 v[104:107], v[48:51], a[40:43], v[104:107]\nv_mfma_f32_16x16x32_bf16 v[108:111], v[48:51], a[44:47], v[108:111]\nds_read_b128 v[48:51], v34 offset:12288\n"
"s_add_u32 m0, s52, 4096\ns_waitcnt lgkmcnt(3)\nv_mfma_f32_16x16x32_bf16 v[112:115], v[52:55], v[168:171], v[112:115]\ns_add_u32 s53, s49, 128\n"
"v_mfma_f32_16x16x32_bf16 v[116:119], v[52:55], v[172:175], v[116:119]\nbuffer_load_dwordx4 v10, s[12:15], s53 offen lds\nv_mfma_f32_16x16x32_bf16 v[120:123], v[52:55], a[40:43], v[120:123]\nv_mfma_f32_16x16x32_bf16 v[124:127], v[52:55], a[44:47], v[124:127]\n"
"ds_read_b128 v[52:55], v35 offset:12288\ns_waitcnt lgkmcnt(3)\nv_mfma_f32_16x16x32_bf16 v[96:99], v[40:43], v[176:179], v[96:99]\nv_mfma_f32_16x16x32_bf16 v[100:103], v[40:43], v[180:183], v[100:103]\n"
"v_mfma_f32_16x16x32_bf16 v[104:107], v[40:43], a[48:51], v[104:107]\nv_mfma_f32_16x16x32_bf16 v[108:111], v[40:43], a[52:55], v[108:111]\nds_read_b128 v[40:43], v32 offset:16384\ns_waitcnt lgkmcnt(3)\n"
"v_mfma_f32_16x16x32_bf16 v[112:115], v[44:47], v[176:179], v[112:115]\nv_mfma_f32_16x16x32_bf16 v[116:119], v[44:47], v[180:183], v[116:119]\nv_mfma_f32_16x16x32_bf16 v[120:123], v[44:47], a[48:51], v[120:123]\ns_add_u32 m0, s52, 8192\n"
"v_mfma_f32_16x16x32_bf16 v[124:127], v[44:47], a[52:55], v[124:127]\nds_read_b128 v[44:47], v33 offset:16384\ns_add_u32 s53, s49, 256\ns_waitcnt lgkmcnt(3)\n"
"v_mfma_f32_16x16x32_bf16 v[96:99], v[48:51], v[184:187], v[96:99]\nbuffer_load_dwordx4 v10, s[12:15], s53 offen lds\nv_mfma_f32_16x16x32_bf16 v[100:103], v[48:51], v[188:191], v[100:103]\nv_mfma_f32_16x16x32_bf16 v[104:107], v[48:51], a[56:59], v[104:107]\n"
"v_mfma_f32_16x16x32_bf16 v[108:111], v[48:51], a[60:63], v[108:111]\nds_read_b128 v[48:51], v34 offset:16384\ns_waitcnt lgkmcnt(3)\nv_mfma_f32_16x16x32_bf16 v[112:115], v[52:55], v[184:187], v[112:115]\n"
"v_mfma_f32_16x16x32_bf16 v[116:119], v[52:55], v[188:191], v[116:119]\nv_mfma_f32_16x16x32_bf16 v[120:123], v[52:55], a[56:59], v[120:123]\nv_mfma_f32_16x16x32_bf16 v[124:127], v[52:55], a[60:63], v[124:127]\nds_read_b128 v[52:55], v35 offset:16384\n"
"s_waitcnt lgkmcnt(3)\nv_mfma_f32_16x16x32_bf16 v[96:99], v[40:43], v[192:195], v[96:99]\nv_mfma_f32_16x16x32_bf16 v[100:103], v[40:43], v[196:199], v[100:103]\ns_add_u32 m0, s52, 12288\n"
"v_mfma_f32_16x16x32_bf16 v[104:107], v[40:43], a[64:67], v[104:107]\ns_add_u32 s53, s49, 384\nv_mfma_f32_16x16x32_bf16 v[108:111], v[40:43], a[68:71], v[108:111]\nds_read_b128 v[40:43], v32 offset:20480\n"
"buffer_load_dwordx4 v10, s[12:15], s53 offen lds\ns_waitcnt lgkmcnt(3)\nv_mfma_f32_16x16x32_bf16 v[112:115], v[44:47], v[192:195], v[112:115]\nv_mfma_f32_16x16x32_bf16 v[116:119], v[44:47], v[196:199], v[116:119]\n"
"v_mfma_f32_16x16x32_bf16 v[120:123], v[44:47], a[64:67], v[120:123]\nv_mfma_f32_16x16x32_bf16 v[124:127], v[44:47], a[68:71], v[124:127]\nds_read_b128 v[44:47], v33 offset:20480\ns_waitcnt lgkmcnt(3)\n"
"v_mfma_f32_16x16x32_bf16 v[96:99], v[48:51], v[200:203], v[96:99]\nv_mfma_f32_16x16x32_bf16 v[100:103], v[48:51], v[204:207], v[100:103]\nv_mfma_f32_16x16x32_bf16 v[104:107], v[48:51], a[72:75], v[104:107]\nv_mfma_f32_16x16x32_bf16 v[108:111], v[48:51], a[76:79], v[108:111]\n"
"ds_read_b128 v[48:51], v34 offset:20480\ns_waitcnt lgkmcnt(3)\nv_mfma_f32_16x16x32_bf16 v[112:115], v[52:55], v[200:203], v[112:115]\ns_add_u32 m0, s52, 16384\n"
"v_mfma_f32_16x16x32_bf16 v[116:119], v[52:55], v[204:207], v[116:119]\ns_add_u32 s53, s49, 512\nv_mfma_f32_16x16x32_bf16 v[120:123], v[52:55], a[72:75], v[120:123]\nbuffer_load_dwordx4 v10, s[12:15], s53 offen lds\n"
"v_mfma_f32_16x16x32_bf16 v[124:127], v[52:55], a[76:79], v[124:127]\nds_read_b128 v[52:55], v35 offset:20480\ns_waitcnt lgkmcnt(3)\nv_mfma_f32_16x16x32_bf16 v[96:99], v[40:43], v[208:211], v[96:99]\n"
"v_mfma_f32_16x16x32_bf16 v[100:103], v[40:43], v[212:215], v[100:103]\nv_mfma_f32_16x16x32_bf16 v[104:107], v[40:43], a[80:83], v[104:107]\nv_mfma_f32_16x16x32_bf16 v[108:111], v[40:43], a[84:87], v[108:111]\nds_read_b128 v[40:43], v32 offset:24576\n"
"s_waitcnt lgkmcnt(3)\nv_mfma_f32_16x16x32_bf16 v[112:115], v[44:47], v[208:211], v[112:115]\nv_mfma_f32_16x16x32_bf16 v[116:119], v[44:47], v[212:215], v[116:119]\nv_mfma_f32_16x16x32_bf16 v[120:123], v[44:47], a[80:83], v[120:123]\n"
"v_mfma_f32_16x16x32_bf16 v[124:127], v[44:47], a[84:87], v[124:127]\nds_read_b128 v[44:47], v33 offset:24576\ns_add_u32 m0, s52, 20480\ns_waitcnt lgkmcnt(3)\n"
"v_mfma_f32_16x16x32_bf16 v[96:99], v[48:51], v[216:219], v[96:99]\ns_add_u32 s54, s51, 32768\ns_cmp_eq_u32 s54, 163840\nv_mfma_f32_16x16x32_bf16 v[100:103], v[48:51], v[220:223], v[100:103]\n"
"v_mfma_f32_16x16x32_bf16 v[104:107], v[48:51], a[88:91], v[104:107]\nv_mfma_f32_16x16x32_bf16 v[108:111], v[48:51], a[92:95], v[108:111]\ns_cselect_b32 s54, 0, s54\ns_sub_u32 s54, s54, s51\n"
"ds_read_b128 v[48:51], v34 offset:24576\ns_add_u32 s53, s49, 640\ns_waitcnt lgkmcnt(3)\nv_mfma_f32_16x16x32_bf16 v[112:115], v[52:55], v[216:219], v[112:115]\n"
"buffer_load_dwordx4 v10, s[12:15], s53 offen lds\nv_mfma_f32_16x16x32_bf16 v[116:119], v[52:55], v[220:223], v[116:119]\nv_mfma_f32_16x16x32_bf16 v[120:123], v[52:55], a[88:91], v[120:123]\nv_mfma_f32_16x16x32_bf16 v[124:127], v[52:55], a[92:95], v[124:127]\n"
"ds_read_b128 v[52:55], v35 offset:24576\ns_waitcnt lgkmcnt(3)\nv_mfma_f32_16x16x32_bf16 v[96:99], v[40:43], v[224:227], v[96:99]\nv_add_u32 v32, s54, v32\n"
"v_mfma_f32_16x16x32_bf16 v[100:103], v[40:43], v[228:231], v[100:103]\nv_mfma_f32_16x16x32_bf16 v[104:107], v[40:43], a[96:99], v[104:107]\nv_mfma_f32_16x16x32_bf16 v[108:111], v[40:43], a[100:103], v[108:111]\nds_read_b128 v[40:43], v32 offset:0\n"
"s_waitcnt lgkmcnt(3)\nv_mfma_f32_16x16x32_bf16 v[112:115], v[44:47], v[224:227], v[112:115]\nv_add_u32 v33, s54, v33\nv_mfma_f32_16x16x32_bf16 v[116:119], v[44:47], v[228:231], v[116:119]\n"
"s_add_u32 m0, s52, 24576\nv_mfma_f32_16x16x32_bf16 v[120:123], v[44:47], a[96:99], v[120:123]\ns_add_u32 s53, s49, 768\nv_mfma_f32_16x16x32_bf16 v[124:127], v[44:47], a[100:103], v[124:127]\n"
"ds_read_b128 v[44:47], v33 offset:0\nbuffer_load_dwordx4 v10, s[12:15], s53 offen lds\ns_waitcnt lgkmcnt(3)\nv_mfma_f32_16x16x32_bf16 v[96:99], v[48:51], v[232:235], v[96:99]\n"
"v_add_u32 v34, s54, v34\nv_mfma_f32_16x16x32_bf16 v[100:103], v[48:51], v[236:239], v[100:103]\nv_mfma_f32_16x16x32_bf16 v[104:107], v[48:51], a[104:107], v[104:107]\nv_mfma_f32_16x16x32_bf16 v[108:111], v[48:51], a[108:111], v[108:111]\n"
"ds_read_b128 v[48:51], v34 offset:0\ns_waitcnt lgkmcnt(3)\nv_mfma_f32_16x16x32_bf16 v[112:115], v[52:55], v[232:235], v[112:115]\nv_add_u32 v35, s54, v35\n"
"v_mfma_f32_16x16x32_bf16 v[116:119], v[52:55], v[236:239], v[116:119]\nv_mfma_f32_16x16x32_bf16 v[120:123], v[52:55], a[104:107], v[120:123]\nv_mfma_f32_16x16x32_bf16 v[124:127], v[52:55], a[108:111], v[124:127]\nds_read_b128 v[52:55], v35 offset:0\n"
"s_mov_b32 s52, s47\ns_add_u32 s47, s51, s32\ns_add_u32 s51, s51, s54\ns_add_u32 s49, s49, 28672\n"
"s_cmp_lt_u32 s49, 1921024\ns_cbranch_scc1 fg1_loop\ns_nop 7\nv_cvt_pk_bf16_f32 v56, v96, v97\n"
"v_cvt_pk_bf16_f32 v57, v98, v99\nv_cvt_pk_bf16_f32 v58, v112, v113\nv_cvt_pk_bf16_f32 v59, v114, v115\ns_nop 0\n"
"s_nop 0\nbuffer_store_dwordx4 v[56:59], v15, s[20:23], s50 offen sc1\nv_cvt_pk_bf16_f32 v60, v100, v101\nv_cvt_pk_bf16_f32 v61, v102, v103\n"
"v_cvt_pk_bf16_f32 v62, v116, v117\nv_cvt_pk_bf16_f32 v63, v118, v119\ns_nop 0\ns_nop 0\n"
"buffer_store_dwordx4 v[60:63], v16, s[20:23], s50 offen sc1\nv_cvt_pk_bf16_f32 v56, v104, v105\nv_cvt_pk_bf16_f32 v57, v106, v107\nv_cvt_pk_bf16_f32 v58, v120, v121\n"
"v_cvt_pk_bf16_f32 v59, v122, v123\ns_nop 0\ns_nop 0\nbuffer_store_dwordx4 v[56:59], v17, s[20:23], s50 offen sc1\n"
"v_cvt_pk_bf16_f32 v60, v108, v109\nv_cvt_pk_bf16_f32 v61, v110, v111\nv_cvt_pk_bf16_f32 v62, v124, v125\nv_cvt_pk_bf16_f32 v63, v126, v127\n"
"s_nop 0\ns_nop 0\nbuffer_store_dwordx4 v[60:63], v18, s[20:23], s50 offen sc1\ns_add_u32 s48, s28, 64\n"
"v_mov_b32 v36, s48\ns_waitcnt vmcnt(0) lgkmcnt(0)\ns_barrier\nbuffer_store_dword v36, v19, s[24:27], s33 offen sc1\n"
"fg1_end:\n"
    : "+{v[244:251]}"(w) : "{s[8:23]}"(s0), "{s[24:39]}"(s1), "{v[8:23]}"(v0) : "v24", "v25", "v26", "v27", "v28", "v29", "v30", "v31", "v32", "v33", "v34", "v35", "v36", "v37", "v38", "v39", "v40", "v41", "v42", "v43", "v44", "v45", "v46", "v47", "v48", "v49", "v50", "v51", "v52", "v53", "v54", "v55", "v56", "v57", "v58", "v59", "v60", "v61", "v62", "v63", "v64", "v65", "v66", "v67", "v68", "v69", "v70", "v71", "v72", "v73", "v74", "v75", "v76", "v77", "v78", "v79", "v80", "v81", "v82", "v83", "v84", "v85", "v86", "v87", "v88", "v89", "v90", "v91", "v92", "v93", "v94", "v95", "v96", "v97", "v98", "v99", "v100", "v101", "v102", "v103", "v104", "v105", "v106", "v107", "v108", "v109", "v110", "v111", "v112", "v113", "v114", "v115", "v116", "v117", "v118", "v119", "v120", "v121", "v122", "v123", "v124", "v125", "v126", "v127", "v128", "v129", "v130", "v131", "v132", "v133", "v134", "v135", "v136", "v137", "v138", "v139", "v140", "v141", "v142", "v143", "v144", "v145", "v146", "v147", "v148", "v149", "v150", "v151", "v152", "v153", "v154", "v155", "v156", "v157", "v158", "v159", "v160", "v161", "v162", "v163", "v164", "v165", "v166", "v167", "v168", "v169", "v170", "v171", "v172", "v173", "v174", "v175", "v176", "v177", "v178", "v179", "v180", "v181", "v182", "v183", "v184", "v185", "v186", "v187", "v188", "v189", "v190", "v191", "v192", "v193", "v194", "v195", "v196", "v197", "v198", "v199", "v200", "v201", "v202", "v203", "v204", "v205", "v206", "v207", "v208", "v209", "v210", "v211", "v212", "v213", "v214", "v215", "v216", "v217", "v218", "v219", "v220", "v221", "v222", "v223", "v224", "v225", "v226", "v227", "v228", "v229", "v230", "v231", "v232", "v233", "v234", "v235", "v236", "v237", "v238", "v239", "v240", "v241", "v242", "v243", "a0", "a1", "a2", "a3", "a4", "a5", "a6", "a7", "a8", "a9", "a10", "a11", "a12", "a13", "a14", "a15", "a16", "a17", "a18", "a19", "a20", "a21", "a22", "a23", "a24", "a25", "a26", "a27", "a28", "a29", "a30", "a31", "a32", "a33", "a34", "a35", "a36", "a37", "a38", "a39", "a40", "a41", "a42", "a43", "a44", "a45", "a46", "a47", "a48", "a49", "a50", "a51", "a52", "a53", "a54", "a55", "a56", "a57", "a58", "a59", "a60", "a61", "a62", "a63", "a64", "a65", "a66", "a67", "a68", "a69", "a70", "a71", "a72", "a73", "a74", "a75", "a76", "a77", "a78", "a79", "a80", "a81", "a82", "a83", "a84", "a85", "a86", "a87", "a88", "a89", "a90", "a91", "a92", "a93", "a94", "a95", "a96", "a97", "a98", "a99", "a100", "a101", "a102", "a103", "a104", "a105", "a106", "a107", "a108", "a109", "a110", "a111", "a112", "a113", "a114", "a115", "a116", "a117", "a118", "a119", "a120", "a121", "a122", "a123", "a124", "a125", "a126", "a127", "a128", "a129", "a130", "a131", "a132", "a133", "a134", "a135", "a136", "a137", "a138", "a139", "a140", "a141", "a142", "a143", "a144", "a145", "a146", "a147", "a148", "a149", "a150", "a151", "a152", "a153", "a154", "a155", "a156", "a157", "a158", "a159", "a160", "a161", "a162", "a163", "a164", "a165", "a166", "a167", "a168", "a169", "a170", "a171", "a172", "a173", "a174", "a175", "a176", "a177", "a178", "a179", "a180", "a181", "a182", "a183", "a184", "a185", "a186", "a187", "a188", "a189", "a190", "a191", "a192", "a193", "a194", "a195", "a196", "a197", "a198", "a199", "a200", "a201", "a202", "a203", "a204", "a205", "a206", "a207", "a208", "a209", "a210", "a211", "a212", "a213", "a214", "a215", "a216", "a217", "a218", "a219", "a220", "a221", "a222", "a223", "s40", "s41", "s42", "s43", "s44", "s45", "s46", "s47", "s48", "s49", "s50", "s51", "s52", "s53", "s54", "s55", "s56", "s57", "s58", "s59", "s60", "s61", "vcc", "scc", "memory");
    return o;
}
struct OCtx { u4 r; unsigned sg, ssx, sr, su, sum, nt, hx, p0, p1, p2, p3, p4; unsigned vg, vsx, vu, vum, vx, vt, vpx, vpm, vpr, vo, p5, p6, p7, p8, p9, pa; };
DI void o_asm(const OCtx& c) {
    const u16v s0 = {RFL(c.r[0]), RFL(c.r[1]), RFL(c.r[2]), RFL(c.r[3]), RFL(c.sg), RFL(c.ssx), RFL(c.sr), RFL(c.su), RFL(c.sum), RFL(c.nt), RFL(c.hx), RFL(c.p0), RFL(c.p1), RFL(c.p2), RFL(c.p3), RFL(c.p4)}, v0 = {c.vg, c.vsx, c.vu, c.vum, c.vx, c.vt, c.vpx, c.vpm, c.vpr, c.vo, c.p5, c.p6, c.p7, c.p8, c.p9, c.pa};
    asm volatile(
"s_nop 4\ns_xor_b32 s72, s18, 0\ns_mul_i32 s72, s72, s14\ns_add_u32 s72, s72, s12\n"
"buffer_load_dwordx4 a[0:3], v8, s[8:11], s72 offen offset:0 sc1\nbuffer_load_dwordx4 a[4:7], v8, s[8:11], s72 offen offset:512 sc1\nbuffer_load_dwordx4 a[8:11], v8, s[8:11], s72 offen offset:1024 sc1\nbuffer_load_dwordx4 a[12:15], v8, s[8:11], s72 offen offset:1536 sc1\n"
"buffer_load_dwordx4 a[16:19], v8, s[8:11], s72 offen offset:2048 sc1\nbuffer_load_dwordx4 a[20:23], v8, s[8:11], s72 offen offset:2560 sc1\nbuffer_load_dwordx4 a[24:27], v8, s[8:11], s72 offen offset:3072 sc1\nbuffer_load_dwordx4 a[28:31], v8, s[8:11], s72 offen offset:3584 sc1\n"
"s_add_u32 s72, s72, 4096\nbuffer_load_dwordx4 a[32:35], v8, s[8:11], s72 offen offset:0 sc1\nbuffer_load_dwordx4 a[36:39], v8, s[8:11], s72 offen offset:512 sc1\nbuffer_load_dwordx4 a[40:43], v8, s[8:11], s72 offen offset:1024 sc1\n"
"buffer_load_dwordx4 a[44:47], v8, s[8:11], s72 offen offset:1536 sc1\nbuffer_load_dwordx4 a[48:51], v8, s[8:11], s72 offen offset:2048 sc1\nbuffer_load_dwordx4 a[52:55], v8, s[8:11], s72 offen offset:2560 sc1\nbuffer_load_dwordx4 a[56:59], v8, s[8:11], s72 offen offset:3072 sc1\n"
"buffer_load_dwordx4 a[60:63], v8, s[8:11], s72 offen offset:3584 sc1\ns_add_u32 s72, s72, 4096\nbuffer_load_dwordx4 a[64:67], v8, s[8:11], s72 offen offset:0 sc1\nbuffer_load_dwordx4 a[68:71], v8, s[8:11], s72 offen offset:512 sc1\n"
"buffer_load_dwordx4 a[72:75], v8, s[8:11], s72 offen offset:1024 sc1\nbuffer_load_dwordx4 a[76:79], v8, s[8:11], s72 offen offset:1536 sc1\nbuffer_load_dwordx4 a[80:83], v8, s[8:11], s72 offen offset:2048 sc1\nbuffer_load_dwordx4 a[84:87], v8, s[8:11], s72 offen offset:2560 sc1\n"
"buffer_load_dwordx4 a[88:91], v8, s[8:11], s72 offen offset:3072 sc1\nbuffer_load_dwordx4 a[92:95], v8, s[8:11], s72 offen offset:3584 sc1\ns_add_u32 s72, s72, 4096\nbuffer_load_dwordx4 a[96:99], v8, s[8:11], s72 offen offset:0 sc1\n"
"buffer_load_dwordx4 a[100:103], v8, s[8:11], s72 offen offset:512 sc1\nbuffer_load_dwordx4 a[104:107], v8, s[8:11], s72 offen offset:1024 sc1\nbuffer_load_dwordx4 a[108:111], v8, s[8:11], s72 offen offset:1536 sc1\ns_xor_b32 s73, s18, 0\n"
"s_lshl_b32 s73, s73, 2\ns_mul_i32 s73, s73, s19\ns_add_u32 s73, s73, s13\nbuffer_load_dwordx4 v[64:67], v9, s[8:11], s73 offen sc1\n"
"s_add_u32 s73, s73, s19\nbuffer_load_dwordx4 v[68:71], v9, s[8:11], s73 offen sc1\ns_add_u32 s73, s73, s19\nbuffer_load_dwordx4 v[72:75], v9, s[8:11], s73 offen sc1\n"
"s_add_u32 s73, s73, s19\nbuffer_load_dwordx4 v[76:79], v9, s[8:11], s73 offen sc1\ns_xor_b32 s72, s18, 1\ns_mul_i32 s72, s72, s14\n"
"s_add_u32 s72, s72, s12\nbuffer_load_dwordx4 a[112:115], v8, s[8:11], s72 offen offset:0 sc1\nbuffer_load_dwordx4 a[116:119], v8, s[8:11], s72 offen offset:512 sc1\nbuffer_load_dwordx4 a[120:123], v8, s[8:11], s72 offen offset:1024 sc1\n"
"buffer_load_dwordx4 a[124:127], v8, s[8:11], s72 offen offset:1536 sc1\nbuffer_load_dwordx4 a[128:131], v8, s[8:11], s72 offen offset:2048 sc1\nbuffer_load_dwordx4 a[132:135], v8, s[8:11], s72 offen offset:2560 sc1\nbuffer_load_dwordx4 a[136:139], v8, s[8:11], s72 offen offset:3072 sc1\n"
"buffer_load_dwordx4 a[140:143], v8, s[8:11], s72 offen offset:3584 sc1\ns_add_u32 s72, s72, 4096\nbuffer_load_dwordx4 a[144:147], v8, s[8:11], s72 offen offset:0 sc1\nbuffer_load_dwordx4 a[148:151], v8, s[8:11], s72 offen offset:512 sc1\n"
"buffer_load_dwordx4 a[152:155], v8, s[8:11], s72 offen offset:1024 sc1\nbuffer_load_dwordx4 a[156:159], v8, s[8:11], s72 offen offset:1536 sc1\nbuffer_load_dwordx4 a[160:163], v8, s[8:11], s72 offen offset:2048 sc1\nbuffer_load_dwordx4 a[164:167], v8, s[8:11], s72 offen offset:2560 sc1\n"
"buffer_load_dwordx4 a[168:171], v8, s[8:11], s72 offen offset:3072 sc1\nbuffer_load_dwordx4 a[172:175], v8, s[8:11], s72 offen offset:3584 sc1\ns_add_u32 s72, s72, 4096\nbuffer_load_dwordx4 a[176:179], v8, s[8:11], s72 offen offset:0 sc1\n"
"buffer_load_dwordx4 a[180:183], v8, s[8:11], s72 offen offset:512 sc1\nbuffer_load_dwordx4 a[184:187], v8, s[8:11], s72 offen offset:1024 sc1\nbuffer_load_dwordx4 a[188:191], v8, s[8:11], s72 offen offset:1536 sc1\nbuffer_load_dwordx4 a[192:195], v8, s[8:11], s72 offen offset:2048 sc1\n"
"buffer_load_dwordx4 a[196:199], v8, s[8:11], s72 offen offset:2560 sc1\nbuffer_load_dwordx4 a[200:203], v8, s[8:11], s72 offen offset:3072 sc1\nbuffer_load_dwordx4 a[204:207], v8, s[8:11], s72 offen offset:3584 sc1\ns_add_u32 s72, s72, 4096\n"
"buffer_load_dwordx4 a[208:211], v8, s[8:11], s72 offen offset:0 sc1\nbuffer_load_dwordx4 a[212:215], v8, s[8:11], s72 offen offset:512 sc1\nbuffer_load_dwordx4 a[216:219], v8, s[8:11], s72 offen offset:1024 sc1\nbuffer_load_dwordx4 a[220:223], v8, s[8:11], s72 offen offset:1536 sc1\n"
"s_add_u32 s76, s15, 0\nv_xor_b32 v24, 0, v12\ns_add_u32 s77, s15, 4096\nv_xor_b32 v25, 32, v12\n"
"s_add_u32 s78, s15, 8192\nv_xor_b32 v26, 64, v12\ns_add_u32 s79, s15, 12288\nv_xor_b32 v27, 96, v12\n"
"s_mov_b32 s80, s16\nv_cmp_gt_i32 s[40:41], 0, v13\nv_cmp_gt_i32 s[42:43], 1, v13\nv_cmp_gt_i32 s[44:45], 2, v13\n"
"v_cmp_gt_i32 s[46:47], 3, v13\nv_cmp_gt_i32 s[48:49], 4, v13\nv_cmp_gt_i32 s[50:51], 5, v13\nv_cmp_gt_i32 s[52:53], 6, v13\n"
"v_cmp_gt_i32 s[54:55], 7, v13\nv_cmp_gt_i32 s[56:57], 16, v13\nv_cmp_gt_i32 s[58:59], 17, v13\nv_cmp_gt_i32 s[60:61], 18, v13\n"
"v_cmp_gt_i32 s[62:63], 19, v13\nv_cmp_gt_i32 s[64:65], 20, v13\nv_cmp_gt_i32 s[66:67], 21, v13\nv_cmp_gt_i32 s[68:69], 22, v13\n"
"v_cmp_gt_i32 s[70:71], 23, v13\ns_barrier\nds_read_b128 v[128:131], v24 offset:0\nds_read_b128 v[132:135], v25 offset:0\n"
"ds_read_b128 v[136:139], v26 offset:0\nds_read_b128 v[140:143], v27 offset:0\nds_read_b128 v[144:147], v24 offset:4096\nds_read_b128 v[148:151], v25 offset:4096\n"
"ds_read_b128 v[152:155], v26 offset:4096\nds_read_b128 v[156:159], v27 offset:4096\nds_read_b128 v[160:163], v24 offset:8192\nds_read_b128 v[164:167], v25 offset:8192\n"
"ds_read_b128 v[168:171], v26 offset:8192\nds_read_b128 v[172:175], v27 offset:8192\ns_waitcnt lgkmcnt(0)\nds_read_b128 v[176:179], v24 offset:12288\n"
"ds_read_b128 v[180:183], v25 offset:12288\nds_read_b128 v[184:187], v26 offset:12288\nds_read_b128 v[188:191], v27 offset:12288\nds_read_b128 v[192:195], v24 offset:16384\n"
"ds_read_b128 v[196:199], v25 offset:16384\nds_read_b128 v[200:203], v26 offset:16384\nds_read_b128 v[204:207], v27 offset:16384\nds_read_b128 v[208:211], v24 offset:20480\n"
"ds_read_b128 v[212:215], v25 offset:20480\nds_read_b128 v[216:219], v26 offset:20480\nds_read_b128 v[220:223], v27 offset:20480\ns_waitcnt lgkmcnt(0)\n"
"ds_read_b128 v[224:227], v24 offset:24576\nds_read_b128 v[228:231], v25 offset:24576\nds_read_b128 v[232:235], v26 offset:24576\nds_read_b128 v[236:239], v27 offset:24576\n"
"v_mov_b32 v31, 0xff800000\ns_waitcnt lgkmcnt(0)\ns_waitcnt vmcnt(59)\nv_mfma_f32_32x32x16_bf16 v[40:55], a[0:3], v[128:131], 0\n"
"s_xor_b32 s73, s18, 1\ns_lshl_b32 s73, s73, 2\ns_mul_i32 s73, s73, s19\ns_add_u32 s73, s73, s13\n"
"buffer_load_dwordx4 v[80:83], v9, s[8:11], s73 offen sc1\ns_add_u32 s73, s73, s19\nbuffer_load_dwordx4 v[84:87], v9, s[8:11], s73 offen sc1\ns_add_u32 s73, s73, s19\n"
"buffer_load_dwordx4 v[88:91], v9, s[8:11], s73 offen sc1\ns_add_u32 s73, s73, s19\nbuffer_load_dwordx4 v[92:95], v9, s[8:11], s73 offen sc1\ns_xor_b32 s72, s18, 2\n"
"s_mul_i32 s72, s72, s14\ns_add_u32 s72, s72, s12\nbuffer_load_dwordx4 a[0:3], v8, s[8:11], s72 offen offset:0 sc1\ns_waitcnt vmcnt(63)\n"
"v_mfma_f32_32x32x16_bf16 v[40:55], a[4:7], v[132:135], v[40:55]\nbuffer_load_dwordx4 a[4:7], v8, s[8:11], s72 offen offset:512 sc1\ns_waitcnt vmcnt(63)\nv_mfma_f32_32x32x16_bf16 v[40:55], a[8:11], v[136:139], v[40:55]\n"
"buffer_load_dwordx4 a[8:11], v8, s[8:11], s72 offen offset:1024 sc1\ns_waitcnt vmcnt(63)\nv_mfma_f32_32x32x16_bf16 v[40:55], a[12:15], v[140:143], v[40:55]\nbuffer_load_dwordx4 a[12:15], v8, s[8:11], s72 offen offset:1536 sc1\n"
"s_waitcnt vmcnt(63)\nv_mfma_f32_32x32x16_bf16 v[40:55], a[16:19], v[144:147], v[40:55]\nbuffer_load_dwordx4 a[16:19], v8, s[8:11], s72 offen offset:2048 sc1\ns_waitcnt vmcnt(63)\n"
"v_mfma_f32_32x32x16_bf16 v[40:55], a[20:23], v[148:151], v[40:55]\nbuffer_load_dwordx4 a[20:23], v8, s[8:11], s72 offen offset:2560 sc1\ns_waitcnt vmcnt(63)\nv_mfma_f32_32x32x16_bf16 v[40:55], a[24:27], v[152:155], v[40:55]\n"
"buffer_load_dwordx4 a[24:27], v8, s[8:11], s72 offen offset:3072 sc1\ns_waitcnt vmcnt(63)\nv_mfma_f32_32x32x16_bf16 v[40:55], a[28:31], v[156:159], v[40:55]\nbuffer_load_dwordx4 a[28:31], v8, s[8:11], s72 offen offset:3584 sc1\n"
"s_waitcnt vmcnt(63)\nv_mfma_f32_32x32x16_bf16 v[40:55], a[32:35], v[160:163], v[40:55]\ns_add_u32 s72, s72, 4096\nbuffer_load_dwordx4 a[32:35], v8, s[8:11], s72 offen offset:0 sc1\n"
"s_waitcnt vmcnt(63)\nv_mfma_f32_32x32x16_bf16 v[40:55], a[36:39], v[164:167], v[40:55]\nbuffer_load_dwordx4 a[36:39], v8, s[8:11], s72 offen offset:512 sc1\ns_waitcnt vmcnt(63)\n"
"v_mfma_f32_32x32x16_bf16 v[40:55], a[40:43], v[168:171], v[40:55]\nbuffer_load_dwordx4 a[40:43], v8, s[8:11], s72 offen offset:1024 sc1\ns_waitcnt vmcnt(63)\nv_mfma_f32_32x32x16_bf16 v[40:55], a[44:47], v[172:175], v[40:55]\n"
"buffer_load_dwordx4 a[44:47], v8, s[8:11], s72 offen offset:1536 sc1\ns_waitcnt vmcnt(63)\nv_mfma_f32_32x32x16_bf16 v[40:55], a[48:51], v[176:179], v[40:55]\nbuffer_load_dwordx4 a[48:51], v8, s[8:11], s72 offen offset:2048 sc1\n"
"s_waitcnt vmcnt(63)\nv_mfma_f32_32x32x16_bf16 v[40:55], a[52:55], v[180:183], v[40:55]\nbuffer_load_dwordx4 a[52:55], v8, s[8:11], s72 offen offset:2560 sc1\ns_waitcnt vmcnt(63)\n"
"v_mfma_f32_32x32x16_bf16 v[40:55], a[56:59], v[184:187], v[40:55]\nbuffer_load_dwordx4 a[56:59], v8, s[8:11], s72 offen offset:3072 sc1\ns_waitcnt vmcnt(63)\nv_mfma_f32_32x32x16_bf16 v[40:55], a[60:63], v[188:191], v[40:55]\n"
"buffer_load_dwordx4 a[60:63], v8, s[8:11], s72 offen offset:3584 sc1\ns_waitcnt vmcnt(63)\nv_mfma_f32_32x32x16_bf16 v[40:55], a[64:67], v[192:195], v[40:55]\ns_add_u32 s72, s72, 4096\n"
"buffer_load_dwordx4 a[64:67], v8, s[8:11], s72 offen offset:0 sc1\ns_waitcnt vmcnt(63)\nv_mfma_f32_32x32x16_bf16 v[40:55], a[68:71], v[196:199], v[40:55]\nbuffer_load_dwordx4 a[68:71], v8, s[8:11], s72 offen offset:512 sc1\n"
"s_waitcnt vmcnt(63)\nv_mfma_f32_32x32x16_bf16 v[40:55], a[72:75], v[200:203], v[40:55]\nbuffer_load_dwordx4 a[72:75], v8, s[8:11], s72 offen offset:1024 sc1\ns_waitcnt vmcnt(63)\n"
"v_mfma_f32_32x32x16_bf16 v[40:55], a[76:79], v[204:207], v[40:55]\nbuffer_load_dwordx4 a[76:79], v8, s[8:11], s72 offen offset:1536 sc1\ns_waitcnt vmcnt(63)\nv_mfma_f32_32x32x16_bf16 v[40:55], a[80:83], v[208:211], v[40:55]\n"
"buffer_load_dwordx4 a[80:83], v8, s[8:11], s72 offen offset:2048 sc1\ns_waitcnt vmcnt(63)\nv_mfma_f32_32x32x16_bf16 v[40:55], a[84:87], v[212:215], v[40:55]\nbuffer_load_dwordx4 a[84:87], v8, s[8:11], s72 offen offset:2560 sc1\n"
"s_waitcnt vmcnt(63)\nv_mfma_f32_32x32x16_bf16 v[40:55], a[88:91], v[216:219], v[40:55]\nbuffer_load_dwordx4 a[88:91], v8, s[8:11], s72 offen offset:3072 sc1\ns_waitcnt vmcnt(63)\n"
"v_mfma_f32_32x32x16_bf16 v[40:55], a[92:95], v[220:223], v[40:55]\nbuffer_load_dwordx4 a[92:95], v8, s[8:11], s72 offen offset:3584 sc1\ns_waitcnt vmcnt(63)\nv_mfma_f32_32x32x16_bf16 v[40:55], a[96:99], v[224:227], v[40:55]\n"
"s_add_u32 s72, s72, 4096\nbuffer_load_dwordx4 a[96:99], v8, s[8:11], s72 offen offset:0 sc1\ns_waitcnt vmcnt(63)\nv_mfma_f32_32x32x16_bf16 v[40:55], a[100:103], v[228:231], v[40:55]\n"
"buffer_load_dwordx4 a[100:103], v8, s[8:11], s72 offen offset:512 sc1\ns_waitcnt vmcnt(63)\nv_mfma_f32_32x32x16_bf16 v[40:55], a[104:107], v[232:235], v[40:55]\nbuffer_load_dwordx4 a[104:107], v8, s[8:11], s72 offen offset:1024 sc1\n"
"s_waitcnt vmcnt(63)\nv_mfma_f32_32x32x16_bf16 v[40:55], a[108:111], v[236:239], v[40:55]\nbuffer_load_dwordx4 a[108:111], v8, s[8:11], s72 offen offset:1536 sc1\ns_waitcnt vmcnt(60)\n"
"v_add_f32 v64, v64, v68\nv_add_f32 v72, v72, v76\nv_add_f32 v65, v65, v69\nv_add_f32 v73, v73, v77\n"
"v_add_f32 v66, v66, v70\nv_add_f32 v74, v74, v78\nv_add_f32 v67, v67, v71\nv_add_f32 v75, v75, v79\n"
"v_add_f32 v64, v64, v72\nv_add_f32 v65, v65, v73\nv_add_f32 v66, v66, v74\nv_add_f32 v67, v67, v75\n"
"v_max3_f32 v28, v64, v65, v66\nv_max_f32 v28, v28, v67\nv_cndmask_b32 v40, v40, v31, s[40:41]\nv_cndmask_b32 v41, v41, v31, s[42:43]\n"
"v_cndmask_b32 v42, v42, v31, s[44:45]\nv_cndmask_b32 v43, v43, v31, s[46:47]\nv_cndmask_b32 v44, v44, v31, s[48:49]\nv_cndmask_b32 v45, v45, v31, s[50:51]\n"
"v_cndmask_b32 v46, v46, v31, s[52:53]\nv_cndmask_b32 v47, v47, v31, s[54:55]\nv_cndmask_b32 v48, v48, v31, s[56:57]\nv_cndmask_b32 v49, v49, v31, s[58:59]\n"
"v_cndmask_b32 v50, v50, v31, s[60:61]\nv_cndmask_b32 v51, v51, v31, s[62:63]\nv_cndmask_b32 v52, v52, v31, s[64:65]\nv_cndmask_b32 v53, v53, v31, s[66:67]\n"
"v_cndmask_b32 v54, v54, v31, s[68:69]\nv_cndmask_b32 v55, v55, v31, s[70:71]\nv_max3_f32 v28, v28, v40, v41\nv_max3_f32 v28, v28, v42, v43\n"
"v_max3_f32 v28, v28, v44, v45\nv_max3_f32 v28, v28, v46, v47\nv_max3_f32 v28, v28, v48, v49\nv_max3_f32 v28, v28, v50, v51\n"
"v_max3_f32 v28, v28, v52, v53\nv_max3_f32 v28, v28, v54, v55\nv_mov_b32 v30, v28\ns_nop 1\n"
"v_permlane32_swap_b32 v28, v30\nv_max_f32 v28, v28, v30\nv_sub_f32 v40, v40, v28\nv_sub_f32 v41, v41, v28\n"
"v_sub_f32 v42, v42, v28\nv_sub_f32 v43, v43, v28\nv_sub_f32 v44, v44, v28\nv_sub_f32 v45, v45, v28\n"
"v_sub_f32 v46, v46, v28\nv_sub_f32 v47, v47, v28\nv_sub_f32 v48, v48, v28\nv_sub_f32 v49, v49, v28\n"
"v_sub_f32 v50, v50, v28\nv_sub_f32 v51, v51, v28\nv_sub_f32 v52, v52, v28\nv_sub_f32 v53, v53, v28\n"
"v_sub_f32 v54, v54, v28\nv_sub_f32 v55, v55, v28\nv_sub_f32 v64, v64, v28\nv_sub_f32 v65, v65, v28\n"
"v_sub_f32 v66, v66, v28\nv_sub_f32 v67, v67, v28\nv_exp_f32 v40, v40\nv_exp_f32 v41, v41\n"
"v_exp_f32 v42, v42\nv_exp_f32 v43, v43\nv_exp_f32 v44, v44\nv_exp_f32 v45, v45\n"
"v_exp_f32 v46, v46\nv_exp_f32 v47, v47\nv_exp_f32 v48, v48\nv_exp_f32 v49, v49\n"
"v_exp_f32 v50, v50\nv_exp_f32 v51, v51\nv_exp_f32 v52, v52\nv_exp_f32 v53, v53\n"
"v_exp_f32 v54, v54\nv_exp_f32 v55, v55\nv_exp_f32 v64, v64\nv_exp_f32 v65, v65\n"
"v_exp_f32 v66, v66\nv_exp_f32 v67, v67\nv_add_f32 v29, v40, v41\nv_add_f32 v30, v42, v43\n"
"v_add_f32 v29, v29, v44\nv_add_f32 v30, v30, v45\nv_add_f32 v29, v29, v46\nv_add_f32 v30, v30, v47\n"
"v_add_f32 v29, v29, v48\nv_add_f32 v30, v30, v49\nv_add_f32 v29, v29, v50\nv_add_f32 v30, v30, v51\n"
"v_add_f32 v29, v29, v52\nv_add_f32 v30, v30, v53\nv_add_f32 v29, v29, v54\nv_add_f32 v30, v30, v55\n"
"v_add_f32 v29, v29, v64\nv_add_f32 v30, v30, v65\nv_add_f32 v29, v29, v66\nv_add_f32 v30, v30, v67\n"
"v_add_f32 v29, v29, v30\nv_mov_b32 v30, v29\ns_nop 1\nv_permlane32_swap_b32 v29, v30\n"
"v_add_f32 v29, v29, v30\nv_rcp_f32 v29, v29\ns_nop 0\nv_mul_f32 v40, v40, v29\n"
"v_mul_f32 v41, v41, v29\nv_mul_f32 v42, v42, v29\nv_mul_f32 v43, v43, v29\nv_mul_f32 v44, v44, v29\n"
"v_mul_f32 v45, v45, v29\nv_mul_f32 v46, v46, v29\nv_mul_f32 v47, v47, v29\nv_mul_f32 v48, v48, v29\n"
"v_mul_f32 v49, v49, v29\nv_mul_f32 v50, v50, v29\nv_mul_f32 v51, v51, v29\nv_mul_f32 v52, v52, v29\n"
"v_mul_f32 v53, v53, v29\nv_mul_f32 v54, v54, v29\nv_mul_f32 v55, v55, v29\nv_mul_f32 v64, v64, v29\n"
"v_mul_f32 v65, v65, v29\nv_mul_f32 v66, v66, v29\nv_mul_f32 v67, v67, v29\nv_cvt_pk_bf16_f32 v56, v40, v41\n"
"v_cvt_pk_bf16_f32 v57, v42, v43\nv_cvt_pk_bf16_f32 v58, v44, v45\nv_cvt_pk_bf16_f32 v59, v46, v47\nv_cvt_pk_bf16_f32 v60, v48, v49\n"
"v_cvt_pk_bf16_f32 v61, v50, v51\nv_cvt_pk_bf16_f32 v62, v52, v53\nv_cvt_pk_bf16_f32 v63, v54, v55\nv_cvt_pk_bf16_f32 v32, v64, v65\n"
"v_cvt_pk_bf16_f32 v36, v66, v67\nv_xor_b32 v30, 0, v14\nds_write_b128 v30, v[56:59]\nds_write_b128 v30, v[60:63] offset:1024\n"
"s_waitcnt vmcnt(59)\nv_mfma_f32_32x32x16_bf16 v[40:55], a[112:115], v[128:131], 0\ns_xor_b32 s73, s18, 2\ns_lshl_b32 s73, s73, 2\n"
"s_mul_i32 s73, s73, s19\ns_add_u32 s73, s73, s13\nbuffer_load_dwordx4 v[96:99], v9, s[8:11], s73 offen sc1\ns_add_u32 s73, s73, s19\n"
"buffer_load_dwordx4 v[100:103], v9, s[8:11], s73 offen sc1\ns_add_u32 s73, s73, s19\nbuffer_load_dwordx4 v[104:107], v9, s[8:11], s73 offen sc1\ns_add_u32 s73, s73, s19\n"
"buffer_load_dwordx4 v[108:111], v9, s[8:11], s73 offen sc1\ns_xor_b32 s72, s18, 3\ns_mul_i32 s72, s72, s14\ns_add_u32 s72, s72, s12\n"
"buffer_load_dwordx4 a[112:115], v8, s[8:11], s72 offen offset:0 sc1\ns_waitcnt vmcnt(63)\nv_mfma_f32_32x32x16_bf16 v[40:55], a[116:119], v[132:135], v[40:55]\nbuffer_load_dwordx4 a[116:119], v8, s[8:11], s72 offen offset:512 sc1\n"
"s_waitcnt vmcnt(63)\nv_mfma_f32_32x32x16_bf16 v[40:55], a[120:123], v[136:139], v[40:55]\nbuffer_load_dwordx4 a[120:123], v8, s[8:11], s72 offen offset:1024 sc1\ns_waitcnt vmcnt(63)\n"
"v_mfma_f32_32x32x16_bf16 v[40:55], a[124:127], v[140:143], v[40:55]\nbuffer_load_dwordx4 a[124:127], v8, s[8:11], s72 offen offset:1536 sc1\ns_waitcnt vmcnt(63)\nv_mfma_f32_32x32x16_bf16 v[40:55], a[128:131], v[144:147], v[40:55]\n"
"buffer_load_dwordx4 a[128:131], v8, s[8:11], s72 offen offset:2048 sc1\ns_waitcnt vmcnt(63)\nv_mfma_f32_32x32x16_bf16 v[40:55], a[132:135], v[148:151], v[40:55]\nbuffer_load_dwordx4 a[132:135], v8, s[8:11], s72 offen offset:2560 sc1\n"
"s_waitcnt vmcnt(63)\nv_mfma_f32_32x32x16_bf16 v[40:55], a[136:139], v[152:155], v[40:55]\nbuffer_load_dwordx4 a[136:139], v8, s[8:11], s72 offen offset:3072 sc1\ns_waitcnt vmcnt(63)\n"
"v_mfma_f32_32x32x16_bf16 v[40:55], a[140:143], v[156:159], v[40:55]\nbuffer_load_dwordx4 a[140:143], v8, s[8:11], s72 offen offset:3584 sc1\ns_waitcnt vmcnt(63)\nv_mfma_f32_32x32x16_bf16 v[40:55], a[144:147], v[160:163], v[40:55]\n"
"s_add_u32 s72, s72, 4096\nbuffer_load_dwordx4 a[144:147], v8, s[8:11], s72 offen offset:0 sc1\ns_waitcnt vmcnt(63)\nv_mfma_f32_32x32x16_bf16 v[40:55], a[148:151], v[164:167], v[40:55]\n"
"buffer_load_dwordx4 a[148:151], v8, s[8:11], s72 offen offset:512 sc1\ns_waitcnt vmcnt(63)\nv_mfma_f32_32x32x16_bf16 v[40:55], a[152:155], v[168:171], v[40:55]\nbuffer_load_dwordx4 a[152:155], v8, s[8:11], s72 offen offset:1024 sc1\n"
"s_waitcnt vmcnt(63)\nv_mfma_f32_32x32x16_bf16 v[40:55], a[156:159], v[172:175], v[40:55]\nbuffer_load_dwordx4 a[156:159], v8, s[8:11], s72 offen offset:1536 sc1\ns_waitcnt vmcnt(63)\n"
"v_mfma_f32_32x32x16_bf16 v[40:55], a[160:163], v[176:179], v[40:55]\nbuffer_load_dwordx4 a[160:163], v8, s[8:11], s72 offen offset:2048 sc1\ns_waitcnt vmcnt(63)\nv_mfma_f32_32x32x16_bf16 v[40:55], a[164:167], v[180:183], v[40:55]\n"
"buffer_load_dwordx4 a[164:167], v8, s[8:11], s72 offen offset:2560 sc1\ns_waitcnt vmcnt(63)\nv_mfma_f32_32x32x16_bf16 v[40:55], a[168:171], v[184:187], v[40:55]\nbuffer_load_dwordx4 a[168:171], v8, s[8:11], s72 offen offset:3072 sc1\n"
"s_waitcnt vmcnt(63)\nv_mfma_f32_32x32x16_bf16 v[40:55], a[172:175], v[188:191], v[40:55]\nbuffer_load_dwordx4 a[172:175], v8, s[8:11], s72 offen offset:3584 sc1\ns_waitcnt vmcnt(63)\n"
"v_mfma_f32_32x32x16_bf16 v[40:55], a[176:179], v[192:195], v[40:55]\ns_add_u32 s72, s72, 4096\nbuffer_load_dwordx4 a[176:179], v8, s[8:11], s72 offen offset:0 sc1\ns_waitcnt vmcnt(63)\n"
"v_mfma_f32_32x32x16_bf16 v[40:55], a[180:183], v[196:199], v[40:55]\nbuffer_load_dwordx4 a[180:183], v8, s[8:11], s72 offen offset:512 sc1\ns_waitcnt vmcnt(63)\nv_mfma_f32_32x32x16_bf16 v[40:55], a[184:187], v[200:203], v[40:55]\n"
"buffer_load_dwordx4 a[184:187], v8, s[8:11], s72 offen offset:1024 sc1\ns_waitcnt vmcnt(63)\nv_mfma_f32_32x32x16_bf16 v[40:55], a[188:191], v[204:207], v[40:55]\nbuffer_load_dwordx4 a[188:191], v8, s[8:11], s72 offen offset:1536 sc1\n"
"s_waitcnt vmcnt(63)\nv_mfma_f32_32x32x16_bf16 v[40:55], a[192:195], v[208:211], v[40:55]\nbuffer_load_dwordx4 a[192:195], v8, s[8:11], s72 offen offset:2048 sc1\ns_waitcnt vmcnt(63)\n"
"v_mfma_f32_32x32x16_bf16 v[40:55], a[196:199], v[212:215], v[40:55]\nbuffer_load_dwordx4 a[196:199], v8, s[8:11], s72 offen offset:2560 sc1\ns_waitcnt vmcnt(63)\nv_mfma_f32_32x32x16_bf16 v[40:55], a[200:203], v[216:219], v[40:55]\n"
"buffer_load_dwordx4 a[200:203], v8, s[8:11], s72 offen offset:3072 sc1\ns_waitcnt vmcnt(63)\nv_mfma_f32_32x32x16_bf16 v[40:55], a[204:207], v[220:223], v[40:55]\nbuffer_load_dwordx4 a[204:207], v8, s[8:11], s72 offen offset:3584 sc1\n"
"s_waitcnt vmcnt(63)\nv_mfma_f32_32x32x16_bf16 v[40:55], a[208:211], v[224:227], v[40:55]\ns_add_u32 s72, s72, 4096\nbuffer_load_dwordx4 a[208:211], v8, s[8:11], s72 offen offset:0 sc1\n"
"s_waitcnt vmcnt(63)\nv_mfma_f32_32x32x16_bf16 v[40:55], a[212:215], v[228:231], v[40:55]\nbuffer_load_dwordx4 a[212:215], v8, s[8:11], s72 offen offset:512 sc1\ns_waitcnt vmcnt(63)\n"
"v_mfma_f32_32x32x16_bf16 v[40:55], a[216:219], v[232:235], v[40:55]\nbuffer_load_dwordx4 a[216:219], v8, s[8:11], s72 offen offset:1024 sc1\ns_waitcnt vmcnt(63)\nv_mfma_f32_32x32x16_bf16 v[40:55], a[220:223], v[236:239], v[40:55]\n"
"buffer_load_dwordx4 a[220:223], v8, s[8:11], s72 offen offset:1536 sc1\ns_waitcnt vmcnt(60)\nv_add_f32 v80, v80, v84\nv_add_f32 v88, v88, v92\n"
"v_add_f32 v81, v81, v85\nv_add_f32 v89, v89, v93\nv_add_f32 v82, v82, v86\nv_add_f32 v90, v90, v94\n"
"v_add_f32 v83, v83, v87\nv_add_f32 v91, v91, v95\nv_add_f32 v80, v80, v88\nv_add_f32 v81, v81, v89\n"
"v_add_f32 v82, v82, v90\nv_add_f32 v83, v83, v91\nv_max3_f32 v28, v80, v81, v82\nv_max_f32 v28, v28, v83\n"
"v_cndmask_b32 v40, v40, v31, s[40:41]\nv_cndmask_b32 v41, v41, v31, s[42:43]\nv_cndmask_b32 v42, v42, v31, s[44:45]\nv_cndmask_b32 v43, v43, v31, s[46:47]\n"
"v_cndmask_b32 v44, v44, v31, s[48:49]\nv_cndmask_b32 v45, v45, v31, s[50:51]\nv_cndmask_b32 v46, v46, v31, s[52:53]\nv_cndmask_b32 v47, v47, v31, s[54:55]\n"
"v_cndmask_b32 v48, v48, v31, s[56:57]\nv_cndmask_b32 v49, v49, v31, s[58:59]\nv_cndmask_b32 v50, v50, v31, s[60:61]\nv_cndmask_b32 v51, v51, v31, s[62:63]\n"
"v_cndmask_b32 v52, v52, v31, s[64:65]\nv_cndmask_b32 v53, v53, v31, s[66:67]\nv_cndmask_b32 v54, v54, v31, s[68:69]\nv_cndmask_b32 v55, v55, v31, s[70:71]\n"
"v_max3_f32 v28, v28, v40, v41\nv_max3_f32 v28, v28, v42, v43\nv_max3_f32 v28, v28, v44, v45\nv_max3_f32 v28, v28, v46, v47\n"
"v_max3_f32 v28, v28, v48, v49\nv_max3_f32 v28, v28, v50, v51\nv_max3_f32 v28, v28, v52, v53\nv_max3_f32 v28, v28, v54, v55\n"
"v_mov_b32 v30, v28\ns_nop 1\nv_permlane32_swap_b32 v28, v30\nv_max_f32 v28, v28, v30\n"
"v_sub_f32 v40, v40, v28\nv_sub_f32 v41, v41, v28\nv_sub_f32 v42, v42, v28\nv_sub_f32 v43, v43, v28\n"
"v_sub_f32 v44, v44, v28\nv_sub_f32 v45, v45, v28\nv_sub_f32 v46, v46, v28\nv_sub_f32 v47, v47, v28\n"
"v_sub_f32 v48, v48, v28\nv_sub_f32 v49, v49, v28\nv_sub_f32 v50, v50, v28\nv_sub_f32 v51, v51, v28\n"
"v_sub_f32 v52, v52, v28\nv_sub_f32 v53, v53, v28\nv_sub_f32 v54, v54, v28\nv_sub_f32 v55, v55, v28\n"
"v_sub_f32 v80, v80, v28\nv_sub_f32 v81, v81, v28\nv_sub_f32 v82, v82, v28\nv_sub_f32 v83, v83, v28\n"
"v_exp_f32 v40, v40\nv_exp_f32 v41, v41\nv_exp_f32 v42, v42\nv_exp_f32 v43, v43\n"
"v_exp_f32 v44, v44\nv_exp_f32 v45, v45\nv_exp_f32 v46, v46\nv_exp_f32 v47, v47\n"
"v_exp_f32 v48, v48\nv_exp_f32 v49, v49\nv_exp_f32 v50, v50\nv_exp_f32 v51, v51\n"
"v_exp_f32 v52, v52\nv_exp_f32 v53, v53\nv_exp_f32 v54, v54\nv_exp_f32 v55, v55\n"
"v_exp_f32 v80, v80\nv_exp_f32 v81, v81\nv_exp_f32 v82, v82\nv_exp_f32 v83, v83\n"
"v_add_f32 v29, v40, v41\nv_add_f32 v30, v42, v43\nv_add_f32 v29, v29, v44\nv_add_f32 v30, v30, v45\n"
"v_add_f32 v29, v29, v46\nv_add_f32 v30, v30, v47\nv_add_f32 v29, v29, v48\nv_add_f32 v30, v30, v49\n"
"v_add_f32 v29, v29, v50\nv_add_f32 v30, v30, v51\nv_add_f32 v29, v29, v52\nv_add_f32 v30, v30, v53\n"
"v_add_f32 v29, v29, v54\nv_add_f32 v30, v30, v55\nv_add_f32 v29, v29, v80\nv_add_f32 v30, v30, v81\n"
"v_add_f32 v29, v29, v82\nv_add_f32 v30, v30, v83\nv_add_f32 v29, v29, v30\nv_mov_b32 v30, v29\n"
"s_nop 1\nv_permlane32_swap_b32 v29, v30\nv_add_f32 v29, v29, v30\nv_rcp_f32 v29, v29\n"
"s_nop 0\nv_mul_f32 v40, v40, v29\nv_mul_f32 v41, v41, v29\nv_mul_f32 v42, v42, v29\n"
"v_mul_f32 v43, v43, v29\nv_mul_f32 v44, v44, v29\nv_mul_f32 v45, v45, v29\nv_mul_f32 v46, v46, v29\n"
"v_mul_f32 v47, v47, v29\nv_mul_f32 v48, v48, v29\nv_mul_f32 v49, v49, v29\nv_mul_f32 v50, v50, v29\n"
"v_mul_f32 v51, v51, v29\nv_mul_f32 v52, v52, v29\nv_mul_f32 v53, v53, v29\nv_mul_f32 v54, v54, v29\n"
"v_mul_f32 v55, v55, v29\nv_mul_f32 v80, v80, v29\nv_mul_f32 v81, v81, v29\nv_mul_f32 v82, v82, v29\n"
"v_mul_f32 v83, v83, v29\nv_cvt_pk_bf16_f32 v56, v40, v41\nv_cvt_pk_bf16_f32 v57, v42, v43\nv_cvt_pk_bf16_f32 v58, v44, v45\n"
"v_cvt_pk_bf16_f32 v59, v46, v47\nv_cvt_pk_bf16_f32 v60, v48, v49\nv_cvt_pk_bf16_f32 v61, v50, v51\nv_cvt_pk_bf16_f32 v62, v52, v53\n"
"v_cvt_pk_bf16_f32 v63, v54, v55\nv_cvt_pk_bf16_f32 v33, v80, v81\nv_cvt_pk_bf16_f32 v37, v82, v83\nv_xor_b32 v30, 2048, v14\n"
"ds_write_b128 v30, v[56:59]\nds_write_b128 v30, v[60:63] offset:1024\ns_waitcnt vmcnt(59)\nv_mfma_f32_32x32x16_bf16 v[40:55], a[0:3], v[128:131], 0\n"
"s_xor_b32 s73, s18, 3\ns_lshl_b32 s73, s73, 2\ns_mul_i32 s73, s73, s19\ns_add_u32 s73, s73, s13\n"
"buffer_load_dwordx4 v[112:115], v9, s[8:11], s73 offen sc1\ns_add_u32 s73, s73, s19\nbuffer_load_dwordx4 v[116:119], v9, s[8:11], s73 offen sc1\ns_add_u32 s73, s73, s19\n"
"buffer_load_dwordx4 v[120:123], v9, s[8:11], s73 offen sc1\ns_add_u32 s73, s73, s19\nbuffer_load_dwordx4 v[124:127], v9, s[8:11], s73 offen sc1\nbuffer_load_dwordx4 a[0:3], v10, s[8:11], s76 offen offset:0 sc1\n"
"s_waitcnt vmcnt(63)\nv_mfma_f32_32x32x16_bf16 v[40:55], a[4:7], v[132:135], v[40:55]\nbuffer_load_dwordx4 a[4:7], v10, s[8:11], s76 offen offset:512 sc1\ns_waitcnt vmcnt(63)\n"
"v_mfma_f32_32x32x16_bf16 v[40:55], a[8:11], v[136:139], v[40:55]\nbuffer_load_dwordx4 a[8:11], v10, s[8:11], s76 offen offset:1024 sc1\ns_waitcnt vmcnt(63)\nv_mfma_f32_32x32x16_bf16 v[40:55], a[12:15], v[140:143], v[40:55]\n"
"buffer_load_dwordx4 a[12:15], v10, s[8:11], s76 offen offset:1536 sc1\ns_waitcnt vmcnt(63)\nv_mfma_f32_32x32x16_bf16 v[40:55], a[16:19], v[144:147], v[40:55]\nbuffer_load_dwordx4 a[16:19], v10, s[8:11], s76 offen offset:2048 sc1\n"
"s_waitcnt vmcnt(63)\nv_mfma_f32_32x32x16_bf16 v[40:55], a[20:23], v[148:151], v[40:55]\nbuffer_load_dwordx4 a[20:23], v10, s[8:11], s76 offen offset:2560 sc1\ns_waitcnt vmcnt(63)\n"
"v_mfma_f32_32x32x16_bf16 v[40:55], a[24:27], v[152:155], v[40:55]\nbuffer_load_dwordx4 a[24:27], v10, s[8:11], s76 offen offset:3072 sc1\ns_waitcnt vmcnt(63)\nv_mfma_f32_32x32x16_bf16 v[40:55], a[28:31], v[156:159], v[40:55]\n"
"buffer_load_dwordx4 a[28:31], v10, s[8:11], s76 offen offset:3584 sc1\ns_waitcnt vmcnt(63)\nv_mfma_f32_32x32x16_bf16 v[40:55], a[32:35], v[160:163], v[40:55]\nbuffer_load_dwordx4 a[32:35], v10, s[8:11], s77 offen offset:0 sc1\n"
"s_waitcnt vmcnt(63)\nv_mfma_f32_32x32x16_bf16 v[40:55], a[36:39], v[164:167], v[40:55]\nbuffer_load_dwordx4 a[36:39], v10, s[8:11], s77 offen offset:512 sc1\ns_waitcnt vmcnt(63)\n"
"v_mfma_f32_32x32x16_bf16 v[40:55], a[40:43], v[168:171], v[40:55]\nbuffer_load_dwordx4 a[40:43], v10, s[8:11], s77 offen offset:1024 sc1\ns_waitcnt vmcnt(63)\nv_mfma_f32_32x32x16_bf16 v[40:55], a[44:47], v[172:175], v[40:55]\n"
"buffer_load_dwordx4 a[44:47], v10, s[8:11], s77 offen offset:1536 sc1\ns_waitcnt vmcnt(63)\nv_mfma_f32_32x32x16_bf16 v[40:55], a[48:51], v[176:179], v[40:55]\nbuffer_load_dwordx4 a[48:51], v10, s[8:11], s77 offen offset:2048 sc1\n"
"s_waitcnt vmcnt(63)\nv_mfma_f32_32x32x16_bf16 v[40:55], a[52:55], v[180:183], v[40:55]\nbuffer_load_dwordx4 a[52:55], v10, s[8:11], s77 offen offset:2560 sc1\ns_waitcnt vmcnt(63)\n"
"v_mfma_f32_32x32x16_bf16 v[40:55], a[56:59], v[184:187], v[40:55]\nbuffer_load_dwordx4 a[56:59], v10, s[8:11], s77 offen offset:3072 sc1\ns_waitcnt vmcnt(63)\nv_mfma_f32_32x32x16_bf16 v[40:55], a[60:63], v[188:191], v[40:55]\n"
"buffer_load_dwordx4 a[60:63], v10, s[8:11], s77 offen offset:3584 sc1\ns_waitcnt vmcnt(63)\nv_mfma_f32_32x32x16_bf16 v[40:55], a[64:67], v[192:195], v[40:55]\nbuffer_load_dwordx4 a[64:67], v10, s[8:11], s78 offen offset:0 sc1\n"
"s_waitcnt vmcnt(63)\nv_mfma_f32_32x32x16_bf16 v[40:55], a[68:71], v[196:199], v[40:55]\nbuffer_load_dwordx4 a[68:71], v10, s[8:11], s78 offen offset:512 sc1\ns_waitcnt vmcnt(63)\n"
"v_mfma_f32_32x32x16_bf16 v[40:55], a[72:75], v[200:203], v[40:55]\nbuffer_load_dwordx4 a[72:75], v10, s[8:11], s78 offen offset:1024 sc1\ns_waitcnt vmcnt(63)\nv_mfma_f32_32x32x16_bf16 v[40:55], a[76:79], v[204:207], v[40:55]\n"
"buffer_load_dwordx4 a[76:79], v10, s[8:11], s78 offen offset:1536 sc1\ns_waitcnt vmcnt(63)\nv_mfma_f32_32x32x16_bf16 v[40:55], a[80:83], v[208:211], v[40:55]\nbuffer_load_dwordx4 a[80:83], v10, s[8:11], s78 offen offset:2048 sc1\n"
"s_waitcnt vmcnt(63)\nv_mfma_f32_32x32x16_bf16 v[40:55], a[84:87], v[212:215], v[40:55]\nbuffer_load_dwordx4 a[84:87], v10, s[8:11], s78 offen offset:2560 sc1\ns_waitcnt vmcnt(63)\n"
"v_mfma_f32_32x32x16_bf16 v[40:55], a[88:91], v[216:219], v[40:55]\nbuffer_load_dwordx4 a[88:91], v10, s[8:11], s78 offen offset:3072 sc1\ns_waitcnt vmcnt(63)\nv_mfma_f32_32x32x16_bf16 v[40:55], a[92:95], v[220:223], v[40:55]\n"
"buffer_load_dwordx4 a[92:95], v10, s[8:11], s78 offen offset:3584 sc1\ns_waitcnt vmcnt(63)\nv_mfma_f32_32x32x16_bf16 v[40:55], a[96:99], v[224:227], v[40:55]\nbuffer_load_dwordx4 a[96:99], v10, s[8:11], s79 offen offset:0 sc1\n"
"s_waitcnt vmcnt(63)\nv_mfma_f32_32x32x16_bf16 v[40:55], a[100:103], v[228:231], v[40:55]\nbuffer_load_dwordx4 a[100:103], v10, s[8:11], s79 offen offset:512 sc1\ns_waitcnt vmcnt(63)\n"
"v_mfma_f32_32x32x16_bf16 v[40:55], a[104:107], v[232:235], v[40:55]\nbuffer_load_dwordx4 a[104:107], v10, s[8:11], s79 offen offset:1024 sc1\ns_waitcnt vmcnt(63)\nv_mfma_f32_32x32x16_bf16 v[40:55], a[108:111], v[236:239], v[40:55]\n"
"buffer_load_dwordx4 a[108:111], v10, s[8:11], s79 offen offset:1536 sc1\ns_waitcnt vmcnt(60)\nv_add_f32 v96, v96, v100\nv_add_f32 v104, v104, v108\n"
"v_add_f32 v97, v97, v101\nv_add_f32 v105, v105, v109\nv_add_f32 v98, v98, v102\nv_add_f32 v106, v106, v110\n"
"v_add_f32 v99, v99, v103\nv_add_f32 v107, v107, v111\nv_add_f32 v96, v96, v104\nv_add_f32 v97, v97, v105\n"
"v_add_f32 v98, v98, v106\nv_add_f32 v99, v99, v107\nv_max3_f32 v28, v96, v97, v98\nv_max_f32 v28, v28, v99\n"
"v_cndmask_b32 v40, v40, v31, s[40:41]\nv_cndmask_b32 v41, v41, v31, s[42:43]\nv_cndmask_b32 v42, v42, v31, s[44:45]\nv_cndmask_b32 v43, v43, v31, s[46:47]\n"
"v_cndmask_b32 v44, v44, v31, s[48:49]\nv_cndmask_b32 v45, v45, v31, s[50:51]\nv_cndmask_b32 v46, v46, v31, s[52:53]\nv_cndmask_b32 v47, v47, v31, s[54:55]\n"
"v_cndmask_b32 v48, v48, v31, s[56:57]\nv_cndmask_b32 v49, v49, v31, s[58:59]\nv_cndmask_b32 v50, v50, v31, s[60:61]\nv_cndmask_b32 v51, v51, v31, s[62:63]\n"
"v_cndmask_b32 v52, v52, v31, s[64:65]\nv_cndmask_b32 v53, v53, v31, s[66:67]\nv_cndmask_b32 v54, v54, v31, s[68:69]\nv_cndmask_b32 v55, v55, v31, s[70:71]\n"
"v_max3_f32 v28, v28, v40, v41\nv_max3_f32 v28, v28, v42, v43\nv_max3_f32 v28, v28, v44, v45\nv_max3_f32 v28, v28, v46, v47\n"
"v_max3_f32 v28, v28, v48, v49\nv_max3_f32 v28, v28, v50, v51\nv_max3_f32 v28, v28, v52, v53\nv_max3_f32 v28, v28, v54, v55\n"
"v_mov_b32 v30, v28\ns_nop 1\nv_permlane32_swap_b32 v28, v30\nv_max_f32 v28, v28, v30\n"
"v_sub_f32 v40, v40, v28\nv_sub_f32 v41, v41, v28\nv_sub_f32 v42, v42, v28\nv_sub_f32 v43, v43, v28\n"
"v_sub_f32 v44, v44, v28\nv_sub_f32 v45, v45, v28\nv_sub_f32 v46, v46, v28\nv_sub_f32 v47, v47, v28\n"
"v_sub_f32 v48, v48, v28\nv_sub_f32 v49, v49, v28\nv_sub_f32 v50, v50, v28\nv_sub_f32 v51, v51, v28\n"
"v_sub_f32 v52, v52, v28\nv_sub_f32 v53, v53, v28\nv_sub_f32 v54, v54, v28\nv_sub_f32 v55, v55, v28\n"
"v_sub_f32 v96, v96, v28\nv_sub_f32 v97, v97, v28\nv_sub_f32 v98, v98, v28\nv_sub_f32 v99, v99, v28\n"
"v_exp_f32 v40, v40\nv_exp_f32 v41, v41\nv_exp_f32 v42, v42\nv_exp_f32 v43, v43\n"
"v_exp_f32 v44, v44\nv_exp_f32 v45, v45\nv_exp_f32 v46, v46\nv_exp_f32 v47, v47\n"
"v_exp_f32 v48, v48\nv_exp_f32 v49, v49\nv_exp_f32 v50, v50\nv_exp_f32 v51, v51\n"
"v_exp_f32 v52, v52\nv_exp_f32 v53, v53\nv_exp_f32 v54, v54\nv_exp_f32 v55, v55\n"
"v_exp_f32 v96, v96\nv_exp_f32 v97, v97\nv_exp_f32 v98, v98\nv_exp_f32 v99, v99\n"
"v_add_f32 v29, v40, v41\nv_add_f32 v30, v42, v43\nv_add_f32 v29, v29, v44\nv_add_f32 v30, v30, v45\n"
"v_add_f32 v29, v29, v46\nv_add_f32 v30, v30, v47\nv_add_f32 v29, v29, v48\nv_add_f32 v30, v30, v49\n"
"v_add_f32 v29, v29, v50\nv_add_f32 v30, v30, v51\nv_add_f32 v29, v29, v52\nv_add_f32 v30, v30, v53\n"
"v_add_f32 v29, v29, v54\nv_add_f32 v30, v30, v55\nv_add_f32 v29, v29, v96\nv_add_f32 v30, v30, v97\n"
"v_add_f32 v29, v29, v98\nv_add_f32 v30, v30, v99\nv_add_f32 v29, v29, v30\nv_mov_b32 v30, v29\n"
"s_nop 1\nv_permlane32_swap_b32 v29, v30\nv_add_f32 v29, v29, v30\nv_rcp_f32 v29, v29\n"
"s_nop 0\nv_mul_f32 v40, v40, v29\nv_mul_f32 v41, v41, v29\nv_mul_f32 v42, v42, v29\n"
"v_mul_f32 v43, v43, v29\nv_mul_f32 v44, v44, v29\nv_mul_f32 v45, v45, v29\nv_mul_f32 v46, v46, v29\n"
"v_mul_f32 v47, v47, v29\nv_mul_f32 v48, v48, v29\nv_mul_f32 v49, v49, v29\nv_mul_f32 v50, v50, v29\n"
"v_mul_f32 v51, v51, v29\nv_mul_f32 v52, v52, v29\nv_mul_f32 v53, v53, v29\nv_mul_f32 v54, v54, v29\n"
"v_mul_f32 v55, v55, v29\nv_mul_f32 v96, v96, v29\nv_mul_f32 v97, v97, v29\nv_mul_f32 v98, v98, v29\n"
"v_mul_f32 v99, v99, v29\nv_cvt_pk_bf16_f32 v56, v40, v41\nv_cvt_pk_bf16_f32 v57, v42, v43\nv_cvt_pk_bf16_f32 v58, v44, v45\n"
"v_cvt_pk_bf16_f32 v59, v46, v47\nv_cvt_pk_bf16_f32 v60, v48, v49\nv_cvt_pk_bf16_f32 v61, v50, v51\nv_cvt_pk_bf16_f32 v62, v52, v53\n"
"v_cvt_pk_bf16_f32 v63, v54, v55\nv_cvt_pk_bf16_f32 v34, v96, v97\nv_cvt_pk_bf16_f32 v38, v98, v99\nv_xor_b32 v30, 4096, v14\n"
"ds_write_b128 v30, v[56:59]\nds_write_b128 v30, v[60:63] offset:1024\ns_waitcnt vmcnt(59)\nv_mfma_f32_32x32x16_bf16 v[40:55], a[112:115], v[128:131], 0\n"
"buffer_load_dwordx4 a[112:115], v10, s[8:11], s79 offen offset:2048 sc1\ns_waitcnt vmcnt(59)\nv_mfma_f32_32x32x16_bf16 v[40:55], a[116:119], v[132:135], v[40:55]\nbuffer_load_dwordx4 a[116:119], v10, s[8:11], s79 offen offset:2560 sc1\n"
"s_waitcnt vmcnt(59)\nv_mfma_f32_32x32x16_bf16 v[40:55], a[120:123], v[136:139], v[40:55]\nbuffer_load_dwordx4 a[120:123], v10, s[8:11], s79 offen offset:3072 sc1\ns_waitcnt vmcnt(59)\n"
"v_mfma_f32_32x32x16_bf16 v[40:55], a[124:127], v[140:143], v[40:55]\nbuffer_load_dwordx4 a[124:127], v10, s[8:11], s79 offen offset:3584 sc1\ns_waitcnt vmcnt(59)\nv_mfma_f32_32x32x16_bf16 v[40:55], a[128:131], v[144:147], v[40:55]\n"
"buffer_load_dwordx4 a[128:131], v11, s[8:11], s80 offen offset:0 sc1\ns_waitcnt vmcnt(59)\nv_mfma_f32_32x32x16_bf16 v[40:55], a[132:135], v[148:151], v[40:55]\nbuffer_load_dwordx4 a[132:135], v11, s[8:11], s80 offen offset:256 sc1\n"
"s_waitcnt vmcnt(59)\nv_mfma_f32_32x32x16_bf16 v[40:55], a[136:139], v[152:155], v[40:55]\nbuffer_load_dwordx4 a[136:139], v11, s[8:11], s80 offen offset:1024 sc1\ns_waitcnt vmcnt(59)\n"
"v_mfma_f32_32x32x16_bf16 v[40:55], a[140:143], v[156:159], v[40:55]\nbuffer_load_dwordx4 a[140:143], v11, s[8:11], s80 offen offset:1280 sc1\ns_waitcnt vmcnt(59)\nv_mfma_f32_32x32x16_bf16 v[40:55], a[144:147], v[160:163], v[40:55]\n"
"buffer_load_dwordx4 a[144:147], v11, s[8:11], s80 offen offset:2048 sc1\ns_waitcnt vmcnt(59)\nv_mfma_f32_32x32x16_bf16 v[40:55], a[148:151], v[164:167], v[40:55]\nbuffer_load_dwordx4 a[148:151], v11, s[8:11], s80 offen offset:2304 sc1\n"
"s_waitcnt vmcnt(59)\nv_mfma_f32_32x32x16_bf16 v[40:55], a[152:155], v[168:171], v[40:55]\nbuffer_load_dwordx4 a[152:155], v11, s[8:11], s80 offen offset:3072 sc1\ns_waitcnt vmcnt(59)\n"
"v_mfma_f32_32x32x16_bf16 v[40:55], a[156:159], v[172:175], v[40:55]\nbuffer_load_dwordx4 a[156:159], v11, s[8:11], s80 offen offset:3328 sc1\ns_waitcnt vmcnt(59)\nv_mfma_f32_32x32x16_bf16 v[40:55], a[160:163], v[176:179], v[40:55]\n"
"s_waitcnt vmcnt(58)\nv_mfma_f32_32x32x16_bf16 v[40:55], a[164:167], v[180:183], v[40:55]\ns_waitcnt vmcnt(57)\nv_mfma_f32_32x32x16_bf16 v[40:55], a[168:171], v[184:187], v[40:55]\n"
"s_waitcnt vmcnt(56)\nv_mfma_f32_32x32x16_bf16 v[40:55], a[172:175], v[188:191], v[40:55]\ns_waitcnt vmcnt(55)\nv_mfma_f32_32x32x16_bf16 v[40:55], a[176:179], v[192:195], v[40:55]\n"
"s_waitcnt vmcnt(54)\nv_mfma_f32_32x32x16_bf16 v[40:55], a[180:183], v[196:199], v[40:55]\ns_waitcnt vmcnt(53)\nv_mfma_f32_32x32x16_bf16 v[40:55], a[184:187], v[200:203], v[40:55]\n"
"s_waitcnt vmcnt(52)\nv_mfma_f32_32x32x16_bf16 v[40:55], a[188:191], v[204:207], v[40:55]\ns_waitcnt vmcnt(51)\nv_mfma_f32_32x32x16_bf16 v[40:55], a[192:195], v[208:211], v[40:55]\n"
"s_waitcnt vmcnt(50)\nv_mfma_f32_32x32x16_bf16 v[40:55], a[196:199], v[212:215], v[40:55]\ns_waitcnt vmcnt(49)\nv_mfma_f32_32x32x16_bf16 v[40:55], a[200:203], v[216:219], v[40:55]\n"
"s_waitcnt vmcnt(48)\nv_mfma_f32_32x32x16_bf16 v[40:55], a[204:207], v[220:223], v[40:55]\ns_waitcnt vmcnt(47)\nv_mfma_f32_32x32x16_bf16 v[40:55], a[208:211], v[224:227], v[40:55]\n"
"s_waitcnt vmcnt(46)\nv_mfma_f32_32x32x16_bf16 v[40:55], a[212:215], v[228:231], v[40:55]\ns_waitcnt vmcnt(45)\nv_mfma_f32_32x32x16_bf16 v[40:55], a[216:219], v[232:235], v[40:55]\n"
"s_waitcnt vmcnt(44)\nv_mfma_f32_32x32x16_bf16 v[40:55], a[220:223], v[236:239], v[40:55]\ns_waitcnt vmcnt(40)\nv_add_f32 v112, v112, v116\n"
"v_add_f32 v120, v120, v124\nv_add_f32 v113, v113, v117\nv_add_f32 v121, v121, v125\nv_add_f32 v114, v114, v118\n"
"v_add_f32 v122, v122, v126\nv_add_f32 v115, v115, v119\nv_add_f32 v123, v123, v127\nv_add_f32 v112, v112, v120\n"
"v_add_f32 v113, v113, v121\nv_add_f32 v114, v114, v122\nv_add_f32 v115, v115, v123\nv_max3_f32 v28, v112, v113, v114\n"
"v_max_f32 v28, v28, v115\nv_cndmask_b32 v40, v40, v31, s[40:41]\nv_cndmask_b32 v41, v41, v31, s[42:43]\nv_cndmask_b32 v42, v42, v31, s[44:45]\n"
"v_cndmask_b32 v43, v43, v31, s[46:47]\nv_cndmask_b32 v44, v44, v31, s[48:49]\nv_cndmask_b32 v45, v45, v31, s[50:51]\nv_cndmask_b32 v46, v46, v31, s[52:53]\n"
"v_cndmask_b32 v47, v47, v31, s[54:55]\nv_cndmask_b32 v48, v48, v31, s[56:57]\nv_cndmask_b32 v49, v49, v31, s[58:59]\nv_cndmask_b32 v50, v50, v31, s[60:61]\n"
"v_cndmask_b32 v51, v51, v31, s[62:63]\nv_cndmask_b32 v52, v52, v31, s[64:65]\nv_cndmask_b32 v53, v53, v31, s[66:67]\nv_cndmask_b32 v54, v54, v31, s[68:69]\n"
"v_cndmask_b32 v55, v55, v31, s[70:71]\nv_max3_f32 v28, v28, v40, v41\nv_max3_f32 v28, v28, v42, v43\nv_max3_f32 v28, v28, v44, v45\n"
"v_max3_f32 v28, v28, v46, v47\nv_max3_f32 v28, v28, v48, v49\nv_max3_f32 v28, v28, v50, v51\nv_max3_f32 v28, v28, v52, v53\n"
"v_max3_f32 v28, v28, v54, v55\nv_mov_b32 v30, v28\ns_nop 1\nv_permlane32_swap_b32 v28, v30\n"
"v_max_f32 v28, v28, v30\nv_sub_f32 v40, v40, v28\nv_sub_f32 v41, v41, v28\nv_sub_f32 v42, v42, v28\n"
"v_sub_f32 v43, v43, v28\nv_sub_f32 v44, v44, v28\nv_sub_f32 v45, v45, v28\nv_sub_f32 v46, v46, v28\n"
"v_sub_f32 v47, v47, v28\nv_sub_f32 v48, v48, v28\nv_sub_f32 v49, v49, v28\nv_sub_f32 v50, v50, v28\n"
"v_sub_f32 v51, v51, v28\nv_sub_f32 v52, v52, v28\nv_sub_f32 v53, v53, v28\nv_sub_f32 v54, v54, v28\n"
"v_sub_f32 v55, v55, v28\nv_sub_f32 v112, v112, v28\nv_sub_f32 v113, v113, v28\nv_sub_f32 v114, v114, v28\n"
"v_sub_f32 v115, v115, v28\nv_exp_f32 v40, v40\nv_exp_f32 v41, v41\nv_exp_f32 v42, v42\n"
"v_exp_f32 v43, v43\nv_exp_f32 v44, v44\nv_exp_f32 v45, v45\nv_exp_f32 v46, v46\n"
"v_exp_f32 v47, v47\nv_exp_f32 v48, v48\nv_exp_f32 v49, v49\nv_exp_f32 v50, v50\n"
"v_exp_f32 v51, v51\nv_exp_f32 v52, v52\nv_exp_f32 v53, v53\nv_exp_f32 v54, v54\n"
"v_exp_f32 v55, v55\nv_exp_f32 v112, v112\nv_exp_f32 v113, v113\nv_exp_f32 v114, v114\n"
"v_exp_f32 v115, v115\nv_add_f32 v29, v40, v41\nv_add_f32 v30, v42, v43\nv_add_f32 v29, v29, v44\n"
"v_add_f32 v30, v30, v45\nv_add_f32 v29, v29, v46\nv_add_f32 v30, v30, v47\nv_add_f32 v29, v29, v48\n"
"v_add_f32 v30, v30, v49\nv_add_f32 v29, v29, v50\nv_add_f32 v30, v30, v51\nv_add_f32 v29, v29, v52\n"
"v_add_f32 v30, v30, v53\nv_add_f32 v29, v29, v54\nv_add_f32 v30, v30, v55\nv_add_f32 v29, v29, v112\n"
"v_add_f32 v30, v30, v113\nv_add_f32 v29, v29, v114\nv_add_f32 v30, v30, v115\nv_add_f32 v29, v29, v30\n"
"v_mov_b32 v30, v29\ns_nop 1\nv_permlane32_swap_b32 v29, v30\nv_add_f32 v29, v29, v30\n"
"v_rcp_f32 v29, v29\ns_nop 0\nv_mul_f32 v40, v40, v29\nv_mul_f32 v41, v41, v29\n"
"v_mul_f32 v42, v42, v29\nv_mul_f32 v43, v43, v29\nv_mul_f32 v44, v44, v29\nv_mul_f32 v45, v45, v29\n"
"v_mul_f32 v46, v46, v29\nv_mul_f32 v47, v47, v29\nv_mul_f32 v48, v48, v29\nv_mul_f32 v49, v49, v29\n"
"v_mul_f32 v50, v50, v29\nv_mul_f32 v51, v51, v29\nv_mul_f32 v52, v52, v29\nv_mul_f32 v53, v53, v29\n"
"v_mul_f32 v54, v54, v29\nv_mul_f32 v55, v55, v29\nv_mul_f32 v112, v112, v29\nv_mul_f32 v113, v113, v29\n"
"v_mul_f32 v114, v114, v29\nv_mul_f32 v115, v115, v29\nv_cvt_pk_bf16_f32 v56, v40, v41\nv_cvt_pk_bf16_f32 v57, v42, v43\n"
"v_cvt_pk_bf16_f32 v58, v44, v45\nv_cvt_pk_bf16_f32 v59, v46, v47\nv_cvt_pk_bf16_f32 v60, v48, v49\nv_cvt_pk_bf16_f32 v61, v50, v51\n"
"v_cvt_pk_bf16_f32 v62, v52, v53\nv_cvt_pk_bf16_f32 v63, v54, v55\nv_cvt_pk_bf16_f32 v35, v112, v113\nv_cvt_pk_bf16_f32 v39, v114, v115\n"
"v_xor_b32 v30, 6144, v14\nds_write_b128 v30, v[56:59]\nds_write_b128 v30, v[60:63] offset:1024\nds_write_b128 v15, v[32:35]\n"
"ds_write_b128 v15, v[36:39] offset:1024\ns_waitcnt lgkmcnt(0)\ns_barrier\nds_read_b128 v[96:99], v16 offset:0\n"
"ds_read_b128 v[100:103], v16 offset:1024\nds_read_b128 v[104:107], v16 offset:2048\nds_read_b128 v[108:111], v16 offset:3072\nds_read_b128 v[112:115], v16 offset:4096\n"
"ds_read_b128 v[116:119], v16 offset:5120\nds_read_b128 v[120:123], v16 offset:6144\nds_read_b128 v[124:127], v16 offset:7168\nds_read_b128 v[128:131], v16 offset:8192\n"
"ds_read_b128 v[132:135], v16 offset:9216\nds_read_b128 v[136:139], v16 offset:10240\nds_read_b128 v[140:143], v16 offset:11264\ns_waitcnt lgkmcnt(0)\n"
"ds_read_b128 v[144:147], v16 offset:12288\nds_read_b128 v[148:151], v16 offset:13312\nds_read_b128 v[152:155], v16 offset:14336\nds_read_b128 v[156:159], v16 offset:15360\n"
"ds_read_b128 v[160:163], v16 offset:16384\nds_read_b128 v[164:167], v16 offset:17408\nds_read_b128 v[168:171], v16 offset:18432\nds_read_b128 v[172:175], v16 offset:19456\n"
"ds_read_b128 v[176:179], v16 offset:20480\nds_read_b128 v[180:183], v16 offset:21504\nds_read_b128 v[184:187], v16 offset:22528\nds_read_b128 v[188:191], v16 offset:23552\n"
"s_waitcnt lgkmcnt(0)\nds_read_b128 v[192:195], v16 offset:24576\nds_read_b128 v[196:199], v16 offset:25600\nds_read_b128 v[200:203], v16 offset:26624\n"
"ds_read_b128 v[204:207], v16 offset:27648\nds_read_b128 v[208:211], v16 offset:28672\nds_read_b128 v[212:215], v16 offset:29696\nds_read_b128 v[216:219], v16 offset:30720\n"
"ds_read_b128 v[220:223], v16 offset:31744\nds_read_b128 v[224:227], v16 offset:32768\nds_read_b128 v[228:231], v16 offset:33792\nds_read_b128 v[232:235], v16 offset:34816\n"
"ds_read_b128 v[236:239], v16 offset:35840\ns_waitcnt lgkmcnt(0)\nds_read_b128 v[240:243], v16 offset:36864\nds_read_b128 v[244:247], v16 offset:37888\n"
"ds_read_b128 v[248:251], v16 offset:38912\nds_read_b128 v[252:255], v16 offset:39936\nv_mov_b32 v24, v17\ns_sub_u32 s72, s17, 1\n"
"s_waitcnt lgkmcnt(0)\nfo_loop:\ns_add_u32 s76, s76, s20\ns_add_u32 s77, s77, s20\n"
"s_add_u32 s78, s78, s20\ns_add_u32 s79, s79, s20\ns_add_u32 s80, s80, s21\ns_waitcnt vmcnt(39)\n"
"v_mfma_f32_32x32x16_bf16 v[40:55], a[0:3], v[96:99], 0\nbuffer_load_dwordx4 a[0:3], v10, s[8:11], s76 offen offset:0 sc1\ns_waitcnt vmcnt(39)\nv_mfma_f32_32x32x16_bf16 v[40:55], a[4:7], v[100:103], v[40:55]\n"
"buffer_load_dwordx4 a[4:7], v10, s[8:11], s76 offen offset:512 sc1\ns_waitcnt vmcnt(39)\nv_mfma_f32_32x32x16_bf16 v[40:55], a[8:11], v[104:107], v[40:55]\nbuffer_load_dwordx4 a[8:11], v10, s[8:11], s76 offen offset:1024 sc1\n"
"s_waitcnt vmcnt(39)\nv_mfma_f32_32x32x16_bf16 v[40:55], a[12:15], v[108:111], v[40:55]\nbuffer_load_dwordx4 a[12:15], v10, s[8:11], s76 offen offset:1536 sc1\ns_waitcnt vmcnt(39)\n"
"v_mfma_f32_32x32x16_bf16 v[40:55], a[16:19], v[112:115], v[40:55]\nbuffer_load_dwordx4 a[16:19], v10, s[8:11], s76 offen offset:2048 sc1\ns_waitcnt vmcnt(39)\nv_mfma_f32_32x32x16_bf16 v[40:55], a[20:23], v[116:119], v[40:55]\n"
"buffer_load_dwordx4 a[20:23], v10, s[8:11], s76 offen offset:2560 sc1\ns_waitcnt vmcnt(39)\nv_mfma_f32_32x32x16_bf16 v[40:55], a[24:27], v[120:123], v[40:55]\nbuffer_load_dwordx4 a[24:27], v10, s[8:11], s76 offen offset:3072 sc1\n"
"s_waitcnt vmcnt(39)\nv_mfma_f32_32x32x16_bf16 v[40:55], a[28:31], v[124:127], v[40:55]\nbuffer_load_dwordx4 a[28:31], v10, s[8:11], s76 offen offset:3584 sc1\ns_waitcnt vmcnt(39)\n"
"v_mfma_f32_32x32x16_bf16 v[40:55], a[32:35], v[128:131], v[40:55]\nbuffer_load_dwordx4 a[32:35], v10, s[8:11], s77 offen offset:0 sc1\ns_waitcnt vmcnt(39)\nv_mfma_f32_32x32x16_bf16 v[40:55], a[36:39], v[132:135], v[40:55]\n"
"buffer_load_dwordx4 a[36:39], v10, s[8:11], s77 offen offset:512 sc1\ns_waitcnt vmcnt(39)\nv_mfma_f32_32x32x16_bf16 v[40:55], a[40:43], v[136:139], v[40:55]\nbuffer_load_dwordx4 a[40:43], v10, s[8:11], s77 offen offset:1024 sc1\n"
"s_waitcnt vmcnt(39)\nv_mfma_f32_32x32x16_bf16 v[40:55], a[44:47], v[140:143], v[40:55]\nbuffer_load_dwordx4 a[44:47], v10, s[8:11], s77 offen offset:1536 sc1\ns_waitcnt vmcnt(39)\n"
"v_mfma_f32_32x32x16_bf16 v[40:55], a[48:51], v[144:147], v[40:55]\nbuffer_load_dwordx4 a[48:51], v10, s[8:11], s77 offen offset:2048 sc1\ns_waitcnt vmcnt(39)\nv_mfma_f32_32x32x16_bf16 v[40:55], a[52:55], v[148:151], v[40:55]\n"
"buffer_load_dwordx4 a[52:55], v10, s[8:11], s77 offen offset:2560 sc1\ns_waitcnt vmcnt(39)\nv_mfma_f32_32x32x16_bf16 v[40:55], a[56:59], v[152:155], v[40:55]\nbuffer_load_dwordx4 a[56:59], v10, s[8:11], s77 offen offset:3072 sc1\n"
"s_waitcnt vmcnt(39)\nv_mfma_f32_32x32x16_bf16 v[40:55], a[60:63], v[156:159], v[40:55]\nbuffer_load_dwordx4 a[60:63], v10, s[8:11], s77 offen offset:3584 sc1\ns_waitcnt vmcnt(39)\n"
"v_mfma_f32_32x32x16_bf16 v[40:55], a[64:67], v[160:163], v[40:55]\nbuffer_load_dwordx4 a[64:67], v10, s[8:11], s78 offen offset:0 sc1\ns_waitcnt vmcnt(39)\nv_mfma_f32_32x32x16_bf16 v[40:55], a[68:71], v[164:167], v[40:55]\n"
"buffer_load_dwordx4 a[68:71], v10, s[8:11], s78 offen offset:512 sc1\ns_waitcnt vmcnt(39)\nv_mfma_f32_32x32x16_bf16 v[40:55], a[72:75], v[168:171], v[40:55]\nbuffer_load_dwordx4 a[72:75], v10, s[8:11], s78 offen offset:1024 sc1\n"
"s_waitcnt vmcnt(39)\nv_mfma_f32_32x32x16_bf16 v[40:55], a[76:79], v[172:175], v[40:55]\nbuffer_load_dwordx4 a[76:79], v10, s[8:11], s78 offen offset:1536 sc1\ns_waitcnt vmcnt(39)\n"
"v_mfma_f32_32x32x16_bf16 v[40:55], a[80:83], v[176:179], v[40:55]\nbuffer_load_dwordx4 a[80:83], v10, s[8:11], s78 offen offset:2048 sc1\ns_waitcnt vmcnt(39)\nv_mfma_f32_32x32x16_bf16 v[40:55], a[84:87], v[180:183], v[40:55]\n"
"buffer_load_dwordx4 a[84:87], v10, s[8:11], s78 offen offset:2560 sc1\ns_waitcnt vmcnt(39)\nv_mfma_f32_32x32x16_bf16 v[40:55], a[88:91], v[184:187], v[40:55]\nbuffer_load_dwordx4 a[88:91], v10, s[8:11], s78 offen offset:3072 sc1\n"
"s_waitcnt vmcnt(39)\nv_mfma_f32_32x32x16_bf16 v[40:55], a[92:95], v[188:191], v[40:55]\nbuffer_load_dwordx4 a[92:95], v10, s[8:11], s78 offen offset:3584 sc1\ns_waitcnt vmcnt(39)\n"
"v_mfma_f32_32x32x16_bf16 v[40:55], a[96:99], v[192:195], v[40:55]\nbuffer_load_dwordx4 a[96:99], v10, s[8:11], s79 offen offset:0 sc1\ns_waitcnt vmcnt(39)\nv_mfma_f32_32x32x16_bf16 v[40:55], a[100:103], v[196:199], v[40:55]\n"
"buffer_load_dwordx4 a[100:103], v10, s[8:11], s79 offen offset:512 sc1\ns_waitcnt vmcnt(39)\nv_mfma_f32_32x32x16_bf16 v[40:55], a[104:107], v[200:203], v[40:55]\nbuffer_load_dwordx4 a[104:107], v10, s[8:11], s79 offen offset:1024 sc1\n"
"s_waitcnt vmcnt(39)\nv_mfma_f32_32x32x16_bf16 v[40:55], a[108:111], v[204:207], v[40:55]\nbuffer_load_dwordx4 a[108:111], v10, s[8:11], s79 offen offset:1536 sc1\ns_waitcnt vmcnt(39)\n"
"v_mfma_f32_32x32x16_bf16 v[40:55], a[112:115], v[208:211], v[40:55]\nbuffer_load_dwordx4 a[112:115], v10, s[8:11], s79 offen offset:2048 sc1\ns_waitcnt vmcnt(39)\nv_mfma_f32_32x32x16_bf16 v[40:55], a[116:119], v[212:215], v[40:55]\n"
"buffer_load_dwordx4 a[116:119], v10, s[8:11], s79 offen offset:2560 sc1\ns_waitcnt vmcnt(39)\nv_mfma_f32_32x32x16_bf16 v[40:55], a[120:123], v[216:219], v[40:55]\nbuffer_load_dwordx4 a[120:123], v10, s[8:11], s79 offen offset:3072 sc1\n"
"s_waitcnt vmcnt(39)\nv_mfma_f32_32x32x16_bf16 v[40:55], a[124:127], v[220:223], v[40:55]\nbuffer_load_dwordx4 a[124:127], v10, s[8:11], s79 offen offset:3584 sc1\ns_waitcnt vmcnt(39)\n"
"v_mfma_f32_32x32x16_bf16 v[40:55], a[128:131], v[224:227], v[40:55]\nbuffer_load_dwordx4 a[128:131], v11, s[8:11], s80 offen offset:0 sc1\ns_waitcnt vmcnt(39)\nv_mfma_f32_32x32x16_bf16 v[40:55], a[132:135], v[228:231], v[40:55]\n"
"buffer_load_dwordx4 a[132:135], v11, s[8:11], s80 offen offset:256 sc1\ns_waitcnt vmcnt(39)\nv_mfma_f32_32x32x16_bf16 v[40:55], a[136:139], v[232:235], v[40:55]\nbuffer_load_dwordx4 a[136:139], v11, s[8:11], s80 offen offset:1024 sc1\n"
"s_waitcnt vmcnt(39)\nv_mfma_f32_32x32x16_bf16 v[40:55], a[140:143], v[236:239], v[40:55]\nbuffer_load_dwordx4 a[140:143], v11, s[8:11], s80 offen offset:1280 sc1\ns_waitcnt vmcnt(39)\n"
"v_mfma_f32_32x32x16_bf16 v[40:55], a[144:147], v[240:243], v[40:55]\nbuffer_load_dwordx4 a[144:147], v11, s[8:11], s80 offen offset:2048 sc1\ns_waitcnt vmcnt(39)\nv_mfma_f32_32x32x16_bf16 v[40:55], a[148:151], v[244:247], v[40:55]\n"
"buffer_load_dwordx4 a[148:151], v11, s[8:11], s80 offen offset:2304 sc1\ns_waitcnt vmcnt(39)\nv_mfma_f32_32x32x16_bf16 v[40:55], a[152:155], v[248:251], v[40:55]\nbuffer_load_dwordx4 a[152:155], v11, s[8:11], s80 offen offset:3072 sc1\n"
"s_waitcnt vmcnt(39)\nv_mfma_f32_32x32x16_bf16 v[40:55], a[156:159], v[252:255], v[40:55]\nbuffer_load_dwordx4 a[156:159], v11, s[8:11], s80 offen offset:3328 sc1\ns_nop 11\n"
"v_cvt_pk_bf16_f32 v56, v40, v41\nv_cvt_pk_bf16_f32 v57, v42, v43\nv_cvt_pk_bf16_f32 v58, v44, v45\nv_cvt_pk_bf16_f32 v59, v46, v47\n"
"v_cvt_pk_bf16_f32 v60, v48, v49\nv_cvt_pk_bf16_f32 v61, v50, v51\nv_cvt_pk_bf16_f32 v62, v52, v53\nv_cvt_pk_bf16_f32 v63, v54, v55\n"
"ds_write_b128 v24, v[56:59]\nds_write_b128 v24, v[60:63] offset:32\nv_add_u32 v24, 256, v24\ns_sub_u32 s72, s72, 1\n"
"s_cmp_lg_u32 s72, 0\ns_cbranch_scc1 fo_loop\ns_waitcnt vmcnt(39)\nv_mfma_f32_32x32x16_bf16 v[40:55], a[0:3], v[96:99], 0\n"
"s_waitcnt vmcnt(38)\nv_mfma_f32_32x32x16_bf16 v[40:55], a[4:7], v[100:103], v[40:55]\ns_waitcnt vmcnt(37)\nv_mfma_f32_32x32x16_bf16 v[40:55], a[8:11], v[104:107], v[40:55]\n"
"s_waitcnt vmcnt(36)\nv_mfma_f32_32x32x16_bf16 v[40:55], a[12:15], v[108:111], v[40:55]\ns_waitcnt vmcnt(35)\nv_mfma_f32_32x32x16_bf16 v[40:55], a[16:19], v[112:115], v[40:55]\n"
"s_waitcnt vmcnt(34)\nv_mfma_f32_32x32x16_bf16 v[40:55], a[20:23], v[116:119], v[40:55]\ns_waitcnt vmcnt(33)\nv_mfma_f32_32x32x16_bf16 v[40:55], a[24:27], v[120:123], v[40:55]\n"
"s_waitcnt vmcnt(32)\nv_mfma_f32_32x32x16_bf16 v[40:55], a[28:31], v[124:127], v[40:55]\ns_waitcnt vmcnt(31)\nv_mfma_f32_32x32x16_bf16 v[40:55], a[32:35], v[128:131], v[40:55]\n"
"s_waitcnt vmcnt(30)\nv_mfma_f32_32x32x16_bf16 v[40:55], a[36:39], v[132:135], v[40:55]\ns_waitcnt vmcnt(29)\nv_mfma_f32_32x32x16_bf16 v[40:55], a[40:43], v[136:139], v[40:55]\n"
"s_waitcnt vmcnt(28)\nv_mfma_f32_32x32x16_bf16 v[40:55], a[44:47], v[140:143], v[40:55]\ns_waitcnt vmcnt(27)\nv_mfma_f32_32x32x16_bf16 v[40:55], a[48:51], v[144:147], v[40:55]\n"
"s_waitcnt vmcnt(26)\nv_mfma_f32_32x32x16_bf16 v[40:55], a[52:55], v[148:151], v[40:55]\ns_waitcnt vmcnt(25)\nv_mfma_f32_32x32x16_bf16 v[40:55], a[56:59], v[152:155], v[40:55]\n"
"s_waitcnt vmcnt(24)\nv_mfma_f32_32x32x16_bf16 v[40:55], a[60:63], v[156:159], v[40:55]\ns_waitcnt vmcnt(23)\nv_mfma_f32_32x32x16_bf16 v[40:55], a[64:67], v[160:163], v[40:55]\n"
"s_waitcnt vmcnt(22)\nv_mfma_f32_32x32x16_bf16 v[40:55], a[68:71], v[164:167], v[40:55]\ns_waitcnt vmcnt(21)\nv_mfma_f32_32x32x16_bf16 v[40:55], a[72:75], v[168:171], v[40:55]\n"
"s_waitcnt vmcnt(20)\nv_mfma_f32_32x32x16_bf16 v[40:55], a[76:79], v[172:175], v[40:55]\ns_waitcnt vmcnt(19)\nv_mfma_f32_32x32x16_bf16 v[40:55], a[80:83], v[176:179], v[40:55]\n"
"s_waitcnt vmcnt(18)\nv_mfma_f32_32x32x16_bf16 v[40:55], a[84:87], v[180:183], v[40:55]\ns_waitcnt vmcnt(17)\nv_mfma_f32_32x32x16_bf16 v[40:55], a[88:91], v[184:187], v[40:55]\n"
"s_waitcnt vmcnt(16)\nv_mfma_f32_32x32x16_bf16 v[40:55], a[92:95], v[188:191], v[40:55]\ns_waitcnt vmcnt(15)\nv_mfma_f32_32x32x16_bf16 v[40:55], a[96:99], v[192:195], v[40:55]\n"
"s_waitcnt vmcnt(14)\nv_mfma_f32_32x32x16_bf16 v[40:55], a[100:103], v[196:199], v[40:55]\ns_waitcnt vmcnt(13)\nv_mfma_f32_32x32x16_bf16 v[40:55], a[104:107], v[200:203], v[40:55]\n"
"s_waitcnt vmcnt(12)\nv_mfma_f32_32x32x16_bf16 v[40:55], a[108:111], v[204:207], v[40:55]\ns_waitcnt vmcnt(11)\nv_mfma_f32_32x32x16_bf16 v[40:55], a[112:115], v[208:211], v[40:55]\n"
"s_waitcnt vmcnt(10)\nv_mfma_f32_32x32x16_bf16 v[40:55], a[116:119], v[212:215], v[40:55]\ns_waitcnt vmcnt(9)\nv_mfma_f32_32x32x16_bf16 v[40:55], a[120:123], v[216:219], v[40:55]\n"
"s_waitcnt vmcnt(8)\nv_mfma_f32_32x32x16_bf16 v[40:55], a[124:127], v[220:223], v[40:55]\ns_waitcnt vmcnt(7)\nv_mfma_f32_32x32x16_bf16 v[40:55], a[128:131], v[224:227], v[40:55]\n"
"s_waitcnt vmcnt(6)\nv_mfma_f32_32x32x16_bf16 v[40:55], a[132:135], v[228:231], v[40:55]\ns_waitcnt vmcnt(5)\nv_mfma_f32_32x32x16_bf16 v[40:55], a[136:139], v[232:235], v[40:55]\n"
"s_waitcnt vmcnt(4)\nv_mfma_f32_32x32x16_bf16 v[40:55], a[140:143], v[236:239], v[40:55]\ns_waitcnt vmcnt(3)\nv_mfma_f32_32x32x16_bf16 v[40:55], a[144:147], v[240:243], v[40:55]\n"
"s_waitcnt vmcnt(2)\nv_mfma_f32_32x32x16_bf16 v[40:55], a[148:151], v[244:247], v[40:55]\ns_waitcnt vmcnt(1)\nv_mfma_f32_32x32x16_bf16 v[40:55], a[152:155], v[248:251], v[40:55]\n"
"s_waitcnt vmcnt(0)\nv_mfma_f32_32x32x16_bf16 v[40:55], a[156:159], v[252:255], v[40:55]\ns_nop 11\nv_cvt_pk_bf16_f32 v56, v40, v41\n"
"v_cvt_pk_bf16_f32 v57, v42, v43\nv_cvt_pk_bf16_f32 v58, v44, v45\nv_cvt_pk_bf16_f32 v59, v46, v47\nv_cvt_pk_bf16_f32 v60, v48, v49\n"
"v_cvt_pk_bf16_f32 v61, v50, v51\nv_cvt_pk_bf16_f32 v62, v52, v53\nv_cvt_pk_bf16_f32 v63, v54, v55\nds_write_b128 v24, v[56:59]\n"
"ds_write_b128 v24, v[60:63] offset:32\nv_add_u32 v24, 256, v24\n"
    :: "{s[8:23]}"(s0), "{v[8:23]}"(v0) : "v24", "v25", "v26", "v27", "v28", "v29", "v30", "v31", "v32", "v33", "v34", "v35", "v36", "v37", "v38", "v39", "v40", "v41", "v42", "v43", "v44", "v45", "v46", "v47", "v48", "v49", "v50", "v51", "v52", "v53", "v54", "v55", "v56", "v57", "v58", "v59", "v60", "v61", "v62", "v63", "v64", "v65", "v66", "v67", "v68", "v69", "v70", "v71", "v72", "v73", "v74", "v75", "v76", "v77", "v78", "v79", "v80", "v81", "v82", "v83", "v84", "v85", "v86", "v87", "v88", "v89", "v90", "v91", "v92", "v93", "v94", "v95", "v96", "v97", "v98", "v99", "v100", "v101", "v102", "v103", "v104", "v105", "v106", "v107", "v108", "v109", "v110", "v111", "v112", "v113", "v114", "v115", "v116", "v117", "v118", "v119", "v120", "v121", "v122", "v123", "v124", "v125", "v126", "v127", "v128", "v129", "v130", "v131", "v132", "v133", "v134", "v135", "v136", "v137", "v138", "v139", "v140", "v141", "v142", "v143", "v144", "v145", "v146", "v147", "v148", "v149", "v150", "v151", "v152", "v153", "v154", "v155", "v156", "v157", "v158", "v159", "v160", "v161", "v162", "v163", "v164", "v165", "v166", "v167", "v168", "v169", "v170", "v171", "v172", "v173", "v174", "v175", "v176", "v177", "v178", "v179", "v180", "v181", "v182", "v183", "v184", "v185", "v186", "v187", "v188", "v189", "v190", "v191", "v192", "v193", "v194", "v195", "v196", "v197", "v198", "v199", "v200", "v201", "v202", "v203", "v204", "v205", "v206", "v207", "v208", "v209", "v210", "v211", "v212", "v213", "v214", "v215", "v216", "v217", "v218", "v219", "v220", "v221", "v222", "v223", "v224", "v225", "v226", "v227", "v228", "v229", "v230", "v231", "v232", "v233", "v234", "v235", "v236", "v237", "v238", "v239", "v240", "v241", "v242", "v243", "v244", "v245", "v246", "v247", "v248", "v249", "v250", "v251", "v252", "v253", "v254", "v255", "a0", "a1", "a2", "a3", "a4", "a5", "a6", "a7", "a8", "a9", "a10", "a11", "a12", "a13", "a14", "a15", "a16", "a17", "a18", "a19", "a20", "a21", "a22", "a23", "a24", "a25", "a26", "a27", "a28", "a29", "a30", "a31", "a32", "a33", "a34", "a35", "a36", "a37", "a38", "a39", "a40", "a41", "a42", "a43", "a44", "a45", "a46", "a47", "a48", "a49", "a50", "a51", "a52", "a53", "a54", "a55", "a56", "a57", "a58", "a59", "a60", "a61", "a62", "a63", "a64", "a65", "a66", "a67", "a68", "a69", "a70", "a71", "a72", "a73", "a74", "a75", "a76", "a77", "a78", "a79", "a80", "a81", "a82", "a83", "a84", "a85", "a86", "a87", "a88", "a89", "a90", "a91", "a92", "a93", "a94", "a95", "a96", "a97", "a98", "a99", "a100", "a101", "a102", "a103", "a104", "a105", "a106", "a107", "a108", "a109", "a110", "a111", "a112", "a113", "a114", "a115", "a116", "a117", "a118", "a119", "a120", "a121", "a122", "a123", "a124", "a125", "a126", "a127", "a128", "a129", "a130", "a131", "a132", "a133", "a134", "a135", "a136", "a137", "a138", "a139", "a140", "a141", "a142", "a143", "a144", "a145", "a146", "a147", "a148", "a149", "a150", "a151", "a152", "a153", "a154", "a155", "a156", "a157", "a158", "a159", "a160", "a161", "a162", "a163", "a164", "a165", "a166", "a167", "a168", "a169", "a170", "a171", "a172", "a173", "a174", "a175", "a176", "a177", "a178", "a179", "a180", "a181", "a182", "a183", "a184", "a185", "a186", "a187", "a188", "a189", "a190", "a191", "a192", "a193", "a194", "a195", "a196", "a197", "a198", "a199", "a200", "a201", "a202", "a203", "a204", "a205", "a206", "a207", "a208", "a209", "a210", "a211", "a212", "a213", "a214", "a215", "a216", "a217", "a218", "a219", "a220", "a221", "a222", "a223", "s40", "s41", "s42", "s43", "s44", "s45", "s46", "s47", "s48", "s49", "s50", "s51", "s52", "s53", "s54", "s55", "s56", "s57", "s58", "s59", "s60", "s61", "s62", "s63", "s64", "s65", "s66", "s67", "s68", "s69", "s70", "s71", "s72", "s73", "s74", "s75", "s76", "s77", "s78", "s79", "s80", "s81", "vcc", "scc", "memory");
}
DI u4 pack8(const f16v& x, int s, float sc) { return (u4){pk2(x[8 * s] * sc, x[8 * s + 1] * sc), pk2(x[8 * s + 2] * sc, x[8 * s + 3] * sc), pk2(x[8 * s + 4] * sc, x[8 * s + 5] * sc), pk2(x[8 * s + 6] * sc, x[8 * s + 7] * sc)}; }
DI u4 pack8(const f16v& x, int s) { return (u4){pk2(x[8 * s], x[8 * s + 1]), pk2(x[8 * s + 2], x[8 * s + 3]), pk2(x[8 * s + 4], x[8 * s + 5]), pk2(x[8 * s + 6], x[8 * s + 7])}; }
DI void dma_pair(u4 srd, int v0, int v1, int soff, unsigned m) {
    asm volatile("s_mov_b32 m0, %4\n\ts_nop 0\n\tbuffer_load_dwordx4 %0, %2, %3 offen lds\n\tbuffer_load_dwordx4 %1, %2, %3 offen offset:1024 lds" :: "v"(v0), "v"(v1), "s"(srd), "s"(soff), "s"(m) : "memory");
}
DI void dma_one(u4 srd, int v0, int soff, unsigned m) {
    asm volatile("s_mov_b32 m0, %3\n\ts_nop 0\n\tbuffer_load_dwordx4 %0, %1, %2 offen lds" :: "v"(v0), "s"(srd), "s"(soff), "s"(m) : "memory");
}
DI void mV_va(f16v& c, u4 x, u4 y) { asm volatile("v_mfma_f32_32x32x16_bf16 %0, %1, %2, %0" : "+v"(c) : "v"(x), "a"(y)); }
DI void mV_va0(f16v& c, u4 x, u4 y) { asm volatile("v_mfma_f32_32x32x16_bf16 %0, %1, %2, 0" : "=&v"(c) : "v"(x), "a"(y)); }
DI void mV_av(f16v& c, u4 x, u4 y) { asm volatile("v_mfma_f32_32x32x16_bf16 %0, %1, %2, %0" : "+v"(c) : "a"(x), "v"(y)); }
DI void mV_av0(f16v& c, u4 x, u4 y) { asm volatile("v_mfma_f32_32x32x16_bf16 %0, %1, %2, 0" : "=&v"(c) : "a"(x), "v"(y)); }
DI void mV_vv(f16v& c, u4 x, u4 y) { asm volatile("v_mfma_f32_32x32x16_bf16 %0, %1, %2, %0" : "+v"(c) : "v"(x), "v"(y)); }
DI void mV_vv0(f16v& c, u4 x, u4 y) { asm volatile("v_mfma_f32_32x32x16_bf16 %0, %1, %2, 0" : "=&v"(c) : "v"(x), "v"(y)); }
template <int N, int PD, class RD, class MM, class EX> DI void pipe(RD rd, MM mm, EX ex) {
    u4 f[PD];
#pragma unroll
    for (int j = 0; j < PD; ++j) f[j] = rd(j);
    SB();
#pragma unroll
    for (int j = 0; j < N; ++j) { mm(j, f[j % PD]); if (j + PD < N) f[j % PD] = rd(j + PD); ex(j); SB(); }
}
DI int swzr(int row) { return ((row >> 2) & 1) | ((row & 3) << 1) | (row & 8); }
DI int swap23(int x) { return (x & ~12) | ((x & 4) << 1) | ((x & 8) >> 1); }
#define SLICE 32768
#define MFW32(x, y) asm volatile("s_nop 11" : "+v"(x), "+v"(y))
struct SideCtx {
    char LAS* sm; unsigned smb; int lane, wave;
    int vr0, vr1, vc0, vc1;
};
DI SideCtx make_ctx(int wave) {
    SideCtx c; c.sm = (char LAS*)smem_; c.smb = (unsigned)(size_t)c.sm; c.lane = fresh_lane(); c.wave = wave;
    asm volatile("" : "+s"(c.wave), "+s"(c.smb));
    const int l = c.lane;
    { int row = 4 * c.wave + (l >> 5); c.vr0 = row * 8192 + (((l & 31) ^ swzr(row)) * 16); row += 2; c.vr1 = row * 8192 + (((l & 31) ^ swzr(row)) * 16) - 1024; }
    { int row = l >> 3; c.vc0 = row * 896 + (((l & 7) ^ (((row >> 1) & 1) << 2)) * 16); c.vc1 = c.vc0 + 8 * 896 - 1024; }
    return c;
}
DI void dma_rows_q(const SideCtx& c, u4 srd, int h, int r0, int slot, int g) { dma_pair(srd, c.vr0, c.vr1, (r0 + 16 * g) * 8192 + h * 512, c.smb + slot * SLICE + (8 * g + 2 * c.wave) * 1024); }
DI void dma_cols_q(const SideCtx& c, u4 srd, int h, int c0, int slot, int g) { const int k = 8 * c.wave + 2 * g; dma_pair(srd, c.vc0, c.vc1, (h * 256 + 8 * k) * 896 + c0 * 2, c.smb + slot * SLICE + k * 1024); }
DI void dma_rows(const SideCtx& c, u4 srd, int h, int r0, int slot) {
#pragma unroll
    for (int g = 0; g < 4; ++g) dma_rows_q(c, srd, h, r0, slot, g);
}
DI void dma_cols(const SideCtx& c, u4 srd, int h, int c0, int slot) {
#pragma unroll
    for (int g = 0; g < 4; ++g) dma_cols_q(c, srd, h, c0, slot, g);
}
DI void set_flag(const SideCtx& c, u4 rhb, int off, unsigned tag) {
    waitvm<0>(); bar();
    if (c.wave == 0) sst32(tag, rhb, c.lane == 0 ? off : OOB, 0);
}

DI void slp(int n) {
#pragma nounroll
    for (int i = 0; i < n; ++i) asm volatile("s_sleep 1");
}
DI int xsrc(int wave, int lane, int k) { const int n = 64 * (7 * (wave & 1) + k) + lane, row = (n * 1171) >> 16, pos = n - 56 * row; return (row + 16 * (wave >> 1)) * 896 + ((pos ^ ((row >> 1) & 7)) * 16); }
DI int xrd(int row, int hi, int k) { return row * 896 + (((2 * k + hi) ^ ((row >> 1) & 7)) * 16); }
#define WSL(u) ((u) + 1)

DI int xswz(int type, int row) { return type ? ((row >> 1) & 1) | (((row >> 3) & 3) << 1) : (row >> 1) & 7; }
DI int xsrc8(int type, int wave, int lane) { const int row = 8 * wave + (lane >> 3); return row * 896 + (((lane & 7) ^ xswz(type, row)) * 16); }
#define C_AT -1
#define C_PF 3
#define W_PF 2
#define X_PF 4
DI void dma_line(u4 rw1, const SideCtx& c, int half, int h, int sub) {
    const int n = (sub * 4 + c.wave) * 48 + c.lane;
    asm volatile("s_mov_b32 m0, %3\n\ts_nop 0\n\tbuffer_load_dword %0, %1, %2 offen lds" :: "v"(c.lane < 48 ? (n >> 2) * 8192 + (n & 3) * 128 : OOB), "s"(rw1), "s"((256 * half + 64) * 8192 + h * 512), "s"(c.smb + 28672 + 256) : "memory");
}
DI const char* cold_line(int half, int type, int h, int sub, int wave, int l) {
    CArgs& a = get_args();
    const int wid = (((half * 2 + type) * 16 + h) * 4 + sub) * 4 + wave;
    if (!(C_PF & 2) && l >= 14) l = 13;
    return l < 14 ? (const char*)a.X + (wid * 14 + l) * 128 : l < 28 ? (const char*)a.Au + (wid * 14 + l - 14) * 128
         : (const char*)((wid & 2) ? ((wid & 1) ? a.ACi : a.ACr) : ((wid & 1) ? a.Aim : a.AlogA)) + ((wid >> 2) * 7 + min(l - 28, 6)) * 128;
}
template <int PART>
DI void gemm_first(int wave, int half, int type, int h, int sub) {
    CArgs& a = get_args();
    const SideCtx c = make_ctx(wave);
    h = __builtin_amdgcn_readfirstlane(h); sub = __builtin_amdgcn_readfirstlane(sub); half = __builtin_amdgcn_readfirstlane(half); asm volatile("" : "+s"(h), "+s"(sub), "+s"(half));
    if (PART == 0) {
        const u4 rw2 = mksrd(type ? a.Wo : a.Wq, 448 * 8192);
        const int c1 = sub == 3 ? 384 : sub * 128 + 64;
        if (type) { dma_cols(c, rw2, h, sub * 128, WSL(2)); dma_cols(c, rw2, h, c1, WSL(3)); } else { dma_rows(c, rw2, h, sub * 128, WSL(2)); dma_rows(c, rw2, h, c1, WSL(3)); }
    } else {
        const u4 rw1 = mksrd(type ? a.Wv : a.Wk, 448 * 8192);
        if (W_PF == 1) dma_line(rw1, c, half, h, sub);
        dma_rows(c, rw1, h, 256 * half, WSL(0));
        if (W_PF == 2) dma_line(rw1, c, half, h, sub);
        const int n = (((type * 4 + (h & 3)) * 4 + sub) * 4 + c.wave) * 8 + c.lane;
        asm volatile("s_mov_b32 m0, %2\n\ts_nop 0\n\tbuffer_load_dword %0, %1, 0 offen lds" :: "v"(c.lane < 8 ? n * 128 : OOB), "s"(mksrd(a.X, half ? 28672 : X_PF * 28672)), "s"(c.smb + 28672 + 256) : "memory");
        if (C_AT < 0) asm volatile("global_load_lds_dword %0, off" :: "v"(cold_line(half, type, h, sub, c.wave, c.lane)) : "memory");
    }
}
template <int TYPE>
DI u8v gemm_role(unsigned tag, int vb, int wave, int half, int h, int sub) {
    CArgs& a = get_args();
    const SideCtx c = make_ctx(wave);
    const int lane = c.lane, g = lane >> 4, n = lane & 15;
    h = __builtin_amdgcn_readfirstlane(h); sub = __builtin_amdgcn_readfirstlane(sub); half = __builtin_amdgcn_readfirstlane(half); asm volatile("" : "+s"(h), "+s"(sub), "+s"(half));
    const int B = 4 * sub + c.wave, prow = 8 * (n >> 2) + (n & 3);
    GCtx x;
    x.rw1 = mksrd(TYPE ? a.Wv : a.Wk, 448 * 8192); x.rx = mksrd(a.X, half ? 0 : 2048 * 896); x.rhb = mksrd(a.HB, HB_SZ);
    x.rexp = TYPE ? mksrd(a.UM, 28 * MH) : mksrd(a.SXM, 64 * SH); x.rout = mksrd(TYPE ? a.UF : a.GF, GSZ);
    x.tag = tag; x.rot = 4 * half; x.sw1 = h * 512; x.mw = c.smb + c.wave * 2048; x.mx = c.smb + c.wave * 1024;
    x.sfg = O_FG - O_FW + (h * 2 + TYPE - TYPE * 16 - h) * 256; x.stay = half ? 2 : sub < 2; x.csc = __float_as_uint(CSCALE); x.ostr = TYPE ? 16384 : 28672;
    x.p1 = (unsigned)(u64)a.Au; x.p2 = (unsigned)((u64)a.Au >> 32); x.p5 = min(((TYPE * 16 + h) * 16 + B) * 3584, 2048 * 896 - 4096) + lane * 16; x.kb = 65536 + c.wave * 16384;
    x.vr0 = c.vr0; x.vr1 = c.vr1; x.vx = xsrc8(TYPE, c.wave, lane);
    x.wa = c.smb + SLICE + prow * 512 + ((g ^ swzr(prow)) * 16);
    { const int row = TYPE ? prow : n; x.xa = c.smb + row * 128 + ((g ^ xswz(TYPE, row)) * 16); }
    auto wof = [&](int blk) { return (TYPE ? 2 * blk * MH + NC * 4096 + h * 14336 : (h * 4 + (blk >> 2)) * SH + NC * 1024 + (blk & 3) * 28672) + lane * 16; };
    auto oof = [&](int blk, int i) { return blk > 13 ? OOB : (TYPE ? (2 * blk + i) * UH + h * 1024 : h * GH + i * 14336 + blk * 1024) + lane * 16; };
    x.vexp = B > 13 ? OOB : wof(B); x.vimp = B > 5 ? OOB : wof(B + 8);
    x.vk = c.smb + (3 + (c.wave >> 1)) * SLICE + (8 * g + (n >> 2)) * 128 + (((4 * (c.wave & 1) + ((n & 3) >> 1)) ^ ((n >> 3) << 2)) * 16) + 8 * (n & 1);
    x.vo0 = oof(B, 0); x.vo1 = oof(B, 1); x.vo2 = oof(B + 8, 0); x.vo3 = oof(B + 8, 1);
    x.vfl = (c.wave == 0 && lane == 0) ? O_FW + half * 8192 + ((TYPE * 16 + h) * 4 + sub) * 64 : OOB;
    x.vpf = O_FW + (lane & 1) * 8192 + ((TYPE * 16 + h) * 4 + (sub | 2)) * 64; x.vpo = O_FW + 8192 + ((TYPE * 16 + h) * 4 + sub) * 64;
    if (half) {
        const int id = vb >> 3;
        x.vpo = need_off(vb & 3, id, c.wave, lane); x.vpf = O_PAR + 4; x.vo0 = O_PAR + 512 + min(lane, 35) * 32;
        if (TYPE && id >= 20 && id < 27) { x.stay = 3; x.vimp = lane * 16; x.ostr = ((vb & 3) * 7 + id - 20) * MH + NC * 4096 + 4 * c.wave * 14336; }
    }
    PSTAMP(1);
    u8v w = {};
    { const u64 p = (u64)cold_line(half, TYPE, h, sub, c.wave, lane); w[0] = (unsigned)p; w[1] = (unsigned)(p >> 32); }
    const unsigned tw = TYPE ? gemm_asm1(x, w) : gemm_asm0(x, w);
    return w;
}

#define O_LAG 2
#define O_LAGN 24
DI void o_task(unsigned tag, int wave, int s) {
    {
        CArgs& a = get_args();
        const SideCtx c = make_ctx(wave);
        const int lane = c.lane, hi = lane >> 5, l31 = lane & 31, w = c.wave, pc = swap23(l31);
        s = __builtin_amdgcn_readfirstlane(s); asm volatile("" : "+s"(s));
        const u4 rhb = mksrd(a.HB, HB_SZ), rx = mksrd(a.X, 2048 * 896);
        bar();
        const int vx = xsrc8(0, w, lane);
#pragma unroll
        for (int k = 0; k < 7; ++k) dma_one(rx, vx, s * 28672 + k * 128, c.smb + k * 4096 + w * 1024);
        wait_flags(rhb, lane < 16 ? O_FH + (16 * w + lane) * 128 : OOB, tag + (s < O_LAGN ? s + O_LAG - (O_LAG - 2) * s / O_LAGN : s + 2));
        OCtx x;
        x.r = mksrd(a.HB, 0x7000000);
        x.sg = (unsigned)((char*)a.GF - a.HB) + s * 28672; x.ssx = (unsigned)((char*)a.SXM - a.HB) + s * 1024; x.hx = 4 * w;
        x.su = (unsigned)((char*)a.UF - a.HB) + s * 16384 + w * 2 * UH; x.sum = (unsigned)((char*)a.UM - a.HB) + s * 4096 + w * 2 * MH; x.nt = w < 2 ? 4 : 3;
        x.sr = GH; x.p0 = SH; x.p1 = 8 * UH; x.p2 = 8 * MH; x.p3 = x.p4 = x.p5 = x.p6 = x.p7 = x.p8 = x.p9 = x.pa = 0;
        x.vg = (pc >> 4) * 14336 + hi * 256 + (pc & 15) * 16;
        x.vsx = (l31 >> 4) * 512 + hi * 256 + (l31 & 15) * 16;
        x.vu = (pc >> 4) * UH + hi * 256 + (pc & 15) * 16; x.vum = (pc >> 4) * MH + hi * 512 + (pc & 15) * 16;
        x.vx = c.smb + l31 * 128 + ((hi ^ xswz(0, l31)) * 16); x.vt = l31 - 8 * hi;
        x.vpx = c.smb + 32768 + x.hx * 2048 + lane * 16; x.vpm = c.smb + 65536 + w * 2048 + lane * 16; x.vpr = c.smb + 32768 + lane * 16;
        x.vo = c.smb + 73728 + l31 * 912 + w * 64 + hi * 16;
        o_asm(x);
    }
    bar();
    const int tid = wave * 64 + fresh_lane();
    const u4 ro = mksrd(get_args().out, 2048 * 1792);
#pragma unroll
    for (int k = 0; k < 7; ++k) sst128(*(const u4 LAS*)(smem_ + 73728 + (tid >> 3) * 912 + (tid & 7) * 16 + k * 128), ro, (tid >> 3) * 1792 + (tid & 7) * 16, s * 32 * 1792 + 896 + k * 128);
}

DI void e_task(unsigned tag, int wave, int s, int hg) {
    CArgs& a = get_args();
    const SideCtx c = make_ctx(wave);
    const int lane = c.lane, hi = lane >> 5, l31 = lane & 31;
    s = __builtin_amdgcn_readfirstlane(s); hg = __builtin_amdgcn_readfirstlane(hg); asm volatile("" : "+s"(s), "+s"(hg));
    const int h = 4 * hg + c.wave;
    const u4 rhb = mksrd(a.HB, HB_SZ), rg = mksrd(a.GF, GSZ), rx = mksrd(a.X, 2048 * 896);
    bar();
#pragma unroll
    for (int k = 0; k < 7; ++k) dma_one(rx, xsrc(c.wave, lane, k), s * 28672, c.smb + (7 * c.wave + k) * 1024);
    wait_flags(rhb, lane < 4 ? O_FG + (h * 8 + lane) * 64 : OOB, tag + s + 1);
    u4 gf[28];
    const int key = swap23(l31), o_g = (key >> 4) * 14336 + hi * 256 + (key & 15) * 16;
#pragma unroll
    for (int k = 0; k < 28; ++k) asm volatile("buffer_load_dwordx4 %0, %1, %2, %3 offen sc1" : "=a"(gf[k]) : "v"(o_g), "s"(rg), "s"(h * GH + s * 28672 + k * 512) : "memory");
    bar();
    f16v sx;
    pipe<28, 4>([&](int k) { return *(const u4 LAS*)(c.sm + xrd(l31, hi, k & 3) + (k >> 2) * 128); },
                [&](int k, u4 f) { if (k == 0) WAITV(21, "+a"(gf[0]), "+a"(gf[1]), "+a"(gf[2]), "+a"(gf[3]), "+a"(gf[4]), "+a"(gf[5]), "+a"(gf[6]));
                                   if (k == 7) WAITV(14, "+a"(gf[7]), "+a"(gf[8]), "+a"(gf[9]), "+a"(gf[10]), "+a"(gf[11]), "+a"(gf[12]), "+a"(gf[13]));
                                   if (k == 14) WAITV(7, "+a"(gf[14]), "+a"(gf[15]), "+a"(gf[16]), "+a"(gf[17]), "+a"(gf[18]), "+a"(gf[19]), "+a"(gf[20]));
                                   if (k == 21) WAITV(0, "+a"(gf[21]), "+a"(gf[22]), "+a"(gf[23]), "+a"(gf[24]), "+a"(gf[25]), "+a"(gf[26]), "+a"(gf[27]));
                                   if (k == 0) mV_av0(sx, gf[k], f); else mV_av(sx, gf[k], f); },
                [&](int) {});
    asm volatile("s_nop 11" : "+v"(sx));
    float m = -3.0e38f;
#pragma unroll
    for (int j = 0; j < 16; ++j) { if (16 * (j >> 3) + 8 * hi + (j & 7) > l31) sx[j] = -INFINITY; m = fmaxf(m, sx[j]); }
    m = x32max(m);
    float z = 0.f;
#pragma unroll
    for (int j = 0; j < 16; ++j) { sx[j] = __builtin_amdgcn_exp2f(sx[j] - m); z += sx[j]; }
    z = x32add(z);
    const u4 rex = mksrd(a.EX, 64 * 16 * 2048), rmz = mksrd(a.MZX, 64 * 16 * 256);
    sst128(pack8(sx, 0), rex, lane * 16, (s * 16 + h) * 2048); sst128(pack8(sx, 1), rex, lane * 16, (s * 16 + h) * 2048 + 1024);
    sst64((u2){__float_as_uint(m), __float_as_uint(z)}, rmz, lane < 32 ? lane * 8 : OOB, (s * 16 + h) * 256);
    set_flag(c, rhb, O_FE + (s * 4 + hg) * 64, tag);
}

#define NA 4
#define A_SLP 0
#define A_STG 0
#define U_W 0
#define A_LATE 1
#define X_JIT 4
#define P_XSLP 24
#define P_USLP 4
DI void x_task(unsigned tag, int wave, int s, int cb) {
    CArgs& a = get_args();
    const SideCtx c = make_ctx(wave);
    const int lane = c.lane, hi = lane >> 5, l31 = lane & 31, w = c.wave;
    s = __builtin_amdgcn_readfirstlane(s); cb = __builtin_amdgcn_readfirstlane(cb); asm volatile("" : "+s"(s), "+s"(cb));
    const u4 rhb = mksrd(a.HB, HB_SZ);
    const u4 ru = mksrd(a.UF, GSZ), rex = mksrd(a.EX, 64 * 16 * 2048), rmz = mksrd(a.MZX, 64 * 16 * 256);
    const u4 rum = mksrd(a.UM, 28 * MH), rsx = mksrd(a.SXM, 64 * SH);
    if (X_JIT) wait_flags(rhb, lane == 0 ? O_FH + (cb * 4 + w) * 128 : OOB, tag + s - X_JIT);
    wait_flags(rhb, lane < 8 ? O_FG + (((4 * w + (lane & 3)) * 2 + 1) * 4 + ((cb >> 1) & (lane < 4 ? 3 : 1))) * 64 : lane == 8 ? O_FE + (s * 4 + w) * 64 : OOB, lane < 8 ? tag + s + 1 : tag);
    u4 uf[4][2][2], ex[4][2]; u2 mz[4];
    const int o_u = (l31 >> 4) * UH + hi * 256 + (l31 & 15) * 16;
#pragma unroll
    for (int k = 0; k < 4; ++k) {
        const int h = 4 * w + k, sh = s * 16 + h;
#pragma unroll
        for (int sg = 0; sg < 2; ++sg) {
            ex[k][sg] = sld128(rex, lane * 16, sh * 2048 + sg * 1024);
#pragma unroll
            for (int nt = 0; nt < 2; ++nt) uf[k][sg][nt] = sld128(ru, o_u, (4 * cb + 2 * nt) * UH + sh * 1024 + sg * 512);
        }
        mz[k] = sld64(rmz, l31 * 8, sh * 256);
        asm volatile("s_sleep %0" :: "i"(P_XSLP));
    }
    waitvm<0>();
    f16v rr[4][2];
#pragma unroll
    for (int k = 0; k < 4; ++k) {
        asm volatile("" : "+v"(mz[k]), "+v"(ex[k][0]), "+v"(ex[k][1]), "+v"(uf[k][0][0]), "+v"(uf[k][0][1]), "+v"(uf[k][1][0]), "+v"(uf[k][1][1]));
        mV_vv0(rr[k][0], uf[k][0][0], ex[k][0]); mV_vv0(rr[k][1], uf[k][0][1], ex[k][0]);
        mV_vv(rr[k][0], uf[k][1][0], ex[k][1]); mV_vv(rr[k][1], uf[k][1][1], ex[k][1]);
        MFW32(rr[k][0], rr[k][1]);
    }
    wait_flags<2>(rhb, lane < 16 ? O_FH + (16 * w + lane) * 128 : s == 63 && lane < 32 ? O_FH + (16 * w + lane - 16) * 128 + 64 : OOB, tag + (s < 62 ? s + 2 : s + 1));
    u4 sp[4][4], um[2][2];
    const int o_sx = (l31 >> 4) * 512 + hi * 256 + (l31 & 15) * 16, o_um = (l31 >> 4) * MH + hi * 512 + (l31 & 15) * 16;
#pragma unroll
    for (int k = 0; k < 16; ++k) sp[k >> 2][k & 3] = sld128(rsx, o_sx, ((4 * w + (k >> 2)) * 4 + (k & 3)) * SH + s * 1024);
    if (s >= 62) wait_flags<2>(rhb, lane < 4 ? O_FP + ((4 * cb + lane) * 4 + w) * 64 : OOB, tag + 64);
#pragma unroll
    for (int k = 0; k < 4; ++k) um[k >> 1][k & 1] = sld128(rum, o_um, (4 * cb + 2 * (k & 1)) * MH + s * 4096 + w * 1024 + (k >> 1) * 256);
    u2 pm[4]; float scx[4];
#pragma unroll
    for (int k = 0; k < 4; ++k) {
        if (k == 0) WAITV(16, "+v"(sp[0][0]), "+v"(sp[0][1]), "+v"(sp[0][2]), "+v"(sp[0][3]));
        if (k == 1) WAITV(12, "+v"(sp[1][0]), "+v"(sp[1][1]), "+v"(sp[1][2]), "+v"(sp[1][3]));
        if (k == 2) WAITV(8, "+v"(sp[2][0]), "+v"(sp[2][1]), "+v"(sp[2][2]), "+v"(sp[2][3]));
        if (k == 3) WAITV(4, "+v"(sp[3][0]), "+v"(sp[3][1]), "+v"(sp[3][2]), "+v"(sp[3][3]));
        const f4 sc = (__builtin_bit_cast(f4, sp[k][0]) + __builtin_bit_cast(f4, sp[k][1])) + (__builtin_bit_cast(f4, sp[k][2]) + __builtin_bit_cast(f4, sp[k][3]));
        const float mx = __uint_as_float(mz[k][0]);
        float m = fmaxf(x32max(fmaxf(fmaxf(sc[0], sc[1]), fmaxf(sc[2], sc[3]))), mx);
        float e[4];
#pragma unroll
        for (int j = 0; j < 4; ++j) e[j] = __builtin_amdgcn_exp2f(sc[j] - m);
        const float cx = __builtin_amdgcn_exp2f(mx - m);
        const float z = x32add((e[0] + e[1]) + (e[2] + e[3])) + cx * __uint_as_float(mz[k][1]);
        const float inv = __builtin_amdgcn_rcpf(z);
        scx[k] = cx * inv;
        pm[k] = (u2){pk2(e[0] * inv, e[1] * inv), pk2(e[2] * inv, e[3] * inv)};
    }
    WAITV(0, "+v"(um[0][0]), "+v"(um[0][1]), "+v"(um[1][0]), "+v"(um[1][1]));
    f16v d0, d1;
    {
        u4 p0 = {pm[0][0], pm[1][0], pm[2][0], pm[3][0]}, p1 = {pm[0][1], pm[1][1], pm[2][1], pm[3][1]};
        asm volatile("s_nop 1\n\tv_mfma_f32_32x32x16_bf16 %0, %2, %6, 0\n\tv_mfma_f32_32x32x16_bf16 %1, %3, %6, 0\n\tv_mfma_f32_32x32x16_bf16 %0, %4, %7, %0\n\tv_mfma_f32_32x32x16_bf16 %1, %5, %7, %1"
                     : "=&v"(d0), "=&v"(d1) : "v"(um[0][0]), "v"(um[0][1]), "v"(um[1][0]), "v"(um[1][1]), "v"(p0), "v"(p1));
    }
    f16v a0 = (rr[0][0] * scx[0] + rr[1][0] * scx[1]) + (rr[2][0] * scx[2] + rr[3][0] * scx[3]);
    f16v a1 = (rr[0][1] * scx[0] + rr[1][1] * scx[1]) + (rr[2][1] * scx[2] + rr[3][1] * scx[3]);
    asm volatile("s_nop 11" : "+v"(d0), "+v"(d1), "+v"(a0), "+v"(a1));
    a0 += d0; a1 += d1;
    bar();
#pragma unroll
    for (int g = 0; g < 4; ++g) {
        *(f4 LAS*)(c.sm + w * 8704 + l31 * 272 + (8 * g + 4 * hi) * 4) = (f4){a0[4 * g], a0[4 * g + 1], a0[4 * g + 2], a0[4 * g + 3]};
        *(f4 LAS*)(c.sm + w * 8704 + l31 * 272 + 128 + (8 * g + 4 * hi) * 4) = (f4){a1[4 * g], a1[4 * g + 1], a1[4 * g + 2], a1[4 * g + 3]};
    }
    bar();
    {
        const int t = w * 8 + (lane >> 3), c8 = lane & 7;
        f4 s0 = {0.f, 0.f, 0.f, 0.f}, s1 = s0;
#pragma unroll
        for (int p = 0; p < 4; ++p) { s0 += *(const f4 LAS*)(c.sm + p * 8704 + t * 272 + c8 * 32); s1 += *(const f4 LAS*)(c.sm + p * 8704 + t * 272 + c8 * 32 + 16); }
        sst128((u4){pk2(s0[0], s0[1]), pk2(s0[2], s0[3]), pk2(s1[0], s1[1]), pk2(s1[2], s1[3])}, mksrd(a.out, 2048 * 1792), t * 1792 + c8 * 16, s * 32 * 1792 + 896 + cb * 128);
    }
}

#define PA_UT 0
#define PA_TU 32896
#define PA_KH (PA_TU + 33280)
#define PA_SC (PA_KH + 7680)
#define PA_KT (PA_SC + 128)
#define PA_RED (PA_KT + 24576)
typedef __attribute__((ext_vector_type(2))) _Float16 h2;
typedef __attribute__((ext_vector_type(8))) _Float16 h8;
DI unsigned pkh(float a, float b) { f2 v = {a, b}; return __builtin_bit_cast(unsigned, __builtin_convertvector(v, h2)); }
DI void parta_task(unsigned tag, int wave, int task) {
    CArgs& a = get_args();
    const SideCtx c = make_ctx(wave);
    char LAS* sm = c.sm;
    const int lane = c.lane, w = c.wave, tid = w * 64 + lane, hi = lane >> 5, l31 = lane & 31, l15 = lane & 15, kk = lane >> 4;
    task = __builtin_amdgcn_readfirstlane(task); asm volatile("" : "+s"(task));
    const int d0 = task * 8;
    const u4 rpa = mksrd(a.PA, 56 * 32768);
    if (task >= NA) slp(((task * 23) % 52) * A_STG);
    bar();
#pragma unroll
    for (int g = 0; g < 2; ++g) {
        const int d = d0 + 4 * g + w;
        const float dt = bfr(expf(bf2f(a.Alogdt[d])));
        const u2 x0 = *(const u2*)(a.AlogA + d * 256 + lane * 4), x1 = *(const u2*)(a.Aim + d * 256 + lane * 4), x2 = *(const u2*)(a.ACr + d * 256 + lane * 4), x3 = *(const u2*)(a.ACi + d * 256 + lane * 4);
        f4 z[4]; unsigned mx = 0;
#pragma unroll
        for (int i = 0; i < 4; ++i) {
            auto el = [&](u2 x) { return bf2f((u16)(x[i >> 1] >> (16 * (i & 1)))); };
            z[i] = (f4){-bfr(expf(el(x0))) * dt * LOG2E, el(x1) * dt * 0.15915494309189535f, 2.f * el(x2) * dt, 2.f * el(x3) * dt};
            mx = max(mx, max(__float_as_uint(z[i][2]) & 0x7f800000u, __float_as_uint(z[i][3]) & 0x7f800000u));
        }
#pragma unroll
        for (int o = 1; o < 64; o <<= 1) mx = max(mx, (unsigned)__shfl_xor((int)mx, o));
        mx = min(max(mx, 0x00800000u), 0x7e800000u);
        *(unsigned LAS*)(sm + PA_SC + (4 * g + w) * 4) = mx;
        const float inv = __uint_as_float(0x7f000000u - mx);
#pragma unroll
        for (int i = 0; i < 4; ++i) { z[i][2] *= inv; z[i][3] *= inv; *(f4 LAS*)(sm + PA_RED + w * 9216 + g * 4096 + (i * 64 + lane) * 16) = z[i]; }
    }
    const u4 dsk4 = *(const u4*)(a.ADsk + d0);
    if (tid < 8) *(u4 LAS*)(sm + PA_UT + tid * 4112 + 4096) = (u4){0u, 0u, 0u, 0u};
    if (tid < 155) *(u4 LAS*)(sm + PA_KH + tid * 16) = (u4){0u, 0u, 0u, 0u};
    float qpos[4];
    qpos[0] = (float)l15;
    qpos[1] = l15 < 8 ? (float)(256 + 2 * l15) : (float)(512 + ((l15 - 8) >> 2) * 256 + (l15 & 3) * 4);
    qpos[2] = l15 < 8 ? (float)(1024 + (l15 >> 1) * 256 + (l15 & 1) * 8) : 2048.f;
    qpos[3] = (float)(16 * l15);
    auto kgen = [&](int g) __attribute__((always_inline)) {
        const char LAS* pt = sm + PA_RED + w * 9216 + g * 4096 + kk * 16;
        f4 acc[3] = {{0.f, 0.f, 0.f, 0.f}, {0.f, 0.f, 0.f, 0.f}, {0.f, 0.f, 0.f, 0.f}};
#pragma nounroll
        for (int st = 0; st < 16; ++st) {
            u4 op[4];
#pragma unroll
            for (int i = 0; i < 4; ++i) {
                const f4 z = *(const f4 LAS*)(pt + st * 256 + i * 64);
#pragma unroll
                for (int t = 0; t < 4; ++t) {
                    const float ex = __builtin_amdgcn_exp2f(z[0] * qpos[t]), rv = __builtin_amdgcn_fractf(z[1] * qpos[t]);
                    const float er = ex * __builtin_amdgcn_cosf(rv), ei = ex * __builtin_amdgcn_sinf(rv);
                    op[t][i] = t < 3 ? pkh(er, ei) : pkh(z[2] * er - z[3] * ei, -(z[2] * ei + z[3] * er));
                }
            }
#pragma unroll
            for (int t = 0; t < 3; ++t) acc[t] = __builtin_amdgcn_mfma_f32_16x16x32_f16(__builtin_bit_cast(h8, op[3]), __builtin_bit_cast(h8, op[t]), acc[t], 0, 0, 0);
        }
#pragma unroll
        for (int t = 0; t < 3; ++t) *(f4 LAS*)(sm + PA_KT + (4 * g + w) * 3072 + ((t * 16 + l15) * 16 + 4 * kk) * 4) = acc[t];
    };
    if (U_W) {
        const u4 ru = mksrd(a.Au, 2048 * 896);
#pragma unroll
        for (int i = 0; i < 8; ++i) { dma_one(ru, tid * 16, task * 32768 + i * 4096, c.smb + PA_TU + w * 1024); asm volatile("s_sleep 4"); }
    }
    kgen(0);
    if (A_LATE && task < NA) kgen(1);
    {
        const u4 ru = mksrd(a.Au, 2048 * 896);
#pragma unroll
        for (int i = 0; i < 8; ++i) { dma_one(ru, tid * 896, i * 256 * 896 + d0 * 2, c.smb + PA_TU + i * 4096 + w * 1024); asm volatile("s_sleep %0" :: "i"(P_USLP)); }
    }
    if (!(A_LATE && task < NA)) kgen(1);
    waitvm<0>(); bar();
    u4 uv[8];
#pragma unroll
    for (int i = 0; i < 8; ++i) uv[i] = *(const u4 LAS*)(sm + PA_TU + tid * 128 + i * 16);
#pragma unroll
    for (int ch = 0; ch < 8; ++ch) {
        u4 o;
#pragma unroll
        for (int j = 0; j < 4; ++j) o[j] = __builtin_amdgcn_perm(uv[6 - 2 * j][ch >> 1], uv[7 - 2 * j][ch >> 1], (ch & 1) ? 0x07060302u : 0x05040100u);
        *(u4 LAS*)(sm + PA_UT + ch * 4112 + (255 - tid) * 16) = o;
    }
    {
        typedef __attribute__((ext_vector_type(2))) unsigned short us2;
        u4 mm;
#pragma unroll
        for (int j = 0; j < 4; ++j) {
            us2 m = {0, 0};
#pragma unroll
            for (int i = 0; i < 8; ++i) m = __builtin_elementwise_max(m, __builtin_bit_cast(us2, uv[i][j] & 0x7f807f80u));
#pragma unroll
            for (int o = 1; o < 64; o <<= 1) m = __builtin_elementwise_max(m, __builtin_bit_cast(us2, __shfl_xor(__builtin_bit_cast(int, m), o)));
            mm[j] = __builtin_bit_cast(unsigned, m);
        }
        *(u4 LAS*)(sm + PA_SC + 64 + w * 16) = mm;
    }
    int toff[8];
#pragma unroll
    for (int j = 0; j < 8; ++j) {
        const int p = (int)bfr((float)(tid * 8 + j));
        int col, ar = (p & 255) >> 4;
        if (p < 256) col = p & 15; else if (p < 512) col = 16 + ((p & 15) >> 1); else if (p < 1024) col = 24 + ((p - 512) >> 8) * 4 + ((p & 15) >> 2);
        else if (p < 2048) col = 32 + ((p - 1024) >> 8) * 2 + ((p & 15) >> 3); else col = 40;
        toff[j] = (col * 16 + ar) * 4;
    }
    bar();
    if (tid < 32) *(u4 LAS*)(sm + PA_TU + ((tid >> 2) * 260 + 256 + (tid & 3)) * 16) = (u4){0u, 0u, 0u, 0u};
    {
#pragma nounroll
        for (int ch = 0; ch < 8; ++ch) {
            if (task < NA) slp(ch ? A_SLP : (task % 10) * (A_SLP + 70) / 10);
            const u16 LAS* mp = (const u16 LAS*)(sm + PA_SC + 64) + ch;
            const unsigned ue = min(max((unsigned)max(max(mp[0], mp[8]), max(mp[16], mp[24])) << 16, 0x07800000u), 0x7e800000u) - 0x07000000u;
            {
                const u4 x0 = *(const u4 LAS*)(sm + PA_UT + ch * 4112 + tid * 16), x1 = *(const u4 LAS*)(sm + PA_UT + ch * 4112 + tid * 16 + 16);
                float kv[8];
#pragma unroll
                for (int j = 0; j < 8; ++j) kv[j] = *(const float LAS*)(sm + PA_KT + ch * 3072 + toff[j]);
                const float us = __uint_as_float(0x7f000000u - ue);
                unsigned r[8];
#pragma unroll
                for (int j = 0; j < 8; ++j) { const unsigned x = j < 4 ? x0[j & 3] : x1[j & 3]; r[j] = pkh(__uint_as_float(x << 16) * us, __uint_as_float(x & 0xffff0000u) * us); }
#pragma unroll
                for (int cp = 0; cp < 8; ++cp) {
                    u4 o;
#pragma unroll
                    for (int j = 0; j < 4; ++j) o[j] = (cp & 1) ? __builtin_amdgcn_alignbit(r[(cp >> 1) + j + 1], r[(cp >> 1) + j], 16) : r[(cp >> 1) + j];
                    *(u4 LAS*)(sm + PA_TU + (cp * 260 + tid) * 16) = o;
                }
                *(u4 LAS*)(sm + PA_KH + (31 + (tid >> 2)) * 80 + (tid & 3) * 16) = (u4){pkh(kv[0], kv[1]), pkh(kv[2], kv[3]), pkh(kv[4], kv[5]), pkh(kv[6], kv[7])};
            }
            bar();
            {
                f16v y0, y1;
                const int m00 = 2047 - l31 + 8 * hi, cp = m00 & 7;
                const char LAS* ab = sm + PA_TU + (cp * 260 + (m00 >> 3)) * 16 - 63 * 64;
                const char LAS* bb = sm + PA_KH + l31 * 80 + hi * 16;
                const char LAS* a1w = ab - w * 64; const char LAS* b1w = bb - w * 80;
                const char LAS* a2w = ab + w * 64; const char LAS* b2w = bb + w * 80;
                u4 fr[4][3];
                auto rdg = [&](int g) __attribute__((always_inline)) {
                    const int i = (g & 15) >> 1, ks = g & 1, sl = g & 3;
                    if (g < 16) { fr[sl][0] = *(const u4 LAS*)(b1w + (31 - 4 * i) * 80 + ks * 32); fr[sl][1] = *(const u4 LAS*)(a1w + (63 - 4 * i) * 64 + ks * 32);
                                  fr[sl][2] = *(const u4 LAS*)(a1w + (31 - 4 * i) * 64 + ks * 32); }
                    else { fr[sl][0] = *(const u4 LAS*)(b2w + (32 + 4 * i) * 80 + ks * 32); fr[sl][2] = *(const u4 LAS*)(a2w + (32 + 4 * i) * 64 + ks * 32); }
                };
                rdg(0); rdg(1); rdg(2); SB();
#pragma unroll
                for (int g = 0; g < 32; ++g) {
                    const int sl = g & 3;
                    if (g + 3 < 32) rdg(g + 3);
                    SB();
                    if (g == 0) asm volatile("v_mfma_f32_32x32x16_f16 %0, %2, %4, 0\n\tv_mfma_f32_32x32x16_f16 %1, %3, %4, 0" : "=&v"(y0), "=&v"(y1) : "v"(fr[0][1]), "v"(fr[0][2]), "v"(fr[0][0]));
                    else { if (g < 16) asm volatile("v_mfma_f32_32x32x16_f16 %0, %1, %2, %0" : "+v"(y0) : "v"(fr[sl][1]), "v"(fr[sl][0]));
                           asm volatile("v_mfma_f32_32x32x16_f16 %0, %1, %2, %0" : "+v"(y1) : "v"(fr[sl][2]), "v"(fr[sl][0])); }
                    SB();
                }
                MFW32(y0, y1);
#pragma unroll
                for (int g = 0; g < 4; ++g) {
                    *(f4 LAS*)(sm + PA_RED + w * 9216 + l31 * 144 + (8 * g + 4 * hi) * 4) = (f4){y0[4 * g], y0[4 * g + 1], y0[4 * g + 2], y0[4 * g + 3]};
                    *(f4 LAS*)(sm + PA_RED + w * 9216 + (32 + l31) * 144 + (8 * g + 4 * hi) * 4) = (f4){y1[4 * g], y1[4 * g + 1], y1[4 * g + 2], y1[4 * g + 3]};
                }
            }
            bar();
            {
                f4 s0 = {0.f, 0.f, 0.f, 0.f}, s1 = s0;
#pragma unroll
                for (int p = 0; p < 4; ++p) { const char LAS* q_ = sm + PA_RED + p * 9216 + (tid >> 2) * 144 + (tid & 3) * 32; s0 += *(const f4 LAS*)q_; s1 += *(const f4 LAS*)(q_ + 16); }
                const u4 uu = *(const u4 LAS*)(sm + PA_UT + ch * 4112 + (255 - tid) * 16);
                const unsigned dw = (ch & 4) ? ((ch & 2) ? dsk4[3] : dsk4[2]) : ((ch & 2) ? dsk4[1] : dsk4[0]);
                const float dsk = __uint_as_float((ch & 1) ? dw & 0xffff0000u : dw << 16);
                const float ks_ = *(const float LAS*)(sm + PA_SC + ch * 4), us_ = __uint_as_float(ue);
                float yv[8];
#pragma unroll
                for (int j = 0; j < 8; ++j) yv[j] = bfr((j < 4 ? s0[j & 3] : s1[j & 3]) * us_ * ks_) + bfr(__uint_as_float((j & 1) ? uu[3 - (j >> 1)] << 16 : uu[3 - (j >> 1)] & 0xffff0000u) * dsk);
                sst128((u4){pk2(yv[0], yv[1]), pk2(yv[2], yv[3]), pk2(yv[4], yv[5]), pk2(yv[6], yv[7])}, rpa, tid * 16, (d0 + ch) * 4096);
            }
        }
    }
    set_flag(c, mksrd(a.HB, HB_SZ), O_FA + task * 64, tag);
}
#define P_ASLP 8
DI void pa_out(unsigned tag, int wave, int t) {
    CArgs& a = get_args();
    const SideCtx c = make_ctx(wave);
    const int lane = c.lane, tid = c.wave * 64 + lane;
    t = __builtin_amdgcn_readfirstlane(t); asm volatile("" : "+s"(t));
    const u4 rpa = mksrd(a.PA, 56 * 32768), ro = mksrd(a.out, 2048 * 1792);
    wait_flags(mksrd(a.HB, HB_SZ), lane < 8 ? O_FA + ((t & ~7) + lane) * 64 : OOB, tag);
    u4 v[8];
#pragma unroll
    for (int i = 0; i < 8; ++i) v[i] = sld128(rpa, (tid >> 5) * 4096 + (tid & 31) * 16, ((t >> 3) * 64 + i * 8) * 4096 + (t & 7) * 512);
    WAITV(0, "+v"(v[0]), "+v"(v[1]), "+v"(v[2]), "+v"(v[3]), "+v"(v[4]), "+v"(v[5]), "+v"(v[6]), "+v"(v[7]));
    bar();
#pragma unroll
    for (int i = 0; i < 8; ++i) *(u4 LAS*)(c.sm + (i * 8 + (tid >> 5)) * 528 + (tid & 31) * 16) = v[i];
    bar();
#pragma unroll
    for (int k = 0; k < 8; ++k) {
        const char LAS* p = c.sm + (tid & 7) * 8 * 528 + ((tid >> 3) + 32 * k) * 2;
        u4 o;
#pragma unroll
        for (int j = 0; j < 4; ++j) o[j] = (unsigned)*(const u16 LAS*)(p + (2 * j) * 528) | ((unsigned)*(const u16 LAS*)(p + (2 * j + 1) * 528) << 16);
        sst128(o, ro, (tid >> 3) * 1792 + (tid & 7) * 16, ((t & 7) * 256 + 32 * k) * 1792 + (t >> 3) * 128);
        asm volatile("s_sleep %0" :: "i"(P_ASLP));
    }
}
#define P_R 92
#define P_EPS 64
#define P_M 0ull

#define P_OWN 1
#define P_S1 1
#define P_S3 5
#define P_STG 8
#define P_XOR 4
#define L_SCR 28672
#define V_M P_M
#define V_A P_A
#define V_B P_B

DI void chain_role(unsigned tag, int vb, int wave, u8v pre) {
    CArgs& a = get_args();
    const int nx = vb & 3, id = vb >> 3, W = wave;
    const int lane = fresh_lane(), q = lane >> 4, n = lane & 15;
    const u4 r = mksrd(a.HB, HB_SZ);
    if (W == 0 && lane == 0) *(u4 LAS*)(smem_ + 24576) = (u4){0u, 0u, 0u, 0u};
    const unsigned want = tag + (id >= 16 && id < 20);
    u2 v = {pre[0], pre[1]}, w = {pre[4], pre[5]}; unsigned f = pre[2], sp = 0;
#pragma nounroll
    while (!__all((v[0] ^ v[1]) == tag && f - want <= 72u && (w[0] ^ w[1]) == tag)) {
        if (++sp > SPIN_MAX) __builtin_amdgcn_endpgm();
        asm volatile("s_sleep 2" ::: "memory");
        v = sld64(r, O_PAR + 4, 0); f = sld32(r, need_off(nx, id, W, lane), 0); w = sld64(r, O_PAR + 512 + min(lane, 35) * 32, 0); WAITV(0, "+v"(v), "+v"(f), "+v"(w));
    }
    unsigned win = w[0]; const unsigned t0 = __builtin_amdgcn_readfirstlane(v[0]);
    asm volatile("s_nop 1\n\tv_min_u32_dpp %0, %0, %0 row_ror:8 row_mask:0xf bank_mask:0xf\n\ts_nop 1\n\tv_min_u32_dpp %0, %0, %0 row_ror:4 row_mask:0xf bank_mask:0xf\n\ts_nop 1\n\t"
                 "v_min_u32_dpp %0, %0, %0 row_ror:2 row_mask:0xf bank_mask:0xf\n\ts_nop 1\n\tv_min_u32_dpp %0, %0, %0 row_ror:1 row_mask:0xf bank_mask:0xf\n\ts_nop 1" : "+v"(win));
    win = min(min((unsigned)__builtin_amdgcn_readlane(win, 0), (unsigned)__builtin_amdgcn_readlane(win, 16)), (unsigned)__builtin_amdgcn_readlane(win, 32)) & 3u;
    const int wA = win * 8192, wB = wA ^ 16384, wF = 524288 + wA;
    auto copy_off = [&](int g) { return (2 + (g >> 1)) * 32768 + ((g & 1) ? wA : wB); };
    if (id == 27) {
        unsigned m[7];
#pragma unroll
        for (int k = 0; k < 7; ++k) m[k] = san(*(const unsigned*)(a.mem0 + (lane >> 3) * 448 + (4 * k + W) * 16 + 4 * (lane & 3) + 2 * ((lane >> 2) & 1)));
        bar();
#pragma unroll
        for (int k = 0; k < 7; ++k) sst32(m[k], r, copy_off(nx) + lane * 4, 3 * 131072 + (4 * k + W) * 256);
        return;
    }
    Ctx c;
    c.r = r; c.rts = r; c.t0 = t0; c.per = V_A + V_B; c.R = P_R; c.tag = tag; c.lv0 = (unsigned)(V_M); c.lv1 = (unsigned)((V_M) >> 32); c.des = P_EPS; c.role = (id < 16 ? 0 : id < 20 ? 4 : 8) + W; c.tf0 = 0;
    c.idx = (lane & 1) ? 0xEEEE : 0x4444; c.vl16 = lane * 16;
    c.vg = c.vg3 = c.vtl = c.vtr = c.vsel = c.vraw = c.vfo = c.ve = c.vs = c.vrw = c.vrr = c.vr1 = c.vr2 = c.vdbg = 0;
    c.vtb = lane < 28 ? O_ANN + lane * 128 : OOB;
    c.vts = 32768 + W * 4096 + (STAMPS ? lane * 4 + (lane < 16 ? 0 : 32768) : 0);
    if (id < 16) {
        const int hb = nx * 16 + id, h = hb >> 2, part = hb & 3;
        c.rw = mksrd(a.SXM, 64 * SH); c.sw = part * 7; c.vsel = h * 4 * SH + NC * 1024 + ((n >> 2) & 1) * 14336 + q * 256 + (4 * (n >> 3) + (n & 3)) * 16;
        c.rx = mksrd(a.SXM, 64 * SH); c.sx = hb * SH + W * 512;
        c.rg = mksrd(a.X, 2048 * 896); c.sg = 0;
        c.vm = copy_off(nx) + (W >> 1) * 4096 + lane * 16;
        c.vtl = q * 1088 + n * 8 + (W >> 1) * 512;
        const int r_ = (lane >> 2) & 3, c_ = lane & 3;
        const int rec = (h >> 1) * 1024 + part * 256 + (h & 1) * 128;
        const int raw = (h >> 2) * 32768 + wF + (h & 3) * 2048 + part * 512;
        c.vfl = lane == 0 ? O_FH + hb * 128 + W * 64 : OOB; c.vpub = OOB;
        if (W < 2) {
            c.vg = (16 * W + (n & 7)) * 896 + (112 * part + 32 * (q >> 1) + 16 * (n >> 3) + 8 * (q & 1)) * 2; c.vg3 = (q >= 2 && n >= 8) ? OOB : c.vg + 128;
            c.vtr = c_ * 1088 + (8 * q + r_) * 8;
            c.vpub = (q >> 1) * 256 + n * 16 + (q & 1) * 8;
        } else if (W == 2) {
            c.vraw = lane < 16 ? (lane < 8 ? wA : wB) + rec + (lane & 7) * 16 : lane < 32 ? raw + (lane - 16) * 16 : id < 14 ? copy_off(nx) + id * 512 + (lane - 32) * 16 : OOB;
        } else {
            c.vg = copy_off(nx) + (7 * part + 2 * (q >> 1) + (n >> 3)) * 256 + (2 * (n & 7) + (q & 1)) * 16; c.vg3 = (q >= 2 && n >= 8) ? OOB : c.vg + 1024;
            c.vtr = c_ * 1088 + (16 * (q >> 1) + 4 * (r_ >> 1) + 2 * (q & 1) + (r_ & 1)) * 8;
            c.vpub = (n < 8 ? wA : wB) + rec + (n & 7) * 16 + q * 4;
            c.vraw = n < 8 ? raw + n * 32 + q * 8 : OOB;
        }
    } else if (id < 20) {
        const int h = nx * 4 + id - 16;
        c.rw = r; c.sw = 0; c.rx = r; c.sx = 0;
        c.rg = mksrd(a.GF, GSZ); c.sg = h * GH + W * 4096;
        c.vm = copy_off(nx) + W * 2048 + lane * 16;
        c.vtl = W * 1280 + 2 * q * 160 + n * 4;
        c.vtr = ((lane >> 2) & 7) * 160 + (8 * W + 2 * (lane & 3)) * 4;
        const int rec = 32768 + (h >> 1) * 1024 + W * 256 + (h & 1) * 128;
        const int raw = (h >> 2) * 32768 + wF + (h & 3) * 2048 + W * 512 + 256;
        c.vpub = (lane < 32 ? wA : wB) + rec + (lane & 31) * 4;
        c.vraw = lane < 32 ? raw + lane * 8 : OOB;
        c.vsel = lane < 16 ? (lane < 8 ? wA : wB) + rec + (lane & 7) * 16 : lane < 32 ? raw + (lane - 16) * 16 : OOB;
        c.vfl = O_FG + (h * 8 + (lane & 7)) * 64;
    } else {
        const int pid = nx * 7 + id - 20;
        c.rw = mksrd(a.UM, 28 * MH); c.sw = pid * MH + NC * 4096 + 4 * W * 14336;
        c.rg = mksrd(a.UF, GSZ); c.sg = pid * UH + W * 4096;
        c.rx = mksrd(a.UM, 28 * MH); c.sx = pid * MH + W * 1024;
        c.tf0 = V_A;
        c.vm = copy_off(nx) + W * 2048 + lane * 16;
        c.vpub = copy_off(W) + pid * 256 + lane * 4;
        c.vs = ((nx & 1) ? wA : wB) + W * 2048 + lane * 16; c.ve = c.vs + 32768;
        auto qb = [](int x) { return x * 256 + (x & 1) * 32 + (x >> 1) * 128; };
        c.vrw = 8192 + W * 1280 + qb(q) + n * 8;
        c.vrr = 8192 + qb(lane & 3) + ((lane >> 3) + 8 * ((lane >> 2) & 1)) * 8;
        c.vr1 = W * 32768 + wF + (n >> 3) * 2048 + (n & 7) * 32 + q * 8;
        c.vr2 = W * 32768 + wF + (n >> 3) * 2048 + q * 512 + 256 + (n & 7) * 32;
        c.vg = O_FG + (((4 * W + (lane & 3)) * 2 + 1) * 4 + (pid >> 3)) * 64;
        c.vfl = lane == 0 ? O_FP + (pid * 4 + W) * 64 : OOB;
        { const int o = P_OWN ? (W - nx - 1) & 3 : W; c.vtl = 8 + 16 * (o == 3 ? P_S3 : o * P_S1); }
        c.vtr = need_off(nx, id, W, lane) + 8192;
        c.vg3 = W < 3 ? c.vm + 1024 : c.vm - 6144;
        c.vsel = (pid == 0 && W == 0 && lane == 0) ? O_EPOCH : OOB;
        c.vfo = (lane == 0 && W == 0) ? O_ANN + pid * 128 : OOB;
    }
    PSTAMP(5);
    asm volatile("s_waitcnt lgkmcnt(0)" ::: "memory");
    chain_asm(c);
}

__global__ __launch_bounds__(256, 1) void fused_kernel(Args a_) {
    CArgs& a = get_args();
    const int wave = __builtin_amdgcn_readfirstlane(threadIdx.x >> 6);
#define PT(k)
    const u4 rhb = mksrd(a.HB, HB_SZ);
    unsigned tag;
    asm volatile("s_load_dword %0, %1, %2" : "=s"(tag) : "s"(a.HB), "s"(O_EPOCH) : "memory");
    const int xcc = (__builtin_amdgcn_s_getreg((31 << 11) | 20) & 7) ^ P_XOR;
    int vb = xcc + (blockIdx.x & ~7);
    const bool prober = (vb & 4) == 0 && (vb >= 216 || (vb >> 5) == 4) && wave == 3;
    const int po = O_PROBE + (vb & 3) * 32768 + ((vb >> 3) & 15) * 128;
    if (prober) { const int l = fresh_lane(); sst32(0u, rhb, l < 4 ? l * 8192 : OOB, po); }
    if (wave) {
        u64 pc; unsigned len;
        asm volatile("s_getpc_b64 %0\nfk_code_lo:\n\ts_max_i32 %1, fk_code_hi-fk_code_lo, 0" : "=s"(pc), "=s"(len) :: "scc");
        const u4 rc = mksrd((const void*)pc, len & ~15u);
        const int vo = (((blockIdx.x >> 3) * 3 + wave - 1) * 2) * 1024 + (threadIdx.x & 63) * 16;
#pragma unroll
        for (int i = 0; i < 2; ++i) dma_one(rc, vo + i * 1024, 0, (unsigned)(size_t)(char LAS*)smem_ + L_SCR + wave * 1024);
    }
    PT(4);
    unsigned best = 0xfffu;
#pragma nounroll
    for (int pass = 0;; ++pass) {
        gemm_first<0>(wave, (vb & 4) == 0, vb >> 7, 4 * (vb & 3) + ((vb >> 5) & 3), (vb >> 3) & 3);
        unsigned old = 0;
        PT(0);
        if (pass == 0) {
            asm volatile("s_waitcnt lgkmcnt(0)" : "+s"(tag) :: "memory");
            tag += 0x9E3779B4u; old = tag;
            if (wave == 0) asm volatile("s_nop 4\n\tbuffer_atomic_swap %0, %1, %2, %3 offen sc0 sc1" : "+v"(old) : "v"(threadIdx.x == 0 ? O_CNT : OOB), "s"(rhb), "s"((xcc * 32 + (int)(blockIdx.x >> 3)) * 128) : "memory");
        }
        if (pass == 0 && prober) {
            waitvm<18>();
#pragma nounroll
            for (int k = 0; k < 4; ++k) {
                u64 c0, c1; unsigned d;
                asm volatile("s_memtime %0\n\ts_waitcnt lgkmcnt(0)\n\ts_load_dword %2, %3, %4 glc\n\ts_waitcnt lgkmcnt(0)\n\ts_memtime %1\n\ts_waitcnt lgkmcnt(0)" : "=&s"(c0), "=&s"(c1), "=&s"(d) : "s"(a.HB), "s"(po + ((k + (vb >> 3)) & 3) * 8192) : "memory");
                best = min(best, ((unsigned)(c1 - c0) & ~3u) + (((k + (vb >> 3)) & 3) ^ (~vb & 1) * 2));
            }
        }
        PT(1);
        gemm_first<1>(wave, (vb & 4) == 0, vb >> 7, 4 * (vb & 3) + ((vb >> 5) & 3), (vb >> 3) & 3);
        PT(2);
        if (C_AT < 0) WAITV(11, "+v"(old)); else WAITV(10, "+v"(old));
        PT(3);
        if (pass) { bar(); break; }
        if (threadIdx.x == 0) {
            unsigned slot = (unsigned)xcc * 32u + (blockIdx.x >> 3);
            if (old == tag)
                for (int k = 0; k < 256; ++k) { slot = (slot + 1u) & 255u; if (__hip_atomic_exchange((unsigned*)(a.HB + O_CNT) + slot * 32u, tag, __ATOMIC_RELAXED, __HIP_MEMORY_SCOPE_AGENT) != tag) break; }
            *(unsigned LAS*)(smem_ + L_SCR) = (slot >> 5) + 8u * (slot & 31u);
        }
        bar();
        const int got = __builtin_amdgcn_readfirstlane(*(const unsigned LAS*)(smem_ + L_SCR));
        if (got == vb) break;
        best = 0xfffu;
        vb = got; waitvm<0>(); bar();
    }
    vb = __builtin_amdgcn_readfirstlane(vb); tag = __builtin_amdgcn_readfirstlane(tag);
    const int res = vb & 7, idx = vb >> 3;
    if (res < 4) {
        const int lane = fresh_lane(), nb = res * 32 + idx;
        if (vb == 249 && wave == 2) { const unsigned t0 = rt_now(); sst128((u4){0u, t0, t0 ^ tag, tag}, rhb, lane == 0 ? O_PAR : OOB, 0); }
        if ((idx >= 27 || (idx >> 2) == 4) && wave == 3) sst64((u2){best, best ^ tag}, rhb, lane == 0 ? O_PAR + 512 + ((idx >= 27 ? idx - 23 : idx - 16) * 4 + res) * 32 : OOB, 0);
        sst128((u4){FF, FF, FF, FF}, rhb, lane * 16, nb * 8192 + wave * 2048); sst128((u4){FF, FF, FF, FF}, rhb, lane * 16, nb * 8192 + wave * 2048 + 1024);
        if (nb == 0) sst128((u4){0u, 0u, 0u, 0u}, rhb, lane * 16, O_ANN + wave * 1024);
    }
    const u8v pre = (idx >> 4) ? gemm_role<1>(tag, vb, wave, res < 4, 4 * (res & 3) + ((idx >> 2) & 3), idx & 3) : gemm_role<0>(tag, vb, wave, res < 4, 4 * (res & 3) + ((idx >> 2) & 3), idx & 3);
    PSTAMP(3);
    if (res < 4 && idx < 28) chain_role(tag, vb, wave, pre);
    constexpr int NO = 8 + NA;
    if (res < 4 && idx < 32 - NA / 4) return;
    const int f = res < 4 ? -1 : ((idx & 2) ? 0 : 64) + (idx >> 2) * 8 + (idx & 1) * 4 + (res - 4);
    if (f < 56 - NA) { parta_task(tag, wave, f < 0 ? res * (NA / 4) + 31 - idx : NA + (f & 3) * (14 - NA / 4) + (f >> 2)); PSTAMP(5); }
    if (f < 0) return;
#pragma nounroll
    for (int it = 0; it < 6; ++it) {
        int t = 0;
        if (f >= 64) t = it == 0 ? 0x3000 + 20 + f - 64 : 0;
        else if (f >= 56 - NA) { const int i = f - (56 - NA) + NO * it; t = i < 36 ? 0x1000 + i : i < 36 + NO ? 0x4000 + i - 36 : 0; }
        else if (f < 16) t = it == 0 ? 0x1000 + 36 + f : it == 1 && f < 8 ? 0x4000 + 48 + f : 0;
        else { const int i = f - 16 + (40 - NA) * it; t = i < 48 - NO ? 0x4000 + NO + i : i < 96 - NO ? 0x2000 + i - (48 - NO) : i < 116 - NO ? 0x3000 + i - (96 - NO) : 0; if (it == 0) slp((f - 16) * P_STG); }
        const int k = t >> 12, j = t & 4095;
        if (k == 1) o_task(tag, wave, j); else if (k == 2) e_task(tag, wave, 52 + (j >> 2), j & 3); else if (k == 3) x_task(tag, wave, 52 + j / 7, j % 7); else if (k == 4) pa_out(tag, wave, j);
    }
    PSTAMP(6);
}
__device__ __attribute__((used, noinline)) void fk_tail() { asm volatile("fk_code_hi:"); }

extern "C" void kernel_launch(void* const* d_in, const int* in_sizes, int n_in, void* d_out, int out_size, void* d_ws, size_t ws_size, hipStream_t stream) {
    Args a;
    a.Au = (const u16*)d_in[0]; a.AlogA = (const u16*)d_in[1]; a.Aim = (const u16*)d_in[2]; a.ACr = (const u16*)d_in[3]; a.ACi = (const u16*)d_in[4];
    a.Alogdt = (const u16*)d_in[5]; a.ADsk = (const u16*)d_in[6]; a.X = (const u16*)d_in[7]; a.mem0 = (const u16*)d_in[8];
    a.Wq = (const u16*)d_in[9]; a.Wk = (const u16*)d_in[10]; a.Wv = (const u16*)d_in[11]; a.Wo = (const u16*)d_in[12];
    a.out = (u16*)d_out;
    char* base = (char*)(((size_t)d_ws + (2u << 20) - 1) & ~(size_t)((2u << 20) - 1));
    a.HB = base;
    size_t off = HB_SZ;
    auto alloc = [&](size_t n) { char* p = base + off; off += (n + 4095) & ~(size_t)4095; return p; };
    a.TS = (unsigned*)alloc(8u << 20);
    a.UM = (u16*)alloc(28 * MH); a.SXM = (float*)alloc(64 * SH); a.GF = (u16*)alloc(GSZ); a.UF = (u16*)alloc(GSZ); a.EX = (u16*)alloc(2u << 20); a.PA = (u16*)alloc(2u << 20); a.MZX = (float*)alloc(64 * 16 * 256);
    (void)hipFuncSetAttribute((const void*)fused_kernel, hipFuncAttributeMaxDynamicSharedMemorySize, 160 * 1024);
    fused_kernel<<<256, 256, 160 * 1024, stream>>>(a);
}
```

```cpp
#include <hip/hip_runtime.h>
#include <stdint.h>
typedef unsigned short u16;
typedef unsigned long long u64;
typedef __attribute__((ext_vector_type(2))) __bf16 bf2;
typedef __attribute__((ext_vector_type(2))) float f2;
typedef __attribute__((ext_vector_type(4))) float f4;
typedef __attribute__((ext_vector_type(16))) float f16v;
typedef __attribute__((ext_vector_type(2))) unsigned u2;
typedef __attribute__((ext_vector_type(4))) unsigned u4;
typedef __attribute__((ext_vector_type(16))) unsigned u16v;
typedef __attribute__((ext_vector_type(8))) unsigned u8v;
typedef __attribute__((ext_vector_type(4))) short s4;
#define LAS __attribute__((address_space(3)))
#define DI __device__ __forceinline__
#define SB() __builtin_amdgcn_sched_barrier(0)
#define PIN(x) asm volatile("" : "+v"(x))
#define WAITV(n, ...) asm volatile("s_waitcnt vmcnt(" #n ")" : __VA_ARGS__ :: "memory")
#define LOG2E 1.4426950408889634f
#define CSCALE (LOG2E * 0.0625f)
#define FF 0xFFFFFFFFu
#define OOB 0x7f000000
#define SPIN_MAX (1u << 18)

#define HB_SZ (2 << 20)
#define O_CTL (1 << 20)
#define O_ANN O_CTL
#define O_PAR (O_CTL + 4096)
#define O_FW (O_CTL + 8192)
#define O_FG (O_CTL + 24576)
#define O_FE (O_CTL + 32768)
#define O_FH (O_CTL + 49152)
#define O_FP (O_CTL + 57344)
#define O_FA (O_CTL + 65536)
#define O_CNT (O_CTL + 73728)
#define O_EPOCH (O_CNT + 32768)
#define O_PROBE (O_CTL + 131072)

#define GH 1835008
#define UH 1048576
#define NC 8
#define MH (NC * 4096 + 16 * 14336)
#define SH (NC * 1024 + 8 * 14336)
#define GSZ (34 << 20)
struct Args {
    const u16 *Au, *AlogA, *Aim, *ACr, *ACi, *Alogdt, *ADsk, *X, *mem0, *Wq, *Wk, *Wv, *Wo;
    u16* out;
    char* HB;
    u16 *GF, *UF, *EX, *UM, *PA; float *MZX, *SXM;
    unsigned* TS;
};
typedef const Args __attribute__((address_space(4))) CArgs;
DI CArgs& get_args() { CArgs* p = (CArgs*)__builtin_amdgcn_kernarg_segment_ptr(); asm volatile("" : "+s"(p)); return *p; }
extern __shared__ __attribute__((aligned(16))) char smem_[];

DI unsigned pk2(float a, float b) { f2 v = {a, b}; return __builtin_bit_cast(unsigned, __builtin_convertvector(v, bf2)); }
DI float bfr(float a) { return __uint_as_float(pk2(a, a) & 0xffff0000u); }
DI float bf2f(u16 x) { return __uint_as_float((unsigned)x << 16); }
DI unsigned san(unsigned x) { return x < 0xFFFFFFFEu ? x : 0xFFFFFFFEu; }
DI void bar() { asm volatile("s_waitcnt lgkmcnt(0)\n\ts_barrier" ::: "memory"); SB(); }
DI float x32max(float a) { auto r = __builtin_amdgcn_permlane32_swap(__float_as_uint(a), __float_as_uint(a), false, false); return fmaxf(__uint_as_float(r[0]), __uint_as_float(r[1])); }
DI float x32add(float a) { auto r = __builtin_amdgcn_permlane32_swap(__float_as_uint(a), __float_as_uint(a), false, false); return __uint_as_float(r[0]) + __uint_as_float(r[1]); }
DI u2 trread(const char LAS* p) { return __builtin_bit_cast(u2, __builtin_amdgcn_ds_read_tr16_b64_v4i16((LAS s4*)p)); }
DI u4 mksrd(const void* p, unsigned bytes) { u64 x = (u64)p; return (u4){(unsigned)x, (unsigned)(x >> 32) & 0xffffu, bytes, 0x00020000u}; }
DI unsigned now32() { u64 t; asm volatile("s_memtime %0\n\ts_waitcnt lgkmcnt(0)" : "=s"(t) :: "memory"); return (unsigned)t; }
DI unsigned rt_now() { u64 t; asm volatile("s_memrealtime %0\n\ts_waitcnt lgkmcnt(0)" : "=s"(t) :: "memory"); return (unsigned)t; }
DI u4 ld128(u4 srd, int voff, int soff) { u4 v; asm volatile("buffer_load_dwordx4 %0, %1, %2, %3 offen sc1" : "=v"(v) : "v"(voff), "s"(srd), "s"(soff) : "memory"); return v; }
DI void st128(u4 v, u4 srd, int voff, int soff) { asm volatile("buffer_store_dwordx4 %0, %1, %2, %3 offen sc1\n\ts_nop 1" :: "v"(v), "v"(voff), "s"(srd), "s"(soff) : "memory"); }
DI void st32(unsigned v, u4 srd, int voff, int soff) { asm volatile("buffer_store_dword %0, %1, %2, %3 offen sc1" :: "v"(v), "v"(voff), "s"(srd), "s"(soff) : "memory"); }
DI u4 sld128(u4 srd, int voff, int soff) { u4 v; asm volatile("s_nop 4\n\tbuffer_load_dwordx4 %0, %1, %2, %3 offen sc1" : "=v"(v) : "v"(voff), "s"(srd), "s"(soff) : "memory"); return v; }
DI u2 sld64(u4 srd, int voff, int soff) { u2 v; asm volatile("s_nop 4\n\tbuffer_load_dwordx2 %0, %1, %2, %3 offen sc1" : "=v"(v) : "v"(voff), "s"(srd), "s"(soff) : "memory"); return v; }
DI unsigned sld32(u4 srd, int voff, int soff) { unsigned v; asm volatile("s_nop 4\n\tbuffer_load_dword %0, %1, %2, %3 offen sc1" : "=v"(v) : "v"(voff), "s"(srd), "s"(soff) : "memory"); return v; }
DI void sst128(u4 v, u4 srd, int voff, int soff) { asm volatile("s_nop 4\n\tbuffer_store_dwordx4 %0, %1, %2, %3 offen sc1\n\ts_nop 1" :: "v"(v), "v"(voff), "s"(srd), "s"(soff) : "memory"); }
DI void sst64(u2 v, u4 srd, int voff, int soff) { asm volatile("s_nop 4\n\tbuffer_store_dwordx2 %0, %1, %2, %3 offen sc1" :: "v"(v), "v"(voff), "s"(srd), "s"(soff) : "memory"); }
DI void sst32(unsigned v, u4 srd, int voff, int soff) { asm volatile("s_nop 4\n\tbuffer_store_dword %0, %1, %2, %3 offen sc1" :: "v"(v), "v"(voff), "s"(srd), "s"(soff) : "memory"); }
template <int N> DI void waitvm() { asm volatile("s_waitcnt vmcnt(%0)" :: "i"(N) : "memory"); }
DI int fresh_lane() { int l; asm volatile("v_mbcnt_lo_u32_b32 %0, -1, 0\n\tv_mbcnt_hi_u32_b32 %0, -1, %0" : "=v"(l)); __builtin_assume(l >= 0 && l < 64); return l; }
#define W_SLP 8
template <int SLP = W_SLP>
DI void wait_flags(u4 rhb, int voff_or_oob, unsigned want) {
    unsigned sp = 0;
#pragma nounroll
    for (;;) {
        unsigned v = sld32(rhb, voff_or_oob, 0); WAITV(0, "+v"(v));
        if (__all((v - want <= 72u) | (voff_or_oob >= OOB))) break;
        unsigned k = __builtin_amdgcn_readfirstlane(want - v);
        k = k > 64u ? (SLP < W_SLP ? 1u : 2u) : k;
        asm volatile("s_sleep %0" :: "i"(SLP) : "memory");
#pragma nounroll
        for (unsigned i = 1; i < k; ++i) asm volatile("s_sleep 30" ::: "memory");
        if (++sp > SPIN_MAX / 16) __builtin_amdgcn_endpgm();
    }
}
DI int need_off(int nx, int id, int W, int lane) {
    return id < 16 ? O_FW + (W >> 1) * 8192 + ((nx * 4 + (id >> 2)) * 4 + (lane & 3)) * 64 : id < 20 ? O_FG + ((nx * 4 + id - 16) * 8 + (lane & 3)) * 64
         : id < 27 ? O_FW + ((16 + 4 * W + (lane & 3)) * 4 + ((nx * 7 + id - 20) >> 3)) * 64 : O_FW + 8192 + (W & 1) * 4096 + lane * 64;
}
#define PSTAMP(k) do { } while (0)
#define RFL(x) (unsigned)__builtin_amdgcn_readfirstlane(x)
#define STAMPS 0
struct Ctx { u4 r, rw, rg, rx, rts; unsigned lv0, lv1, t0, tf0, per, R, tag, sw, sg, sx, des, role; unsigned idx, vl16, vm, vg, vg3, vtl, vtr, vsel, vpub, vraw, vfl, vfo, ve, vs, vrw, vrr, vr1, vr2, vts, vdbg, vtb; };
DI void chain_asm(const Ctx& c) {
    const u16v s0 = {RFL(c.r[0]), RFL(c.r[1]), RFL(c.r[2]), RFL(c.r[3]), RFL(c.rw[0]), RFL(c.rw[1]), RFL(c.rw[2]), RFL(c.rw[3]), RFL(c.rg[0]), RFL(c.rg[1]), RFL(c.rg[2]), RFL(c.rg[3]), RFL(c.rx[0]), RFL(c.rx[1]), RFL(c.rx[2]), RFL(c.rx[3])}, s1 = {RFL(c.rts[0]), RFL(c.rts[1]), RFL(c.rts[2]), RFL(c.rts[3]), RFL(c.lv0), RFL(c.lv1), RFL(c.t0), RFL(c.tf0), RFL(c.per), RFL(c.R), RFL(c.tag), RFL(c.sw), RFL(c.sg), RFL(c.sx), RFL(c.des), RFL(c.role)}, v0 = {c.idx, c.vl16, c.vm, c.vg, c.vg3, c.vtl, c.vtr, c.vsel, c.vpub, c.vraw, c.vfl, c.vfo, c.ve, c.vs, c.vrw, c.vrr}, v1 = {c.vr1, c.vr2, c.vts, c.vdbg, c.vtb, 0u, 0u, 0u, 0u, 0u, 0u, 0u, 0u, 0u, 0u, 0u};
    asm volatile(
"s_nop 4\ns_cmp_eq_u32 s39, 0\ns_cbranch_scc1 fk_jh0\ns_cmp_eq_u32 s39, 1\n"
"s_cbranch_scc1 fk_jh1\ns_cmp_eq_u32 s39, 2\ns_cbranch_scc1 fk_jh2\ns_cmp_eq_u32 s39, 3\n"
"s_cbranch_scc1 fk_jh3\ns_cmp_eq_u32 s39, 4\ns_cbranch_scc1 fk_jx0\ns_cmp_eq_u32 s39, 5\n"
"s_cbranch_scc1 fk_jx1\ns_cmp_eq_u32 s39, 6\ns_cbranch_scc1 fk_jx2\ns_cmp_eq_u32 s39, 7\n"
"s_cbranch_scc1 fk_jx3\ns_cmp_ge_u32 s39, 8\ns_cbranch_scc1 fk_jp0\ns_endpgm\n"
"fk_jh0:\ns_branch fk_h0\nfk_jh1:\ns_branch fk_h1\n"
"fk_jh2:\ns_branch fk_h2\nfk_jh3:\ns_branch fk_h3\n"
"fk_jx0:\ns_branch fk_x0\nfk_jx1:\ns_branch fk_x1\n"
"fk_jx2:\ns_branch fk_x2\nfk_jx3:\ns_branch fk_x3\n"
"fk_jp0:\ns_branch fk_p0\nfk_h0:\ns_mov_b32 s40, 0\n"
"s_mov_b32 s41, 0\ns_mov_b32 s42, 262144\ns_mov_b32 s43, 393216\ns_mov_b32 s44, s30\n"
"s_mov_b32 s45, 0\ns_bcnt1_i32_b64 s46, s[28:29]\ns_mov_b32 s92, 99\ns_mov_b32 s49, 0\n"
"s_mov_b32 s53, 0\ns_mov_b32 s50, s36\ns_mov_b32 s51, s37\nv_mov_b32 v131, v13\n"
"v_mov_b32 v132, v14\nv_mov_b32 v134, 24576\nv_mov_b32 v115, 0\nv_mov_b32 v120, -1\n"
"v_mov_b32 v121, -1\nv_mov_b32 v122, -1\nv_mov_b32 v123, -1\ns_add_u32 s47, s35, 0\n"
"s_and_b32 s52, s47, 1\ns_mul_i32 s52, s52, -14208\ns_mul_i32 s48, s47, 14336\ns_add_u32 s48, s48, s52\n"
"s_lshr_b32 s52, s47, 3\ns_mul_i32 s52, s52, 8192\ns_add_u32 s48, s48, s52\ns_add_u32 s47, s48, 0\n"
"buffer_load_dwordx4 a[0:3], v15, s[12:15], s47 offen sc1\nbuffer_load_dwordx4 a[4:7], v15, s[12:15], s47 offen offset:1024 sc1\ns_add_u32 s47, s48, 2048\nbuffer_load_dwordx4 a[8:11], v15, s[12:15], s47 offen sc1\n"
"buffer_load_dwordx4 a[12:15], v15, s[12:15], s47 offen offset:1024 sc1\ns_add_u32 s47, s48, 4096\nbuffer_load_dwordx4 a[16:19], v15, s[12:15], s47 offen sc1\nbuffer_load_dwordx4 a[20:23], v15, s[12:15], s47 offen offset:1024 sc1\n"
"s_add_u32 s47, s48, 6144\nbuffer_load_dwordx4 a[24:27], v15, s[12:15], s47 offen sc1\nbuffer_load_dwordx4 a[28:31], v15, s[12:15], s47 offen offset:1024 sc1\ns_add_u32 s47, s35, 1\n"
"s_and_b32 s52, s47, 1\ns_mul_i32 s52, s52, -14208\ns_mul_i32 s48, s47, 14336\ns_add_u32 s48, s48, s52\n"
"s_lshr_b32 s52, s47, 3\ns_mul_i32 s52, s52, 8192\ns_add_u32 s48, s48, s52\ns_add_u32 s47, s48, 0\n"
"buffer_load_dwordx4 a[32:35], v15, s[12:15], s47 offen sc1\nbuffer_load_dwordx4 a[36:39], v15, s[12:15], s47 offen offset:1024 sc1\ns_add_u32 s47, s48, 2048\nbuffer_load_dwordx4 a[40:43], v15, s[12:15], s47 offen sc1\n"
"buffer_load_dwordx4 a[44:47], v15, s[12:15], s47 offen offset:1024 sc1\ns_add_u32 s47, s48, 4096\nbuffer_load_dwordx4 a[48:51], v15, s[12:15], s47 offen sc1\nbuffer_load_dwordx4 a[52:55], v15, s[12:15], s47 offen offset:1024 sc1\n"
"s_add_u32 s47, s48, 6144\nbuffer_load_dwordx4 a[56:59], v15, s[12:15], s47 offen sc1\nbuffer_load_dwordx4 a[60:63], v15, s[12:15], s47 offen offset:1024 sc1\ns_add_u32 s47, s35, 2\n"
"s_and_b32 s52, s47, 1\ns_mul_i32 s52, s52, -14208\ns_mul_i32 s48, s47, 14336\ns_add_u32 s48, s48, s52\n"
"s_lshr_b32 s52, s47, 3\ns_mul_i32 s52, s52, 8192\ns_add_u32 s48, s48, s52\ns_add_u32 s47, s48, 0\n"
"buffer_load_dwordx4 a[64:67], v15, s[12:15], s47 offen sc1\nbuffer_load_dwordx4 a[68:71], v15, s[12:15], s47 offen offset:1024 sc1\ns_add_u32 s47, s48, 2048\nbuffer_load_dwordx4 a[72:75], v15, s[12:15], s47 offen sc1\n"
"buffer_load_dwordx4 a[76:79], v15, s[12:15], s47 offen offset:1024 sc1\ns_add_u32 s47, s48, 4096\nbuffer_load_dwordx4 a[80:83], v15, s[12:15], s47 offen sc1\nbuffer_load_dwordx4 a[84:87], v15, s[12:15], s47 offen offset:1024 sc1\n"
"s_add_u32 s47, s48, 6144\nbuffer_load_dwordx4 a[88:91], v15, s[12:15], s47 offen sc1\nbuffer_load_dwordx4 a[92:95], v15, s[12:15], s47 offen offset:1024 sc1\ns_add_u32 s47, s35, 3\n"
"s_and_b32 s52, s47, 1\ns_mul_i32 s52, s52, -14208\ns_mul_i32 s48, s47, 14336\ns_add_u32 s48, s48, s52\n"
"s_lshr_b32 s52, s47, 3\ns_mul_i32 s52, s52, 8192\ns_add_u32 s48, s48, s52\ns_add_u32 s47, s48, 0\n"
"buffer_load_dwordx4 a[96:99], v15, s[12:15], s47 offen sc1\nbuffer_load_dwordx4 a[100:103], v15, s[12:15], s47 offen offset:1024 sc1\ns_add_u32 s47, s48, 2048\nbuffer_load_dwordx4 a[104:107], v15, s[12:15], s47 offen sc1\n"
"buffer_load_dwordx4 a[108:111], v15, s[12:15], s47 offen offset:1024 sc1\ns_add_u32 s47, s48, 4096\nbuffer_load_dwordx4 a[112:115], v15, s[12:15], s47 offen sc1\nbuffer_load_dwordx4 a[116:119], v15, s[12:15], s47 offen offset:1024 sc1\n"
"s_add_u32 s47, s48, 6144\nbuffer_load_dwordx4 a[120:123], v15, s[12:15], s47 offen sc1\nbuffer_load_dwordx4 a[124:127], v15, s[12:15], s47 offen offset:1024 sc1\ns_add_u32 s47, s50, 7168\n"
"buffer_load_dwordx4 v[72:75], v11, s[16:19], s50 offen\nbuffer_load_dwordx4 v[76:79], v12, s[16:19], s50 offen\nbuffer_load_dwordx4 v[80:83], v11, s[16:19], s47 offen\nbuffer_load_dwordx4 v[84:87], v12, s[16:19], s47 offen\n"
"s_add_u32 s50, s50, 28672\ns_waitcnt vmcnt(0)\ns_barrier\ns_memrealtime s[54:55]\n"
"s_waitcnt lgkmcnt(0)\ns_add_u32 s44, s54, 16\ns_mov_b32 s45, 8\n.p2align 6\n"
"top_h0:\nv_mov_b32 v56, 0\nv_mov_b32 v57, 0\nv_mov_b32 v58, 0\n"
"v_mov_b32 v59, 0\nv_mov_b32 v60, 0\nv_mov_b32 v61, 0\nv_mov_b32 v62, 0\n"
"v_mov_b32 v63, 0\nv_mov_b32 v64, 0\nv_mov_b32 v65, 0\nv_mov_b32 v66, 0\n"
"v_mov_b32 v67, 0\nv_mov_b32 v68, 0\nv_mov_b32 v69, 0\nv_mov_b32 v70, 0\n"
"v_mov_b32 v71, 0\nv_mov_b32 v128, 0\ns_mul_i32 s48, s45, 6\ns_and_b32 s48, s48, -4\n"
"s_getpc_b64 s[60:61]\nspc_h0:\ns_sub_u32 s48, sgo_h0-spc_h0, s48\ns_add_u32 s60, s60, s48\n"
"s_addc_u32 s61, s61, 0\nsf_h0:\ns_memrealtime s[54:55]\ns_nop 2\n"
"s_memrealtime s[56:57]\ns_nop 2\ns_memrealtime s[58:59]\nsg_h0:\n"
"s_waitcnt lgkmcnt(4)\ns_sub_u32 s47, s54, s44\ns_add_u32 s48, s47, 12\ns_cmp_ge_u32 s48, 12\n"
"s_cbranch_scc1 so_h0\ns_memrealtime s[54:55]\ns_waitcnt lgkmcnt(4)\ns_cmp_ge_u32 s56, s44\n"
"s_cbranch_scc1 sx_h0\ns_memrealtime s[56:57]\ns_waitcnt lgkmcnt(4)\ns_cmp_ge_u32 s58, s44\n"
"s_cbranch_scc1 sx_h0\ns_memrealtime s[58:59]\ns_waitcnt lgkmcnt(4)\ns_cmp_ge_u32 s54, s44\n"
"s_cbranch_scc1 sx_h0\ns_memrealtime s[54:55]\ns_waitcnt lgkmcnt(4)\ns_cmp_ge_u32 s56, s44\n"
"s_cbranch_scc1 sx_h0\ns_memrealtime s[56:57]\ns_waitcnt lgkmcnt(4)\ns_cmp_ge_u32 s58, s44\n"
"s_cbranch_scc1 sx_h0\ns_memrealtime s[58:59]\ns_waitcnt lgkmcnt(4)\ns_cmp_ge_u32 s54, s44\n"
"s_cbranch_scc1 sx_h0\ns_memrealtime s[54:55]\ns_waitcnt lgkmcnt(4)\ns_cmp_ge_u32 s56, s44\n"
"s_cbranch_scc1 sx_h0\ns_memrealtime s[56:57]\ns_waitcnt lgkmcnt(4)\ns_cmp_ge_u32 s58, s44\n"
"s_cbranch_scc1 sx_h0\ns_memrealtime s[58:59]\ns_waitcnt lgkmcnt(4)\ns_cmp_ge_u32 s54, s44\n"
"s_cbranch_scc1 sx_h0\ns_memrealtime s[54:55]\ns_waitcnt lgkmcnt(4)\ns_cmp_ge_u32 s56, s44\n"
"s_cbranch_scc1 sx_h0\ns_memrealtime s[56:57]\ns_waitcnt lgkmcnt(4)\ns_cmp_ge_u32 s58, s44\n"
"s_cbranch_scc1 sx_h0\ns_memrealtime s[58:59]\ns_waitcnt lgkmcnt(4)\ns_cmp_ge_u32 s54, s44\n"
"s_cbranch_scc1 sx_h0\ns_memrealtime s[54:55]\ns_waitcnt lgkmcnt(4)\ns_cmp_ge_u32 s56, s44\n"
"s_cbranch_scc1 sx_h0\ns_memrealtime s[56:57]\ns_waitcnt lgkmcnt(4)\ns_cmp_ge_u32 s58, s44\n"
"s_cbranch_scc1 sx_h0\ns_memrealtime s[58:59]\ns_waitcnt lgkmcnt(4)\ns_cmp_ge_u32 s54, s44\n"
"s_cbranch_scc1 sx_h0\ns_memrealtime s[54:55]\ns_waitcnt lgkmcnt(4)\ns_cmp_ge_u32 s56, s44\n"
"s_cbranch_scc1 sx_h0\ns_memrealtime s[56:57]\ns_branch sx_h0\nso_h0:\n"
"s_waitcnt lgkmcnt(0)\ns_cmp_lt_i32 s47, 0\ns_cbranch_scc1 sf_h0\ns_branch sgo_h0\n"
"sx_h0:\ns_setpc_b64 s[60:61]\ns_nop 0\ns_nop 0\n"
"s_nop 0\ns_nop 0\ns_nop 0\ns_nop 0\n"
"s_nop 0\ns_nop 0\ns_nop 0\ns_nop 0\n"
"s_nop 0\ns_nop 0\ns_nop 0\ns_nop 0\n"
"s_nop 0\ns_nop 0\ns_nop 0\ns_nop 0\n"
"s_nop 0\ns_nop 0\ns_nop 0\ns_nop 0\n"
"s_nop 0\ns_nop 0\nsgo_h0:\nbuffer_load_dwordx4 v[40:43], v10, s[8:11], s43 offen offset:0 sc1\n"
"buffer_load_dwordx4 v[44:47], v10, s[8:11], s43 offen offset:1024 sc1\nbuffer_load_dwordx4 v[48:51], v10, s[8:11], s43 offen offset:2048 sc1\ns_nop 15\ns_nop 7\n"
"buffer_load_dwordx4 v[52:55], v10, s[8:11], s43 offen offset:3072 sc1\nlanded_h0:\ns_waitcnt vmcnt(3)\nv_smfmac_f32_16x16x64_bf16 v[56:59], v[40:43], a[0:7], v8\n"
"v_max3_u32 v128, v128, v40, v41\nv_smfmac_f32_16x16x64_bf16 v[60:63], v[40:43], a[32:39], v8\nv_max3_u32 v128, v128, v42, v43\nv_smfmac_f32_16x16x64_bf16 v[64:67], v[40:43], a[64:71], v8\n"
"v_smfmac_f32_16x16x64_bf16 v[68:71], v[40:43], a[96:103], v8\ns_waitcnt vmcnt(2)\nv_smfmac_f32_16x16x64_bf16 v[56:59], v[44:47], a[8:15], v8\nv_max3_u32 v128, v128, v44, v45\n"
"v_smfmac_f32_16x16x64_bf16 v[60:63], v[44:47], a[40:47], v8\nv_max3_u32 v128, v128, v46, v47\nv_smfmac_f32_16x16x64_bf16 v[64:67], v[44:47], a[72:79], v8\nv_smfmac_f32_16x16x64_bf16 v[68:71], v[44:47], a[104:111], v8\n"
"s_waitcnt vmcnt(1)\nv_smfmac_f32_16x16x64_bf16 v[56:59], v[48:51], a[16:23], v8\nv_max3_u32 v128, v128, v48, v49\nv_smfmac_f32_16x16x64_bf16 v[60:63], v[48:51], a[48:55], v8\n"
"v_max3_u32 v128, v128, v50, v51\nv_smfmac_f32_16x16x64_bf16 v[64:67], v[48:51], a[80:87], v8\nv_smfmac_f32_16x16x64_bf16 v[68:71], v[48:51], a[112:119], v8\ns_waitcnt vmcnt(0)\n"
"v_smfmac_f32_16x16x64_bf16 v[56:59], v[52:55], a[24:31], v8\nv_max3_u32 v128, v128, v52, v53\nv_smfmac_f32_16x16x64_bf16 v[60:63], v[52:55], a[56:63], v8\nv_max3_u32 v128, v128, v54, v55\n"
"v_smfmac_f32_16x16x64_bf16 v[64:67], v[52:55], a[88:95], v8\nv_cmp_eq_u32 vcc, -1, v128\nv_smfmac_f32_16x16x64_bf16 v[68:71], v[52:55], a[120:127], v8\nv_add_f32 v129, v56, v57\n"
"v_add_f32 v130, v58, v59\nv_cvt_pk_bf16_f32 v112, v129, v130\nv_add_f32 v129, v60, v61\nv_add_f32 v130, v62, v63\n"
"v_cvt_pk_bf16_f32 v113, v129, v130\nv_add_f32 v129, v64, v65\nv_add_f32 v130, v66, v67\nv_cvt_pk_bf16_f32 v114, v129, v130\n"
"v_add_f32 v129, v68, v69\nv_add_f32 v130, v70, v71\nv_cvt_pk_bf16_f32 v115, v129, v130\nds_write_b64 v131, v[112:113] offset:0\n"
"ds_write_b64 v131, v[114:115] offset:128\ns_cbranch_vccnz retry_h0\ns_waitcnt lgkmcnt(0)\ns_barrier\n"
"s_nop 9\nds_read_b64_tr_b16 v[88:89], v132 offset:0\nds_read_b64_tr_b16 v[90:91], v132 offset:32\nds_read_b64_tr_b16 v[92:93], v132 offset:256\n"
"ds_read_b64_tr_b16 v[94:95], v132 offset:288\nds_read_b64_tr_b16 v[96:97], v132 offset:512\nds_read_b64_tr_b16 v[98:99], v132 offset:544\nds_read_b64_tr_b16 v[100:101], v132 offset:768\n"
"ds_read_b64_tr_b16 v[102:103], v132 offset:800\ns_waitcnt lgkmcnt(6)\nv_mfma_f32_16x16x32_bf16 v[104:107], v[88:91], v[72:75], 0\nv_mfma_f32_16x16x32_bf16 v[108:111], v[88:91], v[80:83], 0\n"
"s_waitcnt lgkmcnt(4)\nv_mfma_f32_16x16x32_bf16 v[104:107], v[92:95], v[76:79], v[104:107]\nv_mfma_f32_16x16x32_bf16 v[108:111], v[92:95], v[84:87], v[108:111]\ns_waitcnt lgkmcnt(2)\n"
"v_mfma_f32_16x16x32_bf16 v[104:107], v[96:99], v[72:75], v[104:107]\nv_mfma_f32_16x16x32_bf16 v[108:111], v[96:99], v[80:83], v[108:111]\ns_waitcnt lgkmcnt(0)\nv_mfma_f32_16x16x32_bf16 v[104:107], v[100:103], v[76:79], v[104:107]\n"
"v_mfma_f32_16x16x32_bf16 v[108:111], v[100:103], v[84:87], v[108:111]\ns_nop 7\nv_add_f32_dpp v124, v106, v104 row_ror:8 row_mask:0xf bank_mask:0x3\nv_add_f32_dpp v124, v108, v110 row_ror:8 row_mask:0xf bank_mask:0xc\n"
"v_add_f32_dpp v125, v107, v105 row_ror:8 row_mask:0xf bank_mask:0x3\nv_add_f32_dpp v125, v109, v111 row_ror:8 row_mask:0xf bank_mask:0xc\ns_add_u32 s47, s50, 7168\nbuffer_load_dwordx4 v[72:75], v11, s[16:19], s50 offen\n"
"buffer_load_dwordx4 v[76:79], v12, s[16:19], s50 offen\nbuffer_load_dwordx4 v[80:83], v11, s[16:19], s47 offen\nbuffer_load_dwordx4 v[84:87], v12, s[16:19], s47 offen\ns_add_u32 s50, s50, 28672\n"
"buffer_store_dwordx2 v[124:125], v16, s[20:23], s51 offen sc1\ns_add_u32 s51, s51, 1024\ns_cmp_lg_u32 s40, 40\ns_cbranch_scc1 np_h0\n"
"s_add_u32 s47, s51, 20480\nbuffer_store_dwordx2 v[120:121], v16, s[20:23], s47 offen offset:0 sc1\nbuffer_store_dwordx2 v[120:121], v16, s[20:23], s47 offen offset:1024 sc1\nbuffer_store_dwordx2 v[120:121], v16, s[20:23], s47 offen offset:2048 sc1\n"
"np_h0:\ns_add_u32 s47, s40, s34\nv_mov_b32 v135, s47\nbuffer_store_dword v135, v18, s[8:11], 0 offen sc1\n"
"ds_read_b32 v133, v134\nv_xor_b32 v131, 8192, v131\nv_xor_b32 v132, 8192, v132\ns_waitcnt lgkmcnt(0)\n"
"v_readfirstlane_b32 s47, v133\ns_sub_u32 s47, s47, s40\ns_cmp_eq_u32 s47, 1\ns_cbranch_scc1 adopt_h0\n"
"back_h0:\ns_mul_i32 s48, s46, 8\ns_add_u32 s45, s45, s48\ns_add_u32 s45, s45, s32\n"
"s_lshr_b32 s48, s45, 4\ns_lshl_b32 s48, s48, 2\ns_add_u32 s44, s44, s48\ns_and_b32 s45, s45, 15\n"
"s_add_u32 s52, s40, 1\ns_bitcmp1_b64 s[28:29], s52\ns_subb_u32 s46, s46, 0\ns_add_u32 s40, s40, 1\n"
"s_mov_b32 s43, s41\ns_add_u32 s41, s41, 131072\ns_and_b32 s41, s41, 524287\ns_add_u32 s92, s92, 1\n"
"s_xor_b32 s42, s41, 262144\ns_waitcnt lgkmcnt(0)\ns_cmp_lt_u32 s40, 64\ns_cbranch_scc1 top_h0\n"
"s_branch end_h0\nretry2_h0:\ns_sleep 3\nretry_h0:\n"
"s_add_u32 s53, s53, 1\ns_add_u32 s49, s49, 1\ns_cmp_gt_u32 s49, 65536\ns_cbranch_scc1 end_h0\n"
"buffer_load_dwordx4 v[40:43], v10, s[8:11], s43 offen offset:0 sc1\nbuffer_load_dwordx4 v[44:47], v10, s[8:11], s43 offen offset:1024 sc1\nbuffer_load_dwordx4 v[48:51], v10, s[8:11], s43 offen offset:2048 sc1\nbuffer_load_dwordx4 v[52:55], v10, s[8:11], s43 offen offset:3072 sc1\n"
"v_mov_b32 v128, 0\ns_add_u32 s47, s40, 1\nv_mov_b32 v133, s47\nds_write_b32 v134, v133\n"
"s_waitcnt vmcnt(0)\nv_max3_u32 v128, v128, v40, v41\nv_max3_u32 v128, v128, v42, v43\nv_max3_u32 v128, v128, v44, v45\n"
"v_max3_u32 v128, v128, v46, v47\nv_max3_u32 v128, v128, v48, v49\nv_max3_u32 v128, v128, v50, v51\nv_max3_u32 v128, v128, v52, v53\n"
"v_max3_u32 v128, v128, v54, v55\nv_cmp_eq_u32 vcc, -1, v128\nv_mov_b32 v56, 0\nv_mov_b32 v57, 0\n"
"v_mov_b32 v58, 0\nv_mov_b32 v59, 0\nv_mov_b32 v60, 0\nv_mov_b32 v61, 0\n"
"v_mov_b32 v62, 0\nv_mov_b32 v63, 0\nv_mov_b32 v64, 0\nv_mov_b32 v65, 0\n"
"v_mov_b32 v66, 0\nv_mov_b32 v67, 0\nv_mov_b32 v68, 0\nv_mov_b32 v69, 0\n"
"v_mov_b32 v70, 0\nv_mov_b32 v71, 0\ns_cbranch_vccnz retry2_h0\ns_branch landed_h0\n"
"adopt_h0:\nbuffer_load_dword v116, v28, s[8:11], 0 offen sc1\ns_lshl_b32 s52, s40, 26\ns_or_b32 s47, s52, 0x3ffffff\n"
"s_waitcnt vmcnt(0)\nv_cmp_ge_u32 vcc, s47, v116\ns_nop 1\nv_cndmask_b32 v133, 0, v116, vcc\n"
"s_nop 1\nv_max_u32_dpp v133, v133, v133 row_ror:8 row_mask:0xf bank_mask:0xf\ns_nop 1\nv_max_u32_dpp v133, v133, v133 row_ror:4 row_mask:0xf bank_mask:0xf\n"
"s_nop 1\nv_max_u32_dpp v133, v133, v133 row_ror:2 row_mask:0xf bank_mask:0xf\ns_nop 1\nv_max_u32_dpp v133, v133, v133 row_ror:1 row_mask:0xf bank_mask:0xf\n"
"s_nop 0\nv_readlane_b32 s47, v133, 0\nv_readlane_b32 s48, v133, 16\ns_max_u32 s48, s48, s47\n"
"s_cmp_le_u32 s48, s52\ns_cbranch_scc1 back_h0\ns_bfe_u32 s52, s48, 1179656\ns_add_u32 s52, s52, s30\n"
"s_sub_u32 s45, s52, s44\ns_and_b32 s45, s45, 3\ns_sub_u32 s44, s52, s45\ns_lshl_b32 s45, s45, 2\n"
"s_and_b32 s46, s48, 255\ns_mov_b32 s92, 0\ns_branch back_h0\nend_h0:\n"
"s_branch fk_done\nfk_h1:\ns_mov_b32 s40, 0\ns_mov_b32 s41, 0\n"
"s_mov_b32 s42, 262144\ns_mov_b32 s43, 393216\ns_mov_b32 s44, s30\ns_mov_b32 s45, 0\n"
"s_bcnt1_i32_b64 s46, s[28:29]\ns_mov_b32 s92, 99\ns_mov_b32 s49, 0\ns_mov_b32 s53, 0\n"
"s_mov_b32 s50, s36\ns_mov_b32 s51, s37\nv_mov_b32 v131, v13\nv_mov_b32 v132, v14\n"
"v_mov_b32 v134, 24576\nv_mov_b32 v115, 0\nv_mov_b32 v120, -1\nv_mov_b32 v121, -1\n"
"v_mov_b32 v122, -1\nv_mov_b32 v123, -1\ns_add_u32 s47, s35, 4\ns_and_b32 s52, s47, 1\n"
"s_mul_i32 s52, s52, -14208\ns_mul_i32 s48, s47, 14336\ns_add_u32 s48, s48, s52\ns_lshr_b32 s52, s47, 3\n"
"s_mul_i32 s52, s52, 8192\ns_add_u32 s48, s48, s52\ns_add_u32 s47, s48, 2048\nbuffer_load_dwordx4 a[8:11], v15, s[12:15], s47 offen sc1\n"
"buffer_load_dwordx4 a[12:15], v15, s[12:15], s47 offen offset:1024 sc1\ns_add_u32 s47, s48, 0\nbuffer_load_dwordx4 a[0:3], v15, s[12:15], s47 offen sc1\nbuffer_load_dwordx4 a[4:7], v15, s[12:15], s47 offen offset:1024 sc1\n"
"s_add_u32 s47, s48, 6144\nbuffer_load_dwordx4 a[24:27], v15, s[12:15], s47 offen sc1\nbuffer_load_dwordx4 a[28:31], v15, s[12:15], s47 offen offset:1024 sc1\ns_add_u32 s47, s48, 4096\n"
"buffer_load_dwordx4 a[16:19], v15, s[12:15], s47 offen sc1\nbuffer_load_dwordx4 a[20:23], v15, s[12:15], s47 offen offset:1024 sc1\ns_add_u32 s47, s35, 5\ns_and_b32 s52, s47, 1\n"
"s_mul_i32 s52, s52, -14208\ns_mul_i32 s48, s47, 14336\ns_add_u32 s48, s48, s52\ns_lshr_b32 s52, s47, 3\n"
"s_mul_i32 s52, s52, 8192\ns_add_u32 s48, s48, s52\ns_add_u32 s47, s48, 2048\nbuffer_load_dwordx4 a[40:43], v15, s[12:15], s47 offen sc1\n"
"buffer_load_dwordx4 a[44:47], v15, s[12:15], s47 offen offset:1024 sc1\ns_add_u32 s47, s48, 0\nbuffer_load_dwordx4 a[32:35], v15, s[12:15], s47 offen sc1\nbuffer_load_dwordx4 a[36:39], v15, s[12:15], s47 offen offset:1024 sc1\n"
"s_add_u32 s47, s48, 6144\nbuffer_load_dwordx4 a[56:59], v15, s[12:15], s47 offen sc1\nbuffer_load_dwordx4 a[60:63], v15, s[12:15], s47 offen offset:1024 sc1\ns_add_u32 s47, s48, 4096\n"
"buffer_load_dwordx4 a[48:51], v15, s[12:15], s47 offen sc1\nbuffer_load_dwordx4 a[52:55], v15, s[12:15], s47 offen offset:1024 sc1\ns_add_u32 s47, s35, 6\ns_and_b32 s52, s47, 1\n"
"s_mul_i32 s52, s52, -14208\ns_mul_i32 s48, s47, 14336\ns_add_u32 s48, s48, s52\ns_lshr_b32 s52, s47, 3\n"
"s_mul_i32 s52, s52, 8192\ns_add_u32 s48, s48, s52\ns_add_u32 s47, s48, 2048\nbuffer_load_dwordx4 a[72:75], v15, s[12:15], s47 offen sc1\n"
"buffer_load_dwordx4 a[76:79], v15, s[12:15], s47 offen offset:1024 sc1\ns_add_u32 s47, s48, 0\nbuffer_load_dwordx4 a[64:67], v15, s[12:15], s47 offen sc1\nbuffer_load_dwordx4 a[68:71], v15, s[12:15], s47 offen offset:1024 sc1\n"
"s_add_u32 s47, s48, 6144\nbuffer_load_dwordx4 a[88:91], v15, s[12:15], s47 offen sc1\nbuffer_load_dwordx4 a[92:95], v15, s[12:15], s47 offen offset:1024 sc1\ns_add_u32 s47, s48, 4096\n"
"buffer_load_dwordx4 a[80:83], v15, s[12:15], s47 offen sc1\nbuffer_load_dwordx4 a[84:87], v15, s[12:15], s47 offen offset:1024 sc1\ns_add_u32 s47, s50, 7168\nbuffer_load_dwordx4 v[72:75], v11, s[16:19], s50 offen\n"
"buffer_load_dwordx4 v[76:79], v12, s[16:19], s50 offen\nbuffer_load_dwordx4 v[80:83], v11, s[16:19], s47 offen\nbuffer_load_dwordx4 v[84:87], v12, s[16:19], s47 offen\ns_add_u32 s50, s50, 28672\n"
"s_waitcnt vmcnt(0)\ns_barrier\ns_memrealtime s[54:55]\ns_waitcnt lgkmcnt(0)\n"
"s_add_u32 s44, s54, 16\ns_mov_b32 s45, 8\n.p2align 6\ntop_h1:\n"
"v_mov_b32 v56, 0\nv_mov_b32 v57, 0\nv_mov_b32 v58, 0\nv_mov_b32 v59, 0\n"
"v_mov_b32 v60, 0\nv_mov_b32 v61, 0\nv_mov_b32 v62, 0\nv_mov_b32 v63, 0\n"
"v_mov_b32 v64, 0\nv_mov_b32 v65, 0\nv_mov_b32 v66, 0\nv_mov_b32 v67, 0\n"
"v_mov_b32 v128, 0\ns_mul_i32 s48, s45, 6\ns_and_b32 s48, s48, -4\ns_getpc_b64 s[60:61]\n"
"spc_h1:\ns_sub_u32 s48, sgo_h1-spc_h1, s48\ns_add_u32 s60, s60, s48\ns_addc_u32 s61, s61, 0\n"
"sf_h1:\ns_memrealtime s[54:55]\ns_nop 2\ns_memrealtime s[56:57]\n"
"s_nop 2\ns_memrealtime s[58:59]\nsg_h1:\ns_waitcnt lgkmcnt(4)\n"
"s_sub_u32 s47, s54, s44\ns_add_u32 s48, s47, 12\ns_cmp_ge_u32 s48, 12\ns_cbranch_scc1 so_h1\n"
"s_memrealtime s[54:55]\ns_waitcnt lgkmcnt(4)\ns_cmp_ge_u32 s56, s44\ns_cbranch_scc1 sx_h1\n"
"s_memrealtime s[56:57]\ns_waitcnt lgkmcnt(4)\ns_cmp_ge_u32 s58, s44\ns_cbranch_scc1 sx_h1\n"
"s_memrealtime s[58:59]\ns_waitcnt lgkmcnt(4)\ns_cmp_ge_u32 s54, s44\ns_cbranch_scc1 sx_h1\n"
"s_memrealtime s[54:55]\ns_waitcnt lgkmcnt(4)\ns_cmp_ge_u32 s56, s44\ns_cbranch_scc1 sx_h1\n"
"s_memrealtime s[56:57]\ns_waitcnt lgkmcnt(4)\ns_cmp_ge_u32 s58, s44\ns_cbranch_scc1 sx_h1\n"
"s_memrealtime s[58:59]\ns_waitcnt lgkmcnt(4)\ns_cmp_ge_u32 s54, s44\ns_cbranch_scc1 sx_h1\n"
"s_memrealtime s[54:55]\ns_waitcnt lgkmcnt(4)\ns_cmp_ge_u32 s56, s44\ns_cbranch_scc1 sx_h1\n"
"s_memrealtime s[56:57]\ns_waitcnt lgkmcnt(4)\ns_cmp_ge_u32 s58, s44\ns_cbranch_scc1 sx_h1\n"
"s_memrealtime s[58:59]\ns_waitcnt lgkmcnt(4)\ns_cmp_ge_u32 s54, s44\ns_cbranch_scc1 sx_h1\n"
"s_memrealtime s[54:55]\ns_waitcnt lgkmcnt(4)\ns_cmp_ge_u32 s56, s44\ns_cbranch_scc1 sx_h1\n"
"s_memrealtime s[56:57]\ns_waitcnt lgkmcnt(4)\ns_cmp_ge_u32 s58, s44\ns_cbranch_scc1 sx_h1\n"
"s_memrealtime s[58:59]\ns_waitcnt lgkmcnt(4)\ns_cmp_ge_u32 s54, s44\ns_cbranch_scc1 sx_h1\n"
"s_memrealtime s[54:55]\ns_waitcnt lgkmcnt(4)\ns_cmp_ge_u32 s56, s44\ns_cbranch_scc1 sx_h1\n"
"s_memrealtime s[56:57]\ns_waitcnt lgkmcnt(4)\ns_cmp_ge_u32 s58, s44\ns_cbranch_scc1 sx_h1\n"
"s_memrealtime s[58:59]\ns_waitcnt lgkmcnt(4)\ns_cmp_ge_u32 s54, s44\ns_cbranch_scc1 sx_h1\n"
"s_memrealtime s[54:55]\ns_waitcnt lgkmcnt(4)\ns_cmp_ge_u32 s56, s44\ns_cbranch_scc1 sx_h1\n"
"s_memrealtime s[56:57]\ns_branch sx_h1\nso_h1:\ns_waitcnt lgkmcnt(0)\n"
"s_cmp_lt_i32 s47, 0\ns_cbranch_scc1 sf_h1\ns_branch sgo_h1\nsx_h1:\n"
"s_setpc_b64 s[60:61]\ns_nop 0\ns_nop 0\ns_nop 0\n"
"s_nop 0\ns_nop 0\ns_nop 0\ns_nop 0\n"
"s_nop 0\ns_nop 0\ns_nop 0\ns_nop 0\n"
"s_nop 0\ns_nop 0\ns_nop 0\ns_nop 0\n"
"s_nop 0\ns_nop 0\ns_nop 0\ns_nop 0\n"
"s_nop 0\ns_nop 0\ns_nop 0\ns_nop 0\n"
"s_nop 0\nsgo_h1:\ns_nop 13\nbuffer_load_dwordx4 v[40:43], v10, s[8:11], s43 offen offset:1024 sc1\n"
"s_nop 5\nbuffer_load_dwordx4 v[44:47], v10, s[8:11], s43 offen offset:0 sc1\ns_nop 9\nbuffer_load_dwordx4 v[48:51], v10, s[8:11], s43 offen offset:3072 sc1\n"
"s_nop 9\nbuffer_load_dwordx4 v[52:55], v10, s[8:11], s43 offen offset:2048 sc1\nlanded_h1:\ns_waitcnt vmcnt(3)\n"
"v_smfmac_f32_16x16x64_bf16 v[56:59], v[40:43], a[8:15], v8\nv_max3_u32 v128, v128, v40, v41\nv_smfmac_f32_16x16x64_bf16 v[60:63], v[40:43], a[40:47], v8\nv_max3_u32 v128, v128, v42, v43\n"
"v_smfmac_f32_16x16x64_bf16 v[64:67], v[40:43], a[72:79], v8\ns_waitcnt vmcnt(2)\nv_smfmac_f32_16x16x64_bf16 v[56:59], v[44:47], a[0:7], v8\nv_max3_u32 v128, v128, v44, v45\n"
"v_smfmac_f32_16x16x64_bf16 v[60:63], v[44:47], a[32:39], v8\nv_max3_u32 v128, v128, v46, v47\nv_smfmac_f32_16x16x64_bf16 v[64:67], v[44:47], a[64:71], v8\ns_waitcnt vmcnt(1)\n"
"v_smfmac_f32_16x16x64_bf16 v[56:59], v[48:51], a[24:31], v8\nv_max3_u32 v128, v128, v48, v49\nv_smfmac_f32_16x16x64_bf16 v[60:63], v[48:51], a[56:63], v8\nv_max3_u32 v128, v128, v50, v51\n"
"v_smfmac_f32_16x16x64_bf16 v[64:67], v[48:51], a[88:95], v8\ns_waitcnt vmcnt(0)\nv_smfmac_f32_16x16x64_bf16 v[56:59], v[52:55], a[16:23], v8\nv_max3_u32 v128, v128, v52, v53\n"
"v_smfmac_f32_16x16x64_bf16 v[60:63], v[52:55], a[48:55], v8\nv_max3_u32 v128, v128, v54, v55\nv_smfmac_f32_16x16x64_bf16 v[64:67], v[52:55], a[80:87], v8\nv_add_f32 v129, v56, v57\n"
"v_add_f32 v130, v58, v59\nv_cvt_pk_bf16_f32 v112, v129, v130\nv_add_f32 v129, v60, v61\nv_add_f32 v130, v62, v63\n"
"v_cvt_pk_bf16_f32 v113, v129, v130\nv_cmp_eq_u32 vcc, -1, v128\nv_add_f32 v129, v64, v65\nv_add_f32 v130, v66, v67\n"
"v_cvt_pk_bf16_f32 v114, v129, v130\nds_write_b64 v131, v[112:113] offset:256\nds_write_b64 v131, v[114:115] offset:384\ns_cbranch_vccnz retry_h1\n"
"s_waitcnt lgkmcnt(0)\ns_barrier\ns_nop 9\nds_read_b64_tr_b16 v[88:89], v132 offset:0\n"
"ds_read_b64_tr_b16 v[90:91], v132 offset:32\nds_read_b64_tr_b16 v[92:93], v132 offset:256\nds_read_b64_tr_b16 v[94:95], v132 offset:288\nds_read_b64_tr_b16 v[96:97], v132 offset:512\n"
"ds_read_b64_tr_b16 v[98:99], v132 offset:544\nds_read_b64_tr_b16 v[100:101], v132 offset:768\nds_read_b64_tr_b16 v[102:103], v132 offset:800\ns_waitcnt lgkmcnt(6)\n"
"v_mfma_f32_16x16x32_bf16 v[104:107], v[88:91], v[72:75], 0\nv_mfma_f32_16x16x32_bf16 v[108:111], v[88:91], v[80:83], 0\ns_waitcnt lgkmcnt(4)\nv_mfma_f32_16x16x32_bf16 v[104:107], v[92:95], v[76:79], v[104:107]\n"
"v_mfma_f32_16x16x32_bf16 v[108:111], v[92:95], v[84:87], v[108:111]\ns_waitcnt lgkmcnt(2)\nv_mfma_f32_16x16x32_bf16 v[104:107], v[96:99], v[72:75], v[104:107]\nv_mfma_f32_16x16x32_bf16 v[108:111], v[96:99], v[80:83], v[108:111]\n"
"s_waitcnt lgkmcnt(0)\nv_mfma_f32_16x16x32_bf16 v[104:107], v[100:103], v[76:79], v[104:107]\nv_mfma_f32_16x16x32_bf16 v[108:111], v[100:103], v[84:87], v[108:111]\ns_nop 7\n"
"v_add_f32_dpp v124, v106, v104 row_ror:8 row_mask:0xf bank_mask:0x3\nv_add_f32_dpp v124, v108, v110 row_ror:8 row_mask:0xf bank_mask:0xc\nv_add_f32_dpp v125, v107, v105 row_ror:8 row_mask:0xf bank_mask:0x3\nv_add_f32_dpp v125, v109, v111 row_ror:8 row_mask:0xf bank_mask:0xc\n"
"s_add_u32 s47, s50, 7168\nbuffer_load_dwordx4 v[72:75], v11, s[16:19], s50 offen\nbuffer_load_dwordx4 v[76:79], v12, s[16:19], s50 offen\nbuffer_load_dwordx4 v[80:83], v11, s[16:19], s47 offen\n"
"buffer_load_dwordx4 v[84:87], v12, s[16:19], s47 offen\ns_add_u32 s50, s50, 28672\nbuffer_store_dwordx2 v[124:125], v16, s[20:23], s51 offen sc1\ns_add_u32 s51, s51, 1024\n"
"s_cmp_lg_u32 s40, 40\ns_cbranch_scc1 np_h1\ns_add_u32 s47, s51, 20480\nbuffer_store_dwordx2 v[120:121], v16, s[20:23], s47 offen offset:0 sc1\n"
"buffer_store_dwordx2 v[120:121], v16, s[20:23], s47 offen offset:1024 sc1\nbuffer_store_dwordx2 v[120:121], v16, s[20:23], s47 offen offset:2048 sc1\nnp_h1:\nds_read_b32 v133, v134\n"
"v_xor_b32 v131, 8192, v131\nv_xor_b32 v132, 8192, v132\ns_waitcnt lgkmcnt(0)\nv_readfirstlane_b32 s47, v133\n"
"s_sub_u32 s47, s47, s40\ns_cmp_eq_u32 s47, 1\ns_cbranch_scc1 adopt_h1\nback_h1:\n"
"s_mul_i32 s48, s46, 8\ns_add_u32 s45, s45, s48\ns_add_u32 s45, s45, s32\ns_lshr_b32 s48, s45, 4\n"
"s_lshl_b32 s48, s48, 2\ns_add_u32 s44, s44, s48\ns_and_b32 s45, s45, 15\ns_add_u32 s52, s40, 1\n"
"s_bitcmp1_b64 s[28:29], s52\ns_subb_u32 s46, s46, 0\ns_add_u32 s40, s40, 1\ns_mov_b32 s43, s41\n"
"s_add_u32 s41, s41, 131072\ns_and_b32 s41, s41, 524287\ns_add_u32 s92, s92, 1\ns_xor_b32 s42, s41, 262144\n"
"s_waitcnt lgkmcnt(0)\ns_cmp_lt_u32 s40, 64\ns_cbranch_scc1 top_h1\ns_branch end_h1\n"
"retry2_h1:\ns_sleep 3\nretry_h1:\ns_add_u32 s53, s53, 1\n"
"s_add_u32 s49, s49, 1\ns_cmp_gt_u32 s49, 65536\ns_cbranch_scc1 end_h1\nbuffer_load_dwordx4 v[40:43], v10, s[8:11], s43 offen offset:1024 sc1\n"
"buffer_load_dwordx4 v[44:47], v10, s[8:11], s43 offen offset:0 sc1\nbuffer_load_dwordx4 v[48:51], v10, s[8:11], s43 offen offset:3072 sc1\nbuffer_load_dwordx4 v[52:55], v10, s[8:11], s43 offen offset:2048 sc1\nv_mov_b32 v128, 0\n"
"s_add_u32 s47, s40, 1\nv_mov_b32 v133, s47\nds_write_b32 v134, v133\ns_waitcnt vmcnt(0)\n"
"v_max3_u32 v128, v128, v40, v41\nv_max3_u32 v128, v128, v42, v43\nv_max3_u32 v128, v128, v44, v45\nv_max3_u32 v128, v128, v46, v47\n"
"v_max3_u32 v128, v128, v48, v49\nv_max3_u32 v128, v128, v50, v51\nv_max3_u32 v128, v128, v52, v53\nv_max3_u32 v128, v128, v54, v55\n"
"v_cmp_eq_u32 vcc, -1, v128\nv_mov_b32 v56, 0\nv_mov_b32 v57, 0\nv_mov_b32 v58, 0\n"
"v_mov_b32 v59, 0\nv_mov_b32 v60, 0\nv_mov_b32 v61, 0\nv_mov_b32 v62, 0\n"
"v_mov_b32 v63, 0\nv_mov_b32 v64, 0\nv_mov_b32 v65, 0\nv_mov_b32 v66, 0\n"
"v_mov_b32 v67, 0\ns_cbranch_vccnz retry2_h1\ns_branch landed_h1\nadopt_h1:\n"
"buffer_load_dword v116, v28, s[8:11], 0 offen sc1\ns_lshl_b32 s52, s40, 26\ns_or_b32 s47, s52, 0x3ffffff\ns_waitcnt vmcnt(0)\n"
"v_cmp_ge_u32 vcc, s47, v116\ns_nop 1\nv_cndmask_b32 v133, 0, v116, vcc\ns_nop 1\n"
"v_max_u32_dpp v133, v133, v133 row_ror:8 row_mask:0xf bank_mask:0xf\ns_nop 1\nv_max_u32_dpp v133, v133, v133 row_ror:4 row_mask:0xf bank_mask:0xf\ns_nop 1\n"
"v_max_u32_dpp v133, v133, v133 row_ror:2 row_mask:0xf bank_mask:0xf\ns_nop 1\nv_max_u32_dpp v133, v133, v133 row_ror:1 row_mask:0xf bank_mask:0xf\ns_nop 0\n"
"v_readlane_b32 s47, v133, 0\nv_readlane_b32 s48, v133, 16\ns_max_u32 s48, s48, s47\ns_cmp_le_u32 s48, s52\n"
"s_cbranch_scc1 back_h1\ns_bfe_u32 s52, s48, 1179656\ns_add_u32 s52, s52, s30\ns_sub_u32 s45, s52, s44\n"
"s_and_b32 s45, s45, 3\ns_sub_u32 s44, s52, s45\ns_lshl_b32 s45, s45, 2\ns_and_b32 s46, s48, 255\n"
"s_mov_b32 s92, 0\ns_branch back_h1\nend_h1:\ns_branch fk_done\n"
"fk_h2:\ns_mov_b32 s40, 0\ns_mov_b32 s41, 0\ns_mov_b32 s42, 262144\n"
"s_mov_b32 s43, 393216\ns_mov_b32 s44, s30\ns_mov_b32 s45, 0\ns_bcnt1_i32_b64 s46, s[28:29]\n"
"s_mov_b32 s92, 99\ns_mov_b32 s49, 0\ns_mov_b32 s53, 0\ns_mov_b32 s50, s36\n"
"s_mov_b32 s51, s37\nv_mov_b32 v127, v13\nv_mov_b32 v128, v14\nv_mov_b32 v130, 24576\n"
"v_mov_b32 v111, 0\nv_mov_b32 v116, -1\nv_mov_b32 v117, -1\nv_mov_b32 v118, -1\n"
"v_mov_b32 v119, -1\ns_add_u32 s47, s35, 0\ns_and_b32 s52, s47, 1\ns_mul_i32 s52, s52, -14208\n"
"s_mul_i32 s48, s47, 14336\ns_add_u32 s48, s48, s52\ns_lshr_b32 s52, s47, 3\ns_mul_i32 s52, s52, 8192\n"
"s_add_u32 s48, s48, s52\ns_add_u32 s47, s48, 8192\nbuffer_load_dwordx4 a[0:3], v15, s[12:15], s47 offen sc1\nbuffer_load_dwordx4 a[4:7], v15, s[12:15], s47 offen offset:1024 sc1\n"
"s_add_u32 s47, s48, 10240\nbuffer_load_dwordx4 a[8:11], v15, s[12:15], s47 offen sc1\nbuffer_load_dwordx4 a[12:15], v15, s[12:15], s47 offen offset:1024 sc1\ns_add_u32 s47, s48, 12288\n"
"buffer_load_dwordx4 a[16:19], v15, s[12:15], s47 offen sc1\nbuffer_load_dwordx4 a[20:23], v15, s[12:15], s47 offen offset:1024 sc1\ns_add_u32 s47, s35, 1\ns_and_b32 s52, s47, 1\n"
"s_mul_i32 s52, s52, -14208\ns_mul_i32 s48, s47, 14336\ns_add_u32 s48, s48, s52\ns_lshr_b32 s52, s47, 3\n"
"s_mul_i32 s52, s52, 8192\ns_add_u32 s48, s48, s52\ns_add_u32 s47, s48, 8192\nbuffer_load_dwordx4 a[32:35], v15, s[12:15], s47 offen sc1\n"
"buffer_load_dwordx4 a[36:39], v15, s[12:15], s47 offen offset:1024 sc1\ns_add_u32 s47, s48, 10240\nbuffer_load_dwordx4 a[40:43], v15, s[12:15], s47 offen sc1\nbuffer_load_dwordx4 a[44:47], v15, s[12:15], s47 offen offset:1024 sc1\n"
"s_add_u32 s47, s48, 12288\nbuffer_load_dwordx4 a[48:51], v15, s[12:15], s47 offen sc1\nbuffer_load_dwordx4 a[52:55], v15, s[12:15], s47 offen offset:1024 sc1\ns_add_u32 s47, s35, 2\n"
"s_and_b32 s52, s47, 1\ns_mul_i32 s52, s52, -14208\ns_mul_i32 s48, s47, 14336\ns_add_u32 s48, s48, s52\n"
"s_lshr_b32 s52, s47, 3\ns_mul_i32 s52, s52, 8192\ns_add_u32 s48, s48, s52\ns_add_u32 s47, s48, 8192\n"
"buffer_load_dwordx4 a[64:67], v15, s[12:15], s47 offen sc1\nbuffer_load_dwordx4 a[68:71], v15, s[12:15], s47 offen offset:1024 sc1\ns_add_u32 s47, s48, 10240\nbuffer_load_dwordx4 a[72:75], v15, s[12:15], s47 offen sc1\n"
"buffer_load_dwordx4 a[76:79], v15, s[12:15], s47 offen offset:1024 sc1\ns_add_u32 s47, s48, 12288\nbuffer_load_dwordx4 a[80:83], v15, s[12:15], s47 offen sc1\nbuffer_load_dwordx4 a[84:87], v15, s[12:15], s47 offen offset:1024 sc1\n"
"s_add_u32 s47, s35, 3\ns_and_b32 s52, s47, 1\ns_mul_i32 s52, s52, -14208\ns_mul_i32 s48, s47, 14336\n"
"s_add_u32 s48, s48, s52\ns_lshr_b32 s52, s47, 3\ns_mul_i32 s52, s52, 8192\ns_add_u32 s48, s48, s52\n"
"s_add_u32 s47, s48, 8192\nbuffer_load_dwordx4 a[96:99], v15, s[12:15], s47 offen sc1\nbuffer_load_dwordx4 a[100:103], v15, s[12:15], s47 offen offset:1024 sc1\ns_add_u32 s47, s48, 10240\n"
"buffer_load_dwordx4 a[104:107], v15, s[12:15], s47 offen sc1\nbuffer_load_dwordx4 a[108:111], v15, s[12:15], s47 offen offset:1024 sc1\ns_add_u32 s47, s48, 12288\nbuffer_load_dwordx4 a[112:115], v15, s[12:15], s47 offen sc1\n"
"buffer_load_dwordx4 a[116:119], v15, s[12:15], s47 offen offset:1024 sc1\ns_waitcnt vmcnt(0)\ns_barrier\ns_memrealtime s[54:55]\n"
"s_waitcnt lgkmcnt(0)\ns_add_u32 s44, s54, 16\ns_mov_b32 s45, 8\n.p2align 6\n"
"top_h2:\nv_mov_b32 v52, 0\nv_mov_b32 v53, 0\nv_mov_b32 v54, 0\n"
"v_mov_b32 v55, 0\nv_mov_b32 v56, 0\nv_mov_b32 v57, 0\nv_mov_b32 v58, 0\n"
"v_mov_b32 v59, 0\nv_mov_b32 v60, 0\nv_mov_b32 v61, 0\nv_mov_b32 v62, 0\n"
"v_mov_b32 v63, 0\nv_mov_b32 v64, 0\nv_mov_b32 v65, 0\nv_mov_b32 v66, 0\n"
"v_mov_b32 v67, 0\nv_mov_b32 v124, 0\ns_mul_i32 s48, s45, 6\ns_and_b32 s48, s48, -4\n"
"s_getpc_b64 s[60:61]\nspc_h2:\ns_sub_u32 s48, sgo_h2-spc_h2, s48\ns_add_u32 s60, s60, s48\n"
"s_addc_u32 s61, s61, 0\nsf_h2:\ns_memrealtime s[54:55]\ns_nop 2\n"
"s_memrealtime s[56:57]\ns_nop 2\ns_memrealtime s[58:59]\nsg_h2:\n"
"s_waitcnt lgkmcnt(4)\ns_sub_u32 s47, s54, s44\ns_add_u32 s48, s47, 12\ns_cmp_ge_u32 s48, 12\n"
"s_cbranch_scc1 so_h2\ns_memrealtime s[54:55]\ns_waitcnt lgkmcnt(4)\ns_cmp_ge_u32 s56, s44\n"
"s_cbranch_scc1 sx_h2\ns_memrealtime s[56:57]\ns_waitcnt lgkmcnt(4)\ns_cmp_ge_u32 s58, s44\n"
"s_cbranch_scc1 sx_h2\ns_memrealtime s[58:59]\ns_waitcnt lgkmcnt(4)\ns_cmp_ge_u32 s54, s44\n"
"s_cbranch_scc1 sx_h2\ns_memrealtime s[54:55]\ns_waitcnt lgkmcnt(4)\ns_cmp_ge_u32 s56, s44\n"
"s_cbranch_scc1 sx_h2\ns_memrealtime s[56:57]\ns_waitcnt lgkmcnt(4)\ns_cmp_ge_u32 s58, s44\n"
"s_cbranch_scc1 sx_h2\ns_memrealtime s[58:59]\ns_waitcnt lgkmcnt(4)\ns_cmp_ge_u32 s54, s44\n"
"s_cbranch_scc1 sx_h2\ns_memrealtime s[54:55]\ns_waitcnt lgkmcnt(4)\ns_cmp_ge_u32 s56, s44\n"
"s_cbranch_scc1 sx_h2\ns_memrealtime s[56:57]\ns_waitcnt lgkmcnt(4)\ns_cmp_ge_u32 s58, s44\n"
"s_cbranch_scc1 sx_h2\ns_memrealtime s[58:59]\ns_waitcnt lgkmcnt(4)\ns_cmp_ge_u32 s54, s44\n"
"s_cbranch_scc1 sx_h2\ns_memrealtime s[54:55]\ns_waitcnt lgkmcnt(4)\ns_cmp_ge_u32 s56, s44\n"
"s_cbranch_scc1 sx_h2\ns_memrealtime s[56:57]\ns_waitcnt lgkmcnt(4)\ns_cmp_ge_u32 s58, s44\n"
"s_cbranch_scc1 sx_h2\ns_memrealtime s[58:59]\ns_waitcnt lgkmcnt(4)\ns_cmp_ge_u32 s54, s44\n"
"s_cbranch_scc1 sx_h2\ns_memrealtime s[54:55]\ns_waitcnt lgkmcnt(4)\ns_cmp_ge_u32 s56, s44\n"
"s_cbranch_scc1 sx_h2\ns_memrealtime s[56:57]\ns_waitcnt lgkmcnt(4)\ns_cmp_ge_u32 s58, s44\n"
"s_cbranch_scc1 sx_h2\ns_memrealtime s[58:59]\ns_waitcnt lgkmcnt(4)\ns_cmp_ge_u32 s54, s44\n"
"s_cbranch_scc1 sx_h2\ns_memrealtime s[54:55]\ns_waitcnt lgkmcnt(4)\ns_cmp_ge_u32 s56, s44\n"
"s_cbranch_scc1 sx_h2\ns_memrealtime s[56:57]\ns_branch sx_h2\nso_h2:\n"
"s_waitcnt lgkmcnt(0)\ns_cmp_lt_i32 s47, 0\ns_cbranch_scc1 sf_h2\ns_branch sgo_h2\n"
"sx_h2:\ns_setpc_b64 s[60:61]\ns_nop 0\ns_nop 0\n"
"s_nop 0\ns_nop 0\ns_nop 0\ns_nop 0\n"
"s_nop 0\ns_nop 0\ns_nop 0\ns_nop 0\n"
"s_nop 0\ns_nop 0\ns_nop 0\ns_nop 0\n"
"s_nop 0\ns_nop 0\ns_nop 0\ns_nop 0\n"
"s_nop 0\ns_nop 0\ns_nop 0\ns_nop 0\n"
"s_nop 0\ns_nop 0\nsgo_h2:\ns_nop 9\n"
"buffer_load_dwordx4 v[40:43], v10, s[8:11], s43 offen offset:0 sc1\ns_nop 13\nbuffer_load_dwordx4 v[44:47], v10, s[8:11], s43 offen offset:1024 sc1\ns_nop 9\n"
"buffer_load_dwordx4 v[48:51], v10, s[8:11], s43 offen offset:2048 sc1\nlanded_h2:\ns_waitcnt vmcnt(2)\nv_smfmac_f32_16x16x64_bf16 v[52:55], v[40:43], a[0:7], v8\n"
"v_max3_u32 v124, v124, v40, v41\nv_smfmac_f32_16x16x64_bf16 v[56:59], v[40:43], a[32:39], v8\nv_max3_u32 v124, v124, v42, v43\nv_smfmac_f32_16x16x64_bf16 v[60:63], v[40:43], a[64:71], v8\n"
"v_smfmac_f32_16x16x64_bf16 v[64:67], v[40:43], a[96:103], v8\ns_waitcnt vmcnt(1)\nv_smfmac_f32_16x16x64_bf16 v[52:55], v[44:47], a[8:15], v8\nv_max3_u32 v124, v124, v44, v45\n"
"v_smfmac_f32_16x16x64_bf16 v[56:59], v[44:47], a[40:47], v8\nv_max3_u32 v124, v124, v46, v47\nv_smfmac_f32_16x16x64_bf16 v[60:63], v[44:47], a[72:79], v8\nv_smfmac_f32_16x16x64_bf16 v[64:67], v[44:47], a[104:111], v8\n"
"s_waitcnt vmcnt(0)\nv_smfmac_f32_16x16x64_bf16 v[52:55], v[48:51], a[16:23], v8\nv_max3_u32 v124, v124, v48, v49\nv_smfmac_f32_16x16x64_bf16 v[56:59], v[48:51], a[48:55], v8\n"
"v_max3_u32 v124, v124, v50, v51\nv_smfmac_f32_16x16x64_bf16 v[60:63], v[48:51], a[80:87], v8\nv_cmp_eq_u32 vcc, -1, v124\nv_smfmac_f32_16x16x64_bf16 v[64:67], v[48:51], a[112:119], v8\n"
"v_add_f32 v125, v52, v53\nv_add_f32 v126, v54, v55\nv_cvt_pk_bf16_f32 v108, v125, v126\nv_add_f32 v125, v56, v57\n"
"v_add_f32 v126, v58, v59\nv_cvt_pk_bf16_f32 v109, v125, v126\nv_add_f32 v125, v60, v61\nv_add_f32 v126, v62, v63\n"
"v_cvt_pk_bf16_f32 v110, v125, v126\nv_add_f32 v125, v64, v65\nv_add_f32 v126, v66, v67\nv_cvt_pk_bf16_f32 v111, v125, v126\n"
"ds_write_b64 v127, v[108:109] offset:0\nds_write_b64 v127, v[110:111] offset:128\ns_cbranch_vccnz retry_h2\ns_waitcnt lgkmcnt(0)\n"
"s_barrier\nv_mov_b32 v125, 0\ns_nop 1\nv_mov_b32 v125, 0\n"
"s_nop 1\nv_mov_b32 v125, 0\ns_nop 1\nv_mov_b32 v125, 0\n"
"s_nop 1\nv_mov_b32 v125, 0\ns_nop 1\nv_mov_b32 v125, 0\n"
"s_nop 1\nv_mov_b32 v125, 0\ns_nop 1\nv_mov_b32 v125, 0\n"
"s_nop 1\nv_mov_b32 v125, 0\ns_nop 1\nv_mov_b32 v125, 0\n"
"s_nop 1\nv_mov_b32 v125, 0\ns_nop 1\nv_mov_b32 v125, 0\n"
"s_nop 1\nv_mov_b32 v125, 0\ns_nop 1\nv_mov_b32 v125, 0\n"
"s_nop 1\nv_mov_b32 v125, 0\ns_nop 1\nv_mov_b32 v125, 0\n"
"s_nop 1\nv_mov_b32 v125, 0\ns_nop 1\nv_mov_b32 v125, 0\n"
"s_nop 1\nv_mov_b32 v125, 0\ns_nop 1\nv_mov_b32 v125, 0\n"
"s_nop 1\nv_mov_b32 v125, 0\ns_nop 1\nv_mov_b32 v125, 0\n"
"buffer_store_dwordx4 v[116:119], v17, s[8:11], s42 offen sc1\ns_sub_u32 s47, s40, 35\ns_cmp_gt_u32 s47, 5\ns_cbranch_scc1 np_h2\n"
"s_lshl_b32 s47, s47, 10\nbuffer_store_dwordx4 v[116:119], v11, s[16:19], s47 offen sc1\nnp_h2:\nds_read_b32 v129, v130\n"
"v_xor_b32 v127, 8192, v127\nv_xor_b32 v128, 8192, v128\ns_waitcnt lgkmcnt(0)\nv_readfirstlane_b32 s47, v129\n"
"s_sub_u32 s47, s47, s40\ns_cmp_eq_u32 s47, 1\ns_cbranch_scc1 adopt_h2\nback_h2:\n"
"s_mul_i32 s48, s46, 8\ns_add_u32 s45, s45, s48\ns_add_u32 s45, s45, s32\ns_lshr_b32 s48, s45, 4\n"
"s_lshl_b32 s48, s48, 2\ns_add_u32 s44, s44, s48\ns_and_b32 s45, s45, 15\ns_add_u32 s52, s40, 1\n"
"s_bitcmp1_b64 s[28:29], s52\ns_subb_u32 s46, s46, 0\ns_add_u32 s40, s40, 1\ns_mov_b32 s43, s41\n"
"s_add_u32 s41, s41, 131072\ns_and_b32 s41, s41, 524287\ns_add_u32 s92, s92, 1\ns_xor_b32 s42, s41, 262144\n"
"s_waitcnt lgkmcnt(0)\ns_cmp_lt_u32 s40, 64\ns_cbranch_scc1 top_h2\ns_branch end_h2\n"
"retry2_h2:\ns_sleep 3\nretry_h2:\ns_add_u32 s53, s53, 1\n"
"s_add_u32 s49, s49, 1\ns_cmp_gt_u32 s49, 65536\ns_cbranch_scc1 end_h2\nbuffer_load_dwordx4 v[40:43], v10, s[8:11], s43 offen offset:0 sc1\n"
"buffer_load_dwordx4 v[44:47], v10, s[8:11], s43 offen offset:1024 sc1\nbuffer_load_dwordx4 v[48:51], v10, s[8:11], s43 offen offset:2048 sc1\nv_mov_b32 v124, 0\ns_add_u32 s47, s40, 1\n"
"v_mov_b32 v129, s47\nds_write_b32 v130, v129\ns_waitcnt vmcnt(0)\nv_max3_u32 v124, v124, v40, v41\n"
"v_max3_u32 v124, v124, v42, v43\nv_max3_u32 v124, v124, v44, v45\nv_max3_u32 v124, v124, v46, v47\nv_max3_u32 v124, v124, v48, v49\n"
"v_max3_u32 v124, v124, v50, v51\nv_cmp_eq_u32 vcc, -1, v124\nv_mov_b32 v52, 0\nv_mov_b32 v53, 0\n"
"v_mov_b32 v54, 0\nv_mov_b32 v55, 0\nv_mov_b32 v56, 0\nv_mov_b32 v57, 0\n"
"v_mov_b32 v58, 0\nv_mov_b32 v59, 0\nv_mov_b32 v60, 0\nv_mov_b32 v61, 0\n"
"v_mov_b32 v62, 0\nv_mov_b32 v63, 0\nv_mov_b32 v64, 0\nv_mov_b32 v65, 0\n"
"v_mov_b32 v66, 0\nv_mov_b32 v67, 0\ns_cbranch_vccnz retry2_h2\ns_branch landed_h2\n"
"adopt_h2:\nbuffer_load_dword v112, v28, s[8:11], 0 offen sc1\ns_lshl_b32 s52, s40, 26\ns_or_b32 s47, s52, 0x3ffffff\n"
"s_waitcnt vmcnt(0)\nv_cmp_ge_u32 vcc, s47, v112\ns_nop 1\nv_cndmask_b32 v129, 0, v112, vcc\n"
"s_nop 1\nv_max_u32_dpp v129, v129, v129 row_ror:8 row_mask:0xf bank_mask:0xf\ns_nop 1\nv_max_u32_dpp v129, v129, v129 row_ror:4 row_mask:0xf bank_mask:0xf\n"
"s_nop 1\nv_max_u32_dpp v129, v129, v129 row_ror:2 row_mask:0xf bank_mask:0xf\ns_nop 1\nv_max_u32_dpp v129, v129, v129 row_ror:1 row_mask:0xf bank_mask:0xf\n"
"s_nop 0\nv_readlane_b32 s47, v129, 0\nv_readlane_b32 s48, v129, 16\ns_max_u32 s48, s48, s47\n"
"s_cmp_le_u32 s48, s52\ns_cbranch_scc1 back_h2\ns_bfe_u32 s52, s48, 1179656\ns_add_u32 s52, s52, s30\n"
"s_sub_u32 s45, s52, s44\ns_and_b32 s45, s45, 3\ns_sub_u32 s44, s52, s45\ns_lshl_b32 s45, s45, 2\n"
"s_and_b32 s46, s48, 255\ns_mov_b32 s92, 0\ns_branch back_h2\nend_h2:\n"
"s_branch fk_done\nfk_h3:\ns_mov_b32 s40, 0\ns_mov_b32 s41, 0\n"
"s_mov_b32 s42, 262144\ns_mov_b32 s43, 393216\ns_mov_b32 s44, s30\ns_mov_b32 s45, 0\n"
"s_bcnt1_i32_b64 s46, s[28:29]\ns_mov_b32 s92, 99\ns_mov_b32 s49, 0\ns_mov_b32 s53, 0\n"
"s_mov_b32 s50, s36\ns_mov_b32 s51, s37\nv_mov_b32 v127, v13\nv_mov_b32 v128, v14\n"
"v_mov_b32 v130, 24576\nv_mov_b32 v111, 0\nv_mov_b32 v116, -1\nv_mov_b32 v117, -1\n"
"v_mov_b32 v118, -1\nv_mov_b32 v119, -1\ns_add_u32 s47, s35, 4\ns_and_b32 s52, s47, 1\n"
"s_mul_i32 s52, s52, -14208\ns_mul_i32 s48, s47, 14336\ns_add_u32 s48, s48, s52\ns_lshr_b32 s52, s47, 3\n"
"s_mul_i32 s52, s52, 8192\ns_add_u32 s48, s48, s52\ns_add_u32 s47, s48, 10240\nbuffer_load_dwordx4 a[8:11], v15, s[12:15], s47 offen sc1\n"
"buffer_load_dwordx4 a[12:15], v15, s[12:15], s47 offen offset:1024 sc1\ns_add_u32 s47, s48, 8192\nbuffer_load_dwordx4 a[0:3], v15, s[12:15], s47 offen sc1\nbuffer_load_dwordx4 a[4:7], v15, s[12:15], s47 offen offset:1024 sc1\n"
"s_add_u32 s47, s48, 12288\nbuffer_load_dwordx4 a[16:19], v15, s[12:15], s47 offen sc1\nbuffer_load_dwordx4 a[20:23], v15, s[12:15], s47 offen offset:1024 sc1\ns_add_u32 s47, s35, 5\n"
"s_and_b32 s52, s47, 1\ns_mul_i32 s52, s52, -14208\ns_mul_i32 s48, s47, 14336\ns_add_u32 s48, s48, s52\n"
"s_lshr_b32 s52, s47, 3\ns_mul_i32 s52, s52, 8192\ns_add_u32 s48, s48, s52\ns_add_u32 s47, s48, 10240\n"
"buffer_load_dwordx4 a[40:43], v15, s[12:15], s47 offen sc1\nbuffer_load_dwordx4 a[44:47], v15, s[12:15], s47 offen offset:1024 sc1\ns_add_u32 s47, s48, 8192\nbuffer_load_dwordx4 a[32:35], v15, s[12:15], s47 offen sc1\n"
"buffer_load_dwordx4 a[36:39], v15, s[12:15], s47 offen offset:1024 sc1\ns_add_u32 s47, s48, 12288\nbuffer_load_dwordx4 a[48:51], v15, s[12:15], s47 offen sc1\nbuffer_load_dwordx4 a[52:55], v15, s[12:15], s47 offen offset:1024 sc1\n"
"s_add_u32 s47, s35, 6\ns_and_b32 s52, s47, 1\ns_mul_i32 s52, s52, -14208\ns_mul_i32 s48, s47, 14336\n"
"s_add_u32 s48, s48, s52\ns_lshr_b32 s52, s47, 3\ns_mul_i32 s52, s52, 8192\ns_add_u32 s48, s48, s52\n"
"s_add_u32 s47, s48, 10240\nbuffer_load_dwordx4 a[72:75], v15, s[12:15], s47 offen sc1\nbuffer_load_dwordx4 a[76:79], v15, s[12:15], s47 offen offset:1024 sc1\ns_add_u32 s47, s48, 8192\n"
"buffer_load_dwordx4 a[64:67], v15, s[12:15], s47 offen sc1\nbuffer_load_dwordx4 a[68:71], v15, s[12:15], s47 offen offset:1024 sc1\ns_add_u32 s47, s48, 12288\nbuffer_load_dwordx4 a[80:83], v15, s[12:15], s47 offen sc1\n"
"buffer_load_dwordx4 a[84:87], v15, s[12:15], s47 offen offset:1024 sc1\nv_lshlrev_b32 v114, 2, v9\ns_mov_b32 s52, 1064960\nbuffer_load_dword v112, v114, s[8:11], s52 offen sc1\n"
"s_add_u32 s52, s52, 4096\nbuffer_load_dword v113, v114, s[8:11], s52 offen sc1\ns_waitcnt vmcnt(0)\nar_h3:\n"
"v_xor_b32 v112, s34, v112\nv_xor_b32 v113, s34, v113\nv_or_b32 v112, v112, v113\nv_cmp_ne_u32 vcc, 0, v112\n"
"s_cbranch_vccz arok_h3\ns_add_u32 s53, s53, 0\ns_add_u32 s49, s49, 1\ns_cmp_gt_u32 s49, 65536\n"
"s_cbranch_scc1 end_h3\ns_sleep 2\nv_lshlrev_b32 v114, 2, v9\ns_mov_b32 s52, 1064960\n"
"buffer_load_dword v112, v114, s[8:11], s52 offen sc1\ns_add_u32 s52, s52, 4096\nbuffer_load_dword v113, v114, s[8:11], s52 offen sc1\ns_waitcnt vmcnt(0)\n"
"s_branch ar_h3\narok_h3:\ns_barrier\ns_memrealtime s[54:55]\n"
"s_waitcnt lgkmcnt(0)\ns_add_u32 s44, s54, 16\ns_mov_b32 s45, 8\n.p2align 6\n"
"top_h3:\nv_mov_b32 v52, 0\nv_mov_b32 v53, 0\nv_mov_b32 v54, 0\n"
"v_mov_b32 v55, 0\nv_mov_b32 v56, 0\nv_mov_b32 v57, 0\nv_mov_b32 v58, 0\n"
"v_mov_b32 v59, 0\nv_mov_b32 v60, 0\nv_mov_b32 v61, 0\nv_mov_b32 v62, 0\n"
"v_mov_b32 v63, 0\nv_mov_b32 v124, 0\ns_mul_i32 s48, s45, 6\ns_and_b32 s48, s48, -4\n"
"s_getpc_b64 s[60:61]\nspc_h3:\ns_sub_u32 s48, sgo_h3-spc_h3, s48\ns_add_u32 s60, s60, s48\n"
"s_addc_u32 s61, s61, 0\nsf_h3:\ns_memrealtime s[54:55]\ns_nop 2\n"
"s_memrealtime s[56:57]\ns_nop 2\ns_memrealtime s[58:59]\nsg_h3:\n"
"s_waitcnt lgkmcnt(4)\ns_sub_u32 s47, s54, s44\ns_add_u32 s48, s47, 12\ns_cmp_ge_u32 s48, 12\n"
"s_cbranch_scc1 so_h3\ns_memrealtime s[54:55]\ns_waitcnt lgkmcnt(4)\ns_cmp_ge_u32 s56, s44\n"
"s_cbranch_scc1 sx_h3\ns_memrealtime s[56:57]\ns_waitcnt lgkmcnt(4)\ns_cmp_ge_u32 s58, s44\n"
"s_cbranch_scc1 sx_h3\ns_memrealtime s[58:59]\ns_waitcnt lgkmcnt(4)\ns_cmp_ge_u32 s54, s44\n"
"s_cbranch_scc1 sx_h3\ns_memrealtime s[54:55]\ns_waitcnt lgkmcnt(4)\ns_cmp_ge_u32 s56, s44\n"
"s_cbranch_scc1 sx_h3\ns_memrealtime s[56:57]\ns_waitcnt lgkmcnt(4)\ns_cmp_ge_u32 s58, s44\n"
"s_cbranch_scc1 sx_h3\ns_memrealtime s[58:59]\ns_waitcnt lgkmcnt(4)\ns_cmp_ge_u32 s54, s44\n"
"s_cbranch_scc1 sx_h3\ns_memrealtime s[54:55]\ns_waitcnt lgkmcnt(4)\ns_cmp_ge_u32 s56, s44\n"
"s_cbranch_scc1 sx_h3\ns_memrealtime s[56:57]\ns_waitcnt lgkmcnt(4)\ns_cmp_ge_u32 s58, s44\n"
"s_cbranch_scc1 sx_h3\ns_memrealtime s[58:59]\ns_waitcnt lgkmcnt(4)\ns_cmp_ge_u32 s54, s44\n"
"s_cbranch_scc1 sx_h3\ns_memrealtime s[54:55]\ns_waitcnt lgkmcnt(4)\ns_cmp_ge_u32 s56, s44\n"
"s_cbranch_scc1 sx_h3\ns_memrealtime s[56:57]\ns_waitcnt lgkmcnt(4)\ns_cmp_ge_u32 s58, s44\n"
"s_cbranch_scc1 sx_h3\ns_memrealtime s[58:59]\ns_waitcnt lgkmcnt(4)\ns_cmp_ge_u32 s54, s44\n"
"s_cbranch_scc1 sx_h3\ns_memrealtime s[54:55]\ns_waitcnt lgkmcnt(4)\ns_cmp_ge_u32 s56, s44\n"
"s_cbranch_scc1 sx_h3\ns_memrealtime s[56:57]\ns_waitcnt lgkmcnt(4)\ns_cmp_ge_u32 s58, s44\n"
"s_cbranch_scc1 sx_h3\ns_memrealtime s[58:59]\ns_waitcnt lgkmcnt(4)\ns_cmp_ge_u32 s54, s44\n"
"s_cbranch_scc1 sx_h3\ns_memrealtime s[54:55]\ns_waitcnt lgkmcnt(4)\ns_cmp_ge_u32 s56, s44\n"
"s_cbranch_scc1 sx_h3\ns_memrealtime s[56:57]\ns_branch sx_h3\nso_h3:\n"
"s_waitcnt lgkmcnt(0)\ns_cmp_lt_i32 s47, 0\ns_cbranch_scc1 sf_h3\ns_branch sgo_h3\n"
"sx_h3:\ns_setpc_b64 s[60:61]\ns_nop 0\ns_nop 0\n"
"s_nop 0\ns_nop 0\ns_nop 0\ns_nop 0\n"
"s_nop 0\ns_nop 0\ns_nop 0\ns_nop 0\n"
"s_nop 0\ns_nop 0\ns_nop 0\ns_nop 0\n"
"s_nop 0\ns_nop 0\ns_nop 0\ns_nop 0\n"
"s_nop 0\ns_nop 0\ns_nop 0\ns_nop 0\n"
"s_nop 0\ns_nop 0\nsgo_h3:\ns_nop 15\n"
"s_nop 7\nbuffer_load_dwordx4 v[40:43], v10, s[8:11], s43 offen offset:1024 sc1\ns_nop 9\nbuffer_load_dwordx4 v[44:47], v10, s[8:11], s43 offen offset:0 sc1\n"
"s_nop 9\nbuffer_load_dwordx4 v[48:51], v10, s[8:11], s43 offen offset:2048 sc1\ns_nop 15\ns_nop 7\n"
"buffer_load_dwordx4 v[68:71], v11, s[8:11], s43 offen sc1\nbuffer_load_dwordx4 v[72:75], v12, s[8:11], s43 offen sc1\nv_mov_b32 v125, 0\ns_nop 1\n"
"v_mov_b32 v125, 0\ns_nop 1\nv_mov_b32 v125, 0\ns_nop 1\n"
"v_mov_b32 v125, 0\ns_nop 1\nv_mov_b32 v125, 0\ns_nop 1\n"
"v_mov_b32 v125, 0\ns_nop 1\nv_mov_b32 v125, 0\ns_nop 1\n"
"v_mov_b32 v125, 0\ns_nop 1\nv_mov_b32 v125, 0\nlanded_h3:\n"
"s_waitcnt vmcnt(4)\nv_smfmac_f32_16x16x64_bf16 v[52:55], v[40:43], a[8:15], v8\nv_max3_u32 v124, v124, v40, v41\nv_smfmac_f32_16x16x64_bf16 v[56:59], v[40:43], a[40:47], v8\n"
"v_max3_u32 v124, v124, v42, v43\nv_smfmac_f32_16x16x64_bf16 v[60:63], v[40:43], a[72:79], v8\ns_waitcnt vmcnt(3)\nv_smfmac_f32_16x16x64_bf16 v[52:55], v[44:47], a[0:7], v8\n"
"v_max3_u32 v124, v124, v44, v45\nv_smfmac_f32_16x16x64_bf16 v[56:59], v[44:47], a[32:39], v8\nv_max3_u32 v124, v124, v46, v47\nv_smfmac_f32_16x16x64_bf16 v[60:63], v[44:47], a[64:71], v8\n"
"s_waitcnt vmcnt(2)\nv_smfmac_f32_16x16x64_bf16 v[52:55], v[48:51], a[16:23], v8\nv_max3_u32 v124, v124, v48, v49\nv_smfmac_f32_16x16x64_bf16 v[56:59], v[48:51], a[48:55], v8\n"
"v_max3_u32 v124, v124, v50, v51\nv_smfmac_f32_16x16x64_bf16 v[60:63], v[48:51], a[80:87], v8\nv_add_f32 v125, v52, v53\nv_add_f32 v126, v54, v55\n"
"v_cvt_pk_bf16_f32 v108, v125, v126\nv_add_f32 v125, v56, v57\nv_add_f32 v126, v58, v59\nv_cvt_pk_bf16_f32 v109, v125, v126\n"
"v_cmp_eq_u32 vcc, -1, v124\nv_add_f32 v125, v60, v61\nv_add_f32 v126, v62, v63\nv_cvt_pk_bf16_f32 v110, v125, v126\n"
"ds_write_b64 v127, v[108:109] offset:256\nds_write_b64 v127, v[110:111] offset:384\ns_cbranch_vccnz retry_h3\ns_waitcnt lgkmcnt(0)\n"
"s_barrier\nds_read_b64_tr_b16 v[84:85], v128 offset:0\nds_read_b64_tr_b16 v[86:87], v128 offset:64\nds_read_b64_tr_b16 v[88:89], v128 offset:256\n"
"ds_read_b64_tr_b16 v[90:91], v128 offset:320\nds_read_b64_tr_b16 v[92:93], v128 offset:512\nds_read_b64_tr_b16 v[94:95], v128 offset:576\nds_read_b64_tr_b16 v[96:97], v128 offset:768\n"
"ds_read_b64_tr_b16 v[98:99], v128 offset:832\nmb_h3:\ns_waitcnt vmcnt(0)\nv_max3_u32 v124, v124, v68, v69\n"
"v_max3_u32 v124, v124, v70, v71\nv_max3_u32 v124, v124, v72, v73\nv_max3_u32 v124, v124, v74, v75\nv_cmp_eq_u32 vcc, -1, v124\n"
"s_waitcnt lgkmcnt(6)\nv_mfma_f32_16x16x32_bf16 v[100:103], v[84:87], v[68:71], 0\ns_waitcnt lgkmcnt(4)\nv_mfma_f32_16x16x32_bf16 v[100:103], v[88:91], v[72:75], v[100:103]\n"
"s_waitcnt lgkmcnt(2)\nv_mfma_f32_16x16x32_bf16 v[100:103], v[92:95], v[68:71], v[100:103]\ns_waitcnt lgkmcnt(0)\nv_mfma_f32_16x16x32_bf16 v[100:103], v[96:99], v[72:75], v[100:103]\n"
"s_cbranch_vccnz mbr_h3\ns_nop 6\nv_add_f32_dpp v120, v102, v100 row_ror:8 row_mask:0xf bank_mask:0x3\nv_add_f32_dpp v120, v100, v102 row_ror:8 row_mask:0xf bank_mask:0xc\n"
"v_add_f32_dpp v121, v103, v101 row_ror:8 row_mask:0xf bank_mask:0x3\nv_add_f32_dpp v121, v101, v103 row_ror:8 row_mask:0xf bank_mask:0xc\nv_cvt_pk_f16_f32 v125, v120, v121\nbuffer_store_dword v125, v16, s[8:11], s41 offen sc1\n"
"s_nop 15\ns_nop 15\ns_nop 15\nbuffer_store_dwordx2 v[120:121], v17, s[8:11], s41 offen sc1\n"
"ds_read_b32 v129, v130\nv_xor_b32 v127, 8192, v127\nv_xor_b32 v128, 8192, v128\ns_waitcnt lgkmcnt(0)\n"
"v_readfirstlane_b32 s47, v129\ns_sub_u32 s47, s47, s40\ns_cmp_eq_u32 s47, 1\ns_cbranch_scc1 adopt_h3\n"
"back_h3:\ns_mul_i32 s48, s46, 8\ns_add_u32 s45, s45, s48\ns_add_u32 s45, s45, s32\n"
"s_lshr_b32 s48, s45, 4\ns_lshl_b32 s48, s48, 2\ns_add_u32 s44, s44, s48\ns_and_b32 s45, s45, 15\n"
"s_add_u32 s52, s40, 1\ns_bitcmp1_b64 s[28:29], s52\ns_subb_u32 s46, s46, 0\ns_add_u32 s40, s40, 1\n"
"s_mov_b32 s43, s41\ns_add_u32 s41, s41, 131072\ns_and_b32 s41, s41, 524287\ns_add_u32 s92, s92, 1\n"
"s_xor_b32 s42, s41, 262144\ns_waitcnt lgkmcnt(0)\ns_cmp_lt_u32 s40, 64\ns_cbranch_scc1 top_h3\n"
"s_branch end_h3\nretry2_h3:\ns_sleep 3\nretry_h3:\n"
"s_add_u32 s53, s53, 1\ns_add_u32 s49, s49, 1\ns_cmp_gt_u32 s49, 65536\ns_cbranch_scc1 end_h3\n"
"buffer_load_dwordx4 v[40:43], v10, s[8:11], s43 offen offset:1024 sc1\nbuffer_load_dwordx4 v[44:47], v10, s[8:11], s43 offen offset:0 sc1\nbuffer_load_dwordx4 v[48:51], v10, s[8:11], s43 offen offset:2048 sc1\nbuffer_load_dwordx4 v[68:71], v11, s[8:11], s43 offen sc1\n"
"buffer_load_dwordx4 v[72:75], v12, s[8:11], s43 offen sc1\nv_mov_b32 v124, 0\ns_add_u32 s47, s40, 1\nv_mov_b32 v129, s47\n"
"ds_write_b32 v130, v129\ns_waitcnt vmcnt(0)\nv_max3_u32 v124, v124, v40, v41\nv_max3_u32 v124, v124, v42, v43\n"
"v_max3_u32 v124, v124, v44, v45\nv_max3_u32 v124, v124, v46, v47\nv_max3_u32 v124, v124, v48, v49\nv_max3_u32 v124, v124, v50, v51\n"
"v_cmp_eq_u32 vcc, -1, v124\nv_mov_b32 v52, 0\nv_mov_b32 v53, 0\nv_mov_b32 v54, 0\n"
"v_mov_b32 v55, 0\nv_mov_b32 v56, 0\nv_mov_b32 v57, 0\nv_mov_b32 v58, 0\n"
"v_mov_b32 v59, 0\nv_mov_b32 v60, 0\nv_mov_b32 v61, 0\nv_mov_b32 v62, 0\n"
"v_mov_b32 v63, 0\ns_cbranch_vccnz retry2_h3\ns_branch landed_h3\nadopt_h3:\n"
"buffer_load_dword v112, v28, s[8:11], 0 offen sc1\ns_lshl_b32 s52, s40, 26\ns_or_b32 s47, s52, 0x3ffffff\ns_waitcnt vmcnt(0)\n"
"v_cmp_ge_u32 vcc, s47, v112\ns_nop 1\nv_cndmask_b32 v129, 0, v112, vcc\ns_nop 1\n"
"v_max_u32_dpp v129, v129, v129 row_ror:8 row_mask:0xf bank_mask:0xf\ns_nop 1\nv_max_u32_dpp v129, v129, v129 row_ror:4 row_mask:0xf bank_mask:0xf\ns_nop 1\n"
"v_max_u32_dpp v129, v129, v129 row_ror:2 row_mask:0xf bank_mask:0xf\ns_nop 1\nv_max_u32_dpp v129, v129, v129 row_ror:1 row_mask:0xf bank_mask:0xf\ns_nop 0\n"
"v_readlane_b32 s47, v129, 0\nv_readlane_b32 s48, v129, 16\ns_max_u32 s48, s48, s47\ns_cmp_le_u32 s48, s52\n"
"s_cbranch_scc1 back_h3\ns_bfe_u32 s52, s48, 1179656\ns_add_u32 s52, s52, s30\ns_sub_u32 s45, s52, s44\n"
"s_and_b32 s45, s45, 3\ns_sub_u32 s44, s52, s45\ns_lshl_b32 s45, s45, 2\ns_and_b32 s46, s48, 255\n"
"s_mov_b32 s92, 0\ns_branch back_h3\nmbr_h3:\ns_add_u32 s53, s53, 1\n"
"s_add_u32 s49, s49, 1\ns_cmp_gt_u32 s49, 65536\ns_cbranch_scc1 end_h3\nbuffer_load_dwordx4 v[68:71], v11, s[8:11], s43 offen sc1\n"
"buffer_load_dwordx4 v[72:75], v12, s[8:11], s43 offen sc1\nv_mov_b32 v124, 0\ns_branch mb_h3\nend_h3:\n"
"s_branch fk_done\nfk_x0:\ns_mov_b32 s40, 0\ns_mov_b32 s41, 0\n"
"s_mov_b32 s42, 262144\ns_mov_b32 s43, 393216\ns_mov_b32 s44, s30\ns_mov_b32 s45, 0\n"
"s_bcnt1_i32_b64 s46, s[28:29]\ns_mov_b32 s92, 99\ns_mov_b32 s49, 0\ns_mov_b32 s53, 0\n"
"s_mov_b32 s50, s36\ns_mov_b32 s51, s37\nv_mov_b32 v81, v13\nv_mov_b32 v82, v14\n"
"v_mov_b32 v84, 24576\ns_add_u32 s51, s50, 14336\nv_mov_b32 v64, -1\nv_mov_b32 v65, -1\n"
"v_mov_b32 v66, -1\nv_mov_b32 v67, -1\nbuffer_load_dwordx4 a[0:3], v9, s[16:19], s50 offen offset:0 sc1\nbuffer_load_dwordx4 a[4:7], v9, s[16:19], s50 offen offset:1024 sc1\n"
"buffer_load_dwordx4 a[8:11], v9, s[16:19], s50 offen offset:2048 sc1\nbuffer_load_dwordx4 a[12:15], v9, s[16:19], s50 offen offset:3072 sc1\nbuffer_load_dwordx4 a[16:19], v9, s[16:19], s51 offen offset:0 sc1\nbuffer_load_dwordx4 a[20:23], v9, s[16:19], s51 offen offset:1024 sc1\n"
"buffer_load_dwordx4 a[24:27], v9, s[16:19], s51 offen offset:2048 sc1\nbuffer_load_dwordx4 a[28:31], v9, s[16:19], s51 offen offset:3072 sc1\ns_add_u32 s50, s50, 28672\ns_add_u32 s51, s51, 28672\n"
"buffer_load_dword v85, v18, s[8:11], 0 offen sc1\ns_waitcnt vmcnt(0)\nf0_x0:\ns_add_u32 s47, s40, 2\n"
"s_min_u32 s47, s47, 64\ns_add_u32 s47, s47, s34\nv_subrev_u32 v85, s47, v85\nv_cmp_lt_u32 vcc, 64, v85\n"
"s_cbranch_vccz f0ok_x0\ns_add_u32 s53, s53, 0\ns_add_u32 s49, s49, 1\ns_cmp_gt_u32 s49, 65536\n"
"s_cbranch_scc1 end_x0\ns_sleep 2\nbuffer_load_dword v85, v18, s[8:11], 0 offen sc1\ns_waitcnt vmcnt(0)\n"
"s_branch f0_x0\nf0ok_x0:\ns_barrier\ns_memrealtime s[54:55]\n"
"s_waitcnt lgkmcnt(0)\ns_add_u32 s44, s54, 16\ns_mov_b32 s45, 8\n.p2align 6\n"
"top_x0:\nv_mov_b32 v48, 0\nv_mov_b32 v49, 0\nv_mov_b32 v50, 0\n"
"v_mov_b32 v51, 0\nv_mov_b32 v52, 0\nv_mov_b32 v53, 0\nv_mov_b32 v54, 0\n"
"v_mov_b32 v55, 0\nv_mov_b32 v78, 0\ns_mul_i32 s48, s45, 6\ns_and_b32 s48, s48, -4\n"
"s_getpc_b64 s[60:61]\nspc_x0:\ns_sub_u32 s48, sgo_x0-spc_x0, s48\ns_add_u32 s60, s60, s48\n"
"s_addc_u32 s61, s61, 0\nsf_x0:\ns_memrealtime s[54:55]\ns_nop 2\n"
"s_memrealtime s[56:57]\ns_nop 2\ns_memrealtime s[58:59]\nsg_x0:\n"
"s_waitcnt lgkmcnt(4)\ns_sub_u32 s47, s54, s44\ns_add_u32 s48, s47, 12\ns_cmp_ge_u32 s48, 12\n"
"s_cbranch_scc1 so_x0\ns_memrealtime s[54:55]\ns_waitcnt lgkmcnt(4)\ns_cmp_ge_u32 s56, s44\n"
"s_cbranch_scc1 sx_x0\ns_memrealtime s[56:57]\ns_waitcnt lgkmcnt(4)\ns_cmp_ge_u32 s58, s44\n"
"s_cbranch_scc1 sx_x0\ns_memrealtime s[58:59]\ns_waitcnt lgkmcnt(4)\ns_cmp_ge_u32 s54, s44\n"
"s_cbranch_scc1 sx_x0\ns_memrealtime s[54:55]\ns_waitcnt lgkmcnt(4)\ns_cmp_ge_u32 s56, s44\n"
"s_cbranch_scc1 sx_x0\ns_memrealtime s[56:57]\ns_waitcnt lgkmcnt(4)\ns_cmp_ge_u32 s58, s44\n"
"s_cbranch_scc1 sx_x0\ns_memrealtime s[58:59]\ns_waitcnt lgkmcnt(4)\ns_cmp_ge_u32 s54, s44\n"
"s_cbranch_scc1 sx_x0\ns_memrealtime s[54:55]\ns_waitcnt lgkmcnt(4)\ns_cmp_ge_u32 s56, s44\n"
"s_cbranch_scc1 sx_x0\ns_memrealtime s[56:57]\ns_waitcnt lgkmcnt(4)\ns_cmp_ge_u32 s58, s44\n"
"s_cbranch_scc1 sx_x0\ns_memrealtime s[58:59]\ns_waitcnt lgkmcnt(4)\ns_cmp_ge_u32 s54, s44\n"
"s_cbranch_scc1 sx_x0\ns_memrealtime s[54:55]\ns_waitcnt lgkmcnt(4)\ns_cmp_ge_u32 s56, s44\n"
"s_cbranch_scc1 sx_x0\ns_memrealtime s[56:57]\ns_waitcnt lgkmcnt(4)\ns_cmp_ge_u32 s58, s44\n"
"s_cbranch_scc1 sx_x0\ns_memrealtime s[58:59]\ns_waitcnt lgkmcnt(4)\ns_cmp_ge_u32 s54, s44\n"
"s_cbranch_scc1 sx_x0\ns_memrealtime s[54:55]\ns_waitcnt lgkmcnt(4)\ns_cmp_ge_u32 s56, s44\n"
"s_cbranch_scc1 sx_x0\ns_memrealtime s[56:57]\ns_waitcnt lgkmcnt(4)\ns_cmp_ge_u32 s58, s44\n"
"s_cbranch_scc1 sx_x0\ns_memrealtime s[58:59]\ns_waitcnt lgkmcnt(4)\ns_cmp_ge_u32 s54, s44\n"
"s_cbranch_scc1 sx_x0\ns_memrealtime s[54:55]\ns_waitcnt lgkmcnt(4)\ns_cmp_ge_u32 s56, s44\n"
"s_cbranch_scc1 sx_x0\ns_memrealtime s[56:57]\ns_branch sx_x0\nso_x0:\n"
"s_waitcnt lgkmcnt(0)\ns_cmp_lt_i32 s47, 0\ns_cbranch_scc1 sf_x0\ns_branch sgo_x0\n"
"sx_x0:\ns_setpc_b64 s[60:61]\ns_nop 0\ns_nop 0\n"
"s_nop 0\ns_nop 0\ns_nop 0\ns_nop 0\n"
"s_nop 0\ns_nop 0\ns_nop 0\ns_nop 0\n"
"s_nop 0\ns_nop 0\ns_nop 0\ns_nop 0\n"
"s_nop 0\ns_nop 0\ns_nop 0\ns_nop 0\n"
"s_nop 0\ns_nop 0\ns_nop 0\ns_nop 0\n"
"s_nop 0\ns_nop 0\nsgo_x0:\nbuffer_load_dwordx4 v[40:43], v10, s[8:11], s43 offen offset:0 sc1\n"
"buffer_load_dwordx4 v[44:47], v10, s[8:11], s43 offen offset:1024 sc1\nlanded_x0:\ns_waitcnt vmcnt(1)\nv_smfmac_f32_16x16x64_bf16 v[48:51], v[40:43], a[0:7], v8\n"
"v_max3_u32 v78, v78, v40, v41\nv_smfmac_f32_16x16x64_bf16 v[52:55], v[40:43], a[16:23], v8\nv_max3_u32 v78, v78, v42, v43\ns_waitcnt vmcnt(0)\n"
"v_smfmac_f32_16x16x64_bf16 v[48:51], v[44:47], a[8:15], v8\nv_max3_u32 v78, v78, v44, v45\nv_smfmac_f32_16x16x64_bf16 v[52:55], v[44:47], a[24:31], v8\nv_max3_u32 v78, v78, v46, v47\n"
"v_cmp_eq_u32 vcc, -1, v78\ns_nop 0\nv_add_f32 v56, v48, v49\nv_add_f32 v57, v50, v51\n"
"s_nop 1\nv_add_f32 v58, v52, v53\nv_add_f32 v59, v54, v55\nds_write2_b32 v81, v56, v57 offset0:0 offset1:40\n"
"ds_write2_b32 v81, v58, v59 offset0:16 offset1:56\ns_cbranch_vccnz retry_x0\nv_mov_b32 v79, 0\ns_nop 1\n"
"v_mov_b32 v79, 0\ns_nop 1\nv_mov_b32 v79, 0\ns_waitcnt lgkmcnt(0)\n"
"s_barrier\nds_read_b64 v[68:69], v82 offset:0\nds_read_b64 v[70:71], v82 offset:1280\nds_read_b64 v[72:73], v82 offset:2560\n"
"ds_read_b64 v[74:75], v82 offset:3840\nv_mov_b32 v79, 0\ns_nop 1\nv_mov_b32 v79, 0\n"
"s_nop 1\nv_mov_b32 v79, 0\ns_nop 1\ns_waitcnt lgkmcnt(2)\n"
"v_pk_add_f32 v[68:69], v[68:69], v[70:71]\ns_waitcnt lgkmcnt(0)\nv_pk_add_f32 v[72:73], v[72:73], v[74:75]\nv_pk_add_f32 v[68:69], v[68:69], v[72:73]\n"
"v_exp_f32 v79, v68\nv_exp_f32 v80, v69\ns_nop 0\nv_cvt_pk_bf16_f32 v79, v79, v80\n"
"buffer_store_dword v79, v16, s[8:11], s41 offen sc1\ns_barrier\nbuffer_load_dwordx4 a[0:3], v9, s[16:19], s50 offen offset:0 sc1\ns_nop 15\n"
"s_nop 7\nbuffer_load_dwordx4 a[4:7], v9, s[16:19], s50 offen offset:1024 sc1\ns_nop 15\ns_nop 7\n"
"buffer_load_dwordx4 a[8:11], v9, s[16:19], s50 offen offset:2048 sc1\ns_nop 15\ns_nop 7\nbuffer_load_dwordx4 a[12:15], v9, s[16:19], s50 offen offset:3072 sc1\n"
"s_nop 15\ns_nop 7\nbuffer_load_dwordx4 a[16:19], v9, s[16:19], s51 offen offset:0 sc1\ns_nop 15\n"
"s_nop 7\nbuffer_load_dwordx4 a[20:23], v9, s[16:19], s51 offen offset:1024 sc1\ns_nop 15\ns_nop 7\n"
"buffer_load_dwordx4 a[24:27], v9, s[16:19], s51 offen offset:2048 sc1\ns_nop 15\ns_nop 7\nbuffer_load_dwordx4 a[28:31], v9, s[16:19], s51 offen offset:3072 sc1\n"
"s_nop 15\ns_nop 7\ns_add_u32 s50, s50, 28672\ns_add_u32 s51, s51, 28672\n"
"buffer_store_dwordx2 v[68:69], v17, s[8:11], s41 offen sc1\nbuffer_store_dwordx4 v[64:67], v15, s[8:11], s42 offen sc1\nds_read_b32 v83, v84\nv_xor_b32 v81, 8192, v81\n"
"v_xor_b32 v82, 8192, v82\ns_waitcnt lgkmcnt(0)\nv_readfirstlane_b32 s47, v83\ns_sub_u32 s47, s47, s40\n"
"s_cmp_eq_u32 s47, 1\ns_cbranch_scc1 adopt_x0\nback_x0:\ns_mul_i32 s48, s46, 8\n"
"s_add_u32 s45, s45, s48\ns_add_u32 s45, s45, s32\ns_lshr_b32 s48, s45, 4\ns_lshl_b32 s48, s48, 2\n"
"s_add_u32 s44, s44, s48\ns_and_b32 s45, s45, 15\ns_add_u32 s52, s40, 1\ns_bitcmp1_b64 s[28:29], s52\n"
"s_subb_u32 s46, s46, 0\ns_add_u32 s40, s40, 1\ns_mov_b32 s43, s41\ns_add_u32 s41, s41, 131072\n"
"s_and_b32 s41, s41, 524287\ns_add_u32 s92, s92, 1\ns_xor_b32 s42, s41, 262144\ns_waitcnt lgkmcnt(0)\n"
"s_cmp_lt_u32 s40, 63\ns_cbranch_scc1 top_x0\ns_add_u32 s52, s44, 16\ns_add_u32 s48, s34, 63\n"
"v_mov_b32 v79, s48\ntw_x0:\ns_memrealtime s[54:55]\ns_waitcnt lgkmcnt(0)\n"
"s_sub_u32 s47, s54, s52\ns_cmp_lt_i32 s47, 0\ns_cbranch_scc1 tw_x0\nbuffer_store_dword v79, v19, s[8:11], 0 offen sc1\n"
"s_branch end_x0\nretry2_x0:\ns_sleep 3\nretry_x0:\n"
"s_add_u32 s53, s53, 1\ns_add_u32 s49, s49, 1\ns_cmp_gt_u32 s49, 65536\ns_cbranch_scc1 end_x0\n"
"buffer_load_dwordx4 v[40:43], v10, s[8:11], s43 offen offset:0 sc1\nbuffer_load_dwordx4 v[44:47], v10, s[8:11], s43 offen offset:1024 sc1\nv_mov_b32 v78, 0\ns_add_u32 s47, s40, 1\n"
"v_mov_b32 v83, s47\nds_write_b32 v84, v83\ns_waitcnt vmcnt(0)\nv_max3_u32 v78, v78, v40, v41\n"
"v_max3_u32 v78, v78, v42, v43\nv_max3_u32 v78, v78, v44, v45\nv_max3_u32 v78, v78, v46, v47\nv_cmp_eq_u32 vcc, -1, v78\n"
"v_mov_b32 v48, 0\nv_mov_b32 v49, 0\nv_mov_b32 v50, 0\nv_mov_b32 v51, 0\n"
"v_mov_b32 v52, 0\nv_mov_b32 v53, 0\nv_mov_b32 v54, 0\nv_mov_b32 v55, 0\n"
"s_cbranch_vccnz retry2_x0\ns_branch landed_x0\nadopt_x0:\nbuffer_load_dword v60, v28, s[8:11], 0 offen sc1\n"
"s_lshl_b32 s52, s40, 26\ns_or_b32 s47, s52, 0x3ffffff\ns_waitcnt vmcnt(0)\nv_cmp_ge_u32 vcc, s47, v60\n"
"s_nop 1\nv_cndmask_b32 v83, 0, v60, vcc\ns_nop 1\nv_max_u32_dpp v83, v83, v83 row_ror:8 row_mask:0xf bank_mask:0xf\n"
"s_nop 1\nv_max_u32_dpp v83, v83, v83 row_ror:4 row_mask:0xf bank_mask:0xf\ns_nop 1\nv_max_u32_dpp v83, v83, v83 row_ror:2 row_mask:0xf bank_mask:0xf\n"
"s_nop 1\nv_max_u32_dpp v83, v83, v83 row_ror:1 row_mask:0xf bank_mask:0xf\ns_nop 0\nv_readlane_b32 s47, v83, 0\n"
"v_readlane_b32 s48, v83, 16\ns_max_u32 s48, s48, s47\ns_cmp_le_u32 s48, s52\ns_cbranch_scc1 back_x0\n"
"s_bfe_u32 s52, s48, 1179656\ns_add_u32 s52, s52, s30\ns_sub_u32 s45, s52, s44\ns_and_b32 s45, s45, 3\n"
"s_sub_u32 s44, s52, s45\ns_lshl_b32 s45, s45, 2\ns_and_b32 s46, s48, 255\ns_mov_b32 s92, 0\n"
"s_branch back_x0\nend_x0:\ns_branch fk_done\nfk_x1:\n"
"s_mov_b32 s40, 0\ns_mov_b32 s41, 0\ns_mov_b32 s42, 262144\ns_mov_b32 s43, 393216\n"
"s_mov_b32 s44, s30\ns_mov_b32 s45, 0\ns_bcnt1_i32_b64 s46, s[28:29]\ns_mov_b32 s92, 99\n"
"s_mov_b32 s49, 0\ns_mov_b32 s53, 0\ns_mov_b32 s50, s36\ns_mov_b32 s51, s37\n"
"v_mov_b32 v81, v13\nv_mov_b32 v82, v14\nv_mov_b32 v84, 24576\ns_add_u32 s51, s50, 14336\n"
"v_mov_b32 v64, -1\nv_mov_b32 v65, -1\nv_mov_b32 v66, -1\nv_mov_b32 v67, -1\n"
"buffer_load_dwordx4 a[8:11], v9, s[16:19], s50 offen offset:2048 sc1\nbuffer_load_dwordx4 a[12:15], v9, s[16:19], s50 offen offset:3072 sc1\nbuffer_load_dwordx4 a[0:3], v9, s[16:19], s50 offen offset:0 sc1\nbuffer_load_dwordx4 a[4:7], v9, s[16:19], s50 offen offset:1024 sc1\n"
"buffer_load_dwordx4 a[24:27], v9, s[16:19], s51 offen offset:2048 sc1\nbuffer_load_dwordx4 a[28:31], v9, s[16:19], s51 offen offset:3072 sc1\nbuffer_load_dwordx4 a[16:19], v9, s[16:19], s51 offen offset:0 sc1\nbuffer_load_dwordx4 a[20:23], v9, s[16:19], s51 offen offset:1024 sc1\n"
"s_add_u32 s50, s50, 28672\ns_add_u32 s51, s51, 28672\nbuffer_load_dword v85, v18, s[8:11], 0 offen sc1\ns_waitcnt vmcnt(0)\n"
"f0_x1:\ns_add_u32 s47, s40, 2\ns_min_u32 s47, s47, 64\ns_add_u32 s47, s47, s34\n"
"v_subrev_u32 v85, s47, v85\nv_cmp_lt_u32 vcc, 64, v85\ns_cbranch_vccz f0ok_x1\ns_add_u32 s53, s53, 0\n"
"s_add_u32 s49, s49, 1\ns_cmp_gt_u32 s49, 65536\ns_cbranch_scc1 end_x1\ns_sleep 2\n"
"buffer_load_dword v85, v18, s[8:11], 0 offen sc1\ns_waitcnt vmcnt(0)\ns_branch f0_x1\nf0ok_x1:\n"
"s_barrier\ns_memrealtime s[54:55]\ns_waitcnt lgkmcnt(0)\ns_add_u32 s44, s54, 16\n"
"s_mov_b32 s45, 8\n.p2align 6\ntop_x1:\nv_mov_b32 v48, 0\n"
"v_mov_b32 v49, 0\nv_mov_b32 v50, 0\nv_mov_b32 v51, 0\nv_mov_b32 v52, 0\n"
"v_mov_b32 v53, 0\nv_mov_b32 v54, 0\nv_mov_b32 v55, 0\nv_mov_b32 v78, 0\n"
"s_mul_i32 s48, s45, 6\ns_and_b32 s48, s48, -4\ns_getpc_b64 s[60:61]\nspc_x1:\n"
"s_sub_u32 s48, sgo_x1-spc_x1, s48\ns_add_u32 s60, s60, s48\ns_addc_u32 s61, s61, 0\nsf_x1:\n"
"s_memrealtime s[54:55]\ns_nop 2\ns_memrealtime s[56:57]\ns_nop 2\n"
"s_memrealtime s[58:59]\nsg_x1:\ns_waitcnt lgkmcnt(4)\ns_sub_u32 s47, s54, s44\n"
"s_add_u32 s48, s47, 12\ns_cmp_ge_u32 s48, 12\ns_cbranch_scc1 so_x1\ns_memrealtime s[54:55]\n"
"s_waitcnt lgkmcnt(4)\ns_cmp_ge_u32 s56, s44\ns_cbranch_scc1 sx_x1\ns_memrealtime s[56:57]\n"
"s_waitcnt lgkmcnt(4)\ns_cmp_ge_u32 s58, s44\ns_cbranch_scc1 sx_x1\ns_memrealtime s[58:59]\n"
"s_waitcnt lgkmcnt(4)\ns_cmp_ge_u32 s54, s44\ns_cbranch_scc1 sx_x1\ns_memrealtime s[54:55]\n"
"s_waitcnt lgkmcnt(4)\ns_cmp_ge_u32 s56, s44\ns_cbranch_scc1 sx_x1\ns_memrealtime s[56:57]\n"
"s_waitcnt lgkmcnt(4)\ns_cmp_ge_u32 s58, s44\ns_cbranch_scc1 sx_x1\ns_memrealtime s[58:59]\n"
"s_waitcnt lgkmcnt(4)\ns_cmp_ge_u32 s54, s44\ns_cbranch_scc1 sx_x1\ns_memrealtime s[54:55]\n"
"s_waitcnt lgkmcnt(4)\ns_cmp_ge_u32 s56, s44\ns_cbranch_scc1 sx_x1\ns_memrealtime s[56:57]\n"
"s_waitcnt lgkmcnt(4)\ns_cmp_ge_u32 s58, s44\ns_cbranch_scc1 sx_x1\ns_memrealtime s[58:59]\n"
"s_waitcnt lgkmcnt(4)\ns_cmp_ge_u32 s54, s44\ns_cbranch_scc1 sx_x1\ns_memrealtime s[54:55]\n"
"s_waitcnt lgkmcnt(4)\ns_cmp_ge_u32 s56, s44\ns_cbranch_scc1 sx_x1\ns_memrealtime s[56:57]\n"
"s_waitcnt lgkmcnt(4)\ns_cmp_ge_u32 s58, s44\ns_cbranch_scc1 sx_x1\ns_memrealtime s[58:59]\n"
"s_waitcnt lgkmcnt(4)\ns_cmp_ge_u32 s54, s44\ns_cbranch_scc1 sx_x1\ns_memrealtime s[54:55]\n"
"s_waitcnt lgkmcnt(4)\ns_cmp_ge_u32 s56, s44\ns_cbranch_scc1 sx_x1\ns_memrealtime s[56:57]\n"
"s_waitcnt lgkmcnt(4)\ns_cmp_ge_u32 s58, s44\ns_cbranch_scc1 sx_x1\ns_memrealtime s[58:59]\n"
"s_waitcnt lgkmcnt(4)\ns_cmp_ge_u32 s54, s44\ns_cbranch_scc1 sx_x1\ns_memrealtime s[54:55]\n"
"s_waitcnt lgkmcnt(4)\ns_cmp_ge_u32 s56, s44\ns_cbranch_scc1 sx_x1\ns_memrealtime s[56:57]\n"
"s_branch sx_x1\nso_x1:\ns_waitcnt lgkmcnt(0)\ns_cmp_lt_i32 s47, 0\n"
"s_cbranch_scc1 sf_x1\ns_branch sgo_x1\nsx_x1:\ns_setpc_b64 s[60:61]\n"
"s_nop 0\ns_nop 0\ns_nop 0\ns_nop 0\n"
"s_nop 0\ns_nop 0\ns_nop 0\ns_nop 0\n"
"s_nop 0\ns_nop 0\ns_nop 0\ns_nop 0\n"
"s_nop 0\ns_nop 0\ns_nop 0\ns_nop 0\n"
"s_nop 0\ns_nop 0\ns_nop 0\ns_nop 0\n"
"s_nop 0\ns_nop 0\ns_nop 0\ns_nop 0\n"
"sgo_x1:\ns_nop 5\nbuffer_load_dwordx4 v[40:43], v10, s[8:11], s43 offen offset:1024 sc1\nbuffer_load_dwordx4 v[44:47], v10, s[8:11], s43 offen offset:0 sc1\n"
"landed_x1:\ns_waitcnt vmcnt(1)\nv_smfmac_f32_16x16x64_bf16 v[48:51], v[40:43], a[8:15], v8\nv_max3_u32 v78, v78, v40, v41\n"
"v_smfmac_f32_16x16x64_bf16 v[52:55], v[40:43], a[24:31], v8\nv_max3_u32 v78, v78, v42, v43\ns_waitcnt vmcnt(0)\nv_smfmac_f32_16x16x64_bf16 v[48:51], v[44:47], a[0:7], v8\n"
"v_max3_u32 v78, v78, v44, v45\nv_smfmac_f32_16x16x64_bf16 v[52:55], v[44:47], a[16:23], v8\nv_max3_u32 v78, v78, v46, v47\nv_cmp_eq_u32 vcc, -1, v78\n"
"s_nop 0\nv_add_f32 v56, v48, v49\nv_add_f32 v57, v50, v51\ns_nop 1\n"
"v_add_f32 v58, v52, v53\nv_add_f32 v59, v54, v55\nds_write2_b32 v81, v56, v57 offset0:0 offset1:40\nds_write2_b32 v81, v58, v59 offset0:16 offset1:56\n"
"s_cbranch_vccnz retry_x1\nv_mov_b32 v79, 0\ns_nop 1\nv_mov_b32 v79, 0\n"
"s_nop 1\nv_mov_b32 v79, 0\ns_waitcnt lgkmcnt(0)\ns_barrier\n"
"ds_read_b64 v[68:69], v82 offset:0\nds_read_b64 v[70:71], v82 offset:1280\nds_read_b64 v[72:73], v82 offset:2560\nds_read_b64 v[74:75], v82 offset:3840\n"
"v_mov_b32 v79, 0\ns_nop 1\nv_mov_b32 v79, 0\ns_nop 1\n"
"v_mov_b32 v79, 0\ns_nop 1\ns_waitcnt lgkmcnt(2)\nv_pk_add_f32 v[68:69], v[68:69], v[70:71]\n"
"s_waitcnt lgkmcnt(0)\nv_pk_add_f32 v[72:73], v[72:73], v[74:75]\nv_pk_add_f32 v[68:69], v[68:69], v[72:73]\nv_exp_f32 v79, v68\n"
"v_exp_f32 v80, v69\ns_nop 0\nv_cvt_pk_bf16_f32 v79, v79, v80\nbuffer_store_dword v79, v16, s[8:11], s41 offen sc1\n"
"s_barrier\nbuffer_load_dwordx4 a[8:11], v9, s[16:19], s50 offen offset:2048 sc1\ns_nop 15\ns_nop 7\n"
"buffer_load_dwordx4 a[12:15], v9, s[16:19], s50 offen offset:3072 sc1\ns_nop 15\ns_nop 7\nbuffer_load_dwordx4 a[0:3], v9, s[16:19], s50 offen offset:0 sc1\n"
"s_nop 15\ns_nop 7\nbuffer_load_dwordx4 a[4:7], v9, s[16:19], s50 offen offset:1024 sc1\ns_nop 15\n"
"s_nop 7\nbuffer_load_dwordx4 a[24:27], v9, s[16:19], s51 offen offset:2048 sc1\ns_nop 15\ns_nop 7\n"
"buffer_load_dwordx4 a[28:31], v9, s[16:19], s51 offen offset:3072 sc1\ns_nop 15\ns_nop 7\nbuffer_load_dwordx4 a[16:19], v9, s[16:19], s51 offen offset:0 sc1\n"
"s_nop 15\ns_nop 7\nbuffer_load_dwordx4 a[20:23], v9, s[16:19], s51 offen offset:1024 sc1\ns_nop 15\n"
"s_nop 7\ns_add_u32 s50, s50, 28672\ns_add_u32 s51, s51, 28672\nbuffer_store_dwordx2 v[68:69], v17, s[8:11], s41 offen sc1\n"
"buffer_store_dwordx4 v[64:67], v15, s[8:11], s42 offen sc1\nds_read_b32 v83, v84\nv_xor_b32 v81, 8192, v81\nv_xor_b32 v82, 8192, v82\n"
"s_waitcnt lgkmcnt(0)\nv_readfirstlane_b32 s47, v83\ns_sub_u32 s47, s47, s40\ns_cmp_eq_u32 s47, 1\n"
"s_cbranch_scc1 adopt_x1\nback_x1:\ns_mul_i32 s48, s46, 8\ns_add_u32 s45, s45, s48\n"
"s_add_u32 s45, s45, s32\ns_lshr_b32 s48, s45, 4\ns_lshl_b32 s48, s48, 2\ns_add_u32 s44, s44, s48\n"
"s_and_b32 s45, s45, 15\ns_add_u32 s52, s40, 1\ns_bitcmp1_b64 s[28:29], s52\ns_subb_u32 s46, s46, 0\n"
"s_add_u32 s40, s40, 1\ns_mov_b32 s43, s41\ns_add_u32 s41, s41, 131072\ns_and_b32 s41, s41, 524287\n"
"s_add_u32 s92, s92, 1\ns_xor_b32 s42, s41, 262144\ns_waitcnt lgkmcnt(0)\ns_cmp_lt_u32 s40, 63\n"
"s_cbranch_scc1 top_x1\ns_branch end_x1\nretry2_x1:\ns_sleep 3\n"
"retry_x1:\ns_add_u32 s53, s53, 1\ns_add_u32 s49, s49, 1\ns_cmp_gt_u32 s49, 65536\n"
"s_cbranch_scc1 end_x1\nbuffer_load_dwordx4 v[40:43], v10, s[8:11], s43 offen offset:1024 sc1\nbuffer_load_dwordx4 v[44:47], v10, s[8:11], s43 offen offset:0 sc1\nv_mov_b32 v78, 0\n"
"s_add_u32 s47, s40, 1\nv_mov_b32 v83, s47\nds_write_b32 v84, v83\ns_waitcnt vmcnt(0)\n"
"v_max3_u32 v78, v78, v40, v41\nv_max3_u32 v78, v78, v42, v43\nv_max3_u32 v78, v78, v44, v45\nv_max3_u32 v78, v78, v46, v47\n"
"v_cmp_eq_u32 vcc, -1, v78\nv_mov_b32 v48, 0\nv_mov_b32 v49, 0\nv_mov_b32 v50, 0\n"
"v_mov_b32 v51, 0\nv_mov_b32 v52, 0\nv_mov_b32 v53, 0\nv_mov_b32 v54, 0\n"
"v_mov_b32 v55, 0\ns_cbranch_vccnz retry2_x1\ns_branch landed_x1\nadopt_x1:\n"
"buffer_load_dword v60, v28, s[8:11], 0 offen sc1\ns_lshl_b32 s52, s40, 26\ns_or_b32 s47, s52, 0x3ffffff\ns_waitcnt vmcnt(0)\n"
"v_cmp_ge_u32 vcc, s47, v60\ns_nop 1\nv_cndmask_b32 v83, 0, v60, vcc\ns_nop 1\n"
"v_max_u32_dpp v83, v83, v83 row_ror:8 row_mask:0xf bank_mask:0xf\ns_nop 1\nv_max_u32_dpp v83, v83, v83 row_ror:4 row_mask:0xf bank_mask:0xf\ns_nop 1\n"
"v_max_u32_dpp v83, v83, v83 row_ror:2 row_mask:0xf bank_mask:0xf\ns_nop 1\nv_max_u32_dpp v83, v83, v83 row_ror:1 row_mask:0xf bank_mask:0xf\ns_nop 0\n"
"v_readlane_b32 s47, v83, 0\nv_readlane_b32 s48, v83, 16\ns_max_u32 s48, s48, s47\ns_cmp_le_u32 s48, s52\n"
"s_cbranch_scc1 back_x1\ns_bfe_u32 s52, s48, 1179656\ns_add_u32 s52, s52, s30\ns_sub_u32 s45, s52, s44\n"
"s_and_b32 s45, s45, 3\ns_sub_u32 s44, s52, s45\ns_lshl_b32 s45, s45, 2\ns_and_b32 s46, s48, 255\n"
"s_mov_b32 s92, 0\ns_branch back_x1\nend_x1:\ns_branch fk_done\n"
"fk_x2:\ns_mov_b32 s40, 0\ns_mov_b32 s41, 0\ns_mov_b32 s42, 262144\n"
"s_mov_b32 s43, 393216\ns_mov_b32 s44, s30\ns_mov_b32 s45, 0\ns_bcnt1_i32_b64 s46, s[28:29]\n"
"s_mov_b32 s92, 99\ns_mov_b32 s49, 0\ns_mov_b32 s53, 0\ns_mov_b32 s50, s36\n"
"s_mov_b32 s51, s37\nv_mov_b32 v81, v13\nv_mov_b32 v82, v14\nv_mov_b32 v84, 24576\n"
"s_add_u32 s51, s50, 14336\nv_mov_b32 v64, -1\nv_mov_b32 v65, -1\nv_mov_b32 v66, -1\n"
"v_mov_b32 v67, -1\nbuffer_load_dwordx4 a[0:3], v9, s[16:19], s50 offen offset:0 sc1\nbuffer_load_dwordx4 a[4:7], v9, s[16:19], s50 offen offset:1024 sc1\nbuffer_load_dwordx4 a[8:11], v9, s[16:19], s50 offen offset:2048 sc1\n"
"buffer_load_dwordx4 a[12:15], v9, s[16:19], s50 offen offset:3072 sc1\nbuffer_load_dwordx4 a[16:19], v9, s[16:19], s51 offen offset:0 sc1\nbuffer_load_dwordx4 a[20:23], v9, s[16:19], s51 offen offset:1024 sc1\nbuffer_load_dwordx4 a[24:27], v9, s[16:19], s51 offen offset:2048 sc1\n"
"buffer_load_dwordx4 a[28:31], v9, s[16:19], s51 offen offset:3072 sc1\ns_add_u32 s50, s50, 28672\ns_add_u32 s51, s51, 28672\nbuffer_load_dword v85, v18, s[8:11], 0 offen sc1\n"
"s_waitcnt vmcnt(0)\nf0_x2:\ns_add_u32 s47, s40, 2\ns_min_u32 s47, s47, 64\n"
"s_add_u32 s47, s47, s34\nv_subrev_u32 v85, s47, v85\nv_cmp_lt_u32 vcc, 64, v85\ns_cbranch_vccz f0ok_x2\n"
"s_add_u32 s53, s53, 0\ns_add_u32 s49, s49, 1\ns_cmp_gt_u32 s49, 65536\ns_cbranch_scc1 end_x2\n"
"s_sleep 2\nbuffer_load_dword v85, v18, s[8:11], 0 offen sc1\ns_waitcnt vmcnt(0)\ns_branch f0_x2\n"
"f0ok_x2:\ns_barrier\ns_memrealtime s[54:55]\ns_waitcnt lgkmcnt(0)\n"
"s_add_u32 s44, s54, 16\ns_mov_b32 s45, 8\n.p2align 6\ntop_x2:\n"
"v_mov_b32 v48, 0\nv_mov_b32 v49, 0\nv_mov_b32 v50, 0\nv_mov_b32 v51, 0\n"
"v_mov_b32 v52, 0\nv_mov_b32 v53, 0\nv_mov_b32 v54, 0\nv_mov_b32 v55, 0\n"
"v_mov_b32 v78, 0\ns_mul_i32 s48, s45, 6\ns_and_b32 s48, s48, -4\ns_getpc_b64 s[60:61]\n"
"spc_x2:\ns_sub_u32 s48, sgo_x2-spc_x2, s48\ns_add_u32 s60, s60, s48\ns_addc_u32 s61, s61, 0\n"
"sf_x2:\ns_memrealtime s[54:55]\ns_nop 2\ns_memrealtime s[56:57]\n"
"s_nop 2\ns_memrealtime s[58:59]\nsg_x2:\ns_waitcnt lgkmcnt(4)\n"
"s_sub_u32 s47, s54, s44\ns_add_u32 s48, s47, 12\ns_cmp_ge_u32 s48, 12\ns_cbranch_scc1 so_x2\n"
"s_memrealtime s[54:55]\ns_waitcnt lgkmcnt(4)\ns_cmp_ge_u32 s56, s44\ns_cbranch_scc1 sx_x2\n"
"s_memrealtime s[56:57]\ns_waitcnt lgkmcnt(4)\ns_cmp_ge_u32 s58, s44\ns_cbranch_scc1 sx_x2\n"
"s_memrealtime s[58:59]\ns_waitcnt lgkmcnt(4)\ns_cmp_ge_u32 s54, s44\ns_cbranch_scc1 sx_x2\n"
"s_memrealtime s[54:55]\ns_waitcnt lgkmcnt(4)\ns_cmp_ge_u32 s56, s44\ns_cbranch_scc1 sx_x2\n"
"s_memrealtime s[56:57]\ns_waitcnt lgkmcnt(4)\ns_cmp_ge_u32 s58, s44\ns_cbranch_scc1 sx_x2\n"
"s_memrealtime s[58:59]\ns_waitcnt lgkmcnt(4)\ns_cmp_ge_u32 s54, s44\ns_cbranch_scc1 sx_x2\n"
"s_memrealtime s[54:55]\ns_waitcnt lgkmcnt(4)\ns_cmp_ge_u32 s56, s44\ns_cbranch_scc1 sx_x2\n"
"s_memrealtime s[56:57]\ns_waitcnt lgkmcnt(4)\ns_cmp_ge_u32 s58, s44\ns_cbranch_scc1 sx_x2\n"
"s_memrealtime s[58:59]\ns_waitcnt lgkmcnt(4)\ns_cmp_ge_u32 s54, s44\ns_cbranch_scc1 sx_x2\n"
"s_memrealtime s[54:55]\ns_waitcnt lgkmcnt(4)\ns_cmp_ge_u32 s56, s44\ns_cbranch_scc1 sx_x2\n"
"s_memrealtime s[56:57]\ns_waitcnt lgkmcnt(4)\ns_cmp_ge_u32 s58, s44\ns_cbranch_scc1 sx_x2\n"
"s_memrealtime s[58:59]\ns_waitcnt lgkmcnt(4)\ns_cmp_ge_u32 s54, s44\ns_cbranch_scc1 sx_x2\n"
"s_memrealtime s[54:55]\ns_waitcnt lgkmcnt(4)\ns_cmp_ge_u32 s56, s44\ns_cbranch_scc1 sx_x2\n"
"s_memrealtime s[56:57]\ns_waitcnt lgkmcnt(4)\ns_cmp_ge_u32 s58, s44\ns_cbranch_scc1 sx_x2\n"
"s_memrealtime s[58:59]\ns_waitcnt lgkmcnt(4)\ns_cmp_ge_u32 s54, s44\ns_cbranch_scc1 sx_x2\n"
"s_memrealtime s[54:55]\ns_waitcnt lgkmcnt(4)\ns_cmp_ge_u32 s56, s44\ns_cbranch_scc1 sx_x2\n"
"s_memrealtime s[56:57]\ns_branch sx_x2\nso_x2:\ns_waitcnt lgkmcnt(0)\n"
"s_cmp_lt_i32 s47, 0\ns_cbranch_scc1 sf_x2\ns_branch sgo_x2\nsx_x2:\n"
"s_setpc_b64 s[60:61]\ns_nop 0\ns_nop 0\ns_nop 0\n"
"s_nop 0\ns_nop 0\ns_nop 0\ns_nop 0\n"
"s_nop 0\ns_nop 0\ns_nop 0\ns_nop 0\n"
"s_nop 0\ns_nop 0\ns_nop 0\ns_nop 0\n"
"s_nop 0\ns_nop 0\ns_nop 0\ns_nop 0\n"
"s_nop 0\ns_nop 0\ns_nop 0\ns_nop 0\n"
"s_nop 0\nsgo_x2:\ns_nop 11\nbuffer_load_dwordx4 v[40:43], v10, s[8:11], s43 offen offset:0 sc1\n"
"buffer_load_dwordx4 v[44:47], v10, s[8:11], s43 offen offset:1024 sc1\nv_mov_b32 v79, 0\ns_nop 1\nv_mov_b32 v79, 0\n"
"s_nop 1\nv_mov_b32 v79, 0\ns_nop 1\nv_mov_b32 v79, 0\n"
"s_nop 1\nv_mov_b32 v79, 0\ns_nop 1\nv_mov_b32 v79, 0\n"
"s_nop 1\nv_mov_b32 v79, 0\ns_nop 1\nv_mov_b32 v79, 0\n"
"s_nop 1\nv_mov_b32 v79, 0\ns_nop 1\nv_mov_b32 v79, 0\n"
"s_nop 1\nv_mov_b32 v79, 0\ns_nop 1\nv_mov_b32 v79, 0\n"
"s_nop 1\nv_mov_b32 v79, 0\ns_nop 1\nv_mov_b32 v79, 0\n"
"s_nop 1\nv_mov_b32 v79, 0\ns_nop 1\nv_mov_b32 v79, 0\n"
"s_nop 1\nv_mov_b32 v79, 0\ns_nop 1\nv_mov_b32 v79, 0\n"
"s_nop 1\nv_mov_b32 v79, 0\ns_nop 1\nv_mov_b32 v79, 0\n"
"s_nop 1\nv_mov_b32 v79, 0\ns_nop 1\nv_mov_b32 v79, 0\n"
"s_nop 1\nv_mov_b32 v79, 0\ns_nop 1\nv_mov_b32 v79, 0\n"
"s_nop 1\nv_mov_b32 v79, 0\ns_nop 1\nv_mov_b32 v79, 0\n"
"s_nop 1\nv_mov_b32 v79, 0\ns_nop 1\nv_mov_b32 v79, 0\n"
"s_nop 1\nv_mov_b32 v79, 0\nlanded_x2:\ns_waitcnt vmcnt(1)\n"
"v_smfmac_f32_16x16x64_bf16 v[48:51], v[40:43], a[0:7], v8\nv_max3_u32 v78, v78, v40, v41\nv_smfmac_f32_16x16x64_bf16 v[52:55], v[40:43], a[16:23], v8\nv_max3_u32 v78, v78, v42, v43\n"
"s_waitcnt vmcnt(0)\nv_smfmac_f32_16x16x64_bf16 v[48:51], v[44:47], a[8:15], v8\nv_max3_u32 v78, v78, v44, v45\nv_smfmac_f32_16x16x64_bf16 v[52:55], v[44:47], a[24:31], v8\n"
"v_max3_u32 v78, v78, v46, v47\nv_cmp_eq_u32 vcc, -1, v78\ns_nop 0\nv_add_f32 v56, v48, v49\n"
"v_add_f32 v57, v50, v51\ns_nop 1\nv_add_f32 v58, v52, v53\nv_add_f32 v59, v54, v55\n"
"ds_write2_b32 v81, v56, v57 offset0:0 offset1:40\nds_write2_b32 v81, v58, v59 offset0:16 offset1:56\ns_cbranch_vccnz retry_x2\nv_mov_b32 v79, 0\n"
"s_nop 1\nv_mov_b32 v79, 0\ns_nop 1\nv_mov_b32 v79, 0\n"
"s_waitcnt lgkmcnt(0)\ns_barrier\nds_read_b64 v[68:69], v82 offset:0\nds_read_b64 v[70:71], v82 offset:1280\n"
"ds_read_b64 v[72:73], v82 offset:2560\nds_read_b64 v[74:75], v82 offset:3840\nv_mov_b32 v79, 0\ns_nop 1\n"
"v_mov_b32 v79, 0\ns_nop 1\nv_mov_b32 v79, 0\ns_nop 1\n"
"s_waitcnt lgkmcnt(2)\nv_pk_add_f32 v[68:69], v[68:69], v[70:71]\ns_waitcnt lgkmcnt(0)\nv_pk_add_f32 v[72:73], v[72:73], v[74:75]\n"
"v_pk_add_f32 v[68:69], v[68:69], v[72:73]\nv_exp_f32 v79, v68\nv_exp_f32 v80, v69\ns_nop 0\n"
"v_cvt_pk_bf16_f32 v79, v79, v80\nbuffer_store_dword v79, v16, s[8:11], s41 offen sc1\ns_barrier\nbuffer_load_dwordx4 a[0:3], v9, s[16:19], s50 offen offset:0 sc1\n"
"s_nop 15\ns_nop 7\nbuffer_load_dwordx4 a[4:7], v9, s[16:19], s50 offen offset:1024 sc1\ns_nop 15\n"
"s_nop 7\nbuffer_load_dwordx4 a[8:11], v9, s[16:19], s50 offen offset:2048 sc1\ns_nop 15\ns_nop 7\n"
"buffer_load_dwordx4 a[12:15], v9, s[16:19], s50 offen offset:3072 sc1\ns_nop 15\ns_nop 7\nbuffer_load_dwordx4 a[16:19], v9, s[16:19], s51 offen offset:0 sc1\n"
"s_nop 15\ns_nop 7\nbuffer_load_dwordx4 a[20:23], v9, s[16:19], s51 offen offset:1024 sc1\ns_nop 15\n"
"s_nop 7\nbuffer_load_dwordx4 a[24:27], v9, s[16:19], s51 offen offset:2048 sc1\ns_nop 15\ns_nop 7\n"
"buffer_load_dwordx4 a[28:31], v9, s[16:19], s51 offen offset:3072 sc1\ns_nop 15\ns_nop 7\ns_add_u32 s50, s50, 28672\n"
"s_add_u32 s51, s51, 28672\nbuffer_store_dwordx2 v[68:69], v17, s[8:11], s41 offen sc1\nbuffer_store_dwordx4 v[64:67], v15, s[8:11], s42 offen sc1\nds_read_b32 v83, v84\n"
"v_xor_b32 v81, 8192, v81\nv_xor_b32 v82, 8192, v82\ns_waitcnt lgkmcnt(0)\nv_readfirstlane_b32 s47, v83\n"
"s_sub_u32 s47, s47, s40\ns_cmp_eq_u32 s47, 1\ns_cbranch_scc1 adopt_x2\nback_x2:\n"
"s_mul_i32 s48, s46, 8\ns_add_u32 s45, s45, s48\ns_add_u32 s45, s45, s32\ns_lshr_b32 s48, s45, 4\n"
"s_lshl_b32 s48, s48, 2\ns_add_u32 s44, s44, s48\ns_and_b32 s45, s45, 15\ns_add_u32 s52, s40, 1\n"
"s_bitcmp1_b64 s[28:29], s52\ns_subb_u32 s46, s46, 0\ns_add_u32 s40, s40, 1\ns_mov_b32 s43, s41\n"
"s_add_u32 s41, s41, 131072\ns_and_b32 s41, s41, 524287\ns_add_u32 s92, s92, 1\ns_xor_b32 s42, s41, 262144\n"
"s_waitcnt lgkmcnt(0)\ns_cmp_lt_u32 s40, 63\ns_cbranch_scc1 top_x2\ns_branch end_x2\n"
"retry2_x2:\ns_sleep 3\nretry_x2:\ns_add_u32 s53, s53, 1\n"
"s_add_u32 s49, s49, 1\ns_cmp_gt_u32 s49, 65536\ns_cbranch_scc1 end_x2\nbuffer_load_dwordx4 v[40:43], v10, s[8:11], s43 offen offset:0 sc1\n"
"buffer_load_dwordx4 v[44:47], v10, s[8:11], s43 offen offset:1024 sc1\nv_mov_b32 v78, 0\ns_add_u32 s47, s40, 1\nv_mov_b32 v83, s47\n"
"ds_write_b32 v84, v83\ns_waitcnt vmcnt(0)\nv_max3_u32 v78, v78, v40, v41\nv_max3_u32 v78, v78, v42, v43\n"
"v_max3_u32 v78, v78, v44, v45\nv_max3_u32 v78, v78, v46, v47\nv_cmp_eq_u32 vcc, -1, v78\nv_mov_b32 v48, 0\n"
"v_mov_b32 v49, 0\nv_mov_b32 v50, 0\nv_mov_b32 v51, 0\nv_mov_b32 v52, 0\n"
"v_mov_b32 v53, 0\nv_mov_b32 v54, 0\nv_mov_b32 v55, 0\ns_cbranch_vccnz retry2_x2\n"
"s_branch landed_x2\nadopt_x2:\nbuffer_load_dword v60, v28, s[8:11], 0 offen sc1\ns_lshl_b32 s52, s40, 26\n"
"s_or_b32 s47, s52, 0x3ffffff\ns_waitcnt vmcnt(0)\nv_cmp_ge_u32 vcc, s47, v60\ns_nop 1\n"
"v_cndmask_b32 v83, 0, v60, vcc\ns_nop 1\nv_max_u32_dpp v83, v83, v83 row_ror:8 row_mask:0xf bank_mask:0xf\ns_nop 1\n"
"v_max_u32_dpp v83, v83, v83 row_ror:4 row_mask:0xf bank_mask:0xf\ns_nop 1\nv_max_u32_dpp v83, v83, v83 row_ror:2 row_mask:0xf bank_mask:0xf\ns_nop 1\n"
"v_max_u32_dpp v83, v83, v83 row_ror:1 row_mask:0xf bank_mask:0xf\ns_nop 0\nv_readlane_b32 s47, v83, 0\nv_readlane_b32 s48, v83, 16\n"
"s_max_u32 s48, s48, s47\ns_cmp_le_u32 s48, s52\ns_cbranch_scc1 back_x2\ns_bfe_u32 s52, s48, 1179656\n"
"s_add_u32 s52, s52, s30\ns_sub_u32 s45, s52, s44\ns_and_b32 s45, s45, 3\ns_sub_u32 s44, s52, s45\n"
"s_lshl_b32 s45, s45, 2\ns_and_b32 s46, s48, 255\ns_mov_b32 s92, 0\ns_branch back_x2\n"
"end_x2:\ns_branch fk_done\nfk_x3:\ns_mov_b32 s40, 0\n"
"s_mov_b32 s41, 0\ns_mov_b32 s42, 262144\ns_mov_b32 s43, 393216\ns_mov_b32 s44, s30\n"
"s_mov_b32 s45, 0\ns_bcnt1_i32_b64 s46, s[28:29]\ns_mov_b32 s92, 99\ns_mov_b32 s49, 0\n"
"s_mov_b32 s53, 0\ns_mov_b32 s50, s36\ns_mov_b32 s51, s37\nv_mov_b32 v77, v13\n"
"v_mov_b32 v78, v14\nv_mov_b32 v80, 24576\ns_add_u32 s51, s50, 14336\nv_mov_b32 v60, -1\n"
"v_mov_b32 v61, -1\nv_mov_b32 v62, -1\nv_mov_b32 v63, -1\nbuffer_load_dwordx4 a[0:3], v9, s[16:19], s50 offen offset:0 sc1\n"
"buffer_load_dwordx4 a[4:7], v9, s[16:19], s50 offen offset:1024 sc1\nbuffer_load_dwordx4 a[16:19], v9, s[16:19], s51 offen offset:0 sc1\nbuffer_load_dwordx4 a[20:23], v9, s[16:19], s51 offen offset:1024 sc1\ns_add_u32 s50, s50, 28672\n"
"s_add_u32 s51, s51, 28672\nv_lshlrev_b32 v58, 2, v9\ns_mov_b32 s52, 1064960\nbuffer_load_dword v56, v58, s[8:11], s52 offen sc1\n"
"s_add_u32 s52, s52, 4096\nbuffer_load_dword v57, v58, s[8:11], s52 offen sc1\nbuffer_load_dword v81, v18, s[8:11], 0 offen sc1\ns_waitcnt vmcnt(0)\n"
"ar_x3:\nv_xor_b32 v56, s34, v56\nv_xor_b32 v57, s34, v57\nv_or_b32 v56, v56, v57\n"
"v_cmp_ne_u32 vcc, 0, v56\ns_cbranch_vccz arok_x3\ns_add_u32 s53, s53, 0\ns_add_u32 s49, s49, 1\n"
"s_cmp_gt_u32 s49, 65536\ns_cbranch_scc1 end_x3\ns_sleep 2\nv_lshlrev_b32 v58, 2, v9\n"
"s_mov_b32 s52, 1064960\nbuffer_load_dword v56, v58, s[8:11], s52 offen sc1\ns_add_u32 s52, s52, 4096\nbuffer_load_dword v57, v58, s[8:11], s52 offen sc1\n"
"s_waitcnt vmcnt(0)\ns_branch ar_x3\narok_x3:\nf0_x3:\n"
"s_add_u32 s47, s40, 2\ns_min_u32 s47, s47, 64\ns_add_u32 s47, s47, s34\nv_subrev_u32 v81, s47, v81\n"
"v_cmp_lt_u32 vcc, 64, v81\ns_cbranch_vccz f0ok_x3\ns_add_u32 s53, s53, 0\ns_add_u32 s49, s49, 1\n"
"s_cmp_gt_u32 s49, 65536\ns_cbranch_scc1 end_x3\ns_sleep 2\nbuffer_load_dword v81, v18, s[8:11], 0 offen sc1\n"
"s_waitcnt vmcnt(0)\ns_branch f0_x3\nf0ok_x3:\ns_barrier\n"
"s_memrealtime s[54:55]\ns_waitcnt lgkmcnt(0)\ns_add_u32 s44, s54, 16\ns_mov_b32 s45, 8\n"
".p2align 6\ntop_x3:\nv_mov_b32 v44, 0\nv_mov_b32 v45, 0\n"
"v_mov_b32 v46, 0\nv_mov_b32 v47, 0\nv_mov_b32 v48, 0\nv_mov_b32 v49, 0\n"
"v_mov_b32 v50, 0\nv_mov_b32 v51, 0\nv_mov_b32 v74, 0\ns_mul_i32 s48, s45, 6\n"
"s_and_b32 s48, s48, -4\ns_getpc_b64 s[60:61]\nspc_x3:\ns_sub_u32 s48, sgo_x3-spc_x3, s48\n"
"s_add_u32 s60, s60, s48\ns_addc_u32 s61, s61, 0\nsf_x3:\ns_memrealtime s[54:55]\n"
"s_nop 2\ns_memrealtime s[56:57]\ns_nop 2\ns_memrealtime s[58:59]\n"
"sg_x3:\ns_waitcnt lgkmcnt(4)\ns_sub_u32 s47, s54, s44\ns_add_u32 s48, s47, 12\n"
"s_cmp_ge_u32 s48, 12\ns_cbranch_scc1 so_x3\ns_memrealtime s[54:55]\ns_waitcnt lgkmcnt(4)\n"
"s_cmp_ge_u32 s56, s44\ns_cbranch_scc1 sx_x3\ns_memrealtime s[56:57]\ns_waitcnt lgkmcnt(4)\n"
"s_cmp_ge_u32 s58, s44\ns_cbranch_scc1 sx_x3\ns_memrealtime s[58:59]\ns_waitcnt lgkmcnt(4)\n"
"s_cmp_ge_u32 s54, s44\ns_cbranch_scc1 sx_x3\ns_memrealtime s[54:55]\ns_waitcnt lgkmcnt(4)\n"
"s_cmp_ge_u32 s56, s44\ns_cbranch_scc1 sx_x3\ns_memrealtime s[56:57]\ns_waitcnt lgkmcnt(4)\n"
"s_cmp_ge_u32 s58, s44\ns_cbranch_scc1 sx_x3\ns_memrealtime s[58:59]\ns_waitcnt lgkmcnt(4)\n"
"s_cmp_ge_u32 s54, s44\ns_cbranch_scc1 sx_x3\ns_memrealtime s[54:55]\ns_waitcnt lgkmcnt(4)\n"
"s_cmp_ge_u32 s56, s44\ns_cbranch_scc1 sx_x3\ns_memrealtime s[56:57]\ns_waitcnt lgkmcnt(4)\n"
"s_cmp_ge_u32 s58, s44\ns_cbranch_scc1 sx_x3\ns_memrealtime s[58:59]\ns_waitcnt lgkmcnt(4)\n"
"s_cmp_ge_u32 s54, s44\ns_cbranch_scc1 sx_x3\ns_memrealtime s[54:55]\ns_waitcnt lgkmcnt(4)\n"
"s_cmp_ge_u32 s56, s44\ns_cbranch_scc1 sx_x3\ns_memrealtime s[56:57]\ns_waitcnt lgkmcnt(4)\n"
"s_cmp_ge_u32 s58, s44\ns_cbranch_scc1 sx_x3\ns_memrealtime s[58:59]\ns_waitcnt lgkmcnt(4)\n"
"s_cmp_ge_u32 s54, s44\ns_cbranch_scc1 sx_x3\ns_memrealtime s[54:55]\ns_waitcnt lgkmcnt(4)\n"
"s_cmp_ge_u32 s56, s44\ns_cbranch_scc1 sx_x3\ns_memrealtime s[56:57]\ns_waitcnt lgkmcnt(4)\n"
"s_cmp_ge_u32 s58, s44\ns_cbranch_scc1 sx_x3\ns_memrealtime s[58:59]\ns_waitcnt lgkmcnt(4)\n"
"s_cmp_ge_u32 s54, s44\ns_cbranch_scc1 sx_x3\ns_memrealtime s[54:55]\ns_waitcnt lgkmcnt(4)\n"
"s_cmp_ge_u32 s56, s44\ns_cbranch_scc1 sx_x3\ns_memrealtime s[56:57]\ns_branch sx_x3\n"
"so_x3:\ns_waitcnt lgkmcnt(0)\ns_cmp_lt_i32 s47, 0\ns_cbranch_scc1 sf_x3\n"
"s_branch sgo_x3\nsx_x3:\ns_setpc_b64 s[60:61]\ns_nop 0\n"
"s_nop 0\ns_nop 0\ns_nop 0\ns_nop 0\n"
"s_nop 0\ns_nop 0\ns_nop 0\ns_nop 0\n"
"s_nop 0\ns_nop 0\ns_nop 0\ns_nop 0\n"
"s_nop 0\ns_nop 0\ns_nop 0\ns_nop 0\n"
"s_nop 0\ns_nop 0\ns_nop 0\ns_nop 0\n"
"s_nop 0\ns_nop 0\ns_nop 0\nsgo_x3:\n"
"s_nop 15\ns_nop 1\nbuffer_load_dwordx4 v[40:43], v10, s[8:11], s43 offen offset:0 sc1\nv_mov_b32 v75, 0\n"
"s_nop 1\nv_mov_b32 v75, 0\ns_nop 1\nv_mov_b32 v75, 0\n"
"s_nop 1\nv_mov_b32 v75, 0\ns_nop 1\nv_mov_b32 v75, 0\n"
"s_nop 1\nv_mov_b32 v75, 0\ns_nop 1\nv_mov_b32 v75, 0\n"
"s_nop 1\nv_mov_b32 v75, 0\ns_nop 1\nv_mov_b32 v75, 0\n"
"s_nop 1\nv_mov_b32 v75, 0\ns_nop 1\nv_mov_b32 v75, 0\n"
"s_nop 1\nv_mov_b32 v75, 0\ns_nop 1\nv_mov_b32 v75, 0\n"
"s_nop 1\nv_mov_b32 v75, 0\ns_nop 1\nv_mov_b32 v75, 0\n"
"s_nop 1\nv_mov_b32 v75, 0\ns_nop 1\nv_mov_b32 v75, 0\n"
"s_nop 1\nv_mov_b32 v75, 0\ns_nop 1\nv_mov_b32 v75, 0\n"
"s_nop 1\nv_mov_b32 v75, 0\ns_nop 1\nv_mov_b32 v75, 0\n"
"s_nop 1\nv_mov_b32 v75, 0\ns_nop 1\nv_mov_b32 v75, 0\n"
"s_nop 1\nv_mov_b32 v75, 0\ns_nop 1\nv_mov_b32 v75, 0\n"
"s_nop 1\nv_mov_b32 v75, 0\ns_nop 1\nv_mov_b32 v75, 0\n"
"s_nop 1\nv_mov_b32 v75, 0\ns_nop 1\nv_mov_b32 v75, 0\n"
"s_nop 1\nv_mov_b32 v75, 0\ns_nop 1\nv_mov_b32 v75, 0\n"
"s_nop 1\nv_mov_b32 v75, 0\ns_nop 0\nlanded_x3:\n"
"s_waitcnt vmcnt(0)\nv_smfmac_f32_16x16x64_bf16 v[44:47], v[40:43], a[0:7], v8\nv_max3_u32 v74, v74, v40, v41\nv_smfmac_f32_16x16x64_bf16 v[48:51], v[40:43], a[16:23], v8\n"
"v_max3_u32 v74, v74, v42, v43\nv_cmp_eq_u32 vcc, -1, v74\ns_nop 0\nv_add_f32 v52, v44, v45\n"
"v_add_f32 v53, v46, v47\ns_nop 1\nv_add_f32 v54, v48, v49\nv_add_f32 v55, v50, v51\n"
"ds_write2_b32 v77, v52, v53 offset0:0 offset1:40\nds_write2_b32 v77, v54, v55 offset0:16 offset1:56\ns_cbranch_vccnz retry_x3\nv_mov_b32 v75, 0\n"
"s_nop 1\nv_mov_b32 v75, 0\ns_nop 1\nv_mov_b32 v75, 0\n"
"s_waitcnt lgkmcnt(0)\ns_barrier\nds_read_b64 v[64:65], v78 offset:0\nds_read_b64 v[66:67], v78 offset:1280\n"
"ds_read_b64 v[68:69], v78 offset:2560\nds_read_b64 v[70:71], v78 offset:3840\nv_mov_b32 v75, 0\ns_nop 1\n"
"v_mov_b32 v75, 0\ns_nop 1\nv_mov_b32 v75, 0\ns_nop 1\n"
"s_waitcnt lgkmcnt(2)\nv_pk_add_f32 v[64:65], v[64:65], v[66:67]\ns_waitcnt lgkmcnt(0)\nv_pk_add_f32 v[68:69], v[68:69], v[70:71]\n"
"v_pk_add_f32 v[64:65], v[64:65], v[68:69]\nv_exp_f32 v75, v64\nv_exp_f32 v76, v65\ns_nop 0\n"
"v_cvt_pk_bf16_f32 v75, v75, v76\nbuffer_store_dword v75, v16, s[8:11], s41 offen sc1\ns_barrier\nbuffer_load_dword v81, v18, s[8:11], 0 offen sc1\n"
"buffer_load_dwordx4 a[0:3], v9, s[16:19], s50 offen offset:0 sc1\ns_nop 15\ns_nop 7\nbuffer_load_dwordx4 a[4:7], v9, s[16:19], s50 offen offset:1024 sc1\n"
"s_nop 15\ns_nop 7\nbuffer_load_dwordx4 a[16:19], v9, s[16:19], s51 offen offset:0 sc1\ns_nop 15\n"
"s_nop 7\nbuffer_load_dwordx4 a[20:23], v9, s[16:19], s51 offen offset:1024 sc1\ns_nop 15\ns_nop 7\n"
"s_add_u32 s50, s50, 28672\ns_add_u32 s51, s51, 28672\nbuffer_store_dwordx2 v[64:65], v17, s[8:11], s41 offen sc1\nbuffer_store_dwordx4 v[60:63], v15, s[8:11], s42 offen sc1\n"
"ds_read_b32 v79, v80\nv_xor_b32 v77, 8192, v77\nv_xor_b32 v78, 8192, v78\ns_waitcnt lgkmcnt(0)\n"
"v_readfirstlane_b32 s47, v79\ns_sub_u32 s47, s47, s40\ns_cmp_eq_u32 s47, 1\ns_cbranch_scc1 adopt_x3\n"
"back_x3:\ns_waitcnt vmcnt(6)\nfl_x3:\ns_add_u32 s47, s40, 4\n"
"s_min_u32 s47, s47, 64\ns_add_u32 s47, s47, s34\nv_subrev_u32 v81, s47, v81\nv_cmp_lt_u32 vcc, 64, v81\n"
"s_cbranch_vccz flok_x3\ns_add_u32 s53, s53, 0\ns_add_u32 s49, s49, 1\ns_cmp_gt_u32 s49, 65536\n"
"s_cbranch_scc1 end_x3\ns_sleep 2\nbuffer_load_dword v81, v18, s[8:11], 0 offen sc1\ns_waitcnt vmcnt(0)\n"
"s_branch fl_x3\nflok_x3:\ns_mul_i32 s48, s46, 8\ns_add_u32 s45, s45, s48\n"
"s_add_u32 s45, s45, s32\ns_lshr_b32 s48, s45, 4\ns_lshl_b32 s48, s48, 2\ns_add_u32 s44, s44, s48\n"
"s_and_b32 s45, s45, 15\ns_add_u32 s52, s40, 1\ns_bitcmp1_b64 s[28:29], s52\ns_subb_u32 s46, s46, 0\n"
"s_add_u32 s40, s40, 1\ns_mov_b32 s43, s41\ns_add_u32 s41, s41, 131072\ns_and_b32 s41, s41, 524287\n"
"s_add_u32 s92, s92, 1\ns_xor_b32 s42, s41, 262144\ns_waitcnt lgkmcnt(0)\ns_cmp_lt_u32 s40, 63\n"
"s_cbranch_scc1 top_x3\ns_branch end_x3\nretry2_x3:\ns_sleep 3\n"
"retry_x3:\ns_add_u32 s53, s53, 1\ns_add_u32 s49, s49, 1\ns_cmp_gt_u32 s49, 65536\n"
"s_cbranch_scc1 end_x3\nbuffer_load_dwordx4 v[40:43], v10, s[8:11], s43 offen offset:0 sc1\nv_mov_b32 v74, 0\ns_add_u32 s47, s40, 1\n"
"v_mov_b32 v79, s47\nds_write_b32 v80, v79\ns_waitcnt vmcnt(0)\nv_max3_u32 v74, v74, v40, v41\n"
"v_max3_u32 v74, v74, v42, v43\nv_cmp_eq_u32 vcc, -1, v74\nv_mov_b32 v44, 0\nv_mov_b32 v45, 0\n"
"v_mov_b32 v46, 0\nv_mov_b32 v47, 0\nv_mov_b32 v48, 0\nv_mov_b32 v49, 0\n"
"v_mov_b32 v50, 0\nv_mov_b32 v51, 0\ns_cbranch_vccnz retry2_x3\ns_branch landed_x3\n"
"adopt_x3:\nbuffer_load_dword v56, v28, s[8:11], 0 offen sc1\ns_lshl_b32 s52, s40, 26\ns_or_b32 s47, s52, 0x3ffffff\n"
"s_waitcnt vmcnt(0)\nv_cmp_ge_u32 vcc, s47, v56\ns_nop 1\nv_cndmask_b32 v79, 0, v56, vcc\n"
"s_nop 1\nv_max_u32_dpp v79, v79, v79 row_ror:8 row_mask:0xf bank_mask:0xf\ns_nop 1\nv_max_u32_dpp v79, v79, v79 row_ror:4 row_mask:0xf bank_mask:0xf\n"
"s_nop 1\nv_max_u32_dpp v79, v79, v79 row_ror:2 row_mask:0xf bank_mask:0xf\ns_nop 1\nv_max_u32_dpp v79, v79, v79 row_ror:1 row_mask:0xf bank_mask:0xf\n"
"s_nop 0\nv_readlane_b32 s47, v79, 0\nv_readlane_b32 s48, v79, 16\ns_max_u32 s48, s48, s47\n"
"s_cmp_le_u32 s48, s52\ns_cbranch_scc1 back_x3\ns_bfe_u32 s52, s48, 1179656\ns_add_u32 s52, s52, s30\n"
"s_sub_u32 s45, s52, s44\ns_and_b32 s45, s45, 3\ns_sub_u32 s44, s52, s45\ns_lshl_b32 s45, s45, 2\n"
"s_and_b32 s46, s48, 255\ns_mov_b32 s92, 0\ns_branch back_x3\nend_x3:\n"
"s_branch fk_done\nfk_p0:\ns_mov_b32 s40, 0\ns_mov_b32 s41, 0\n"
"s_mov_b32 s42, 262144\ns_mov_b32 s43, 393216\ns_mov_b32 s44, s30\ns_mov_b32 s45, 0\n"
"s_bcnt1_i32_b64 s46, s[28:29]\ns_mov_b32 s92, 99\ns_mov_b32 s49, 0\ns_mov_b32 s53, 0\n"
"s_mov_b32 s50, s36\ns_mov_b32 s51, s37\ns_sub_u32 s93, s39, 8\ns_lshl_b32 s47, s93, 1\n"
"s_add_u32 s47, s47, 0\ns_sub_u32 s48, s47, 7\ns_cmp_ge_u32 s47, 7\ns_cselect_b32 s47, s48, s47\n"
"s_lshl_b32 s47, s47, 10\nv_add_u32 v226, s47, v9\ns_lshl_b32 s47, s47, 1\ns_add_u32 s81, s47, s35\n"
"s_lshl_b32 s47, s93, 1\ns_add_u32 s47, s47, 1\ns_sub_u32 s48, s47, 7\ns_cmp_ge_u32 s47, 7\n"
"s_cselect_b32 s47, s48, s47\ns_lshl_b32 s47, s47, 10\nv_add_u32 v227, s47, v9\ns_lshl_b32 s47, s47, 1\n"
"s_add_u32 s82, s47, s35\ns_lshl_b32 s47, s93, 1\ns_add_u32 s47, s47, 2\ns_sub_u32 s48, s47, 7\n"
"s_cmp_ge_u32 s47, 7\ns_cselect_b32 s47, s48, s47\ns_lshl_b32 s47, s47, 10\nv_add_u32 v228, s47, v9\n"
"s_lshl_b32 s47, s47, 1\ns_add_u32 s83, s47, s35\ns_lshl_b32 s47, s93, 1\ns_add_u32 s47, s47, 3\n"
"s_sub_u32 s48, s47, 7\ns_cmp_ge_u32 s47, 7\ns_cselect_b32 s47, s48, s47\ns_lshl_b32 s47, s47, 10\n"
"v_add_u32 v229, s47, v9\ns_lshl_b32 s47, s47, 1\ns_add_u32 s84, s47, s35\ns_lshl_b32 s47, s93, 1\n"
"s_add_u32 s47, s47, 4\ns_sub_u32 s48, s47, 7\ns_cmp_ge_u32 s47, 7\ns_cselect_b32 s47, s48, s47\n"
"s_lshl_b32 s47, s47, 10\nv_add_u32 v230, s47, v9\ns_lshl_b32 s47, s47, 1\ns_add_u32 s85, s47, s35\n"
"s_lshl_b32 s47, s93, 1\ns_add_u32 s47, s47, 5\ns_sub_u32 s48, s47, 7\ns_cmp_ge_u32 s47, 7\n"
"s_cselect_b32 s47, s48, s47\ns_lshl_b32 s47, s47, 10\nv_add_u32 v231, s47, v9\ns_lshl_b32 s47, s47, 1\n"
"s_add_u32 s86, s47, s35\ns_lshl_b32 s47, s93, 1\ns_add_u32 s47, s47, 6\ns_sub_u32 s48, s47, 7\n"
"s_cmp_ge_u32 s47, 7\ns_cselect_b32 s47, s48, s47\ns_lshl_b32 s47, s47, 10\nv_add_u32 v232, s47, v9\n"
"s_lshl_b32 s47, s47, 1\ns_add_u32 s87, s47, s35\nbuffer_load_dword v225, v14, s[8:11], 0 offen sc1\ns_add_u32 s42, s35, 8192\n"
"s_cmp_ge_u32 s81, s42\ns_cbranch_scc1 is00_p0\ns_add_u32 s47, s81, 28672\nbuffer_load_dwordx4 a[112:115], v9, s[12:15], s47 offen sc1\n"
"buffer_load_dwordx4 a[116:119], v9, s[12:15], s47 offen offset:1024 sc1\ns_add_u32 s47, s81, 43008\nbuffer_load_dwordx4 a[168:171], v9, s[12:15], s47 offen sc1\nbuffer_load_dwordx4 a[172:175], v9, s[12:15], s47 offen offset:1024 sc1\n"
"is00_p0:\ns_cmp_ge_u32 s82, s42\ns_cbranch_scc1 is01_p0\ns_add_u32 s47, s82, 28672\n"
"buffer_load_dwordx4 a[120:123], v9, s[12:15], s47 offen sc1\nbuffer_load_dwordx4 a[124:127], v9, s[12:15], s47 offen offset:1024 sc1\ns_add_u32 s47, s82, 43008\nbuffer_load_dwordx4 a[176:179], v9, s[12:15], s47 offen sc1\n"
"buffer_load_dwordx4 a[180:183], v9, s[12:15], s47 offen offset:1024 sc1\nis01_p0:\ns_cmp_ge_u32 s83, s42\ns_cbranch_scc1 is02_p0\n"
"s_add_u32 s47, s83, 28672\nbuffer_load_dwordx4 a[128:131], v9, s[12:15], s47 offen sc1\nbuffer_load_dwordx4 a[132:135], v9, s[12:15], s47 offen offset:1024 sc1\ns_add_u32 s47, s83, 43008\n"
"buffer_load_dwordx4 a[184:187], v9, s[12:15], s47 offen sc1\nbuffer_load_dwordx4 a[188:191], v9, s[12:15], s47 offen offset:1024 sc1\nis02_p0:\ns_cmp_ge_u32 s84, s42\n"
"s_cbranch_scc1 is03_p0\ns_add_u32 s47, s84, 28672\nbuffer_load_dwordx4 a[136:139], v9, s[12:15], s47 offen sc1\nbuffer_load_dwordx4 a[140:143], v9, s[12:15], s47 offen offset:1024 sc1\n"
"s_add_u32 s47, s84, 43008\nbuffer_load_dwordx4 a[192:195], v9, s[12:15], s47 offen sc1\nbuffer_load_dwordx4 a[196:199], v9, s[12:15], s47 offen offset:1024 sc1\nis03_p0:\n"
"s_cmp_ge_u32 s85, s42\ns_cbranch_scc1 is04_p0\ns_add_u32 s47, s85, 28672\nbuffer_load_dwordx4 a[144:147], v9, s[12:15], s47 offen sc1\n"
"buffer_load_dwordx4 a[148:151], v9, s[12:15], s47 offen offset:1024 sc1\ns_add_u32 s47, s85, 43008\nbuffer_load_dwordx4 a[200:203], v9, s[12:15], s47 offen sc1\nbuffer_load_dwordx4 a[204:207], v9, s[12:15], s47 offen offset:1024 sc1\n"
"is04_p0:\ns_cmp_ge_u32 s86, s42\ns_cbranch_scc1 is05_p0\ns_add_u32 s47, s86, 28672\n"
"buffer_load_dwordx4 a[152:155], v9, s[12:15], s47 offen sc1\nbuffer_load_dwordx4 a[156:159], v9, s[12:15], s47 offen offset:1024 sc1\ns_add_u32 s47, s86, 43008\nbuffer_load_dwordx4 a[208:211], v9, s[12:15], s47 offen sc1\n"
"buffer_load_dwordx4 a[212:215], v9, s[12:15], s47 offen offset:1024 sc1\nis05_p0:\ns_cmp_ge_u32 s87, s42\ns_cbranch_scc1 is06_p0\n"
"s_add_u32 s47, s87, 28672\nbuffer_load_dwordx4 a[160:163], v9, s[12:15], s47 offen sc1\nbuffer_load_dwordx4 a[164:167], v9, s[12:15], s47 offen offset:1024 sc1\ns_add_u32 s47, s87, 43008\n"
"buffer_load_dwordx4 a[216:219], v9, s[12:15], s47 offen sc1\nbuffer_load_dwordx4 a[220:223], v9, s[12:15], s47 offen offset:1024 sc1\nis06_p0:\nv_mov_b32 v222, 24576\n"
"s_mov_b32 s62, 0x00ff00ff\ns_mov_b32 s63, 0x00ff00ff\nv_cndmask_b32 v223, 0, -1, s[62:63]\nv_not_b32 v224, v223\n"
"v_bfe_u32 v219, v9, 4, 1\nv_lshlrev_b32 v220, 4, v219\nv_mov_b32 v218, 0x3c00\nv_lshlrev_b32 v218, v220, v218\n"
"v_bfe_u32 v220, v9, 5, 3\nv_cmp_eq_u32 vcc, 0, v220\ns_nop 1\nv_cndmask_b32 v168, 0, v218, vcc\n"
"v_cmp_eq_u32 vcc, 2, v220\ns_nop 1\nv_cndmask_b32 v169, 0, v218, vcc\nv_cmp_eq_u32 vcc, 4, v220\n"
"s_nop 1\nv_cndmask_b32 v170, 0, v218, vcc\nv_cmp_eq_u32 vcc, 6, v220\ns_nop 1\n"
"v_cndmask_b32 v171, 0, v218, vcc\nv_cmp_eq_u32 vcc, 1, v220\ns_nop 1\nv_cndmask_b32 v172, 0, v218, vcc\n"
"v_cmp_eq_u32 vcc, 3, v220\ns_nop 1\nv_cndmask_b32 v173, 0, v218, vcc\nv_cmp_eq_u32 vcc, 5, v220\n"
"s_nop 1\nv_cndmask_b32 v174, 0, v218, vcc\nv_cmp_eq_u32 vcc, 7, v220\ns_nop 1\n"
"v_cndmask_b32 v175, 0, v218, vcc\nv_mov_b32 v218, 0x3f803f80\nv_cmp_eq_u32 vcc, 0, v219\ns_nop 1\n"
"v_cndmask_b32 v176, 0, v218, vcc\nv_cndmask_b32 v177, 0, v218, vcc\nv_cndmask_b32 v178, 0, v218, vcc\nv_cndmask_b32 v179, 0, v218, vcc\n"
"v_cndmask_b32 v214, 0, v218, vcc\nv_cmp_eq_u32 vcc, 1, v219\ns_nop 1\nv_cndmask_b32 v180, 0, v218, vcc\n"
"v_cndmask_b32 v181, 0, v218, vcc\nv_cndmask_b32 v182, 0, v218, vcc\nv_cndmask_b32 v183, 0, v218, vcc\nv_cndmask_b32 v215, 0, v218, vcc\n"
"s_mul_i32 s47, s93, 3696\ns_getpc_b64 s[60:61]\ndp0_p0:\ns_add_u32 s47, s47, vb0_p0-dp0_p0+0\n"
"s_add_u32 s60, s60, s47\ns_addc_u32 s61, s61, 0\ns_mov_b64 exec, 0\ns_swappc_b64 s[88:89], s[60:61]\n"
"s_mov_b64 exec, -1\ns_waitcnt lgkmcnt(0)\ns_waitcnt vmcnt(16)\nfw_p0:\n"
"s_add_u32 s47, s40, 0\ns_min_u32 s47, s47, 64\ns_add_u32 s47, s47, s34\nv_subrev_u32 v225, s47, v225\n"
"v_cmp_lt_u32 vcc, 64, v225\ns_cbranch_vccz fwok_p0\ns_add_u32 s53, s53, 0\ns_add_u32 s49, s49, 1\n"
"s_cmp_gt_u32 s49, 65536\ns_cbranch_scc1 end_p0\ns_sleep 2\nbuffer_load_dword v225, v14, s[8:11], 0 offen sc1\n"
"s_waitcnt vmcnt(0)\ns_branch fw_p0\nfwok_p0:\ns_cmp_lt_u32 s81, s42\n"
"s_cbranch_scc1 is10_p0\ns_add_u32 s47, s81, 0\nbuffer_load_dwordx4 a[0:3], v9, s[12:15], s47 offen sc1\nbuffer_load_dwordx4 a[4:7], v9, s[12:15], s47 offen offset:1024 sc1\n"
"s_add_u32 s47, s81, 14336\nbuffer_load_dwordx4 a[56:59], v9, s[12:15], s47 offen sc1\nbuffer_load_dwordx4 a[60:63], v9, s[12:15], s47 offen offset:1024 sc1\nis10_p0:\n"
"s_cmp_lt_u32 s82, s42\ns_cbranch_scc1 is11_p0\ns_add_u32 s47, s82, 0\nbuffer_load_dwordx4 a[8:11], v9, s[12:15], s47 offen sc1\n"
"buffer_load_dwordx4 a[12:15], v9, s[12:15], s47 offen offset:1024 sc1\ns_add_u32 s47, s82, 14336\nbuffer_load_dwordx4 a[64:67], v9, s[12:15], s47 offen sc1\nbuffer_load_dwordx4 a[68:71], v9, s[12:15], s47 offen offset:1024 sc1\n"
"is11_p0:\ns_cmp_lt_u32 s83, s42\ns_cbranch_scc1 is12_p0\ns_add_u32 s47, s83, 0\n"
"buffer_load_dwordx4 a[16:19], v9, s[12:15], s47 offen sc1\nbuffer_load_dwordx4 a[20:23], v9, s[12:15], s47 offen offset:1024 sc1\ns_add_u32 s47, s83, 14336\nbuffer_load_dwordx4 a[72:75], v9, s[12:15], s47 offen sc1\n"
"buffer_load_dwordx4 a[76:79], v9, s[12:15], s47 offen offset:1024 sc1\nis12_p0:\ns_cmp_lt_u32 s84, s42\ns_cbranch_scc1 is13_p0\n"
"s_add_u32 s47, s84, 0\nbuffer_load_dwordx4 a[24:27], v9, s[12:15], s47 offen sc1\nbuffer_load_dwordx4 a[28:31], v9, s[12:15], s47 offen offset:1024 sc1\ns_add_u32 s47, s84, 14336\n"
"buffer_load_dwordx4 a[80:83], v9, s[12:15], s47 offen sc1\nbuffer_load_dwordx4 a[84:87], v9, s[12:15], s47 offen offset:1024 sc1\nis13_p0:\ns_cmp_lt_u32 s85, s42\n"
"s_cbranch_scc1 is14_p0\ns_add_u32 s47, s85, 0\nbuffer_load_dwordx4 a[32:35], v9, s[12:15], s47 offen sc1\nbuffer_load_dwordx4 a[36:39], v9, s[12:15], s47 offen offset:1024 sc1\n"
"s_add_u32 s47, s85, 14336\nbuffer_load_dwordx4 a[88:91], v9, s[12:15], s47 offen sc1\nbuffer_load_dwordx4 a[92:95], v9, s[12:15], s47 offen offset:1024 sc1\nis14_p0:\n"
"s_cmp_lt_u32 s86, s42\ns_cbranch_scc1 is15_p0\ns_add_u32 s47, s86, 0\nbuffer_load_dwordx4 a[40:43], v9, s[12:15], s47 offen sc1\n"
"buffer_load_dwordx4 a[44:47], v9, s[12:15], s47 offen offset:1024 sc1\ns_add_u32 s47, s86, 14336\nbuffer_load_dwordx4 a[96:99], v9, s[12:15], s47 offen sc1\nbuffer_load_dwordx4 a[100:103], v9, s[12:15], s47 offen offset:1024 sc1\n"
"is15_p0:\ns_cmp_lt_u32 s87, s42\ns_cbranch_scc1 is16_p0\ns_add_u32 s47, s87, 0\n"
"buffer_load_dwordx4 a[48:51], v9, s[12:15], s47 offen sc1\nbuffer_load_dwordx4 a[52:55], v9, s[12:15], s47 offen offset:1024 sc1\ns_add_u32 s47, s87, 14336\nbuffer_load_dwordx4 a[104:107], v9, s[12:15], s47 offen sc1\n"
"buffer_load_dwordx4 a[108:111], v9, s[12:15], s47 offen offset:1024 sc1\nis16_p0:\ns_mul_i32 s47, s93, 3696\ns_getpc_b64 s[60:61]\n"
"dp1_p0:\ns_add_u32 s47, s47, vb0_p0-dp1_p0+1848\ns_cmp_eq_u32 s93, 3\ns_cselect_b32 s47, rp0_p0-dp1_p0, s47\n"
"s_add_u32 s60, s60, s47\ns_addc_u32 s61, s61, 0\ns_mov_b64 exec, 0\ns_swappc_b64 s[88:89], s[60:61]\n"
"s_mov_b64 exec, -1\ns_waitcnt lgkmcnt(0)\ns_cmp_lt_u32 s81, s42\ns_cbranch_scc1 is20_p0\n"
"s_add_u32 s47, s81, 28672\nbuffer_load_dwordx4 a[112:115], v9, s[12:15], s47 offen sc1\nbuffer_load_dwordx4 a[116:119], v9, s[12:15], s47 offen offset:1024 sc1\ns_add_u32 s47, s81, 43008\n"
"buffer_load_dwordx4 a[168:171], v9, s[12:15], s47 offen sc1\nbuffer_load_dwordx4 a[172:175], v9, s[12:15], s47 offen offset:1024 sc1\nis20_p0:\ns_cmp_lt_u32 s82, s42\n"
"s_cbranch_scc1 is21_p0\ns_add_u32 s47, s82, 28672\nbuffer_load_dwordx4 a[120:123], v9, s[12:15], s47 offen sc1\nbuffer_load_dwordx4 a[124:127], v9, s[12:15], s47 offen offset:1024 sc1\n"
"s_add_u32 s47, s82, 43008\nbuffer_load_dwordx4 a[176:179], v9, s[12:15], s47 offen sc1\nbuffer_load_dwordx4 a[180:183], v9, s[12:15], s47 offen offset:1024 sc1\nis21_p0:\n"
"s_cmp_lt_u32 s83, s42\ns_cbranch_scc1 is22_p0\ns_add_u32 s47, s83, 28672\nbuffer_load_dwordx4 a[128:131], v9, s[12:15], s47 offen sc1\n"
"buffer_load_dwordx4 a[132:135], v9, s[12:15], s47 offen offset:1024 sc1\ns_add_u32 s47, s83, 43008\nbuffer_load_dwordx4 a[184:187], v9, s[12:15], s47 offen sc1\nbuffer_load_dwordx4 a[188:191], v9, s[12:15], s47 offen offset:1024 sc1\n"
"is22_p0:\ns_cmp_lt_u32 s84, s42\ns_cbranch_scc1 is23_p0\ns_add_u32 s47, s84, 28672\n"
"buffer_load_dwordx4 a[136:139], v9, s[12:15], s47 offen sc1\nbuffer_load_dwordx4 a[140:143], v9, s[12:15], s47 offen offset:1024 sc1\ns_add_u32 s47, s84, 43008\nbuffer_load_dwordx4 a[192:195], v9, s[12:15], s47 offen sc1\n"
"buffer_load_dwordx4 a[196:199], v9, s[12:15], s47 offen offset:1024 sc1\nis23_p0:\ns_cmp_lt_u32 s85, s42\ns_cbranch_scc1 is24_p0\n"
"s_add_u32 s47, s85, 28672\nbuffer_load_dwordx4 a[144:147], v9, s[12:15], s47 offen sc1\nbuffer_load_dwordx4 a[148:151], v9, s[12:15], s47 offen offset:1024 sc1\ns_add_u32 s47, s85, 43008\n"
"buffer_load_dwordx4 a[200:203], v9, s[12:15], s47 offen sc1\nbuffer_load_dwordx4 a[204:207], v9, s[12:15], s47 offen offset:1024 sc1\nis24_p0:\ns_cmp_lt_u32 s86, s42\n"
"s_cbranch_scc1 is25_p0\ns_add_u32 s47, s86, 28672\nbuffer_load_dwordx4 a[152:155], v9, s[12:15], s47 offen sc1\nbuffer_load_dwordx4 a[156:159], v9, s[12:15], s47 offen offset:1024 sc1\n"
"s_add_u32 s47, s86, 43008\nbuffer_load_dwordx4 a[208:211], v9, s[12:15], s47 offen sc1\nbuffer_load_dwordx4 a[212:215], v9, s[12:15], s47 offen offset:1024 sc1\nis25_p0:\n"
"s_cmp_lt_u32 s87, s42\ns_cbranch_scc1 is26_p0\ns_add_u32 s47, s87, 28672\nbuffer_load_dwordx4 a[160:163], v9, s[12:15], s47 offen sc1\n"
"buffer_load_dwordx4 a[164:167], v9, s[12:15], s47 offen offset:1024 sc1\ns_add_u32 s47, s87, 43008\nbuffer_load_dwordx4 a[216:219], v9, s[12:15], s47 offen sc1\nbuffer_load_dwordx4 a[220:223], v9, s[12:15], s47 offen offset:1024 sc1\n"
"is26_p0:\nv_readfirstlane_b32 s93, v13\nv_lshlrev_b32 v166, 2, v9\ns_mov_b32 s52, 1064960\n"
"buffer_load_dword v164, v166, s[8:11], s52 offen sc1\ns_add_u32 s52, s52, 4096\nbuffer_load_dword v165, v166, s[8:11], s52 offen sc1\nbuffer_load_dword v218, v11, s[8:11], 0 offen sc1\n"
"s_mov_b64 s[94:95], 1\ns_barrier\ns_waitcnt vmcnt(0)\nar_p0:\n"
"v_xor_b32 v164, s34, v164\nv_xor_b32 v165, s34, v165\nv_or_b32 v164, v164, v165\nv_cmp_ne_u32 vcc, 0, v164\n"
"s_cbranch_vccz arok_p0\ns_add_u32 s53, s53, 0\ns_add_u32 s49, s49, 1\ns_cmp_gt_u32 s49, 65536\n"
"s_cbranch_scc1 end_p0\ns_sleep 2\nv_lshlrev_b32 v166, 2, v9\ns_mov_b32 s52, 1064960\n"
"buffer_load_dword v164, v166, s[8:11], s52 offen sc1\ns_add_u32 s52, s52, 4096\nbuffer_load_dword v165, v166, s[8:11], s52 offen sc1\ns_waitcnt vmcnt(0)\n"
"s_branch ar_p0\narok_p0:\nfl_p0:\ns_add_u32 s47, s40, 3\n"
"s_min_u32 s47, s47, 64\ns_add_u32 s47, s47, s34\nv_subrev_u32 v218, s47, v218\nv_cmp_lt_u32 vcc, 64, v218\n"
"s_cbranch_vccz flok_p0\ns_add_u32 s53, s53, 0\ns_add_u32 s49, s49, 1\ns_cmp_gt_u32 s49, 65536\n"
"s_cbranch_scc1 end_p0\ns_sleep 2\nbuffer_load_dword v218, v11, s[8:11], 0 offen sc1\ns_waitcnt vmcnt(0)\n"
"s_branch fl_p0\nflok_p0:\nbuffer_load_dwordx4 v[100:103], v9, s[16:19], s50 offen offset:0 sc1\nbuffer_load_dwordx4 v[104:107], v9, s[16:19], s50 offen offset:1024 sc1\n"
"buffer_load_dwordx4 v[108:111], v9, s[16:19], s50 offen offset:2048 sc1\nbuffer_load_dwordx4 v[112:115], v9, s[16:19], s50 offen offset:3072 sc1\ns_add_u32 s50, s50, 16384\ns_memrealtime s[54:55]\n"
"s_waitcnt lgkmcnt(0)\ns_add_u32 s44, s54, 12\ns_mov_b32 s45, 8\ns_mov_b32 s42, 1\n"
"s_mul_i32 s48, s45, 6\ns_and_b32 s48, s48, -4\ns_getpc_b64 s[88:89]\nmpc_p0:\n"
"s_sub_u32 s48, mgo_p0-mpc_p0, s48\ns_add_u32 s88, s88, s48\ns_addc_u32 s89, s89, 0\ns_branch mf_p0\n"
".p2align 6\ntop_p0:\nmf_p0:\ns_memrealtime s[54:55]\n"
"s_nop 2\ns_memrealtime s[56:57]\ns_nop 2\ns_memrealtime s[58:59]\n"
"mg_p0:\ns_waitcnt lgkmcnt(4)\ns_sub_u32 s47, s54, s44\ns_add_u32 s48, s47, 12\n"
"s_cmp_ge_u32 s48, 12\ns_cbranch_scc1 mo_p0\ns_memrealtime s[54:55]\ns_waitcnt lgkmcnt(4)\n"
"s_cmp_ge_u32 s56, s44\ns_cbranch_scc1 mx_p0\ns_memrealtime s[56:57]\ns_waitcnt lgkmcnt(4)\n"
"s_cmp_ge_u32 s58, s44\ns_cbranch_scc1 mx_p0\ns_memrealtime s[58:59]\ns_waitcnt lgkmcnt(4)\n"
"s_cmp_ge_u32 s54, s44\ns_cbranch_scc1 mx_p0\ns_memrealtime s[54:55]\ns_waitcnt lgkmcnt(4)\n"
"s_cmp_ge_u32 s56, s44\ns_cbranch_scc1 mx_p0\ns_memrealtime s[56:57]\ns_waitcnt lgkmcnt(4)\n"
"s_cmp_ge_u32 s58, s44\ns_cbranch_scc1 mx_p0\ns_memrealtime s[58:59]\ns_waitcnt lgkmcnt(4)\n"
"s_cmp_ge_u32 s54, s44\ns_cbranch_scc1 mx_p0\ns_memrealtime s[54:55]\ns_waitcnt lgkmcnt(4)\n"
"s_cmp_ge_u32 s56, s44\ns_cbranch_scc1 mx_p0\ns_memrealtime s[56:57]\ns_waitcnt lgkmcnt(4)\n"
"s_cmp_ge_u32 s58, s44\ns_cbranch_scc1 mx_p0\ns_memrealtime s[58:59]\ns_waitcnt lgkmcnt(4)\n"
"s_cmp_ge_u32 s54, s44\ns_cbranch_scc1 mx_p0\ns_memrealtime s[54:55]\ns_waitcnt lgkmcnt(4)\n"
"s_cmp_ge_u32 s56, s44\ns_cbranch_scc1 mx_p0\ns_memrealtime s[56:57]\ns_waitcnt lgkmcnt(4)\n"
"s_cmp_ge_u32 s58, s44\ns_cbranch_scc1 mx_p0\ns_memrealtime s[58:59]\ns_waitcnt lgkmcnt(4)\n"
"s_cmp_ge_u32 s54, s44\ns_cbranch_scc1 mx_p0\ns_memrealtime s[54:55]\ns_waitcnt lgkmcnt(4)\n"
"s_cmp_ge_u32 s56, s44\ns_cbranch_scc1 mx_p0\ns_memrealtime s[56:57]\ns_waitcnt lgkmcnt(4)\n"
"s_cmp_ge_u32 s58, s44\ns_cbranch_scc1 mx_p0\ns_memrealtime s[58:59]\ns_waitcnt lgkmcnt(4)\n"
"s_cmp_ge_u32 s54, s44\ns_cbranch_scc1 mx_p0\ns_memrealtime s[54:55]\ns_waitcnt lgkmcnt(4)\n"
"s_cmp_ge_u32 s56, s44\ns_cbranch_scc1 mx_p0\ns_memrealtime s[56:57]\ns_branch mx_p0\n"
"mo_p0:\ns_waitcnt lgkmcnt(0)\ns_cmp_lt_i32 s47, 0\ns_cbranch_scc1 mf_p0\n"
"s_branch mgo_p0\nmx_p0:\ns_setpc_b64 s[88:89]\ns_nop 0\n"
"s_nop 0\ns_nop 0\ns_nop 0\ns_nop 0\n"
"s_nop 0\ns_nop 0\ns_nop 0\ns_nop 0\n"
"s_nop 0\ns_nop 0\ns_nop 0\ns_nop 0\n"
"s_nop 0\ns_nop 0\ns_nop 0\ns_nop 0\n"
"s_nop 0\ns_nop 0\ns_nop 0\ns_nop 0\n"
"s_nop 0\ns_nop 0\ns_nop 0\nmgo_p0:\n"
"s_nop 15\ns_nop 7\ntop2_p0:\nbuffer_load_dwordx4 v[40:43], v10, s[8:11], s43 offen sc1\n"
"buffer_load_dwordx4 v[44:47], v12, s[8:11], s43 offen sc1\nv_mov_b32 v68, 0\nv_mov_b32 v69, 0\nv_mov_b32 v70, 0\n"
"v_mov_b32 v71, 0\nv_mov_b32 v72, 0\nv_mov_b32 v73, 0\nv_mov_b32 v74, 0\n"
"v_mov_b32 v75, 0\nv_mov_b32 v76, 0\nv_mov_b32 v77, 0\nv_mov_b32 v78, 0\n"
"v_mov_b32 v79, 0\nv_mov_b32 v80, 0\nv_mov_b32 v81, 0\nv_mov_b32 v82, 0\n"
"v_mov_b32 v83, 0\nv_mov_b32 v218, 0\ns_cmp_eq_u32 s42, 0\ns_cbranch_scc1 np_p0\n"
"s_mov_b32 s42, 0\ns_and_b32 s47, s46, 15\ns_mul_i32 s47, s47, 4\ns_add_u32 s47, s47, s31\n"
"s_add_u32 s47, s47, s45\ns_and_b32 s91, s47, 15\ns_lshr_b32 s47, s47, 4\ns_lshl_b32 s47, s47, 2\n"
"s_add_u32 s90, s44, s47\ns_mul_i32 s47, s91, 6\ns_add_u32 s47, s47, s93\ns_lshr_b32 s47, s47, 4\n"
"s_mul_i32 s47, s47, 300\ns_getpc_b64 s[60:61]\nep1_p0:\ns_add_u32 s47, s47, sgo_p0-ep1_p0\n"
"s_add_u32 s60, s60, s47\ns_addc_u32 s61, s61, 0\ns_mul_i32 s48, s46, 8\ns_add_u32 s45, s45, s48\n"
"s_add_u32 s45, s45, s32\ns_lshr_b32 s48, s45, 4\ns_lshl_b32 s48, s48, 2\ns_add_u32 s44, s44, s48\n"
"s_and_b32 s45, s45, 15\ns_add_u32 s52, s40, 1\ns_bitcmp1_b64 s[28:29], s52\ns_subb_u32 s46, s46, 0\n"
"s_mul_i32 s48, s45, 6\ns_and_b32 s48, s48, -4\ns_getpc_b64 s[88:89]\nmpc1_p0:\n"
"s_sub_u32 s48, mgo_p0-mpc1_p0, s48\ns_add_u32 s88, s88, s48\ns_addc_u32 s89, s89, -1\nnp_p0:\n"
"s_waitcnt vmcnt(1)\nds_write_b128 v226, v[40:43]\nv_max3_u32 v218, v218, v40, v41\nv_max3_u32 v218, v218, v42, v43\n"
"s_waitcnt vmcnt(0)\nds_write_b128 v227, v[44:47]\nv_max3_u32 v218, v218, v44, v45\nv_max3_u32 v218, v218, v46, v47\n"
"v_cmp_eq_u32 vcc, -1, v218\nv_smfmac_f32_16x16x64_bf16 v[68:71], v[40:43], a[0:7], v8\nv_mov_b32 v84, v100\nv_mov_b32 v85, v101\n"
"v_mov_b32 v86, v102\nv_mov_b32 v87, v103\nv_mov_b32 v88, v104\nv_smfmac_f32_16x16x64_bf16 v[72:75], v[40:43], a[56:63], v8\n"
"v_mov_b32 v89, v105\nv_mov_b32 v90, v106\nv_mov_b32 v91, v107\nv_mov_b32 v92, v108\n"
"v_mov_b32 v93, v109\ns_cbranch_vccnz retry1_p0\ns_waitcnt lgkmcnt(0)\ns_barrier\n"
"ds_read_b32 v221, v222\nds_read_b128 v[48:51], v228\nds_read_b128 v[52:55], v229\nds_read_b128 v[56:59], v230\n"
"ds_read_b128 v[60:63], v231\nds_read_b128 v[64:67], v232\nv_smfmac_f32_16x16x64_bf16 v[76:79], v[40:43], a[112:119], v8\nv_mov_b32 v94, v110\n"
"v_smfmac_f32_16x16x64_bf16 v[80:83], v[40:43], a[168:175], v8\nv_mov_b32 v95, v111\nv_smfmac_f32_16x16x64_bf16 v[68:71], v[44:47], a[8:15], v8\nv_mov_b32 v96, v112\n"
"v_smfmac_f32_16x16x64_bf16 v[72:75], v[44:47], a[64:71], v8\nv_mov_b32 v97, v113\ns_waitcnt lgkmcnt(0)\nv_readfirstlane_b32 s47, v221\n"
"s_memrealtime s[54:55]\nv_smfmac_f32_16x16x64_bf16 v[76:79], v[44:47], a[120:127], v8\nv_mov_b32 v98, v114\ns_memrealtime s[56:57]\n"
"v_smfmac_f32_16x16x64_bf16 v[80:83], v[44:47], a[176:183], v8\nv_mov_b32 v99, v115\ns_cmp_eq_u32 s40, 63\ns_cbranch_scc1 last_p0\n"
"s_memrealtime s[58:59]\ns_sub_u32 s47, s47, s40\ns_cmp_eq_u32 s47, 1\ns_cbranch_scc1 adopt_p0\n"
"s_waitcnt lgkmcnt(4)\ns_sub_u32 s47, s54, s90\ns_cmp_ge_i32 s47, 0\ns_cbranch_scc1 so_p0\n"
"s_memrealtime s[54:55]\ngr_p0:\nv_smfmac_f32_16x16x64_bf16 v[68:71], v[48:51], a[16:23], v8\ns_waitcnt lgkmcnt(4)\n"
"s_cmp_ge_u32 s56, s90\ns_cbranch_scc1 sx1_p0\ns_memrealtime s[56:57]\nv_smfmac_f32_16x16x64_bf16 v[72:75], v[48:51], a[72:79], v8\n"
"s_waitcnt lgkmcnt(4)\ns_cmp_ge_u32 s58, s90\ns_cbranch_scc1 sx2_p0\ns_memrealtime s[58:59]\n"
"v_smfmac_f32_16x16x64_bf16 v[76:79], v[48:51], a[128:135], v8\ns_waitcnt lgkmcnt(4)\ns_cmp_ge_u32 s54, s90\ns_cbranch_scc1 sx3_p0\n"
"s_memrealtime s[54:55]\nv_smfmac_f32_16x16x64_bf16 v[80:83], v[48:51], a[184:191], v8\ns_waitcnt lgkmcnt(4)\ns_cmp_ge_u32 s56, s90\n"
"s_cbranch_scc1 sx4_p0\ns_memrealtime s[56:57]\nv_smfmac_f32_16x16x64_bf16 v[68:71], v[52:55], a[24:31], v8\ns_waitcnt lgkmcnt(4)\n"
"s_cmp_ge_u32 s58, s90\ns_cbranch_scc1 sx5_p0\ns_memrealtime s[58:59]\nv_smfmac_f32_16x16x64_bf16 v[72:75], v[52:55], a[80:87], v8\n"
"s_waitcnt lgkmcnt(4)\ns_cmp_ge_u32 s54, s90\ns_cbranch_scc1 sx6_p0\ns_memrealtime s[54:55]\n"
"v_smfmac_f32_16x16x64_bf16 v[76:79], v[52:55], a[136:143], v8\ns_waitcnt lgkmcnt(4)\ns_cmp_ge_u32 s56, s90\ns_cbranch_scc1 sx7_p0\n"
"s_memrealtime s[56:57]\nv_smfmac_f32_16x16x64_bf16 v[80:83], v[52:55], a[192:199], v8\ns_waitcnt lgkmcnt(4)\ns_cmp_ge_u32 s58, s90\n"
"s_cbranch_scc1 sx8_p0\ns_memrealtime s[58:59]\nv_smfmac_f32_16x16x64_bf16 v[68:71], v[56:59], a[32:39], v8\ns_waitcnt lgkmcnt(4)\n"
"s_cmp_ge_u32 s54, s90\ns_cbranch_scc1 sx9_p0\ns_memrealtime s[54:55]\nv_smfmac_f32_16x16x64_bf16 v[72:75], v[56:59], a[88:95], v8\n"
"s_waitcnt lgkmcnt(4)\ns_cmp_ge_u32 s56, s90\ns_cbranch_scc1 sx10_p0\ns_memrealtime s[56:57]\n"
"v_smfmac_f32_16x16x64_bf16 v[76:79], v[56:59], a[144:151], v8\ns_waitcnt lgkmcnt(4)\ns_cmp_ge_u32 s58, s90\ns_cbranch_scc1 sx11_p0\n"
"s_memrealtime s[58:59]\nv_smfmac_f32_16x16x64_bf16 v[80:83], v[56:59], a[200:207], v8\ns_waitcnt lgkmcnt(4)\ns_cmp_ge_u32 s54, s90\n"
"s_cbranch_scc1 sx12_p0\ns_memrealtime s[54:55]\nv_smfmac_f32_16x16x64_bf16 v[68:71], v[60:63], a[40:47], v8\ns_waitcnt lgkmcnt(4)\n"
"s_cmp_ge_u32 s56, s90\ns_cbranch_scc1 sx13_p0\ns_memrealtime s[56:57]\nv_smfmac_f32_16x16x64_bf16 v[72:75], v[60:63], a[96:103], v8\n"
"s_waitcnt lgkmcnt(4)\ns_cmp_ge_u32 s58, s90\ns_cbranch_scc1 sx14_p0\ns_memrealtime s[58:59]\n"
"v_smfmac_f32_16x16x64_bf16 v[76:79], v[60:63], a[152:159], v8\ns_waitcnt lgkmcnt(4)\ns_cmp_ge_u32 s54, s90\ns_cbranch_scc1 sx15_p0\n"
"s_memrealtime s[54:55]\nv_smfmac_f32_16x16x64_bf16 v[80:83], v[60:63], a[208:215], v8\ns_waitcnt lgkmcnt(4)\ns_cmp_ge_u32 s56, s90\n"
"s_cbranch_scc1 sx16_p0\ns_memrealtime s[56:57]\nv_smfmac_f32_16x16x64_bf16 v[68:71], v[64:67], a[48:55], v8\ns_waitcnt lgkmcnt(4)\n"
"s_cmp_ge_u32 s58, s90\ns_cbranch_scc1 sx17_p0\ns_memrealtime s[58:59]\nv_smfmac_f32_16x16x64_bf16 v[72:75], v[64:67], a[104:111], v8\n"
"s_waitcnt lgkmcnt(4)\ns_cmp_ge_u32 s54, s90\ns_cbranch_scc1 sx18_p0\ns_memrealtime s[54:55]\n"
"v_smfmac_f32_16x16x64_bf16 v[76:79], v[64:67], a[160:167], v8\ns_waitcnt lgkmcnt(4)\ns_cmp_ge_u32 s56, s90\ns_cbranch_scc1 sx19_p0\n"
"s_memrealtime s[56:57]\nv_smfmac_f32_16x16x64_bf16 v[80:83], v[64:67], a[216:223], v8\ns_waitcnt lgkmcnt(4)\ns_cmp_ge_u32 s58, s90\n"
"s_cbranch_scc1 sx20_p0\ns_memrealtime s[58:59]\nsp_p0:\ns_waitcnt lgkmcnt(4)\n"
"s_sub_u32 s47, s54, s90\ns_cmp_ge_i32 s47, 0\ns_cbranch_scc1 sx20_p0\ns_memrealtime s[54:55]\n"
"s_waitcnt lgkmcnt(4)\ns_sub_u32 s47, s56, s90\ns_cmp_ge_i32 s47, 0\ns_cbranch_scc1 sx20_p0\n"
"s_memrealtime s[56:57]\ns_waitcnt lgkmcnt(4)\ns_sub_u32 s47, s58, s90\ns_cmp_ge_i32 s47, 0\n"
"s_cbranch_scc1 sx20_p0\ns_memrealtime s[58:59]\ns_waitcnt lgkmcnt(4)\ns_sub_u32 s47, s54, s90\n"
"s_cmp_ge_i32 s47, 0\ns_cbranch_scc1 sx20_p0\ns_memrealtime s[54:55]\ns_waitcnt lgkmcnt(4)\n"
"s_sub_u32 s47, s56, s90\ns_cmp_ge_i32 s47, 0\ns_cbranch_scc1 sx20_p0\ns_memrealtime s[56:57]\n"
"s_waitcnt lgkmcnt(4)\ns_sub_u32 s47, s58, s90\ns_cmp_ge_i32 s47, 0\ns_cbranch_scc1 sx20_p0\n"
"s_memrealtime s[58:59]\ns_cbranch_execnz sp_p0\ns_branch tl10_p0\nso_p0:\n"
"s_cmp_gt_i32 s47, 4\ns_cbranch_scc1 late_p0\ns_branch sgo_p0\ntl10_p0:\n"
"v_smfmac_f32_16x16x64_bf16 v[76:79], v[56:59], a[144:151], v8\ntl11_p0:\nv_smfmac_f32_16x16x64_bf16 v[80:83], v[56:59], a[200:207], v8\ntl12_p0:\n"
"v_smfmac_f32_16x16x64_bf16 v[68:71], v[60:63], a[40:47], v8\ntl13_p0:\nv_smfmac_f32_16x16x64_bf16 v[72:75], v[60:63], a[96:103], v8\ntl14_p0:\n"
"v_smfmac_f32_16x16x64_bf16 v[76:79], v[60:63], a[152:159], v8\ntl15_p0:\nv_smfmac_f32_16x16x64_bf16 v[80:83], v[60:63], a[208:215], v8\ntl16_p0:\n"
"v_smfmac_f32_16x16x64_bf16 v[68:71], v[64:67], a[48:55], v8\ntl17_p0:\nv_smfmac_f32_16x16x64_bf16 v[72:75], v[64:67], a[104:111], v8\ntl18_p0:\n"
"v_smfmac_f32_16x16x64_bf16 v[76:79], v[64:67], a[160:167], v8\ntl19_p0:\nv_smfmac_f32_16x16x64_bf16 v[80:83], v[64:67], a[216:223], v8\ntl20_p0:\n"
"v_add_f32 v219, v68, v69\nv_add_f32 v220, v70, v71\nv_cvt_pk_bf16_f32 v156, v219, v220\ntl21_p0:\n"
"v_add_f32 v219, v72, v73\nv_add_f32 v220, v74, v75\nv_cvt_pk_bf16_f32 v157, v219, v220\ntl22_p0:\n"
"v_add_f32 v219, v76, v77\nv_add_f32 v220, v78, v79\nv_cvt_pk_bf16_f32 v158, v219, v220\ntl23_p0:\n"
"v_add_f32 v219, v80, v81\nv_add_f32 v220, v82, v83\nv_cvt_pk_bf16_f32 v159, v219, v220\ntl24_p0:\n"
"s_cbranch_execz warm_p0\nbuffer_load_dwordx4 v[100:103], v9, s[16:19], s50 offen offset:0 sc1\nbuffer_load_dwordx4 v[104:107], v9, s[16:19], s50 offen offset:1024 sc1\nbuffer_load_dwordx4 v[108:111], v9, s[16:19], s50 offen offset:2048 sc1\n"
"buffer_load_dwordx4 v[112:115], v9, s[16:19], s50 offen offset:3072 sc1\ns_add_u32 s50, s50, 16384\nbuffer_store_dwordx4 v[156:159], v9, s[20:23], s51 offen sc1\ns_waitcnt vmcnt(8)\n"
"v_mfma_f32_16x16x32_bf16 v[132:135], v[84:87], v[116:119], 0\nv_mfma_f32_16x16x32_bf16 v[136:139], v[88:91], v[116:119], 0\nv_mfma_f32_16x16x32_bf16 v[148:151], v[176:179], v[116:119], 0\ns_waitcnt vmcnt(7)\n"
"v_mfma_f32_16x16x32_bf16 v[140:143], v[92:95], v[120:123], 0\nv_mfma_f32_16x16x32_bf16 v[144:147], v[96:99], v[120:123], 0\nv_mfma_f32_16x16x32_bf16 v[148:151], v[180:183], v[120:123], v[148:151]\nv_cndmask_b32 v132, v136, v132, s[62:63]\n"
"v_cndmask_b32 v133, v137, v133, s[62:63]\nv_cndmask_b32 v134, v138, v134, s[62:63]\nv_cndmask_b32 v135, v139, v135, s[62:63]\ns_waitcnt vmcnt(6)\n"
"v_mfma_f32_16x16x32_f16 v[152:155], v[168:171], v[124:127], 0\ns_waitcnt vmcnt(5)\nv_mfma_f32_16x16x32_f16 v[152:155], v[172:175], v[128:131], v[152:155]\nv_cndmask_b32 v140, v144, v140, s[62:63]\n"
"v_cndmask_b32 v141, v145, v141, s[62:63]\nv_cndmask_b32 v142, v146, v142, s[62:63]\nv_cndmask_b32 v143, v147, v143, s[62:63]\ns_nop 2\n"
"v_exp_f32 v152, v152\nv_exp_f32 v153, v153\nv_exp_f32 v154, v154\nv_exp_f32 v155, v155\n"
"v_cvt_pk_bf16_f32 v200, v152, v153\nv_cvt_pk_bf16_f32 v201, v154, v155\nv_and_b32 v160, v200, v223\nv_and_b32 v161, v200, v224\n"
"v_mfma_f32_16x16x16_bf16 v[148:151], v[214:215], v[200:201], v[148:151]\nv_and_b32 v162, v201, v223\nv_and_b32 v163, v201, v224\nv_mfma_f32_16x16x16_bf16 v[132:135], v[156:157], v[160:161], v[132:135]\n"
"s_nop 0\nv_mfma_f32_16x16x16_bf16 v[140:143], v[158:159], v[162:163], v[140:143]\nv_rcp_f32 v202, v148\nv_rcp_f32 v203, v149\n"
"v_mul_f32 v132, v132, v202\nv_mul_f32 v133, v133, v202\nv_mul_f32 v134, v134, v202\nv_mul_f32 v135, v135, v202\n"
"v_fmac_f32 v132, v140, v203\nv_fmac_f32 v133, v141, v203\nv_fmac_f32 v134, v142, v203\nv_fmac_f32 v135, v143, v203\n"
"v_add_f32_dpp v204, v132, v132 row_ror:8 row_mask:0xf bank_mask:0x3\nv_add_f32_dpp v205, v133, v133 row_ror:8 row_mask:0xf bank_mask:0x3\nv_add_f32_dpp v204, v134, v134 row_ror:8 row_mask:0xf bank_mask:0xc\nv_add_f32_dpp v205, v135, v135 row_ror:8 row_mask:0xf bank_mask:0xc\n"
"v_subrev_u32 v219, 0x23800000, v148\nv_subrev_u32 v220, 0x23800000, v149\nv_max_u32 v219, v219, v220\nv_cmp_le_u32 vcc, 0x38000000, v219\n"
"ds_write_b64 v22, v[204:205]\ns_or_b64 s[56:57], vcc, s[94:95]\ns_cbranch_scc1 slow_p0\njoin_p0:\n"
"v_mov_b32 v219, 0\ns_nop 1\nv_mov_b32 v219, 0\ns_waitcnt lgkmcnt(0)\n"
"s_barrier\nds_read_b64 v[206:207], v23 offset:0\nds_read_b64 v[208:209], v23 offset:1280\nds_read_b64 v[210:211], v23 offset:2560\n"
"ds_read_b64 v[212:213], v23 offset:3840\nds_read_b32 v221, v222\nv_mov_b32 v219, 0\ns_add_u32 s40, s40, 1\n"
"s_mov_b32 s43, s41\ns_add_u32 s41, s41, 131072\nv_mov_b32 v219, 0\ns_and_b32 s41, s41, 524287\n"
"s_add_u32 s92, s92, 1\ns_add_u32 s51, s51, 4096\nv_mov_b32 v219, 0\ns_mov_b32 s42, 1\n"
"s_waitcnt lgkmcnt(3)\nv_pk_add_f32 v[206:207], v[206:207], v[208:209]\ns_waitcnt lgkmcnt(1)\nv_pk_add_f32 v[210:211], v[210:211], v[212:213]\n"
"v_pk_add_f32 v[206:207], v[206:207], v[210:211]\nv_cvt_pk_bf16_f32 v220, v206, v207\nbuffer_store_dword v220, v16, s[8:11], s43 offen sc1\ns_waitcnt lgkmcnt(0)\n"
"s_memrealtime s[54:55]\nv_readfirstlane_b32 s47, v221\ns_nop 1\ns_memrealtime s[56:57]\n"
"s_sub_u32 s47, s47, s40\ns_cmp_lg_u32 s47, 1\ns_memrealtime s[58:59]\ns_cbranch_scc1 mg_p0\n"
"rp0_p0:\ns_cbranch_execz rp1_p0\ns_lshl_b32 s52, s40, 26\ns_cmp_le_u32 s92, 2\n"
"s_cselect_b32 s48, s33, s38\ns_waitcnt lgkmcnt(0)\ns_sub_u32 s47, s54, s30\ns_add_u32 s47, s47, s48\n"
"rp1_p0:\ns_cbranch_execz rp2_p0\ns_min_u32 s47, s47, 0x3ffff\ns_lshl_b32 s47, s47, 8\n"
"s_or_b32 s47, s47, s52\ns_sub_u32 s52, s92, 2\ns_cmp_lt_u32 s52, 200\ns_cselect_b32 s48, 1, 0\n"
"rp2_p0:\ns_cbranch_execz rp3_p0\ns_cmp_gt_u32 s40, 3\ns_cselect_b32 s48, s48, 0\n"
"s_add_u32 s48, s48, s46\ns_min_u32 s48, s48, 15\ns_or_b32 s47, s47, s48\nv_mov_b32 v225, s47\n"
"rp3_p0:\ns_cbranch_execz rp4_p0\nbuffer_store_dword v225, v19, s[8:11], 0 offen sc1\ns_sleep 6\n"
"s_branch top2_p0\nlast_p0:\nv_smfmac_f32_16x16x64_bf16 v[68:71], v[48:51], a[16:23], v8\nv_smfmac_f32_16x16x64_bf16 v[72:75], v[48:51], a[72:79], v8\n"
"v_smfmac_f32_16x16x64_bf16 v[76:79], v[48:51], a[128:135], v8\nrp4_p0:\ns_cbranch_execz rp5_p0\nv_smfmac_f32_16x16x64_bf16 v[80:83], v[48:51], a[184:191], v8\n"
"v_smfmac_f32_16x16x64_bf16 v[68:71], v[52:55], a[24:31], v8\nv_smfmac_f32_16x16x64_bf16 v[72:75], v[52:55], a[80:87], v8\nv_smfmac_f32_16x16x64_bf16 v[76:79], v[52:55], a[136:143], v8\nv_smfmac_f32_16x16x64_bf16 v[80:83], v[52:55], a[192:199], v8\n"
"v_smfmac_f32_16x16x64_bf16 v[68:71], v[56:59], a[32:39], v8\nrp5_p0:\ns_cbranch_execz rp6_p0\nv_smfmac_f32_16x16x64_bf16 v[72:75], v[56:59], a[88:95], v8\n"
"v_smfmac_f32_16x16x64_bf16 v[76:79], v[56:59], a[144:151], v8\nv_smfmac_f32_16x16x64_bf16 v[80:83], v[56:59], a[200:207], v8\nv_smfmac_f32_16x16x64_bf16 v[68:71], v[60:63], a[40:47], v8\nv_smfmac_f32_16x16x64_bf16 v[72:75], v[60:63], a[96:103], v8\n"
"v_smfmac_f32_16x16x64_bf16 v[76:79], v[60:63], a[152:159], v8\nrp6_p0:\ns_cbranch_execz rp7_p0\nv_smfmac_f32_16x16x64_bf16 v[80:83], v[60:63], a[208:215], v8\n"
"v_smfmac_f32_16x16x64_bf16 v[68:71], v[64:67], a[48:55], v8\nv_smfmac_f32_16x16x64_bf16 v[72:75], v[64:67], a[104:111], v8\nv_smfmac_f32_16x16x64_bf16 v[76:79], v[64:67], a[160:167], v8\nv_smfmac_f32_16x16x64_bf16 v[80:83], v[64:67], a[216:223], v8\n"
"v_add_f32 v219, v68, v69\nrp7_p0:\ns_cbranch_execz rp8_p0\nv_add_f32 v220, v70, v71\n"
"v_cvt_pk_bf16_f32 v156, v219, v220\nv_add_f32 v219, v72, v73\nv_add_f32 v220, v74, v75\nv_cvt_pk_bf16_f32 v157, v219, v220\n"
"v_add_f32 v219, v76, v77\nrp8_p0:\ns_cbranch_execz rp9_p0\nv_add_f32 v220, v78, v79\n"
"v_cvt_pk_bf16_f32 v158, v219, v220\nv_add_f32 v219, v80, v81\nv_add_f32 v220, v82, v83\nv_cvt_pk_bf16_f32 v159, v219, v220\n"
"buffer_store_dwordx4 v[156:159], v9, s[20:23], s51 offen sc1\nrp9_p0:\ns_cbranch_execz rp10_p0\nv_mov_b32 v220, s34\n"
"buffer_store_dword v220, v15, s[8:11], 0 offen sc1\ns_branch end_p0\nretry1_p0:\ns_add_u32 s53, s53, 256\n"
"s_add_u32 s49, s49, 1\ns_cmp_gt_u32 s49, 65536\nrp10_p0:\ns_cbranch_execz rp11_p0\n"
"s_cbranch_scc1 end_p0\ns_add_u32 s47, s40, 1\nv_mov_b32 v221, s47\nds_write_b32 v222, v221\n"
"s_sleep 2\ns_branch top2_p0\nadopt_p0:\nbuffer_load_dword v164, v28, s[8:11], 0 offen sc1\n"
"rp11_p0:\ns_cbranch_execz rp12_p0\nv_smfmac_f32_16x16x64_bf16 v[68:71], v[48:51], a[16:23], v8\nv_smfmac_f32_16x16x64_bf16 v[72:75], v[48:51], a[72:79], v8\n"
"v_smfmac_f32_16x16x64_bf16 v[76:79], v[48:51], a[128:135], v8\nv_smfmac_f32_16x16x64_bf16 v[80:83], v[48:51], a[184:191], v8\nv_smfmac_f32_16x16x64_bf16 v[68:71], v[52:55], a[24:31], v8\nv_smfmac_f32_16x16x64_bf16 v[72:75], v[52:55], a[80:87], v8\n"
"rp12_p0:\ns_cbranch_execz rp13_p0\nv_smfmac_f32_16x16x64_bf16 v[76:79], v[52:55], a[136:143], v8\nv_smfmac_f32_16x16x64_bf16 v[80:83], v[52:55], a[192:199], v8\n"
"v_smfmac_f32_16x16x64_bf16 v[68:71], v[56:59], a[32:39], v8\nv_smfmac_f32_16x16x64_bf16 v[72:75], v[56:59], a[88:95], v8\nv_smfmac_f32_16x16x64_bf16 v[76:79], v[56:59], a[144:151], v8\nv_smfmac_f32_16x16x64_bf16 v[80:83], v[56:59], a[200:207], v8\n"
"rp13_p0:\ns_cbranch_execz rp14_p0\nv_smfmac_f32_16x16x64_bf16 v[68:71], v[60:63], a[40:47], v8\nv_smfmac_f32_16x16x64_bf16 v[72:75], v[60:63], a[96:103], v8\n"
"v_smfmac_f32_16x16x64_bf16 v[76:79], v[60:63], a[152:159], v8\nv_smfmac_f32_16x16x64_bf16 v[80:83], v[60:63], a[208:215], v8\nv_smfmac_f32_16x16x64_bf16 v[68:71], v[64:67], a[48:55], v8\nv_smfmac_f32_16x16x64_bf16 v[72:75], v[64:67], a[104:111], v8\n"
"rp14_p0:\ns_cbranch_execz rp15_p0\nv_smfmac_f32_16x16x64_bf16 v[76:79], v[64:67], a[160:167], v8\nv_smfmac_f32_16x16x64_bf16 v[80:83], v[64:67], a[216:223], v8\n"
"s_lshl_b32 s52, s40, 26\ns_or_b32 s47, s52, 0x3ffffff\ns_waitcnt vmcnt(0)\nv_cmp_ge_u32 vcc, s47, v164\n"
"rp15_p0:\ns_cbranch_execz rp16_p0\ns_nop 1\nv_cndmask_b32 v221, 0, v164, vcc\n"
"s_nop 1\nv_max_u32_dpp v221, v221, v221 row_ror:8 row_mask:0xf bank_mask:0xf\ns_nop 1\nv_max_u32_dpp v221, v221, v221 row_ror:4 row_mask:0xf bank_mask:0xf\n"
"rp16_p0:\ns_cbranch_execz rp17_p0\ns_nop 1\nv_max_u32_dpp v221, v221, v221 row_ror:2 row_mask:0xf bank_mask:0xf\n"
"s_nop 1\nv_max_u32_dpp v221, v221, v221 row_ror:1 row_mask:0xf bank_mask:0xf\ns_nop 0\nv_readlane_b32 s47, v221, 0\n"
"rp17_p0:\ns_cbranch_execz rp18_p0\nv_readlane_b32 s48, v221, 16\ns_max_u32 s48, s48, s47\n"
"s_cmp_le_u32 s48, s52\ns_cbranch_scc1 back3_p0\ns_bfe_u32 s52, s48, 1179656\ns_add_u32 s52, s52, s30\n"
"rp18_p0:\ns_cbranch_execz rp19_p0\ns_sub_u32 s45, s52, s44\ns_and_b32 s45, s45, 3\n"
"s_sub_u32 s44, s52, s45\ns_lshl_b32 s45, s45, 2\ns_and_b32 s46, s48, 255\ns_mov_b32 s92, 0\n"
"rp19_p0:\ns_cbranch_execz rp20_p0\ns_branch back2_p0\nback2_p0:\n"
"s_and_b32 s47, s46, 15\ns_mul_i32 s47, s47, 4\ns_add_u32 s47, s47, s31\ns_add_u32 s47, s47, s45\n"
"s_and_b32 s91, s47, 15\nrp20_p0:\ns_cbranch_execz rp21_p0\ns_lshr_b32 s47, s47, 4\n"
"s_lshl_b32 s47, s47, 2\ns_add_u32 s90, s44, s47\ns_mul_i32 s47, s91, 6\ns_add_u32 s47, s47, s93\n"
"s_lshr_b32 s47, s47, 4\nrp21_p0:\ns_cbranch_execz rp22_p0\ns_mul_i32 s47, s47, 300\n"
"s_getpc_b64 s[60:61]\nep2_p0:\ns_add_u32 s47, s47, sgo_p0-ep2_p0\ns_add_u32 s60, s60, s47\n"
"s_addc_u32 s61, s61, 0\ns_mul_i32 s48, s46, 8\nrp22_p0:\ns_cbranch_execz rp23_p0\n"
"s_add_u32 s45, s45, s48\ns_add_u32 s45, s45, s32\ns_lshr_b32 s48, s45, 4\ns_lshl_b32 s48, s48, 2\n"
"s_add_u32 s44, s44, s48\ns_and_b32 s45, s45, 15\nrp23_p0:\ns_cbranch_execz rp24_p0\n"
"s_add_u32 s52, s40, 1\ns_bitcmp1_b64 s[28:29], s52\ns_subb_u32 s46, s46, 0\ns_mul_i32 s48, s45, 6\n"
"s_and_b32 s48, s48, -4\ns_getpc_b64 s[88:89]\nmpc2_p0:\ns_sub_u32 s48, mgo_p0-mpc2_p0, s48\n"
"rp24_p0:\ns_cbranch_execz rp25_p0\ns_add_u32 s88, s88, s48\ns_addc_u32 s89, s89, -1\n"
"back3_p0:\ns_waitcnt lgkmcnt(0)\ns_memrealtime s[54:55]\ns_nop 2\n"
"s_memrealtime s[56:57]\nrp25_p0:\ns_cbranch_execz rp26_p0\ns_nop 2\n"
"s_memrealtime s[58:59]\ns_waitcnt lgkmcnt(0)\ns_sub_u32 s47, s58, s90\ns_cmp_lt_i32 s47, 0\n"
"s_cbranch_scc1 sp_p0\nrp26_p0:\ns_cbranch_execz rp27_p0\ns_cmp_le_i32 s47, 0\n"
"s_cbranch_scc1 sgn_p0\ns_add_u32 s53, s53, 16777216\ns_mov_b64 s[94:95], 1\ns_add_u32 s52, s90, 32\n"
"lw2_p0:\ns_memrealtime s[54:55]\nrp27_p0:\ns_cbranch_execz rp28_p0\n"
"s_waitcnt lgkmcnt(0)\ns_sub_u32 s47, s54, s52\ns_cmp_lt_i32 s47, 0\ns_cbranch_scc1 lw2_p0\n"
"s_branch sgn_p0\nlate_p0:\ns_add_u32 s53, s53, 16777216\nrp28_p0:\n"
"s_cbranch_execz rp29_p0\ns_mov_b64 s[94:95], 1\ns_add_u32 s52, s90, 32\nlw_p0:\n"
"s_memrealtime s[54:55]\ns_waitcnt lgkmcnt(0)\ns_sub_u32 s47, s54, s52\ns_cmp_lt_i32 s47, 0\n"
"rp29_p0:\ns_cbranch_execz rp30_p0\ns_cbranch_scc1 lw_p0\ns_branch sgo_p0\n"
"slow_p0:\ns_mov_b64 s[94:95], 0\ns_cbranch_vccnz slow2_p0\ns_add_u32 s47, s40, 2\n"
"v_mov_b32 v221, s47\nrp30_p0:\ns_cbranch_execz rp31_p0\nds_write_b32 v222, v221\n"
"s_branch join_p0\nslow2_p0:\nv_max3_u32 v218, v124, v125, v126\nv_max_u32 v218, v218, v127\n"
"v_max3_u32 v218, v218, v128, v129\nv_max3_u32 v218, v218, v130, v131\nrp31_p0:\ns_cbranch_execz rp32_p0\n"
"v_max3_u32 v218, v218, v116, v117\nv_max3_u32 v218, v218, v118, v119\nv_max3_u32 v218, v218, v120, v121\nv_max3_u32 v218, v218, v122, v123\n"
"v_cmp_eq_u32 vcc, -1, v218\ns_cbranch_vccz exact_p0\nrp32_p0:\ns_cbranch_execz rp33_p0\n"
"s_branch spin_p0\nspin2_p0:\ns_sleep 3\nspin_p0:\n"
"s_add_u32 s53, s53, 1\ns_add_u32 s49, s49, 1\ns_cmp_gt_u32 s49, 65536\ns_cbranch_scc1 end_p0\n"
"rp33_p0:\ns_cbranch_execz rp34_p0\nbuffer_load_dwordx4 v[116:119], v20, s[8:11], s41 offen offset:0 sc1\nbuffer_load_dwordx4 v[120:123], v20, s[8:11], s41 offen offset:1024 sc1\n"
"buffer_load_dwordx4 v[124:127], v21, s[8:11], s41 offen offset:0 sc1\nbuffer_load_dwordx4 v[128:131], v21, s[8:11], s41 offen offset:1024 sc1\ns_waitcnt vmcnt(0)\nv_max3_u32 v218, v124, v125, v126\n"
"rp34_p0:\ns_cbranch_execz rp35_p0\nv_max_u32 v218, v218, v127\nv_max3_u32 v218, v218, v128, v129\n"
"v_max3_u32 v218, v218, v130, v131\nv_max3_u32 v218, v218, v116, v117\nv_max3_u32 v218, v218, v118, v119\nv_max3_u32 v218, v218, v120, v121\n"
"rp35_p0:\ns_cbranch_execz rp36_p0\nv_max3_u32 v218, v218, v122, v123\nv_cmp_eq_u32 vcc, -1, v218\n"
"s_cbranch_vccnz spin2_p0\ns_add_u32 s47, s40, 2\nv_mov_b32 v221, s47\nds_write_b32 v222, v221\n"
"rp36_p0:\ns_cbranch_execz rp37_p0\nv_mfma_f32_16x16x32_bf16 v[132:135], v[84:87], v[116:119], 0\nv_mfma_f32_16x16x32_bf16 v[136:139], v[88:91], v[116:119], 0\n"
"v_mfma_f32_16x16x32_bf16 v[148:151], v[176:179], v[116:119], 0\nv_mfma_f32_16x16x32_bf16 v[140:143], v[92:95], v[120:123], 0\nv_mfma_f32_16x16x32_bf16 v[144:147], v[96:99], v[120:123], 0\nv_mfma_f32_16x16x32_bf16 v[148:151], v[180:183], v[120:123], v[148:151]\n"
"rp37_p0:\ns_cbranch_execz rp38_p0\nv_cndmask_b32 v132, v136, v132, s[62:63]\nv_cndmask_b32 v133, v137, v133, s[62:63]\n"
"v_cndmask_b32 v134, v138, v134, s[62:63]\nv_cndmask_b32 v135, v139, v135, s[62:63]\nv_mfma_f32_16x16x32_f16 v[152:155], v[168:171], v[124:127], 0\nv_mfma_f32_16x16x32_f16 v[152:155], v[172:175], v[128:131], v[152:155]\n"
"rp38_p0:\ns_cbranch_execz rp39_p0\nv_cndmask_b32 v140, v144, v140, s[62:63]\nv_cndmask_b32 v141, v145, v141, s[62:63]\n"
"v_cndmask_b32 v142, v146, v142, s[62:63]\nv_cndmask_b32 v143, v147, v143, s[62:63]\ns_nop 2\nv_exp_f32 v152, v152\n"
"rp39_p0:\ns_cbranch_execz rp40_p0\nv_exp_f32 v153, v153\nv_exp_f32 v154, v154\n"
"v_exp_f32 v155, v155\nv_cvt_pk_bf16_f32 v200, v152, v153\nv_cvt_pk_bf16_f32 v201, v154, v155\nv_and_b32 v160, v200, v223\n"
"rp40_p0:\ns_cbranch_execz rp41_p0\nv_and_b32 v161, v200, v224\nv_mfma_f32_16x16x16_bf16 v[148:151], v[214:215], v[200:201], v[148:151]\n"
"v_and_b32 v162, v201, v223\nv_and_b32 v163, v201, v224\nv_mfma_f32_16x16x16_bf16 v[132:135], v[156:157], v[160:161], v[132:135]\ns_nop 0\n"
"rp41_p0:\ns_cbranch_execz rp42_p0\nv_mfma_f32_16x16x16_bf16 v[140:143], v[158:159], v[162:163], v[140:143]\nv_rcp_f32 v202, v148\n"
"v_rcp_f32 v203, v149\nv_mul_f32 v132, v132, v202\nv_mul_f32 v133, v133, v202\nv_mul_f32 v134, v134, v202\n"
"rp42_p0:\ns_cbranch_execz rp43_p0\nv_mul_f32 v135, v135, v202\nv_fmac_f32 v132, v140, v203\n"
"v_fmac_f32 v133, v141, v203\nv_fmac_f32 v134, v142, v203\nv_fmac_f32 v135, v143, v203\nv_add_f32_dpp v204, v132, v132 row_ror:8 row_mask:0xf bank_mask:0x3\n"
"rp43_p0:\ns_cbranch_execz rp44_p0\nv_add_f32_dpp v205, v133, v133 row_ror:8 row_mask:0xf bank_mask:0x3\nv_add_f32_dpp v204, v134, v134 row_ror:8 row_mask:0xf bank_mask:0xc\n"
"v_add_f32_dpp v205, v135, v135 row_ror:8 row_mask:0xf bank_mask:0xc\nv_subrev_u32 v219, 0x23800000, v148\nv_subrev_u32 v220, 0x23800000, v149\nv_max_u32 v219, v219, v220\n"
"rp44_p0:\ns_cbranch_execz rp45_p0\nv_cmp_le_u32 vcc, 0x38000000, v219\nds_write_b64 v22, v[204:205]\n"
"s_cbranch_vccz join_p0\nrp45_p0:\ns_cbranch_execz warm_p0\nexact_p0:\n"
"s_add_u32 s53, s53, 65536\nex0_p0:\ns_add_u32 s47, s41, 0\nbuffer_load_dwordx2 v[184:185], v24, s[8:11], s47 offen offset:0 sc1\n"
"buffer_load_dwordx2 v[186:187], v24, s[8:11], s47 offen offset:512 sc1\nbuffer_load_dwordx2 v[188:189], v24, s[8:11], s47 offen offset:1024 sc1\nbuffer_load_dwordx2 v[190:191], v24, s[8:11], s47 offen offset:1536 sc1\nbuffer_load_dwordx4 v[192:195], v25, s[8:11], s47 offen sc1\n"
"buffer_load_dwordx4 v[196:199], v25, s[8:11], s47 offen offset:16 sc1\nv_mov_b32 v218, 0\ns_waitcnt vmcnt(0)\nv_max3_u32 v218, v218, v184, v185\n"
"v_max3_u32 v218, v218, v186, v187\nv_max3_u32 v218, v218, v188, v189\nv_max3_u32 v218, v218, v190, v191\nv_max3_u32 v218, v218, v192, v193\n"
"v_max3_u32 v218, v218, v194, v195\nv_max3_u32 v218, v218, v196, v197\nv_max3_u32 v218, v218, v198, v199\nv_cmp_eq_u32 vcc, -1, v218\n"
"s_cbranch_vccz exok0_p0\ns_add_u32 s53, s53, 0\ns_add_u32 s49, s49, 1\ns_cmp_gt_u32 s49, 65536\n"
"s_cbranch_scc1 end_p0\ns_branch ex0_p0\nexok0_p0:\nv_add_f32 v184, v184, v186\n"
"v_add_f32 v188, v188, v190\nv_add_f32 v184, v184, v188\nv_add_f32 v185, v185, v187\nv_add_f32 v189, v189, v191\n"
"v_add_f32 v185, v185, v189\nv_max_f32 v225, v184, v185\nv_max3_f32 v225, v225, v192, v193\nv_max3_f32 v225, v225, v194, v195\n"
"v_max3_f32 v225, v225, v196, v197\nv_max3_f32 v225, v225, v198, v199\nv_mov_b32 v219, v225\ns_nop 1\n"
"v_permlane16_swap_b32 v225, v219\nv_max_f32 v225, v225, v219\nv_mov_b32 v219, v225\ns_nop 1\n"
"v_permlane32_swap_b32 v225, v219\nv_max_f32 v225, v225, v219\nv_sub_f32 v184, v184, v225\nv_exp_f32 v152, v184\n"
"v_sub_f32 v185, v185, v225\nv_exp_f32 v153, v185\nv_sub_f32 v192, v192, v225\nv_exp_f32 v192, v192\n"
"v_sub_f32 v193, v193, v225\nv_exp_f32 v193, v193\nv_sub_f32 v194, v194, v225\nv_exp_f32 v194, v194\n"
"v_sub_f32 v195, v195, v225\nv_exp_f32 v195, v195\nv_sub_f32 v196, v196, v225\nv_exp_f32 v196, v196\n"
"v_sub_f32 v197, v197, v225\nv_exp_f32 v197, v197\nv_sub_f32 v198, v198, v225\nv_exp_f32 v198, v198\n"
"v_sub_f32 v199, v199, v225\nv_exp_f32 v199, v199\ns_nop 0\nv_cvt_pk_bf16_f32 v116, v192, v193\n"
"v_cvt_pk_bf16_f32 v117, v194, v195\nv_cvt_pk_bf16_f32 v118, v196, v197\nv_cvt_pk_bf16_f32 v119, v198, v199\nex1_p0:\n"
"s_add_u32 s47, s41, 4096\nbuffer_load_dwordx2 v[184:185], v24, s[8:11], s47 offen offset:0 sc1\nbuffer_load_dwordx2 v[186:187], v24, s[8:11], s47 offen offset:512 sc1\nbuffer_load_dwordx2 v[188:189], v24, s[8:11], s47 offen offset:1024 sc1\n"
"buffer_load_dwordx2 v[190:191], v24, s[8:11], s47 offen offset:1536 sc1\nbuffer_load_dwordx4 v[192:195], v25, s[8:11], s47 offen sc1\nbuffer_load_dwordx4 v[196:199], v25, s[8:11], s47 offen offset:16 sc1\nv_mov_b32 v218, 0\n"
"s_waitcnt vmcnt(0)\nv_max3_u32 v218, v218, v184, v185\nv_max3_u32 v218, v218, v186, v187\nv_max3_u32 v218, v218, v188, v189\n"
"v_max3_u32 v218, v218, v190, v191\nv_max3_u32 v218, v218, v192, v193\nv_max3_u32 v218, v218, v194, v195\nv_max3_u32 v218, v218, v196, v197\n"
"v_max3_u32 v218, v218, v198, v199\nv_cmp_eq_u32 vcc, -1, v218\ns_cbranch_vccz exok1_p0\ns_add_u32 s53, s53, 0\n"
"s_add_u32 s49, s49, 1\ns_cmp_gt_u32 s49, 65536\ns_cbranch_scc1 end_p0\ns_branch ex1_p0\n"
"exok1_p0:\nv_add_f32 v184, v184, v186\nv_add_f32 v188, v188, v190\nv_add_f32 v184, v184, v188\n"
"v_add_f32 v185, v185, v187\nv_add_f32 v189, v189, v191\nv_add_f32 v185, v185, v189\nv_max_f32 v225, v184, v185\n"
"v_max3_f32 v225, v225, v192, v193\nv_max3_f32 v225, v225, v194, v195\nv_max3_f32 v225, v225, v196, v197\nv_max3_f32 v225, v225, v198, v199\n"
"v_mov_b32 v219, v225\ns_nop 1\nv_permlane16_swap_b32 v225, v219\nv_max_f32 v225, v225, v219\n"
"v_mov_b32 v219, v225\ns_nop 1\nv_permlane32_swap_b32 v225, v219\nv_max_f32 v225, v225, v219\n"
"v_sub_f32 v184, v184, v225\nv_exp_f32 v154, v184\nv_sub_f32 v185, v185, v225\nv_exp_f32 v155, v185\n"
"v_sub_f32 v192, v192, v225\nv_exp_f32 v192, v192\nv_sub_f32 v193, v193, v225\nv_exp_f32 v193, v193\n"
"v_sub_f32 v194, v194, v225\nv_exp_f32 v194, v194\nv_sub_f32 v195, v195, v225\nv_exp_f32 v195, v195\n"
"v_sub_f32 v196, v196, v225\nv_exp_f32 v196, v196\nv_sub_f32 v197, v197, v225\nv_exp_f32 v197, v197\n"
"v_sub_f32 v198, v198, v225\nv_exp_f32 v198, v198\nv_sub_f32 v199, v199, v225\nv_exp_f32 v199, v199\n"
"s_nop 0\nv_cvt_pk_bf16_f32 v120, v192, v193\nv_cvt_pk_bf16_f32 v121, v194, v195\nv_cvt_pk_bf16_f32 v122, v196, v197\n"
"v_cvt_pk_bf16_f32 v123, v198, v199\ns_add_u32 s47, s40, 2\nv_mov_b32 v221, s47\nds_write_b32 v222, v221\n"
"v_mfma_f32_16x16x32_bf16 v[132:135], v[84:87], v[116:119], 0\nv_mfma_f32_16x16x32_bf16 v[136:139], v[88:91], v[116:119], 0\nv_mfma_f32_16x16x32_bf16 v[148:151], v[176:179], v[116:119], 0\nv_mfma_f32_16x16x32_bf16 v[140:143], v[92:95], v[120:123], 0\n"
"v_mfma_f32_16x16x32_bf16 v[144:147], v[96:99], v[120:123], 0\nv_mfma_f32_16x16x32_bf16 v[148:151], v[180:183], v[120:123], v[148:151]\nv_cndmask_b32 v132, v136, v132, s[62:63]\nv_cndmask_b32 v133, v137, v133, s[62:63]\n"
"v_cndmask_b32 v134, v138, v134, s[62:63]\nv_cndmask_b32 v135, v139, v135, s[62:63]\ns_nop 0\nv_cndmask_b32 v140, v144, v140, s[62:63]\n"
"v_cndmask_b32 v141, v145, v141, s[62:63]\nv_cndmask_b32 v142, v146, v142, s[62:63]\nv_cndmask_b32 v143, v147, v143, s[62:63]\nv_cvt_pk_bf16_f32 v200, v152, v153\n"
"v_cvt_pk_bf16_f32 v201, v154, v155\nv_and_b32 v160, v200, v223\nv_and_b32 v161, v200, v224\nv_mfma_f32_16x16x16_bf16 v[148:151], v[214:215], v[200:201], v[148:151]\n"
"v_and_b32 v162, v201, v223\nv_and_b32 v163, v201, v224\nv_mfma_f32_16x16x16_bf16 v[132:135], v[156:157], v[160:161], v[132:135]\ns_nop 0\n"
"v_mfma_f32_16x16x16_bf16 v[140:143], v[158:159], v[162:163], v[140:143]\nv_rcp_f32 v202, v148\nv_rcp_f32 v203, v149\nv_mul_f32 v132, v132, v202\n"
"v_mul_f32 v133, v133, v202\nv_mul_f32 v134, v134, v202\nv_mul_f32 v135, v135, v202\nv_fmac_f32 v132, v140, v203\n"
"v_fmac_f32 v133, v141, v203\nv_fmac_f32 v134, v142, v203\nv_fmac_f32 v135, v143, v203\nv_add_f32_dpp v204, v132, v132 row_ror:8 row_mask:0xf bank_mask:0x3\n"
"v_add_f32_dpp v205, v133, v133 row_ror:8 row_mask:0xf bank_mask:0x3\nv_add_f32_dpp v204, v134, v134 row_ror:8 row_mask:0xf bank_mask:0xc\nv_add_f32_dpp v205, v135, v135 row_ror:8 row_mask:0xf bank_mask:0xc\nds_write_b64 v22, v[204:205]\n"
"s_branch join_p0\n.p2align 6\nva0_p0:\ns_branch warm_p0\n"
"sx0_p0:\ns_add_u32 s60, s60, 308\ns_addc_u32 s61, s61, 0\ns_setpc_b64 s[60:61]\n"
"vb0_p0:\ns_branch vr0_p0\ns_nop 1\ns_nop 1\n"
"s_nop 1\ns_nop 1\ns_nop 1\ns_nop 1\n"
"s_nop 1\ns_nop 1\ns_nop 1\ns_nop 1\n"
"s_nop 1\ns_nop 1\ns_nop 1\ns_nop 1\n"
"vr0_p0:\ns_nop 1\ns_nop 1\ns_nop 1\n"
"s_nop 1\ns_nop 1\ns_nop 1\ns_nop 1\n"
"s_nop 1\nsgo_p0:\ns_cbranch_execz vc0x0_p0\nbuffer_load_dwordx4 v[116:119], v20, s[8:11], s41 offen offset:0 sc1\n"
"buffer_load_dwordx4 v[120:123], v20, s[8:11], s41 offen offset:1024 sc1\nv_smfmac_f32_16x16x64_bf16 v[68:71], v[48:51], a[16:23], v8\nv_smfmac_f32_16x16x64_bf16 v[72:75], v[48:51], a[72:79], v8\nv_smfmac_f32_16x16x64_bf16 v[76:79], v[48:51], a[128:135], v8\n"
"v_smfmac_f32_16x16x64_bf16 v[80:83], v[48:51], a[184:191], v8\nv_smfmac_f32_16x16x64_bf16 v[68:71], v[52:55], a[24:31], v8\nvc0x0_p0:\ns_cbranch_execz vc0x1_p0\n"
"v_smfmac_f32_16x16x64_bf16 v[72:75], v[52:55], a[80:87], v8\nv_smfmac_f32_16x16x64_bf16 v[76:79], v[52:55], a[136:143], v8\nv_smfmac_f32_16x16x64_bf16 v[80:83], v[52:55], a[192:199], v8\nv_smfmac_f32_16x16x64_bf16 v[68:71], v[56:59], a[32:39], v8\n"
"v_smfmac_f32_16x16x64_bf16 v[72:75], v[56:59], a[88:95], v8\nvc0x1_p0:\ns_cbranch_execz vd0_p0\nbuffer_load_dwordx4 v[124:127], v21, s[8:11], s41 offen offset:0 sc1\n"
"buffer_load_dwordx4 v[128:131], v21, s[8:11], s41 offen offset:1024 sc1\nvd0_p0:\ns_cbranch_execnz tl10_p0\ns_nop 0\n"
"s_nop 0\ns_nop 0\ns_nop 0\ns_nop 0\n"
"s_nop 0\ns_nop 0\ns_nop 0\ns_nop 0\n"
"s_nop 0\ns_nop 0\ns_nop 0\ns_nop 0\n"
"s_nop 0\ns_nop 0\ns_nop 0\ns_nop 0\n"
"va1_p0:\ns_branch vb1_p0\nsx1_p0:\ns_add_u32 s60, s60, 308\n"
"s_addc_u32 s61, s61, 0\ns_setpc_b64 s[60:61]\nvb1_p0:\ns_branch vr1_p0\n"
"s_nop 1\ns_nop 1\ns_nop 1\ns_nop 1\n"
"s_nop 1\ns_nop 1\ns_nop 1\ns_nop 1\n"
"s_nop 1\ns_nop 1\ns_nop 1\ns_nop 1\n"
"s_nop 1\ns_nop 1\nvr1_p0:\ns_nop 1\n"
"s_nop 1\ns_nop 1\ns_nop 1\ns_nop 1\n"
"s_nop 1\nv_smfmac_f32_16x16x64_bf16 v[68:71], v[48:51], a[16:23], v8\ns_cbranch_execz vc1x0_p0\nbuffer_load_dwordx4 v[116:119], v20, s[8:11], s41 offen offset:0 sc1\n"
"buffer_load_dwordx4 v[120:123], v20, s[8:11], s41 offen offset:1024 sc1\nv_smfmac_f32_16x16x64_bf16 v[72:75], v[48:51], a[72:79], v8\nv_smfmac_f32_16x16x64_bf16 v[76:79], v[48:51], a[128:135], v8\nv_smfmac_f32_16x16x64_bf16 v[80:83], v[48:51], a[184:191], v8\n"
"v_smfmac_f32_16x16x64_bf16 v[68:71], v[52:55], a[24:31], v8\nv_smfmac_f32_16x16x64_bf16 v[72:75], v[52:55], a[80:87], v8\nvc1x0_p0:\ns_cbranch_execz vc1x1_p0\n"
"v_smfmac_f32_16x16x64_bf16 v[76:79], v[52:55], a[136:143], v8\nv_smfmac_f32_16x16x64_bf16 v[80:83], v[52:55], a[192:199], v8\nv_smfmac_f32_16x16x64_bf16 v[68:71], v[56:59], a[32:39], v8\nv_smfmac_f32_16x16x64_bf16 v[72:75], v[56:59], a[88:95], v8\n"
"v_smfmac_f32_16x16x64_bf16 v[76:79], v[56:59], a[144:151], v8\nvc1x1_p0:\ns_cbranch_execz vd1_p0\nbuffer_load_dwordx4 v[124:127], v21, s[8:11], s41 offen offset:0 sc1\n"
"buffer_load_dwordx4 v[128:131], v21, s[8:11], s41 offen offset:1024 sc1\nvd1_p0:\ns_cbranch_execnz tl11_p0\ns_nop 0\n"
"s_nop 0\ns_nop 0\ns_nop 0\ns_nop 0\n"
"s_nop 0\ns_nop 0\ns_nop 0\ns_nop 0\n"
"s_nop 0\ns_nop 0\ns_nop 0\ns_nop 0\n"
"s_nop 0\ns_nop 0\ns_nop 0\ns_nop 0\n"
"va2_p0:\ns_branch vb2_p0\nsx2_p0:\ns_add_u32 s60, s60, 616\n"
"s_addc_u32 s61, s61, 0\ns_setpc_b64 s[60:61]\nvb2_p0:\ns_branch vr2_p0\n"
"s_nop 1\ns_nop 1\ns_nop 1\ns_nop 1\n"
"s_nop 1\ns_nop 1\ns_nop 1\ns_nop 1\n"
"s_nop 1\ns_nop 1\ns_nop 1\ns_nop 1\n"
"s_nop 1\ns_nop 1\nvr2_p0:\ns_nop 1\n"
"s_nop 1\ns_nop 1\ns_nop 1\nv_smfmac_f32_16x16x64_bf16 v[68:71], v[48:51], a[16:23], v8\n"
"v_smfmac_f32_16x16x64_bf16 v[72:75], v[48:51], a[72:79], v8\ns_cbranch_execz vc2x0_p0\nbuffer_load_dwordx4 v[116:119], v20, s[8:11], s41 offen offset:0 sc1\nbuffer_load_dwordx4 v[120:123], v20, s[8:11], s41 offen offset:1024 sc1\n"
"v_smfmac_f32_16x16x64_bf16 v[76:79], v[48:51], a[128:135], v8\nv_smfmac_f32_16x16x64_bf16 v[80:83], v[48:51], a[184:191], v8\nv_smfmac_f32_16x16x64_bf16 v[68:71], v[52:55], a[24:31], v8\nv_smfmac_f32_16x16x64_bf16 v[72:75], v[52:55], a[80:87], v8\n"
"v_smfmac_f32_16x16x64_bf16 v[76:79], v[52:55], a[136:143], v8\nvc2x0_p0:\ns_cbranch_execz vc2x1_p0\nv_smfmac_f32_16x16x64_bf16 v[80:83], v[52:55], a[192:199], v8\n"
"v_smfmac_f32_16x16x64_bf16 v[68:71], v[56:59], a[32:39], v8\nv_smfmac_f32_16x16x64_bf16 v[72:75], v[56:59], a[88:95], v8\nv_smfmac_f32_16x16x64_bf16 v[76:79], v[56:59], a[144:151], v8\nv_smfmac_f32_16x16x64_bf16 v[80:83], v[56:59], a[200:207], v8\n"
"vc2x1_p0:\ns_cbranch_execz vd2_p0\nbuffer_load_dwordx4 v[124:127], v21, s[8:11], s41 offen offset:0 sc1\nbuffer_load_dwordx4 v[128:131], v21, s[8:11], s41 offen offset:1024 sc1\n"
"vd2_p0:\ns_cbranch_execnz tl12_p0\ns_nop 0\ns_nop 0\n"
"s_nop 0\ns_nop 0\ns_nop 0\ns_nop 0\n"
"s_nop 0\ns_nop 0\ns_nop 0\ns_nop 0\n"
"s_nop 0\ns_nop 0\ns_nop 0\ns_nop 0\n"
"s_nop 0\ns_nop 0\ns_nop 0\nva3_p0:\n"
"s_branch vb3_p0\nsx3_p0:\ns_add_u32 s60, s60, 924\ns_addc_u32 s61, s61, 0\n"
"s_setpc_b64 s[60:61]\nvb3_p0:\ns_branch vr3_p0\ns_nop 1\n"
"s_nop 1\ns_nop 1\ns_nop 1\ns_nop 1\n"
"s_nop 1\ns_nop 1\ns_nop 1\ns_nop 1\n"
"s_nop 1\ns_nop 1\ns_nop 1\ns_nop 1\n"
"s_nop 1\nvr3_p0:\ns_nop 1\ns_nop 1\n"
"v_smfmac_f32_16x16x64_bf16 v[68:71], v[48:51], a[16:23], v8\nv_smfmac_f32_16x16x64_bf16 v[72:75], v[48:51], a[72:79], v8\nv_smfmac_f32_16x16x64_bf16 v[76:79], v[48:51], a[128:135], v8\ns_cbranch_execz vc3x0_p0\n"
"buffer_load_dwordx4 v[116:119], v20, s[8:11], s41 offen offset:0 sc1\nbuffer_load_dwordx4 v[120:123], v20, s[8:11], s41 offen offset:1024 sc1\nv_smfmac_f32_16x16x64_bf16 v[80:83], v[48:51], a[184:191], v8\nv_smfmac_f32_16x16x64_bf16 v[68:71], v[52:55], a[24:31], v8\n"
"v_smfmac_f32_16x16x64_bf16 v[72:75], v[52:55], a[80:87], v8\nv_smfmac_f32_16x16x64_bf16 v[76:79], v[52:55], a[136:143], v8\nv_smfmac_f32_16x16x64_bf16 v[80:83], v[52:55], a[192:199], v8\nvc3x0_p0:\n"
"s_cbranch_execz vc3x1_p0\nv_smfmac_f32_16x16x64_bf16 v[68:71], v[56:59], a[32:39], v8\nv_smfmac_f32_16x16x64_bf16 v[72:75], v[56:59], a[88:95], v8\nv_smfmac_f32_16x16x64_bf16 v[76:79], v[56:59], a[144:151], v8\n"
"v_smfmac_f32_16x16x64_bf16 v[80:83], v[56:59], a[200:207], v8\nv_smfmac_f32_16x16x64_bf16 v[68:71], v[60:63], a[40:47], v8\nvc3x1_p0:\ns_cbranch_execz vd3_p0\n"
"buffer_load_dwordx4 v[124:127], v21, s[8:11], s41 offen offset:0 sc1\nbuffer_load_dwordx4 v[128:131], v21, s[8:11], s41 offen offset:1024 sc1\nvd3_p0:\ns_cbranch_execnz tl13_p0\n"
"s_nop 0\ns_nop 0\ns_nop 0\ns_nop 0\n"
"s_nop 0\ns_nop 0\ns_nop 0\ns_nop 0\n"
"s_nop 0\ns_nop 0\ns_nop 0\ns_nop 0\n"
"s_nop 0\ns_nop 0\ns_nop 0\ns_nop 0\n"
"s_nop 0\nva4_p0:\ns_branch vb4_p0\nsx4_p0:\n"
"s_add_u32 s60, s60, 1232\ns_addc_u32 s61, s61, 0\ns_setpc_b64 s[60:61]\nvb4_p0:\n"
"s_branch vr4_p0\ns_nop 1\ns_nop 1\ns_nop 1\n"
"s_nop 1\ns_nop 1\ns_nop 1\ns_nop 1\n"
"s_nop 1\ns_nop 1\ns_nop 1\ns_nop 1\n"
"s_nop 1\ns_nop 1\ns_nop 1\nvr4_p0:\n"
"v_smfmac_f32_16x16x64_bf16 v[68:71], v[48:51], a[16:23], v8\nv_smfmac_f32_16x16x64_bf16 v[72:75], v[48:51], a[72:79], v8\nv_smfmac_f32_16x16x64_bf16 v[76:79], v[48:51], a[128:135], v8\nv_smfmac_f32_16x16x64_bf16 v[80:83], v[48:51], a[184:191], v8\n"
"s_cbranch_execz vc4x0_p0\nbuffer_load_dwordx4 v[116:119], v20, s[8:11], s41 offen offset:0 sc1\nbuffer_load_dwordx4 v[120:123], v20, s[8:11], s41 offen offset:1024 sc1\nv_smfmac_f32_16x16x64_bf16 v[68:71], v[52:55], a[24:31], v8\n"
"v_smfmac_f32_16x16x64_bf16 v[72:75], v[52:55], a[80:87], v8\nv_smfmac_f32_16x16x64_bf16 v[76:79], v[52:55], a[136:143], v8\nv_smfmac_f32_16x16x64_bf16 v[80:83], v[52:55], a[192:199], v8\nv_smfmac_f32_16x16x64_bf16 v[68:71], v[56:59], a[32:39], v8\n"
"vc4x0_p0:\ns_cbranch_execz vc4x1_p0\nv_smfmac_f32_16x16x64_bf16 v[72:75], v[56:59], a[88:95], v8\nv_smfmac_f32_16x16x64_bf16 v[76:79], v[56:59], a[144:151], v8\n"
"v_smfmac_f32_16x16x64_bf16 v[80:83], v[56:59], a[200:207], v8\nv_smfmac_f32_16x16x64_bf16 v[68:71], v[60:63], a[40:47], v8\nv_smfmac_f32_16x16x64_bf16 v[72:75], v[60:63], a[96:103], v8\nvc4x1_p0:\n"
"s_cbranch_execz vd4_p0\nbuffer_load_dwordx4 v[124:127], v21, s[8:11], s41 offen offset:0 sc1\nbuffer_load_dwordx4 v[128:131], v21, s[8:11], s41 offen offset:1024 sc1\nvd4_p0:\n"
"s_cbranch_execnz tl14_p0\ns_nop 0\ns_nop 0\ns_nop 0\n"
"s_nop 0\ns_nop 0\ns_nop 0\ns_nop 0\n"
"s_nop 0\ns_nop 0\ns_nop 0\ns_nop 0\n"
"s_nop 0\ns_nop 0\ns_nop 0\ns_nop 0\n"
"s_nop 0\ns_nop 0\nva5_p0:\ns_branch vb5_p0\n"
"sx5_p0:\ns_add_u32 s60, s60, 1540\ns_addc_u32 s61, s61, 0\ns_setpc_b64 s[60:61]\n"
"vb5_p0:\ns_branch vr5_p0\ns_nop 1\ns_nop 1\n"
"s_nop 1\ns_nop 1\ns_nop 1\ns_nop 1\n"
"s_nop 1\ns_nop 1\ns_nop 1\ns_nop 1\n"
"s_nop 1\ns_nop 1\nv_smfmac_f32_16x16x64_bf16 v[68:71], v[48:51], a[16:23], v8\nvr5_p0:\n"
"v_smfmac_f32_16x16x64_bf16 v[72:75], v[48:51], a[72:79], v8\nv_smfmac_f32_16x16x64_bf16 v[76:79], v[48:51], a[128:135], v8\nv_smfmac_f32_16x16x64_bf16 v[80:83], v[48:51], a[184:191], v8\nv_smfmac_f32_16x16x64_bf16 v[68:71], v[52:55], a[24:31], v8\n"
"s_cbranch_execz vc5x0_p0\nbuffer_load_dwordx4 v[116:119], v20, s[8:11], s41 offen offset:0 sc1\nbuffer_load_dwordx4 v[120:123], v20, s[8:11], s41 offen offset:1024 sc1\nv_smfmac_f32_16x16x64_bf16 v[72:75], v[52:55], a[80:87], v8\n"
"v_smfmac_f32_16x16x64_bf16 v[76:79], v[52:55], a[136:143], v8\nv_smfmac_f32_16x16x64_bf16 v[80:83], v[52:55], a[192:199], v8\nv_smfmac_f32_16x16x64_bf16 v[68:71], v[56:59], a[32:39], v8\nv_smfmac_f32_16x16x64_bf16 v[72:75], v[56:59], a[88:95], v8\n"
"vc5x0_p0:\ns_cbranch_execz vc5x1_p0\nv_smfmac_f32_16x16x64_bf16 v[76:79], v[56:59], a[144:151], v8\nv_smfmac_f32_16x16x64_bf16 v[80:83], v[56:59], a[200:207], v8\n"
"v_smfmac_f32_16x16x64_bf16 v[68:71], v[60:63], a[40:47], v8\nv_smfmac_f32_16x16x64_bf16 v[72:75], v[60:63], a[96:103], v8\nv_smfmac_f32_16x16x64_bf16 v[76:79], v[60:63], a[152:159], v8\nvc5x1_p0:\n"
"s_cbranch_execz vd5_p0\nbuffer_load_dwordx4 v[124:127], v21, s[8:11], s41 offen offset:0 sc1\nbuffer_load_dwordx4 v[128:131], v21, s[8:11], s41 offen offset:1024 sc1\nvd5_p0:\n"
"s_cbranch_execnz tl15_p0\ns_nop 0\ns_nop 0\ns_nop 0\n"
"s_nop 0\ns_nop 0\ns_nop 0\ns_nop 0\n"
"s_nop 0\ns_nop 0\ns_nop 0\ns_nop 0\n"
"s_nop 0\ns_nop 0\ns_nop 0\ns_nop 0\n"
"s_nop 0\ns_nop 0\nva6_p0:\ns_branch warm_p0\n"
"sx6_p0:\ns_add_u32 s60, s60, 1848\ns_addc_u32 s61, s61, 0\ns_setpc_b64 s[60:61]\n"
"vb6_p0:\ns_branch vr6_p0\ns_nop 1\ns_nop 1\n"
"s_nop 1\ns_nop 1\ns_nop 1\ns_nop 1\n"
"s_nop 1\ns_nop 1\ns_nop 1\ns_nop 1\n"
"v_smfmac_f32_16x16x64_bf16 v[68:71], v[48:51], a[16:23], v8\nv_smfmac_f32_16x16x64_bf16 v[72:75], v[48:51], a[72:79], v8\nvr6_p0:\nv_smfmac_f32_16x16x64_bf16 v[76:79], v[48:51], a[128:135], v8\n"
"v_smfmac_f32_16x16x64_bf16 v[80:83], v[48:51], a[184:191], v8\nv_smfmac_f32_16x16x64_bf16 v[68:71], v[52:55], a[24:31], v8\nv_smfmac_f32_16x16x64_bf16 v[72:75], v[52:55], a[80:87], v8\ns_cbranch_execz vc6x0_p0\n"
"buffer_load_dwordx4 v[116:119], v20, s[8:11], s41 offen offset:0 sc1\nbuffer_load_dwordx4 v[120:123], v20, s[8:11], s41 offen offset:1024 sc1\nv_smfmac_f32_16x16x64_bf16 v[76:79], v[52:55], a[136:143], v8\nv_smfmac_f32_16x16x64_bf16 v[80:83], v[52:55], a[192:199], v8\n"
"v_smfmac_f32_16x16x64_bf16 v[68:71], v[56:59], a[32:39], v8\nv_smfmac_f32_16x16x64_bf16 v[72:75], v[56:59], a[88:95], v8\nv_smfmac_f32_16x16x64_bf16 v[76:79], v[56:59], a[144:151], v8\nvc6x0_p0:\n"
"s_cbranch_execz vc6x1_p0\nv_smfmac_f32_16x16x64_bf16 v[80:83], v[56:59], a[200:207], v8\nv_smfmac_f32_16x16x64_bf16 v[68:71], v[60:63], a[40:47], v8\nv_smfmac_f32_16x16x64_bf16 v[72:75], v[60:63], a[96:103], v8\n"
"v_smfmac_f32_16x16x64_bf16 v[76:79], v[60:63], a[152:159], v8\nv_smfmac_f32_16x16x64_bf16 v[80:83], v[60:63], a[208:215], v8\nvc6x1_p0:\ns_cbranch_execz vd6_p0\n"
"buffer_load_dwordx4 v[124:127], v21, s[8:11], s41 offen offset:0 sc1\nbuffer_load_dwordx4 v[128:131], v21, s[8:11], s41 offen offset:1024 sc1\ns_nop 1\nvd6_p0:\n"
"s_cbranch_execnz tl16_p0\ns_nop 0\ns_nop 0\ns_nop 0\n"
"s_nop 0\ns_nop 0\ns_nop 0\ns_nop 0\n"
"s_nop 0\ns_nop 0\ns_nop 0\ns_nop 0\n"
"s_nop 0\ns_nop 0\ns_nop 0\ns_nop 0\n"
"s_nop 0\nva7_p0:\ns_branch vb7_p0\nsx7_p0:\n"
"s_add_u32 s60, s60, 2156\ns_addc_u32 s61, s61, 0\ns_setpc_b64 s[60:61]\nvb7_p0:\n"
"s_branch vr7_p0\ns_nop 1\ns_nop 1\ns_nop 1\n"
"s_nop 1\ns_nop 1\ns_nop 1\ns_nop 1\n"
"s_nop 1\nv_smfmac_f32_16x16x64_bf16 v[68:71], v[48:51], a[16:23], v8\nv_smfmac_f32_16x16x64_bf16 v[72:75], v[48:51], a[72:79], v8\nv_smfmac_f32_16x16x64_bf16 v[76:79], v[48:51], a[128:135], v8\n"
"vr7_p0:\nv_smfmac_f32_16x16x64_bf16 v[80:83], v[48:51], a[184:191], v8\nv_smfmac_f32_16x16x64_bf16 v[68:71], v[52:55], a[24:31], v8\nv_smfmac_f32_16x16x64_bf16 v[72:75], v[52:55], a[80:87], v8\n"
"v_smfmac_f32_16x16x64_bf16 v[76:79], v[52:55], a[136:143], v8\ns_cbranch_execz vc7x0_p0\nbuffer_load_dwordx4 v[116:119], v20, s[8:11], s41 offen offset:0 sc1\nbuffer_load_dwordx4 v[120:123], v20, s[8:11], s41 offen offset:1024 sc1\n"
"v_smfmac_f32_16x16x64_bf16 v[80:83], v[52:55], a[192:199], v8\nv_smfmac_f32_16x16x64_bf16 v[68:71], v[56:59], a[32:39], v8\nv_smfmac_f32_16x16x64_bf16 v[72:75], v[56:59], a[88:95], v8\nv_smfmac_f32_16x16x64_bf16 v[76:79], v[56:59], a[144:151], v8\n"
"v_smfmac_f32_16x16x64_bf16 v[80:83], v[56:59], a[200:207], v8\nvc7x0_p0:\ns_cbranch_execz vc7x1_p0\nv_smfmac_f32_16x16x64_bf16 v[68:71], v[60:63], a[40:47], v8\n"
"v_smfmac_f32_16x16x64_bf16 v[72:75], v[60:63], a[96:103], v8\nv_smfmac_f32_16x16x64_bf16 v[76:79], v[60:63], a[152:159], v8\nv_smfmac_f32_16x16x64_bf16 v[80:83], v[60:63], a[208:215], v8\nv_smfmac_f32_16x16x64_bf16 v[68:71], v[64:67], a[48:55], v8\n"
"vc7x1_p0:\ns_cbranch_execz vd7_p0\nbuffer_load_dwordx4 v[124:127], v21, s[8:11], s41 offen offset:0 sc1\nbuffer_load_dwordx4 v[128:131], v21, s[8:11], s41 offen offset:1024 sc1\n"
"s_nop 5\nvd7_p0:\ns_cbranch_execnz tl17_p0\ns_nop 0\n"
"s_nop 0\ns_nop 0\ns_nop 0\ns_nop 0\n"
"s_nop 0\ns_nop 0\ns_nop 0\ns_nop 0\n"
"s_nop 0\ns_nop 0\ns_nop 0\ns_nop 0\n"
"s_nop 0\ns_nop 0\ns_nop 0\nva8_p0:\n"
"s_branch vb8_p0\nsx8_p0:\ns_add_u32 s60, s60, 2464\ns_addc_u32 s61, s61, 0\n"
"s_setpc_b64 s[60:61]\nvb8_p0:\ns_branch vr8_p0\ns_nop 1\n"
"s_nop 1\ns_nop 1\ns_nop 1\ns_nop 1\n"
"s_nop 1\nv_smfmac_f32_16x16x64_bf16 v[68:71], v[48:51], a[16:23], v8\nv_smfmac_f32_16x16x64_bf16 v[72:75], v[48:51], a[72:79], v8\nv_smfmac_f32_16x16x64_bf16 v[76:79], v[48:51], a[128:135], v8\n"
"v_smfmac_f32_16x16x64_bf16 v[80:83], v[48:51], a[184:191], v8\nvr8_p0:\nv_smfmac_f32_16x16x64_bf16 v[68:71], v[52:55], a[24:31], v8\nv_smfmac_f32_16x16x64_bf16 v[72:75], v[52:55], a[80:87], v8\n"
"v_smfmac_f32_16x16x64_bf16 v[76:79], v[52:55], a[136:143], v8\nv_smfmac_f32_16x16x64_bf16 v[80:83], v[52:55], a[192:199], v8\ns_cbranch_execz vc8x0_p0\nbuffer_load_dwordx4 v[116:119], v20, s[8:11], s41 offen offset:0 sc1\n"
"buffer_load_dwordx4 v[120:123], v20, s[8:11], s41 offen offset:1024 sc1\nv_smfmac_f32_16x16x64_bf16 v[68:71], v[56:59], a[32:39], v8\nv_smfmac_f32_16x16x64_bf16 v[72:75], v[56:59], a[88:95], v8\nv_smfmac_f32_16x16x64_bf16 v[76:79], v[56:59], a[144:151], v8\n"
"v_smfmac_f32_16x16x64_bf16 v[80:83], v[56:59], a[200:207], v8\nv_smfmac_f32_16x16x64_bf16 v[68:71], v[60:63], a[40:47], v8\nvc8x0_p0:\ns_cbranch_execz vc8x1_p0\n"
"v_smfmac_f32_16x16x64_bf16 v[72:75], v[60:63], a[96:103], v8\nv_smfmac_f32_16x16x64_bf16 v[76:79], v[60:63], a[152:159], v8\nv_smfmac_f32_16x16x64_bf16 v[80:83], v[60:63], a[208:215], v8\nv_smfmac_f32_16x16x64_bf16 v[68:71], v[64:67], a[48:55], v8\n"
"v_smfmac_f32_16x16x64_bf16 v[72:75], v[64:67], a[104:111], v8\nvc8x1_p0:\ns_cbranch_execz vd8_p0\nbuffer_load_dwordx4 v[124:127], v21, s[8:11], s41 offen offset:0 sc1\n"
"buffer_load_dwordx4 v[128:131], v21, s[8:11], s41 offen offset:1024 sc1\ns_nop 9\nvd8_p0:\ns_cbranch_execnz tl18_p0\n"
"s_nop 0\ns_nop 0\ns_nop 0\ns_nop 0\n"
"s_nop 0\ns_nop 0\ns_nop 0\ns_nop 0\n"
"s_nop 0\ns_nop 0\ns_nop 0\ns_nop 0\n"
"s_nop 0\ns_nop 0\ns_nop 0\ns_nop 0\n"
"va9_p0:\ns_branch vb9_p0\nsx9_p0:\ns_add_u32 s60, s60, 2772\n"
"s_addc_u32 s61, s61, 0\ns_setpc_b64 s[60:61]\nvb9_p0:\ns_branch vr9_p0\n"
"s_nop 1\ns_nop 1\ns_nop 1\ns_nop 1\n"
"v_smfmac_f32_16x16x64_bf16 v[68:71], v[48:51], a[16:23], v8\nv_smfmac_f32_16x16x64_bf16 v[72:75], v[48:51], a[72:79], v8\nv_smfmac_f32_16x16x64_bf16 v[76:79], v[48:51], a[128:135], v8\nv_smfmac_f32_16x16x64_bf16 v[80:83], v[48:51], a[184:191], v8\n"
"v_smfmac_f32_16x16x64_bf16 v[68:71], v[52:55], a[24:31], v8\nvr9_p0:\nv_smfmac_f32_16x16x64_bf16 v[72:75], v[52:55], a[80:87], v8\nv_smfmac_f32_16x16x64_bf16 v[76:79], v[52:55], a[136:143], v8\n"
"v_smfmac_f32_16x16x64_bf16 v[80:83], v[52:55], a[192:199], v8\nv_smfmac_f32_16x16x64_bf16 v[68:71], v[56:59], a[32:39], v8\ns_cbranch_execz vc9x0_p0\nbuffer_load_dwordx4 v[116:119], v20, s[8:11], s41 offen offset:0 sc1\n"
"buffer_load_dwordx4 v[120:123], v20, s[8:11], s41 offen offset:1024 sc1\nv_smfmac_f32_16x16x64_bf16 v[72:75], v[56:59], a[88:95], v8\nv_smfmac_f32_16x16x64_bf16 v[76:79], v[56:59], a[144:151], v8\nv_smfmac_f32_16x16x64_bf16 v[80:83], v[56:59], a[200:207], v8\n"
"v_smfmac_f32_16x16x64_bf16 v[68:71], v[60:63], a[40:47], v8\nv_smfmac_f32_16x16x64_bf16 v[72:75], v[60:63], a[96:103], v8\nvc9x0_p0:\ns_cbranch_execz vc9x1_p0\n"
"v_smfmac_f32_16x16x64_bf16 v[76:79], v[60:63], a[152:159], v8\nv_smfmac_f32_16x16x64_bf16 v[80:83], v[60:63], a[208:215], v8\nv_smfmac_f32_16x16x64_bf16 v[68:71], v[64:67], a[48:55], v8\nv_smfmac_f32_16x16x64_bf16 v[72:75], v[64:67], a[104:111], v8\n"
"v_smfmac_f32_16x16x64_bf16 v[76:79], v[64:67], a[160:167], v8\nvc9x1_p0:\ns_cbranch_execz vd9_p0\nbuffer_load_dwordx4 v[124:127], v21, s[8:11], s41 offen offset:0 sc1\n"
"buffer_load_dwordx4 v[128:131], v21, s[8:11], s41 offen offset:1024 sc1\ns_nop 13\nvd9_p0:\ns_cbranch_execnz tl19_p0\n"
"s_nop 0\ns_nop 0\ns_nop 0\ns_nop 0\n"
"s_nop 0\ns_nop 0\ns_nop 0\ns_nop 0\n"
"s_nop 0\ns_nop 0\ns_nop 0\ns_nop 0\n"
"s_nop 0\ns_nop 0\ns_nop 0\ns_nop 0\n"
"va10_p0:\ns_branch vb10_p0\nsx10_p0:\ns_add_u32 s60, s60, 3080\n"
"s_addc_u32 s61, s61, 0\ns_setpc_b64 s[60:61]\nvb10_p0:\ns_branch vr10_p0\n"
"s_nop 1\ns_nop 1\nv_smfmac_f32_16x16x64_bf16 v[68:71], v[48:51], a[16:23], v8\nv_smfmac_f32_16x16x64_bf16 v[72:75], v[48:51], a[72:79], v8\n"
"v_smfmac_f32_16x16x64_bf16 v[76:79], v[48:51], a[128:135], v8\nv_smfmac_f32_16x16x64_bf16 v[80:83], v[48:51], a[184:191], v8\nv_smfmac_f32_16x16x64_bf16 v[68:71], v[52:55], a[24:31], v8\nv_smfmac_f32_16x16x64_bf16 v[72:75], v[52:55], a[80:87], v8\n"
"vr10_p0:\nv_smfmac_f32_16x16x64_bf16 v[76:79], v[52:55], a[136:143], v8\nv_smfmac_f32_16x16x64_bf16 v[80:83], v[52:55], a[192:199], v8\nv_smfmac_f32_16x16x64_bf16 v[68:71], v[56:59], a[32:39], v8\n"
"v_smfmac_f32_16x16x64_bf16 v[72:75], v[56:59], a[88:95], v8\ns_cbranch_execz vc10x0_p0\nbuffer_load_dwordx4 v[116:119], v20, s[8:11], s41 offen offset:0 sc1\nbuffer_load_dwordx4 v[120:123], v20, s[8:11], s41 offen offset:1024 sc1\n"
"v_smfmac_f32_16x16x64_bf16 v[76:79], v[56:59], a[144:151], v8\nv_smfmac_f32_16x16x64_bf16 v[80:83], v[56:59], a[200:207], v8\nv_smfmac_f32_16x16x64_bf16 v[68:71], v[60:63], a[40:47], v8\nv_smfmac_f32_16x16x64_bf16 v[72:75], v[60:63], a[96:103], v8\n"
"v_smfmac_f32_16x16x64_bf16 v[76:79], v[60:63], a[152:159], v8\nvc10x0_p0:\ns_cbranch_execz vc10x1_p0\nv_smfmac_f32_16x16x64_bf16 v[80:83], v[60:63], a[208:215], v8\n"
"v_smfmac_f32_16x16x64_bf16 v[68:71], v[64:67], a[48:55], v8\nv_smfmac_f32_16x16x64_bf16 v[72:75], v[64:67], a[104:111], v8\nv_smfmac_f32_16x16x64_bf16 v[76:79], v[64:67], a[160:167], v8\nv_smfmac_f32_16x16x64_bf16 v[80:83], v[64:67], a[216:223], v8\n"
"vc10x1_p0:\ns_cbranch_execz vd10_p0\nbuffer_load_dwordx4 v[124:127], v21, s[8:11], s41 offen offset:0 sc1\nbuffer_load_dwordx4 v[128:131], v21, s[8:11], s41 offen offset:1024 sc1\n"
"s_nop 15\ns_nop 1\nvd10_p0:\ns_cbranch_execnz tl20_p0\n"
"s_nop 0\ns_nop 0\ns_nop 0\ns_nop 0\n"
"s_nop 0\ns_nop 0\ns_nop 0\ns_nop 0\n"
"s_nop 0\ns_nop 0\ns_nop 0\ns_nop 0\n"
"s_nop 0\ns_nop 0\ns_nop 0\nva11_p0:\n"
"s_branch vb11_p0\nsx11_p0:\ns_add_u32 s60, s60, 3388\ns_addc_u32 s61, s61, 0\n"
"s_setpc_b64 s[60:61]\nvb11_p0:\ns_branch vr11_p0\nv_smfmac_f32_16x16x64_bf16 v[68:71], v[48:51], a[16:23], v8\n"
"v_smfmac_f32_16x16x64_bf16 v[72:75], v[48:51], a[72:79], v8\nv_smfmac_f32_16x16x64_bf16 v[76:79], v[48:51], a[128:135], v8\nv_smfmac_f32_16x16x64_bf16 v[80:83], v[48:51], a[184:191], v8\nv_smfmac_f32_16x16x64_bf16 v[68:71], v[52:55], a[24:31], v8\n"
"v_smfmac_f32_16x16x64_bf16 v[72:75], v[52:55], a[80:87], v8\nv_smfmac_f32_16x16x64_bf16 v[76:79], v[52:55], a[136:143], v8\nvr11_p0:\nv_smfmac_f32_16x16x64_bf16 v[80:83], v[52:55], a[192:199], v8\n"
"v_smfmac_f32_16x16x64_bf16 v[68:71], v[56:59], a[32:39], v8\nv_smfmac_f32_16x16x64_bf16 v[72:75], v[56:59], a[88:95], v8\nv_smfmac_f32_16x16x64_bf16 v[76:79], v[56:59], a[144:151], v8\ns_cbranch_execz vc11x0_p0\n"
"buffer_load_dwordx4 v[116:119], v20, s[8:11], s41 offen offset:0 sc1\nbuffer_load_dwordx4 v[120:123], v20, s[8:11], s41 offen offset:1024 sc1\nv_smfmac_f32_16x16x64_bf16 v[80:83], v[56:59], a[200:207], v8\nv_smfmac_f32_16x16x64_bf16 v[68:71], v[60:63], a[40:47], v8\n"
"v_smfmac_f32_16x16x64_bf16 v[72:75], v[60:63], a[96:103], v8\nv_smfmac_f32_16x16x64_bf16 v[76:79], v[60:63], a[152:159], v8\nv_smfmac_f32_16x16x64_bf16 v[80:83], v[60:63], a[208:215], v8\nvc11x0_p0:\n"
"s_cbranch_execz vc11x1_p0\nv_smfmac_f32_16x16x64_bf16 v[68:71], v[64:67], a[48:55], v8\nv_smfmac_f32_16x16x64_bf16 v[72:75], v[64:67], a[104:111], v8\nv_smfmac_f32_16x16x64_bf16 v[76:79], v[64:67], a[160:167], v8\n"
"v_smfmac_f32_16x16x64_bf16 v[80:83], v[64:67], a[216:223], v8\nv_add_f32 v219, v68, v69\nv_add_f32 v220, v70, v71\nv_cvt_pk_bf16_f32 v156, v219, v220\n"
"s_nop 0\nvc11x1_p0:\ns_cbranch_execz vd11_p0\nbuffer_load_dwordx4 v[124:127], v21, s[8:11], s41 offen offset:0 sc1\n"
"buffer_load_dwordx4 v[128:131], v21, s[8:11], s41 offen offset:1024 sc1\ns_nop 15\ns_nop 4\nvd11_p0:\n"
"s_cbranch_execnz tl21_p0\ns_nop 0\ns_nop 0\ns_nop 0\n"
"s_nop 0\ns_nop 0\ns_nop 0\ns_nop 0\n"
"s_nop 0\ns_nop 0\ns_nop 0\ns_nop 0\n"
"s_nop 0\nva12_p0:\ns_branch warm_p0\nsx12_p0:\n"
"s_add_u32 s60, s60, 3696\ns_addc_u32 s61, s61, 0\ns_setpc_b64 s[60:61]\nvb12_p0:\n"
"s_branch vr12_p0\nv_smfmac_f32_16x16x64_bf16 v[72:75], v[48:51], a[72:79], v8\nv_smfmac_f32_16x16x64_bf16 v[76:79], v[48:51], a[128:135], v8\nv_smfmac_f32_16x16x64_bf16 v[80:83], v[48:51], a[184:191], v8\n"
"v_smfmac_f32_16x16x64_bf16 v[68:71], v[52:55], a[24:31], v8\nv_smfmac_f32_16x16x64_bf16 v[72:75], v[52:55], a[80:87], v8\nv_smfmac_f32_16x16x64_bf16 v[76:79], v[52:55], a[136:143], v8\nv_smfmac_f32_16x16x64_bf16 v[80:83], v[52:55], a[192:199], v8\n"
"vr12_p0:\nv_smfmac_f32_16x16x64_bf16 v[68:71], v[56:59], a[32:39], v8\nv_smfmac_f32_16x16x64_bf16 v[72:75], v[56:59], a[88:95], v8\nv_smfmac_f32_16x16x64_bf16 v[76:79], v[56:59], a[144:151], v8\n"
"v_smfmac_f32_16x16x64_bf16 v[80:83], v[56:59], a[200:207], v8\ns_cbranch_execz vc12x0_p0\nbuffer_load_dwordx4 v[116:119], v20, s[8:11], s41 offen offset:0 sc1\nbuffer_load_dwordx4 v[120:123], v20, s[8:11], s41 offen offset:1024 sc1\n"
"v_smfmac_f32_16x16x64_bf16 v[68:71], v[60:63], a[40:47], v8\nv_smfmac_f32_16x16x64_bf16 v[72:75], v[60:63], a[96:103], v8\nv_smfmac_f32_16x16x64_bf16 v[76:79], v[60:63], a[152:159], v8\nv_smfmac_f32_16x16x64_bf16 v[80:83], v[60:63], a[208:215], v8\n"
"v_smfmac_f32_16x16x64_bf16 v[68:71], v[64:67], a[48:55], v8\nvc12x0_p0:\ns_cbranch_execz vc12x1_p0\nv_smfmac_f32_16x16x64_bf16 v[72:75], v[64:67], a[104:111], v8\n"
"v_smfmac_f32_16x16x64_bf16 v[76:79], v[64:67], a[160:167], v8\nv_smfmac_f32_16x16x64_bf16 v[80:83], v[64:67], a[216:223], v8\nv_add_f32 v219, v68, v69\nv_add_f32 v220, v70, v71\n"
"v_cvt_pk_bf16_f32 v156, v219, v220\ns_nop 0\nvc12x1_p0:\ns_cbranch_execz vc12x2_p0\n"
"v_add_f32 v219, v72, v73\nv_add_f32 v220, v74, v75\nv_cvt_pk_bf16_f32 v157, v219, v220\ns_nop 0\n"
"vc12x2_p0:\ns_cbranch_execz vd12_p0\nbuffer_load_dwordx4 v[124:127], v21, s[8:11], s41 offen offset:0 sc1\nbuffer_load_dwordx4 v[128:131], v21, s[8:11], s41 offen offset:1024 sc1\n"
"s_nop 15\ns_nop 7\nvd12_p0:\ns_cbranch_execnz tl22_p0\n"
"s_nop 0\ns_nop 0\ns_nop 0\ns_nop 0\n"
"s_nop 0\ns_nop 0\ns_nop 0\ns_nop 0\n"
"va13_p0:\ns_branch vb13_p0\nsx13_p0:\ns_add_u32 s60, s60, 4004\n"
"s_addc_u32 s61, s61, 0\ns_setpc_b64 s[60:61]\nvb13_p0:\ns_branch vr13_p0\n"
"v_smfmac_f32_16x16x64_bf16 v[76:79], v[48:51], a[128:135], v8\nv_smfmac_f32_16x16x64_bf16 v[80:83], v[48:51], a[184:191], v8\nv_smfmac_f32_16x16x64_bf16 v[68:71], v[52:55], a[24:31], v8\nv_smfmac_f32_16x16x64_bf16 v[72:75], v[52:55], a[80:87], v8\n"
"v_smfmac_f32_16x16x64_bf16 v[76:79], v[52:55], a[136:143], v8\nv_smfmac_f32_16x16x64_bf16 v[80:83], v[52:55], a[192:199], v8\nv_smfmac_f32_16x16x64_bf16 v[68:71], v[56:59], a[32:39], v8\nvr13_p0:\n"
"v_smfmac_f32_16x16x64_bf16 v[72:75], v[56:59], a[88:95], v8\nv_smfmac_f32_16x16x64_bf16 v[76:79], v[56:59], a[144:151], v8\nv_smfmac_f32_16x16x64_bf16 v[80:83], v[56:59], a[200:207], v8\nv_smfmac_f32_16x16x64_bf16 v[68:71], v[60:63], a[40:47], v8\n"
"s_cbranch_execz vc13x0_p0\nbuffer_load_dwordx4 v[116:119], v20, s[8:11], s41 offen offset:0 sc1\nbuffer_load_dwordx4 v[120:123], v20, s[8:11], s41 offen offset:1024 sc1\nv_smfmac_f32_16x16x64_bf16 v[72:75], v[60:63], a[96:103], v8\n"
"v_smfmac_f32_16x16x64_bf16 v[76:79], v[60:63], a[152:159], v8\nv_smfmac_f32_16x16x64_bf16 v[80:83], v[60:63], a[208:215], v8\nv_smfmac_f32_16x16x64_bf16 v[68:71], v[64:67], a[48:55], v8\nv_smfmac_f32_16x16x64_bf16 v[72:75], v[64:67], a[104:111], v8\n"
"vc13x0_p0:\ns_cbranch_execz vc13x1_p0\nv_smfmac_f32_16x16x64_bf16 v[76:79], v[64:67], a[160:167], v8\nv_smfmac_f32_16x16x64_bf16 v[80:83], v[64:67], a[216:223], v8\n"
"v_add_f32 v219, v68, v69\nv_add_f32 v220, v70, v71\nv_cvt_pk_bf16_f32 v156, v219, v220\ns_nop 0\n"
"v_add_f32 v219, v72, v73\nv_add_f32 v220, v74, v75\nv_cvt_pk_bf16_f32 v157, v219, v220\ns_nop 0\n"
"vc13x1_p0:\ns_cbranch_execz vc13x2_p0\nv_add_f32 v219, v76, v77\nv_add_f32 v220, v78, v79\n"
"v_cvt_pk_bf16_f32 v158, v219, v220\ns_nop 0\nvc13x2_p0:\ns_cbranch_execz vd13_p0\n"
"buffer_load_dwordx4 v[124:127], v21, s[8:11], s41 offen offset:0 sc1\nbuffer_load_dwordx4 v[128:131], v21, s[8:11], s41 offen offset:1024 sc1\ns_nop 15\ns_nop 10\n"
"vd13_p0:\ns_cbranch_execnz tl23_p0\ns_nop 0\ns_nop 0\n"
"s_nop 0\ns_nop 0\ns_nop 0\nva14_p0:\n"
"s_branch vb14_p0\nsx14_p0:\ns_add_u32 s60, s60, 4312\ns_addc_u32 s61, s61, 0\n"
"s_setpc_b64 s[60:61]\nvb14_p0:\ns_branch vr14_p0\nv_smfmac_f32_16x16x64_bf16 v[80:83], v[48:51], a[184:191], v8\n"
"v_smfmac_f32_16x16x64_bf16 v[68:71], v[52:55], a[24:31], v8\nv_smfmac_f32_16x16x64_bf16 v[72:75], v[52:55], a[80:87], v8\nv_smfmac_f32_16x16x64_bf16 v[76:79], v[52:55], a[136:143], v8\nv_smfmac_f32_16x16x64_bf16 v[80:83], v[52:55], a[192:199], v8\n"
"v_smfmac_f32_16x16x64_bf16 v[68:71], v[56:59], a[32:39], v8\nv_smfmac_f32_16x16x64_bf16 v[72:75], v[56:59], a[88:95], v8\nvr14_p0:\nv_smfmac_f32_16x16x64_bf16 v[76:79], v[56:59], a[144:151], v8\n"
"v_smfmac_f32_16x16x64_bf16 v[80:83], v[56:59], a[200:207], v8\nv_smfmac_f32_16x16x64_bf16 v[68:71], v[60:63], a[40:47], v8\nv_smfmac_f32_16x16x64_bf16 v[72:75], v[60:63], a[96:103], v8\ns_cbranch_execz vc14x0_p0\n"
"buffer_load_dwordx4 v[116:119], v20, s[8:11], s41 offen offset:0 sc1\nbuffer_load_dwordx4 v[120:123], v20, s[8:11], s41 offen offset:1024 sc1\nv_smfmac_f32_16x16x64_bf16 v[76:79], v[60:63], a[152:159], v8\nv_smfmac_f32_16x16x64_bf16 v[80:83], v[60:63], a[208:215], v8\n"
"v_smfmac_f32_16x16x64_bf16 v[68:71], v[64:67], a[48:55], v8\nv_smfmac_f32_16x16x64_bf16 v[72:75], v[64:67], a[104:111], v8\nv_smfmac_f32_16x16x64_bf16 v[76:79], v[64:67], a[160:167], v8\nvc14x0_p0:\n"
"s_cbranch_execz vc14x1_p0\nv_smfmac_f32_16x16x64_bf16 v[80:83], v[64:67], a[216:223], v8\nv_add_f32 v219, v68, v69\nv_add_f32 v220, v70, v71\n"
"v_cvt_pk_bf16_f32 v156, v219, v220\ns_nop 0\nv_add_f32 v219, v72, v73\nv_add_f32 v220, v74, v75\n"
"v_cvt_pk_bf16_f32 v157, v219, v220\ns_nop 0\nvc14x1_p0:\ns_cbranch_execz vc14x2_p0\n"
"v_add_f32 v219, v76, v77\nv_add_f32 v220, v78, v79\nv_cvt_pk_bf16_f32 v158, v219, v220\ns_nop 0\n"
"v_add_f32 v219, v80, v81\nv_add_f32 v220, v82, v83\nv_cvt_pk_bf16_f32 v159, v219, v220\ns_nop 0\n"
"vc14x2_p0:\ns_cbranch_execz vd14_p0\nbuffer_load_dwordx4 v[124:127], v21, s[8:11], s41 offen offset:0 sc1\nbuffer_load_dwordx4 v[128:131], v21, s[8:11], s41 offen offset:1024 sc1\n"
"s_nop 15\ns_nop 13\nvd14_p0:\ns_cbranch_execnz tl24_p0\n"
"s_nop 0\ns_nop 0\nva15_p0:\ns_branch vb15_p0\n"
"sx15_p0:\ns_add_u32 s60, s60, 4620\ns_addc_u32 s61, s61, 0\ns_setpc_b64 s[60:61]\n"
"vb15_p0:\ns_branch vr15_p0\nv_smfmac_f32_16x16x64_bf16 v[68:71], v[52:55], a[24:31], v8\nv_smfmac_f32_16x16x64_bf16 v[72:75], v[52:55], a[80:87], v8\n"
"v_smfmac_f32_16x16x64_bf16 v[76:79], v[52:55], a[136:143], v8\nv_smfmac_f32_16x16x64_bf16 v[80:83], v[52:55], a[192:199], v8\nv_smfmac_f32_16x16x64_bf16 v[68:71], v[56:59], a[32:39], v8\nv_smfmac_f32_16x16x64_bf16 v[72:75], v[56:59], a[88:95], v8\n"
"v_smfmac_f32_16x16x64_bf16 v[76:79], v[56:59], a[144:151], v8\nvr15_p0:\nv_smfmac_f32_16x16x64_bf16 v[80:83], v[56:59], a[200:207], v8\nv_smfmac_f32_16x16x64_bf16 v[68:71], v[60:63], a[40:47], v8\n"
"v_smfmac_f32_16x16x64_bf16 v[72:75], v[60:63], a[96:103], v8\nv_smfmac_f32_16x16x64_bf16 v[76:79], v[60:63], a[152:159], v8\ns_cbranch_execz vc15x0_p0\nbuffer_load_dwordx4 v[116:119], v20, s[8:11], s41 offen offset:0 sc1\n"
"buffer_load_dwordx4 v[120:123], v20, s[8:11], s41 offen offset:1024 sc1\nv_smfmac_f32_16x16x64_bf16 v[80:83], v[60:63], a[208:215], v8\nv_smfmac_f32_16x16x64_bf16 v[68:71], v[64:67], a[48:55], v8\nv_smfmac_f32_16x16x64_bf16 v[72:75], v[64:67], a[104:111], v8\n"
"v_smfmac_f32_16x16x64_bf16 v[76:79], v[64:67], a[160:167], v8\nv_smfmac_f32_16x16x64_bf16 v[80:83], v[64:67], a[216:223], v8\nvc15x0_p0:\ns_cbranch_execz vc15x1_p0\n"
"v_add_f32 v219, v68, v69\nv_add_f32 v220, v70, v71\nv_cvt_pk_bf16_f32 v156, v219, v220\ns_nop 0\n"
"v_add_f32 v219, v72, v73\nv_add_f32 v220, v74, v75\nv_cvt_pk_bf16_f32 v157, v219, v220\ns_nop 0\n"
"vc15x1_p0:\ns_cbranch_execz vc15x2_p0\nv_add_f32 v219, v76, v77\nv_add_f32 v220, v78, v79\n"
"v_cvt_pk_bf16_f32 v158, v219, v220\ns_nop 0\nv_add_f32 v219, v80, v81\nv_add_f32 v220, v82, v83\n"
"v_cvt_pk_bf16_f32 v159, v219, v220\ns_nop 0\ns_nop 1\ns_nop 1\n"
"vc15x2_p0:\ns_cbranch_execz vd15_p0\nbuffer_load_dwordx4 v[124:127], v21, s[8:11], s41 offen offset:0 sc1\nbuffer_load_dwordx4 v[128:131], v21, s[8:11], s41 offen offset:1024 sc1\n"
"s_nop 15\ns_nop 13\nvd15_p0:\ns_cbranch_execnz tl24_p0\n"
"s_nop 0\ns_nop 0\nva16_p0:\ns_branch vb16_p0\n"
"sx16_p0:\ns_add_u32 s60, s60, 4928\ns_addc_u32 s61, s61, 0\ns_setpc_b64 s[60:61]\n"
"vb16_p0:\ns_branch vr16_p0\nv_smfmac_f32_16x16x64_bf16 v[72:75], v[52:55], a[80:87], v8\nv_smfmac_f32_16x16x64_bf16 v[76:79], v[52:55], a[136:143], v8\n"
"v_smfmac_f32_16x16x64_bf16 v[80:83], v[52:55], a[192:199], v8\nv_smfmac_f32_16x16x64_bf16 v[68:71], v[56:59], a[32:39], v8\nv_smfmac_f32_16x16x64_bf16 v[72:75], v[56:59], a[88:95], v8\nv_smfmac_f32_16x16x64_bf16 v[76:79], v[56:59], a[144:151], v8\n"
"v_smfmac_f32_16x16x64_bf16 v[80:83], v[56:59], a[200:207], v8\nvr16_p0:\nv_smfmac_f32_16x16x64_bf16 v[68:71], v[60:63], a[40:47], v8\nv_smfmac_f32_16x16x64_bf16 v[72:75], v[60:63], a[96:103], v8\n"
"v_smfmac_f32_16x16x64_bf16 v[76:79], v[60:63], a[152:159], v8\nv_smfmac_f32_16x16x64_bf16 v[80:83], v[60:63], a[208:215], v8\ns_cbranch_execz vc16x0_p0\nbuffer_load_dwordx4 v[116:119], v20, s[8:11], s41 offen offset:0 sc1\n"
"buffer_load_dwordx4 v[120:123], v20, s[8:11], s41 offen offset:1024 sc1\nv_smfmac_f32_16x16x64_bf16 v[68:71], v[64:67], a[48:55], v8\nv_smfmac_f32_16x16x64_bf16 v[72:75], v[64:67], a[104:111], v8\nv_smfmac_f32_16x16x64_bf16 v[76:79], v[64:67], a[160:167], v8\n"
"v_smfmac_f32_16x16x64_bf16 v[80:83], v[64:67], a[216:223], v8\nvc16x0_p0:\ns_cbranch_execz vc16x1_p0\nv_add_f32 v219, v68, v69\n"
"v_add_f32 v220, v70, v71\nv_cvt_pk_bf16_f32 v156, v219, v220\ns_nop 0\nv_add_f32 v219, v72, v73\n"
"v_add_f32 v220, v74, v75\nv_cvt_pk_bf16_f32 v157, v219, v220\ns_nop 0\nvc16x1_p0:\n"
"s_cbranch_execz vc16x2_p0\nv_add_f32 v219, v76, v77\nv_add_f32 v220, v78, v79\nv_cvt_pk_bf16_f32 v158, v219, v220\n"
"s_nop 0\nv_add_f32 v219, v80, v81\nv_add_f32 v220, v82, v83\nv_cvt_pk_bf16_f32 v159, v219, v220\n"
"s_nop 0\ns_nop 1\ns_nop 1\ns_nop 1\n"
"s_nop 1\nvc16x2_p0:\ns_cbranch_execz vd16_p0\nbuffer_load_dwordx4 v[124:127], v21, s[8:11], s41 offen offset:0 sc1\n"
"buffer_load_dwordx4 v[128:131], v21, s[8:11], s41 offen offset:1024 sc1\ns_nop 15\ns_nop 13\nvd16_p0:\n"
"s_cbranch_execnz tl24_p0\ns_nop 0\ns_nop 0\nva17_p0:\n"
"s_branch vb17_p0\nsx17_p0:\ns_add_u32 s60, s60, 5236\ns_addc_u32 s61, s61, 0\n"
"s_setpc_b64 s[60:61]\nvb17_p0:\ns_branch vr17_p0\nv_smfmac_f32_16x16x64_bf16 v[76:79], v[52:55], a[136:143], v8\n"
"v_smfmac_f32_16x16x64_bf16 v[80:83], v[52:55], a[192:199], v8\nv_smfmac_f32_16x16x64_bf16 v[68:71], v[56:59], a[32:39], v8\nv_smfmac_f32_16x16x64_bf16 v[72:75], v[56:59], a[88:95], v8\nv_smfmac_f32_16x16x64_bf16 v[76:79], v[56:59], a[144:151], v8\n"
"v_smfmac_f32_16x16x64_bf16 v[80:83], v[56:59], a[200:207], v8\nv_smfmac_f32_16x16x64_bf16 v[68:71], v[60:63], a[40:47], v8\nvr17_p0:\nv_smfmac_f32_16x16x64_bf16 v[72:75], v[60:63], a[96:103], v8\n"
"v_smfmac_f32_16x16x64_bf16 v[76:79], v[60:63], a[152:159], v8\nv_smfmac_f32_16x16x64_bf16 v[80:83], v[60:63], a[208:215], v8\nv_smfmac_f32_16x16x64_bf16 v[68:71], v[64:67], a[48:55], v8\ns_cbranch_execz vc17x0_p0\n"
"buffer_load_dwordx4 v[116:119], v20, s[8:11], s41 offen offset:0 sc1\nbuffer_load_dwordx4 v[120:123], v20, s[8:11], s41 offen offset:1024 sc1\nv_smfmac_f32_16x16x64_bf16 v[72:75], v[64:67], a[104:111], v8\nv_smfmac_f32_16x16x64_bf16 v[76:79], v[64:67], a[160:167], v8\n"
"v_smfmac_f32_16x16x64_bf16 v[80:83], v[64:67], a[216:223], v8\nvc17x0_p0:\ns_cbranch_execz vc17x1_p0\nv_add_f32 v219, v68, v69\n"
"v_add_f32 v220, v70, v71\nv_cvt_pk_bf16_f32 v156, v219, v220\ns_nop 0\nv_add_f32 v219, v72, v73\n"
"v_add_f32 v220, v74, v75\nv_cvt_pk_bf16_f32 v157, v219, v220\ns_nop 0\nvc17x1_p0:\n"
"s_cbranch_execz vc17x2_p0\nv_add_f32 v219, v76, v77\nv_add_f32 v220, v78, v79\nv_cvt_pk_bf16_f32 v158, v219, v220\n"
"s_nop 0\nv_add_f32 v219, v80, v81\nv_add_f32 v220, v82, v83\nv_cvt_pk_bf16_f32 v159, v219, v220\n"
"s_nop 0\ns_nop 1\ns_nop 1\ns_nop 1\n"
"s_nop 1\nvc17x2_p0:\ns_cbranch_execz vc17x3_p0\ns_nop 1\n"
"s_nop 1\nvc17x3_p0:\ns_cbranch_execz vd17_p0\nbuffer_load_dwordx4 v[124:127], v21, s[8:11], s41 offen offset:0 sc1\n"
"buffer_load_dwordx4 v[128:131], v21, s[8:11], s41 offen offset:1024 sc1\ns_nop 15\ns_nop 13\nvd17_p0:\n"
"s_cbranch_execnz tl24_p0\ns_nop 0\nva18_p0:\ns_branch warm_p0\n"
"sx18_p0:\ns_add_u32 s60, s60, 5544\ns_addc_u32 s61, s61, 0\ns_setpc_b64 s[60:61]\n"
"vb18_p0:\ns_branch vr18_p0\nv_smfmac_f32_16x16x64_bf16 v[80:83], v[52:55], a[192:199], v8\nv_smfmac_f32_16x16x64_bf16 v[68:71], v[56:59], a[32:39], v8\n"
"v_smfmac_f32_16x16x64_bf16 v[72:75], v[56:59], a[88:95], v8\nv_smfmac_f32_16x16x64_bf16 v[76:79], v[56:59], a[144:151], v8\nv_smfmac_f32_16x16x64_bf16 v[80:83], v[56:59], a[200:207], v8\nv_smfmac_f32_16x16x64_bf16 v[68:71], v[60:63], a[40:47], v8\n"
"v_smfmac_f32_16x16x64_bf16 v[72:75], v[60:63], a[96:103], v8\nvr18_p0:\nv_smfmac_f32_16x16x64_bf16 v[76:79], v[60:63], a[152:159], v8\nv_smfmac_f32_16x16x64_bf16 v[80:83], v[60:63], a[208:215], v8\n"
"v_smfmac_f32_16x16x64_bf16 v[68:71], v[64:67], a[48:55], v8\nv_smfmac_f32_16x16x64_bf16 v[72:75], v[64:67], a[104:111], v8\ns_cbranch_execz vc18x0_p0\nbuffer_load_dwordx4 v[116:119], v20, s[8:11], s41 offen offset:0 sc1\n"
"buffer_load_dwordx4 v[120:123], v20, s[8:11], s41 offen offset:1024 sc1\nv_smfmac_f32_16x16x64_bf16 v[76:79], v[64:67], a[160:167], v8\nv_smfmac_f32_16x16x64_bf16 v[80:83], v[64:67], a[216:223], v8\nv_add_f32 v219, v68, v69\n"
"v_add_f32 v220, v70, v71\nv_cvt_pk_bf16_f32 v156, v219, v220\ns_nop 0\nvc18x0_p0:\n"
"s_cbranch_execz vc18x1_p0\nv_add_f32 v219, v72, v73\nv_add_f32 v220, v74, v75\nv_cvt_pk_bf16_f32 v157, v219, v220\n"
"s_nop 0\nv_add_f32 v219, v76, v77\nv_add_f32 v220, v78, v79\nv_cvt_pk_bf16_f32 v158, v219, v220\n"
"s_nop 0\nvc18x1_p0:\ns_cbranch_execz vc18x2_p0\nv_add_f32 v219, v80, v81\n"
"v_add_f32 v220, v82, v83\nv_cvt_pk_bf16_f32 v159, v219, v220\ns_nop 0\ns_nop 1\n"
"s_nop 1\ns_nop 1\ns_nop 1\ns_nop 1\n"
"s_nop 1\ns_nop 1\ns_nop 1\nvc18x2_p0:\n"
"s_cbranch_execz vd18_p0\nbuffer_load_dwordx4 v[124:127], v21, s[8:11], s41 offen offset:0 sc1\nbuffer_load_dwordx4 v[128:131], v21, s[8:11], s41 offen offset:1024 sc1\ns_nop 15\n"
"s_nop 13\nvd18_p0:\ns_cbranch_execnz tl24_p0\ns_nop 0\n"
"s_nop 0\nva19_p0:\ns_branch vb19_p0\nsx19_p0:\n"
"s_add_u32 s60, s60, 5852\ns_addc_u32 s61, s61, 0\ns_setpc_b64 s[60:61]\nvb19_p0:\n"
"s_branch vr19_p0\nv_smfmac_f32_16x16x64_bf16 v[68:71], v[56:59], a[32:39], v8\nv_smfmac_f32_16x16x64_bf16 v[72:75], v[56:59], a[88:95], v8\nv_smfmac_f32_16x16x64_bf16 v[76:79], v[56:59], a[144:151], v8\n"
"v_smfmac_f32_16x16x64_bf16 v[80:83], v[56:59], a[200:207], v8\nv_smfmac_f32_16x16x64_bf16 v[68:71], v[60:63], a[40:47], v8\nv_smfmac_f32_16x16x64_bf16 v[72:75], v[60:63], a[96:103], v8\nv_smfmac_f32_16x16x64_bf16 v[76:79], v[60:63], a[152:159], v8\n"
"vr19_p0:\nv_smfmac_f32_16x16x64_bf16 v[80:83], v[60:63], a[208:215], v8\nv_smfmac_f32_16x16x64_bf16 v[68:71], v[64:67], a[48:55], v8\nv_smfmac_f32_16x16x64_bf16 v[72:75], v[64:67], a[104:111], v8\n"
"v_smfmac_f32_16x16x64_bf16 v[76:79], v[64:67], a[160:167], v8\ns_cbranch_execz vc19x0_p0\nbuffer_load_dwordx4 v[116:119], v20, s[8:11], s41 offen offset:0 sc1\nbuffer_load_dwordx4 v[120:123], v20, s[8:11], s41 offen offset:1024 sc1\n"
"v_smfmac_f32_16x16x64_bf16 v[80:83], v[64:67], a[216:223], v8\nv_add_f32 v219, v68, v69\nv_add_f32 v220, v70, v71\nv_cvt_pk_bf16_f32 v156, v219, v220\n"
"s_nop 0\nvc19x0_p0:\ns_cbranch_execz vc19x1_p0\nv_add_f32 v219, v72, v73\n"
"v_add_f32 v220, v74, v75\nv_cvt_pk_bf16_f32 v157, v219, v220\ns_nop 0\nv_add_f32 v219, v76, v77\n"
"v_add_f32 v220, v78, v79\nv_cvt_pk_bf16_f32 v158, v219, v220\ns_nop 0\nvc19x1_p0:\n"
"s_cbranch_execz vc19x2_p0\nv_add_f32 v219, v80, v81\nv_add_f32 v220, v82, v83\nv_cvt_pk_bf16_f32 v159, v219, v220\n"
"s_nop 0\ns_nop 1\ns_nop 1\ns_nop 1\n"
"s_nop 1\ns_nop 1\ns_nop 1\ns_nop 1\n"
"s_nop 1\nvc19x2_p0:\ns_cbranch_execz vc19x3_p0\ns_nop 1\n"
"s_nop 1\nvc19x3_p0:\ns_cbranch_execz vd19_p0\nbuffer_load_dwordx4 v[124:127], v21, s[8:11], s41 offen offset:0 sc1\n"
"buffer_load_dwordx4 v[128:131], v21, s[8:11], s41 offen offset:1024 sc1\ns_nop 15\ns_nop 13\nvd19_p0:\n"
"s_cbranch_execnz tl24_p0\ns_nop 0\nva20_p0:\ns_branch vb20_p0\n"
"sx20_p0:\ns_add_u32 s60, s60, 6160\ns_addc_u32 s61, s61, 0\ns_setpc_b64 s[60:61]\n"
"vb20_p0:\ns_branch vr20_p0\nv_smfmac_f32_16x16x64_bf16 v[72:75], v[56:59], a[88:95], v8\nv_smfmac_f32_16x16x64_bf16 v[76:79], v[56:59], a[144:151], v8\n"
"v_smfmac_f32_16x16x64_bf16 v[80:83], v[56:59], a[200:207], v8\nv_smfmac_f32_16x16x64_bf16 v[68:71], v[60:63], a[40:47], v8\nv_smfmac_f32_16x16x64_bf16 v[72:75], v[60:63], a[96:103], v8\nv_smfmac_f32_16x16x64_bf16 v[76:79], v[60:63], a[152:159], v8\n"
"v_smfmac_f32_16x16x64_bf16 v[80:83], v[60:63], a[208:215], v8\nvr20_p0:\nv_smfmac_f32_16x16x64_bf16 v[68:71], v[64:67], a[48:55], v8\nv_smfmac_f32_16x16x64_bf16 v[72:75], v[64:67], a[104:111], v8\n"
"v_smfmac_f32_16x16x64_bf16 v[76:79], v[64:67], a[160:167], v8\nv_smfmac_f32_16x16x64_bf16 v[80:83], v[64:67], a[216:223], v8\nsgn_p0:\ns_cbranch_execz vc20x0_p0\n"
"buffer_load_dwordx4 v[116:119], v20, s[8:11], s41 offen offset:0 sc1\nbuffer_load_dwordx4 v[120:123], v20, s[8:11], s41 offen offset:1024 sc1\nv_add_f32 v219, v68, v69\nv_add_f32 v220, v70, v71\n"
"v_cvt_pk_bf16_f32 v156, v219, v220\ns_nop 0\nv_add_f32 v219, v72, v73\nv_add_f32 v220, v74, v75\n"
"v_cvt_pk_bf16_f32 v157, v219, v220\ns_nop 0\nvc20x0_p0:\ns_cbranch_execz vc20x1_p0\n"
"v_add_f32 v219, v76, v77\nv_add_f32 v220, v78, v79\nv_cvt_pk_bf16_f32 v158, v219, v220\ns_nop 0\n"
"v_add_f32 v219, v80, v81\nv_add_f32 v220, v82, v83\nv_cvt_pk_bf16_f32 v159, v219, v220\ns_nop 0\n"
"s_nop 1\ns_nop 1\ns_nop 1\ns_nop 1\n"
"vc20x1_p0:\ns_cbranch_execz vc20x2_p0\ns_nop 1\ns_nop 1\n"
"s_nop 1\ns_nop 1\ns_nop 1\ns_nop 1\n"
"s_nop 1\ns_nop 1\nvc20x2_p0:\ns_cbranch_execz vd20_p0\n"
"buffer_load_dwordx4 v[124:127], v21, s[8:11], s41 offen offset:0 sc1\nbuffer_load_dwordx4 v[128:131], v21, s[8:11], s41 offen offset:1024 sc1\ns_nop 15\ns_nop 13\n"
"vd20_p0:\ns_cbranch_execnz tl24_p0\ns_nop 0\ns_nop 0\n"
"va21_p0:\ns_branch vb21_p0\nsx21_p0:\ns_add_u32 s60, s60, 6468\n"
"s_addc_u32 s61, s61, 0\ns_setpc_b64 s[60:61]\nvb21_p0:\ns_branch vr21_p0\n"
"v_smfmac_f32_16x16x64_bf16 v[76:79], v[56:59], a[144:151], v8\nv_smfmac_f32_16x16x64_bf16 v[80:83], v[56:59], a[200:207], v8\nv_smfmac_f32_16x16x64_bf16 v[68:71], v[60:63], a[40:47], v8\nv_smfmac_f32_16x16x64_bf16 v[72:75], v[60:63], a[96:103], v8\n"
"v_smfmac_f32_16x16x64_bf16 v[76:79], v[60:63], a[152:159], v8\nv_smfmac_f32_16x16x64_bf16 v[80:83], v[60:63], a[208:215], v8\nv_smfmac_f32_16x16x64_bf16 v[68:71], v[64:67], a[48:55], v8\nvr21_p0:\n"
"v_smfmac_f32_16x16x64_bf16 v[72:75], v[64:67], a[104:111], v8\nv_smfmac_f32_16x16x64_bf16 v[76:79], v[64:67], a[160:167], v8\nv_smfmac_f32_16x16x64_bf16 v[80:83], v[64:67], a[216:223], v8\ns_nop 1\n"
"s_nop 1\ns_cbranch_execz vc21x0_p0\nbuffer_load_dwordx4 v[116:119], v20, s[8:11], s41 offen offset:0 sc1\nbuffer_load_dwordx4 v[120:123], v20, s[8:11], s41 offen offset:1024 sc1\n"
"v_add_f32 v219, v68, v69\nv_add_f32 v220, v70, v71\nv_cvt_pk_bf16_f32 v156, v219, v220\ns_nop 0\n"
"v_add_f32 v219, v72, v73\nv_add_f32 v220, v74, v75\nv_cvt_pk_bf16_f32 v157, v219, v220\ns_nop 0\n"
"vc21x0_p0:\ns_cbranch_execz vc21x1_p0\nv_add_f32 v219, v76, v77\nv_add_f32 v220, v78, v79\n"
"v_cvt_pk_bf16_f32 v158, v219, v220\ns_nop 0\nv_add_f32 v219, v80, v81\nv_add_f32 v220, v82, v83\n"
"v_cvt_pk_bf16_f32 v159, v219, v220\ns_nop 0\ns_nop 1\ns_nop 1\n"
"s_nop 1\ns_nop 1\nvc21x1_p0:\ns_cbranch_execz vc21x2_p0\n"
"s_nop 1\ns_nop 1\ns_nop 1\ns_nop 1\n"
"s_nop 1\ns_nop 1\ns_nop 1\ns_nop 1\n"
"vc21x2_p0:\ns_cbranch_execz vd21_p0\nbuffer_load_dwordx4 v[124:127], v21, s[8:11], s41 offen offset:0 sc1\nbuffer_load_dwordx4 v[128:131], v21, s[8:11], s41 offen offset:1024 sc1\n"
"s_nop 15\ns_nop 13\nvd21_p0:\ns_cbranch_execnz tl24_p0\n"
"s_nop 0\ns_nop 0\nva22_p0:\ns_branch vb22_p0\n"
"sx22_p0:\ns_add_u32 s60, s60, 6776\ns_addc_u32 s61, s61, 0\ns_setpc_b64 s[60:61]\n"
"vb22_p0:\ns_branch vr22_p0\nv_smfmac_f32_16x16x64_bf16 v[80:83], v[56:59], a[200:207], v8\nv_smfmac_f32_16x16x64_bf16 v[68:71], v[60:63], a[40:47], v8\n"
"v_smfmac_f32_16x16x64_bf16 v[72:75], v[60:63], a[96:103], v8\nv_smfmac_f32_16x16x64_bf16 v[76:79], v[60:63], a[152:159], v8\nv_smfmac_f32_16x16x64_bf16 v[80:83], v[60:63], a[208:215], v8\nv_smfmac_f32_16x16x64_bf16 v[68:71], v[64:67], a[48:55], v8\n"
"v_smfmac_f32_16x16x64_bf16 v[72:75], v[64:67], a[104:111], v8\nvr22_p0:\nv_smfmac_f32_16x16x64_bf16 v[76:79], v[64:67], a[160:167], v8\nv_smfmac_f32_16x16x64_bf16 v[80:83], v[64:67], a[216:223], v8\n"
"s_nop 1\ns_nop 1\ns_nop 1\ns_nop 1\n"
"s_cbranch_execz vc22x0_p0\nbuffer_load_dwordx4 v[116:119], v20, s[8:11], s41 offen offset:0 sc1\nbuffer_load_dwordx4 v[120:123], v20, s[8:11], s41 offen offset:1024 sc1\nv_add_f32 v219, v68, v69\n"
"v_add_f32 v220, v70, v71\nv_cvt_pk_bf16_f32 v156, v219, v220\ns_nop 0\nv_add_f32 v219, v72, v73\n"
"v_add_f32 v220, v74, v75\nv_cvt_pk_bf16_f32 v157, v219, v220\ns_nop 0\nvc22x0_p0:\n"
"s_cbranch_execz vc22x1_p0\nv_add_f32 v219, v76, v77\nv_add_f32 v220, v78, v79\nv_cvt_pk_bf16_f32 v158, v219, v220\n"
"s_nop 0\nv_add_f32 v219, v80, v81\nv_add_f32 v220, v82, v83\nv_cvt_pk_bf16_f32 v159, v219, v220\n"
"s_nop 0\ns_nop 1\ns_nop 1\ns_nop 1\n"
"s_nop 1\nvc22x1_p0:\ns_cbranch_execz vc22x2_p0\ns_nop 1\n"
"s_nop 1\ns_nop 1\ns_nop 1\ns_nop 1\n"
"s_nop 1\ns_nop 1\ns_nop 1\nvc22x2_p0:\n"
"s_cbranch_execz vd22_p0\nbuffer_load_dwordx4 v[124:127], v21, s[8:11], s41 offen offset:0 sc1\nbuffer_load_dwordx4 v[128:131], v21, s[8:11], s41 offen offset:1024 sc1\ns_nop 15\n"
"s_nop 13\nvd22_p0:\ns_cbranch_execnz tl24_p0\ns_nop 0\n"
"s_nop 0\nva23_p0:\ns_branch vb23_p0\nsx23_p0:\n"
"s_add_u32 s60, s60, 7084\ns_addc_u32 s61, s61, 0\ns_setpc_b64 s[60:61]\nvb23_p0:\n"
"s_branch vr23_p0\nv_smfmac_f32_16x16x64_bf16 v[68:71], v[60:63], a[40:47], v8\nv_smfmac_f32_16x16x64_bf16 v[72:75], v[60:63], a[96:103], v8\nv_smfmac_f32_16x16x64_bf16 v[76:79], v[60:63], a[152:159], v8\n"
"v_smfmac_f32_16x16x64_bf16 v[80:83], v[60:63], a[208:215], v8\nv_smfmac_f32_16x16x64_bf16 v[68:71], v[64:67], a[48:55], v8\nv_smfmac_f32_16x16x64_bf16 v[72:75], v[64:67], a[104:111], v8\nv_smfmac_f32_16x16x64_bf16 v[76:79], v[64:67], a[160:167], v8\n"
"vr23_p0:\nv_smfmac_f32_16x16x64_bf16 v[80:83], v[64:67], a[216:223], v8\ns_nop 1\ns_nop 1\n"
"s_nop 1\ns_nop 1\ns_nop 1\ns_nop 1\n"
"s_cbranch_execz vc23x0_p0\nbuffer_load_dwordx4 v[116:119], v20, s[8:11], s41 offen offset:0 sc1\nbuffer_load_dwordx4 v[120:123], v20, s[8:11], s41 offen offset:1024 sc1\nv_add_f32 v219, v68, v69\n"
"v_add_f32 v220, v70, v71\nv_cvt_pk_bf16_f32 v156, v219, v220\ns_nop 0\nv_add_f32 v219, v72, v73\n"
"v_add_f32 v220, v74, v75\nv_cvt_pk_bf16_f32 v157, v219, v220\ns_nop 0\nvc23x0_p0:\n"
"s_cbranch_execz vc23x1_p0\nv_add_f32 v219, v76, v77\nv_add_f32 v220, v78, v79\nv_cvt_pk_bf16_f32 v158, v219, v220\n"
"s_nop 0\nv_add_f32 v219, v80, v81\nv_add_f32 v220, v82, v83\nv_cvt_pk_bf16_f32 v159, v219, v220\n"
"s_nop 0\ns_nop 1\ns_nop 1\ns_nop 1\n"
"s_nop 1\nvc23x1_p0:\ns_cbranch_execz vc23x2_p0\ns_nop 1\n"
"s_nop 1\ns_nop 1\ns_nop 1\ns_nop 1\n"
"s_nop 1\ns_nop 1\ns_nop 1\nvc23x2_p0:\n"
"s_cbranch_execz vd23_p0\nbuffer_load_dwordx4 v[124:127], v21, s[8:11], s41 offen offset:0 sc1\nbuffer_load_dwordx4 v[128:131], v21, s[8:11], s41 offen offset:1024 sc1\ns_nop 15\n"
"s_nop 13\nvd23_p0:\ns_cbranch_execnz tl24_p0\ns_nop 0\n"
"s_nop 0\nva24_p0:\ns_branch warm_p0\nsx24_p0:\n"
"s_add_u32 s60, s60, 7392\ns_addc_u32 s61, s61, 0\ns_setpc_b64 s[60:61]\nvb24_p0:\n"
"s_branch vr24_p0\nv_smfmac_f32_16x16x64_bf16 v[72:75], v[60:63], a[96:103], v8\nv_smfmac_f32_16x16x64_bf16 v[76:79], v[60:63], a[152:159], v8\nv_smfmac_f32_16x16x64_bf16 v[80:83], v[60:63], a[208:215], v8\n"
"v_smfmac_f32_16x16x64_bf16 v[68:71], v[64:67], a[48:55], v8\nv_smfmac_f32_16x16x64_bf16 v[72:75], v[64:67], a[104:111], v8\nv_smfmac_f32_16x16x64_bf16 v[76:79], v[64:67], a[160:167], v8\nv_smfmac_f32_16x16x64_bf16 v[80:83], v[64:67], a[216:223], v8\n"
"vr24_p0:\ns_nop 1\ns_nop 1\ns_nop 1\n"
"s_nop 1\ns_nop 1\ns_nop 1\ns_nop 1\n"
"s_nop 1\ns_cbranch_execz vc24x0_p0\nbuffer_load_dwordx4 v[116:119], v20, s[8:11], s41 offen offset:0 sc1\nbuffer_load_dwordx4 v[120:123], v20, s[8:11], s41 offen offset:1024 sc1\n"
"v_add_f32 v219, v68, v69\nv_add_f32 v220, v70, v71\nv_cvt_pk_bf16_f32 v156, v219, v220\ns_nop 0\n"
"v_add_f32 v219, v72, v73\nv_add_f32 v220, v74, v75\nv_cvt_pk_bf16_f32 v157, v219, v220\ns_nop 0\n"
"vc24x0_p0:\ns_cbranch_execz vc24x1_p0\nv_add_f32 v219, v76, v77\nv_add_f32 v220, v78, v79\n"
"v_cvt_pk_bf16_f32 v158, v219, v220\ns_nop 0\nv_add_f32 v219, v80, v81\nv_add_f32 v220, v82, v83\n"
"v_cvt_pk_bf16_f32 v159, v219, v220\ns_nop 0\ns_nop 1\ns_nop 1\n"
"s_nop 1\ns_nop 1\nvc24x1_p0:\ns_cbranch_execz vc24x2_p0\n"
"s_nop 1\ns_nop 1\ns_nop 1\ns_nop 1\n"
"s_nop 1\ns_nop 1\ns_nop 1\ns_nop 1\n"
"vc24x2_p0:\ns_cbranch_execz vd24_p0\nbuffer_load_dwordx4 v[124:127], v21, s[8:11], s41 offen offset:0 sc1\nbuffer_load_dwordx4 v[128:131], v21, s[8:11], s41 offen offset:1024 sc1\n"
"s_nop 15\ns_nop 13\nvd24_p0:\ns_cbranch_execnz tl24_p0\n"
"s_nop 0\ns_nop 0\nva25_p0:\ns_branch vb25_p0\n"
"sx25_p0:\ns_add_u32 s60, s60, 7700\ns_addc_u32 s61, s61, 0\ns_setpc_b64 s[60:61]\n"
"vb25_p0:\ns_branch vr25_p0\nv_smfmac_f32_16x16x64_bf16 v[76:79], v[60:63], a[152:159], v8\nv_smfmac_f32_16x16x64_bf16 v[80:83], v[60:63], a[208:215], v8\n"
"v_smfmac_f32_16x16x64_bf16 v[68:71], v[64:67], a[48:55], v8\nv_smfmac_f32_16x16x64_bf16 v[72:75], v[64:67], a[104:111], v8\nv_smfmac_f32_16x16x64_bf16 v[76:79], v[64:67], a[160:167], v8\nv_smfmac_f32_16x16x64_bf16 v[80:83], v[64:67], a[216:223], v8\n"
"s_nop 1\ns_nop 1\nvr25_p0:\ns_nop 1\n"
"s_nop 1\ns_nop 1\ns_nop 1\ns_nop 1\n"
"s_nop 1\ns_nop 1\ns_nop 1\ns_cbranch_execz vc25x0_p0\n"
"buffer_load_dwordx4 v[116:119], v20, s[8:11], s41 offen offset:0 sc1\nbuffer_load_dwordx4 v[120:123], v20, s[8:11], s41 offen offset:1024 sc1\nv_add_f32 v219, v68, v69\nv_add_f32 v220, v70, v71\n"
"v_cvt_pk_bf16_f32 v156, v219, v220\ns_nop 0\nv_add_f32 v219, v72, v73\nv_add_f32 v220, v74, v75\n"
"v_cvt_pk_bf16_f32 v157, v219, v220\ns_nop 0\nvc25x0_p0:\ns_cbranch_execz vc25x1_p0\n"
"v_add_f32 v219, v76, v77\nv_add_f32 v220, v78, v79\nv_cvt_pk_bf16_f32 v158, v219, v220\ns_nop 0\n"
"v_add_f32 v219, v80, v81\nv_add_f32 v220, v82, v83\nv_cvt_pk_bf16_f32 v159, v219, v220\ns_nop 0\n"
"s_nop 1\ns_nop 1\ns_nop 1\ns_nop 1\n"
"vc25x1_p0:\ns_cbranch_execz vc25x2_p0\ns_nop 1\ns_nop 1\n"
"s_nop 1\ns_nop 1\ns_nop 1\ns_nop 1\n"
"s_nop 1\ns_nop 1\nvc25x2_p0:\ns_cbranch_execz vd25_p0\n"
"buffer_load_dwordx4 v[124:127], v21, s[8:11], s41 offen offset:0 sc1\nbuffer_load_dwordx4 v[128:131], v21, s[8:11], s41 offen offset:1024 sc1\ns_nop 15\ns_nop 13\n"
"vd25_p0:\ns_cbranch_execnz tl24_p0\ns_nop 0\ns_nop 0\n"
"va26_p0:\ns_branch vb26_p0\nsx26_p0:\ns_add_u32 s60, s60, 8008\n"
"s_addc_u32 s61, s61, 0\ns_setpc_b64 s[60:61]\nvb26_p0:\ns_branch vr26_p0\n"
"v_smfmac_f32_16x16x64_bf16 v[80:83], v[60:63], a[208:215], v8\nv_smfmac_f32_16x16x64_bf16 v[68:71], v[64:67], a[48:55], v8\nv_smfmac_f32_16x16x64_bf16 v[72:75], v[64:67], a[104:111], v8\nv_smfmac_f32_16x16x64_bf16 v[76:79], v[64:67], a[160:167], v8\n"
"v_smfmac_f32_16x16x64_bf16 v[80:83], v[64:67], a[216:223], v8\ns_nop 1\ns_nop 1\ns_nop 1\n"
"s_nop 1\nvr26_p0:\ns_nop 1\ns_nop 1\n"
"s_nop 1\ns_nop 1\ns_nop 1\ns_nop 1\n"
"s_nop 1\ns_nop 1\ns_cbranch_execz vc26x0_p0\nbuffer_load_dwordx4 v[116:119], v20, s[8:11], s41 offen offset:0 sc1\n"
"buffer_load_dwordx4 v[120:123], v20, s[8:11], s41 offen offset:1024 sc1\nv_add_f32 v219, v68, v69\nv_add_f32 v220, v70, v71\nv_cvt_pk_bf16_f32 v156, v219, v220\n"
"s_nop 0\nv_add_f32 v219, v72, v73\nv_add_f32 v220, v74, v75\nv_cvt_pk_bf16_f32 v157, v219, v220\n"
"s_nop 0\nvc26x0_p0:\ns_cbranch_execz vc26x1_p0\nv_add_f32 v219, v76, v77\n"
"v_add_f32 v220, v78, v79\nv_cvt_pk_bf16_f32 v158, v219, v220\ns_nop 0\nv_add_f32 v219, v80, v81\n"
"v_add_f32 v220, v82, v83\nv_cvt_pk_bf16_f32 v159, v219, v220\ns_nop 0\ns_nop 1\n"
"s_nop 1\ns_nop 1\ns_nop 1\nvc26x1_p0:\n"
"s_cbranch_execz vc26x2_p0\ns_nop 1\ns_nop 1\ns_nop 1\n"
"s_nop 1\ns_nop 1\ns_nop 1\ns_nop 1\n"
"s_nop 1\nvc26x2_p0:\ns_cbranch_execz vd26_p0\nbuffer_load_dwordx4 v[124:127], v21, s[8:11], s41 offen offset:0 sc1\n"
"buffer_load_dwordx4 v[128:131], v21, s[8:11], s41 offen offset:1024 sc1\ns_nop 15\ns_nop 13\nvd26_p0:\n"
"s_cbranch_execnz tl24_p0\ns_nop 0\ns_nop 0\nva27_p0:\n"
"s_branch vb27_p0\nsx27_p0:\ns_add_u32 s60, s60, 8316\ns_addc_u32 s61, s61, 0\n"
"s_setpc_b64 s[60:61]\nvb27_p0:\ns_branch vr27_p0\nv_smfmac_f32_16x16x64_bf16 v[68:71], v[64:67], a[48:55], v8\n"
"v_smfmac_f32_16x16x64_bf16 v[72:75], v[64:67], a[104:111], v8\nv_smfmac_f32_16x16x64_bf16 v[76:79], v[64:67], a[160:167], v8\nv_smfmac_f32_16x16x64_bf16 v[80:83], v[64:67], a[216:223], v8\ns_nop 1\n"
"s_nop 1\ns_nop 1\ns_nop 1\ns_nop 1\n"
"s_nop 1\nvr27_p0:\ns_nop 1\ns_nop 1\n"
"s_nop 1\ns_nop 1\ns_nop 1\ns_nop 1\n"
"s_nop 1\ns_nop 1\ns_cbranch_execz vc27x0_p0\nbuffer_load_dwordx4 v[116:119], v20, s[8:11], s41 offen offset:0 sc1\n"
"buffer_load_dwordx4 v[120:123], v20, s[8:11], s41 offen offset:1024 sc1\nv_add_f32 v219, v68, v69\nv_add_f32 v220, v70, v71\nv_cvt_pk_bf16_f32 v156, v219, v220\n"
"s_nop 0\nv_add_f32 v219, v72, v73\nv_add_f32 v220, v74, v75\nv_cvt_pk_bf16_f32 v157, v219, v220\n"
"s_nop 0\nvc27x0_p0:\ns_cbranch_execz vc27x1_p0\nv_add_f32 v219, v76, v77\n"
"v_add_f32 v220, v78, v79\nv_cvt_pk_bf16_f32 v158, v219, v220\ns_nop 0\nv_add_f32 v219, v80, v81\n"
"v_add_f32 v220, v82, v83\nv_cvt_pk_bf16_f32 v159, v219, v220\ns_nop 0\ns_nop 1\n"
"s_nop 1\ns_nop 1\ns_nop 1\nvc27x1_p0:\n"
"s_cbranch_execz vc27x2_p0\ns_nop 1\ns_nop 1\ns_nop 1\n"
"s_nop 1\ns_nop 1\ns_nop 1\ns_nop 1\n"
"s_nop 1\nvc27x2_p0:\ns_cbranch_execz vd27_p0\nbuffer_load_dwordx4 v[124:127], v21, s[8:11], s41 offen offset:0 sc1\n"
"buffer_load_dwordx4 v[128:131], v21, s[8:11], s41 offen offset:1024 sc1\ns_nop 15\ns_nop 13\nvd27_p0:\n"
"s_cbranch_execnz tl24_p0\ns_nop 0\ns_nop 0\nva28_p0:\n"
"s_branch vb28_p0\nsx28_p0:\ns_add_u32 s60, s60, 8624\ns_addc_u32 s61, s61, 0\n"
"s_setpc_b64 s[60:61]\nvb28_p0:\ns_branch vr28_p0\nv_smfmac_f32_16x16x64_bf16 v[72:75], v[64:67], a[104:111], v8\n"
"v_smfmac_f32_16x16x64_bf16 v[76:79], v[64:67], a[160:167], v8\nv_smfmac_f32_16x16x64_bf16 v[80:83], v[64:67], a[216:223], v8\ns_nop 1\ns_nop 1\n"
"s_nop 1\ns_nop 1\ns_nop 1\ns_nop 1\n"
"s_nop 1\ns_nop 1\nvr28_p0:\ns_nop 1\n"
"s_nop 1\ns_nop 1\ns_nop 1\ns_nop 1\n"
"s_nop 1\ns_nop 1\ns_nop 1\ns_cbranch_execz vc28x0_p0\n"
"buffer_load_dwordx4 v[116:119], v20, s[8:11], s41 offen offset:0 sc1\nbuffer_load_dwordx4 v[120:123], v20, s[8:11], s41 offen offset:1024 sc1\nv_add_f32 v219, v68, v69\nv_add_f32 v220, v70, v71\n"
"v_cvt_pk_bf16_f32 v156, v219, v220\ns_nop 0\nv_add_f32 v219, v72, v73\nv_add_f32 v220, v74, v75\n"
"v_cvt_pk_bf16_f32 v157, v219, v220\ns_nop 0\nvc28x0_p0:\ns_cbranch_execz vc28x1_p0\n"
"v_add_f32 v219, v76, v77\nv_add_f32 v220, v78, v79\nv_cvt_pk_bf16_f32 v158, v219, v220\ns_nop 0\n"
"v_add_f32 v219, v80, v81\nv_add_f32 v220, v82, v83\nv_cvt_pk_bf16_f32 v159, v219, v220\ns_nop 0\n"
"s_nop 1\ns_nop 1\ns_nop 1\ns_nop 1\n"
"vc28x1_p0:\ns_cbranch_execz vc28x2_p0\ns_nop 1\ns_nop 1\n"
"s_nop 1\ns_nop 1\ns_nop 1\ns_nop 1\n"
"s_nop 1\ns_nop 1\nvc28x2_p0:\ns_cbranch_execz vd28_p0\n"
"buffer_load_dwordx4 v[124:127], v21, s[8:11], s41 offen offset:0 sc1\nbuffer_load_dwordx4 v[128:131], v21, s[8:11], s41 offen offset:1024 sc1\ns_nop 15\ns_nop 13\n"
"vd28_p0:\ns_cbranch_execnz tl24_p0\ns_nop 0\ns_nop 0\n"
"va29_p0:\ns_branch vb29_p0\nsx29_p0:\ns_add_u32 s60, s60, 8932\n"
"s_addc_u32 s61, s61, 0\ns_setpc_b64 s[60:61]\nvb29_p0:\ns_branch vr29_p0\n"
"v_smfmac_f32_16x16x64_bf16 v[76:79], v[64:67], a[160:167], v8\nv_smfmac_f32_16x16x64_bf16 v[80:83], v[64:67], a[216:223], v8\ns_nop 1\ns_nop 1\n"
"s_nop 1\ns_nop 1\ns_nop 1\ns_nop 1\n"
"s_nop 1\ns_nop 1\ns_nop 1\ns_nop 1\n"
"vr29_p0:\ns_nop 1\ns_nop 1\ns_nop 1\n"
"s_nop 1\ns_nop 1\ns_nop 1\ns_nop 1\n"
"s_nop 1\ns_cbranch_execz vc29x0_p0\nbuffer_load_dwordx4 v[116:119], v20, s[8:11], s41 offen offset:0 sc1\nbuffer_load_dwordx4 v[120:123], v20, s[8:11], s41 offen offset:1024 sc1\n"
"v_add_f32 v219, v68, v69\nv_add_f32 v220, v70, v71\nv_cvt_pk_bf16_f32 v156, v219, v220\ns_nop 0\n"
"v_add_f32 v219, v72, v73\nv_add_f32 v220, v74, v75\nv_cvt_pk_bf16_f32 v157, v219, v220\ns_nop 0\n"
"vc29x0_p0:\ns_cbranch_execz vc29x1_p0\nv_add_f32 v219, v76, v77\nv_add_f32 v220, v78, v79\n"
"v_cvt_pk_bf16_f32 v158, v219, v220\ns_nop 0\nv_add_f32 v219, v80, v81\nv_add_f32 v220, v82, v83\n"
"v_cvt_pk_bf16_f32 v159, v219, v220\ns_nop 0\ns_nop 1\ns_nop 1\n"
"s_nop 1\ns_nop 1\nvc29x1_p0:\ns_cbranch_execz vc29x2_p0\n"
"s_nop 1\ns_nop 1\ns_nop 1\ns_nop 1\n"
"s_nop 1\ns_nop 1\ns_nop 1\ns_nop 1\n"
"vc29x2_p0:\ns_cbranch_execz vd29_p0\nbuffer_load_dwordx4 v[124:127], v21, s[8:11], s41 offen offset:0 sc1\nbuffer_load_dwordx4 v[128:131], v21, s[8:11], s41 offen offset:1024 sc1\n"
"s_nop 15\ns_nop 13\nvd29_p0:\ns_cbranch_execnz tl24_p0\n"
"s_nop 0\ns_nop 0\nva30_p0:\ns_branch warm_p0\n"
"sx30_p0:\ns_add_u32 s60, s60, 9240\ns_addc_u32 s61, s61, 0\ns_setpc_b64 s[60:61]\n"
"vb30_p0:\ns_branch vr30_p0\nv_smfmac_f32_16x16x64_bf16 v[80:83], v[64:67], a[216:223], v8\ns_nop 1\n"
"s_nop 1\ns_nop 1\ns_nop 1\ns_nop 1\n"
"s_nop 1\ns_nop 1\ns_nop 1\ns_nop 1\n"
"s_nop 1\ns_nop 1\ns_nop 1\nvr30_p0:\n"
"s_nop 1\ns_nop 1\ns_nop 1\ns_nop 1\n"
"s_nop 1\ns_nop 1\ns_nop 1\ns_nop 1\n"
"s_cbranch_execz vc30x0_p0\nbuffer_load_dwordx4 v[116:119], v20, s[8:11], s41 offen offset:0 sc1\nbuffer_load_dwordx4 v[120:123], v20, s[8:11], s41 offen offset:1024 sc1\nv_add_f32 v219, v68, v69\n"
"v_add_f32 v220, v70, v71\nv_cvt_pk_bf16_f32 v156, v219, v220\ns_nop 0\nv_add_f32 v219, v72, v73\n"
"v_add_f32 v220, v74, v75\nv_cvt_pk_bf16_f32 v157, v219, v220\ns_nop 0\nvc30x0_p0:\n"
"s_cbranch_execz vc30x1_p0\nv_add_f32 v219, v76, v77\nv_add_f32 v220, v78, v79\nv_cvt_pk_bf16_f32 v158, v219, v220\n"
"s_nop 0\nv_add_f32 v219, v80, v81\nv_add_f32 v220, v82, v83\nv_cvt_pk_bf16_f32 v159, v219, v220\n"
"s_nop 0\ns_nop 1\ns_nop 1\ns_nop 1\n"
"s_nop 1\nvc30x1_p0:\ns_cbranch_execz vc30x2_p0\ns_nop 1\n"
"s_nop 1\ns_nop 1\ns_nop 1\ns_nop 1\n"
"s_nop 1\ns_nop 1\ns_nop 1\nvc30x2_p0:\n"
"s_cbranch_execz vd30_p0\nbuffer_load_dwordx4 v[124:127], v21, s[8:11], s41 offen offset:0 sc1\nbuffer_load_dwordx4 v[128:131], v21, s[8:11], s41 offen offset:1024 sc1\ns_nop 15\n"
"s_nop 13\nvd30_p0:\ns_cbranch_execnz tl24_p0\ns_nop 0\n"
"s_nop 0\nva31_p0:\ns_branch vb31_p0\nsx31_p0:\n"
"s_add_u32 s60, s60, 9548\ns_addc_u32 s61, s61, 0\ns_setpc_b64 s[60:61]\nvb31_p0:\n"
"s_branch vr31_p0\ns_nop 1\ns_nop 1\ns_nop 1\n"
"s_nop 1\ns_nop 1\ns_nop 1\ns_nop 1\n"
"s_nop 1\ns_nop 1\ns_nop 1\ns_nop 1\n"
"s_nop 1\ns_nop 1\ns_nop 1\nvr31_p0:\n"
"s_nop 1\ns_nop 1\ns_nop 1\ns_nop 1\n"
"s_nop 1\ns_nop 1\ns_nop 1\ns_nop 1\n"
"s_cbranch_execz vc31x0_p0\nbuffer_load_dwordx4 v[116:119], v20, s[8:11], s41 offen offset:0 sc1\nbuffer_load_dwordx4 v[120:123], v20, s[8:11], s41 offen offset:1024 sc1\nv_add_f32 v219, v68, v69\n"
"v_add_f32 v220, v70, v71\nv_cvt_pk_bf16_f32 v156, v219, v220\ns_nop 0\nv_add_f32 v219, v72, v73\n"
"v_add_f32 v220, v74, v75\nv_cvt_pk_bf16_f32 v157, v219, v220\ns_nop 0\nvc31x0_p0:\n"
"s_cbranch_execz vc31x1_p0\nv_add_f32 v219, v76, v77\nv_add_f32 v220, v78, v79\nv_cvt_pk_bf16_f32 v158, v219, v220\n"
"s_nop 0\nv_add_f32 v219, v80, v81\nv_add_f32 v220, v82, v83\nv_cvt_pk_bf16_f32 v159, v219, v220\n"
"s_nop 0\ns_nop 1\ns_nop 1\ns_nop 1\n"
"s_nop 1\nvc31x1_p0:\ns_cbranch_execz vc31x2_p0\ns_nop 1\n"
"s_nop 1\ns_nop 1\ns_nop 1\ns_nop 1\n"
"s_nop 1\ns_nop 1\ns_nop 1\nvc31x2_p0:\n"
"s_cbranch_execz vd31_p0\nbuffer_load_dwordx4 v[124:127], v21, s[8:11], s41 offen offset:0 sc1\nbuffer_load_dwordx4 v[128:131], v21, s[8:11], s41 offen offset:1024 sc1\ns_nop 15\n"
"s_nop 13\nvd31_p0:\ns_cbranch_execnz tl24_p0\ns_nop 0\n"
"s_nop 0\nva32_p0:\ns_branch vb32_p0\nsx32_p0:\n"
"s_add_u32 s60, s60, 9856\ns_addc_u32 s61, s61, 0\ns_setpc_b64 s[60:61]\nvb32_p0:\n"
"s_branch vr32_p0\ns_nop 1\ns_nop 1\ns_nop 1\n"
"s_nop 1\ns_nop 1\ns_nop 1\ns_nop 1\n"
"s_nop 1\ns_nop 1\ns_nop 1\ns_nop 1\n"
"s_nop 1\ns_nop 1\ns_nop 1\nvr32_p0:\n"
"s_nop 1\ns_nop 1\ns_nop 1\ns_nop 1\n"
"s_nop 1\ns_nop 1\ns_nop 1\ns_nop 1\n"
"s_cbranch_execz vc32x0_p0\nbuffer_load_dwordx4 v[116:119], v20, s[8:11], s41 offen offset:0 sc1\nbuffer_load_dwordx4 v[120:123], v20, s[8:11], s41 offen offset:1024 sc1\nv_add_f32 v219, v68, v69\n"
"v_add_f32 v220, v70, v71\nv_cvt_pk_bf16_f32 v156, v219, v220\ns_nop 0\nv_add_f32 v219, v72, v73\n"
"v_add_f32 v220, v74, v75\nv_cvt_pk_bf16_f32 v157, v219, v220\ns_nop 0\nvc32x0_p0:\n"
"s_cbranch_execz vc32x1_p0\nv_add_f32 v219, v76, v77\nv_add_f32 v220, v78, v79\nv_cvt_pk_bf16_f32 v158, v219, v220\n"
"s_nop 0\nv_add_f32 v219, v80, v81\nv_add_f32 v220, v82, v83\nv_cvt_pk_bf16_f32 v159, v219, v220\n"
"s_nop 0\ns_nop 1\ns_nop 1\ns_nop 1\n"
"s_nop 1\nvc32x1_p0:\ns_cbranch_execz vc32x2_p0\ns_nop 1\n"
"s_nop 1\ns_nop 1\ns_nop 1\ns_nop 1\n"
"s_nop 1\ns_nop 1\ns_nop 1\nvc32x2_p0:\n"
"s_cbranch_execz vd32_p0\nbuffer_load_dwordx4 v[124:127], v21, s[8:11], s41 offen offset:0 sc1\nbuffer_load_dwordx4 v[128:131], v21, s[8:11], s41 offen offset:1024 sc1\ns_nop 15\n"
"s_nop 13\nvd32_p0:\ns_cbranch_execnz tl24_p0\ns_nop 0\n"
"s_nop 0\nva33_p0:\ns_branch vb33_p0\nsx33_p0:\n"
"s_add_u32 s60, s60, 10164\ns_addc_u32 s61, s61, 0\ns_setpc_b64 s[60:61]\nvb33_p0:\n"
"s_branch vr33_p0\ns_nop 1\ns_nop 1\ns_nop 1\n"
"s_nop 1\ns_nop 1\ns_nop 1\ns_nop 1\n"
"s_nop 1\ns_nop 1\ns_nop 1\ns_nop 1\n"
"s_nop 1\ns_nop 1\ns_nop 1\nvr33_p0:\n"
"s_nop 1\ns_nop 1\ns_nop 1\ns_nop 1\n"
"s_nop 1\ns_nop 1\ns_nop 1\ns_nop 1\n"
"s_cbranch_execz vc33x0_p0\nbuffer_load_dwordx4 v[116:119], v20, s[8:11], s41 offen offset:0 sc1\nbuffer_load_dwordx4 v[120:123], v20, s[8:11], s41 offen offset:1024 sc1\nv_add_f32 v219, v68, v69\n"
"v_add_f32 v220, v70, v71\nv_cvt_pk_bf16_f32 v156, v219, v220\ns_nop 0\nv_add_f32 v219, v72, v73\n"
"v_add_f32 v220, v74, v75\nv_cvt_pk_bf16_f32 v157, v219, v220\ns_nop 0\nvc33x0_p0:\n"
"s_cbranch_execz vc33x1_p0\nv_add_f32 v219, v76, v77\nv_add_f32 v220, v78, v79\nv_cvt_pk_bf16_f32 v158, v219, v220\n"
"s_nop 0\nv_add_f32 v219, v80, v81\nv_add_f32 v220, v82, v83\nv_cvt_pk_bf16_f32 v159, v219, v220\n"
"s_nop 0\ns_nop 1\ns_nop 1\ns_nop 1\n"
"s_nop 1\nvc33x1_p0:\ns_cbranch_execz vc33x2_p0\ns_nop 1\n"
"s_nop 1\ns_nop 1\ns_nop 1\ns_nop 1\n"
"s_nop 1\ns_nop 1\ns_nop 1\nvc33x2_p0:\n"
"s_cbranch_execz vd33_p0\nbuffer_load_dwordx4 v[124:127], v21, s[8:11], s41 offen offset:0 sc1\nbuffer_load_dwordx4 v[128:131], v21, s[8:11], s41 offen offset:1024 sc1\ns_nop 15\n"
"s_nop 13\nvd33_p0:\ns_cbranch_execnz tl24_p0\ns_nop 0\n"
"s_nop 0\nva34_p0:\ns_branch vb34_p0\nsx34_p0:\n"
"s_add_u32 s60, s60, 10472\ns_addc_u32 s61, s61, 0\ns_setpc_b64 s[60:61]\nvb34_p0:\n"
"s_branch vr34_p0\ns_nop 1\ns_nop 1\ns_nop 1\n"
"s_nop 1\ns_nop 1\ns_nop 1\ns_nop 1\n"
"s_nop 1\ns_nop 1\ns_nop 1\ns_nop 1\n"
"s_nop 1\ns_nop 1\ns_nop 1\nvr34_p0:\n"
"s_nop 1\ns_nop 1\ns_nop 1\ns_nop 1\n"
"s_nop 1\ns_nop 1\ns_nop 1\ns_nop 1\n"
"s_cbranch_execz vc34x0_p0\nbuffer_load_dwordx4 v[116:119], v20, s[8:11], s41 offen offset:0 sc1\nbuffer_load_dwordx4 v[120:123], v20, s[8:11], s41 offen offset:1024 sc1\nv_add_f32 v219, v68, v69\n"
"v_add_f32 v220, v70, v71\nv_cvt_pk_bf16_f32 v156, v219, v220\ns_nop 0\nv_add_f32 v219, v72, v73\n"
"v_add_f32 v220, v74, v75\nv_cvt_pk_bf16_f32 v157, v219, v220\ns_nop 0\nvc34x0_p0:\n"
"s_cbranch_execz vc34x1_p0\nv_add_f32 v219, v76, v77\nv_add_f32 v220, v78, v79\nv_cvt_pk_bf16_f32 v158, v219, v220\n"
"s_nop 0\nv_add_f32 v219, v80, v81\nv_add_f32 v220, v82, v83\nv_cvt_pk_bf16_f32 v159, v219, v220\n"
"s_nop 0\ns_nop 1\ns_nop 1\ns_nop 1\n"
"s_nop 1\nvc34x1_p0:\ns_cbranch_execz vc34x2_p0\ns_nop 1\n"
"s_nop 1\ns_nop 1\ns_nop 1\ns_nop 1\n"
"s_nop 1\ns_nop 1\ns_nop 1\nvc34x2_p0:\n"
"s_cbranch_execz vd34_p0\nbuffer_load_dwordx4 v[124:127], v21, s[8:11], s41 offen offset:0 sc1\nbuffer_load_dwordx4 v[128:131], v21, s[8:11], s41 offen offset:1024 sc1\ns_nop 15\n"
"s_nop 13\nvd34_p0:\ns_cbranch_execnz tl24_p0\ns_nop 0\n"
"s_nop 0\nva35_p0:\ns_branch vb35_p0\nsx35_p0:\n"
"s_add_u32 s60, s60, 10780\ns_addc_u32 s61, s61, 0\ns_setpc_b64 s[60:61]\nvb35_p0:\n"
"s_branch vr35_p0\ns_nop 1\ns_nop 1\ns_nop 1\n"
"s_nop 1\ns_nop 1\ns_nop 1\ns_nop 1\n"
"s_nop 1\ns_nop 1\ns_nop 1\ns_nop 1\n"
"s_nop 1\ns_nop 1\ns_nop 1\nvr35_p0:\n"
"s_nop 1\ns_nop 1\ns_nop 1\ns_nop 1\n"
"s_nop 1\ns_nop 1\ns_nop 1\ns_nop 1\n"
"s_cbranch_execz vc35x0_p0\nbuffer_load_dwordx4 v[116:119], v20, s[8:11], s41 offen offset:0 sc1\nbuffer_load_dwordx4 v[120:123], v20, s[8:11], s41 offen offset:1024 sc1\nv_add_f32 v219, v68, v69\n"
"v_add_f32 v220, v70, v71\nv_cvt_pk_bf16_f32 v156, v219, v220\ns_nop 0\nv_add_f32 v219, v72, v73\n"
"v_add_f32 v220, v74, v75\nv_cvt_pk_bf16_f32 v157, v219, v220\ns_nop 0\nvc35x0_p0:\n"
"s_cbranch_execz vc35x1_p0\nv_add_f32 v219, v76, v77\nv_add_f32 v220, v78, v79\nv_cvt_pk_bf16_f32 v158, v219, v220\n"
"s_nop 0\nv_add_f32 v219, v80, v81\nv_add_f32 v220, v82, v83\nv_cvt_pk_bf16_f32 v159, v219, v220\n"
"s_nop 0\ns_nop 1\ns_nop 1\ns_nop 1\n"
"s_nop 1\nvc35x1_p0:\ns_cbranch_execz vc35x2_p0\ns_nop 1\n"
"s_nop 1\ns_nop 1\ns_nop 1\ns_nop 1\n"
"s_nop 1\ns_nop 1\ns_nop 1\nvc35x2_p0:\n"
"s_cbranch_execz vd35_p0\nbuffer_load_dwordx4 v[124:127], v21, s[8:11], s41 offen offset:0 sc1\nbuffer_load_dwordx4 v[128:131], v21, s[8:11], s41 offen offset:1024 sc1\ns_nop 15\n"
"s_nop 13\nvd35_p0:\ns_cbranch_execnz tl24_p0\ns_nop 0\n"
"s_nop 0\nva36_p0:\nwarm_p0:\ns_setpc_b64 s[88:89]\n"
"s_nop 0\ns_nop 0\ns_nop 0\ns_nop 0\n"
"s_memrealtime s[54:55]\ns_waitcnt lgkmcnt(0)\ns_add_u32 s90, s54, 0x1000\ns_cselect_b32 s90, -1, s90\n"
"s_mov_b32 s56, s54\ns_mov_b32 s58, s54\ns_getpc_b64 s[60:61]\ndq_p0:\n"
"s_sub_u32 s60, s60, dq_p0-vb0_p0\ns_subb_u32 s61, s61, 0\ns_branch gr_p0\nend_p0:\n"
"s_branch fk_done\nfk_done:\ns_endpgm\n"
    :: "{s[8:23]}"(s0), "{s[24:39]}"(s1), "{v[8:23]}"(v0), "{v[24:39]}"(v1) : "v40", "v41", "v42", "v43", "v44", "v45", "v46", "v47", "v48", "v49", "v50", "v51", "v52", "v53", "v54", "v55", "v56", "v57", "v58", "v59", "v60", "v61", "v62", "v63", "v64", "v65", "v66", "v67", "v68", "v69", "v70", "v71", "v72", "v73", "v74", "v75", "v76", "v77", "v78", "v79", "v80", "v81", "v82", "v83", "v84", "v85", "v86", "v87", "v88", "v89", "v90", "v91", "v92", "v93", "v94", "v95", "v96", "v97", "v98", "v99", "v100", "v101", "v102", "v103", "v104", "v105", "v106", "v107", "v108", "v109", "v110", "v111", "v112", "v113", "v114", "v115", "v116", "v117", "v118", "v119", "v120", "v121", "v122", "v123", "v124", "v125", "v126", "v127", "v128", "v129", "v130", "v131", "v132", "v133", "v134", "v135", "v136", "v137", "v138", "v139", "v140", "v141", "v142", "v143", "v144", "v145", "v146", "v147", "v148", "v149", "v150", "v151", "v152", "v153", "v154", "v155", "v156", "v157", "v158", "v159", "v160", "v161", "v162", "v163", "v164", "v165", "v166", "v167", "v168", "v169", "v170", "v171", "v172", "v173", "v174", "v175", "v176", "v177", "v178", "v179", "v180", "v181", "v182", "v183", "v184", "v185", "v186", "v187", "v188", "v189", "v190", "v191", "v192", "v193", "v194", "v195", "v196", "v197", "v198", "v199", "v200", "v201", "v202", "v203", "v204", "v205", "v206", "v207", "v208", "v209", "v210", "v211", "v212", "v213", "v214", "v215", "v216", "v217", "v218", "v219", "v220", "v221", "v222", "v223", "v224", "v225", "v226", "v227", "v228", "v229", "v230", "v231", "v232", "v233", "v234", "v235", "v236", "v237", "v238", "v239", "v240", "v241", "v242", "v243", "v244", "v245", "v246", "v247", "v248", "v249", "v250", "v251", "v252", "v253", "v254", "v255", "a0", "a1", "a2", "a3", "a4", "a5", "a6", "a7", "a8", "a9", "a10", "a11", "a12", "a13", "a14", "a15", "a16", "a17", "a18", "a19", "a20", "a21", "a22", "a23", "a24", "a25", "a26", "a27", "a28", "a29", "a30", "a31", "a32", "a33", "a34", "a35", "a36", "a37", "a38", "a39", "a40", "a41", "a42", "a43", "a44", "a45", "a46", "a47", "a48", "a49", "a50", "a51", "a52", "a53", "a54", "a55", "a56", "a57", "a58", "a59", "a60", "a61", "a62", "a63", "a64", "a65", "a66", "a67", "a68", "a69", "a70", "a71", "a72", "a73", "a74", "a75", "a76", "a77", "a78", "a79", "a80", "a81", "a82", "a83", "a84", "a85", "a86", "a87", "a88", "a89", "a90", "a91", "a92", "a93", "a94", "a95", "a96", "a97", "a98", "a99", "a100", "a101", "a102", "a103", "a104", "a105", "a106", "a107", "a108", "a109", "a110", "a111", "a112", "a113", "a114", "a115", "a116", "a117", "a118", "a119", "a120", "a121", "a122", "a123", "a124", "a125", "a126", "a127", "a128", "a129", "a130", "a131", "a132", "a133", "a134", "a135", "a136", "a137", "a138", "a139", "a140", "a141", "a142", "a143", "a144", "a145", "a146", "a147", "a148", "a149", "a150", "a151", "a152", "a153", "a154", "a155", "a156", "a157", "a158", "a159", "a160", "a161", "a162", "a163", "a164", "a165", "a166", "a167", "a168", "a169", "a170", "a171", "a172", "a173", "a174", "a175", "a176", "a177", "a178", "a179", "a180", "a181", "a182", "a183", "a184", "a185", "a186", "a187", "a188", "a189", "a190", "a191", "a192", "a193", "a194", "a195", "a196", "a197", "a198", "a199", "a200", "a201", "a202", "a203", "a204", "a205", "a206", "a207", "a208", "a209", "a210", "a211", "a212", "a213", "a214", "a215", "a216", "a217", "a218", "a219", "a220", "a221", "a222", "a223", "a224", "a225", "a226", "a227", "a228", "a229", "a230", "a231", "a232", "a233", "a234", "a235", "a236", "a237", "a238", "a239", "a240", "a241", "a242", "a243", "a244", "a245", "a246", "a247", "a248", "a249", "a250", "a251", "a252", "a253", "a254", "a255", "s40", "s41", "s42", "s43", "s44", "s45", "s46", "s47", "s48", "s49", "s50", "s51", "s52", "s53", "s54", "s55", "s56", "s57", "s58", "s59", "s60", "s61", "s62", "s63", "s64", "s65", "s66", "s67", "s68", "s69", "s70", "s71", "s72", "s73", "s74", "s75", "s76", "s77", "s78", "s79", "s80", "s81", "s82", "s83", "s84", "s85", "s86", "s87", "s88", "s89", "s90", "s91", "s92", "s93", "s94", "s95", "vcc", "scc", "memory");
    __builtin_unreachable();
}
struct GCtx { u4 rw1, rx, rexp, rout, rhb; unsigned tag, rot, sw1, mw, mx, sfg, stay, csc, ostr, kb, p1, p2; unsigned vr0, vr1, vx, wa, xa, vexp, vimp, vo0, vo1, vo2, vo3, vfl, vpf, vpo, vk, p5; };
DI unsigned gemm_asm0(const GCtx& c, u8v& w) {
    const u16v s0 = {RFL(c.rw1[0]), RFL(c.rw1[1]), RFL(c.rw1[2]), RFL(c.rw1[3]), RFL(c.rx[0]), RFL(c.rx[1]), RFL(c.rx[2]), RFL(c.rx[3]), RFL(c.rexp[0]), RFL(c.rexp[1]), RFL(c.rexp[2]), RFL(c.rexp[3]), RFL(c.rout[0]), RFL(c.rout[1]), RFL(c.rout[2]), RFL(c.rout[3])}, s1 = {RFL(c.rhb[0]), RFL(c.rhb[1]), RFL(c.rhb[2]), RFL(c.rhb[3]), RFL(c.tag), RFL(c.rot), RFL(c.sw1), RFL(c.mw), RFL(c.mx), RFL(c.sfg), RFL(c.stay), RFL(c.csc), RFL(c.ostr), RFL(c.kb), RFL(c.p1), RFL(c.p2)}, v0 = {c.vr0, c.vr1, c.vx, c.wa, c.xa, c.vexp, c.vimp, c.vo0, c.vo1, c.vo2, c.vo3, c.vfl, c.vpf, c.vpo, c.vk, c.p5};
    unsigned o = 0;
    asm volatile(
"s_nop 4\ns_add_u32 s40, s29, 0\ns_add_u32 s41, s29, 1\ns_add_u32 s42, s29, 2\n"
"s_add_u32 s43, s29, 3\nv_xor_b32 v24, 0, v11\nv_xor_b32 v25, 64, v11\nv_xor_b32 v26, 128, v11\n"
"v_xor_b32 v27, 192, v11\nv_xor_b32 v28, 2064, v11\nv_xor_b32 v29, 2128, v11\nv_xor_b32 v30, 2192, v11\n"
"v_xor_b32 v31, 2256, v11\nv_add_u32 v40, s37, v24\nv_add_u32 v41, s37, v25\nv_add_u32 v42, s37, v26\n"
"v_add_u32 v43, s37, v27\nv_add_u32 v44, s37, v28\nv_add_u32 v45, s37, v29\nv_add_u32 v46, s37, v30\n"
"v_add_u32 v47, s37, v31\nds_read_b128 a[128:131], v40 offset:0\nds_read_b128 a[132:135], v41 offset:0\nds_read_b128 a[136:139], v42 offset:0\n"
"ds_read_b128 a[140:143], v43 offset:0\ns_lshl_b32 s53, s41, 19\ns_add_u32 s53, s53, s30\ns_add_u32 m0, s31, 65536\n"
"s_add_u32 s53, s53, 0\nbuffer_load_dwordx4 v8, s[8:11], s53 offen lds\nbuffer_load_dwordx4 v9, s[8:11], s53 offen offset:1024 lds\nds_read_b128 a[144:147], v40 offset:256\n"
"ds_read_b128 a[148:151], v41 offset:256\nds_read_b128 a[152:155], v42 offset:256\nds_read_b128 a[156:159], v43 offset:256\ns_lshl_b32 s53, s41, 19\n"
"s_add_u32 s53, s53, s30\ns_add_u32 m0, s31, 73728\ns_add_u32 s53, s53, 131072\nbuffer_load_dwordx4 v8, s[8:11], s53 offen lds\n"
"buffer_load_dwordx4 v9, s[8:11], s53 offen offset:1024 lds\ns_waitcnt lgkmcnt(0)\nds_read_b128 a[160:163], v44 offset:0\nds_read_b128 a[164:167], v45 offset:0\n"
"ds_read_b128 a[168:171], v46 offset:0\nds_read_b128 a[172:175], v47 offset:0\ns_lshl_b32 s53, s41, 19\ns_add_u32 s53, s53, s30\n"
"s_add_u32 m0, s31, 81920\ns_add_u32 s53, s53, 262144\nbuffer_load_dwordx4 v8, s[8:11], s53 offen lds\nbuffer_load_dwordx4 v9, s[8:11], s53 offen offset:1024 lds\n"
"ds_read_b128 a[176:179], v44 offset:256\nds_read_b128 a[180:183], v45 offset:256\nds_read_b128 a[184:187], v46 offset:256\nds_read_b128 a[188:191], v47 offset:256\n"
"s_lshl_b32 s53, s41, 19\ns_add_u32 s53, s53, s30\ns_add_u32 m0, s31, 90112\ns_add_u32 s53, s53, 393216\n"
"buffer_load_dwordx4 v8, s[8:11], s53 offen lds\nbuffer_load_dwordx4 v9, s[8:11], s53 offen offset:1024 lds\ns_waitcnt lgkmcnt(0)\nv_add_u32 v40, 65536, v24\n"
"v_add_u32 v41, 65536, v25\nv_add_u32 v42, 65536, v26\nv_add_u32 v43, 65536, v27\nv_add_u32 v44, 65536, v28\n"
"v_add_u32 v45, 65536, v29\nv_add_u32 v46, 65536, v30\nv_add_u32 v47, 65536, v31\nv_xor_b32 v32, 0, v12\n"
"v_xor_b32 v33, 2048, v12\nv_xor_b32 v34, 64, v12\nv_xor_b32 v35, 2112, v12\ns_mov_b32 s55, 0\n"
"s_waitcnt vmcnt(11)\ns_barrier\nds_read_b128 v[96:99], v24 offset:0\nds_read_b128 v[100:103], v28 offset:0\n"
"ds_read_b128 v[104:107], v25 offset:0\nds_read_b128 v[108:111], v29 offset:0\nds_read_b128 v[112:115], v26 offset:0\nds_read_b128 v[116:119], v30 offset:0\n"
"ds_read_b128 v[120:123], v27 offset:0\nds_read_b128 v[124:127], v31 offset:0\ns_waitcnt lgkmcnt(6)\nv_mfma_f32_16x16x32_bf16 v[64:67], v[96:99], a[128:131], 0\n"
"s_lshl_b32 s53, s42, 19\ns_add_u32 s53, s53, s30\nv_mfma_f32_16x16x32_bf16 v[68:71], v[96:99], a[160:163], 0\nds_read_b128 v[96:99], v24 offset:256\n"
"s_add_u32 m0, s31, 98304\ns_add_u32 s53, s53, 0\nv_mfma_f32_16x16x32_bf16 v[72:75], v[100:103], a[128:131], 0\nbuffer_load_dwordx4 v8, s[8:11], s53 offen lds\n"
"v_mfma_f32_16x16x32_bf16 v[76:79], v[100:103], a[160:163], 0\nds_read_b128 v[100:103], v28 offset:256\nbuffer_load_dwordx4 v9, s[8:11], s53 offen offset:1024 lds\ns_waitcnt lgkmcnt(6)\n"
"v_mfma_f32_16x16x32_bf16 v[64:67], v[104:107], a[132:135], v[64:67]\ns_lshl_b32 s53, s42, 19\ns_add_u32 s53, s53, s30\nv_mfma_f32_16x16x32_bf16 v[68:71], v[104:107], a[164:167], v[68:71]\n"
"ds_read_b128 v[104:107], v25 offset:256\ns_add_u32 m0, s31, 106496\ns_add_u32 s53, s53, 131072\nv_mfma_f32_16x16x32_bf16 v[72:75], v[108:111], a[132:135], v[72:75]\n"
"buffer_load_dwordx4 v8, s[8:11], s53 offen lds\nv_mfma_f32_16x16x32_bf16 v[76:79], v[108:111], a[164:167], v[76:79]\nds_read_b128 v[108:111], v29 offset:256\nbuffer_load_dwordx4 v9, s[8:11], s53 offen offset:1024 lds\n"
"s_waitcnt lgkmcnt(6)\nv_mfma_f32_16x16x32_bf16 v[64:67], v[112:115], a[136:139], v[64:67]\nv_mfma_f32_16x16x32_bf16 v[68:71], v[112:115], a[168:171], v[68:71]\nds_read_b128 v[112:115], v26 offset:256\n"
"v_mfma_f32_16x16x32_bf16 v[72:75], v[116:119], a[136:139], v[72:75]\nv_mfma_f32_16x16x32_bf16 v[76:79], v[116:119], a[168:171], v[76:79]\nds_read_b128 v[116:119], v30 offset:256\ns_waitcnt lgkmcnt(6)\n"
"v_mfma_f32_16x16x32_bf16 v[64:67], v[120:123], a[140:143], v[64:67]\nv_mfma_f32_16x16x32_bf16 v[68:71], v[120:123], a[172:175], v[68:71]\nds_read_b128 v[120:123], v27 offset:256\nv_mfma_f32_16x16x32_bf16 v[72:75], v[124:127], a[140:143], v[72:75]\n"
"v_mfma_f32_16x16x32_bf16 v[76:79], v[124:127], a[172:175], v[76:79]\nds_read_b128 v[124:127], v31 offset:256\ns_waitcnt lgkmcnt(6)\nv_mfma_f32_16x16x32_bf16 v[64:67], v[96:99], a[144:147], v[64:67]\n"
"v_mfma_f32_16x16x32_bf16 v[68:71], v[96:99], a[176:179], v[68:71]\nds_read_b128 v[96:99], v24 offset:16384\nv_mfma_f32_16x16x32_bf16 v[72:75], v[100:103], a[144:147], v[72:75]\nv_mfma_f32_16x16x32_bf16 v[76:79], v[100:103], a[176:179], v[76:79]\n"
"ds_read_b128 v[100:103], v28 offset:16384\ns_waitcnt lgkmcnt(6)\nv_mfma_f32_16x16x32_bf16 v[64:67], v[104:107], a[148:151], v[64:67]\nv_mfma_f32_16x16x32_bf16 v[68:71], v[104:107], a[180:183], v[68:71]\n"
"ds_read_b128 v[104:107], v25 offset:16384\nv_mfma_f32_16x16x32_bf16 v[72:75], v[108:111], a[148:151], v[72:75]\nv_mfma_f32_16x16x32_bf16 v[76:79], v[108:111], a[180:183], v[76:79]\nds_read_b128 v[108:111], v29 offset:16384\n"
"s_waitcnt lgkmcnt(6)\nv_mfma_f32_16x16x32_bf16 v[64:67], v[112:115], a[152:155], v[64:67]\nv_mfma_f32_16x16x32_bf16 v[68:71], v[112:115], a[184:187], v[68:71]\nds_read_b128 v[112:115], v26 offset:16384\n"
"v_mfma_f32_16x16x32_bf16 v[72:75], v[116:119], a[152:155], v[72:75]\nv_mfma_f32_16x16x32_bf16 v[76:79], v[116:119], a[184:187], v[76:79]\nds_read_b128 v[116:119], v30 offset:16384\ns_waitcnt lgkmcnt(6)\n"
"v_mfma_f32_16x16x32_bf16 v[64:67], v[120:123], a[156:159], v[64:67]\nv_mfma_f32_16x16x32_bf16 v[68:71], v[120:123], a[188:191], v[68:71]\nds_read_b128 v[120:123], v27 offset:16384\nv_mfma_f32_16x16x32_bf16 v[72:75], v[124:127], a[156:159], v[72:75]\n"
"v_mfma_f32_16x16x32_bf16 v[76:79], v[124:127], a[188:191], v[76:79]\nds_read_b128 v[124:127], v31 offset:16384\ns_waitcnt lgkmcnt(6)\nv_mfma_f32_16x16x32_bf16 v[80:83], v[96:99], a[128:131], 0\n"
"s_lshl_b32 s53, s42, 19\ns_add_u32 s53, s53, s30\nv_mfma_f32_16x16x32_bf16 v[84:87], v[96:99], a[160:163], 0\nds_read_b128 v[96:99], v24 offset:16640\n"
"s_add_u32 m0, s31, 114688\ns_add_u32 s53, s53, 262144\nv_mfma_f32_16x16x32_bf16 v[88:91], v[100:103], a[128:131], 0\nbuffer_load_dwordx4 v8, s[8:11], s53 offen lds\n"
"v_mfma_f32_16x16x32_bf16 v[92:95], v[100:103], a[160:163], 0\nds_read_b128 v[100:103], v28 offset:16640\nbuffer_load_dwordx4 v9, s[8:11], s53 offen offset:1024 lds\ns_waitcnt lgkmcnt(6)\n"
"v_mfma_f32_16x16x32_bf16 v[80:83], v[104:107], a[132:135], v[80:83]\nv_mul_f32 v64, s35, v64\nv_mul_f32 v65, s35, v65\nv_mfma_f32_16x16x32_bf16 v[84:87], v[104:107], a[164:167], v[84:87]\n"
"ds_read_b128 v[104:107], v25 offset:16640\nv_mul_f32 v66, s35, v66\nv_mul_f32 v67, s35, v67\nv_mfma_f32_16x16x32_bf16 v[88:91], v[108:111], a[132:135], v[88:91]\n"
"v_cvt_pk_bf16_f32 v128, v64, v65\nv_cvt_pk_bf16_f32 v129, v66, v67\nv_mfma_f32_16x16x32_bf16 v[92:95], v[108:111], a[164:167], v[92:95]\nds_read_b128 v[108:111], v29 offset:16640\n"
"v_mul_f32 v72, s35, v72\nv_mul_f32 v73, s35, v73\ns_waitcnt lgkmcnt(6)\nv_mfma_f32_16x16x32_bf16 v[80:83], v[112:115], a[136:139], v[80:83]\n"
"v_mul_f32 v74, s35, v74\nv_mul_f32 v75, s35, v75\nv_mfma_f32_16x16x32_bf16 v[84:87], v[112:115], a[168:171], v[84:87]\nds_read_b128 v[112:115], v26 offset:16640\n"
"v_cvt_pk_bf16_f32 v130, v72, v73\nv_cvt_pk_bf16_f32 v131, v74, v75\nv_mfma_f32_16x16x32_bf16 v[88:91], v[116:119], a[136:139], v[88:91]\nv_mul_f32 v68, s35, v68\n"
"v_mul_f32 v69, s35, v69\nv_mfma_f32_16x16x32_bf16 v[92:95], v[116:119], a[168:171], v[92:95]\nds_read_b128 v[116:119], v30 offset:16640\nv_mul_f32 v70, s35, v70\n"
"v_mul_f32 v71, s35, v71\ns_waitcnt lgkmcnt(6)\nv_mfma_f32_16x16x32_bf16 v[80:83], v[120:123], a[140:143], v[80:83]\nv_cvt_pk_bf16_f32 v132, v68, v69\n"
"v_cvt_pk_bf16_f32 v133, v70, v71\nv_mfma_f32_16x16x32_bf16 v[84:87], v[120:123], a[172:175], v[84:87]\nds_read_b128 v[120:123], v27 offset:16640\nv_mul_f32 v76, s35, v76\n"
"v_mul_f32 v77, s35, v77\nv_mfma_f32_16x16x32_bf16 v[88:91], v[124:127], a[140:143], v[88:91]\nv_mul_f32 v78, s35, v78\nv_mul_f32 v79, s35, v79\n"
"v_mfma_f32_16x16x32_bf16 v[92:95], v[124:127], a[172:175], v[92:95]\nds_read_b128 v[124:127], v31 offset:16640\nv_cvt_pk_bf16_f32 v134, v76, v77\nv_cvt_pk_bf16_f32 v135, v78, v79\n"
"s_waitcnt vmcnt(6)\ns_barrier\ns_waitcnt lgkmcnt(6)\nv_mfma_f32_16x16x32_bf16 v[80:83], v[96:99], a[144:147], v[80:83]\n"
"s_lshl_b32 s54, s40, 11\ns_add_u32 s54, s54, 0\nv_mfma_f32_16x16x32_bf16 v[84:87], v[96:99], a[176:179], v[84:87]\nds_read_b128 v[96:99], v24 offset:32768\n"
"buffer_store_dwordx4 v[128:131], v13, s[16:19], s54 offen sc1\nv_mfma_f32_16x16x32_bf16 v[88:91], v[100:103], a[144:147], v[88:91]\ns_add_u32 s54, s54, 14336\nv_mfma_f32_16x16x32_bf16 v[92:95], v[100:103], a[176:179], v[92:95]\n"
"ds_read_b128 v[100:103], v28 offset:32768\nbuffer_store_dwordx4 v[132:135], v13, s[16:19], s54 offen sc1\ns_waitcnt lgkmcnt(6)\nv_mfma_f32_16x16x32_bf16 v[80:83], v[104:107], a[148:151], v[80:83]\n"
"s_lshl_b32 s53, s42, 19\ns_add_u32 s53, s53, s30\nv_mfma_f32_16x16x32_bf16 v[84:87], v[104:107], a[180:183], v[84:87]\nds_read_b128 v[104:107], v25 offset:32768\n"
"s_add_u32 m0, s31, 122880\ns_add_u32 s53, s53, 393216\nv_mfma_f32_16x16x32_bf16 v[88:91], v[108:111], a[148:151], v[88:91]\nbuffer_load_dwordx4 v8, s[8:11], s53 offen lds\n"
"v_mfma_f32_16x16x32_bf16 v[92:95], v[108:111], a[180:183], v[92:95]\nds_read_b128 v[108:111], v29 offset:32768\nbuffer_load_dwordx4 v9, s[8:11], s53 offen offset:1024 lds\ns_waitcnt lgkmcnt(6)\n"
"v_mfma_f32_16x16x32_bf16 v[80:83], v[112:115], a[152:155], v[80:83]\nv_mfma_f32_16x16x32_bf16 v[84:87], v[112:115], a[184:187], v[84:87]\nds_read_b128 v[112:115], v26 offset:32768\nv_mfma_f32_16x16x32_bf16 v[88:91], v[116:119], a[152:155], v[88:91]\n"
"v_mfma_f32_16x16x32_bf16 v[92:95], v[116:119], a[184:187], v[92:95]\nds_read_b128 v[116:119], v30 offset:32768\ns_waitcnt lgkmcnt(6)\nv_mfma_f32_16x16x32_bf16 v[80:83], v[120:123], a[156:159], v[80:83]\n"
"v_mfma_f32_16x16x32_bf16 v[84:87], v[120:123], a[188:191], v[84:87]\nds_read_b128 v[120:123], v27 offset:32768\nv_mfma_f32_16x16x32_bf16 v[88:91], v[124:127], a[156:159], v[88:91]\nv_mfma_f32_16x16x32_bf16 v[92:95], v[124:127], a[188:191], v[92:95]\n"
"ds_read_b128 v[124:127], v31 offset:32768\ns_waitcnt lgkmcnt(6)\nv_mfma_f32_16x16x32_bf16 v[64:67], v[96:99], a[128:131], 0\ns_lshl_b32 s53, s43, 19\n"
"s_add_u32 s53, s53, s30\nv_mfma_f32_16x16x32_bf16 v[68:71], v[96:99], a[160:163], 0\nds_read_b128 v[96:99], v24 offset:33024\ns_add_u32 m0, s31, 131072\n"
"s_add_u32 s53, s53, 0\nv_mfma_f32_16x16x32_bf16 v[72:75], v[100:103], a[128:131], 0\nbuffer_load_dwordx4 v8, s[8:11], s53 offen lds\nv_mfma_f32_16x16x32_bf16 v[76:79], v[100:103], a[160:163], 0\n"
"ds_read_b128 v[100:103], v28 offset:33024\nbuffer_load_dwordx4 v9, s[8:11], s53 offen offset:1024 lds\ns_waitcnt lgkmcnt(6)\nv_mfma_f32_16x16x32_bf16 v[64:67], v[104:107], a[132:135], v[64:67]\n"
"v_mul_f32 v80, s35, v80\nv_mul_f32 v81, s35, v81\nv_mfma_f32_16x16x32_bf16 v[68:71], v[104:107], a[164:167], v[68:71]\nds_read_b128 v[104:107], v25 offset:33024\n"
"v_mul_f32 v82, s35, v82\nv_mul_f32 v83, s35, v83\nv_mfma_f32_16x16x32_bf16 v[72:75], v[108:111], a[132:135], v[72:75]\nv_cvt_pk_bf16_f32 v136, v80, v81\n"
"v_cvt_pk_bf16_f32 v137, v82, v83\nv_mfma_f32_16x16x32_bf16 v[76:79], v[108:111], a[164:167], v[76:79]\nds_read_b128 v[108:111], v29 offset:33024\nv_mul_f32 v88, s35, v88\n"
"v_mul_f32 v89, s35, v89\ns_waitcnt lgkmcnt(6)\nv_mfma_f32_16x16x32_bf16 v[64:67], v[112:115], a[136:139], v[64:67]\nv_mul_f32 v90, s35, v90\n"
"v_mul_f32 v91, s35, v91\nv_mfma_f32_16x16x32_bf16 v[68:71], v[112:115], a[168:171], v[68:71]\nds_read_b128 v[112:115], v26 offset:33024\nv_cvt_pk_bf16_f32 v138, v88, v89\n"
"v_cvt_pk_bf16_f32 v139, v90, v91\nv_mfma_f32_16x16x32_bf16 v[72:75], v[116:119], a[136:139], v[72:75]\nv_mul_f32 v84, s35, v84\nv_mul_f32 v85, s35, v85\n"
"v_mfma_f32_16x16x32_bf16 v[76:79], v[116:119], a[168:171], v[76:79]\nds_read_b128 v[116:119], v30 offset:33024\nv_mul_f32 v86, s35, v86\nv_mul_f32 v87, s35, v87\n"
"s_waitcnt lgkmcnt(6)\nv_mfma_f32_16x16x32_bf16 v[64:67], v[120:123], a[140:143], v[64:67]\nv_cvt_pk_bf16_f32 v140, v84, v85\nv_cvt_pk_bf16_f32 v141, v86, v87\n"
"v_mfma_f32_16x16x32_bf16 v[68:71], v[120:123], a[172:175], v[68:71]\nds_read_b128 v[120:123], v27 offset:33024\nv_mul_f32 v92, s35, v92\nv_mul_f32 v93, s35, v93\n"
"v_mfma_f32_16x16x32_bf16 v[72:75], v[124:127], a[140:143], v[72:75]\nv_mul_f32 v94, s35, v94\nv_mul_f32 v95, s35, v95\nv_mfma_f32_16x16x32_bf16 v[76:79], v[124:127], a[172:175], v[76:79]\n"
"ds_read_b128 v[124:127], v31 offset:33024\nv_cvt_pk_bf16_f32 v142, v92, v93\nv_cvt_pk_bf16_f32 v143, v94, v95\ns_waitcnt lgkmcnt(6)\n"
"v_mfma_f32_16x16x32_bf16 v[64:67], v[96:99], a[144:147], v[64:67]\ns_lshl_b32 s54, s40, 11\ns_add_u32 s54, s54, 1024\nv_mfma_f32_16x16x32_bf16 v[68:71], v[96:99], a[176:179], v[68:71]\n"
"ds_read_b128 v[96:99], v24 offset:49152\nbuffer_store_dwordx4 v[136:139], v13, s[16:19], s54 offen sc1\nv_mfma_f32_16x16x32_bf16 v[72:75], v[100:103], a[144:147], v[72:75]\ns_add_u32 s54, s54, 14336\n"
"v_mfma_f32_16x16x32_bf16 v[76:79], v[100:103], a[176:179], v[76:79]\nds_read_b128 v[100:103], v28 offset:49152\nbuffer_store_dwordx4 v[140:143], v13, s[16:19], s54 offen sc1\ns_waitcnt lgkmcnt(6)\n"
"v_mfma_f32_16x16x32_bf16 v[64:67], v[104:107], a[148:151], v[64:67]\ns_lshl_b32 s53, s43, 19\ns_add_u32 s53, s53, s30\nv_mfma_f32_16x16x32_bf16 v[68:71], v[104:107], a[180:183], v[68:71]\n"
"ds_read_b128 v[104:107], v25 offset:49152\ns_add_u32 m0, s31, 139264\ns_add_u32 s53, s53, 131072\nv_mfma_f32_16x16x32_bf16 v[72:75], v[108:111], a[148:151], v[72:75]\n"
"buffer_load_dwordx4 v8, s[8:11], s53 offen lds\nv_mfma_f32_16x16x32_bf16 v[76:79], v[108:111], a[180:183], v[76:79]\nds_read_b128 v[108:111], v29 offset:49152\nbuffer_load_dwordx4 v9, s[8:11], s53 offen offset:1024 lds\n"
"s_waitcnt lgkmcnt(6)\nv_mfma_f32_16x16x32_bf16 v[64:67], v[112:115], a[152:155], v[64:67]\nv_mfma_f32_16x16x32_bf16 v[68:71], v[112:115], a[184:187], v[68:71]\nds_read_b128 v[112:115], v26 offset:49152\n"
"v_mfma_f32_16x16x32_bf16 v[72:75], v[116:119], a[152:155], v[72:75]\nv_mfma_f32_16x16x32_bf16 v[76:79], v[116:119], a[184:187], v[76:79]\nds_read_b128 v[116:119], v30 offset:49152\ns_waitcnt lgkmcnt(6)\n"
"v_mfma_f32_16x16x32_bf16 v[64:67], v[120:123], a[156:159], v[64:67]\nv_mfma_f32_16x16x32_bf16 v[68:71], v[120:123], a[188:191], v[68:71]\nds_read_b128 v[120:123], v27 offset:49152\nv_mfma_f32_16x16x32_bf16 v[72:75], v[124:127], a[156:159], v[72:75]\n"
"v_mfma_f32_16x16x32_bf16 v[76:79], v[124:127], a[188:191], v[76:79]\nds_read_b128 v[124:127], v31 offset:49152\ns_waitcnt lgkmcnt(6)\nv_mfma_f32_16x16x32_bf16 v[80:83], v[96:99], a[128:131], 0\n"
"s_lshl_b32 s53, s43, 19\ns_add_u32 s53, s53, s30\nv_mfma_f32_16x16x32_bf16 v[84:87], v[96:99], a[160:163], 0\nds_read_b128 v[96:99], v24 offset:49408\n"
"s_add_u32 m0, s31, 147456\ns_add_u32 s53, s53, 262144\nv_mfma_f32_16x16x32_bf16 v[88:91], v[100:103], a[128:131], 0\nbuffer_load_dwordx4 v8, s[8:11], s53 offen lds\n"
"v_mfma_f32_16x16x32_bf16 v[92:95], v[100:103], a[160:163], 0\nds_read_b128 v[100:103], v28 offset:49408\nbuffer_load_dwordx4 v9, s[8:11], s53 offen offset:1024 lds\ns_waitcnt lgkmcnt(6)\n"
"v_mfma_f32_16x16x32_bf16 v[80:83], v[104:107], a[132:135], v[80:83]\nv_mul_f32 v64, s35, v64\nv_mul_f32 v65, s35, v65\nv_mfma_f32_16x16x32_bf16 v[84:87], v[104:107], a[164:167], v[84:87]\n"
"ds_read_b128 v[104:107], v25 offset:49408\nv_mul_f32 v66, s35, v66\nv_mul_f32 v67, s35, v67\nv_mfma_f32_16x16x32_bf16 v[88:91], v[108:111], a[132:135], v[88:91]\n"
"v_cvt_pk_bf16_f32 v144, v64, v65\nv_cvt_pk_bf16_f32 v145, v66, v67\nv_mfma_f32_16x16x32_bf16 v[92:95], v[108:111], a[164:167], v[92:95]\nds_read_b128 v[108:111], v29 offset:49408\n"
"v_mul_f32 v72, s35, v72\nv_mul_f32 v73, s35, v73\ns_waitcnt lgkmcnt(6)\nv_mfma_f32_16x16x32_bf16 v[80:83], v[112:115], a[136:139], v[80:83]\n"
"v_mul_f32 v74, s35, v74\nv_mul_f32 v75, s35, v75\nv_mfma_f32_16x16x32_bf16 v[84:87], v[112:115], a[168:171], v[84:87]\nds_read_b128 v[112:115], v26 offset:49408\n"
"v_cvt_pk_bf16_f32 v146, v72, v73\nv_cvt_pk_bf16_f32 v147, v74, v75\nv_mfma_f32_16x16x32_bf16 v[88:91], v[116:119], a[136:139], v[88:91]\nv_mul_f32 v68, s35, v68\n"
"v_mul_f32 v69, s35, v69\nv_mfma_f32_16x16x32_bf16 v[92:95], v[116:119], a[168:171], v[92:95]\nds_read_b128 v[116:119], v30 offset:49408\nv_mul_f32 v70, s35, v70\n"
"v_mul_f32 v71, s35, v71\ns_waitcnt lgkmcnt(6)\nv_mfma_f32_16x16x32_bf16 v[80:83], v[120:123], a[140:143], v[80:83]\nv_cvt_pk_bf16_f32 v148, v68, v69\n"
"v_cvt_pk_bf16_f32 v149, v70, v71\nv_mfma_f32_16x16x32_bf16 v[84:87], v[120:123], a[172:175], v[84:87]\nds_read_b128 v[120:123], v27 offset:49408\nv_mul_f32 v76, s35, v76\n"
"v_mul_f32 v77, s35, v77\nv_mfma_f32_16x16x32_bf16 v[88:91], v[124:127], a[140:143], v[88:91]\nv_mul_f32 v78, s35, v78\nv_mul_f32 v79, s35, v79\n"
"v_mfma_f32_16x16x32_bf16 v[92:95], v[124:127], a[172:175], v[92:95]\nds_read_b128 v[124:127], v31 offset:49408\nv_cvt_pk_bf16_f32 v150, v76, v77\nv_cvt_pk_bf16_f32 v151, v78, v79\n"
"s_waitcnt vmcnt(8)\ns_barrier\ns_waitcnt lgkmcnt(6)\nv_mfma_f32_16x16x32_bf16 v[80:83], v[96:99], a[144:147], v[80:83]\n"
"s_lshl_b32 s54, s41, 11\ns_add_u32 s54, s54, 0\nv_mfma_f32_16x16x32_bf16 v[84:87], v[96:99], a[176:179], v[84:87]\nds_read_b128 v[96:99], v40 offset:0\n"
"buffer_store_dwordx4 v[144:147], v13, s[16:19], s54 offen sc1\nv_mfma_f32_16x16x32_bf16 v[88:91], v[100:103], a[144:147], v[88:91]\ns_add_u32 s54, s54, 14336\nv_mfma_f32_16x16x32_bf16 v[92:95], v[100:103], a[176:179], v[92:95]\n"
"ds_read_b128 v[100:103], v44 offset:0\nbuffer_store_dwordx4 v[148:151], v13, s[16:19], s54 offen sc1\ns_waitcnt lgkmcnt(6)\nv_mfma_f32_16x16x32_bf16 v[80:83], v[104:107], a[148:151], v[80:83]\n"
"s_lshl_b32 s53, s43, 19\ns_add_u32 s53, s53, s30\nv_mfma_f32_16x16x32_bf16 v[84:87], v[104:107], a[180:183], v[84:87]\nds_read_b128 v[104:107], v41 offset:0\n"
"s_add_u32 m0, s31, 155648\ns_add_u32 s53, s53, 393216\nv_mfma_f32_16x16x32_bf16 v[88:91], v[108:111], a[148:151], v[88:91]\nbuffer_load_dwordx4 v8, s[8:11], s53 offen lds\n"
"v_mfma_f32_16x16x32_bf16 v[92:95], v[108:111], a[180:183], v[92:95]\nds_read_b128 v[108:111], v45 offset:0\nbuffer_load_dwordx4 v9, s[8:11], s53 offen offset:1024 lds\ns_waitcnt lgkmcnt(6)\n"
"v_mfma_f32_16x16x32_bf16 v[80:83], v[112:115], a[152:155], v[80:83]\nv_mfma_f32_16x16x32_bf16 v[84:87], v[112:115], a[184:187], v[84:87]\nds_read_b128 v[112:115], v42 offset:0\nv_mfma_f32_16x16x32_bf16 v[88:91], v[116:119], a[152:155], v[88:91]\n"
"v_mfma_f32_16x16x32_bf16 v[92:95], v[116:119], a[184:187], v[92:95]\nds_read_b128 v[116:119], v46 offset:0\ns_waitcnt lgkmcnt(6)\nv_mfma_f32_16x16x32_bf16 v[80:83], v[120:123], a[156:159], v[80:83]\n"
"v_mfma_f32_16x16x32_bf16 v[84:87], v[120:123], a[188:191], v[84:87]\nds_read_b128 v[120:123], v43 offset:0\nv_mfma_f32_16x16x32_bf16 v[88:91], v[124:127], a[156:159], v[88:91]\nv_mfma_f32_16x16x32_bf16 v[92:95], v[124:127], a[188:191], v[92:95]\n"
"ds_read_b128 v[124:127], v47 offset:0\ns_waitcnt lgkmcnt(6)\nv_mfma_f32_16x16x32_bf16 v[64:67], v[96:99], a[128:131], 0\ns_add_u32 m0, s32, 0\n"
"v_mfma_f32_16x16x32_bf16 v[68:71], v[96:99], a[160:163], 0\nds_read_b128 v[96:99], v40 offset:256\ns_mov_b32 s53, 0\nv_mfma_f32_16x16x32_bf16 v[72:75], v[100:103], a[128:131], 0\n"
"buffer_load_dwordx4 v10, s[12:15], s53 offen lds\nv_mfma_f32_16x16x32_bf16 v[76:79], v[100:103], a[160:163], 0\nds_read_b128 v[100:103], v44 offset:256\nv_mul_f32 v80, s35, v80\n"
"v_mul_f32 v81, s35, v81\ns_waitcnt lgkmcnt(6)\nv_mfma_f32_16x16x32_bf16 v[64:67], v[104:107], a[132:135], v[64:67]\nv_mul_f32 v82, s35, v82\n"
"v_mul_f32 v83, s35, v83\nv_mfma_f32_16x16x32_bf16 v[68:71], v[104:107], a[164:167], v[68:71]\nds_read_b128 v[104:107], v41 offset:256\nv_cvt_pk_bf16_f32 v152, v80, v81\n"
"v_cvt_pk_bf16_f32 v153, v82, v83\nv_mfma_f32_16x16x32_bf16 v[72:75], v[108:111], a[132:135], v[72:75]\nv_mul_f32 v88, s35, v88\nv_mul_f32 v89, s35, v89\n"
"v_mfma_f32_16x16x32_bf16 v[76:79], v[108:111], a[164:167], v[76:79]\nds_read_b128 v[108:111], v45 offset:256\nv_mul_f32 v90, s35, v90\nv_mul_f32 v91, s35, v91\n"
"s_waitcnt lgkmcnt(6)\nv_mfma_f32_16x16x32_bf16 v[64:67], v[112:115], a[136:139], v[64:67]\nv_cvt_pk_bf16_f32 v154, v88, v89\nv_cvt_pk_bf16_f32 v155, v90, v91\n"
"v_mfma_f32_16x16x32_bf16 v[68:71], v[112:115], a[168:171], v[68:71]\nds_read_b128 v[112:115], v42 offset:256\nv_mul_f32 v84, s35, v84\nv_mul_f32 v85, s35, v85\n"
"v_mfma_f32_16x16x32_bf16 v[72:75], v[116:119], a[136:139], v[72:75]\nv_mul_f32 v86, s35, v86\nv_mul_f32 v87, s35, v87\nv_mfma_f32_16x16x32_bf16 v[76:79], v[116:119], a[168:171], v[76:79]\n"
"ds_read_b128 v[116:119], v46 offset:256\nv_cvt_pk_bf16_f32 v156, v84, v85\nv_cvt_pk_bf16_f32 v157, v86, v87\ns_waitcnt lgkmcnt(6)\n"
"v_mfma_f32_16x16x32_bf16 v[64:67], v[120:123], a[140:143], v[64:67]\nv_mul_f32 v92, s35, v92\nv_mul_f32 v93, s35, v93\nv_mfma_f32_16x16x32_bf16 v[68:71], v[120:123], a[172:175], v[68:71]\n"
"ds_read_b128 v[120:123], v43 offset:256\nv_mul_f32 v94, s35, v94\nv_mul_f32 v95, s35, v95\nv_mfma_f32_16x16x32_bf16 v[72:75], v[124:127], a[140:143], v[72:75]\n"
"v_cvt_pk_bf16_f32 v158, v92, v93\nv_cvt_pk_bf16_f32 v159, v94, v95\nv_mfma_f32_16x16x32_bf16 v[76:79], v[124:127], a[172:175], v[76:79]\nds_read_b128 v[124:127], v47 offset:256\n"
"s_lshl_b32 s54, s41, 11\ns_add_u32 s54, s54, 1024\ns_waitcnt lgkmcnt(6)\nv_mfma_f32_16x16x32_bf16 v[64:67], v[96:99], a[144:147], v[64:67]\n"
"buffer_store_dwordx4 v[152:155], v13, s[16:19], s54 offen sc1\nv_mfma_f32_16x16x32_bf16 v[68:71], v[96:99], a[176:179], v[68:71]\nds_read_b128 v[96:99], v40 offset:16384\ns_add_u32 s54, s54, 14336\n"
"v_mfma_f32_16x16x32_bf16 v[72:75], v[100:103], a[144:147], v[72:75]\nbuffer_store_dwordx4 v[156:159], v13, s[16:19], s54 offen sc1\nv_mfma_f32_16x16x32_bf16 v[76:79], v[100:103], a[176:179], v[76:79]\nds_read_b128 v[100:103], v44 offset:16384\n"
"s_add_u32 m0, s32, 4096\ns_waitcnt lgkmcnt(6)\nv_mfma_f32_16x16x32_bf16 v[64:67], v[104:107], a[148:151], v[64:67]\ns_mov_b32 s53, 128\n"
"v_mfma_f32_16x16x32_bf16 v[68:71], v[104:107], a[180:183], v[68:71]\nds_read_b128 v[104:107], v41 offset:16384\nbuffer_load_dwordx4 v10, s[12:15], s53 offen lds\nv_mfma_f32_16x16x32_bf16 v[72:75], v[108:111], a[148:151], v[72:75]\n"
"s_add_u32 m0, s32, 8192\nv_mfma_f32_16x16x32_bf16 v[76:79], v[108:111], a[180:183], v[76:79]\nds_read_b128 v[108:111], v45 offset:16384\ns_mov_b32 s53, 256\n"
"s_waitcnt lgkmcnt(6)\nv_mfma_f32_16x16x32_bf16 v[64:67], v[112:115], a[152:155], v[64:67]\nbuffer_load_dwordx4 v10, s[12:15], s53 offen lds\nv_mfma_f32_16x16x32_bf16 v[68:71], v[112:115], a[184:187], v[68:71]\n"
"ds_read_b128 v[112:115], v42 offset:16384\ns_add_u32 m0, s32, 12288\nv_mfma_f32_16x16x32_bf16 v[72:75], v[116:119], a[152:155], v[72:75]\ns_mov_b32 s53, 384\n"
"v_mfma_f32_16x16x32_bf16 v[76:79], v[116:119], a[184:187], v[76:79]\nds_read_b128 v[116:119], v46 offset:16384\nbuffer_load_dwordx4 v10, s[12:15], s53 offen lds\ns_waitcnt lgkmcnt(6)\n"
"v_mfma_f32_16x16x32_bf16 v[64:67], v[120:123], a[156:159], v[64:67]\nv_mfma_f32_16x16x32_bf16 v[68:71], v[120:123], a[188:191], v[68:71]\nds_read_b128 v[120:123], v43 offset:16384\nv_mfma_f32_16x16x32_bf16 v[72:75], v[124:127], a[156:159], v[72:75]\n"
"v_mfma_f32_16x16x32_bf16 v[76:79], v[124:127], a[188:191], v[76:79]\nds_read_b128 v[124:127], v47 offset:16384\ns_waitcnt lgkmcnt(6)\nv_mfma_f32_16x16x32_bf16 v[80:83], v[96:99], a[128:131], 0\n"
"s_add_u32 m0, s32, 16384\nv_mfma_f32_16x16x32_bf16 v[84:87], v[96:99], a[160:163], 0\nds_read_b128 v[96:99], v40 offset:16640\ns_mov_b32 s53, 512\n"
"v_mfma_f32_16x16x32_bf16 v[88:91], v[100:103], a[128:131], 0\nbuffer_load_dwordx4 v10, s[12:15], s53 offen lds\nv_mfma_f32_16x16x32_bf16 v[92:95], v[100:103], a[160:163], 0\nds_read_b128 v[100:103], v44 offset:16640\n"
"v_mul_f32 v64, s35, v64\nv_mul_f32 v65, s35, v65\ns_waitcnt lgkmcnt(6)\nv_mfma_f32_16x16x32_bf16 v[80:83], v[104:107], a[132:135], v[80:83]\n"
"v_mul_f32 v66, s35, v66\nv_mul_f32 v67, s35, v67\nv_mfma_f32_16x16x32_bf16 v[84:87], v[104:107], a[164:167], v[84:87]\nds_read_b128 v[104:107], v41 offset:16640\n"
"v_cvt_pk_bf16_f32 v160, v64, v65\nv_cvt_pk_bf16_f32 v161, v66, v67\nv_mfma_f32_16x16x32_bf16 v[88:91], v[108:111], a[132:135], v[88:91]\nv_mul_f32 v72, s35, v72\n"
"v_mul_f32 v73, s35, v73\nv_mfma_f32_16x16x32_bf16 v[92:95], v[108:111], a[164:167], v[92:95]\nds_read_b128 v[108:111], v45 offset:16640\nv_mul_f32 v74, s35, v74\n"
"v_mul_f32 v75, s35, v75\ns_waitcnt lgkmcnt(6)\nv_mfma_f32_16x16x32_bf16 v[80:83], v[112:115], a[136:139], v[80:83]\nv_cvt_pk_bf16_f32 v162, v72, v73\n"
"v_cvt_pk_bf16_f32 v163, v74, v75\nv_mfma_f32_16x16x32_bf16 v[84:87], v[112:115], a[168:171], v[84:87]\nds_read_b128 v[112:115], v42 offset:16640\nv_mul_f32 v68, s35, v68\n"
"v_mul_f32 v69, s35, v69\nv_mfma_f32_16x16x32_bf16 v[88:91], v[116:119], a[136:139], v[88:91]\nv_mul_f32 v70, s35, v70\nv_mul_f32 v71, s35, v71\n"
"v_mfma_f32_16x16x32_bf16 v[92:95], v[116:119], a[168:171], v[92:95]\nds_read_b128 v[116:119], v46 offset:16640\nv_cvt_pk_bf16_f32 v164, v68, v69\nv_cvt_pk_bf16_f32 v165, v70, v71\n"
"s_waitcnt lgkmcnt(6)\nv_mfma_f32_16x16x32_bf16 v[80:83], v[120:123], a[140:143], v[80:83]\nv_mul_f32 v76, s35, v76\nv_mul_f32 v77, s35, v77\n"
"v_mfma_f32_16x16x32_bf16 v[84:87], v[120:123], a[172:175], v[84:87]\nds_read_b128 v[120:123], v43 offset:16640\nv_mul_f32 v78, s35, v78\nv_mul_f32 v79, s35, v79\n"
"v_mfma_f32_16x16x32_bf16 v[88:91], v[124:127], a[140:143], v[88:91]\nv_cvt_pk_bf16_f32 v166, v76, v77\nv_cvt_pk_bf16_f32 v167, v78, v79\nv_mfma_f32_16x16x32_bf16 v[92:95], v[124:127], a[172:175], v[92:95]\n"
"ds_read_b128 v[124:127], v47 offset:16640\ns_lshl_b32 s54, s42, 11\ns_add_u32 s54, s54, 0\ns_waitcnt vmcnt(7)\n"
"s_barrier\ns_waitcnt lgkmcnt(6)\nv_mfma_f32_16x16x32_bf16 v[80:83], v[96:99], a[144:147], v[80:83]\nbuffer_store_dwordx4 v[160:163], v13, s[16:19], s54 offen sc1\n"
"v_mfma_f32_16x16x32_bf16 v[84:87], v[96:99], a[176:179], v[84:87]\nds_read_b128 v[96:99], v40 offset:32768\ns_add_u32 s54, s54, 14336\nv_mfma_f32_16x16x32_bf16 v[88:91], v[100:103], a[144:147], v[88:91]\n"
"buffer_store_dwordx4 v[164:167], v13, s[16:19], s54 offen sc1\nv_mfma_f32_16x16x32_bf16 v[92:95], v[100:103], a[176:179], v[92:95]\nds_read_b128 v[100:103], v44 offset:32768\ns_add_u32 m0, s32, 20480\n"
"s_waitcnt lgkmcnt(6)\nv_mfma_f32_16x16x32_bf16 v[80:83], v[104:107], a[148:151], v[80:83]\ns_mov_b32 s53, 640\nv_mfma_f32_16x16x32_bf16 v[84:87], v[104:107], a[180:183], v[84:87]\n"
"ds_read_b128 v[104:107], v41 offset:32768\nbuffer_load_dwordx4 v10, s[12:15], s53 offen lds\nv_mfma_f32_16x16x32_bf16 v[88:91], v[108:111], a[148:151], v[88:91]\ns_add_u32 m0, s32, 24576\n"
"v_mfma_f32_16x16x32_bf16 v[92:95], v[108:111], a[180:183], v[92:95]\nds_read_b128 v[108:111], v45 offset:32768\ns_mov_b32 s53, 768\ns_waitcnt lgkmcnt(6)\n"
"v_mfma_f32_16x16x32_bf16 v[80:83], v[112:115], a[152:155], v[80:83]\nbuffer_load_dwordx4 v10, s[12:15], s53 offen lds\nv_mfma_f32_16x16x32_bf16 v[84:87], v[112:115], a[184:187], v[84:87]\nds_read_b128 v[112:115], v42 offset:32768\n"
"v_mfma_f32_16x16x32_bf16 v[88:91], v[116:119], a[152:155], v[88:91]\nv_mfma_f32_16x16x32_bf16 v[92:95], v[116:119], a[184:187], v[92:95]\nds_read_b128 v[116:119], v46 offset:32768\ns_waitcnt lgkmcnt(6)\n"
"v_mfma_f32_16x16x32_bf16 v[80:83], v[120:123], a[156:159], v[80:83]\nv_mfma_f32_16x16x32_bf16 v[84:87], v[120:123], a[188:191], v[84:87]\nds_read_b128 v[120:123], v43 offset:32768\nv_mfma_f32_16x16x32_bf16 v[88:91], v[124:127], a[156:159], v[88:91]\n"
"v_mfma_f32_16x16x32_bf16 v[92:95], v[124:127], a[188:191], v[92:95]\nds_read_b128 v[124:127], v47 offset:32768\ns_cmp_lt_u32 s34, 2\ns_cbranch_scc1 fg0_main\n"
"s_nop 7\nv_mul_f32 v80, s35, v80\nv_mul_f32 v81, s35, v81\nv_mul_f32 v82, s35, v82\n"
"v_mul_f32 v83, s35, v83\nv_cvt_pk_bf16_f32 v168, v80, v81\nv_cvt_pk_bf16_f32 v169, v82, v83\nv_mul_f32 v88, s35, v88\n"
"v_mul_f32 v89, s35, v89\nv_mul_f32 v90, s35, v90\nv_mul_f32 v91, s35, v91\nv_cvt_pk_bf16_f32 v170, v88, v89\n"
"v_cvt_pk_bf16_f32 v171, v90, v91\nv_mul_f32 v84, s35, v84\nv_mul_f32 v85, s35, v85\nv_mul_f32 v86, s35, v86\n"
"v_mul_f32 v87, s35, v87\nv_cvt_pk_bf16_f32 v172, v84, v85\nv_cvt_pk_bf16_f32 v173, v86, v87\nv_mul_f32 v92, s35, v92\n"
"v_mul_f32 v93, s35, v93\nv_mul_f32 v94, s35, v94\nv_mul_f32 v95, s35, v95\nv_cvt_pk_bf16_f32 v174, v92, v93\n"
"v_cvt_pk_bf16_f32 v175, v94, v95\ns_lshl_b32 s54, s42, 11\ns_add_u32 s54, s54, 1024\nbuffer_store_dwordx4 v[168:171], v13, s[16:19], s54 offen sc1\n"
"s_add_u32 s54, s54, 14336\nbuffer_store_dwordx4 v[172:175], v13, s[16:19], s54 offen sc1\nbuffer_load_dword v246, v21, s[24:27], 0 offen sc1\nbuffer_load_dwordx2 v[244:245], v20, s[24:27], 0 offen sc1\n"
"buffer_load_dwordx2 v[248:249], v15, s[24:27], 0 offen sc1\nv_mov_b32 v36, s28\ns_waitcnt vmcnt(3) lgkmcnt(0)\ns_barrier\n"
"buffer_store_dword v36, v19, s[24:27], 0 offen sc1\ns_waitcnt vmcnt(1)\ns_branch fg0_end\nfg0_main:\n"
"s_waitcnt lgkmcnt(6)\nv_mfma_f32_16x16x32_bf16 v[64:67], v[96:99], a[128:131], 0\ns_add_u32 m0, s32, 32768\nv_mfma_f32_16x16x32_bf16 v[68:71], v[96:99], a[160:163], 0\n"
"ds_read_b128 v[96:99], v40 offset:33024\ns_mov_b32 s53, 28672\nv_mfma_f32_16x16x32_bf16 v[72:75], v[100:103], a[128:131], 0\nbuffer_load_dwordx4 v10, s[12:15], s53 offen lds\n"
"v_mfma_f32_16x16x32_bf16 v[76:79], v[100:103], a[160:163], 0\nds_read_b128 v[100:103], v44 offset:33024\nv_mul_f32 v80, s35, v80\nv_mul_f32 v81, s35, v81\n"
"s_waitcnt lgkmcnt(6)\nv_mfma_f32_16x16x32_bf16 v[64:67], v[104:107], a[132:135], v[64:67]\nv_mul_f32 v82, s35, v82\nv_mul_f32 v83, s35, v83\n"
"v_mfma_f32_16x16x32_bf16 v[68:71], v[104:107], a[164:167], v[68:71]\nds_read_b128 v[104:107], v41 offset:33024\nv_cvt_pk_bf16_f32 v168, v80, v81\nv_cvt_pk_bf16_f32 v169, v82, v83\n"
"v_mfma_f32_16x16x32_bf16 v[72:75], v[108:111], a[132:135], v[72:75]\nv_mul_f32 v88, s35, v88\nv_mul_f32 v89, s35, v89\nv_mfma_f32_16x16x32_bf16 v[76:79], v[108:111], a[164:167], v[76:79]\n"
"ds_read_b128 v[108:111], v45 offset:33024\nv_mul_f32 v90, s35, v90\nv_mul_f32 v91, s35, v91\ns_waitcnt lgkmcnt(6)\n"
"v_mfma_f32_16x16x32_bf16 v[64:67], v[112:115], a[136:139], v[64:67]\nv_cvt_pk_bf16_f32 v170, v88, v89\nv_cvt_pk_bf16_f32 v171, v90, v91\nv_mfma_f32_16x16x32_bf16 v[68:71], v[112:115], a[168:171], v[68:71]\n"
"ds_read_b128 v[112:115], v42 offset:33024\nv_mul_f32 v84, s35, v84\nv_mul_f32 v85, s35, v85\nv_mfma_f32_16x16x32_bf16 v[72:75], v[116:119], a[136:139], v[72:75]\n"
"v_mul_f32 v86, s35, v86\nv_mul_f32 v87, s35, v87\nv_mfma_f32_16x16x32_bf16 v[76:79], v[116:119], a[168:171], v[76:79]\nds_read_b128 v[116:119], v46 offset:33024\n"
"v_cvt_pk_bf16_f32 v172, v84, v85\nv_cvt_pk_bf16_f32 v173, v86, v87\ns_waitcnt lgkmcnt(6)\nv_mfma_f32_16x16x32_bf16 v[64:67], v[120:123], a[140:143], v[64:67]\n"
"v_mul_f32 v92, s35, v92\nv_mul_f32 v93, s35, v93\nv_mfma_f32_16x16x32_bf16 v[68:71], v[120:123], a[172:175], v[68:71]\nds_read_b128 v[120:123], v43 offset:33024\n"
"v_mul_f32 v94, s35, v94\nv_mul_f32 v95, s35, v95\nv_mfma_f32_16x16x32_bf16 v[72:75], v[124:127], a[140:143], v[72:75]\nv_cvt_pk_bf16_f32 v174, v92, v93\n"
"v_cvt_pk_bf16_f32 v175, v94, v95\nv_mfma_f32_16x16x32_bf16 v[76:79], v[124:127], a[172:175], v[76:79]\nds_read_b128 v[124:127], v47 offset:33024\ns_lshl_b32 s54, s42, 11\n"
"s_add_u32 s54, s54, 1024\ns_waitcnt lgkmcnt(6)\nv_mfma_f32_16x16x32_bf16 v[64:67], v[96:99], a[144:147], v[64:67]\nbuffer_store_dwordx4 v[168:171], v13, s[16:19], s54 offen sc1\n"
"v_mfma_f32_16x16x32_bf16 v[68:71], v[96:99], a[176:179], v[68:71]\nds_read_b128 v[96:99], v40 offset:49152\ns_add_u32 s54, s54, 14336\nv_mfma_f32_16x16x32_bf16 v[72:75], v[100:103], a[144:147], v[72:75]\n"
"buffer_store_dwordx4 v[172:175], v13, s[16:19], s54 offen sc1\nv_mfma_f32_16x16x32_bf16 v[76:79], v[100:103], a[176:179], v[76:79]\nds_read_b128 v[100:103], v44 offset:49152\ns_add_u32 m0, s32, 36864\n"
"s_waitcnt lgkmcnt(6)\nv_mfma_f32_16x16x32_bf16 v[64:67], v[104:107], a[148:151], v[64:67]\ns_mov_b32 s53, 28800\nv_mfma_f32_16x16x32_bf16 v[68:71], v[104:107], a[180:183], v[68:71]\n"
"ds_read_b128 v[104:107], v41 offset:49152\nbuffer_load_dwordx4 v10, s[12:15], s53 offen lds\nv_mfma_f32_16x16x32_bf16 v[72:75], v[108:111], a[148:151], v[72:75]\ns_add_u32 m0, s32, 40960\n"
"v_mfma_f32_16x16x32_bf16 v[76:79], v[108:111], a[180:183], v[76:79]\nds_read_b128 v[108:111], v45 offset:49152\ns_mov_b32 s53, 28928\ns_waitcnt lgkmcnt(6)\n"
"v_mfma_f32_16x16x32_bf16 v[64:67], v[112:115], a[152:155], v[64:67]\nbuffer_load_dwordx4 v10, s[12:15], s53 offen lds\nv_mfma_f32_16x16x32_bf16 v[68:71], v[112:115], a[184:187], v[68:71]\nds_read_b128 v[112:115], v42 offset:49152\n"
"s_add_u32 m0, s32, 45056\nv_mfma_f32_16x16x32_bf16 v[72:75], v[116:119], a[152:155], v[72:75]\ns_mov_b32 s53, 29056\nv_mfma_f32_16x16x32_bf16 v[76:79], v[116:119], a[184:187], v[76:79]\n"
"ds_read_b128 v[116:119], v46 offset:49152\nbuffer_load_dwordx4 v10, s[12:15], s53 offen lds\ns_waitcnt lgkmcnt(6)\nv_mfma_f32_16x16x32_bf16 v[64:67], v[120:123], a[156:159], v[64:67]\n"
"v_mfma_f32_16x16x32_bf16 v[68:71], v[120:123], a[188:191], v[68:71]\nds_read_b128 v[120:123], v43 offset:49152\nv_mfma_f32_16x16x32_bf16 v[72:75], v[124:127], a[156:159], v[72:75]\nv_mfma_f32_16x16x32_bf16 v[76:79], v[124:127], a[188:191], v[76:79]\n"
"ds_read_b128 v[124:127], v47 offset:49152\ns_waitcnt lgkmcnt(6)\nv_mfma_f32_16x16x32_bf16 v[80:83], v[96:99], a[128:131], 0\ns_add_u32 m0, s32, 49152\n"
"v_mfma_f32_16x16x32_bf16 v[84:87], v[96:99], a[160:163], 0\nds_read_b128 v[96:99], v40 offset:49408\ns_mov_b32 s53, 29184\nv_mfma_f32_16x16x32_bf16 v[88:91], v[100:103], a[128:131], 0\n"
"buffer_load_dwordx4 v10, s[12:15], s53 offen lds\nv_mfma_f32_16x16x32_bf16 v[92:95], v[100:103], a[160:163], 0\nds_read_b128 v[100:103], v44 offset:49408\nv_mul_f32 v64, s35, v64\n"
"v_mul_f32 v65, s35, v65\ns_waitcnt lgkmcnt(6)\nv_mfma_f32_16x16x32_bf16 v[80:83], v[104:107], a[132:135], v[80:83]\nv_mul_f32 v66, s35, v66\n"
"v_mul_f32 v67, s35, v67\nv_mfma_f32_16x16x32_bf16 v[84:87], v[104:107], a[164:167], v[84:87]\nds_read_b128 v[104:107], v41 offset:49408\nv_cvt_pk_bf16_f32 v176, v64, v65\n"
"v_cvt_pk_bf16_f32 v177, v66, v67\nv_mfma_f32_16x16x32_bf16 v[88:91], v[108:111], a[132:135], v[88:91]\nv_mul_f32 v72, s35, v72\nv_mul_f32 v73, s35, v73\n"
"v_mfma_f32_16x16x32_bf16 v[92:95], v[108:111], a[164:167], v[92:95]\nds_read_b128 v[108:111], v45 offset:49408\nv_mul_f32 v74, s35, v74\nv_mul_f32 v75, s35, v75\n"
"s_waitcnt lgkmcnt(6)\nv_mfma_f32_16x16x32_bf16 v[80:83], v[112:115], a[136:139], v[80:83]\nv_cvt_pk_bf16_f32 v178, v72, v73\nv_cvt_pk_bf16_f32 v179, v74, v75\n"
"v_mfma_f32_16x16x32_bf16 v[84:87], v[112:115], a[168:171], v[84:87]\nds_read_b128 v[112:115], v42 offset:49408\nv_mul_f32 v68, s35, v68\nv_mul_f32 v69, s35, v69\n"
"v_mfma_f32_16x16x32_bf16 v[88:91], v[116:119], a[136:139], v[88:91]\nv_mul_f32 v70, s35, v70\nv_mul_f32 v71, s35, v71\nv_mfma_f32_16x16x32_bf16 v[92:95], v[116:119], a[168:171], v[92:95]\n"
"ds_read_b128 v[116:119], v46 offset:49408\nv_cvt_pk_bf16_f32 v180, v68, v69\nv_cvt_pk_bf16_f32 v181, v70, v71\ns_waitcnt lgkmcnt(6)\n"
"v_mfma_f32_16x16x32_bf16 v[80:83], v[120:123], a[140:143], v[80:83]\nv_mul_f32 v76, s35, v76\nv_mul_f32 v77, s35, v77\nv_mfma_f32_16x16x32_bf16 v[84:87], v[120:123], a[172:175], v[84:87]\n"
"ds_read_b128 v[120:123], v43 offset:49408\nv_mul_f32 v78, s35, v78\nv_mul_f32 v79, s35, v79\nv_mfma_f32_16x16x32_bf16 v[88:91], v[124:127], a[140:143], v[88:91]\n"
"v_cvt_pk_bf16_f32 v182, v76, v77\nv_cvt_pk_bf16_f32 v183, v78, v79\nv_mfma_f32_16x16x32_bf16 v[92:95], v[124:127], a[172:175], v[92:95]\nds_read_b128 v[124:127], v47 offset:49408\n"
"s_lshl_b32 s54, s43, 11\ns_add_u32 s54, s54, 0\ns_waitcnt lgkmcnt(6)\nv_mfma_f32_16x16x32_bf16 v[80:83], v[96:99], a[144:147], v[80:83]\n"
"buffer_store_dwordx4 v[176:179], v13, s[16:19], s54 offen sc1\nv_mfma_f32_16x16x32_bf16 v[84:87], v[96:99], a[176:179], v[84:87]\ns_add_u32 s54, s54, 14336\nv_mfma_f32_16x16x32_bf16 v[88:91], v[100:103], a[144:147], v[88:91]\n"
"buffer_store_dwordx4 v[180:183], v13, s[16:19], s54 offen sc1\nv_mfma_f32_16x16x32_bf16 v[92:95], v[100:103], a[176:179], v[92:95]\ns_add_u32 m0, s32, 53248\ns_waitcnt lgkmcnt(4)\n"
"v_mfma_f32_16x16x32_bf16 v[80:83], v[104:107], a[148:151], v[80:83]\ns_mov_b32 s53, 29312\nv_mfma_f32_16x16x32_bf16 v[84:87], v[104:107], a[180:183], v[84:87]\nbuffer_load_dwordx4 v10, s[12:15], s53 offen lds\n"
"v_mfma_f32_16x16x32_bf16 v[88:91], v[108:111], a[148:151], v[88:91]\ns_add_u32 m0, s32, 57344\nv_mfma_f32_16x16x32_bf16 v[92:95], v[108:111], a[180:183], v[92:95]\ns_mov_b32 s53, 29440\n"
"s_waitcnt lgkmcnt(2)\nv_mfma_f32_16x16x32_bf16 v[80:83], v[112:115], a[152:155], v[80:83]\nbuffer_load_dwordx4 v10, s[12:15], s53 offen lds\nv_mfma_f32_16x16x32_bf16 v[84:87], v[112:115], a[184:187], v[84:87]\n"
"buffer_load_dword v37, v21, s[24:27], 0 offen sc1\nv_mfma_f32_16x16x32_bf16 v[88:91], v[116:119], a[152:155], v[88:91]\nv_mfma_f32_16x16x32_bf16 v[92:95], v[116:119], a[184:187], v[92:95]\ns_waitcnt lgkmcnt(0)\n"
"v_mfma_f32_16x16x32_bf16 v[80:83], v[120:123], a[156:159], v[80:83]\nv_mfma_f32_16x16x32_bf16 v[84:87], v[120:123], a[188:191], v[84:87]\nv_mfma_f32_16x16x32_bf16 v[88:91], v[124:127], a[156:159], v[88:91]\nv_mfma_f32_16x16x32_bf16 v[92:95], v[124:127], a[188:191], v[92:95]\n"
"s_nop 7\nv_mul_f32 v80, s35, v80\nv_mul_f32 v81, s35, v81\nv_mul_f32 v82, s35, v82\n"
"v_mul_f32 v83, s35, v83\nv_cvt_pk_bf16_f32 v184, v80, v81\nv_cvt_pk_bf16_f32 v185, v82, v83\nv_mul_f32 v88, s35, v88\n"
"v_mul_f32 v89, s35, v89\nv_mul_f32 v90, s35, v90\nv_mul_f32 v91, s35, v91\nv_cvt_pk_bf16_f32 v186, v88, v89\n"
"v_cvt_pk_bf16_f32 v187, v90, v91\nv_mul_f32 v84, s35, v84\nv_mul_f32 v85, s35, v85\nv_mul_f32 v86, s35, v86\n"
"v_mul_f32 v87, s35, v87\nv_cvt_pk_bf16_f32 v188, v84, v85\nv_cvt_pk_bf16_f32 v189, v86, v87\nv_mul_f32 v92, s35, v92\n"
"v_mul_f32 v93, s35, v93\nv_mul_f32 v94, s35, v94\nv_mul_f32 v95, s35, v95\nv_cvt_pk_bf16_f32 v190, v92, v93\n"
"v_cvt_pk_bf16_f32 v191, v94, v95\ns_lshl_b32 s54, s43, 11\ns_add_u32 s54, s54, 1024\nbuffer_store_dwordx4 v[184:187], v13, s[16:19], s54 offen sc1\n"
"s_add_u32 s54, s54, 14336\nbuffer_store_dwordx4 v[188:191], v13, s[16:19], s54 offen sc1\ns_waitcnt vmcnt(2)\nv_mov_b32 v36, s28\n"
"v_cmp_ne_u32 vcc, s28, v37\ns_cbranch_vccz fg0_e1\ns_waitcnt vmcnt(0)\ns_barrier\n"
"buffer_store_dword v36, v19, s[24:27], 0 offen sc1\nfg0_po:\nv_cmp_ne_u32 vcc, s28, v37\ns_cbranch_vccz fg0_pook\n"
"s_add_u32 s55, s55, 1\ns_cmp_gt_u32 s55, 262144\ns_cbranch_scc0 fg0_po2\ns_endpgm\n"
"fg0_po2:\ns_sleep 1\nbuffer_load_dword v37, v21, s[24:27], 0 offen sc1\ns_waitcnt vmcnt(0)\n"
"s_branch fg0_po\nfg0_pook:\ns_mov_b32 s53, 8192\nbuffer_load_dwordx4 v[192:195], v13, s[16:19], s53 offen sc1\n"
"s_mov_b32 s53, 22528\nbuffer_load_dwordx4 v[196:199], v13, s[16:19], s53 offen sc1\ns_mov_b32 s53, 9216\nbuffer_load_dwordx4 v[200:203], v13, s[16:19], s53 offen sc1\n"
"s_mov_b32 s53, 23552\nbuffer_load_dwordx4 v[204:207], v13, s[16:19], s53 offen sc1\ns_mov_b32 s53, 10240\nbuffer_load_dwordx4 v[208:211], v13, s[16:19], s53 offen sc1\n"
"s_mov_b32 s53, 24576\nbuffer_load_dwordx4 v[212:215], v13, s[16:19], s53 offen sc1\ns_mov_b32 s53, 11264\nbuffer_load_dwordx4 v[216:219], v13, s[16:19], s53 offen sc1\n"
"s_mov_b32 s53, 25600\nbuffer_load_dwordx4 v[220:223], v13, s[16:19], s53 offen sc1\ns_mov_b32 s53, 12288\nbuffer_load_dwordx4 v[224:227], v13, s[16:19], s53 offen sc1\n"
"s_mov_b32 s53, 26624\nbuffer_load_dwordx4 v[228:231], v13, s[16:19], s53 offen sc1\ns_mov_b32 s53, 13312\nbuffer_load_dwordx4 v[232:235], v13, s[16:19], s53 offen sc1\n"
"s_mov_b32 s53, 27648\nbuffer_load_dwordx4 v[236:239], v13, s[16:19], s53 offen sc1\nbuffer_store_dword v36, v19, s[24:27], 0 offen sc1\ns_branch fg0_e2\n"
"fg0_e1:\ns_mov_b32 s53, 8192\nbuffer_load_dwordx4 v[192:195], v13, s[16:19], s53 offen sc1\ns_mov_b32 s53, 22528\n"
"buffer_load_dwordx4 v[196:199], v13, s[16:19], s53 offen sc1\ns_mov_b32 s53, 9216\nbuffer_load_dwordx4 v[200:203], v13, s[16:19], s53 offen sc1\ns_mov_b32 s53, 23552\n"
"buffer_load_dwordx4 v[204:207], v13, s[16:19], s53 offen sc1\ns_mov_b32 s53, 10240\nbuffer_load_dwordx4 v[208:211], v13, s[16:19], s53 offen sc1\ns_mov_b32 s53, 24576\n"
"buffer_load_dwordx4 v[212:215], v13, s[16:19], s53 offen sc1\ns_mov_b32 s53, 11264\nbuffer_load_dwordx4 v[216:219], v13, s[16:19], s53 offen sc1\ns_mov_b32 s53, 25600\n"
"buffer_load_dwordx4 v[220:223], v13, s[16:19], s53 offen sc1\ns_mov_b32 s53, 12288\nbuffer_load_dwordx4 v[224:227], v13, s[16:19], s53 offen sc1\ns_mov_b32 s53, 26624\n"
"buffer_load_dwordx4 v[228:231], v13, s[16:19], s53 offen sc1\ns_mov_b32 s53, 13312\nbuffer_load_dwordx4 v[232:235], v13, s[16:19], s53 offen sc1\ns_mov_b32 s53, 27648\n"
"buffer_load_dwordx4 v[236:239], v13, s[16:19], s53 offen sc1\ns_waitcnt vmcnt(12)\ns_barrier\nbuffer_store_dword v36, v19, s[24:27], 0 offen sc1\n"
"fg0_e2:\ns_add_u32 m0, s32, 65536\ns_mov_b32 s53, 57344\nbuffer_load_dwordx4 v10, s[12:15], s53 offen lds\n"
"s_add_u32 m0, s32, 69632\ns_mov_b32 s53, 57472\nbuffer_load_dwordx4 v10, s[12:15], s53 offen lds\ns_add_u32 m0, s32, 73728\n"
"s_mov_b32 s53, 57600\nbuffer_load_dwordx4 v10, s[12:15], s53 offen lds\ns_add_u32 m0, s32, 77824\ns_mov_b32 s53, 57728\n"
"buffer_load_dwordx4 v10, s[12:15], s53 offen lds\ns_add_u32 m0, s32, 81920\ns_mov_b32 s53, 57856\nbuffer_load_dwordx4 v10, s[12:15], s53 offen lds\n"
"s_add_u32 m0, s32, 86016\ns_mov_b32 s53, 57984\nbuffer_load_dwordx4 v10, s[12:15], s53 offen lds\ns_add_u32 m0, s32, 90112\n"
"s_mov_b32 s53, 58112\nbuffer_load_dwordx4 v10, s[12:15], s53 offen lds\ns_sub_u32 s48, s28, 1\ns_mov_b32 s49, 86016\n"
"s_mov_b32 s50, 0\ns_mov_b32 s51, 0\ns_add_u32 s52, s32, 98304\ns_add_u32 s47, s32, 131072\n"
"ds_read_b128 v[40:43], v32 offset:0\nds_read_b128 v[44:47], v33 offset:0\nds_read_b128 v[48:51], v34 offset:0\nds_read_b128 v[52:55], v35 offset:0\n"
"s_barrier\ns_waitcnt lgkmcnt(3)\nv_mfma_f32_16x16x32_bf16 v[64:67], v[128:131], v[40:43], 0\nv_mov_b32 v36, s48\n"
"s_add_u32 s48, s48, 1\ns_nop 0\nbuffer_store_dword v36, v19, s[24:27], s33 offen sc1\nv_mfma_f32_16x16x32_bf16 v[68:71], v[132:135], v[40:43], 0\n"
"ds_read_b128 v[40:43], v32 offset:4096\ns_waitcnt lgkmcnt(3)\nv_mfma_f32_16x16x32_bf16 v[72:75], v[128:131], v[44:47], 0\nv_mfma_f32_16x16x32_bf16 v[76:79], v[132:135], v[44:47], 0\n"
"ds_read_b128 v[44:47], v33 offset:4096\ns_waitcnt lgkmcnt(3)\nv_mfma_f32_16x16x32_bf16 v[64:67], v[136:139], v[48:51], v[64:67]\ns_add_u32 m0, s52, 0\n"
"v_mfma_f32_16x16x32_bf16 v[68:71], v[140:143], v[48:51], v[68:71]\nds_read_b128 v[48:51], v34 offset:4096\ns_add_u32 s53, s49, 0\ns_waitcnt lgkmcnt(3)\n"
"v_mfma_f32_16x16x32_bf16 v[72:75], v[136:139], v[52:55], v[72:75]\nbuffer_load_dwordx4 v10, s[12:15], s53 offen lds\nv_mfma_f32_16x16x32_bf16 v[76:79], v[140:143], v[52:55], v[76:79]\nds_read_b128 v[52:55], v35 offset:4096\n"
"s_add_u32 m0, s52, 4096\ns_waitcnt lgkmcnt(3)\nv_mfma_f32_16x16x32_bf16 v[64:67], v[144:147], v[40:43], v[64:67]\ns_add_u32 s53, s49, 128\n"
"v_mfma_f32_16x16x32_bf16 v[68:71], v[148:151], v[40:43], v[68:71]\nds_read_b128 v[40:43], v32 offset:8192\nbuffer_load_dwordx4 v10, s[12:15], s53 offen lds\ns_waitcnt lgkmcnt(3)\n"
"v_mfma_f32_16x16x32_bf16 v[72:75], v[144:147], v[44:47], v[72:75]\ns_add_u32 m0, s52, 8192\nv_mfma_f32_16x16x32_bf16 v[76:79], v[148:151], v[44:47], v[76:79]\nds_read_b128 v[44:47], v33 offset:8192\n"
"s_add_u32 s53, s49, 256\ns_waitcnt lgkmcnt(3)\nv_mfma_f32_16x16x32_bf16 v[64:67], v[152:155], v[48:51], v[64:67]\nbuffer_load_dwordx4 v10, s[12:15], s53 offen lds\n"
"v_mfma_f32_16x16x32_bf16 v[68:71], v[156:159], v[48:51], v[68:71]\nds_read_b128 v[48:51], v34 offset:8192\ns_add_u32 m0, s52, 12288\ns_waitcnt lgkmcnt(3)\n"
"v_mfma_f32_16x16x32_bf16 v[72:75], v[152:155], v[52:55], v[72:75]\ns_add_u32 s53, s49, 384\nv_mfma_f32_16x16x32_bf16 v[76:79], v[156:159], v[52:55], v[76:79]\nds_read_b128 v[52:55], v35 offset:8192\n"
"buffer_load_dwordx4 v10, s[12:15], s53 offen lds\ns_waitcnt lgkmcnt(3)\nv_mfma_f32_16x16x32_bf16 v[64:67], v[160:163], v[40:43], v[64:67]\ns_add_u32 m0, s52, 16384\n"
"v_mfma_f32_16x16x32_bf16 v[68:71], v[164:167], v[40:43], v[68:71]\nds_read_b128 v[40:43], v32 offset:12288\ns_add_u32 s53, s49, 512\ns_waitcnt lgkmcnt(3)\n"
"v_mfma_f32_16x16x32_bf16 v[72:75], v[160:163], v[44:47], v[72:75]\nbuffer_load_dwordx4 v10, s[12:15], s53 offen lds\nv_mfma_f32_16x16x32_bf16 v[76:79], v[164:167], v[44:47], v[76:79]\nds_read_b128 v[44:47], v33 offset:12288\n"
"s_add_u32 m0, s52, 20480\ns_waitcnt lgkmcnt(3)\nv_mfma_f32_16x16x32_bf16 v[64:67], v[168:171], v[48:51], v[64:67]\ns_add_u32 s53, s49, 640\n"
"v_mfma_f32_16x16x32_bf16 v[68:71], v[172:175], v[48:51], v[68:71]\nds_read_b128 v[48:51], v34 offset:12288\nbuffer_load_dwordx4 v10, s[12:15], s53 offen lds\ns_waitcnt lgkmcnt(3)\n"
"v_mfma_f32_16x16x32_bf16 v[72:75], v[168:171], v[52:55], v[72:75]\ns_add_u32 m0, s52, 24576\nv_mfma_f32_16x16x32_bf16 v[76:79], v[172:175], v[52:55], v[76:79]\nds_read_b128 v[52:55], v35 offset:12288\n"
"s_add_u32 s53, s49, 768\ns_waitcnt lgkmcnt(3)\nv_mfma_f32_16x16x32_bf16 v[64:67], v[176:179], v[40:43], v[64:67]\nbuffer_load_dwordx4 v10, s[12:15], s53 offen lds\n"
"v_mfma_f32_16x16x32_bf16 v[68:71], v[180:183], v[40:43], v[68:71]\nds_read_b128 v[40:43], v32 offset:16384\ns_waitcnt lgkmcnt(3)\nv_mfma_f32_16x16x32_bf16 v[72:75], v[176:179], v[44:47], v[72:75]\n"
"v_mfma_f32_16x16x32_bf16 v[76:79], v[180:183], v[44:47], v[76:79]\nds_read_b128 v[44:47], v33 offset:16384\ns_waitcnt lgkmcnt(3)\nv_mfma_f32_16x16x32_bf16 v[64:67], v[184:187], v[48:51], v[64:67]\n"
"v_mfma_f32_16x16x32_bf16 v[68:71], v[188:191], v[48:51], v[68:71]\nds_read_b128 v[48:51], v34 offset:16384\ns_waitcnt lgkmcnt(3)\nv_mfma_f32_16x16x32_bf16 v[72:75], v[184:187], v[52:55], v[72:75]\n"
"v_mfma_f32_16x16x32_bf16 v[76:79], v[188:191], v[52:55], v[76:79]\nds_read_b128 v[52:55], v35 offset:16384\ns_waitcnt vmcnt(16)\ns_waitcnt lgkmcnt(3)\n"
"v_mfma_f32_16x16x32_bf16 v[64:67], v[192:195], v[40:43], v[64:67]\nv_mfma_f32_16x16x32_bf16 v[68:71], v[196:199], v[40:43], v[68:71]\nds_read_b128 v[40:43], v32 offset:20480\ns_waitcnt lgkmcnt(3)\n"
"v_mfma_f32_16x16x32_bf16 v[72:75], v[192:195], v[44:47], v[72:75]\nv_mfma_f32_16x16x32_bf16 v[76:79], v[196:199], v[44:47], v[76:79]\nds_read_b128 v[44:47], v33 offset:20480\ns_waitcnt lgkmcnt(3)\n"
"v_mfma_f32_16x16x32_bf16 v[64:67], v[200:203], v[48:51], v[64:67]\nv_mfma_f32_16x16x32_bf16 v[68:71], v[204:207], v[48:51], v[68:71]\nds_read_b128 v[48:51], v34 offset:20480\ns_waitcnt lgkmcnt(3)\n"
"v_mfma_f32_16x16x32_bf16 v[72:75], v[200:203], v[52:55], v[72:75]\nv_mfma_f32_16x16x32_bf16 v[76:79], v[204:207], v[52:55], v[76:79]\nds_read_b128 v[52:55], v35 offset:20480\ns_waitcnt lgkmcnt(3)\n"
"v_mfma_f32_16x16x32_bf16 v[64:67], v[208:211], v[40:43], v[64:67]\nv_mfma_f32_16x16x32_bf16 v[68:71], v[212:215], v[40:43], v[68:71]\nds_read_b128 v[40:43], v32 offset:24576\ns_waitcnt lgkmcnt(3)\n"
"v_mfma_f32_16x16x32_bf16 v[72:75], v[208:211], v[44:47], v[72:75]\nv_mfma_f32_16x16x32_bf16 v[76:79], v[212:215], v[44:47], v[76:79]\nds_read_b128 v[44:47], v33 offset:24576\ns_waitcnt lgkmcnt(3)\n"
"v_mfma_f32_16x16x32_bf16 v[64:67], v[216:219], v[48:51], v[64:67]\ns_add_u32 s54, s51, 32768\ns_cmp_eq_u32 s54, 163840\nv_mfma_f32_16x16x32_bf16 v[68:71], v[220:223], v[48:51], v[68:71]\n"
"s_cselect_b32 s54, 0, s54\ns_sub_u32 s54, s54, s51\nds_read_b128 v[48:51], v34 offset:24576\ns_waitcnt lgkmcnt(3)\n"
"v_mfma_f32_16x16x32_bf16 v[72:75], v[216:219], v[52:55], v[72:75]\nv_mfma_f32_16x16x32_bf16 v[76:79], v[220:223], v[52:55], v[76:79]\nds_read_b128 v[52:55], v35 offset:24576\ns_waitcnt lgkmcnt(3)\n"
"v_mfma_f32_16x16x32_bf16 v[64:67], v[224:227], v[40:43], v[64:67]\nv_add_u32 v32, s54, v32\nv_mfma_f32_16x16x32_bf16 v[68:71], v[228:231], v[40:43], v[68:71]\nds_read_b128 v[40:43], v32 offset:0\n"
"s_waitcnt lgkmcnt(3)\nv_mfma_f32_16x16x32_bf16 v[72:75], v[224:227], v[44:47], v[72:75]\nv_add_u32 v33, s54, v33\nv_mfma_f32_16x16x32_bf16 v[76:79], v[228:231], v[44:47], v[76:79]\n"
"ds_read_b128 v[44:47], v33 offset:0\ns_waitcnt lgkmcnt(3)\nv_mfma_f32_16x16x32_bf16 v[64:67], v[232:235], v[48:51], v[64:67]\nv_add_u32 v34, s54, v34\n"
"v_mfma_f32_16x16x32_bf16 v[68:71], v[236:239], v[48:51], v[68:71]\nds_read_b128 v[48:51], v34 offset:0\ns_waitcnt lgkmcnt(3)\nv_mfma_f32_16x16x32_bf16 v[72:75], v[232:235], v[52:55], v[72:75]\n"
"v_add_u32 v35, s54, v35\nv_mfma_f32_16x16x32_bf16 v[76:79], v[236:239], v[52:55], v[76:79]\nds_read_b128 v[52:55], v35 offset:0\ns_mov_b32 s52, s47\n"
"s_add_u32 s47, s51, s32\ns_add_u32 s51, s51, s54\ns_add_u32 s49, s49, 28672\ns_waitcnt vmcnt(8)\n"
"s_barrier\ns_waitcnt lgkmcnt(3)\nv_mfma_f32_16x16x32_bf16 v[96:99], v[128:131], v[40:43], 0\nv_mov_b32 v36, s48\n"
"s_add_u32 s48, s48, 1\ns_nop 0\nbuffer_store_dword v36, v19, s[24:27], s33 offen sc1\nv_mfma_f32_16x16x32_bf16 v[100:103], v[132:135], v[40:43], 0\n"
"ds_read_b128 v[40:43], v32 offset:4096\ns_waitcnt lgkmcnt(3)\nv_mfma_f32_16x16x32_bf16 v[104:107], v[128:131], v[44:47], 0\nv_mfma_f32_16x16x32_bf16 v[108:111], v[132:135], v[44:47], 0\n"
"ds_read_b128 v[44:47], v33 offset:4096\nv_cvt_pk_bf16_f32 v56, v64, v65\ns_waitcnt lgkmcnt(3)\nv_mfma_f32_16x16x32_bf16 v[96:99], v[136:139], v[48:51], v[96:99]\n"
"v_cvt_pk_bf16_f32 v57, v66, v67\nv_mfma_f32_16x16x32_bf16 v[100:103], v[140:143], v[48:51], v[100:103]\nds_read_b128 v[48:51], v34 offset:4096\nv_cvt_pk_bf16_f32 v58, v68, v69\n"
"s_waitcnt lgkmcnt(3)\nv_mfma_f32_16x16x32_bf16 v[104:107], v[136:139], v[52:55], v[104:107]\nv_cvt_pk_bf16_f32 v59, v70, v71\nv_mfma_f32_16x16x32_bf16 v[108:111], v[140:143], v[52:55], v[108:111]\n"
"ds_read_b128 v[52:55], v35 offset:4096\ns_waitcnt lgkmcnt(3)\nv_mfma_f32_16x16x32_bf16 v[96:99], v[144:147], v[40:43], v[96:99]\nv_mfma_f32_16x16x32_bf16 v[100:103], v[148:151], v[40:43], v[100:103]\n"
"ds_read_b128 v[40:43], v32 offset:8192\nbuffer_store_dwordx4 v[56:59], v15, s[20:23], s50 offen sc1\ns_waitcnt lgkmcnt(3)\nv_mfma_f32_16x16x32_bf16 v[104:107], v[144:147], v[44:47], v[104:107]\n"
"v_cvt_pk_bf16_f32 v60, v72, v73\nv_mfma_f32_16x16x32_bf16 v[108:111], v[148:151], v[44:47], v[108:111]\nds_read_b128 v[44:47], v33 offset:8192\nv_cvt_pk_bf16_f32 v61, v74, v75\n"
"s_waitcnt lgkmcnt(3)\nv_mfma_f32_16x16x32_bf16 v[96:99], v[152:155], v[48:51], v[96:99]\nv_cvt_pk_bf16_f32 v62, v76, v77\nv_mfma_f32_16x16x32_bf16 v[100:103], v[156:159], v[48:51], v[100:103]\n"
"ds_read_b128 v[48:51], v34 offset:8192\nv_cvt_pk_bf16_f32 v63, v78, v79\ns_waitcnt lgkmcnt(3)\nv_mfma_f32_16x16x32_bf16 v[104:107], v[152:155], v[52:55], v[104:107]\n"
"v_mfma_f32_16x16x32_bf16 v[108:111], v[156:159], v[52:55], v[108:111]\nds_read_b128 v[52:55], v35 offset:8192\ns_waitcnt lgkmcnt(3)\nv_mfma_f32_16x16x32_bf16 v[96:99], v[160:163], v[40:43], v[96:99]\n"
"buffer_store_dwordx4 v[60:63], v16, s[20:23], s50 offen sc1\nv_mfma_f32_16x16x32_bf16 v[100:103], v[164:167], v[40:43], v[100:103]\nds_read_b128 v[40:43], v32 offset:12288\ns_add_u32 s50, s50, s36\n"
"s_waitcnt lgkmcnt(3)\nv_mfma_f32_16x16x32_bf16 v[104:107], v[160:163], v[44:47], v[104:107]\ns_add_u32 m0, s52, 0\nv_mfma_f32_16x16x32_bf16 v[108:111], v[164:167], v[44:47], v[108:111]\n"
"ds_read_b128 v[44:47], v33 offset:12288\ns_add_u32 s53, s49, 0\ns_waitcnt lgkmcnt(3)\nv_mfma_f32_16x16x32_bf16 v[96:99], v[168:171], v[48:51], v[96:99]\n"
"buffer_load_dwordx4 v10, s[12:15], s53 offen lds\nv_mfma_f32_16x16x32_bf16 v[100:103], v[172:175], v[48:51], v[100:103]\nds_read_b128 v[48:51], v34 offset:12288\ns_add_u32 m0, s52, 4096\n"
"s_waitcnt lgkmcnt(3)\nv_mfma_f32_16x16x32_bf16 v[104:107], v[168:171], v[52:55], v[104:107]\ns_add_u32 s53, s49, 128\nv_mfma_f32_16x16x32_bf16 v[108:111], v[172:175], v[52:55], v[108:111]\n"
"ds_read_b128 v[52:55], v35 offset:12288\nbuffer_load_dwordx4 v10, s[12:15], s53 offen lds\ns_waitcnt lgkmcnt(3)\nv_mfma_f32_16x16x32_bf16 v[96:99], v[176:179], v[40:43], v[96:99]\n"
"s_add_u32 m0, s52, 8192\nv_mfma_f32_16x16x32_bf16 v[100:103], v[180:183], v[40:43], v[100:103]\nds_read_b128 v[40:43], v32 offset:16384\ns_add_u32 s53, s49, 256\n"
"s_waitcnt lgkmcnt(3)\nv_mfma_f32_16x16x32_bf16 v[104:107], v[176:179], v[44:47], v[104:107]\nbuffer_load_dwordx4 v10, s[12:15], s53 offen lds\nv_mfma_f32_16x16x32_bf16 v[108:111], v[180:183], v[44:47], v[108:111]\n"
"ds_read_b128 v[44:47], v33 offset:16384\ns_add_u32 m0, s52, 12288\ns_waitcnt lgkmcnt(3)\nv_mfma_f32_16x16x32_bf16 v[96:99], v[184:187], v[48:51], v[96:99]\n"
"s_add_u32 s53, s49, 384\nv_mfma_f32_16x16x32_bf16 v[100:103], v[188:191], v[48:51], v[100:103]\nds_read_b128 v[48:51], v34 offset:16384\nbuffer_load_dwordx4 v10, s[12:15], s53 offen lds\n"
"s_waitcnt lgkmcnt(3)\nv_mfma_f32_16x16x32_bf16 v[104:107], v[184:187], v[52:55], v[104:107]\ns_add_u32 m0, s52, 16384\nv_mfma_f32_16x16x32_bf16 v[108:111], v[188:191], v[52:55], v[108:111]\n"
"ds_read_b128 v[52:55], v35 offset:16384\ns_add_u32 s53, s49, 512\ns_waitcnt lgkmcnt(3)\nv_mfma_f32_16x16x32_bf16 v[96:99], v[192:195], v[40:43], v[96:99]\n"
"buffer_load_dwordx4 v10, s[12:15], s53 offen lds\nv_mfma_f32_16x16x32_bf16 v[100:103], v[196:199], v[40:43], v[100:103]\nds_read_b128 v[40:43], v32 offset:20480\ns_add_u32 m0, s52, 20480\n"
"s_waitcnt lgkmcnt(3)\nv_mfma_f32_16x16x32_bf16 v[104:107], v[192:195], v[44:47], v[104:107]\ns_add_u32 s53, s49, 640\nv_mfma_f32_16x16x32_bf16 v[108:111], v[196:199], v[44:47], v[108:111]\n"
"ds_read_b128 v[44:47], v33 offset:20480\nbuffer_load_dwordx4 v10, s[12:15], s53 offen lds\ns_waitcnt lgkmcnt(3)\nv_mfma_f32_16x16x32_bf16 v[96:99], v[200:203], v[48:51], v[96:99]\n"
"s_add_u32 m0, s52, 24576\nv_mfma_f32_16x16x32_bf16 v[100:103], v[204:207], v[48:51], v[100:103]\nds_read_b128 v[48:51], v34 offset:20480\ns_add_u32 s53, s49, 768\n"
"s_waitcnt lgkmcnt(3)\nv_mfma_f32_16x16x32_bf16 v[104:107], v[200:203], v[52:55], v[104:107]\nbuffer_load_dwordx4 v10, s[12:15], s53 offen lds\nv_mfma_f32_16x16x32_bf16 v[108:111], v[204:207], v[52:55], v[108:111]\n"
"ds_read_b128 v[52:55], v35 offset:20480\ns_waitcnt lgkmcnt(3)\nv_mfma_f32_16x16x32_bf16 v[96:99], v[208:211], v[40:43], v[96:99]\nv_mfma_f32_16x16x32_bf16 v[100:103], v[212:215], v[40:43], v[100:103]\n"
"ds_read_b128 v[40:43], v32 offset:24576\ns_waitcnt lgkmcnt(3)\nv_mfma_f32_16x16x32_bf16 v[104:107], v[208:211], v[44:47], v[104:107]\nv_mfma_f32_16x16x32_bf16 v[108:111], v[212:215], v[44:47], v[108:111]\n"
"ds_read_b128 v[44:47], v33 offset:24576\ns_waitcnt lgkmcnt(3)\nv_mfma_f32_16x16x32_bf16 v[96:99], v[216:219], v[48:51], v[96:99]\ns_add_u32 s54, s51, 32768\n"
"s_cmp_eq_u32 s54, 163840\nv_mfma_f32_16x16x32_bf16 v[100:103], v[220:223], v[48:51], v[100:103]\ns_cselect_b32 s54, 0, s54\ns_sub_u32 s54, s54, s51\n"
"ds_read_b128 v[48:51], v34 offset:24576\ns_waitcnt lgkmcnt(3)\nv_mfma_f32_16x16x32_bf16 v[104:107], v[216:219], v[52:55], v[104:107]\nv_mfma_f32_16x16x32_bf16 v[108:111], v[220:223], v[52:55], v[108:111]\n"
"ds_read_b128 v[52:55], v35 offset:24576\ns_waitcnt lgkmcnt(3)\nv_mfma_f32_16x16x32_bf16 v[96:99], v[224:227], v[40:43], v[96:99]\nv_add_u32 v32, s54, v32\n"
"v_mfma_f32_16x16x32_bf16 v[100:103], v[228:231], v[40:43], v[100:103]\nds_read_b128 v[40:43], v32 offset:0\ns_waitcnt lgkmcnt(3)\nv_mfma_f32_16x16x32_bf16 v[104:107], v[224:227], v[44:47], v[104:107]\n"
"v_add_u32 v33, s54, v33\nv_mfma_f32_16x16x32_bf16 v[108:111], v[228:231], v[44:47], v[108:111]\nds_read_b128 v[44:47], v33 offset:0\ns_waitcnt lgkmcnt(3)\n"
"v_mfma_f32_16x16x32_bf16 v[96:99], v[232:235], v[48:51], v[96:99]\nv_add_u32 v34, s54, v34\nv_mfma_f32_16x16x32_bf16 v[100:103], v[236:239], v[48:51], v[100:103]\nds_read_b128 v[48:51], v34 offset:0\n"
"s_waitcnt lgkmcnt(3)\nv_mfma_f32_16x16x32_bf16 v[104:107], v[232:235], v[52:55], v[104:107]\nv_add_u32 v35, s54, v35\nv_mfma_f32_16x16x32_bf16 v[108:111], v[236:239], v[52:55], v[108:111]\n"
"ds_read_b128 v[52:55], v35 offset:0\ns_mov_b32 s52, s47\ns_add_u32 s47, s51, s32\ns_add_u32 s51, s51, s54\n"
"s_add_u32 s49, s49, 28672\ns_waitcnt vmcnt(7)\ns_barrier\ns_waitcnt lgkmcnt(3)\n"
"v_mfma_f32_16x16x32_bf16 v[64:67], v[128:131], v[40:43], 0\nv_mov_b32 v36, s48\ns_add_u32 s48, s48, 1\ns_nop 0\n"
"buffer_store_dword v36, v19, s[24:27], s33 offen sc1\nbuffer_load_dword v37, v20, s[24:27], 0 offen sc1\nv_mfma_f32_16x16x32_bf16 v[68:71], v[132:135], v[40:43], 0\nds_read_b128 v[40:43], v32 offset:4096\n"
"s_waitcnt lgkmcnt(3)\nv_mfma_f32_16x16x32_bf16 v[72:75], v[128:131], v[44:47], 0\nv_mfma_f32_16x16x32_bf16 v[76:79], v[132:135], v[44:47], 0\nds_read_b128 v[44:47], v33 offset:4096\n"
"v_cvt_pk_bf16_f32 v56, v96, v97\ns_waitcnt lgkmcnt(3)\nv_mfma_f32_16x16x32_bf16 v[64:67], v[136:139], v[48:51], v[64:67]\nv_cvt_pk_bf16_f32 v57, v98, v99\n"
"v_mfma_f32_16x16x32_bf16 v[68:71], v[140:143], v[48:51], v[68:71]\nds_read_b128 v[48:51], v34 offset:4096\nv_cvt_pk_bf16_f32 v58, v100, v101\ns_waitcnt lgkmcnt(3)\n"
"v_mfma_f32_16x16x32_bf16 v[72:75], v[136:139], v[52:55], v[72:75]\nv_cvt_pk_bf16_f32 v59, v102, v103\nv_mfma_f32_16x16x32_bf16 v[76:79], v[140:143], v[52:55], v[76:79]\nds_read_b128 v[52:55], v35 offset:4096\n"
"s_waitcnt lgkmcnt(3)\nv_mfma_f32_16x16x32_bf16 v[64:67], v[144:147], v[40:43], v[64:67]\nv_mfma_f32_16x16x32_bf16 v[68:71], v[148:151], v[40:43], v[68:71]\nds_read_b128 v[40:43], v32 offset:8192\n"
"buffer_store_dwordx4 v[56:59], v15, s[20:23], s50 offen sc1\ns_waitcnt lgkmcnt(3)\nv_mfma_f32_16x16x32_bf16 v[72:75], v[144:147], v[44:47], v[72:75]\nv_cvt_pk_bf16_f32 v60, v104, v105\n"
"v_mfma_f32_16x16x32_bf16 v[76:79], v[148:151], v[44:47], v[76:79]\nds_read_b128 v[44:47], v33 offset:8192\nv_cvt_pk_bf16_f32 v61, v106, v107\ns_waitcnt lgkmcnt(3)\n"
"v_mfma_f32_16x16x32_bf16 v[64:67], v[152:155], v[48:51], v[64:67]\nv_cvt_pk_bf16_f32 v62, v108, v109\nv_mfma_f32_16x16x32_bf16 v[68:71], v[156:159], v[48:51], v[68:71]\nds_read_b128 v[48:51], v34 offset:8192\n"
"v_cvt_pk_bf16_f32 v63, v110, v111\ns_waitcnt lgkmcnt(3)\nv_mfma_f32_16x16x32_bf16 v[72:75], v[152:155], v[52:55], v[72:75]\nv_mfma_f32_16x16x32_bf16 v[76:79], v[156:159], v[52:55], v[76:79]\n"
"ds_read_b128 v[52:55], v35 offset:8192\ns_waitcnt lgkmcnt(3)\nv_mfma_f32_16x16x32_bf16 v[64:67], v[160:163], v[40:43], v[64:67]\nbuffer_store_dwordx4 v[60:63], v16, s[20:23], s50 offen sc1\n"
"v_mfma_f32_16x16x32_bf16 v[68:71], v[164:167], v[40:43], v[68:71]\nds_read_b128 v[40:43], v32 offset:12288\ns_add_u32 s50, s50, s36\ns_waitcnt lgkmcnt(3)\n"
"v_mfma_f32_16x16x32_bf16 v[72:75], v[160:163], v[44:47], v[72:75]\ns_add_u32 m0, s52, 0\nv_mfma_f32_16x16x32_bf16 v[76:79], v[164:167], v[44:47], v[76:79]\nds_read_b128 v[44:47], v33 offset:12288\n"
"s_add_u32 s53, s49, 0\ns_waitcnt lgkmcnt(3)\nv_mfma_f32_16x16x32_bf16 v[64:67], v[168:171], v[48:51], v[64:67]\nbuffer_load_dwordx4 v10, s[12:15], s53 offen lds\n"
"v_mfma_f32_16x16x32_bf16 v[68:71], v[172:175], v[48:51], v[68:71]\nds_read_b128 v[48:51], v34 offset:12288\ns_add_u32 m0, s52, 4096\ns_waitcnt lgkmcnt(3)\n"
"v_mfma_f32_16x16x32_bf16 v[72:75], v[168:171], v[52:55], v[72:75]\ns_add_u32 s53, s49, 128\nv_mfma_f32_16x16x32_bf16 v[76:79], v[172:175], v[52:55], v[76:79]\nds_read_b128 v[52:55], v35 offset:12288\n"
"buffer_load_dwordx4 v10, s[12:15], s53 offen lds\ns_waitcnt lgkmcnt(3)\nv_mfma_f32_16x16x32_bf16 v[64:67], v[176:179], v[40:43], v[64:67]\ns_add_u32 m0, s52, 8192\n"
"v_mfma_f32_16x16x32_bf16 v[68:71], v[180:183], v[40:43], v[68:71]\nds_read_b128 v[40:43], v32 offset:16384\ns_add_u32 s53, s49, 256\ns_waitcnt lgkmcnt(3)\n"
"v_mfma_f32_16x16x32_bf16 v[72:75], v[176:179], v[44:47], v[72:75]\nbuffer_load_dwordx4 v10, s[12:15], s53 offen lds\nv_mfma_f32_16x16x32_bf16 v[76:79], v[180:183], v[44:47], v[76:79]\nds_read_b128 v[44:47], v33 offset:16384\n"
"s_add_u32 m0, s52, 12288\ns_waitcnt lgkmcnt(3)\nv_mfma_f32_16x16x32_bf16 v[64:67], v[184:187], v[48:51], v[64:67]\ns_add_u32 s53, s49, 384\n"
"v_mfma_f32_16x16x32_bf16 v[68:71], v[188:191], v[48:51], v[68:71]\nds_read_b128 v[48:51], v34 offset:16384\nbuffer_load_dwordx4 v10, s[12:15], s53 offen lds\ns_waitcnt lgkmcnt(3)\n"
"v_mfma_f32_16x16x32_bf16 v[72:75], v[184:187], v[52:55], v[72:75]\ns_add_u32 m0, s52, 16384\nv_mfma_f32_16x16x32_bf16 v[76:79], v[188:191], v[52:55], v[76:79]\nds_read_b128 v[52:55], v35 offset:16384\n"
"s_add_u32 s53, s49, 512\ns_waitcnt lgkmcnt(3)\nv_mfma_f32_16x16x32_bf16 v[64:67], v[192:195], v[40:43], v[64:67]\nbuffer_load_dwordx4 v10, s[12:15], s53 offen lds\n"
"v_mfma_f32_16x16x32_bf16 v[68:71], v[196:199], v[40:43], v[68:71]\nds_read_b128 v[40:43], v32 offset:20480\ns_add_u32 m0, s52, 20480\ns_waitcnt lgkmcnt(3)\n"
"v_mfma_f32_16x16x32_bf16 v[72:75], v[192:195], v[44:47], v[72:75]\ns_add_u32 s53, s49, 640\nv_mfma_f32_16x16x32_bf16 v[76:79], v[196:199], v[44:47], v[76:79]\nds_read_b128 v[44:47], v33 offset:20480\n"
"buffer_load_dwordx4 v10, s[12:15], s53 offen lds\ns_waitcnt lgkmcnt(3)\nv_mfma_f32_16x16x32_bf16 v[64:67], v[200:203], v[48:51], v[64:67]\ns_add_u32 m0, s52, 24576\n"
"v_mfma_f32_16x16x32_bf16 v[68:71], v[204:207], v[48:51], v[68:71]\nds_read_b128 v[48:51], v34 offset:20480\ns_add_u32 s53, s49, 768\ns_waitcnt lgkmcnt(3)\n"
"v_mfma_f32_16x16x32_bf16 v[72:75], v[200:203], v[52:55], v[72:75]\nbuffer_load_dwordx4 v10, s[12:15], s53 offen lds\nv_mfma_f32_16x16x32_bf16 v[76:79], v[204:207], v[52:55], v[76:79]\nds_read_b128 v[52:55], v35 offset:20480\n"
"s_waitcnt lgkmcnt(3)\nv_mfma_f32_16x16x32_bf16 v[64:67], v[208:211], v[40:43], v[64:67]\nv_mfma_f32_16x16x32_bf16 v[68:71], v[212:215], v[40:43], v[68:71]\nds_read_b128 v[40:43], v32 offset:24576\n"
"s_waitcnt lgkmcnt(3)\nv_mfma_f32_16x16x32_bf16 v[72:75], v[208:211], v[44:47], v[72:75]\nv_mfma_f32_16x16x32_bf16 v[76:79], v[212:215], v[44:47], v[76:79]\nds_read_b128 v[44:47], v33 offset:24576\n"
"s_waitcnt lgkmcnt(3)\nv_mfma_f32_16x16x32_bf16 v[64:67], v[216:219], v[48:51], v[64:67]\ns_add_u32 s54, s51, 32768\ns_cmp_eq_u32 s54, 163840\n"
"v_mfma_f32_16x16x32_bf16 v[68:71], v[220:223], v[48:51], v[68:71]\ns_cselect_b32 s54, 0, s54\ns_sub_u32 s54, s54, s51\nds_read_b128 v[48:51], v34 offset:24576\n"
"s_waitcnt lgkmcnt(3)\nv_mfma_f32_16x16x32_bf16 v[72:75], v[216:219], v[52:55], v[72:75]\nv_mfma_f32_16x16x32_bf16 v[76:79], v[220:223], v[52:55], v[76:79]\nds_read_b128 v[52:55], v35 offset:24576\n"
"s_waitcnt lgkmcnt(3)\nv_mfma_f32_16x16x32_bf16 v[64:67], v[224:227], v[40:43], v[64:67]\nv_add_u32 v32, s54, v32\nv_mfma_f32_16x16x32_bf16 v[68:71], v[228:231], v[40:43], v[68:71]\n"
"ds_read_b128 v[40:43], v32 offset:0\ns_waitcnt lgkmcnt(3)\nv_mfma_f32_16x16x32_bf16 v[72:75], v[224:227], v[44:47], v[72:75]\nv_add_u32 v33, s54, v33\n"
"v_mfma_f32_16x16x32_bf16 v[76:79], v[228:231], v[44:47], v[76:79]\nds_read_b128 v[44:47], v33 offset:0\ns_waitcnt lgkmcnt(3)\nv_mfma_f32_16x16x32_bf16 v[64:67], v[232:235], v[48:51], v[64:67]\n"
"v_add_u32 v34, s54, v34\nv_mfma_f32_16x16x32_bf16 v[68:71], v[236:239], v[48:51], v[68:71]\nds_read_b128 v[48:51], v34 offset:0\ns_waitcnt lgkmcnt(3)\n"
"v_mfma_f32_16x16x32_bf16 v[72:75], v[232:235], v[52:55], v[72:75]\nv_add_u32 v35, s54, v35\nv_mfma_f32_16x16x32_bf16 v[76:79], v[236:239], v[52:55], v[76:79]\nds_read_b128 v[52:55], v35 offset:0\n"
"s_mov_b32 s52, s47\ns_add_u32 s47, s51, s32\ns_add_u32 s51, s51, s54\ns_add_u32 s49, s49, 28672\n"
"s_waitcnt vmcnt(7)\nfg0_pf:\nv_cmp_ne_u32 vcc, s28, v37\ns_cbranch_vccz fg0_pfok\n"
"s_add_u32 s55, s55, 1\ns_cmp_gt_u32 s55, 262144\ns_cbranch_scc0 fg0_pf2\ns_endpgm\n"
"fg0_pf2:\ns_sleep 1\nbuffer_load_dword v37, v20, s[24:27], 0 offen sc1\ns_waitcnt vmcnt(0)\n"
"s_branch fg0_pf\nfg0_pfok:\ns_barrier\ns_waitcnt lgkmcnt(3)\n"
"v_mfma_f32_16x16x32_bf16 v[96:99], v[128:131], v[40:43], 0\nv_mov_b32 v36, s48\ns_add_u32 s48, s48, 1\ns_nop 0\n"
"buffer_store_dword v36, v19, s[24:27], s33 offen sc1\nv_mfma_f32_16x16x32_bf16 v[100:103], v[132:135], v[40:43], 0\nds_read_b128 v[40:43], v32 offset:4096\ns_waitcnt lgkmcnt(3)\n"
"v_mfma_f32_16x16x32_bf16 v[104:107], v[128:131], v[44:47], 0\nv_mfma_f32_16x16x32_bf16 v[108:111], v[132:135], v[44:47], 0\nds_read_b128 v[44:47], v33 offset:4096\nv_cvt_pk_bf16_f32 v56, v64, v65\n"
"s_waitcnt lgkmcnt(3)\nv_mfma_f32_16x16x32_bf16 v[96:99], v[136:139], v[48:51], v[96:99]\nv_cvt_pk_bf16_f32 v57, v66, v67\nv_mfma_f32_16x16x32_bf16 v[100:103], v[140:143], v[48:51], v[100:103]\n"
"ds_read_b128 v[48:51], v34 offset:4096\nv_cvt_pk_bf16_f32 v58, v68, v69\ns_waitcnt lgkmcnt(3)\nv_mfma_f32_16x16x32_bf16 v[104:107], v[136:139], v[52:55], v[104:107]\n"
"v_cvt_pk_bf16_f32 v59, v70, v71\nv_mfma_f32_16x16x32_bf16 v[108:111], v[140:143], v[52:55], v[108:111]\nds_read_b128 v[52:55], v35 offset:4096\ns_waitcnt lgkmcnt(3)\n"
"v_mfma_f32_16x16x32_bf16 v[96:99], v[144:147], v[40:43], v[96:99]\nv_mfma_f32_16x16x32_bf16 v[100:103], v[148:151], v[40:43], v[100:103]\nds_read_b128 v[40:43], v32 offset:8192\nbuffer_store_dwordx4 v[56:59], v15, s[20:23], s50 offen sc1\n"
"s_waitcnt lgkmcnt(3)\nv_mfma_f32_16x16x32_bf16 v[104:107], v[144:147], v[44:47], v[104:107]\nv_cvt_pk_bf16_f32 v60, v72, v73\nv_mfma_f32_16x16x32_bf16 v[108:111], v[148:151], v[44:47], v[108:111]\n"
"ds_read_b128 v[44:47], v33 offset:8192\nv_cvt_pk_bf16_f32 v61, v74, v75\ns_waitcnt lgkmcnt(3)\nv_mfma_f32_16x16x32_bf16 v[96:99], v[152:155], v[48:51], v[96:99]\n"
"v_cvt_pk_bf16_f32 v62, v76, v77\nv_mfma_f32_16x16x32_bf16 v[100:103], v[156:159], v[48:51], v[100:103]\nds_read_b128 v[48:51], v34 offset:8192\nv_cvt_pk_bf16_f32 v63, v78, v79\n"
"s_waitcnt lgkmcnt(3)\nv_mfma_f32_16x16x32_bf16 v[104:107], v[152:155], v[52:55], v[104:107]\nv_mfma_f32_16x16x32_bf16 v[108:111], v[156:159], v[52:55], v[108:111]\nds_read_b128 v[52:55], v35 offset:8192\n"
"s_waitcnt lgkmcnt(3)\nv_mfma_f32_16x16x32_bf16 v[96:99], v[160:163], v[40:43], v[96:99]\nbuffer_store_dwordx4 v[60:63], v16, s[20:23], s50 offen sc1\nv_mfma_f32_16x16x32_bf16 v[100:103], v[164:167], v[40:43], v[100:103]\n"
"ds_read_b128 v[40:43], v32 offset:12288\ns_add_u32 s50, s50, s36\ns_waitcnt lgkmcnt(3)\nv_mfma_f32_16x16x32_bf16 v[104:107], v[160:163], v[44:47], v[104:107]\n"
"s_add_u32 m0, s52, 0\nv_mfma_f32_16x16x32_bf16 v[108:111], v[164:167], v[44:47], v[108:111]\nds_read_b128 v[44:47], v33 offset:12288\ns_add_u32 s53, s49, 0\n"
"s_waitcnt lgkmcnt(3)\nv_mfma_f32_16x16x32_bf16 v[96:99], v[168:171], v[48:51], v[96:99]\nbuffer_load_dwordx4 v10, s[12:15], s53 offen lds\nv_mfma_f32_16x16x32_bf16 v[100:103], v[172:175], v[48:51], v[100:103]\n"
"ds_read_b128 v[48:51], v34 offset:12288\ns_add_u32 m0, s52, 4096\ns_waitcnt lgkmcnt(3)\nv_mfma_f32_16x16x32_bf16 v[104:107], v[168:171], v[52:55], v[104:107]\n"
"s_add_u32 s53, s49, 128\nv_mfma_f32_16x16x32_bf16 v[108:111], v[172:175], v[52:55], v[108:111]\nds_read_b128 v[52:55], v35 offset:12288\nbuffer_load_dwordx4 v10, s[12:15], s53 offen lds\n"
"s_waitcnt lgkmcnt(3)\nv_mfma_f32_16x16x32_bf16 v[96:99], v[176:179], v[40:43], v[96:99]\ns_add_u32 m0, s52, 8192\nv_mfma_f32_16x16x32_bf16 v[100:103], v[180:183], v[40:43], v[100:103]\n"
"ds_read_b128 v[40:43], v32 offset:16384\ns_add_u32 s53, s49, 256\ns_waitcnt lgkmcnt(3)\nv_mfma_f32_16x16x32_bf16 v[104:107], v[176:179], v[44:47], v[104:107]\n"
"buffer_load_dwordx4 v10, s[12:15], s53 offen lds\nv_mfma_f32_16x16x32_bf16 v[108:111], v[180:183], v[44:47], v[108:111]\nds_read_b128 v[44:47], v33 offset:16384\ns_add_u32 m0, s52, 12288\n"
"s_waitcnt lgkmcnt(3)\nv_mfma_f32_16x16x32_bf16 v[96:99], v[184:187], v[48:51], v[96:99]\ns_add_u32 s53, s49, 384\nv_mfma_f32_16x16x32_bf16 v[100:103], v[188:191], v[48:51], v[100:103]\n"
"ds_read_b128 v[48:51], v34 offset:16384\nbuffer_load_dwordx4 v10, s[12:15], s53 offen lds\ns_waitcnt lgkmcnt(3)\nv_mfma_f32_16x16x32_bf16 v[104:107], v[184:187], v[52:55], v[104:107]\n"
"s_add_u32 m0, s52, 16384\nv_mfma_f32_16x16x32_bf16 v[108:111], v[188:191], v[52:55], v[108:111]\nds_read_b128 v[52:55], v35 offset:16384\ns_add_u32 s53, s49, 512\n"
"s_waitcnt lgkmcnt(3)\nv_mfma_f32_16x16x32_bf16 v[96:99], v[192:195], v[40:43], v[96:99]\nbuffer_load_dwordx4 v10, s[12:15], s53 offen lds\nv_mfma_f32_16x16x32_bf16 v[100:103], v[196:199], v[40:43], v[100:103]\n"
"ds_read_b128 v[40:43], v32 offset:20480\ns_add_u32 m0, s52, 20480\ns_waitcnt lgkmcnt(3)\nv_mfma_f32_16x16x32_bf16 v[104:107], v[192:195], v[44:47], v[104:107]\n"
"s_add_u32 s53, s49, 640\nv_mfma_f32_16x16x32_bf16 v[108:111], v[196:199], v[44:47], v[108:111]\nds_read_b128 v[44:47], v33 offset:20480\nbuffer_load_dwordx4 v10, s[12:15], s53 offen lds\n"
"s_waitcnt lgkmcnt(3)\nv_mfma_f32_16x16x32_bf16 v[96:99], v[200:203], v[48:51], v[96:99]\ns_add_u32 m0, s52, 24576\nv_mfma_f32_16x16x32_bf16 v[100:103], v[204:207], v[48:51], v[100:103]\n"
"ds_read_b128 v[48:51], v34 offset:20480\ns_add_u32 s53, s49, 768\ns_waitcnt lgkmcnt(3)\nv_mfma_f32_16x16x32_bf16 v[104:107], v[200:203], v[52:55], v[104:107]\n"
"buffer_load_dwordx4 v10, s[12:15], s53 offen lds\nv_mfma_f32_16x16x32_bf16 v[108:111], v[204:207], v[52:55], v[108:111]\nds_read_b128 v[52:55], v35 offset:20480\ns_mov_b32 s53, 0\n"
"buffer_load_dwordx4 a[0:3], v14, s[16:19], s53 offen sc1\ns_waitcnt lgkmcnt(3)\nv_mfma_f32_16x16x32_bf16 v[96:99], v[208:211], v[40:43], v[96:99]\ns_mov_b32 s53, 14336\n"
"buffer_load_dwordx4 a[4:7], v14, s[16:19], s53 offen sc1\nv_mfma_f32_16x16x32_bf16 v[100:103], v[212:215], v[40:43], v[100:103]\nds_read_b128 v[40:43], v32 offset:24576\ns_mov_b32 s53, 1024\n"
"buffer_load_dwordx4 a[8:11], v14, s[16:19], s53 offen sc1\ns_waitcnt lgkmcnt(3)\nv_mfma_f32_16x16x32_bf16 v[104:107], v[208:211], v[44:47], v[104:107]\ns_mov_b32 s53, 15360\n"
"buffer_load_dwordx4 a[12:15], v14, s[16:19], s53 offen sc1\nv_mfma_f32_16x16x32_bf16 v[108:111], v[212:215], v[44:47], v[108:111]\nds_read_b128 v[44:47], v33 offset:24576\ns_waitcnt lgkmcnt(3)\n"
"v_mfma_f32_16x16x32_bf16 v[96:99], v[216:219], v[48:51], v[96:99]\ns_add_u32 s54, s51, 32768\ns_cmp_eq_u32 s54, 163840\nv_mfma_f32_16x16x32_bf16 v[100:103], v[220:223], v[48:51], v[100:103]\n"
"s_cselect_b32 s54, 0, s54\ns_sub_u32 s54, s54, s51\nds_read_b128 v[48:51], v34 offset:24576\ns_waitcnt lgkmcnt(3)\n"
"v_mfma_f32_16x16x32_bf16 v[104:107], v[216:219], v[52:55], v[104:107]\nv_mfma_f32_16x16x32_bf16 v[108:111], v[220:223], v[52:55], v[108:111]\nds_read_b128 v[52:55], v35 offset:24576\ns_waitcnt lgkmcnt(3)\n"
"v_mfma_f32_16x16x32_bf16 v[96:99], v[224:227], v[40:43], v[96:99]\nv_add_u32 v32, s54, v32\nv_mfma_f32_16x16x32_bf16 v[100:103], v[228:231], v[40:43], v[100:103]\nds_read_b128 v[40:43], v32 offset:0\n"
"s_waitcnt lgkmcnt(3)\nv_mfma_f32_16x16x32_bf16 v[104:107], v[224:227], v[44:47], v[104:107]\nv_add_u32 v33, s54, v33\nv_mfma_f32_16x16x32_bf16 v[108:111], v[228:231], v[44:47], v[108:111]\n"
"ds_read_b128 v[44:47], v33 offset:0\ns_waitcnt lgkmcnt(3)\nv_mfma_f32_16x16x32_bf16 v[96:99], v[232:235], v[48:51], v[96:99]\nv_add_u32 v34, s54, v34\n"
"v_mfma_f32_16x16x32_bf16 v[100:103], v[236:239], v[48:51], v[100:103]\nds_read_b128 v[48:51], v34 offset:0\ns_waitcnt lgkmcnt(3)\nv_mfma_f32_16x16x32_bf16 v[104:107], v[232:235], v[52:55], v[104:107]\n"
"v_add_u32 v35, s54, v35\nv_mfma_f32_16x16x32_bf16 v[108:111], v[236:239], v[52:55], v[108:111]\nds_read_b128 v[52:55], v35 offset:0\ns_mov_b32 s52, s47\n"
"s_add_u32 s47, s51, s32\ns_add_u32 s51, s51, s54\ns_add_u32 s49, s49, 28672\ns_waitcnt vmcnt(11)\n"
"s_barrier\ns_waitcnt lgkmcnt(3)\nv_mfma_f32_16x16x32_bf16 v[64:67], v[128:131], v[40:43], 0\nv_mov_b32 v36, s48\n"
"s_add_u32 s48, s48, 1\ns_nop 0\nbuffer_store_dword v36, v19, s[24:27], s33 offen sc1\nv_mfma_f32_16x16x32_bf16 v[68:71], v[132:135], v[40:43], 0\n"
"ds_read_b128 v[40:43], v32 offset:4096\ns_waitcnt lgkmcnt(3)\nv_mfma_f32_16x16x32_bf16 v[72:75], v[128:131], v[44:47], 0\nv_mfma_f32_16x16x32_bf16 v[76:79], v[132:135], v[44:47], 0\n"
"ds_read_b128 v[44:47], v33 offset:4096\nv_cvt_pk_bf16_f32 v56, v96, v97\ns_waitcnt lgkmcnt(3)\nv_mfma_f32_16x16x32_bf16 v[64:67], v[136:139], v[48:51], v[64:67]\n"
"v_cvt_pk_bf16_f32 v57, v98, v99\nv_mfma_f32_16x16x32_bf16 v[68:71], v[140:143], v[48:51], v[68:71]\nds_read_b128 v[48:51], v34 offset:4096\nv_cvt_pk_bf16_f32 v58, v100, v101\n"
"s_waitcnt lgkmcnt(3)\nv_mfma_f32_16x16x32_bf16 v[72:75], v[136:139], v[52:55], v[72:75]\nv_cvt_pk_bf16_f32 v59, v102, v103\nv_mfma_f32_16x16x32_bf16 v[76:79], v[140:143], v[52:55], v[76:79]\n"
"ds_read_b128 v[52:55], v35 offset:4096\ns_waitcnt lgkmcnt(3)\nv_mfma_f32_16x16x32_bf16 v[64:67], v[144:147], v[40:43], v[64:67]\nv_mfma_f32_16x16x32_bf16 v[68:71], v[148:151], v[40:43], v[68:71]\n"
"ds_read_b128 v[40:43], v32 offset:8192\nbuffer_store_dwordx4 v[56:59], v15, s[20:23], s50 offen sc1\ns_waitcnt lgkmcnt(3)\nv_mfma_f32_16x16x32_bf16 v[72:75], v[144:147], v[44:47], v[72:75]\n"
"v_cvt_pk_bf16_f32 v60, v104, v105\nv_mfma_f32_16x16x32_bf16 v[76:79], v[148:151], v[44:47], v[76:79]\nds_read_b128 v[44:47], v33 offset:8192\nv_cvt_pk_bf16_f32 v61, v106, v107\n"
"s_waitcnt lgkmcnt(3)\nv_mfma_f32_16x16x32_bf16 v[64:67], v[152:155], v[48:51], v[64:67]\nv_cvt_pk_bf16_f32 v62, v108, v109\nv_mfma_f32_16x16x32_bf16 v[68:71], v[156:159], v[48:51], v[68:71]\n"
"ds_read_b128 v[48:51], v34 offset:8192\nv_cvt_pk_bf16_f32 v63, v110, v111\ns_waitcnt lgkmcnt(3)\nv_mfma_f32_16x16x32_bf16 v[72:75], v[152:155], v[52:55], v[72:75]\n"
"v_mfma_f32_16x16x32_bf16 v[76:79], v[156:159], v[52:55], v[76:79]\nds_read_b128 v[52:55], v35 offset:8192\ns_waitcnt lgkmcnt(3)\nv_mfma_f32_16x16x32_bf16 v[64:67], v[160:163], v[40:43], v[64:67]\n"
"buffer_store_dwordx4 v[60:63], v16, s[20:23], s50 offen sc1\nv_mfma_f32_16x16x32_bf16 v[68:71], v[164:167], v[40:43], v[68:71]\nds_read_b128 v[40:43], v32 offset:12288\ns_add_u32 s50, s50, s36\n"
"s_waitcnt lgkmcnt(3)\nv_mfma_f32_16x16x32_bf16 v[72:75], v[160:163], v[44:47], v[72:75]\ns_add_u32 m0, s52, 0\nv_mfma_f32_16x16x32_bf16 v[76:79], v[164:167], v[44:47], v[76:79]\n"
"ds_read_b128 v[44:47], v33 offset:12288\ns_add_u32 s53, s49, 0\ns_waitcnt lgkmcnt(3)\nv_mfma_f32_16x16x32_bf16 v[64:67], v[168:171], v[48:51], v[64:67]\n"
"buffer_load_dwordx4 v10, s[12:15], s53 offen lds\nv_mfma_f32_16x16x32_bf16 v[68:71], v[172:175], v[48:51], v[68:71]\nds_read_b128 v[48:51], v34 offset:12288\ns_add_u32 m0, s52, 4096\n"
"s_waitcnt lgkmcnt(3)\nv_mfma_f32_16x16x32_bf16 v[72:75], v[168:171], v[52:55], v[72:75]\ns_add_u32 s53, s49, 128\nv_mfma_f32_16x16x32_bf16 v[76:79], v[172:175], v[52:55], v[76:79]\n"
"ds_read_b128 v[52:55], v35 offset:12288\nbuffer_load_dwordx4 v10, s[12:15], s53 offen lds\ns_waitcnt lgkmcnt(3)\nv_mfma_f32_16x16x32_bf16 v[64:67], v[176:179], v[40:43], v[64:67]\n"
"s_add_u32 m0, s52, 8192\nv_mfma_f32_16x16x32_bf16 v[68:71], v[180:183], v[40:43], v[68:71]\nds_read_b128 v[40:43], v32 offset:16384\ns_add_u32 s53, s49, 256\n"
"s_waitcnt lgkmcnt(3)\nv_mfma_f32_16x16x32_bf16 v[72:75], v[176:179], v[44:47], v[72:75]\nbuffer_load_dwordx4 v10, s[12:15], s53 offen lds\nv_mfma_f32_16x16x32_bf16 v[76:79], v[180:183], v[44:47], v[76:79]\n"
"ds_read_b128 v[44:47], v33 offset:16384\ns_add_u32 m0, s52, 12288\ns_waitcnt lgkmcnt(3)\nv_mfma_f32_16x16x32_bf16 v[64:67], v[184:187], v[48:51], v[64:67]\n"
"s_add_u32 s53, s49, 384\nv_mfma_f32_16x16x32_bf16 v[68:71], v[188:191], v[48:51], v[68:71]\nds_read_b128 v[48:51], v34 offset:16384\nbuffer_load_dwordx4 v10, s[12:15], s53 offen lds\n"
"s_waitcnt lgkmcnt(3)\nv_mfma_f32_16x16x32_bf16 v[72:75], v[184:187], v[52:55], v[72:75]\ns_add_u32 m0, s52, 16384\nv_mfma_f32_16x16x32_bf16 v[76:79], v[188:191], v[52:55], v[76:79]\n"
"ds_read_b128 v[52:55], v35 offset:16384\ns_add_u32 s53, s49, 512\ns_waitcnt lgkmcnt(3)\nv_mfma_f32_16x16x32_bf16 v[64:67], v[192:195], v[40:43], v[64:67]\n"
"buffer_load_dwordx4 v10, s[12:15], s53 offen lds\nv_mfma_f32_16x16x32_bf16 v[68:71], v[196:199], v[40:43], v[68:71]\nds_read_b128 v[40:43], v32 offset:20480\ns_add_u32 m0, s52, 20480\n"
"s_waitcnt lgkmcnt(3)\nv_mfma_f32_16x16x32_bf16 v[72:75], v[192:195], v[44:47], v[72:75]\ns_add_u32 s53, s49, 640\nv_mfma_f32_16x16x32_bf16 v[76:79], v[196:199], v[44:47], v[76:79]\n"
"ds_read_b128 v[44:47], v33 offset:20480\nbuffer_load_dwordx4 v10, s[12:15], s53 offen lds\ns_waitcnt lgkmcnt(3)\nv_mfma_f32_16x16x32_bf16 v[64:67], v[200:203], v[48:51], v[64:67]\n"
"s_add_u32 m0, s52, 24576\nv_mfma_f32_16x16x32_bf16 v[68:71], v[204:207], v[48:51], v[68:71]\nds_read_b128 v[48:51], v34 offset:20480\ns_add_u32 s53, s49, 768\n"
"s_waitcnt lgkmcnt(3)\nv_mfma_f32_16x16x32_bf16 v[72:75], v[200:203], v[52:55], v[72:75]\nbuffer_load_dwordx4 v10, s[12:15], s53 offen lds\nv_mfma_f32_16x16x32_bf16 v[76:79], v[204:207], v[52:55], v[76:79]\n"
"ds_read_b128 v[52:55], v35 offset:20480\ns_mov_b32 s53, 2048\nbuffer_load_dwordx4 a[16:19], v14, s[16:19], s53 offen sc1\ns_waitcnt lgkmcnt(3)\n"
"v_mfma_f32_16x16x32_bf16 v[64:67], v[208:211], v[40:43], v[64:67]\ns_mov_b32 s53, 16384\nbuffer_load_dwordx4 a[20:23], v14, s[16:19], s53 offen sc1\nv_mfma_f32_16x16x32_bf16 v[68:71], v[212:215], v[40:43], v[68:71]\n"
"ds_read_b128 v[40:43], v32 offset:24576\ns_mov_b32 s53, 3072\nbuffer_load_dwordx4 a[24:27], v14, s[16:19], s53 offen sc1\ns_waitcnt lgkmcnt(3)\n"
"v_mfma_f32_16x16x32_bf16 v[72:75], v[208:211], v[44:47], v[72:75]\ns_mov_b32 s53, 17408\nbuffer_load_dwordx4 a[28:31], v14, s[16:19], s53 offen sc1\nv_mfma_f32_16x16x32_bf16 v[76:79], v[212:215], v[44:47], v[76:79]\n"
"ds_read_b128 v[44:47], v33 offset:24576\ns_waitcnt lgkmcnt(3)\nv_mfma_f32_16x16x32_bf16 v[64:67], v[216:219], v[48:51], v[64:67]\ns_add_u32 s54, s51, 32768\n"
"s_cmp_eq_u32 s54, 163840\nv_mfma_f32_16x16x32_bf16 v[68:71], v[220:223], v[48:51], v[68:71]\ns_cselect_b32 s54, 0, s54\ns_sub_u32 s54, s54, s51\n"
"ds_read_b128 v[48:51], v34 offset:24576\ns_waitcnt lgkmcnt(3)\nv_mfma_f32_16x16x32_bf16 v[72:75], v[216:219], v[52:55], v[72:75]\nv_mfma_f32_16x16x32_bf16 v[76:79], v[220:223], v[52:55], v[76:79]\n"
"ds_read_b128 v[52:55], v35 offset:24576\ns_waitcnt lgkmcnt(3)\nv_mfma_f32_16x16x32_bf16 v[64:67], v[224:227], v[40:43], v[64:67]\nv_add_u32 v32, s54, v32\n"
"v_mfma_f32_16x16x32_bf16 v[68:71], v[228:231], v[40:43], v[68:71]\nds_read_b128 v[40:43], v32 offset:0\ns_waitcnt lgkmcnt(3)\nv_mfma_f32_16x16x32_bf16 v[72:75], v[224:227], v[44:47], v[72:75]\n"
"v_add_u32 v33, s54, v33\nv_mfma_f32_16x16x32_bf16 v[76:79], v[228:231], v[44:47], v[76:79]\nds_read_b128 v[44:47], v33 offset:0\ns_waitcnt lgkmcnt(3)\n"
"v_mfma_f32_16x16x32_bf16 v[64:67], v[232:235], v[48:51], v[64:67]\nv_add_u32 v34, s54, v34\nv_mfma_f32_16x16x32_bf16 v[68:71], v[236:239], v[48:51], v[68:71]\nds_read_b128 v[48:51], v34 offset:0\n"
"s_waitcnt lgkmcnt(3)\nv_mfma_f32_16x16x32_bf16 v[72:75], v[232:235], v[52:55], v[72:75]\nv_add_u32 v35, s54, v35\nv_mfma_f32_16x16x32_bf16 v[76:79], v[236:239], v[52:55], v[76:79]\n"
"ds_read_b128 v[52:55], v35 offset:0\ns_mov_b32 s52, s47\ns_add_u32 s47, s51, s32\ns_add_u32 s51, s51, s54\n"
"s_add_u32 s49, s49, 28672\ns_waitcnt vmcnt(11)\ns_barrier\ns_waitcnt lgkmcnt(3)\n"
"v_mfma_f32_16x16x32_bf16 v[96:99], v[128:131], v[40:43], 0\nv_mov_b32 v36, s48\ns_add_u32 s48, s48, 1\ns_nop 0\n"
"buffer_store_dword v36, v19, s[24:27], s33 offen sc1\nv_mfma_f32_16x16x32_bf16 v[100:103], v[132:135], v[40:43], 0\nds_read_b128 v[40:43], v32 offset:4096\ns_waitcnt lgkmcnt(3)\n"
"v_mfma_f32_16x16x32_bf16 v[104:107], v[128:131], v[44:47], 0\nv_mfma_f32_16x16x32_bf16 v[108:111], v[132:135], v[44:47], 0\nds_read_b128 v[44:47], v33 offset:4096\nv_cvt_pk_bf16_f32 v56, v64, v65\n"
"s_waitcnt lgkmcnt(3)\nv_mfma_f32_16x16x32_bf16 v[96:99], v[136:139], v[48:51], v[96:99]\nv_cvt_pk_bf16_f32 v57, v66, v67\nv_mfma_f32_16x16x32_bf16 v[100:103], v[140:143], v[48:51], v[100:103]\n"
"ds_read_b128 v[48:51], v34 offset:4096\nv_cvt_pk_bf16_f32 v58, v68, v69\ns_waitcnt lgkmcnt(3)\nv_mfma_f32_16x16x32_bf16 v[104:107], v[136:139], v[52:55], v[104:107]\n"
"v_cvt_pk_bf16_f32 v59, v70, v71\nv_mfma_f32_16x16x32_bf16 v[108:111], v[140:143], v[52:55], v[108:111]\nds_read_b128 v[52:55], v35 offset:4096\ns_waitcnt lgkmcnt(3)\n"
"v_mfma_f32_16x16x32_bf16 v[96:99], v[144:147], v[40:43], v[96:99]\nv_mfma_f32_16x16x32_bf16 v[100:103], v[148:151], v[40:43], v[100:103]\nds_read_b128 v[40:43], v32 offset:8192\nbuffer_store_dwordx4 v[56:59], v15, s[20:23], s50 offen sc1\n"
"s_waitcnt lgkmcnt(3)\nv_mfma_f32_16x16x32_bf16 v[104:107], v[144:147], v[44:47], v[104:107]\nv_cvt_pk_bf16_f32 v60, v72, v73\nv_mfma_f32_16x16x32_bf16 v[108:111], v[148:151], v[44:47], v[108:111]\n"
"ds_read_b128 v[44:47], v33 offset:8192\nv_cvt_pk_bf16_f32 v61, v74, v75\ns_waitcnt lgkmcnt(3)\nv_mfma_f32_16x16x32_bf16 v[96:99], v[152:155], v[48:51], v[96:99]\n"
"v_cvt_pk_bf16_f32 v62, v76, v77\nv_mfma_f32_16x16x32_bf16 v[100:103], v[156:159], v[48:51], v[100:103]\nds_read_b128 v[48:51], v34 offset:8192\nv_cvt_pk_bf16_f32 v63, v78, v79\n"
"s_waitcnt lgkmcnt(3)\nv_mfma_f32_16x16x32_bf16 v[104:107], v[152:155], v[52:55], v[104:107]\nv_mfma_f32_16x16x32_bf16 v[108:111], v[156:159], v[52:55], v[108:111]\nds_read_b128 v[52:55], v35 offset:8192\n"
"s_waitcnt lgkmcnt(3)\nv_mfma_f32_16x16x32_bf16 v[96:99], v[160:163], v[40:43], v[96:99]\nbuffer_store_dwordx4 v[60:63], v16, s[20:23], s50 offen sc1\nv_mfma_f32_16x16x32_bf16 v[100:103], v[164:167], v[40:43], v[100:103]\n"
"ds_read_b128 v[40:43], v32 offset:12288\ns_add_u32 s50, s50, s36\ns_waitcnt lgkmcnt(3)\nv_mfma_f32_16x16x32_bf16 v[104:107], v[160:163], v[44:47], v[104:107]\n"
"s_add_u32 m0, s52, 0\nv_mfma_f32_16x16x32_bf16 v[108:111], v[164:167], v[44:47], v[108:111]\nds_read_b128 v[44:47], v33 offset:12288\ns_add_u32 s53, s49, 0\n"
"s_waitcnt lgkmcnt(3)\nv_mfma_f32_16x16x32_bf16 v[96:99], v[168:171], v[48:51], v[96:99]\nbuffer_load_dwordx4 v10, s[12:15], s53 offen lds\nv_mfma_f32_16x16x32_bf16 v[100:103], v[172:175], v[48:51], v[100:103]\n"
"ds_read_b128 v[48:51], v34 offset:12288\ns_add_u32 m0, s52, 4096\ns_waitcnt lgkmcnt(3)\nv_mfma_f32_16x16x32_bf16 v[104:107], v[168:171], v[52:55], v[104:107]\n"
"s_add_u32 s53, s49, 128\nv_mfma_f32_16x16x32_bf16 v[108:111], v[172:175], v[52:55], v[108:111]\nds_read_b128 v[52:55], v35 offset:12288\nbuffer_load_dwordx4 v10, s[12:15], s53 offen lds\n"
"s_waitcnt lgkmcnt(3)\nv_mfma_f32_16x16x32_bf16 v[96:99], v[176:179], v[40:43], v[96:99]\ns_add_u32 m0, s52, 8192\nv_mfma_f32_16x16x32_bf16 v[100:103], v[180:183], v[40:43], v[100:103]\n"
"ds_read_b128 v[40:43], v32 offset:16384\ns_add_u32 s53, s49, 256\ns_waitcnt lgkmcnt(3)\nv_mfma_f32_16x16x32_bf16 v[104:107], v[176:179], v[44:47], v[104:107]\n"
"buffer_load_dwordx4 v10, s[12:15], s53 offen lds\nv_mfma_f32_16x16x32_bf16 v[108:111], v[180:183], v[44:47], v[108:111]\nds_read_b128 v[44:47], v33 offset:16384\ns_add_u32 m0, s52, 12288\n"
"s_waitcnt lgkmcnt(3)\nv_mfma_f32_16x16x32_bf16 v[96:99], v[184:187], v[48:51], v[96:99]\ns_add_u32 s53, s49, 384\nv_mfma_f32_16x16x32_bf16 v[100:103], v[188:191], v[48:51], v[100:103]\n"
"ds_read_b128 v[48:51], v34 offset:16384\nbuffer_load_dwordx4 v10, s[12:15], s53 offen lds\ns_waitcnt lgkmcnt(3)\nv_mfma_f32_16x16x32_bf16 v[104:107], v[184:187], v[52:55], v[104:107]\n"
"s_add_u32 m0, s52, 16384\nv_mfma_f32_16x16x32_bf16 v[108:111], v[188:191], v[52:55], v[108:111]\nds_read_b128 v[52:55], v35 offset:16384\ns_add_u32 s53, s49, 512\n"
"s_waitcnt lgkmcnt(3)\nv_mfma_f32_16x16x32_bf16 v[96:99], v[192:195], v[40:43], v[96:99]\nbuffer_load_dwordx4 v10, s[12:15], s53 offen lds\nv_mfma_f32_16x16x32_bf16 v[100:103], v[196:199], v[40:43], v[100:103]\n"
"ds_read_b128 v[40:43], v32 offset:20480\ns_add_u32 m0, s52, 20480\ns_waitcnt lgkmcnt(3)\nv_mfma_f32_16x16x32_bf16 v[104:107], v[192:195], v[44:47], v[104:107]\n"
"s_add_u32 s53, s49, 640\nv_mfma_f32_16x16x32_bf16 v[108:111], v[196:199], v[44:47], v[108:111]\nds_read_b128 v[44:47], v33 offset:20480\nbuffer_load_dwordx4 v10, s[12:15], s53 offen lds\n"
"s_waitcnt lgkmcnt(3)\nv_mfma_f32_16x16x32_bf16 v[96:99], v[200:203], v[48:51], v[96:99]\ns_add_u32 m0, s52, 24576\nv_mfma_f32_16x16x32_bf16 v[100:103], v[204:207], v[48:51], v[100:103]\n"
"ds_read_b128 v[48:51], v34 offset:20480\ns_add_u32 s53, s49, 768\ns_waitcnt lgkmcnt(3)\nv_mfma_f32_16x16x32_bf16 v[104:107], v[200:203], v[52:55], v[104:107]\n"
"buffer_load_dwordx4 v10, s[12:15], s53 offen lds\nv_mfma_f32_16x16x32_bf16 v[108:111], v[204:207], v[52:55], v[108:111]\nds_read_b128 v[52:55], v35 offset:20480\ns_mov_b32 s53, 4096\n"
"buffer_load_dwordx4 a[32:35], v14, s[16:19], s53 offen sc1\ns_waitcnt lgkmcnt(3)\nv_mfma_f32_16x16x32_bf16 v[96:99], v[208:211], v[40:43], v[96:99]\ns_mov_b32 s53, 18432\n"
"buffer_load_dwordx4 a[36:39], v14, s[16:19], s53 offen sc1\nv_mfma_f32_16x16x32_bf16 v[100:103], v[212:215], v[40:43], v[100:103]\nds_read_b128 v[40:43], v32 offset:24576\ns_mov_b32 s53, 5120\n"
"buffer_load_dwordx4 a[40:43], v14, s[16:19], s53 offen sc1\ns_waitcnt lgkmcnt(3)\nv_mfma_f32_16x16x32_bf16 v[104:107], v[208:211], v[44:47], v[104:107]\ns_mov_b32 s53, 19456\n"
"buffer_load_dwordx4 a[44:47], v14, s[16:19], s53 offen sc1\nv_mfma_f32_16x16x32_bf16 v[108:111], v[212:215], v[44:47], v[108:111]\nds_read_b128 v[44:47], v33 offset:24576\ns_waitcnt lgkmcnt(3)\n"
"v_mfma_f32_16x16x32_bf16 v[96:99], v[216:219], v[48:51], v[96:99]\ns_add_u32 s54, s51, 32768\ns_cmp_eq_u32 s54, 163840\nv_mfma_f32_16x16x32_bf16 v[100:103], v[220:223], v[48:51], v[100:103]\n"
"s_cselect_b32 s54, 0, s54\ns_sub_u32 s54, s54, s51\nds_read_b128 v[48:51], v34 offset:24576\ns_waitcnt lgkmcnt(3)\n"
"v_mfma_f32_16x16x32_bf16 v[104:107], v[216:219], v[52:55], v[104:107]\nv_mfma_f32_16x16x32_bf16 v[108:111], v[220:223], v[52:55], v[108:111]\nds_read_b128 v[52:55], v35 offset:24576\ns_waitcnt lgkmcnt(3)\n"
"v_mfma_f32_16x16x32_bf16 v[96:99], v[224:227], v[40:43], v[96:99]\nv_add_u32 v32, s54, v32\nv_mfma_f32_16x16x32_bf16 v[100:103], v[228:231], v[40:43], v[100:103]\nds_read_b128 v[40:43], v32 offset:0\n"
"s_waitcnt lgkmcnt(3)\nv_mfma_f32_16x16x32_bf16 v[104:107], v[224:227], v[44:47], v[104:107]\nv_add_u32 v33, s54, v33\nv_mfma_f32_16x16x32_bf16 v[108:111], v[228:231], v[44:47], v[108:111]\n"
"ds_read_b128 v[44:47], v33 offset:0\ns_waitcnt lgkmcnt(3)\nv_mfma_f32_16x16x32_bf16 v[96:99], v[232:235], v[48:51], v[96:99]\nv_add_u32 v34, s54, v34\n"
"v_mfma_f32_16x16x32_bf16 v[100:103], v[236:239], v[48:51], v[100:103]\nds_read_b128 v[48:51], v34 offset:0\ns_waitcnt lgkmcnt(3)\nv_mfma_f32_16x16x32_bf16 v[104:107], v[232:235], v[52:55], v[104:107]\n"
"v_add_u32 v35, s54, v35\nv_mfma_f32_16x16x32_bf16 v[108:111], v[236:239], v[52:55], v[108:111]\nds_read_b128 v[52:55], v35 offset:0\ns_mov_b32 s52, s47\n"
"s_add_u32 s47, s51, s32\ns_add_u32 s51, s51, s54\ns_add_u32 s49, s49, 28672\ns_waitcnt vmcnt(11)\n"
"s_barrier\ns_waitcnt lgkmcnt(3)\nv_mfma_f32_16x16x32_bf16 v[64:67], v[128:131], v[40:43], 0\nv_mov_b32 v36, s48\n"
"s_add_u32 s48, s48, 1\ns_nop 0\nbuffer_store_dword v36, v19, s[24:27], s33 offen sc1\nv_mfma_f32_16x16x32_bf16 v[68:71], v[132:135], v[40:43], 0\n"
"ds_read_b128 v[40:43], v32 offset:4096\ns_waitcnt lgkmcnt(3)\nv_mfma_f32_16x16x32_bf16 v[72:75], v[128:131], v[44:47], 0\nv_mfma_f32_16x16x32_bf16 v[76:79], v[132:135], v[44:47], 0\n"
"ds_read_b128 v[44:47], v33 offset:4096\nv_cvt_pk_bf16_f32 v56, v96, v97\ns_waitcnt lgkmcnt(3)\nv_mfma_f32_16x16x32_bf16 v[64:67], v[136:139], v[48:51], v[64:67]\n"
"v_cvt_pk_bf16_f32 v57, v98, v99\nv_mfma_f32_16x16x32_bf16 v[68:71], v[140:143], v[48:51], v[68:71]\nds_read_b128 v[48:51], v34 offset:4096\nv_cvt_pk_bf16_f32 v58, v100, v101\n"
"s_waitcnt lgkmcnt(3)\nv_mfma_f32_16x16x32_bf16 v[72:75], v[136:139], v[52:55], v[72:75]\nv_cvt_pk_bf16_f32 v59, v102, v103\nv_mfma_f32_16x16x32_bf16 v[76:79], v[140:143], v[52:55], v[76:79]\n"
"ds_read_b128 v[52:55], v35 offset:4096\ns_waitcnt lgkmcnt(3)\nv_mfma_f32_16x16x32_bf16 v[64:67], v[144:147], v[40:43], v[64:67]\nv_mfma_f32_16x16x32_bf16 v[68:71], v[148:151], v[40:43], v[68:71]\n"
"ds_read_b128 v[40:43], v32 offset:8192\nbuffer_store_dwordx4 v[56:59], v15, s[20:23], s50 offen sc1\ns_waitcnt lgkmcnt(3)\nv_mfma_f32_16x16x32_bf16 v[72:75], v[144:147], v[44:47], v[72:75]\n"
"v_cvt_pk_bf16_f32 v60, v104, v105\nv_mfma_f32_16x16x32_bf16 v[76:79], v[148:151], v[44:47], v[76:79]\nds_read_b128 v[44:47], v33 offset:8192\nv_cvt_pk_bf16_f32 v61, v106, v107\n"
"s_waitcnt lgkmcnt(3)\nv_mfma_f32_16x16x32_bf16 v[64:67], v[152:155], v[48:51], v[64:67]\nv_cvt_pk_bf16_f32 v62, v108, v109\nv_mfma_f32_16x16x32_bf16 v[68:71], v[156:159], v[48:51], v[68:71]\n"
"ds_read_b128 v[48:51], v34 offset:8192\nv_cvt_pk_bf16_f32 v63, v110, v111\ns_waitcnt lgkmcnt(3)\nv_mfma_f32_16x16x32_bf16 v[72:75], v[152:155], v[52:55], v[72:75]\n"
"v_mfma_f32_16x16x32_bf16 v[76:79], v[156:159], v[52:55], v[76:79]\nds_read_b128 v[52:55], v35 offset:8192\ns_waitcnt lgkmcnt(3)\nv_mfma_f32_16x16x32_bf16 v[64:67], v[160:163], v[40:43], v[64:67]\n"
"buffer_store_dwordx4 v[60:63], v16, s[20:23], s50 offen sc1\nv_mfma_f32_16x16x32_bf16 v[68:71], v[164:167], v[40:43], v[68:71]\nds_read_b128 v[40:43], v32 offset:12288\ns_add_u32 s50, s50, s36\n"
"s_waitcnt lgkmcnt(3)\nv_mfma_f32_16x16x32_bf16 v[72:75], v[160:163], v[44:47], v[72:75]\ns_add_u32 m0, s52, 0\nv_mfma_f32_16x16x32_bf16 v[76:79], v[164:167], v[44:47], v[76:79]\n"
"ds_read_b128 v[44:47], v33 offset:12288\ns_add_u32 s53, s49, 0\ns_waitcnt lgkmcnt(3)\nv_mfma_f32_16x16x32_bf16 v[64:67], v[168:171], v[48:51], v[64:67]\n"
"buffer_load_dwordx4 v10, s[12:15], s53 offen lds\nv_mfma_f32_16x16x32_bf16 v[68:71], v[172:175], v[48:51], v[68:71]\nds_read_b128 v[48:51], v34 offset:12288\ns_add_u32 m0, s52, 4096\n"
"s_waitcnt lgkmcnt(3)\nv_mfma_f32_16x16x32_bf16 v[72:75], v[168:171], v[52:55], v[72:75]\ns_add_u32 s53, s49, 128\nv_mfma_f32_16x16x32_bf16 v[76:79], v[172:175], v[52:55], v[76:79]\n"
"ds_read_b128 v[52:55], v35 offset:12288\nbuffer_load_dwordx4 v10, s[12:15], s53 offen lds\ns_waitcnt lgkmcnt(3)\nv_mfma_f32_16x16x32_bf16 v[64:67], v[176:179], v[40:43], v[64:67]\n"
"s_add_u32 m0, s52, 8192\nv_mfma_f32_16x16x32_bf16 v[68:71], v[180:183], v[40:43], v[68:71]\nds_read_b128 v[40:43], v32 offset:16384\ns_add_u32 s53, s49, 256\n"
"s_waitcnt lgkmcnt(3)\nv_mfma_f32_16x16x32_bf16 v[72:75], v[176:179], v[44:47], v[72:75]\nbuffer_load_dwordx4 v10, s[12:15], s53 offen lds\nv_mfma_f32_16x16x32_bf16 v[76:79], v[180:183], v[44:47], v[76:79]\n"
"ds_read_b128 v[44:47], v33 offset:16384\ns_add_u32 m0, s52, 12288\ns_waitcnt lgkmcnt(3)\nv_mfma_f32_16x16x32_bf16 v[64:67], v[184:187], v[48:51], v[64:67]\n"
"s_add_u32 s53, s49, 384\nv_mfma_f32_16x16x32_bf16 v[68:71], v[188:191], v[48:51], v[68:71]\nds_read_b128 v[48:51], v34 offset:16384\nbuffer_load_dwordx4 v10, s[12:15], s53 offen lds\n"
"s_waitcnt lgkmcnt(3)\nv_mfma_f32_16x16x32_bf16 v[72:75], v[184:187], v[52:55], v[72:75]\ns_add_u32 m0, s52, 16384\nv_mfma_f32_16x16x32_bf16 v[76:79], v[188:191], v[52:55], v[76:79]\n"
"ds_read_b128 v[52:55], v35 offset:16384\ns_add_u32 s53, s49, 512\ns_waitcnt lgkmcnt(3)\nv_mfma_f32_16x16x32_bf16 v[64:67], v[192:195], v[40:43], v[64:67]\n"
"buffer_load_dwordx4 v10, s[12:15], s53 offen lds\nv_mfma_f32_16x16x32_bf16 v[68:71], v[196:199], v[40:43], v[68:71]\nds_read_b128 v[40:43], v32 offset:20480\ns_add_u32 m0, s52, 20480\n"
"s_waitcnt lgkmcnt(3)\nv_mfma_f32_16x16x32_bf16 v[72:75], v[192:195], v[44:47], v[72:75]\ns_add_u32 s53, s49, 640\nv_mfma_f32_16x16x32_bf16 v[76:79], v[196:199], v[44:47], v[76:79]\n"
"ds_read_b128 v[44:47], v33 offset:20480\nbuffer_load_dwordx4 v10, s[12:15], s53 offen lds\ns_waitcnt lgkmcnt(3)\nv_mfma_f32_16x16x32_bf16 v[64:67], v[200:203], v[48:51], v[64:67]\n"
"s_add_u32 m0, s52, 24576\nv_mfma_f32_16x16x32_bf16 v[68:71], v[204:207], v[48:51], v[68:71]\nds_read_b128 v[48:51], v34 offset:20480\ns_add_u32 s53, s49, 768\n"
"s_waitcnt lgkmcnt(3)\nv_mfma_f32_16x16x32_bf16 v[72:75], v[200:203], v[52:55], v[72:75]\nbuffer_load_dwordx4 v10, s[12:15], s53 offen lds\nv_mfma_f32_16x16x32_bf16 v[76:79], v[204:207], v[52:55], v[76:79]\n"
"ds_read_b128 v[52:55], v35 offset:20480\ns_mov_b32 s53, 6144\nbuffer_load_dwordx4 a[48:51], v14, s[16:19], s53 offen sc1\ns_waitcnt lgkmcnt(3)\n"
"v_mfma_f32_16x16x32_bf16 v[64:67], v[208:211], v[40:43], v[64:67]\ns_mov_b32 s53, 20480\nbuffer_load_dwordx4 a[52:55], v14, s[16:19], s53 offen sc1\nv_mfma_f32_16x16x32_bf16 v[68:71], v[212:215], v[40:43], v[68:71]\n"
"ds_read_b128 v[40:43], v32 offset:24576\ns_mov_b32 s53, 7168\nbuffer_load_dwordx4 a[56:59], v14, s[16:19], s53 offen sc1\ns_waitcnt lgkmcnt(3)\n"
"v_mfma_f32_16x16x32_bf16 v[72:75], v[208:211], v[44:47], v[72:75]\ns_mov_b32 s53, 21504\nbuffer_load_dwordx4 a[60:63], v14, s[16:19], s53 offen sc1\nv_mfma_f32_16x16x32_bf16 v[76:79], v[212:215], v[44:47], v[76:79]\n"
"ds_read_b128 v[44:47], v33 offset:24576\ns_waitcnt lgkmcnt(3)\nv_mfma_f32_16x16x32_bf16 v[64:67], v[216:219], v[48:51], v[64:67]\ns_add_u32 s54, s51, 32768\n"
"s_cmp_eq_u32 s54, 163840\nv_mfma_f32_16x16x32_bf16 v[68:71], v[220:223], v[48:51], v[68:71]\ns_cselect_b32 s54, 0, s54\ns_sub_u32 s54, s54, s51\n"
"ds_read_b128 v[48:51], v34 offset:24576\ns_waitcnt lgkmcnt(3)\nv_mfma_f32_16x16x32_bf16 v[72:75], v[216:219], v[52:55], v[72:75]\nv_mfma_f32_16x16x32_bf16 v[76:79], v[220:223], v[52:55], v[76:79]\n"
"ds_read_b128 v[52:55], v35 offset:24576\ns_waitcnt lgkmcnt(3)\nv_mfma_f32_16x16x32_bf16 v[64:67], v[224:227], v[40:43], v[64:67]\nv_add_u32 v32, s54, v32\n"
"v_mfma_f32_16x16x32_bf16 v[68:71], v[228:231], v[40:43], v[68:71]\nds_read_b128 v[40:43], v32 offset:0\ns_waitcnt lgkmcnt(3)\nv_mfma_f32_16x16x32_bf16 v[72:75], v[224:227], v[44:47], v[72:75]\n"
"v_add_u32 v33, s54, v33\nv_mfma_f32_16x16x32_bf16 v[76:79], v[228:231], v[44:47], v[76:79]\nds_read_b128 v[44:47], v33 offset:0\ns_waitcnt lgkmcnt(3)\n"
"v_mfma_f32_16x16x32_bf16 v[64:67], v[232:235], v[48:51], v[64:67]\nv_add_u32 v34, s54, v34\nv_mfma_f32_16x16x32_bf16 v[68:71], v[236:239], v[48:51], v[68:71]\nds_read_b128 v[48:51], v34 offset:0\n"
"s_waitcnt lgkmcnt(3)\nv_mfma_f32_16x16x32_bf16 v[72:75], v[232:235], v[52:55], v[72:75]\nv_add_u32 v35, s54, v35\nv_mfma_f32_16x16x32_bf16 v[76:79], v[236:239], v[52:55], v[76:79]\n"
"ds_read_b128 v[52:55], v35 offset:0\ns_mov_b32 s52, s47\ns_add_u32 s47, s51, s32\ns_add_u32 s51, s51, s54\n"
"s_add_u32 s49, s49, 28672\ns_waitcnt vmcnt(11)\ns_barrier\ns_waitcnt lgkmcnt(3)\n"
"v_mfma_f32_16x16x32_bf16 v[96:99], v[128:131], v[40:43], 0\nv_mov_b32 v36, s48\ns_add_u32 s48, s48, 1\ns_nop 0\n"
"buffer_store_dword v36, v19, s[24:27], s33 offen sc1\nv_mfma_f32_16x16x32_bf16 v[100:103], v[132:135], v[40:43], 0\nds_read_b128 v[40:43], v32 offset:4096\ns_waitcnt lgkmcnt(3)\n"
"v_mfma_f32_16x16x32_bf16 v[104:107], v[128:131], v[44:47], 0\nv_mfma_f32_16x16x32_bf16 v[108:111], v[132:135], v[44:47], 0\nds_read_b128 v[44:47], v33 offset:4096\nv_cvt_pk_bf16_f32 v56, v64, v65\n"
"s_waitcnt lgkmcnt(3)\nv_mfma_f32_16x16x32_bf16 v[96:99], v[136:139], v[48:51], v[96:99]\nv_cvt_pk_bf16_f32 v57, v66, v67\nv_mfma_f32_16x16x32_bf16 v[100:103], v[140:143], v[48:51], v[100:103]\n"
"ds_read_b128 v[48:51], v34 offset:4096\nv_cvt_pk_bf16_f32 v58, v68, v69\ns_waitcnt lgkmcnt(3)\nv_mfma_f32_16x16x32_bf16 v[104:107], v[136:139], v[52:55], v[104:107]\n"
"v_cvt_pk_bf16_f32 v59, v70, v71\nv_mfma_f32_16x16x32_bf16 v[108:111], v[140:143], v[52:55], v[108:111]\nds_read_b128 v[52:55], v35 offset:4096\ns_waitcnt lgkmcnt(3)\n"
"v_mfma_f32_16x16x32_bf16 v[96:99], v[144:147], v[40:43], v[96:99]\nv_mfma_f32_16x16x32_bf16 v[100:103], v[148:151], v[40:43], v[100:103]\nds_read_b128 v[40:43], v32 offset:8192\nbuffer_store_dwordx4 v[56:59], v15, s[20:23], s50 offen sc1\n"
"s_waitcnt lgkmcnt(3)\nv_mfma_f32_16x16x32_bf16 v[104:107], v[144:147], v[44:47], v[104:107]\nv_cvt_pk_bf16_f32 v60, v72, v73\nv_mfma_f32_16x16x32_bf16 v[108:111], v[148:151], v[44:47], v[108:111]\n"
"ds_read_b128 v[44:47], v33 offset:8192\nv_cvt_pk_bf16_f32 v61, v74, v75\ns_waitcnt lgkmcnt(3)\nv_mfma_f32_16x16x32_bf16 v[96:99], v[152:155], v[48:51], v[96:99]\n"
"v_cvt_pk_bf16_f32 v62, v76, v77\nv_mfma_f32_16x16x32_bf16 v[100:103], v[156:159], v[48:51], v[100:103]\nds_read_b128 v[48:51], v34 offset:8192\nv_cvt_pk_bf16_f32 v63, v78, v79\n"
"s_waitcnt lgkmcnt(3)\nv_mfma_f32_16x16x32_bf16 v[104:107], v[152:155], v[52:55], v[104:107]\nv_mfma_f32_16x16x32_bf16 v[108:111], v[156:159], v[52:55], v[108:111]\nds_read_b128 v[52:55], v35 offset:8192\n"
"s_waitcnt lgkmcnt(3)\nv_mfma_f32_16x16x32_bf16 v[96:99], v[160:163], v[40:43], v[96:99]\nbuffer_store_dwordx4 v[60:63], v16, s[20:23], s50 offen sc1\nv_mfma_f32_16x16x32_bf16 v[100:103], v[164:167], v[40:43], v[100:103]\n"
"ds_read_b128 v[40:43], v32 offset:12288\ns_add_u32 s50, s50, s36\ns_waitcnt lgkmcnt(3)\nv_mfma_f32_16x16x32_bf16 v[104:107], v[160:163], v[44:47], v[104:107]\n"
"s_add_u32 m0, s52, 0\nv_mfma_f32_16x16x32_bf16 v[108:111], v[164:167], v[44:47], v[108:111]\nds_read_b128 v[44:47], v33 offset:12288\ns_add_u32 s53, s49, 0\n"
"s_waitcnt lgkmcnt(3)\nv_mfma_f32_16x16x32_bf16 v[96:99], v[168:171], v[48:51], v[96:99]\nbuffer_load_dwordx4 v10, s[12:15], s53 offen lds\nv_mfma_f32_16x16x32_bf16 v[100:103], v[172:175], v[48:51], v[100:103]\n"
"ds_read_b128 v[48:51], v34 offset:12288\ns_add_u32 m0, s52, 4096\ns_waitcnt lgkmcnt(3)\nv_mfma_f32_16x16x32_bf16 v[104:107], v[168:171], v[52:55], v[104:107]\n"
"s_add_u32 s53, s49, 128\nv_mfma_f32_16x16x32_bf16 v[108:111], v[172:175], v[52:55], v[108:111]\nds_read_b128 v[52:55], v35 offset:12288\nbuffer_load_dwordx4 v10, s[12:15], s53 offen lds\n"
"s_waitcnt lgkmcnt(3)\nv_mfma_f32_16x16x32_bf16 v[96:99], v[176:179], v[40:43], v[96:99]\ns_add_u32 m0, s52, 8192\nv_mfma_f32_16x16x32_bf16 v[100:103], v[180:183], v[40:43], v[100:103]\n"
"ds_read_b128 v[40:43], v32 offset:16384\ns_add_u32 s53, s49, 256\ns_waitcnt lgkmcnt(3)\nv_mfma_f32_16x16x32_bf16 v[104:107], v[176:179], v[44:47], v[104:107]\n"
"buffer_load_dwordx4 v10, s[12:15], s53 offen lds\nv_mfma_f32_16x16x32_bf16 v[108:111], v[180:183], v[44:47], v[108:111]\nds_read_b128 v[44:47], v33 offset:16384\ns_add_u32 m0, s52, 12288\n"
"s_waitcnt lgkmcnt(3)\nv_mfma_f32_16x16x32_bf16 v[96:99], v[184:187], v[48:51], v[96:99]\ns_add_u32 s53, s49, 384\nv_mfma_f32_16x16x32_bf16 v[100:103], v[188:191], v[48:51], v[100:103]\n"
"ds_read_b128 v[48:51], v34 offset:16384\nbuffer_load_dwordx4 v10, s[12:15], s53 offen lds\ns_waitcnt lgkmcnt(3)\nv_mfma_f32_16x16x32_bf16 v[104:107], v[184:187], v[52:55], v[104:107]\n"
"s_add_u32 m0, s52, 16384\nv_mfma_f32_16x16x32_bf16 v[108:111], v[188:191], v[52:55], v[108:111]\nds_read_b128 v[52:55], v35 offset:16384\ns_add_u32 s53, s49, 512\n"
"s_waitcnt lgkmcnt(3)\nv_mfma_f32_16x16x32_bf16 v[96:99], v[192:195], v[40:43], v[96:99]\nbuffer_load_dwordx4 v10, s[12:15], s53 offen lds\nv_mfma_f32_16x16x32_bf16 v[100:103], v[196:199], v[40:43], v[100:103]\n"
"ds_read_b128 v[40:43], v32 offset:20480\ns_add_u32 m0, s52, 20480\ns_waitcnt lgkmcnt(3)\nv_mfma_f32_16x16x32_bf16 v[104:107], v[192:195], v[44:47], v[104:107]\n"
"s_add_u32 s53, s49, 640\nv_mfma_f32_16x16x32_bf16 v[108:111], v[196:199], v[44:47], v[108:111]\nds_read_b128 v[44:47], v33 offset:20480\nbuffer_load_dwordx4 v10, s[12:15], s53 offen lds\n"
"s_waitcnt lgkmcnt(3)\nv_mfma_f32_16x16x32_bf16 v[96:99], v[200:203], v[48:51], v[96:99]\ns_add_u32 m0, s52, 24576\nv_mfma_f32_16x16x32_bf16 v[100:103], v[204:207], v[48:51], v[100:103]\n"
"ds_read_b128 v[48:51], v34 offset:20480\ns_add_u32 s53, s49, 768\ns_waitcnt lgkmcnt(3)\nv_mfma_f32_16x16x32_bf16 v[104:107], v[200:203], v[52:55], v[104:107]\n"
"buffer_load_dwordx4 v10, s[12:15], s53 offen lds\nv_mfma_f32_16x16x32_bf16 v[108:111], v[204:207], v[52:55], v[108:111]\nds_read_b128 v[52:55], v35 offset:20480\ns_mov_b32 s53, 8192\n"
"buffer_load_dwordx4 a[64:67], v14, s[16:19], s53 offen sc1\ns_waitcnt lgkmcnt(3)\nv_mfma_f32_16x16x32_bf16 v[96:99], v[208:211], v[40:43], v[96:99]\ns_mov_b32 s53, 22528\n"
"buffer_load_dwordx4 a[68:71], v14, s[16:19], s53 offen sc1\nv_mfma_f32_16x16x32_bf16 v[100:103], v[212:215], v[40:43], v[100:103]\nds_read_b128 v[40:43], v32 offset:24576\ns_mov_b32 s53, 9216\n"
"buffer_load_dwordx4 a[72:75], v14, s[16:19], s53 offen sc1\ns_waitcnt lgkmcnt(3)\nv_mfma_f32_16x16x32_bf16 v[104:107], v[208:211], v[44:47], v[104:107]\ns_mov_b32 s53, 23552\n"
"buffer_load_dwordx4 a[76:79], v14, s[16:19], s53 offen sc1\nv_mfma_f32_16x16x32_bf16 v[108:111], v[212:215], v[44:47], v[108:111]\nds_read_b128 v[44:47], v33 offset:24576\ns_waitcnt lgkmcnt(3)\n"
"v_mfma_f32_16x16x32_bf16 v[96:99], v[216:219], v[48:51], v[96:99]\ns_add_u32 s54, s51, 32768\ns_cmp_eq_u32 s54, 163840\nv_mfma_f32_16x16x32_bf16 v[100:103], v[220:223], v[48:51], v[100:103]\n"
"s_cselect_b32 s54, 0, s54\ns_sub_u32 s54, s54, s51\nds_read_b128 v[48:51], v34 offset:24576\ns_waitcnt lgkmcnt(3)\n"
"v_mfma_f32_16x16x32_bf16 v[104:107], v[216:219], v[52:55], v[104:107]\nv_mfma_f32_16x16x32_bf16 v[108:111], v[220:223], v[52:55], v[108:111]\nds_read_b128 v[52:55], v35 offset:24576\ns_waitcnt lgkmcnt(3)\n"
"v_mfma_f32_16x16x32_bf16 v[96:99], v[224:227], v[40:43], v[96:99]\nv_add_u32 v32, s54, v32\nv_mfma_f32_16x16x32_bf16 v[100:103], v[228:231], v[40:43], v[100:103]\nds_read_b128 v[40:43], v32 offset:0\n"
"s_waitcnt lgkmcnt(3)\nv_mfma_f32_16x16x32_bf16 v[104:107], v[224:227], v[44:47], v[104:107]\nv_add_u32 v33, s54, v33\nv_mfma_f32_16x16x32_bf16 v[108:111], v[228:231], v[44:47], v[108:111]\n"
"ds_read_b128 v[44:47], v33 offset:0\ns_waitcnt lgkmcnt(3)\nv_mfma_f32_16x16x32_bf16 v[96:99], v[232:235], v[48:51], v[96:99]\nv_add_u32 v34, s54, v34\n"
"v_mfma_f32_16x16x32_bf16 v[100:103], v[236:239], v[48:51], v[100:103]\nds_read_b128 v[48:51], v34 offset:0\ns_waitcnt lgkmcnt(3)\nv_mfma_f32_16x16x32_bf16 v[104:107], v[232:235], v[52:55], v[104:107]\n"
"v_add_u32 v35, s54, v35\nv_mfma_f32_16x16x32_bf16 v[108:111], v[236:239], v[52:55], v[108:111]\nds_read_b128 v[52:55], v35 offset:0\ns_mov_b32 s52, s47\n"
"s_add_u32 s47, s51, s32\ns_add_u32 s51, s51, s54\ns_add_u32 s49, s49, 28672\ns_waitcnt vmcnt(11)\n"
"s_barrier\ns_waitcnt lgkmcnt(3)\nv_mfma_f32_16x16x32_bf16 v[64:67], v[128:131], v[40:43], 0\nv_mov_b32 v36, s48\n"
"s_add_u32 s48, s48, 1\ns_nop 0\nbuffer_store_dword v36, v19, s[24:27], s33 offen sc1\nv_mfma_f32_16x16x32_bf16 v[68:71], v[132:135], v[40:43], 0\n"
"ds_read_b128 v[40:43], v32 offset:4096\ns_waitcnt lgkmcnt(3)\nv_mfma_f32_16x16x32_bf16 v[72:75], v[128:131], v[44:47], 0\nv_mfma_f32_16x16x32_bf16 v[76:79], v[132:135], v[44:47], 0\n"
"ds_read_b128 v[44:47], v33 offset:4096\nv_cvt_pk_bf16_f32 v56, v96, v97\ns_waitcnt lgkmcnt(3)\nv_mfma_f32_16x16x32_bf16 v[64:67], v[136:139], v[48:51], v[64:67]\n"
"v_cvt_pk_bf16_f32 v57, v98, v99\nv_mfma_f32_16x16x32_bf16 v[68:71], v[140:143], v[48:51], v[68:71]\nds_read_b128 v[48:51], v34 offset:4096\nv_cvt_pk_bf16_f32 v58, v100, v101\n"
"s_waitcnt lgkmcnt(3)\nv_mfma_f32_16x16x32_bf16 v[72:75], v[136:139], v[52:55], v[72:75]\nv_cvt_pk_bf16_f32 v59, v102, v103\nv_mfma_f32_16x16x32_bf16 v[76:79], v[140:143], v[52:55], v[76:79]\n"
"ds_read_b128 v[52:55], v35 offset:4096\ns_waitcnt lgkmcnt(3)\nv_mfma_f32_16x16x32_bf16 v[64:67], v[144:147], v[40:43], v[64:67]\nv_mfma_f32_16x16x32_bf16 v[68:71], v[148:151], v[40:43], v[68:71]\n"
"ds_read_b128 v[40:43], v32 offset:8192\nbuffer_store_dwordx4 v[56:59], v15, s[20:23], s50 offen sc1\ns_waitcnt lgkmcnt(3)\nv_mfma_f32_16x16x32_bf16 v[72:75], v[144:147], v[44:47], v[72:75]\n"
"v_cvt_pk_bf16_f32 v60, v104, v105\nv_mfma_f32_16x16x32_bf16 v[76:79], v[148:151], v[44:47], v[76:79]\nds_read_b128 v[44:47], v33 offset:8192\nv_cvt_pk_bf16_f32 v61, v106, v107\n"
"s_waitcnt lgkmcnt(3)\nv_mfma_f32_16x16x32_bf16 v[64:67], v[152:155], v[48:51], v[64:67]\nv_cvt_pk_bf16_f32 v62, v108, v109\nv_mfma_f32_16x16x32_bf16 v[68:71], v[156:159], v[48:51], v[68:71]\n"
"ds_read_b128 v[48:51], v34 offset:8192\nv_cvt_pk_bf16_f32 v63, v110, v111\ns_waitcnt lgkmcnt(3)\nv_mfma_f32_16x16x32_bf16 v[72:75], v[152:155], v[52:55], v[72:75]\n"
"v_mfma_f32_16x16x32_bf16 v[76:79], v[156:159], v[52:55], v[76:79]\nds_read_b128 v[52:55], v35 offset:8192\ns_waitcnt lgkmcnt(3)\nv_mfma_f32_16x16x32_bf16 v[64:67], v[160:163], v[40:43], v[64:67]\n"
"buffer_store_dwordx4 v[60:63], v16, s[20:23], s50 offen sc1\nv_mfma_f32_16x16x32_bf16 v[68:71], v[164:167], v[40:43], v[68:71]\nds_read_b128 v[40:43], v32 offset:12288\ns_add_u32 s50, s50, s36\n"
"s_waitcnt lgkmcnt(3)\nv_mfma_f32_16x16x32_bf16 v[72:75], v[160:163], v[44:47], v[72:75]\ns_add_u32 m0, s52, 0\nv_mfma_f32_16x16x32_bf16 v[76:79], v[164:167], v[44:47], v[76:79]\n"
"ds_read_b128 v[44:47], v33 offset:12288\ns_add_u32 s53, s49, 0\ns_waitcnt lgkmcnt(3)\nv_mfma_f32_16x16x32_bf16 v[64:67], v[168:171], v[48:51], v[64:67]\n"
"buffer_load_dwordx4 v10, s[12:15], s53 offen lds\nv_mfma_f32_16x16x32_bf16 v[68:71], v[172:175], v[48:51], v[68:71]\nds_read_b128 v[48:51], v34 offset:12288\ns_add_u32 m0, s52, 4096\n"
"s_waitcnt lgkmcnt(3)\nv_mfma_f32_16x16x32_bf16 v[72:75], v[168:171], v[52:55], v[72:75]\ns_add_u32 s53, s49, 128\nv_mfma_f32_16x16x32_bf16 v[76:79], v[172:175], v[52:55], v[76:79]\n"
"ds_read_b128 v[52:55], v35 offset:12288\nbuffer_load_dwordx4 v10, s[12:15], s53 offen lds\ns_waitcnt lgkmcnt(3)\nv_mfma_f32_16x16x32_bf16 v[64:67], v[176:179], v[40:43], v[64:67]\n"
"s_add_u32 m0, s52, 8192\nv_mfma_f32_16x16x32_bf16 v[68:71], v[180:183], v[40:43], v[68:71]\nds_read_b128 v[40:43], v32 offset:16384\ns_add_u32 s53, s49, 256\n"
"s_waitcnt lgkmcnt(3)\nv_mfma_f32_16x16x32_bf16 v[72:75], v[176:179], v[44:47], v[72:75]\nbuffer_load_dwordx4 v10, s[12:15], s53 offen lds\nv_mfma_f32_16x16x32_bf16 v[76:79], v[180:183], v[44:47], v[76:79]\n"
"ds_read_b128 v[44:47], v33 offset:16384\ns_add_u32 m0, s52, 12288\ns_waitcnt lgkmcnt(3)\nv_mfma_f32_16x16x32_bf16 v[64:67], v[184:187], v[48:51], v[64:67]\n"
"s_add_u32 s53, s49, 384\nv_mfma_f32_16x16x32_bf16 v[68:71], v[188:191], v[48:51], v[68:71]\nds_read_b128 v[48:51], v34 offset:16384\nbuffer_load_dwordx4 v10, s[12:15], s53 offen lds\n"
"s_waitcnt lgkmcnt(3)\nv_mfma_f32_16x16x32_bf16 v[72:75], v[184:187], v[52:55], v[72:75]\ns_add_u32 m0, s52, 16384\nv_mfma_f32_16x16x32_bf16 v[76:79], v[188:191], v[52:55], v[76:79]\n"
"ds_read_b128 v[52:55], v35 offset:16384\ns_add_u32 s53, s49, 512\ns_waitcnt lgkmcnt(3)\nv_mfma_f32_16x16x32_bf16 v[64:67], v[192:195], v[40:43], v[64:67]\n"
"buffer_load_dwordx4 v10, s[12:15], s53 offen lds\nv_mfma_f32_16x16x32_bf16 v[68:71], v[196:199], v[40:43], v[68:71]\nds_read_b128 v[40:43], v32 offset:20480\ns_add_u32 m0, s52, 20480\n"
"s_waitcnt lgkmcnt(3)\nv_mfma_f32_16x16x32_bf16 v[72:75], v[192:195], v[44:47], v[72:75]\ns_add_u32 s53, s49, 640\nv_mfma_f32_16x16x32_bf16 v[76:79], v[196:199], v[44:47], v[76:79]\n"
"ds_read_b128 v[44:47], v33 offset:20480\nbuffer_load_dwordx4 v10, s[12:15], s53 offen lds\ns_waitcnt lgkmcnt(3)\nv_mfma_f32_16x16x32_bf16 v[64:67], v[200:203], v[48:51], v[64:67]\n"
"s_add_u32 m0, s52, 24576\nv_mfma_f32_16x16x32_bf16 v[68:71], v[204:207], v[48:51], v[68:71]\nds_read_b128 v[48:51], v34 offset:20480\ns_add_u32 s53, s49, 768\n"
"s_waitcnt lgkmcnt(3)\nv_mfma_f32_16x16x32_bf16 v[72:75], v[200:203], v[52:55], v[72:75]\nbuffer_load_dwordx4 v10, s[12:15], s53 offen lds\nv_mfma_f32_16x16x32_bf16 v[76:79], v[204:207], v[52:55], v[76:79]\n"
"ds_read_b128 v[52:55], v35 offset:20480\ns_mov_b32 s53, 10240\nbuffer_load_dwordx4 a[80:83], v14, s[16:19], s53 offen sc1\ns_waitcnt lgkmcnt(3)\n"
"v_mfma_f32_16x16x32_bf16 v[64:67], v[208:211], v[40:43], v[64:67]\ns_mov_b32 s53, 24576\nbuffer_load_dwordx4 a[84:87], v14, s[16:19], s53 offen sc1\nv_mfma_f32_16x16x32_bf16 v[68:71], v[212:215], v[40:43], v[68:71]\n"
"ds_read_b128 v[40:43], v32 offset:24576\ns_mov_b32 s53, 11264\nbuffer_load_dwordx4 a[88:91], v14, s[16:19], s53 offen sc1\ns_waitcnt lgkmcnt(3)\n"
"v_mfma_f32_16x16x32_bf16 v[72:75], v[208:211], v[44:47], v[72:75]\ns_mov_b32 s53, 25600\nbuffer_load_dwordx4 a[92:95], v14, s[16:19], s53 offen sc1\nv_mfma_f32_16x16x32_bf16 v[76:79], v[212:215], v[44:47], v[76:79]\n"
"ds_read_b128 v[44:47], v33 offset:24576\ns_waitcnt lgkmcnt(3)\nv_mfma_f32_16x16x32_bf16 v[64:67], v[216:219], v[48:51], v[64:67]\ns_add_u32 s54, s51, 32768\n"
"s_cmp_eq_u32 s54, 163840\nv_mfma_f32_16x16x32_bf16 v[68:71], v[220:223], v[48:51], v[68:71]\ns_cselect_b32 s54, 0, s54\ns_sub_u32 s54, s54, s51\n"
"ds_read_b128 v[48:51], v34 offset:24576\ns_waitcnt lgkmcnt(3)\nv_mfma_f32_16x16x32_bf16 v[72:75], v[216:219], v[52:55], v[72:75]\nv_mfma_f32_16x16x32_bf16 v[76:79], v[220:223], v[52:55], v[76:79]\n"
"ds_read_b128 v[52:55], v35 offset:24576\ns_waitcnt lgkmcnt(3)\nv_mfma_f32_16x16x32_bf16 v[64:67], v[224:227], v[40:43], v[64:67]\nv_add_u32 v32, s54, v32\n"
"v_mfma_f32_16x16x32_bf16 v[68:71], v[228:231], v[40:43], v[68:71]\nds_read_b128 v[40:43], v32 offset:0\ns_waitcnt lgkmcnt(3)\nv_mfma_f32_16x16x32_bf16 v[72:75], v[224:227], v[44:47], v[72:75]\n"
"v_add_u32 v33, s54, v33\nv_mfma_f32_16x16x32_bf16 v[76:79], v[228:231], v[44:47], v[76:79]\nds_read_b128 v[44:47], v33 offset:0\ns_waitcnt lgkmcnt(3)\n"
"v_mfma_f32_16x16x32_bf16 v[64:67], v[232:235], v[48:51], v[64:67]\nv_add_u32 v34, s54, v34\nv_mfma_f32_16x16x32_bf16 v[68:71], v[236:239], v[48:51], v[68:71]\nds_read_b128 v[48:51], v34 offset:0\n"
"s_waitcnt lgkmcnt(3)\nv_mfma_f32_16x16x32_bf16 v[72:75], v[232:235], v[52:55], v[72:75]\nv_add_u32 v35, s54, v35\nv_mfma_f32_16x16x32_bf16 v[76:79], v[236:239], v[52:55], v[76:79]\n"
"ds_read_b128 v[52:55], v35 offset:0\ns_mov_b32 s52, s47\ns_add_u32 s47, s51, s32\ns_add_u32 s51, s51, s54\n"
"s_add_u32 s49, s49, 28672\ns_waitcnt vmcnt(11)\ns_barrier\ns_waitcnt lgkmcnt(3)\n"
"v_mfma_f32_16x16x32_bf16 v[96:99], v[128:131], v[40:43], 0\nv_mov_b32 v36, s48\ns_add_u32 s48, s48, 1\ns_nop 0\n"
"buffer_store_dword v36, v19, s[24:27], s33 offen sc1\nv_mfma_f32_16x16x32_bf16 v[100:103], v[132:135], v[40:43], 0\nds_read_b128 v[40:43], v32 offset:4096\ns_waitcnt lgkmcnt(3)\n"
"v_mfma_f32_16x16x32_bf16 v[104:107], v[128:131], v[44:47], 0\nv_mfma_f32_16x16x32_bf16 v[108:111], v[132:135], v[44:47], 0\nds_read_b128 v[44:47], v33 offset:4096\nv_cvt_pk_bf16_f32 v56, v64, v65\n"
"s_waitcnt lgkmcnt(3)\nv_mfma_f32_16x16x32_bf16 v[96:99], v[136:139], v[48:51], v[96:99]\nv_cvt_pk_bf16_f32 v57, v66, v67\nv_mfma_f32_16x16x32_bf16 v[100:103], v[140:143], v[48:51], v[100:103]\n"
"ds_read_b128 v[48:51], v34 offset:4096\nv_cvt_pk_bf16_f32 v58, v68, v69\ns_waitcnt lgkmcnt(3)\nv_mfma_f32_16x16x32_bf16 v[104:107], v[136:139], v[52:55], v[104:107]\n"
"v_cvt_pk_bf16_f32 v59, v70, v71\nv_mfma_f32_16x16x32_bf16 v[108:111], v[140:143], v[52:55], v[108:111]\nds_read_b128 v[52:55], v35 offset:4096\ns_waitcnt lgkmcnt(3)\n"
"v_mfma_f32_16x16x32_bf16 v[96:99], v[144:147], v[40:43], v[96:99]\nv_mfma_f32_16x16x32_bf16 v[100:103], v[148:151], v[40:43], v[100:103]\nds_read_b128 v[40:43], v32 offset:8192\nbuffer_store_dwordx4 v[56:59], v15, s[20:23], s50 offen sc1\n"
"s_waitcnt lgkmcnt(3)\nv_mfma_f32_16x16x32_bf16 v[104:107], v[144:147], v[44:47], v[104:107]\nv_cvt_pk_bf16_f32 v60, v72, v73\nv_mfma_f32_16x16x32_bf16 v[108:111], v[148:151], v[44:47], v[108:111]\n"
"ds_read_b128 v[44:47], v33 offset:8192\nv_cvt_pk_bf16_f32 v61, v74, v75\ns_waitcnt lgkmcnt(3)\nv_mfma_f32_16x16x32_bf16 v[96:99], v[152:155], v[48:51], v[96:99]\n"
"v_cvt_pk_bf16_f32 v62, v76, v77\nv_mfma_f32_16x16x32_bf16 v[100:103], v[156:159], v[48:51], v[100:103]\nds_read_b128 v[48:51], v34 offset:8192\nv_cvt_pk_bf16_f32 v63, v78, v79\n"
"s_waitcnt lgkmcnt(3)\nv_mfma_f32_16x16x32_bf16 v[104:107], v[152:155], v[52:55], v[104:107]\nv_mfma_f32_16x16x32_bf16 v[108:111], v[156:159], v[52:55], v[108:111]\nds_read_b128 v[52:55], v35 offset:8192\n"
"s_waitcnt lgkmcnt(3)\nv_mfma_f32_16x16x32_bf16 v[96:99], v[160:163], v[40:43], v[96:99]\nbuffer_store_dwordx4 v[60:63], v16, s[20:23], s50 offen sc1\nv_mfma_f32_16x16x32_bf16 v[100:103], v[164:167], v[40:43], v[100:103]\n"
"ds_read_b128 v[40:43], v32 offset:12288\ns_add_u32 s50, s50, s36\ns_waitcnt lgkmcnt(3)\nv_mfma_f32_16x16x32_bf16 v[104:107], v[160:163], v[44:47], v[104:107]\n"
"s_add_u32 m0, s52, 0\nv_mfma_f32_16x16x32_bf16 v[108:111], v[164:167], v[44:47], v[108:111]\nds_read_b128 v[44:47], v33 offset:12288\ns_add_u32 s53, s49, 0\n"
"s_waitcnt lgkmcnt(3)\nv_mfma_f32_16x16x32_bf16 v[96:99], v[168:171], v[48:51], v[96:99]\nbuffer_load_dwordx4 v10, s[12:15], s53 offen lds\nv_mfma_f32_16x16x32_bf16 v[100:103], v[172:175], v[48:51], v[100:103]\n"
"ds_read_b128 v[48:51], v34 offset:12288\ns_add_u32 m0, s52, 4096\ns_waitcnt lgkmcnt(3)\nv_mfma_f32_16x16x32_bf16 v[104:107], v[168:171], v[52:55], v[104:107]\n"
"s_add_u32 s53, s49, 128\nv_mfma_f32_16x16x32_bf16 v[108:111], v[172:175], v[52:55], v[108:111]\nds_read_b128 v[52:55], v35 offset:12288\nbuffer_load_dwordx4 v10, s[12:15], s53 offen lds\n"
"s_waitcnt lgkmcnt(3)\nv_mfma_f32_16x16x32_bf16 v[96:99], v[176:179], v[40:43], v[96:99]\ns_add_u32 m0, s52, 8192\nv_mfma_f32_16x16x32_bf16 v[100:103], v[180:183], v[40:43], v[100:103]\n"
"ds_read_b128 v[40:43], v32 offset:16384\ns_add_u32 s53, s49, 256\ns_waitcnt lgkmcnt(3)\nv_mfma_f32_16x16x32_bf16 v[104:107], v[176:179], v[44:47], v[104:107]\n"
"buffer_load_dwordx4 v10, s[12:15], s53 offen lds\nv_mfma_f32_16x16x32_bf16 v[108:111], v[180:183], v[44:47], v[108:111]\nds_read_b128 v[44:47], v33 offset:16384\ns_add_u32 m0, s52, 12288\n"
"s_waitcnt lgkmcnt(3)\nv_mfma_f32_16x16x32_bf16 v[96:99], v[184:187], v[48:51], v[96:99]\ns_add_u32 s53, s49, 384\nv_mfma_f32_16x16x32_bf16 v[100:103], v[188:191], v[48:51], v[100:103]\n"
"ds_read_b128 v[48:51], v34 offset:16384\nbuffer_load_dwordx4 v10, s[12:15], s53 offen lds\ns_waitcnt lgkmcnt(3)\nv_mfma_f32_16x16x32_bf16 v[104:107], v[184:187], v[52:55], v[104:107]\n"
"s_add_u32 m0, s52, 16384\nv_mfma_f32_16x16x32_bf16 v[108:111], v[188:191], v[52:55], v[108:111]\nds_read_b128 v[52:55], v35 offset:16384\ns_add_u32 s53, s49, 512\n"
"s_waitcnt lgkmcnt(3)\nv_mfma_f32_16x16x32_bf16 v[96:99], v[192:195], v[40:43], v[96:99]\nbuffer_load_dwordx4 v10, s[12:15], s53 offen lds\nv_mfma_f32_16x16x32_bf16 v[100:103], v[196:199], v[40:43], v[100:103]\n"
"ds_read_b128 v[40:43], v32 offset:20480\ns_add_u32 m0, s52, 20480\ns_waitcnt lgkmcnt(3)\nv_mfma_f32_16x16x32_bf16 v[104:107], v[192:195], v[44:47], v[104:107]\n"
"s_add_u32 s53, s49, 640\nv_mfma_f32_16x16x32_bf16 v[108:111], v[196:199], v[44:47], v[108:111]\nds_read_b128 v[44:47], v33 offset:20480\nbuffer_load_dwordx4 v10, s[12:15], s53 offen lds\n"
"s_waitcnt lgkmcnt(3)\nv_mfma_f32_16x16x32_bf16 v[96:99], v[200:203], v[48:51], v[96:99]\ns_add_u32 m0, s52, 24576\nv_mfma_f32_16x16x32_bf16 v[100:103], v[204:207], v[48:51], v[100:103]\n"
"ds_read_b128 v[48:51], v34 offset:20480\ns_add_u32 s53, s49, 768\ns_waitcnt lgkmcnt(3)\nv_mfma_f32_16x16x32_bf16 v[104:107], v[200:203], v[52:55], v[104:107]\n"
"buffer_load_dwordx4 v10, s[12:15], s53 offen lds\nv_mfma_f32_16x16x32_bf16 v[108:111], v[204:207], v[52:55], v[108:111]\nds_read_b128 v[52:55], v35 offset:20480\ns_mov_b32 s53, 12288\n"
"buffer_load_dwordx4 a[96:99], v14, s[16:19], s53 offen sc1\ns_waitcnt lgkmcnt(3)\nv_mfma_f32_16x16x32_bf16 v[96:99], v[208:211], v[40:43], v[96:99]\ns_mov_b32 s53, 26624\n"
"buffer_load_dwordx4 a[100:103], v14, s[16:19], s53 offen sc1\nv_mfma_f32_16x16x32_bf16 v[100:103], v[212:215], v[40:43], v[100:103]\nds_read_b128 v[40:43], v32 offset:24576\ns_mov_b32 s53, 13312\n"
"buffer_load_dwordx4 a[104:107], v14, s[16:19], s53 offen sc1\ns_waitcnt lgkmcnt(3)\nv_mfma_f32_16x16x32_bf16 v[104:107], v[208:211], v[44:47], v[104:107]\ns_mov_b32 s53, 27648\n"
"buffer_load_dwordx4 a[108:111], v14, s[16:19], s53 offen sc1\nv_mfma_f32_16x16x32_bf16 v[108:111], v[212:215], v[44:47], v[108:111]\nds_read_b128 v[44:47], v33 offset:24576\ns_waitcnt lgkmcnt(3)\n"
"v_mfma_f32_16x16x32_bf16 v[96:99], v[216:219], v[48:51], v[96:99]\ns_add_u32 s54, s51, 32768\ns_cmp_eq_u32 s54, 163840\nv_mfma_f32_16x16x32_bf16 v[100:103], v[220:223], v[48:51], v[100:103]\n"
"s_cselect_b32 s54, 0, s54\ns_sub_u32 s54, s54, s51\nds_read_b128 v[48:51], v34 offset:24576\ns_waitcnt lgkmcnt(3)\n"
"v_mfma_f32_16x16x32_bf16 v[104:107], v[216:219], v[52:55], v[104:107]\nv_mfma_f32_16x16x32_bf16 v[108:111], v[220:223], v[52:55], v[108:111]\nds_read_b128 v[52:55], v35 offset:24576\ns_waitcnt lgkmcnt(3)\n"
"v_mfma_f32_16x16x32_bf16 v[96:99], v[224:227], v[40:43], v[96:99]\nv_add_u32 v32, s54, v32\nv_mfma_f32_16x16x32_bf16 v[100:103], v[228:231], v[40:43], v[100:103]\nds_read_b128 v[40:43], v32 offset:0\n"
"s_waitcnt lgkmcnt(3)\nv_mfma_f32_16x16x32_bf16 v[104:107], v[224:227], v[44:47], v[104:107]\nv_add_u32 v33, s54, v33\nv_mfma_f32_16x16x32_bf16 v[108:111], v[228:231], v[44:47], v[108:111]\n"
"ds_read_b128 v[44:47], v33 offset:0\ns_waitcnt lgkmcnt(3)\nv_mfma_f32_16x16x32_bf16 v[96:99], v[232:235], v[48:51], v[96:99]\nv_add_u32 v34, s54, v34\n"
"v_mfma_f32_16x16x32_bf16 v[100:103], v[236:239], v[48:51], v[100:103]\nds_read_b128 v[48:51], v34 offset:0\ns_waitcnt lgkmcnt(3)\nv_mfma_f32_16x16x32_bf16 v[104:107], v[232:235], v[52:55], v[104:107]\n"
"v_add_u32 v35, s54, v35\nv_mfma_f32_16x16x32_bf16 v[108:111], v[236:239], v[52:55], v[108:111]\nds_read_b128 v[52:55], v35 offset:0\ns_mov_b32 s52, s47\n"
"s_add_u32 s47, s51, s32\ns_add_u32 s51, s51, s54\ns_add_u32 s49, s49, 28672\ns_cmp_lg_u32 s34, 0\n"
"s_cbranch_scc1 fg0_stay\ns_nop 7\nv_cvt_pk_bf16_f32 v56, v96, v97\nv_cvt_pk_bf16_f32 v57, v98, v99\n"
"v_cvt_pk_bf16_f32 v58, v100, v101\nv_cvt_pk_bf16_f32 v59, v102, v103\ns_nop 0\ns_nop 0\n"
"buffer_store_dwordx4 v[56:59], v15, s[20:23], s50 offen sc1\nv_cvt_pk_bf16_f32 v60, v104, v105\nv_cvt_pk_bf16_f32 v61, v106, v107\nv_cvt_pk_bf16_f32 v62, v108, v109\n"
"v_cvt_pk_bf16_f32 v63, v110, v111\ns_nop 0\ns_nop 0\nbuffer_store_dwordx4 v[60:63], v16, s[20:23], s50 offen sc1\n"
"s_add_u32 s48, s28, 64\nv_mov_b32 v36, s48\ns_waitcnt vmcnt(0) lgkmcnt(0)\ns_barrier\n"
"buffer_store_dword v36, v19, s[24:27], s33 offen sc1\ns_branch fg0_end\nfg0_stay:\ns_waitcnt vmcnt(0)\n"
"s_barrier\ns_waitcnt lgkmcnt(3)\nv_mfma_f32_16x16x32_bf16 v[64:67], v[128:131], v[40:43], 0\nv_mov_b32 v36, s48\n"
"s_add_u32 s48, s48, 1\ns_nop 0\nbuffer_store_dword v36, v19, s[24:27], s33 offen sc1\nv_mfma_f32_16x16x32_bf16 v[68:71], v[132:135], v[40:43], 0\n"
"v_mfma_f32_16x16x32_bf16 v[72:75], a[0:3], v[40:43], 0\nv_mfma_f32_16x16x32_bf16 v[76:79], a[4:7], v[40:43], 0\nds_read_b128 v[40:43], v32 offset:4096\nv_cvt_pk_bf16_f32 v56, v96, v97\n"
"s_waitcnt lgkmcnt(3)\nv_mfma_f32_16x16x32_bf16 v[80:83], v[128:131], v[44:47], 0\nv_cvt_pk_bf16_f32 v57, v98, v99\nv_mfma_f32_16x16x32_bf16 v[84:87], v[132:135], v[44:47], 0\n"
"v_cvt_pk_bf16_f32 v58, v100, v101\nv_mfma_f32_16x16x32_bf16 v[88:91], a[0:3], v[44:47], 0\nv_cvt_pk_bf16_f32 v59, v102, v103\nv_mfma_f32_16x16x32_bf16 v[92:95], a[4:7], v[44:47], 0\n"
"ds_read_b128 v[44:47], v33 offset:4096\ns_waitcnt lgkmcnt(3)\nv_mfma_f32_16x16x32_bf16 v[64:67], v[136:139], v[48:51], v[64:67]\nv_mfma_f32_16x16x32_bf16 v[68:71], v[140:143], v[48:51], v[68:71]\n"
"buffer_store_dwordx4 v[56:59], v15, s[20:23], s50 offen sc1\nv_mfma_f32_16x16x32_bf16 v[72:75], a[8:11], v[48:51], v[72:75]\nv_cvt_pk_bf16_f32 v60, v104, v105\nv_mfma_f32_16x16x32_bf16 v[76:79], a[12:15], v[48:51], v[76:79]\n"
"ds_read_b128 v[48:51], v34 offset:4096\nv_cvt_pk_bf16_f32 v61, v106, v107\ns_waitcnt lgkmcnt(3)\nv_mfma_f32_16x16x32_bf16 v[80:83], v[136:139], v[52:55], v[80:83]\n"
"v_cvt_pk_bf16_f32 v62, v108, v109\nv_mfma_f32_16x16x32_bf16 v[84:87], v[140:143], v[52:55], v[84:87]\nv_cvt_pk_bf16_f32 v63, v110, v111\nv_mfma_f32_16x16x32_bf16 v[88:91], a[8:11], v[52:55], v[88:91]\n"
"v_mfma_f32_16x16x32_bf16 v[92:95], a[12:15], v[52:55], v[92:95]\nds_read_b128 v[52:55], v35 offset:4096\ns_waitcnt lgkmcnt(3)\nv_mfma_f32_16x16x32_bf16 v[64:67], v[144:147], v[40:43], v[64:67]\n"
"buffer_store_dwordx4 v[60:63], v16, s[20:23], s50 offen sc1\nv_mfma_f32_16x16x32_bf16 v[68:71], v[148:151], v[40:43], v[68:71]\ns_add_u32 s50, s50, s36\nv_mfma_f32_16x16x32_bf16 v[72:75], a[16:19], v[40:43], v[72:75]\n"
"s_add_u32 m0, s52, 0\nv_mfma_f32_16x16x32_bf16 v[76:79], a[20:23], v[40:43], v[76:79]\nds_read_b128 v[40:43], v32 offset:8192\ns_add_u32 s53, s49, 0\n"
"s_waitcnt lgkmcnt(3)\nv_mfma_f32_16x16x32_bf16 v[80:83], v[144:147], v[44:47], v[80:83]\nbuffer_load_dwordx4 v10, s[12:15], s53 offen lds\nv_mfma_f32_16x16x32_bf16 v[84:87], v[148:151], v[44:47], v[84:87]\n"
"v_mfma_f32_16x16x32_bf16 v[88:91], a[16:19], v[44:47], v[88:91]\nv_mfma_f32_16x16x32_bf16 v[92:95], a[20:23], v[44:47], v[92:95]\nds_read_b128 v[44:47], v33 offset:8192\ns_waitcnt lgkmcnt(3)\n"
"v_mfma_f32_16x16x32_bf16 v[64:67], v[152:155], v[48:51], v[64:67]\nv_mfma_f32_16x16x32_bf16 v[68:71], v[156:159], v[48:51], v[68:71]\nv_mfma_f32_16x16x32_bf16 v[72:75], a[24:27], v[48:51], v[72:75]\nv_mfma_f32_16x16x32_bf16 v[76:79], a[28:31], v[48:51], v[76:79]\n"
"ds_read_b128 v[48:51], v34 offset:8192\ns_waitcnt lgkmcnt(3)\nv_mfma_f32_16x16x32_bf16 v[80:83], v[152:155], v[52:55], v[80:83]\nv_mfma_f32_16x16x32_bf16 v[84:87], v[156:159], v[52:55], v[84:87]\n"
"s_add_u32 m0, s52, 4096\nv_mfma_f32_16x16x32_bf16 v[88:91], a[24:27], v[52:55], v[88:91]\ns_add_u32 s53, s49, 128\nv_mfma_f32_16x16x32_bf16 v[92:95], a[28:31], v[52:55], v[92:95]\n"
"ds_read_b128 v[52:55], v35 offset:8192\nbuffer_load_dwordx4 v10, s[12:15], s53 offen lds\ns_waitcnt lgkmcnt(3)\nv_mfma_f32_16x16x32_bf16 v[64:67], v[160:163], v[40:43], v[64:67]\n"
"v_mfma_f32_16x16x32_bf16 v[68:71], v[164:167], v[40:43], v[68:71]\nv_mfma_f32_16x16x32_bf16 v[72:75], a[32:35], v[40:43], v[72:75]\nv_mfma_f32_16x16x32_bf16 v[76:79], a[36:39], v[40:43], v[76:79]\nds_read_b128 v[40:43], v32 offset:12288\n"
"s_waitcnt lgkmcnt(3)\nv_mfma_f32_16x16x32_bf16 v[80:83], v[160:163], v[44:47], v[80:83]\nv_mfma_f32_16x16x32_bf16 v[84:87], v[164:167], v[44:47], v[84:87]\nv_mfma_f32_16x16x32_bf16 v[88:91], a[32:35], v[44:47], v[88:91]\n"
"v_mfma_f32_16x16x32_bf16 v[92:95], a[36:39], v[44:47], v[92:95]\nds_read_b128 v[44:47], v33 offset:12288\ns_waitcnt lgkmcnt(3)\nv_mfma_f32_16x16x32_bf16 v[64:67], v[168:171], v[48:51], v[64:67]\n"
"s_add_u32 m0, s52, 8192\nv_mfma_f32_16x16x32_bf16 v[68:71], v[172:175], v[48:51], v[68:71]\ns_add_u32 s53, s49, 256\nv_mfma_f32_16x16x32_bf16 v[72:75], a[40:43], v[48:51], v[72:75]\n"
"buffer_load_dwordx4 v10, s[12:15], s53 offen lds\nv_mfma_f32_16x16x32_bf16 v[76:79], a[44:47], v[48:51], v[76:79]\nds_read_b128 v[48:51], v34 offset:12288\ns_waitcnt lgkmcnt(3)\n"
"v_mfma_f32_16x16x32_bf16 v[80:83], v[168:171], v[52:55], v[80:83]\nv_mfma_f32_16x16x32_bf16 v[84:87], v[172:175], v[52:55], v[84:87]\nv_mfma_f32_16x16x32_bf16 v[88:91], a[40:43], v[52:55], v[88:91]\nv_mfma_f32_16x16x32_bf16 v[92:95], a[44:47], v[52:55], v[92:95]\n"
"ds_read_b128 v[52:55], v35 offset:12288\ns_waitcnt lgkmcnt(3)\nv_mfma_f32_16x16x32_bf16 v[64:67], v[176:179], v[40:43], v[64:67]\nv_mfma_f32_16x16x32_bf16 v[68:71], v[180:183], v[40:43], v[68:71]\n"
"v_mfma_f32_16x16x32_bf16 v[72:75], a[48:51], v[40:43], v[72:75]\nv_mfma_f32_16x16x32_bf16 v[76:79], a[52:55], v[40:43], v[76:79]\nds_read_b128 v[40:43], v32 offset:16384\ns_add_u32 m0, s52, 12288\n"
"s_waitcnt lgkmcnt(3)\nv_mfma_f32_16x16x32_bf16 v[80:83], v[176:179], v[44:47], v[80:83]\ns_add_u32 s53, s49, 384\nv_mfma_f32_16x16x32_bf16 v[84:87], v[180:183], v[44:47], v[84:87]\n"
"buffer_load_dwordx4 v10, s[12:15], s53 offen lds\nv_mfma_f32_16x16x32_bf16 v[88:91], a[48:51], v[44:47], v[88:91]\nv_mfma_f32_16x16x32_bf16 v[92:95], a[52:55], v[44:47], v[92:95]\nds_read_b128 v[44:47], v33 offset:16384\n"
"s_waitcnt lgkmcnt(3)\nv_mfma_f32_16x16x32_bf16 v[64:67], v[184:187], v[48:51], v[64:67]\nv_mfma_f32_16x16x32_bf16 v[68:71], v[188:191], v[48:51], v[68:71]\nv_mfma_f32_16x16x32_bf16 v[72:75], a[56:59], v[48:51], v[72:75]\n"
"v_mfma_f32_16x16x32_bf16 v[76:79], a[60:63], v[48:51], v[76:79]\nds_read_b128 v[48:51], v34 offset:16384\ns_waitcnt lgkmcnt(3)\nv_mfma_f32_16x16x32_bf16 v[80:83], v[184:187], v[52:55], v[80:83]\n"
"v_mfma_f32_16x16x32_bf16 v[84:87], v[188:191], v[52:55], v[84:87]\nv_mfma_f32_16x16x32_bf16 v[88:91], a[56:59], v[52:55], v[88:91]\ns_add_u32 m0, s52, 16384\nv_mfma_f32_16x16x32_bf16 v[92:95], a[60:63], v[52:55], v[92:95]\n"
"ds_read_b128 v[52:55], v35 offset:16384\ns_add_u32 s53, s49, 512\ns_waitcnt lgkmcnt(3)\nv_mfma_f32_16x16x32_bf16 v[64:67], v[192:195], v[40:43], v[64:67]\n"
"buffer_load_dwordx4 v10, s[12:15], s53 offen lds\nv_mfma_f32_16x16x32_bf16 v[68:71], v[196:199], v[40:43], v[68:71]\nv_mfma_f32_16x16x32_bf16 v[72:75], a[64:67], v[40:43], v[72:75]\nv_mfma_f32_16x16x32_bf16 v[76:79], a[68:71], v[40:43], v[76:79]\n"
"ds_read_b128 v[40:43], v32 offset:20480\ns_waitcnt lgkmcnt(3)\nv_mfma_f32_16x16x32_bf16 v[80:83], v[192:195], v[44:47], v[80:83]\nv_mfma_f32_16x16x32_bf16 v[84:87], v[196:199], v[44:47], v[84:87]\n"
"v_mfma_f32_16x16x32_bf16 v[88:91], a[64:67], v[44:47], v[88:91]\nv_mfma_f32_16x16x32_bf16 v[92:95], a[68:71], v[44:47], v[92:95]\nds_read_b128 v[44:47], v33 offset:20480\ns_waitcnt lgkmcnt(3)\n"
"v_mfma_f32_16x16x32_bf16 v[64:67], v[200:203], v[48:51], v[64:67]\nv_mfma_f32_16x16x32_bf16 v[68:71], v[204:207], v[48:51], v[68:71]\ns_add_u32 m0, s52, 20480\nv_mfma_f32_16x16x32_bf16 v[72:75], a[72:75], v[48:51], v[72:75]\n"
"s_add_u32 s53, s49, 640\nv_mfma_f32_16x16x32_bf16 v[76:79], a[76:79], v[48:51], v[76:79]\nds_read_b128 v[48:51], v34 offset:20480\nbuffer_load_dwordx4 v10, s[12:15], s53 offen lds\n"
"s_waitcnt lgkmcnt(3)\nv_mfma_f32_16x16x32_bf16 v[80:83], v[200:203], v[52:55], v[80:83]\nv_mfma_f32_16x16x32_bf16 v[84:87], v[204:207], v[52:55], v[84:87]\nv_mfma_f32_16x16x32_bf16 v[88:91], a[72:75], v[52:55], v[88:91]\n"
"v_mfma_f32_16x16x32_bf16 v[92:95], a[76:79], v[52:55], v[92:95]\nds_read_b128 v[52:55], v35 offset:20480\ns_waitcnt lgkmcnt(3)\nv_mfma_f32_16x16x32_bf16 v[64:67], v[208:211], v[40:43], v[64:67]\n"
"v_mfma_f32_16x16x32_bf16 v[68:71], v[212:215], v[40:43], v[68:71]\nv_mfma_f32_16x16x32_bf16 v[72:75], a[80:83], v[40:43], v[72:75]\nv_mfma_f32_16x16x32_bf16 v[76:79], a[84:87], v[40:43], v[76:79]\nds_read_b128 v[40:43], v32 offset:24576\n"
"s_waitcnt lgkmcnt(3)\nv_mfma_f32_16x16x32_bf16 v[80:83], v[208:211], v[44:47], v[80:83]\ns_add_u32 m0, s52, 24576\nv_mfma_f32_16x16x32_bf16 v[84:87], v[212:215], v[44:47], v[84:87]\n"
"s_add_u32 s53, s49, 768\nv_mfma_f32_16x16x32_bf16 v[88:91], a[80:83], v[44:47], v[88:91]\nbuffer_load_dwordx4 v10, s[12:15], s53 offen lds\nv_mfma_f32_16x16x32_bf16 v[92:95], a[84:87], v[44:47], v[92:95]\n"
"ds_read_b128 v[44:47], v33 offset:24576\ns_waitcnt lgkmcnt(3)\nv_mfma_f32_16x16x32_bf16 v[64:67], v[216:219], v[48:51], v[64:67]\ns_add_u32 s54, s51, 32768\n"
"s_cmp_eq_u32 s54, 163840\nv_mfma_f32_16x16x32_bf16 v[68:71], v[220:223], v[48:51], v[68:71]\nv_mfma_f32_16x16x32_bf16 v[72:75], a[88:91], v[48:51], v[72:75]\nv_mfma_f32_16x16x32_bf16 v[76:79], a[92:95], v[48:51], v[76:79]\n"
"s_cselect_b32 s54, 0, s54\ns_sub_u32 s54, s54, s51\nds_read_b128 v[48:51], v34 offset:24576\ns_waitcnt lgkmcnt(3)\n"
"v_mfma_f32_16x16x32_bf16 v[80:83], v[216:219], v[52:55], v[80:83]\nv_mfma_f32_16x16x32_bf16 v[84:87], v[220:223], v[52:55], v[84:87]\nv_mfma_f32_16x16x32_bf16 v[88:91], a[88:91], v[52:55], v[88:91]\nv_mfma_f32_16x16x32_bf16 v[92:95], a[92:95], v[52:55], v[92:95]\n"
"ds_read_b128 v[52:55], v35 offset:24576\ns_waitcnt lgkmcnt(3)\nv_mfma_f32_16x16x32_bf16 v[64:67], v[224:227], v[40:43], v[64:67]\nv_add_u32 v32, s54, v32\n"
"v_mfma_f32_16x16x32_bf16 v[68:71], v[228:231], v[40:43], v[68:71]\nv_mfma_f32_16x16x32_bf16 v[72:75], a[96:99], v[40:43], v[72:75]\nv_mfma_f32_16x16x32_bf16 v[76:79], a[100:103], v[40:43], v[76:79]\nds_read_b128 v[40:43], v32 offset:0\n"
"s_waitcnt lgkmcnt(3)\nv_mfma_f32_16x16x32_bf16 v[80:83], v[224:227], v[44:47], v[80:83]\nv_add_u32 v33, s54, v33\nv_mfma_f32_16x16x32_bf16 v[84:87], v[228:231], v[44:47], v[84:87]\n"
"v_mfma_f32_16x16x32_bf16 v[88:91], a[96:99], v[44:47], v[88:91]\nv_mfma_f32_16x16x32_bf16 v[92:95], a[100:103], v[44:47], v[92:95]\nds_read_b128 v[44:47], v33 offset:0\ns_waitcnt lgkmcnt(3)\n"
"v_mfma_f32_16x16x32_bf16 v[64:67], v[232:235], v[48:51], v[64:67]\nv_add_u32 v34, s54, v34\nv_mfma_f32_16x16x32_bf16 v[68:71], v[236:239], v[48:51], v[68:71]\nv_mfma_f32_16x16x32_bf16 v[72:75], a[104:107], v[48:51], v[72:75]\n"
"v_mfma_f32_16x16x32_bf16 v[76:79], a[108:111], v[48:51], v[76:79]\nds_read_b128 v[48:51], v34 offset:0\ns_waitcnt lgkmcnt(3)\nv_mfma_f32_16x16x32_bf16 v[80:83], v[232:235], v[52:55], v[80:83]\n"
"v_add_u32 v35, s54, v35\nv_mfma_f32_16x16x32_bf16 v[84:87], v[236:239], v[52:55], v[84:87]\nv_mfma_f32_16x16x32_bf16 v[88:91], a[104:107], v[52:55], v[88:91]\nv_mfma_f32_16x16x32_bf16 v[92:95], a[108:111], v[52:55], v[92:95]\n"
"ds_read_b128 v[52:55], v35 offset:0\ns_mov_b32 s52, s47\ns_add_u32 s47, s51, s32\ns_add_u32 s51, s51, s54\n"
"s_add_u32 s49, s49, 28672\ns_waitcnt vmcnt(7)\ns_barrier\ns_waitcnt lgkmcnt(3)\n"
"v_mfma_f32_16x16x32_bf16 v[96:99], v[128:131], v[40:43], 0\nv_mov_b32 v36, s48\ns_add_u32 s48, s48, 1\ns_nop 0\n"
"buffer_store_dword v36, v19, s[24:27], s33 offen sc1\nv_mfma_f32_16x16x32_bf16 v[100:103], v[132:135], v[40:43], 0\nv_mfma_f32_16x16x32_bf16 v[104:107], a[0:3], v[40:43], 0\nv_mfma_f32_16x16x32_bf16 v[108:111], a[4:7], v[40:43], 0\n"
"ds_read_b128 v[40:43], v32 offset:4096\nv_cvt_pk_bf16_f32 v56, v64, v65\ns_waitcnt lgkmcnt(3)\nv_mfma_f32_16x16x32_bf16 v[112:115], v[128:131], v[44:47], 0\n"
"v_cvt_pk_bf16_f32 v57, v66, v67\nv_mfma_f32_16x16x32_bf16 v[116:119], v[132:135], v[44:47], 0\nv_cvt_pk_bf16_f32 v58, v68, v69\nv_mfma_f32_16x16x32_bf16 v[120:123], a[0:3], v[44:47], 0\n"
"v_cvt_pk_bf16_f32 v59, v70, v71\nv_mfma_f32_16x16x32_bf16 v[124:127], a[4:7], v[44:47], 0\nds_read_b128 v[44:47], v33 offset:4096\ns_waitcnt lgkmcnt(3)\n"
"v_mfma_f32_16x16x32_bf16 v[96:99], v[136:139], v[48:51], v[96:99]\nv_mfma_f32_16x16x32_bf16 v[100:103], v[140:143], v[48:51], v[100:103]\nbuffer_store_dwordx4 v[56:59], v15, s[20:23], s50 offen sc1\nv_mfma_f32_16x16x32_bf16 v[104:107], a[8:11], v[48:51], v[104:107]\n"
"v_cvt_pk_bf16_f32 v60, v80, v81\nv_mfma_f32_16x16x32_bf16 v[108:111], a[12:15], v[48:51], v[108:111]\nds_read_b128 v[48:51], v34 offset:4096\nv_cvt_pk_bf16_f32 v61, v82, v83\n"
"s_waitcnt lgkmcnt(3)\nv_mfma_f32_16x16x32_bf16 v[112:115], v[136:139], v[52:55], v[112:115]\nv_cvt_pk_bf16_f32 v62, v84, v85\nv_mfma_f32_16x16x32_bf16 v[116:119], v[140:143], v[52:55], v[116:119]\n"
"v_cvt_pk_bf16_f32 v63, v86, v87\nv_mfma_f32_16x16x32_bf16 v[120:123], a[8:11], v[52:55], v[120:123]\nv_mfma_f32_16x16x32_bf16 v[124:127], a[12:15], v[52:55], v[124:127]\nds_read_b128 v[52:55], v35 offset:4096\n"
"s_waitcnt lgkmcnt(3)\nv_mfma_f32_16x16x32_bf16 v[96:99], v[144:147], v[40:43], v[96:99]\nbuffer_store_dwordx4 v[60:63], v16, s[20:23], s50 offen sc1\nv_mfma_f32_16x16x32_bf16 v[100:103], v[148:151], v[40:43], v[100:103]\n"
"v_cvt_pk_bf16_f32 v56, v72, v73\nv_mfma_f32_16x16x32_bf16 v[104:107], a[16:19], v[40:43], v[104:107]\nv_cvt_pk_bf16_f32 v57, v74, v75\nv_mfma_f32_16x16x32_bf16 v[108:111], a[20:23], v[40:43], v[108:111]\n"
"ds_read_b128 v[40:43], v32 offset:8192\nv_cvt_pk_bf16_f32 v58, v76, v77\ns_waitcnt lgkmcnt(3)\nv_mfma_f32_16x16x32_bf16 v[112:115], v[144:147], v[44:47], v[112:115]\n"
"v_cvt_pk_bf16_f32 v59, v78, v79\nv_mfma_f32_16x16x32_bf16 v[116:119], v[148:151], v[44:47], v[116:119]\nv_mfma_f32_16x16x32_bf16 v[120:123], a[16:19], v[44:47], v[120:123]\nv_mfma_f32_16x16x32_bf16 v[124:127], a[20:23], v[44:47], v[124:127]\n"
"ds_read_b128 v[44:47], v33 offset:8192\nbuffer_store_dwordx4 v[56:59], v17, s[20:23], s50 offen sc1\ns_waitcnt lgkmcnt(3)\nv_mfma_f32_16x16x32_bf16 v[96:99], v[152:155], v[48:51], v[96:99]\n"
"v_cvt_pk_bf16_f32 v60, v88, v89\nv_mfma_f32_16x16x32_bf16 v[100:103], v[156:159], v[48:51], v[100:103]\nv_cvt_pk_bf16_f32 v61, v90, v91\nv_mfma_f32_16x16x32_bf16 v[104:107], a[24:27], v[48:51], v[104:107]\n"
"v_cvt_pk_bf16_f32 v62, v92, v93\nv_mfma_f32_16x16x32_bf16 v[108:111], a[28:31], v[48:51], v[108:111]\nds_read_b128 v[48:51], v34 offset:8192\nv_cvt_pk_bf16_f32 v63, v94, v95\n"
"s_waitcnt lgkmcnt(3)\nv_mfma_f32_16x16x32_bf16 v[112:115], v[152:155], v[52:55], v[112:115]\nv_mfma_f32_16x16x32_bf16 v[116:119], v[156:159], v[52:55], v[116:119]\nv_mfma_f32_16x16x32_bf16 v[120:123], a[24:27], v[52:55], v[120:123]\n"
"buffer_store_dwordx4 v[60:63], v18, s[20:23], s50 offen sc1\nv_mfma_f32_16x16x32_bf16 v[124:127], a[28:31], v[52:55], v[124:127]\nds_read_b128 v[52:55], v35 offset:8192\ns_add_u32 s50, s50, s36\n"
"s_waitcnt lgkmcnt(3)\nv_mfma_f32_16x16x32_bf16 v[96:99], v[160:163], v[40:43], v[96:99]\ns_add_u32 m0, s52, 0\nv_mfma_f32_16x16x32_bf16 v[100:103], v[164:167], v[40:43], v[100:103]\n"
"s_add_u32 s53, s49, 0\nv_mfma_f32_16x16x32_bf16 v[104:107], a[32:35], v[40:43], v[104:107]\nbuffer_load_dwordx4 v10, s[12:15], s53 offen lds\nv_mfma_f32_16x16x32_bf16 v[108:111], a[36:39], v[40:43], v[108:111]\n"
"ds_read_b128 v[40:43], v32 offset:12288\ns_waitcnt lgkmcnt(3)\nv_mfma_f32_16x16x32_bf16 v[112:115], v[160:163], v[44:47], v[112:115]\nv_mfma_f32_16x16x32_bf16 v[116:119], v[164:167], v[44:47], v[116:119]\n"
"v_mfma_f32_16x16x32_bf16 v[120:123], a[32:35], v[44:47], v[120:123]\nv_mfma_f32_16x16x32_bf16 v[124:127], a[36:39], v[44:47], v[124:127]\nds_read_b128 v[44:47], v33 offset:12288\ns_waitcnt lgkmcnt(3)\n"
"v_mfma_f32_16x16x32_bf16 v[96:99], v[168:171], v[48:51], v[96:99]\nv_mfma_f32_16x16x32_bf16 v[100:103], v[172:175], v[48:51], v[100:103]\nv_mfma_f32_16x16x32_bf16 v[104:107], a[40:43], v[48:51], v[104:107]\nv_mfma_f32_16x16x32_bf16 v[108:111], a[44:47], v[48:51], v[108:111]\n"
"ds_read_b128 v[48:51], v34 offset:12288\ns_add_u32 m0, s52, 4096\ns_waitcnt lgkmcnt(3)\nv_mfma_f32_16x16x32_bf16 v[112:115], v[168:171], v[52:55], v[112:115]\n"
"s_add_u32 s53, s49, 128\nv_mfma_f32_16x16x32_bf16 v[116:119], v[172:175], v[52:55], v[116:119]\nbuffer_load_dwordx4 v10, s[12:15], s53 offen lds\nv_mfma_f32_16x16x32_bf16 v[120:123], a[40:43], v[52:55], v[120:123]\n"
"v_mfma_f32_16x16x32_bf16 v[124:127], a[44:47], v[52:55], v[124:127]\nds_read_b128 v[52:55], v35 offset:12288\ns_waitcnt lgkmcnt(3)\nv_mfma_f32_16x16x32_bf16 v[96:99], v[176:179], v[40:43], v[96:99]\n"
"v_mfma_f32_16x16x32_bf16 v[100:103], v[180:183], v[40:43], v[100:103]\nv_mfma_f32_16x16x32_bf16 v[104:107], a[48:51], v[40:43], v[104:107]\nv_mfma_f32_16x16x32_bf16 v[108:111], a[52:55], v[40:43], v[108:111]\nds_read_b128 v[40:43], v32 offset:16384\n"
"s_waitcnt lgkmcnt(3)\nv_mfma_f32_16x16x32_bf16 v[112:115], v[176:179], v[44:47], v[112:115]\nv_mfma_f32_16x16x32_bf16 v[116:119], v[180:183], v[44:47], v[116:119]\nv_mfma_f32_16x16x32_bf16 v[120:123], a[48:51], v[44:47], v[120:123]\n"
"s_add_u32 m0, s52, 8192\nv_mfma_f32_16x16x32_bf16 v[124:127], a[52:55], v[44:47], v[124:127]\nds_read_b128 v[44:47], v33 offset:16384\ns_add_u32 s53, s49, 256\n"
"s_waitcnt lgkmcnt(3)\nv_mfma_f32_16x16x32_bf16 v[96:99], v[184:187], v[48:51], v[96:99]\nbuffer_load_dwordx4 v10, s[12:15], s53 offen lds\nv_mfma_f32_16x16x32_bf16 v[100:103], v[188:191], v[48:51], v[100:103]\n"
"v_mfma_f32_16x16x32_bf16 v[104:107], a[56:59], v[48:51], v[104:107]\nv_mfma_f32_16x16x32_bf16 v[108:111], a[60:63], v[48:51], v[108:111]\nds_read_b128 v[48:51], v34 offset:16384\ns_waitcnt lgkmcnt(3)\n"
"v_mfma_f32_16x16x32_bf16 v[112:115], v[184:187], v[52:55], v[112:115]\nv_mfma_f32_16x16x32_bf16 v[116:119], v[188:191], v[52:55], v[116:119]\nv_mfma_f32_16x16x32_bf16 v[120:123], a[56:59], v[52:55], v[120:123]\nv_mfma_f32_16x16x32_bf16 v[124:127], a[60:63], v[52:55], v[124:127]\n"
"ds_read_b128 v[52:55], v35 offset:16384\ns_waitcnt lgkmcnt(3)\nv_mfma_f32_16x16x32_bf16 v[96:99], v[192:195], v[40:43], v[96:99]\nv_mfma_f32_16x16x32_bf16 v[100:103], v[196:199], v[40:43], v[100:103]\n"
"s_add_u32 m0, s52, 12288\nv_mfma_f32_16x16x32_bf16 v[104:107], a[64:67], v[40:43], v[104:107]\ns_add_u32 s53, s49, 384\nv_mfma_f32_16x16x32_bf16 v[108:111], a[68:71], v[40:43], v[108:111]\n"
"ds_read_b128 v[40:43], v32 offset:20480\nbuffer_load_dwordx4 v10, s[12:15], s53 offen lds\ns_waitcnt lgkmcnt(3)\nv_mfma_f32_16x16x32_bf16 v[112:115], v[192:195], v[44:47], v[112:115]\n"
"v_mfma_f32_16x16x32_bf16 v[116:119], v[196:199], v[44:47], v[116:119]\nv_mfma_f32_16x16x32_bf16 v[120:123], a[64:67], v[44:47], v[120:123]\nv_mfma_f32_16x16x32_bf16 v[124:127], a[68:71], v[44:47], v[124:127]\nds_read_b128 v[44:47], v33 offset:20480\n"
"s_waitcnt lgkmcnt(3)\nv_mfma_f32_16x16x32_bf16 v[96:99], v[200:203], v[48:51], v[96:99]\nv_mfma_f32_16x16x32_bf16 v[100:103], v[204:207], v[48:51], v[100:103]\nv_mfma_f32_16x16x32_bf16 v[104:107], a[72:75], v[48:51], v[104:107]\n"
"v_mfma_f32_16x16x32_bf16 v[108:111], a[76:79], v[48:51], v[108:111]\nds_read_b128 v[48:51], v34 offset:20480\ns_waitcnt lgkmcnt(3)\nv_mfma_f32_16x16x32_bf16 v[112:115], v[200:203], v[52:55], v[112:115]\n"
"s_add_u32 m0, s52, 16384\nv_mfma_f32_16x16x32_bf16 v[116:119], v[204:207], v[52:55], v[116:119]\ns_add_u32 s53, s49, 512\nv_mfma_f32_16x16x32_bf16 v[120:123], a[72:75], v[52:55], v[120:123]\n"
"buffer_load_dwordx4 v10, s[12:15], s53 offen lds\nv_mfma_f32_16x16x32_bf16 v[124:127], a[76:79], v[52:55], v[124:127]\nds_read_b128 v[52:55], v35 offset:20480\ns_waitcnt lgkmcnt(3)\n"
"v_mfma_f32_16x16x32_bf16 v[96:99], v[208:211], v[40:43], v[96:99]\nv_mfma_f32_16x16x32_bf16 v[100:103], v[212:215], v[40:43], v[100:103]\nv_mfma_f32_16x16x32_bf16 v[104:107], a[80:83], v[40:43], v[104:107]\nv_mfma_f32_16x16x32_bf16 v[108:111], a[84:87], v[40:43], v[108:111]\n"
"ds_read_b128 v[40:43], v32 offset:24576\ns_waitcnt lgkmcnt(3)\nv_mfma_f32_16x16x32_bf16 v[112:115], v[208:211], v[44:47], v[112:115]\nv_mfma_f32_16x16x32_bf16 v[116:119], v[212:215], v[44:47], v[116:119]\n"
"v_mfma_f32_16x16x32_bf16 v[120:123], a[80:83], v[44:47], v[120:123]\nv_mfma_f32_16x16x32_bf16 v[124:127], a[84:87], v[44:47], v[124:127]\nds_read_b128 v[44:47], v33 offset:24576\ns_add_u32 m0, s52, 20480\n"
"s_waitcnt lgkmcnt(3)\nv_mfma_f32_16x16x32_bf16 v[96:99], v[216:219], v[48:51], v[96:99]\ns_add_u32 s54, s51, 32768\ns_cmp_eq_u32 s54, 163840\n"
"v_mfma_f32_16x16x32_bf16 v[100:103], v[220:223], v[48:51], v[100:103]\nv_mfma_f32_16x16x32_bf16 v[104:107], a[88:91], v[48:51], v[104:107]\nv_mfma_f32_16x16x32_bf16 v[108:111], a[92:95], v[48:51], v[108:111]\ns_cselect_b32 s54, 0, s54\n"
"s_sub_u32 s54, s54, s51\nds_read_b128 v[48:51], v34 offset:24576\ns_add_u32 s53, s49, 640\ns_waitcnt lgkmcnt(3)\n"
"v_mfma_f32_16x16x32_bf16 v[112:115], v[216:219], v[52:55], v[112:115]\nbuffer_load_dwordx4 v10, s[12:15], s53 offen lds\nv_mfma_f32_16x16x32_bf16 v[116:119], v[220:223], v[52:55], v[116:119]\nv_mfma_f32_16x16x32_bf16 v[120:123], a[88:91], v[52:55], v[120:123]\n"
"v_mfma_f32_16x16x32_bf16 v[124:127], a[92:95], v[52:55], v[124:127]\nds_read_b128 v[52:55], v35 offset:24576\ns_waitcnt lgkmcnt(3)\nv_mfma_f32_16x16x32_bf16 v[96:99], v[224:227], v[40:43], v[96:99]\n"
"v_add_u32 v32, s54, v32\nv_mfma_f32_16x16x32_bf16 v[100:103], v[228:231], v[40:43], v[100:103]\nv_mfma_f32_16x16x32_bf16 v[104:107], a[96:99], v[40:43], v[104:107]\nv_mfma_f32_16x16x32_bf16 v[108:111], a[100:103], v[40:43], v[108:111]\n"
"ds_read_b128 v[40:43], v32 offset:0\ns_waitcnt lgkmcnt(3)\nv_mfma_f32_16x16x32_bf16 v[112:115], v[224:227], v[44:47], v[112:115]\nv_add_u32 v33, s54, v33\n"
"v_mfma_f32_16x16x32_bf16 v[116:119], v[228:231], v[44:47], v[116:119]\ns_add_u32 m0, s52, 24576\nv_mfma_f32_16x16x32_bf16 v[120:123], a[96:99], v[44:47], v[120:123]\ns_add_u32 s53, s49, 768\n"
"v_mfma_f32_16x16x32_bf16 v[124:127], a[100:103], v[44:47], v[124:127]\nds_read_b128 v[44:47], v33 offset:0\nbuffer_load_dwordx4 v10, s[12:15], s53 offen lds\ns_waitcnt lgkmcnt(3)\n"
"v_mfma_f32_16x16x32_bf16 v[96:99], v[232:235], v[48:51], v[96:99]\nv_add_u32 v34, s54, v34\nv_mfma_f32_16x16x32_bf16 v[100:103], v[236:239], v[48:51], v[100:103]\nv_mfma_f32_16x16x32_bf16 v[104:107], a[104:107], v[48:51], v[104:107]\n"
"v_mfma_f32_16x16x32_bf16 v[108:111], a[108:111], v[48:51], v[108:111]\nds_read_b128 v[48:51], v34 offset:0\ns_waitcnt lgkmcnt(3)\nv_mfma_f32_16x16x32_bf16 v[112:115], v[232:235], v[52:55], v[112:115]\n"
"v_add_u32 v35, s54, v35\nv_mfma_f32_16x16x32_bf16 v[116:119], v[236:239], v[52:55], v[116:119]\nv_mfma_f32_16x16x32_bf16 v[120:123], a[104:107], v[52:55], v[120:123]\nv_mfma_f32_16x16x32_bf16 v[124:127], a[108:111], v[52:55], v[124:127]\n"
"ds_read_b128 v[52:55], v35 offset:0\ns_mov_b32 s52, s47\ns_add_u32 s47, s51, s32\ns_add_u32 s51, s51, s54\n"
"s_add_u32 s49, s49, 28672\n.p2align 6\nfg0_loop:\ns_waitcnt vmcnt(7)\n"
"s_barrier\ns_waitcnt lgkmcnt(3)\nv_mfma_f32_16x16x32_bf16 v[64:67], v[128:131], v[40:43], 0\nv_mov_b32 v36, s48\n"
"s_add_u32 s48, s48, 1\ns_nop 0\nbuffer_store_dword v36, v19, s[24:27], s33 offen sc1\nv_mfma_f32_16x16x32_bf16 v[68:71], v[132:135], v[40:43], 0\n"
"v_mfma_f32_16x16x32_bf16 v[72:75], a[0:3], v[40:43], 0\nv_mfma_f32_16x16x32_bf16 v[76:79], a[4:7], v[40:43], 0\nds_read_b128 v[40:43], v32 offset:4096\nv_cvt_pk_bf16_f32 v56, v96, v97\n"
"s_waitcnt lgkmcnt(3)\nv_mfma_f32_16x16x32_bf16 v[80:83], v[128:131], v[44:47], 0\nv_cvt_pk_bf16_f32 v57, v98, v99\nv_mfma_f32_16x16x32_bf16 v[84:87], v[132:135], v[44:47], 0\n"
"v_cvt_pk_bf16_f32 v58, v100, v101\nv_mfma_f32_16x16x32_bf16 v[88:91], a[0:3], v[44:47], 0\nv_cvt_pk_bf16_f32 v59, v102, v103\nv_mfma_f32_16x16x32_bf16 v[92:95], a[4:7], v[44:47], 0\n"
"ds_read_b128 v[44:47], v33 offset:4096\ns_waitcnt lgkmcnt(3)\nv_mfma_f32_16x16x32_bf16 v[64:67], v[136:139], v[48:51], v[64:67]\nv_mfma_f32_16x16x32_bf16 v[68:71], v[140:143], v[48:51], v[68:71]\n"
"buffer_store_dwordx4 v[56:59], v15, s[20:23], s50 offen sc1\nv_mfma_f32_16x16x32_bf16 v[72:75], a[8:11], v[48:51], v[72:75]\nv_cvt_pk_bf16_f32 v60, v112, v113\nv_mfma_f32_16x16x32_bf16 v[76:79], a[12:15], v[48:51], v[76:79]\n"
"ds_read_b128 v[48:51], v34 offset:4096\nv_cvt_pk_bf16_f32 v61, v114, v115\ns_waitcnt lgkmcnt(3)\nv_mfma_f32_16x16x32_bf16 v[80:83], v[136:139], v[52:55], v[80:83]\n"
"v_cvt_pk_bf16_f32 v62, v116, v117\nv_mfma_f32_16x16x32_bf16 v[84:87], v[140:143], v[52:55], v[84:87]\nv_cvt_pk_bf16_f32 v63, v118, v119\nv_mfma_f32_16x16x32_bf16 v[88:91], a[8:11], v[52:55], v[88:91]\n"
"v_mfma_f32_16x16x32_bf16 v[92:95], a[12:15], v[52:55], v[92:95]\nds_read_b128 v[52:55], v35 offset:4096\ns_waitcnt lgkmcnt(3)\nv_mfma_f32_16x16x32_bf16 v[64:67], v[144:147], v[40:43], v[64:67]\n"
"buffer_store_dwordx4 v[60:63], v16, s[20:23], s50 offen sc1\nv_mfma_f32_16x16x32_bf16 v[68:71], v[148:151], v[40:43], v[68:71]\nv_cvt_pk_bf16_f32 v56, v104, v105\nv_mfma_f32_16x16x32_bf16 v[72:75], a[16:19], v[40:43], v[72:75]\n"
"v_cvt_pk_bf16_f32 v57, v106, v107\nv_mfma_f32_16x16x32_bf16 v[76:79], a[20:23], v[40:43], v[76:79]\nds_read_b128 v[40:43], v32 offset:8192\nv_cvt_pk_bf16_f32 v58, v108, v109\n"
"s_waitcnt lgkmcnt(3)\nv_mfma_f32_16x16x32_bf16 v[80:83], v[144:147], v[44:47], v[80:83]\nv_cvt_pk_bf16_f32 v59, v110, v111\nv_mfma_f32_16x16x32_bf16 v[84:87], v[148:151], v[44:47], v[84:87]\n"
"v_mfma_f32_16x16x32_bf16 v[88:91], a[16:19], v[44:47], v[88:91]\nv_mfma_f32_16x16x32_bf16 v[92:95], a[20:23], v[44:47], v[92:95]\nds_read_b128 v[44:47], v33 offset:8192\nbuffer_store_dwordx4 v[56:59], v17, s[20:23], s50 offen sc1\n"
"s_waitcnt lgkmcnt(3)\nv_mfma_f32_16x16x32_bf16 v[64:67], v[152:155], v[48:51], v[64:67]\nv_cvt_pk_bf16_f32 v60, v120, v121\nv_mfma_f32_16x16x32_bf16 v[68:71], v[156:159], v[48:51], v[68:71]\n"
"v_cvt_pk_bf16_f32 v61, v122, v123\nv_mfma_f32_16x16x32_bf16 v[72:75], a[24:27], v[48:51], v[72:75]\nv_cvt_pk_bf16_f32 v62, v124, v125\nv_mfma_f32_16x16x32_bf16 v[76:79], a[28:31], v[48:51], v[76:79]\n"
"ds_read_b128 v[48:51], v34 offset:8192\nv_cvt_pk_bf16_f32 v63, v126, v127\ns_waitcnt lgkmcnt(3)\nv_mfma_f32_16x16x32_bf16 v[80:83], v[152:155], v[52:55], v[80:83]\n"
"v_mfma_f32_16x16x32_bf16 v[84:87], v[156:159], v[52:55], v[84:87]\nv_mfma_f32_16x16x32_bf16 v[88:91], a[24:27], v[52:55], v[88:91]\nbuffer_store_dwordx4 v[60:63], v18, s[20:23], s50 offen sc1\nv_mfma_f32_16x16x32_bf16 v[92:95], a[28:31], v[52:55], v[92:95]\n"
"ds_read_b128 v[52:55], v35 offset:8192\ns_add_u32 s50, s50, s36\ns_waitcnt lgkmcnt(3)\nv_mfma_f32_16x16x32_bf16 v[64:67], v[160:163], v[40:43], v[64:67]\n"
"s_add_u32 m0, s52, 0\nv_mfma_f32_16x16x32_bf16 v[68:71], v[164:167], v[40:43], v[68:71]\ns_add_u32 s53, s49, 0\nv_mfma_f32_16x16x32_bf16 v[72:75], a[32:35], v[40:43], v[72:75]\n"
"buffer_load_dwordx4 v10, s[12:15], s53 offen lds\nv_mfma_f32_16x16x32_bf16 v[76:79], a[36:39], v[40:43], v[76:79]\nds_read_b128 v[40:43], v32 offset:12288\ns_waitcnt lgkmcnt(3)\n"
"v_mfma_f32_16x16x32_bf16 v[80:83], v[160:163], v[44:47], v[80:83]\nv_mfma_f32_16x16x32_bf16 v[84:87], v[164:167], v[44:47], v[84:87]\nv_mfma_f32_16x16x32_bf16 v[88:91], a[32:35], v[44:47], v[88:91]\nv_mfma_f32_16x16x32_bf16 v[92:95], a[36:39], v[44:47], v[92:95]\n"
"ds_read_b128 v[44:47], v33 offset:12288\ns_waitcnt lgkmcnt(3)\nv_mfma_f32_16x16x32_bf16 v[64:67], v[168:171], v[48:51], v[64:67]\nv_mfma_f32_16x16x32_bf16 v[68:71], v[172:175], v[48:51], v[68:71]\n"
"v_mfma_f32_16x16x32_bf16 v[72:75], a[40:43], v[48:51], v[72:75]\nv_mfma_f32_16x16x32_bf16 v[76:79], a[44:47], v[48:51], v[76:79]\nds_read_b128 v[48:51], v34 offset:12288\ns_add_u32 m0, s52, 4096\n"
"s_waitcnt lgkmcnt(3)\nv_mfma_f32_16x16x32_bf16 v[80:83], v[168:171], v[52:55], v[80:83]\ns_add_u32 s53, s49, 128\nv_mfma_f32_16x16x32_bf16 v[84:87], v[172:175], v[52:55], v[84:87]\n"
"buffer_load_dwordx4 v10, s[12:15], s53 offen lds\nv_mfma_f32_16x16x32_bf16 v[88:91], a[40:43], v[52:55], v[88:91]\nv_mfma_f32_16x16x32_bf16 v[92:95], a[44:47], v[52:55], v[92:95]\nds_read_b128 v[52:55], v35 offset:12288\n"
"s_waitcnt lgkmcnt(3)\nv_mfma_f32_16x16x32_bf16 v[64:67], v[176:179], v[40:43], v[64:67]\nv_mfma_f32_16x16x32_bf16 v[68:71], v[180:183], v[40:43], v[68:71]\nv_mfma_f32_16x16x32_bf16 v[72:75], a[48:51], v[40:43], v[72:75]\n"
"v_mfma_f32_16x16x32_bf16 v[76:79], a[52:55], v[40:43], v[76:79]\nds_read_b128 v[40:43], v32 offset:16384\ns_waitcnt lgkmcnt(3)\nv_mfma_f32_16x16x32_bf16 v[80:83], v[176:179], v[44:47], v[80:83]\n"
"v_mfma_f32_16x16x32_bf16 v[84:87], v[180:183], v[44:47], v[84:87]\nv_mfma_f32_16x16x32_bf16 v[88:91], a[48:51], v[44:47], v[88:91]\ns_add_u32 m0, s52, 8192\nv_mfma_f32_16x16x32_bf16 v[92:95], a[52:55], v[44:47], v[92:95]\n"
"ds_read_b128 v[44:47], v33 offset:16384\ns_add_u32 s53, s49, 256\ns_waitcnt lgkmcnt(3)\nv_mfma_f32_16x16x32_bf16 v[64:67], v[184:187], v[48:51], v[64:67]\n"
"buffer_load_dwordx4 v10, s[12:15], s53 offen lds\nv_mfma_f32_16x16x32_bf16 v[68:71], v[188:191], v[48:51], v[68:71]\nv_mfma_f32_16x16x32_bf16 v[72:75], a[56:59], v[48:51], v[72:75]\nv_mfma_f32_16x16x32_bf16 v[76:79], a[60:63], v[48:51], v[76:79]\n"
"ds_read_b128 v[48:51], v34 offset:16384\ns_waitcnt lgkmcnt(3)\nv_mfma_f32_16x16x32_bf16 v[80:83], v[184:187], v[52:55], v[80:83]\nv_mfma_f32_16x16x32_bf16 v[84:87], v[188:191], v[52:55], v[84:87]\n"
"v_mfma_f32_16x16x32_bf16 v[88:91], a[56:59], v[52:55], v[88:91]\nv_mfma_f32_16x16x32_bf16 v[92:95], a[60:63], v[52:55], v[92:95]\nds_read_b128 v[52:55], v35 offset:16384\ns_waitcnt lgkmcnt(3)\n"
"v_mfma_f32_16x16x32_bf16 v[64:67], v[192:195], v[40:43], v[64:67]\nv_mfma_f32_16x16x32_bf16 v[68:71], v[196:199], v[40:43], v[68:71]\ns_add_u32 m0, s52, 12288\nv_mfma_f32_16x16x32_bf16 v[72:75], a[64:67], v[40:43], v[72:75]\n"
"s_add_u32 s53, s49, 384\nv_mfma_f32_16x16x32_bf16 v[76:79], a[68:71], v[40:43], v[76:79]\nds_read_b128 v[40:43], v32 offset:20480\nbuffer_load_dwordx4 v10, s[12:15], s53 offen lds\n"
"s_waitcnt lgkmcnt(3)\nv_mfma_f32_16x16x32_bf16 v[80:83], v[192:195], v[44:47], v[80:83]\nv_mfma_f32_16x16x32_bf16 v[84:87], v[196:199], v[44:47], v[84:87]\nv_mfma_f32_16x16x32_bf16 v[88:91], a[64:67], v[44:47], v[88:91]\n"
"v_mfma_f32_16x16x32_bf16 v[92:95], a[68:71], v[44:47], v[92:95]\nds_read_b128 v[44:47], v33 offset:20480\ns_waitcnt lgkmcnt(3)\nv_mfma_f32_16x16x32_bf16 v[64:67], v[200:203], v[48:51], v[64:67]\n"
"v_mfma_f32_16x16x32_bf16 v[68:71], v[204:207], v[48:51], v[68:71]\nv_mfma_f32_16x16x32_bf16 v[72:75], a[72:75], v[48:51], v[72:75]\nv_mfma_f32_16x16x32_bf16 v[76:79], a[76:79], v[48:51], v[76:79]\nds_read_b128 v[48:51], v34 offset:20480\n"
"s_waitcnt lgkmcnt(3)\nv_mfma_f32_16x16x32_bf16 v[80:83], v[200:203], v[52:55], v[80:83]\ns_add_u32 m0, s52, 16384\nv_mfma_f32_16x16x32_bf16 v[84:87], v[204:207], v[52:55], v[84:87]\n"
"s_add_u32 s53, s49, 512\nv_mfma_f32_16x16x32_bf16 v[88:91], a[72:75], v[52:55], v[88:91]\nbuffer_load_dwordx4 v10, s[12:15], s53 offen lds\nv_mfma_f32_16x16x32_bf16 v[92:95], a[76:79], v[52:55], v[92:95]\n"
"ds_read_b128 v[52:55], v35 offset:20480\ns_waitcnt lgkmcnt(3)\nv_mfma_f32_16x16x32_bf16 v[64:67], v[208:211], v[40:43], v[64:67]\nv_mfma_f32_16x16x32_bf16 v[68:71], v[212:215], v[40:43], v[68:71]\n"
"v_mfma_f32_16x16x32_bf16 v[72:75], a[80:83], v[40:43], v[72:75]\nv_mfma_f32_16x16x32_bf16 v[76:79], a[84:87], v[40:43], v[76:79]\nds_read_b128 v[40:43], v32 offset:24576\ns_waitcnt lgkmcnt(3)\n"
"v_mfma_f32_16x16x32_bf16 v[80:83], v[208:211], v[44:47], v[80:83]\nv_mfma_f32_16x16x32_bf16 v[84:87], v[212:215], v[44:47], v[84:87]\nv_mfma_f32_16x16x32_bf16 v[88:91], a[80:83], v[44:47], v[88:91]\nv_mfma_f32_16x16x32_bf16 v[92:95], a[84:87], v[44:47], v[92:95]\n"
"ds_read_b128 v[44:47], v33 offset:24576\ns_add_u32 m0, s52, 20480\ns_waitcnt lgkmcnt(3)\nv_mfma_f32_16x16x32_bf16 v[64:67], v[216:219], v[48:51], v[64:67]\n"
"s_add_u32 s54, s51, 32768\ns_cmp_eq_u32 s54, 163840\nv_mfma_f32_16x16x32_bf16 v[68:71], v[220:223], v[48:51], v[68:71]\nv_mfma_f32_16x16x32_bf16 v[72:75], a[88:91], v[48:51], v[72:75]\n"
"v_mfma_f32_16x16x32_bf16 v[76:79], a[92:95], v[48:51], v[76:79]\ns_cselect_b32 s54, 0, s54\ns_sub_u32 s54, s54, s51\nds_read_b128 v[48:51], v34 offset:24576\n"
"s_add_u32 s53, s49, 640\ns_waitcnt lgkmcnt(3)\nv_mfma_f32_16x16x32_bf16 v[80:83], v[216:219], v[52:55], v[80:83]\nbuffer_load_dwordx4 v10, s[12:15], s53 offen lds\n"
"v_mfma_f32_16x16x32_bf16 v[84:87], v[220:223], v[52:55], v[84:87]\nv_mfma_f32_16x16x32_bf16 v[88:91], a[88:91], v[52:55], v[88:91]\nv_mfma_f32_16x16x32_bf16 v[92:95], a[92:95], v[52:55], v[92:95]\nds_read_b128 v[52:55], v35 offset:24576\n"
"s_waitcnt lgkmcnt(3)\nv_mfma_f32_16x16x32_bf16 v[64:67], v[224:227], v[40:43], v[64:67]\nv_add_u32 v32, s54, v32\nv_mfma_f32_16x16x32_bf16 v[68:71], v[228:231], v[40:43], v[68:71]\n"
"v_mfma_f32_16x16x32_bf16 v[72:75], a[96:99], v[40:43], v[72:75]\nv_mfma_f32_16x16x32_bf16 v[76:79], a[100:103], v[40:43], v[76:79]\nds_read_b128 v[40:43], v32 offset:0\ns_waitcnt lgkmcnt(3)\n"
"v_mfma_f32_16x16x32_bf16 v[80:83], v[224:227], v[44:47], v[80:83]\nv_add_u32 v33, s54, v33\nv_mfma_f32_16x16x32_bf16 v[84:87], v[228:231], v[44:47], v[84:87]\ns_add_u32 m0, s52, 24576\n"
"v_mfma_f32_16x16x32_bf16 v[88:91], a[96:99], v[44:47], v[88:91]\ns_add_u32 s53, s49, 768\nv_mfma_f32_16x16x32_bf16 v[92:95], a[100:103], v[44:47], v[92:95]\nds_read_b128 v[44:47], v33 offset:0\n"
"buffer_load_dwordx4 v10, s[12:15], s53 offen lds\ns_waitcnt lgkmcnt(3)\nv_mfma_f32_16x16x32_bf16 v[64:67], v[232:235], v[48:51], v[64:67]\nv_add_u32 v34, s54, v34\n"
"v_mfma_f32_16x16x32_bf16 v[68:71], v[236:239], v[48:51], v[68:71]\nv_mfma_f32_16x16x32_bf16 v[72:75], a[104:107], v[48:51], v[72:75]\nv_mfma_f32_16x16x32_bf16 v[76:79], a[108:111], v[48:51], v[76:79]\nds_read_b128 v[48:51], v34 offset:0\n"
"s_waitcnt lgkmcnt(3)\nv_mfma_f32_16x16x32_bf16 v[80:83], v[232:235], v[52:55], v[80:83]\nv_add_u32 v35, s54, v35\nv_mfma_f32_16x16x32_bf16 v[84:87], v[236:239], v[52:55], v[84:87]\n"
"v_mfma_f32_16x16x32_bf16 v[88:91], a[104:107], v[52:55], v[88:91]\nv_mfma_f32_16x16x32_bf16 v[92:95], a[108:111], v[52:55], v[92:95]\nds_read_b128 v[52:55], v35 offset:0\ns_mov_b32 s52, s47\n"
"s_add_u32 s47, s51, s32\ns_add_u32 s51, s51, s54\ns_add_u32 s49, s49, 28672\ns_waitcnt vmcnt(7)\n"
"s_barrier\ns_waitcnt lgkmcnt(3)\nv_mfma_f32_16x16x32_bf16 v[96:99], v[128:131], v[40:43], 0\nv_mov_b32 v36, s48\n"
"s_add_u32 s48, s48, 1\ns_nop 0\nbuffer_store_dword v36, v19, s[24:27], s33 offen sc1\nv_mfma_f32_16x16x32_bf16 v[100:103], v[132:135], v[40:43], 0\n"
"v_mfma_f32_16x16x32_bf16 v[104:107], a[0:3], v[40:43], 0\nv_mfma_f32_16x16x32_bf16 v[108:111], a[4:7], v[40:43], 0\nds_read_b128 v[40:43], v32 offset:4096\nv_cvt_pk_bf16_f32 v56, v64, v65\n"
"s_waitcnt lgkmcnt(3)\nv_mfma_f32_16x16x32_bf16 v[112:115], v[128:131], v[44:47], 0\nv_cvt_pk_bf16_f32 v57, v66, v67\nv_mfma_f32_16x16x32_bf16 v[116:119], v[132:135], v[44:47], 0\n"
"v_cvt_pk_bf16_f32 v58, v68, v69\nv_mfma_f32_16x16x32_bf16 v[120:123], a[0:3], v[44:47], 0\nv_cvt_pk_bf16_f32 v59, v70, v71\nv_mfma_f32_16x16x32_bf16 v[124:127], a[4:7], v[44:47], 0\n"
"ds_read_b128 v[44:47], v33 offset:4096\ns_waitcnt lgkmcnt(3)\nv_mfma_f32_16x16x32_bf16 v[96:99], v[136:139], v[48:51], v[96:99]\nv_mfma_f32_16x16x32_bf16 v[100:103], v[140:143], v[48:51], v[100:103]\n"
"buffer_store_dwordx4 v[56:59], v15, s[20:23], s50 offen sc1\nv_mfma_f32_16x16x32_bf16 v[104:107], a[8:11], v[48:51], v[104:107]\nv_cvt_pk_bf16_f32 v60, v80, v81\nv_mfma_f32_16x16x32_bf16 v[108:111], a[12:15], v[48:51], v[108:111]\n"
"ds_read_b128 v[48:51], v34 offset:4096\nv_cvt_pk_bf16_f32 v61, v82, v83\ns_waitcnt lgkmcnt(3)\nv_mfma_f32_16x16x32_bf16 v[112:115], v[136:139], v[52:55], v[112:115]\n"
"v_cvt_pk_bf16_f32 v62, v84, v85\nv_mfma_f32_16x16x32_bf16 v[116:119], v[140:143], v[52:55], v[116:119]\nv_cvt_pk_bf16_f32 v63, v86, v87\nv_mfma_f32_16x16x32_bf16 v[120:123], a[8:11], v[52:55], v[120:123]\n"
"v_mfma_f32_16x16x32_bf16 v[124:127], a[12:15], v[52:55], v[124:127]\nds_read_b128 v[52:55], v35 offset:4096\ns_waitcnt lgkmcnt(3)\nv_mfma_f32_16x16x32_bf16 v[96:99], v[144:147], v[40:43], v[96:99]\n"
"buffer_store_dwordx4 v[60:63], v16, s[20:23], s50 offen sc1\nv_mfma_f32_16x16x32_bf16 v[100:103], v[148:151], v[40:43], v[100:103]\nv_cvt_pk_bf16_f32 v56, v72, v73\nv_mfma_f32_16x16x32_bf16 v[104:107], a[16:19], v[40:43], v[104:107]\n"
"v_cvt_pk_bf16_f32 v57, v74, v75\nv_mfma_f32_16x16x32_bf16 v[108:111], a[20:23], v[40:43], v[108:111]\nds_read_b128 v[40:43], v32 offset:8192\nv_cvt_pk_bf16_f32 v58, v76, v77\n"
"s_waitcnt lgkmcnt(3)\nv_mfma_f32_16x16x32_bf16 v[112:115], v[144:147], v[44:47], v[112:115]\nv_cvt_pk_bf16_f32 v59, v78, v79\nv_mfma_f32_16x16x32_bf16 v[116:119], v[148:151], v[44:47], v[116:119]\n"
"v_mfma_f32_16x16x32_bf16 v[120:123], a[16:19], v[44:47], v[120:123]\nv_mfma_f32_16x16x32_bf16 v[124:127], a[20:23], v[44:47], v[124:127]\nds_read_b128 v[44:47], v33 offset:8192\nbuffer_store_dwordx4 v[56:59], v17, s[20:23], s50 offen sc1\n"
"s_waitcnt lgkmcnt(3)\nv_mfma_f32_16x16x32_bf16 v[96:99], v[152:155], v[48:51], v[96:99]\nv_cvt_pk_bf16_f32 v60, v88, v89\nv_mfma_f32_16x16x32_bf16 v[100:103], v[156:159], v[48:51], v[100:103]\n"
"v_cvt_pk_bf16_f32 v61, v90, v91\nv_mfma_f32_16x16x32_bf16 v[104:107], a[24:27], v[48:51], v[104:107]\nv_cvt_pk_bf16_f32 v62, v92, v93\nv_mfma_f32_16x16x32_bf16 v[108:111], a[28:31], v[48:51], v[108:111]\n"
"ds_read_b128 v[48:51], v34 offset:8192\nv_cvt_pk_bf16_f32 v63, v94, v95\ns_waitcnt lgkmcnt(3)\nv_mfma_f32_16x16x32_bf16 v[112:115], v[152:155], v[52:55], v[112:115]\n"
"v_mfma_f32_16x16x32_bf16 v[116:119], v[156:159], v[52:55], v[116:119]\nv_mfma_f32_16x16x32_bf16 v[120:123], a[24:27], v[52:55], v[120:123]\nbuffer_store_dwordx4 v[60:63], v18, s[20:23], s50 offen sc1\nv_mfma_f32_16x16x32_bf16 v[124:127], a[28:31], v[52:55], v[124:127]\n"
"ds_read_b128 v[52:55], v35 offset:8192\ns_add_u32 s50, s50, s36\ns_waitcnt lgkmcnt(3)\nv_mfma_f32_16x16x32_bf16 v[96:99], v[160:163], v[40:43], v[96:99]\n"
"s_add_u32 m0, s52, 0\nv_mfma_f32_16x16x32_bf16 v[100:103], v[164:167], v[40:43], v[100:103]\ns_add_u32 s53, s49, 0\nv_mfma_f32_16x16x32_bf16 v[104:107], a[32:35], v[40:43], v[104:107]\n"
"buffer_load_dwordx4 v10, s[12:15], s53 offen lds\nv_mfma_f32_16x16x32_bf16 v[108:111], a[36:39], v[40:43], v[108:111]\nds_read_b128 v[40:43], v32 offset:12288\ns_waitcnt lgkmcnt(3)\n"
"v_mfma_f32_16x16x32_bf16 v[112:115], v[160:163], v[44:47], v[112:115]\nv_mfma_f32_16x16x32_bf16 v[116:119], v[164:167], v[44:47], v[116:119]\nv_mfma_f32_16x16x32_bf16 v[120:123], a[32:35], v[44:47], v[120:123]\nv_mfma_f32_16x16x32_bf16 v[124:127], a[36:39], v[44:47], v[124:127]\n"
"ds_read_b128 v[44:47], v33 offset:12288\ns_waitcnt lgkmcnt(3)\nv_mfma_f32_16x16x32_bf16 v[96:99], v[168:171], v[48:51], v[96:99]\nv_mfma_f32_16x16x32_bf16 v[100:103], v[172:175], v[48:51], v[100:103]\n"
"v_mfma_f32_16x16x32_bf16 v[104:107], a[40:43], v[48:51], v[104:107]\nv_mfma_f32_16x16x32_bf16 v[108:111], a[44:47], v[48:51], v[108:111]\nds_read_b128 v[48:51], v34 offset:12288\ns_add_u32 m0, s52, 4096\n"
"s_waitcnt lgkmcnt(3)\nv_mfma_f32_16x16x32_bf16 v[112:115], v[168:171], v[52:55], v[112:115]\ns_add_u32 s53, s49, 128\nv_mfma_f32_16x16x32_bf16 v[116:119], v[172:175], v[52:55], v[116:119]\n"
"buffer_load_dwordx4 v10, s[12:15], s53 offen lds\nv_mfma_f32_16x16x32_bf16 v[120:123], a[40:43], v[52:55], v[120:123]\nv_mfma_f32_16x16x32_bf16 v[124:127], a[44:47], v[52:55], v[124:127]\nds_read_b128 v[52:55], v35 offset:12288\n"
"s_waitcnt lgkmcnt(3)\nv_mfma_f32_16x16x32_bf16 v[96:99], v[176:179], v[40:43], v[96:99]\nv_mfma_f32_16x16x32_bf16 v[100:103], v[180:183], v[40:43], v[100:103]\nv_mfma_f32_16x16x32_bf16 v[104:107], a[48:51], v[40:43], v[104:107]\n"
"v_mfma_f32_16x16x32_bf16 v[108:111], a[52:55], v[40:43], v[108:111]\nds_read_b128 v[40:43], v32 offset:16384\ns_waitcnt lgkmcnt(3)\nv_mfma_f32_16x16x32_bf16 v[112:115], v[176:179], v[44:47], v[112:115]\n"
"v_mfma_f32_16x16x32_bf16 v[116:119], v[180:183], v[44:47], v[116:119]\nv_mfma_f32_16x16x32_bf16 v[120:123], a[48:51], v[44:47], v[120:123]\ns_add_u32 m0, s52, 8192\nv_mfma_f32_16x16x32_bf16 v[124:127], a[52:55], v[44:47], v[124:127]\n"
"ds_read_b128 v[44:47], v33 offset:16384\ns_add_u32 s53, s49, 256\ns_waitcnt lgkmcnt(3)\nv_mfma_f32_16x16x32_bf16 v[96:99], v[184:187], v[48:51], v[96:99]\n"
"buffer_load_dwordx4 v10, s[12:15], s53 offen lds\nv_mfma_f32_16x16x32_bf16 v[100:103], v[188:191], v[48:51], v[100:103]\nv_mfma_f32_16x16x32_bf16 v[104:107], a[56:59], v[48:51], v[104:107]\nv_mfma_f32_16x16x32_bf16 v[108:111], a[60:63], v[48:51], v[108:111]\n"
"ds_read_b128 v[48:51], v34 offset:16384\ns_waitcnt lgkmcnt(3)\nv_mfma_f32_16x16x32_bf16 v[112:115], v[184:187], v[52:55], v[112:115]\nv_mfma_f32_16x16x32_bf16 v[116:119], v[188:191], v[52:55], v[116:119]\n"
"v_mfma_f32_16x16x32_bf16 v[120:123], a[56:59], v[52:55], v[120:123]\nv_mfma_f32_16x16x32_bf16 v[124:127], a[60:63], v[52:55], v[124:127]\nds_read_b128 v[52:55], v35 offset:16384\ns_waitcnt lgkmcnt(3)\n"
"v_mfma_f32_16x16x32_bf16 v[96:99], v[192:195], v[40:43], v[96:99]\nv_mfma_f32_16x16x32_bf16 v[100:103], v[196:199], v[40:43], v[100:103]\ns_add_u32 m0, s52, 12288\nv_mfma_f32_16x16x32_bf16 v[104:107], a[64:67], v[40:43], v[104:107]\n"
"s_add_u32 s53, s49, 384\nv_mfma_f32_16x16x32_bf16 v[108:111], a[68:71], v[40:43], v[108:111]\nds_read_b128 v[40:43], v32 offset:20480\nbuffer_load_dwordx4 v10, s[12:15], s53 offen lds\n"
"s_waitcnt lgkmcnt(3)\nv_mfma_f32_16x16x32_bf16 v[112:115], v[192:195], v[44:47], v[112:115]\nv_mfma_f32_16x16x32_bf16 v[116:119], v[196:199], v[44:47], v[116:119]\nv_mfma_f32_16x16x32_bf16 v[120:123], a[64:67], v[44:47], v[120:123]\n"
"v_mfma_f32_16x16x32_bf16 v[124:127], a[68:71], v[44:47], v[124:127]\nds_read_b128 v[44:47], v33 offset:20480\ns_waitcnt lgkmcnt(3)\nv_mfma_f32_16x16x32_bf16 v[96:99], v[200:203], v[48:51], v[96:99]\n"
"v_mfma_f32_16x16x32_bf16 v[100:103], v[204:207], v[48:51], v[100:103]\nv_mfma_f32_16x16x32_bf16 v[104:107], a[72:75], v[48:51], v[104:107]\nv_mfma_f32_16x16x32_bf16 v[108:111], a[76:79], v[48:51], v[108:111]\nds_read_b128 v[48:51], v34 offset:20480\n"
"s_waitcnt lgkmcnt(3)\nv_mfma_f32_16x16x32_bf16 v[112:115], v[200:203], v[52:55], v[112:115]\ns_add_u32 m0, s52, 16384\nv_mfma_f32_16x16x32_bf16 v[116:119], v[204:207], v[52:55], v[116:119]\n"
"s_add_u32 s53, s49, 512\nv_mfma_f32_16x16x32_bf16 v[120:123], a[72:75], v[52:55], v[120:123]\nbuffer_load_dwordx4 v10, s[12:15], s53 offen lds\nv_mfma_f32_16x16x32_bf16 v[124:127], a[76:79], v[52:55], v[124:127]\n"
"ds_read_b128 v[52:55], v35 offset:20480\ns_waitcnt lgkmcnt(3)\nv_mfma_f32_16x16x32_bf16 v[96:99], v[208:211], v[40:43], v[96:99]\nv_mfma_f32_16x16x32_bf16 v[100:103], v[212:215], v[40:43], v[100:103]\n"
"v_mfma_f32_16x16x32_bf16 v[104:107], a[80:83], v[40:43], v[104:107]\nv_mfma_f32_16x16x32_bf16 v[108:111], a[84:87], v[40:43], v[108:111]\nds_read_b128 v[40:43], v32 offset:24576\ns_waitcnt lgkmcnt(3)\n"
"v_mfma_f32_16x16x32_bf16 v[112:115], v[208:211], v[44:47], v[112:115]\nv_mfma_f32_16x16x32_bf16 v[116:119], v[212:215], v[44:47], v[116:119]\nv_mfma_f32_16x16x32_bf16 v[120:123], a[80:83], v[44:47], v[120:123]\nv_mfma_f32_16x16x32_bf16 v[124:127], a[84:87], v[44:47], v[124:127]\n"
"ds_read_b128 v[44:47], v33 offset:24576\ns_add_u32 m0, s52, 20480\ns_waitcnt lgkmcnt(3)\nv_mfma_f32_16x16x32_bf16 v[96:99], v[216:219], v[48:51], v[96:99]\n"
"s_add_u32 s54, s51, 32768\ns_cmp_eq_u32 s54, 163840\nv_mfma_f32_16x16x32_bf16 v[100:103], v[220:223], v[48:51], v[100:103]\nv_mfma_f32_16x16x32_bf16 v[104:107], a[88:91], v[48:51], v[104:107]\n"
"v_mfma_f32_16x16x32_bf16 v[108:111], a[92:95], v[48:51], v[108:111]\ns_cselect_b32 s54, 0, s54\ns_sub_u32 s54, s54, s51\nds_read_b128 v[48:51], v34 offset:24576\n"
"s_add_u32 s53, s49, 640\ns_waitcnt lgkmcnt(3)\nv_mfma_f32_16x16x32_bf16 v[112:115], v[216:219], v[52:55], v[112:115]\nbuffer_load_dwordx4 v10, s[12:15], s53 offen lds\n"
"v_mfma_f32_16x16x32_bf16 v[116:119], v[220:223], v[52:55], v[116:119]\nv_mfma_f32_16x16x32_bf16 v[120:123], a[88:91], v[52:55], v[120:123]\nv_mfma_f32_16x16x32_bf16 v[124:127], a[92:95], v[52:55], v[124:127]\nds_read_b128 v[52:55], v35 offset:24576\n"
"s_waitcnt lgkmcnt(3)\nv_mfma_f32_16x16x32_bf16 v[96:99], v[224:227], v[40:43], v[96:99]\nv_add_u32 v32, s54, v32\nv_mfma_f32_16x16x32_bf16 v[100:103], v[228:231], v[40:43], v[100:103]\n"
"v_mfma_f32_16x16x32_bf16 v[104:107], a[96:99], v[40:43], v[104:107]\nv_mfma_f32_16x16x32_bf16 v[108:111], a[100:103], v[40:43], v[108:111]\nds_read_b128 v[40:43], v32 offset:0\ns_waitcnt lgkmcnt(3)\n"
"v_mfma_f32_16x16x32_bf16 v[112:115], v[224:227], v[44:47], v[112:115]\nv_add_u32 v33, s54, v33\nv_mfma_f32_16x16x32_bf16 v[116:119], v[228:231], v[44:47], v[116:119]\ns_add_u32 m0, s52, 24576\n"
"v_mfma_f32_16x16x32_bf16 v[120:123], a[96:99], v[44:47], v[120:123]\ns_add_u32 s53, s49, 768\nv_mfma_f32_16x16x32_bf16 v[124:127], a[100:103], v[44:47], v[124:127]\nds_read_b128 v[44:47], v33 offset:0\n"
"buffer_load_dwordx4 v10, s[12:15], s53 offen lds\ns_waitcnt lgkmcnt(3)\nv_mfma_f32_16x16x32_bf16 v[96:99], v[232:235], v[48:51], v[96:99]\nv_add_u32 v34, s54, v34\n"
"v_mfma_f32_16x16x32_bf16 v[100:103], v[236:239], v[48:51], v[100:103]\nv_mfma_f32_16x16x32_bf16 v[104:107], a[104:107], v[48:51], v[104:107]\nv_mfma_f32_16x16x32_bf16 v[108:111], a[108:111], v[48:51], v[108:111]\nds_read_b128 v[48:51], v34 offset:0\n"
"s_waitcnt lgkmcnt(3)\nv_mfma_f32_16x16x32_bf16 v[112:115], v[232:235], v[52:55], v[112:115]\nv_add_u32 v35, s54, v35\nv_mfma_f32_16x16x32_bf16 v[116:119], v[236:239], v[52:55], v[116:119]\n"
"v_mfma_f32_16x16x32_bf16 v[120:123], a[104:107], v[52:55], v[120:123]\nv_mfma_f32_16x16x32_bf16 v[124:127], a[108:111], v[52:55], v[124:127]\nds_read_b128 v[52:55], v35 offset:0\ns_mov_b32 s52, s47\n"
"s_add_u32 s47, s51, s32\ns_add_u32 s51, s51, s54\ns_add_u32 s49, s49, 28672\ns_cmp_lt_u32 s49, 1921024\n"
"s_cbranch_scc1 fg0_loop\ns_nop 7\nv_cvt_pk_bf16_f32 v56, v96, v97\nv_cvt_pk_bf16_f32 v57, v98, v99\n"
"v_cvt_pk_bf16_f32 v58, v100, v101\nv_cvt_pk_bf16_f32 v59, v102, v103\ns_nop 0\ns_nop 0\n"
"buffer_store_dwordx4 v[56:59], v15, s[20:23], s50 offen sc1\nv_cvt_pk_bf16_f32 v60, v112, v113\nv_cvt_pk_bf16_f32 v61, v114, v115\nv_cvt_pk_bf16_f32 v62, v116, v117\n"
"v_cvt_pk_bf16_f32 v63, v118, v119\ns_nop 0\ns_nop 0\nbuffer_store_dwordx4 v[60:63], v16, s[20:23], s50 offen sc1\n"
"v_cvt_pk_bf16_f32 v56, v104, v105\nv_cvt_pk_bf16_f32 v57, v106, v107\nv_cvt_pk_bf16_f32 v58, v108, v109\nv_cvt_pk_bf16_f32 v59, v110, v111\n"
"s_nop 0\ns_nop 0\nbuffer_store_dwordx4 v[56:59], v17, s[20:23], s50 offen sc1\nv_cvt_pk_bf16_f32 v60, v120, v121\n"
"v_cvt_pk_bf16_f32 v61, v122, v123\nv_cvt_pk_bf16_f32 v62, v124, v125\nv_cvt_pk_bf16_f32 v63, v126, v127\ns_nop 0\n"
"s_nop 0\nbuffer_store_dwordx4 v[60:63], v18, s[20:23], s50 offen sc1\ns_add_u32 s48, s28, 64\nv_mov_b32 v36, s48\n"
"s_waitcnt vmcnt(0) lgkmcnt(0)\ns_barrier\nbuffer_store_dword v36, v19, s[24:27], s33 offen sc1\nfg0_end:\n"
    : "+{v[244:251]}"(w) : "{s[8:23]}"(s0), "{s[24:39]}"(s1), "{v[8:23]}"(v0) : "v24", "v25", "v26", "v27", "v28", "v29", "v30", "v31", "v32", "v33", "v34", "v35", "v36", "v37", "v38", "v39", "v40", "v41", "v42", "v43", "v44", "v45", "v46", "v47", "v48", "v49", "v50", "v51", "v52", "v53", "v54", "v55", "v56", "v57", "v58", "v59", "v60", "v61", "v62", "v63", "v64", "v65", "v66", "v67", "v68", "v69", "v70", "v71", "v72", "v73", "v74", "v75", "v76", "v77", "v78", "v79", "v80", "v81", "v82", "v83", "v84", "v85", "v86", "v87", "v88", "v89", "v90", "v91", "v92", "v93", "v94", "v95", "v96", "v97", "v98", "v99", "v100", "v101", "v102", "v103", "v104", "v105", "v106", "v107", "v108", "v109", "v110", "v111", "v112", "v113", "v114", "v115", "v116", "v117", "v118", "v119", "v120", "v121", "v122", "v123", "v124", "v125", "v126", "v127", "v128", "v129", "v130", "v131", "v132", "v133", "v134", "v135", "v136", "v137", "v138", "v139", "v140", "v141", "v142", "v143", "v144", "v145", "v146", "v147", "v148", "v149", "v150", "v151", "v152", "v153", "v154", "v155", "v156", "v157", "v158", "v159", "v160", "v161", "v162", "v163", "v164", "v165", "v166", "v167", "v168", "v169", "v170", "v171", "v172", "v173", "v174", "v175", "v176", "v177", "v178", "v179", "v180", "v181", "v182", "v183", "v184", "v185", "v186", "v187", "v188", "v189", "v190", "v191", "v192", "v193", "v194", "v195", "v196", "v197", "v198", "v199", "v200", "v201", "v202", "v203", "v204", "v205", "v206", "v207", "v208", "v209", "v210", "v211", "v212", "v213", "v214", "v215", "v216", "v217", "v218", "v219", "v220", "v221", "v222", "v223", "v224", "v225", "v226", "v227", "v228", "v229", "v230", "v231", "v232", "v233", "v234", "v235", "v236", "v237", "v238", "v239", "v240", "v241", "v242", "v243", "a0", "a1", "a2", "a3", "a4", "a5", "a6", "a7", "a8", "a9", "a10", "a11", "a12", "a13", "a14", "a15", "a16", "a17", "a18", "a19", "a20", "a21", "a22", "a23", "a24", "a25", "a26", "a27", "a28", "a29", "a30", "a31", "a32", "a33", "a34", "a35", "a36", "a37", "a38", "a39", "a40", "a41", "a42", "a43", "a44", "a45", "a46", "a47", "a48", "a49", "a50", "a51", "a52", "a53", "a54", "a55", "a56", "a57", "a58", "a59", "a60", "a61", "a62", "a63", "a64", "a65", "a66", "a67", "a68", "a69", "a70", "a71", "a72", "a73", "a74", "a75", "a76", "a77", "a78", "a79", "a80", "a81", "a82", "a83", "a84", "a85", "a86", "a87", "a88", "a89", "a90", "a91", "a92", "a93", "a94", "a95", "a96", "a97", "a98", "a99", "a100", "a101", "a102", "a103", "a104", "a105", "a106", "a107", "a108", "a109", "a110", "a111", "a112", "a113", "a114", "a115", "a116", "a117", "a118", "a119", "a120", "a121", "a122", "a123", "a124", "a125", "a126", "a127", "a128", "a129", "a130", "a131", "a132", "a133", "a134", "a135", "a136", "a137", "a138", "a139", "a140", "a141", "a142", "a143", "a144", "a145", "a146", "a147", "a148", "a149", "a150", "a151", "a152", "a153", "a154", "a155", "a156", "a157", "a158", "a159", "a160", "a161", "a162", "a163", "a164", "a165", "a166", "a167", "a168", "a169", "a170", "a171", "a172", "a173", "a174", "a175", "a176", "a177", "a178", "a179", "a180", "a181", "a182", "a183", "a184", "a185", "a186", "a187", "a188", "a189", "a190", "a191", "a192", "a193", "a194", "a195", "a196", "a197", "a198", "a199", "a200", "a201", "a202", "a203", "a204", "a205", "a206", "a207", "a208", "a209", "a210", "a211", "a212", "a213", "a214", "a215", "a216", "a217", "a218", "a219", "a220", "a221", "a222", "a223", "s40", "s41", "s42", "s43", "s44", "s45", "s46", "s47", "s48", "s49", "s50", "s51", "s52", "s53", "s54", "s55", "s56", "s57", "s58", "s59", "s60", "s61", "vcc", "scc", "memory");
    return o;
}
DI unsigned gemm_asm1(const GCtx& c, u8v& w) {
    const u16v s0 = {RFL(c.rw1[0]), RFL(c.rw1[1]), RFL(c.rw1[2]), RFL(c.rw1[3]), RFL(c.rx[0]), RFL(c.rx[1]), RFL(c.rx[2]), RFL(c.rx[3]), RFL(c.rexp[0]), RFL(c.rexp[1]), RFL(c.rexp[2]), RFL(c.rexp[3]), RFL(c.rout[0]), RFL(c.rout[1]), RFL(c.rout[2]), RFL(c.rout[3])}, s1 = {RFL(c.rhb[0]), RFL(c.rhb[1]), RFL(c.rhb[2]), RFL(c.rhb[3]), RFL(c.tag), RFL(c.rot), RFL(c.sw1), RFL(c.mw), RFL(c.mx), RFL(c.sfg), RFL(c.stay), RFL(c.csc), RFL(c.ostr), RFL(c.kb), RFL(c.p1), RFL(c.p2)}, v0 = {c.vr0, c.vr1, c.vx, c.wa, c.xa, c.vexp, c.vimp, c.vo0, c.vo1, c.vo2, c.vo3, c.vfl, c.vpf, c.vpo, c.vk, c.p5};
    unsigned o = 0;
    asm volatile(
"s_nop 4\ns_add_u32 s40, s29, 0\ns_add_u32 s41, s29, 1\ns_add_u32 s42, s29, 2\n"
"s_add_u32 s43, s29, 3\nv_xor_b32 v24, 0, v11\nv_xor_b32 v25, 64, v11\nv_xor_b32 v26, 128, v11\n"
"v_xor_b32 v27, 192, v11\nv_xor_b32 v28, 2064, v11\nv_xor_b32 v29, 2128, v11\nv_xor_b32 v30, 2192, v11\n"
"v_xor_b32 v31, 2256, v11\nv_xor_b32 v40, 32, v22\nds_read_b64_tr_b16 a[128:129], v22 offset:0\nds_read_b64_tr_b16 a[130:131], v22 offset:512\n"
"ds_read_b64_tr_b16 a[132:133], v22 offset:4096\nds_read_b64_tr_b16 a[134:135], v22 offset:4608\nds_read_b64_tr_b16 a[136:137], v22 offset:8192\nds_read_b64_tr_b16 a[138:139], v22 offset:8704\n"
"ds_read_b64_tr_b16 a[140:141], v22 offset:12288\nds_read_b64_tr_b16 a[142:143], v22 offset:12800\ns_lshl_b32 s53, s41, 19\ns_add_u32 s53, s53, s30\n"
"s_add_u32 m0, s31, 65536\ns_add_u32 s53, s53, 0\nbuffer_load_dwordx4 v8, s[8:11], s53 offen lds\nbuffer_load_dwordx4 v9, s[8:11], s53 offen offset:1024 lds\n"
"s_waitcnt lgkmcnt(0)\nds_read_b64_tr_b16 a[144:145], v22 offset:16384\nds_read_b64_tr_b16 a[146:147], v22 offset:16896\nds_read_b64_tr_b16 a[148:149], v22 offset:20480\n"
"ds_read_b64_tr_b16 a[150:151], v22 offset:20992\nds_read_b64_tr_b16 a[152:153], v22 offset:24576\nds_read_b64_tr_b16 a[154:155], v22 offset:25088\nds_read_b64_tr_b16 a[156:157], v22 offset:28672\n"
"ds_read_b64_tr_b16 a[158:159], v22 offset:29184\ns_lshl_b32 s53, s41, 19\ns_add_u32 s53, s53, s30\ns_add_u32 m0, s31, 73728\n"
"s_add_u32 s53, s53, 131072\nbuffer_load_dwordx4 v8, s[8:11], s53 offen lds\nbuffer_load_dwordx4 v9, s[8:11], s53 offen offset:1024 lds\ns_waitcnt lgkmcnt(0)\n"
"ds_read_b64_tr_b16 a[160:161], v40 offset:0\nds_read_b64_tr_b16 a[162:163], v40 offset:512\nds_read_b64_tr_b16 a[164:165], v40 offset:4096\nds_read_b64_tr_b16 a[166:167], v40 offset:4608\n"
"ds_read_b64_tr_b16 a[168:169], v40 offset:8192\nds_read_b64_tr_b16 a[170:171], v40 offset:8704\nds_read_b64_tr_b16 a[172:173], v40 offset:12288\nds_read_b64_tr_b16 a[174:175], v40 offset:12800\n"
"s_lshl_b32 s53, s41, 19\ns_add_u32 s53, s53, s30\ns_add_u32 m0, s31, 81920\ns_add_u32 s53, s53, 262144\n"
"buffer_load_dwordx4 v8, s[8:11], s53 offen lds\nbuffer_load_dwordx4 v9, s[8:11], s53 offen offset:1024 lds\ns_waitcnt lgkmcnt(0)\nds_read_b64_tr_b16 a[176:177], v40 offset:16384\n"
"ds_read_b64_tr_b16 a[178:179], v40 offset:16896\nds_read_b64_tr_b16 a[180:181], v40 offset:20480\nds_read_b64_tr_b16 a[182:183], v40 offset:20992\nds_read_b64_tr_b16 a[184:185], v40 offset:24576\n"
"ds_read_b64_tr_b16 a[186:187], v40 offset:25088\nds_read_b64_tr_b16 a[188:189], v40 offset:28672\nds_read_b64_tr_b16 a[190:191], v40 offset:29184\ns_lshl_b32 s53, s41, 19\n"
"s_add_u32 s53, s53, s30\ns_add_u32 m0, s31, 90112\ns_add_u32 s53, s53, 393216\nbuffer_load_dwordx4 v8, s[8:11], s53 offen lds\n"
"buffer_load_dwordx4 v9, s[8:11], s53 offen offset:1024 lds\ns_waitcnt lgkmcnt(0)\ns_waitcnt lgkmcnt(0)\nv_add_u32 v40, 65536, v24\n"
"v_add_u32 v41, 65536, v25\nv_add_u32 v42, 65536, v26\nv_add_u32 v43, 65536, v27\nv_add_u32 v44, 65536, v28\n"
"v_add_u32 v45, 65536, v29\nv_add_u32 v46, 65536, v30\nv_add_u32 v47, 65536, v31\nv_xor_b32 v32, 0, v12\n"
"v_xor_b32 v33, 512, v12\nv_xor_b32 v34, 64, v12\nv_xor_b32 v35, 576, v12\ns_mov_b32 s55, 0\n"
"s_waitcnt vmcnt(11)\ns_barrier\nds_read_b128 v[96:99], v24 offset:0\nds_read_b128 v[100:103], v28 offset:0\n"
"ds_read_b128 v[104:107], v25 offset:0\nds_read_b128 v[108:111], v29 offset:0\nds_read_b128 v[112:115], v26 offset:0\nds_read_b128 v[116:119], v30 offset:0\n"
"ds_read_b128 v[120:123], v27 offset:0\nds_read_b128 v[124:127], v31 offset:0\ns_waitcnt lgkmcnt(6)\nv_mfma_f32_16x16x32_bf16 v[64:67], v[96:99], a[128:131], 0\n"
"s_lshl_b32 s53, s42, 19\ns_add_u32 s53, s53, s30\nv_mfma_f32_16x16x32_bf16 v[68:71], v[96:99], a[160:163], 0\nds_read_b128 v[96:99], v24 offset:256\n"
"s_add_u32 m0, s31, 98304\ns_add_u32 s53, s53, 0\nv_mfma_f32_16x16x32_bf16 v[72:75], v[100:103], a[128:131], 0\nbuffer_load_dwordx4 v8, s[8:11], s53 offen lds\n"
"v_mfma_f32_16x16x32_bf16 v[76:79], v[100:103], a[160:163], 0\nds_read_b128 v[100:103], v28 offset:256\nbuffer_load_dwordx4 v9, s[8:11], s53 offen offset:1024 lds\ns_waitcnt lgkmcnt(6)\n"
"v_mfma_f32_16x16x32_bf16 v[64:67], v[104:107], a[132:135], v[64:67]\ns_lshl_b32 s53, s42, 19\ns_add_u32 s53, s53, s30\nv_mfma_f32_16x16x32_bf16 v[68:71], v[104:107], a[164:167], v[68:71]\n"
"ds_read_b128 v[104:107], v25 offset:256\ns_add_u32 m0, s31, 106496\ns_add_u32 s53, s53, 131072\nv_mfma_f32_16x16x32_bf16 v[72:75], v[108:111], a[132:135], v[72:75]\n"
"buffer_load_dwordx4 v8, s[8:11], s53 offen lds\nv_mfma_f32_16x16x32_bf16 v[76:79], v[108:111], a[164:167], v[76:79]\nds_read_b128 v[108:111], v29 offset:256\nbuffer_load_dwordx4 v9, s[8:11], s53 offen offset:1024 lds\n"
"s_waitcnt lgkmcnt(6)\nv_mfma_f32_16x16x32_bf16 v[64:67], v[112:115], a[136:139], v[64:67]\nv_mfma_f32_16x16x32_bf16 v[68:71], v[112:115], a[168:171], v[68:71]\nds_read_b128 v[112:115], v26 offset:256\n"
"v_mfma_f32_16x16x32_bf16 v[72:75], v[116:119], a[136:139], v[72:75]\nv_mfma_f32_16x16x32_bf16 v[76:79], v[116:119], a[168:171], v[76:79]\nds_read_b128 v[116:119], v30 offset:256\ns_waitcnt lgkmcnt(6)\n"
"v_mfma_f32_16x16x32_bf16 v[64:67], v[120:123], a[140:143], v[64:67]\nv_mfma_f32_16x16x32_bf16 v[68:71], v[120:123], a[172:175], v[68:71]\nds_read_b128 v[120:123], v27 offset:256\nv_mfma_f32_16x16x32_bf16 v[72:75], v[124:127], a[140:143], v[72:75]\n"
"v_mfma_f32_16x16x32_bf16 v[76:79], v[124:127], a[172:175], v[76:79]\nds_read_b128 v[124:127], v31 offset:256\ns_waitcnt lgkmcnt(6)\nv_mfma_f32_16x16x32_bf16 v[64:67], v[96:99], a[144:147], v[64:67]\n"
"v_mfma_f32_16x16x32_bf16 v[68:71], v[96:99], a[176:179], v[68:71]\nds_read_b128 v[96:99], v24 offset:16384\nv_mfma_f32_16x16x32_bf16 v[72:75], v[100:103], a[144:147], v[72:75]\nv_mfma_f32_16x16x32_bf16 v[76:79], v[100:103], a[176:179], v[76:79]\n"
"ds_read_b128 v[100:103], v28 offset:16384\ns_waitcnt lgkmcnt(6)\nv_mfma_f32_16x16x32_bf16 v[64:67], v[104:107], a[148:151], v[64:67]\nv_mfma_f32_16x16x32_bf16 v[68:71], v[104:107], a[180:183], v[68:71]\n"
"ds_read_b128 v[104:107], v25 offset:16384\nv_mfma_f32_16x16x32_bf16 v[72:75], v[108:111], a[148:151], v[72:75]\nv_mfma_f32_16x16x32_bf16 v[76:79], v[108:111], a[180:183], v[76:79]\nds_read_b128 v[108:111], v29 offset:16384\n"
"s_waitcnt lgkmcnt(6)\nv_mfma_f32_16x16x32_bf16 v[64:67], v[112:115], a[152:155], v[64:67]\nv_mfma_f32_16x16x32_bf16 v[68:71], v[112:115], a[184:187], v[68:71]\nds_read_b128 v[112:115], v26 offset:16384\n"
"v_mfma_f32_16x16x32_bf16 v[72:75], v[116:119], a[152:155], v[72:75]\nv_mfma_f32_16x16x32_bf16 v[76:79], v[116:119], a[184:187], v[76:79]\nds_read_b128 v[116:119], v30 offset:16384\ns_waitcnt lgkmcnt(6)\n"
"v_mfma_f32_16x16x32_bf16 v[64:67], v[120:123], a[156:159], v[64:67]\nv_mfma_f32_16x16x32_bf16 v[68:71], v[120:123], a[188:191], v[68:71]\nds_read_b128 v[120:123], v27 offset:16384\nv_mfma_f32_16x16x32_bf16 v[72:75], v[124:127], a[156:159], v[72:75]\n"
"v_mfma_f32_16x16x32_bf16 v[76:79], v[124:127], a[188:191], v[76:79]\nds_read_b128 v[124:127], v31 offset:16384\ns_waitcnt lgkmcnt(6)\nv_mfma_f32_16x16x32_bf16 v[80:83], v[96:99], a[128:131], 0\n"
"s_lshl_b32 s53, s42, 19\ns_add_u32 s53, s53, s30\nv_mfma_f32_16x16x32_bf16 v[84:87], v[96:99], a[160:163], 0\nds_read_b128 v[96:99], v24 offset:16640\n"
"s_add_u32 m0, s31, 114688\ns_add_u32 s53, s53, 262144\nv_mfma_f32_16x16x32_bf16 v[88:91], v[100:103], a[128:131], 0\nbuffer_load_dwordx4 v8, s[8:11], s53 offen lds\n"
"v_mfma_f32_16x16x32_bf16 v[92:95], v[100:103], a[160:163], 0\nds_read_b128 v[100:103], v28 offset:16640\nbuffer_load_dwordx4 v9, s[8:11], s53 offen offset:1024 lds\ns_waitcnt lgkmcnt(6)\n"
"v_mfma_f32_16x16x32_bf16 v[80:83], v[104:107], a[132:135], v[80:83]\nv_cvt_pk_bf16_f32 v128, v64, v65\nv_cvt_pk_bf16_f32 v129, v66, v67\nv_mfma_f32_16x16x32_bf16 v[84:87], v[104:107], a[164:167], v[84:87]\n"
"ds_read_b128 v[104:107], v25 offset:16640\nv_cvt_pk_bf16_f32 v130, v72, v73\nv_cvt_pk_bf16_f32 v131, v74, v75\nv_mfma_f32_16x16x32_bf16 v[88:91], v[108:111], a[132:135], v[88:91]\n"
"v_cvt_pk_bf16_f32 v132, v68, v69\nv_cvt_pk_bf16_f32 v133, v70, v71\nv_mfma_f32_16x16x32_bf16 v[92:95], v[108:111], a[164:167], v[92:95]\nds_read_b128 v[108:111], v29 offset:16640\n"
"v_cvt_pk_bf16_f32 v134, v76, v77\nv_cvt_pk_bf16_f32 v135, v78, v79\ns_waitcnt lgkmcnt(6)\nv_mfma_f32_16x16x32_bf16 v[80:83], v[112:115], a[136:139], v[80:83]\n"
"s_lshl_b32 s54, s40, 11\ns_add_u32 s54, s54, 0\nv_mfma_f32_16x16x32_bf16 v[84:87], v[112:115], a[168:171], v[84:87]\nds_read_b128 v[112:115], v26 offset:16640\n"
"buffer_store_dwordx4 v[128:131], v13, s[16:19], s54 offen sc1\nv_mfma_f32_16x16x32_bf16 v[88:91], v[116:119], a[136:139], v[88:91]\ns_add_u32 s54, s54, 262144\nv_mfma_f32_16x16x32_bf16 v[92:95], v[116:119], a[168:171], v[92:95]\n"
"ds_read_b128 v[116:119], v30 offset:16640\nbuffer_store_dwordx4 v[132:135], v13, s[16:19], s54 offen sc1\ns_waitcnt lgkmcnt(6)\nv_mfma_f32_16x16x32_bf16 v[80:83], v[120:123], a[140:143], v[80:83]\n"
"s_lshl_b32 s53, s42, 19\ns_add_u32 s53, s53, s30\nv_mfma_f32_16x16x32_bf16 v[84:87], v[120:123], a[172:175], v[84:87]\nds_read_b128 v[120:123], v27 offset:16640\n"
"s_add_u32 m0, s31, 122880\ns_add_u32 s53, s53, 393216\nv_mfma_f32_16x16x32_bf16 v[88:91], v[124:127], a[140:143], v[88:91]\nbuffer_load_dwordx4 v8, s[8:11], s53 offen lds\n"
"v_mfma_f32_16x16x32_bf16 v[92:95], v[124:127], a[172:175], v[92:95]\nds_read_b128 v[124:127], v31 offset:16640\nbuffer_load_dwordx4 v9, s[8:11], s53 offen offset:1024 lds\ns_waitcnt vmcnt(10)\n"
"s_barrier\ns_waitcnt lgkmcnt(6)\nv_mfma_f32_16x16x32_bf16 v[80:83], v[96:99], a[144:147], v[80:83]\nv_mfma_f32_16x16x32_bf16 v[84:87], v[96:99], a[176:179], v[84:87]\n"
"ds_read_b128 v[96:99], v24 offset:32768\nv_mfma_f32_16x16x32_bf16 v[88:91], v[100:103], a[144:147], v[88:91]\nv_mfma_f32_16x16x32_bf16 v[92:95], v[100:103], a[176:179], v[92:95]\nds_read_b128 v[100:103], v28 offset:32768\n"
"s_waitcnt lgkmcnt(6)\nv_mfma_f32_16x16x32_bf16 v[80:83], v[104:107], a[148:151], v[80:83]\nv_mfma_f32_16x16x32_bf16 v[84:87], v[104:107], a[180:183], v[84:87]\nds_read_b128 v[104:107], v25 offset:32768\n"
"v_mfma_f32_16x16x32_bf16 v[88:91], v[108:111], a[148:151], v[88:91]\nv_mfma_f32_16x16x32_bf16 v[92:95], v[108:111], a[180:183], v[92:95]\nds_read_b128 v[108:111], v29 offset:32768\ns_waitcnt lgkmcnt(6)\n"
"v_mfma_f32_16x16x32_bf16 v[80:83], v[112:115], a[152:155], v[80:83]\nv_mfma_f32_16x16x32_bf16 v[84:87], v[112:115], a[184:187], v[84:87]\nds_read_b128 v[112:115], v26 offset:32768\nv_mfma_f32_16x16x32_bf16 v[88:91], v[116:119], a[152:155], v[88:91]\n"
"v_mfma_f32_16x16x32_bf16 v[92:95], v[116:119], a[184:187], v[92:95]\nds_read_b128 v[116:119], v30 offset:32768\ns_waitcnt lgkmcnt(6)\nv_mfma_f32_16x16x32_bf16 v[80:83], v[120:123], a[156:159], v[80:83]\n"
"v_mfma_f32_16x16x32_bf16 v[84:87], v[120:123], a[188:191], v[84:87]\nds_read_b128 v[120:123], v27 offset:32768\nv_mfma_f32_16x16x32_bf16 v[88:91], v[124:127], a[156:159], v[88:91]\nv_mfma_f32_16x16x32_bf16 v[92:95], v[124:127], a[188:191], v[92:95]\n"
"ds_read_b128 v[124:127], v31 offset:32768\ns_waitcnt lgkmcnt(6)\nv_mfma_f32_16x16x32_bf16 v[64:67], v[96:99], a[128:131], 0\ns_lshl_b32 s53, s43, 19\n"
"s_add_u32 s53, s53, s30\nv_mfma_f32_16x16x32_bf16 v[68:71], v[96:99], a[160:163], 0\nds_read_b128 v[96:99], v24 offset:33024\ns_add_u32 m0, s31, 131072\n"
"s_add_u32 s53, s53, 0\nv_mfma_f32_16x16x32_bf16 v[72:75], v[100:103], a[128:131], 0\nbuffer_load_dwordx4 v8, s[8:11], s53 offen lds\nv_mfma_f32_16x16x32_bf16 v[76:79], v[100:103], a[160:163], 0\n"
"ds_read_b128 v[100:103], v28 offset:33024\nbuffer_load_dwordx4 v9, s[8:11], s53 offen offset:1024 lds\ns_waitcnt lgkmcnt(6)\nv_mfma_f32_16x16x32_bf16 v[64:67], v[104:107], a[132:135], v[64:67]\n"
"v_cvt_pk_bf16_f32 v136, v80, v81\nv_cvt_pk_bf16_f32 v137, v82, v83\nv_mfma_f32_16x16x32_bf16 v[68:71], v[104:107], a[164:167], v[68:71]\nds_read_b128 v[104:107], v25 offset:33024\n"
"v_cvt_pk_bf16_f32 v138, v88, v89\nv_cvt_pk_bf16_f32 v139, v90, v91\nv_mfma_f32_16x16x32_bf16 v[72:75], v[108:111], a[132:135], v[72:75]\nv_cvt_pk_bf16_f32 v140, v84, v85\n"
"v_cvt_pk_bf16_f32 v141, v86, v87\nv_mfma_f32_16x16x32_bf16 v[76:79], v[108:111], a[164:167], v[76:79]\nds_read_b128 v[108:111], v29 offset:33024\nv_cvt_pk_bf16_f32 v142, v92, v93\n"
"v_cvt_pk_bf16_f32 v143, v94, v95\ns_waitcnt lgkmcnt(6)\nv_mfma_f32_16x16x32_bf16 v[64:67], v[112:115], a[136:139], v[64:67]\ns_lshl_b32 s54, s40, 11\n"
"s_add_u32 s54, s54, 1024\nv_mfma_f32_16x16x32_bf16 v[68:71], v[112:115], a[168:171], v[68:71]\nds_read_b128 v[112:115], v26 offset:33024\nbuffer_store_dwordx4 v[136:139], v13, s[16:19], s54 offen sc1\n"
"v_mfma_f32_16x16x32_bf16 v[72:75], v[116:119], a[136:139], v[72:75]\ns_add_u32 s54, s54, 262144\nv_mfma_f32_16x16x32_bf16 v[76:79], v[116:119], a[168:171], v[76:79]\nds_read_b128 v[116:119], v30 offset:33024\n"
"buffer_store_dwordx4 v[140:143], v13, s[16:19], s54 offen sc1\ns_waitcnt lgkmcnt(6)\nv_mfma_f32_16x16x32_bf16 v[64:67], v[120:123], a[140:143], v[64:67]\ns_lshl_b32 s53, s43, 19\n"
"s_add_u32 s53, s53, s30\nv_mfma_f32_16x16x32_bf16 v[68:71], v[120:123], a[172:175], v[68:71]\nds_read_b128 v[120:123], v27 offset:33024\ns_add_u32 m0, s31, 139264\n"
"s_add_u32 s53, s53, 131072\nv_mfma_f32_16x16x32_bf16 v[72:75], v[124:127], a[140:143], v[72:75]\nbuffer_load_dwordx4 v8, s[8:11], s53 offen lds\nv_mfma_f32_16x16x32_bf16 v[76:79], v[124:127], a[172:175], v[76:79]\n"
"ds_read_b128 v[124:127], v31 offset:33024\nbuffer_load_dwordx4 v9, s[8:11], s53 offen offset:1024 lds\ns_waitcnt lgkmcnt(6)\nv_mfma_f32_16x16x32_bf16 v[64:67], v[96:99], a[144:147], v[64:67]\n"
"v_mfma_f32_16x16x32_bf16 v[68:71], v[96:99], a[176:179], v[68:71]\nds_read_b128 v[96:99], v24 offset:49152\nv_mfma_f32_16x16x32_bf16 v[72:75], v[100:103], a[144:147], v[72:75]\nv_mfma_f32_16x16x32_bf16 v[76:79], v[100:103], a[176:179], v[76:79]\n"
"ds_read_b128 v[100:103], v28 offset:49152\ns_waitcnt lgkmcnt(6)\nv_mfma_f32_16x16x32_bf16 v[64:67], v[104:107], a[148:151], v[64:67]\nv_mfma_f32_16x16x32_bf16 v[68:71], v[104:107], a[180:183], v[68:71]\n"
"ds_read_b128 v[104:107], v25 offset:49152\nv_mfma_f32_16x16x32_bf16 v[72:75], v[108:111], a[148:151], v[72:75]\nv_mfma_f32_16x16x32_bf16 v[76:79], v[108:111], a[180:183], v[76:79]\nds_read_b128 v[108:111], v29 offset:49152\n"
"s_waitcnt lgkmcnt(6)\nv_mfma_f32_16x16x32_bf16 v[64:67], v[112:115], a[152:155], v[64:67]\nv_mfma_f32_16x16x32_bf16 v[68:71], v[112:115], a[184:187], v[68:71]\nds_read_b128 v[112:115], v26 offset:49152\n"
"v_mfma_f32_16x16x32_bf16 v[72:75], v[116:119], a[152:155], v[72:75]\nv_mfma_f32_16x16x32_bf16 v[76:79], v[116:119], a[184:187], v[76:79]\nds_read_b128 v[116:119], v30 offset:49152\ns_waitcnt lgkmcnt(6)\n"
"v_mfma_f32_16x16x32_bf16 v[64:67], v[120:123], a[156:159], v[64:67]\nv_mfma_f32_16x16x32_bf16 v[68:71], v[120:123], a[188:191], v[68:71]\nds_read_b128 v[120:123], v27 offset:49152\nv_mfma_f32_16x16x32_bf16 v[72:75], v[124:127], a[156:159], v[72:75]\n"
"v_mfma_f32_16x16x32_bf16 v[76:79], v[124:127], a[188:191], v[76:79]\nds_read_b128 v[124:127], v31 offset:49152\ns_waitcnt lgkmcnt(6)\nv_mfma_f32_16x16x32_bf16 v[80:83], v[96:99], a[128:131], 0\n"
"s_lshl_b32 s53, s43, 19\ns_add_u32 s53, s53, s30\nv_mfma_f32_16x16x32_bf16 v[84:87], v[96:99], a[160:163], 0\nds_read_b128 v[96:99], v24 offset:49408\n"
"s_add_u32 m0, s31, 147456\ns_add_u32 s53, s53, 262144\nv_mfma_f32_16x16x32_bf16 v[88:91], v[100:103], a[128:131], 0\nbuffer_load_dwordx4 v8, s[8:11], s53 offen lds\n"
"v_mfma_f32_16x16x32_bf16 v[92:95], v[100:103], a[160:163], 0\nds_read_b128 v[100:103], v28 offset:49408\nbuffer_load_dwordx4 v9, s[8:11], s53 offen offset:1024 lds\ns_waitcnt lgkmcnt(6)\n"
"v_mfma_f32_16x16x32_bf16 v[80:83], v[104:107], a[132:135], v[80:83]\nv_cvt_pk_bf16_f32 v144, v64, v65\nv_cvt_pk_bf16_f32 v145, v66, v67\nv_mfma_f32_16x16x32_bf16 v[84:87], v[104:107], a[164:167], v[84:87]\n"
"ds_read_b128 v[104:107], v25 offset:49408\nv_cvt_pk_bf16_f32 v146, v72, v73\nv_cvt_pk_bf16_f32 v147, v74, v75\nv_mfma_f32_16x16x32_bf16 v[88:91], v[108:111], a[132:135], v[88:91]\n"
"v_cvt_pk_bf16_f32 v148, v68, v69\nv_cvt_pk_bf16_f32 v149, v70, v71\nv_mfma_f32_16x16x32_bf16 v[92:95], v[108:111], a[164:167], v[92:95]\nds_read_b128 v[108:111], v29 offset:49408\n"
"v_cvt_pk_bf16_f32 v150, v76, v77\nv_cvt_pk_bf16_f32 v151, v78, v79\ns_waitcnt lgkmcnt(6)\nv_mfma_f32_16x16x32_bf16 v[80:83], v[112:115], a[136:139], v[80:83]\n"
"s_lshl_b32 s54, s41, 11\ns_add_u32 s54, s54, 0\nv_mfma_f32_16x16x32_bf16 v[84:87], v[112:115], a[168:171], v[84:87]\nds_read_b128 v[112:115], v26 offset:49408\n"
"buffer_store_dwordx4 v[144:147], v13, s[16:19], s54 offen sc1\nv_mfma_f32_16x16x32_bf16 v[88:91], v[116:119], a[136:139], v[88:91]\ns_add_u32 s54, s54, 262144\nv_mfma_f32_16x16x32_bf16 v[92:95], v[116:119], a[168:171], v[92:95]\n"
"ds_read_b128 v[116:119], v30 offset:49408\nbuffer_store_dwordx4 v[148:151], v13, s[16:19], s54 offen sc1\ns_waitcnt lgkmcnt(6)\nv_mfma_f32_16x16x32_bf16 v[80:83], v[120:123], a[140:143], v[80:83]\n"
"s_lshl_b32 s53, s43, 19\ns_add_u32 s53, s53, s30\nv_mfma_f32_16x16x32_bf16 v[84:87], v[120:123], a[172:175], v[84:87]\nds_read_b128 v[120:123], v27 offset:49408\n"
"s_add_u32 m0, s31, 155648\ns_add_u32 s53, s53, 393216\nv_mfma_f32_16x16x32_bf16 v[88:91], v[124:127], a[140:143], v[88:91]\nbuffer_load_dwordx4 v8, s[8:11], s53 offen lds\n"
"v_mfma_f32_16x16x32_bf16 v[92:95], v[124:127], a[172:175], v[92:95]\nds_read_b128 v[124:127], v31 offset:49408\nbuffer_load_dwordx4 v9, s[8:11], s53 offen offset:1024 lds\ns_waitcnt vmcnt(12)\n"
"s_barrier\ns_waitcnt lgkmcnt(6)\nv_mfma_f32_16x16x32_bf16 v[80:83], v[96:99], a[144:147], v[80:83]\nv_mfma_f32_16x16x32_bf16 v[84:87], v[96:99], a[176:179], v[84:87]\n"
"ds_read_b128 v[96:99], v40 offset:0\nv_mfma_f32_16x16x32_bf16 v[88:91], v[100:103], a[144:147], v[88:91]\nv_mfma_f32_16x16x32_bf16 v[92:95], v[100:103], a[176:179], v[92:95]\nds_read_b128 v[100:103], v44 offset:0\n"
"s_waitcnt lgkmcnt(6)\nv_mfma_f32_16x16x32_bf16 v[80:83], v[104:107], a[148:151], v[80:83]\nv_mfma_f32_16x16x32_bf16 v[84:87], v[104:107], a[180:183], v[84:87]\nds_read_b128 v[104:107], v41 offset:0\n"
"v_mfma_f32_16x16x32_bf16 v[88:91], v[108:111], a[148:151], v[88:91]\nv_mfma_f32_16x16x32_bf16 v[92:95], v[108:111], a[180:183], v[92:95]\nds_read_b128 v[108:111], v45 offset:0\ns_waitcnt lgkmcnt(6)\n"
"v_mfma_f32_16x16x32_bf16 v[80:83], v[112:115], a[152:155], v[80:83]\nv_mfma_f32_16x16x32_bf16 v[84:87], v[112:115], a[184:187], v[84:87]\nds_read_b128 v[112:115], v42 offset:0\nv_mfma_f32_16x16x32_bf16 v[88:91], v[116:119], a[152:155], v[88:91]\n"
"v_mfma_f32_16x16x32_bf16 v[92:95], v[116:119], a[184:187], v[92:95]\nds_read_b128 v[116:119], v46 offset:0\ns_waitcnt lgkmcnt(6)\nv_mfma_f32_16x16x32_bf16 v[80:83], v[120:123], a[156:159], v[80:83]\n"
"v_mfma_f32_16x16x32_bf16 v[84:87], v[120:123], a[188:191], v[84:87]\nds_read_b128 v[120:123], v43 offset:0\nv_mfma_f32_16x16x32_bf16 v[88:91], v[124:127], a[156:159], v[88:91]\nv_mfma_f32_16x16x32_bf16 v[92:95], v[124:127], a[188:191], v[92:95]\n"
"ds_read_b128 v[124:127], v47 offset:0\ns_waitcnt lgkmcnt(6)\nv_mfma_f32_16x16x32_bf16 v[64:67], v[96:99], a[128:131], 0\ns_add_u32 m0, s32, 0\n"
"v_mfma_f32_16x16x32_bf16 v[68:71], v[96:99], a[160:163], 0\nds_read_b128 v[96:99], v40 offset:256\ns_mov_b32 s53, 0\nv_mfma_f32_16x16x32_bf16 v[72:75], v[100:103], a[128:131], 0\n"
"buffer_load_dwordx4 v10, s[12:15], s53 offen lds\nv_mfma_f32_16x16x32_bf16 v[76:79], v[100:103], a[160:163], 0\nds_read_b128 v[100:103], v44 offset:256\nv_cvt_pk_bf16_f32 v152, v80, v81\n"
"v_cvt_pk_bf16_f32 v153, v82, v83\ns_waitcnt lgkmcnt(6)\nv_mfma_f32_16x16x32_bf16 v[64:67], v[104:107], a[132:135], v[64:67]\nv_cvt_pk_bf16_f32 v154, v88, v89\n"
"v_cvt_pk_bf16_f32 v155, v90, v91\nv_mfma_f32_16x16x32_bf16 v[68:71], v[104:107], a[164:167], v[68:71]\nds_read_b128 v[104:107], v41 offset:256\nv_cvt_pk_bf16_f32 v156, v84, v85\n"
"v_cvt_pk_bf16_f32 v157, v86, v87\nv_mfma_f32_16x16x32_bf16 v[72:75], v[108:111], a[132:135], v[72:75]\nv_cvt_pk_bf16_f32 v158, v92, v93\nv_cvt_pk_bf16_f32 v159, v94, v95\n"
"v_mfma_f32_16x16x32_bf16 v[76:79], v[108:111], a[164:167], v[76:79]\nds_read_b128 v[108:111], v45 offset:256\ns_lshl_b32 s54, s41, 11\ns_add_u32 s54, s54, 1024\n"
"s_waitcnt lgkmcnt(6)\nv_mfma_f32_16x16x32_bf16 v[64:67], v[112:115], a[136:139], v[64:67]\nbuffer_store_dwordx4 v[152:155], v13, s[16:19], s54 offen sc1\nv_mfma_f32_16x16x32_bf16 v[68:71], v[112:115], a[168:171], v[68:71]\n"
"ds_read_b128 v[112:115], v42 offset:256\ns_add_u32 s54, s54, 262144\nv_mfma_f32_16x16x32_bf16 v[72:75], v[116:119], a[136:139], v[72:75]\nbuffer_store_dwordx4 v[156:159], v13, s[16:19], s54 offen sc1\n"
"v_mfma_f32_16x16x32_bf16 v[76:79], v[116:119], a[168:171], v[76:79]\nds_read_b128 v[116:119], v46 offset:256\ns_add_u32 m0, s32, 4096\ns_waitcnt lgkmcnt(6)\n"
"v_mfma_f32_16x16x32_bf16 v[64:67], v[120:123], a[140:143], v[64:67]\ns_mov_b32 s53, 128\nv_mfma_f32_16x16x32_bf16 v[68:71], v[120:123], a[172:175], v[68:71]\nds_read_b128 v[120:123], v43 offset:256\n"
"buffer_load_dwordx4 v10, s[12:15], s53 offen lds\nv_mfma_f32_16x16x32_bf16 v[72:75], v[124:127], a[140:143], v[72:75]\ns_add_u32 m0, s32, 8192\nv_mfma_f32_16x16x32_bf16 v[76:79], v[124:127], a[172:175], v[76:79]\n"
"ds_read_b128 v[124:127], v47 offset:256\ns_mov_b32 s53, 256\ns_waitcnt lgkmcnt(6)\nv_mfma_f32_16x16x32_bf16 v[64:67], v[96:99], a[144:147], v[64:67]\n"
"buffer_load_dwordx4 v10, s[12:15], s53 offen lds\nv_mfma_f32_16x16x32_bf16 v[68:71], v[96:99], a[176:179], v[68:71]\nds_read_b128 v[96:99], v40 offset:16384\ns_add_u32 m0, s32, 12288\n"
"v_mfma_f32_16x16x32_bf16 v[72:75], v[100:103], a[144:147], v[72:75]\ns_mov_b32 s53, 384\nv_mfma_f32_16x16x32_bf16 v[76:79], v[100:103], a[176:179], v[76:79]\nds_read_b128 v[100:103], v44 offset:16384\n"
"buffer_load_dwordx4 v10, s[12:15], s53 offen lds\ns_waitcnt lgkmcnt(6)\nv_mfma_f32_16x16x32_bf16 v[64:67], v[104:107], a[148:151], v[64:67]\nv_mfma_f32_16x16x32_bf16 v[68:71], v[104:107], a[180:183], v[68:71]\n"
"ds_read_b128 v[104:107], v41 offset:16384\nv_mfma_f32_16x16x32_bf16 v[72:75], v[108:111], a[148:151], v[72:75]\nv_mfma_f32_16x16x32_bf16 v[76:79], v[108:111], a[180:183], v[76:79]\nds_read_b128 v[108:111], v45 offset:16384\n"
"s_waitcnt lgkmcnt(6)\nv_mfma_f32_16x16x32_bf16 v[64:67], v[112:115], a[152:155], v[64:67]\nv_mfma_f32_16x16x32_bf16 v[68:71], v[112:115], a[184:187], v[68:71]\nds_read_b128 v[112:115], v42 offset:16384\n"
"v_mfma_f32_16x16x32_bf16 v[72:75], v[116:119], a[152:155], v[72:75]\nv_mfma_f32_16x16x32_bf16 v[76:79], v[116:119], a[184:187], v[76:79]\nds_read_b128 v[116:119], v46 offset:16384\ns_waitcnt lgkmcnt(6)\n"
"v_mfma_f32_16x16x32_bf16 v[64:67], v[120:123], a[156:159], v[64:67]\nv_mfma_f32_16x16x32_bf16 v[68:71], v[120:123], a[188:191], v[68:71]\nds_read_b128 v[120:123], v43 offset:16384\nv_mfma_f32_16x16x32_bf16 v[72:75], v[124:127], a[156:159], v[72:75]\n"
"v_mfma_f32_16x16x32_bf16 v[76:79], v[124:127], a[188:191], v[76:79]\nds_read_b128 v[124:127], v47 offset:16384\ns_waitcnt lgkmcnt(6)\nv_mfma_f32_16x16x32_bf16 v[80:83], v[96:99], a[128:131], 0\n"
"s_add_u32 m0, s32, 16384\nv_mfma_f32_16x16x32_bf16 v[84:87], v[96:99], a[160:163], 0\nds_read_b128 v[96:99], v40 offset:16640\ns_mov_b32 s53, 512\n"
"v_mfma_f32_16x16x32_bf16 v[88:91], v[100:103], a[128:131], 0\nbuffer_load_dwordx4 v10, s[12:15], s53 offen lds\nv_mfma_f32_16x16x32_bf16 v[92:95], v[100:103], a[160:163], 0\nds_read_b128 v[100:103], v44 offset:16640\n"
"v_cvt_pk_bf16_f32 v160, v64, v65\nv_cvt_pk_bf16_f32 v161, v66, v67\ns_waitcnt lgkmcnt(6)\nv_mfma_f32_16x16x32_bf16 v[80:83], v[104:107], a[132:135], v[80:83]\n"
"v_cvt_pk_bf16_f32 v162, v72, v73\nv_cvt_pk_bf16_f32 v163, v74, v75\nv_mfma_f32_16x16x32_bf16 v[84:87], v[104:107], a[164:167], v[84:87]\nds_read_b128 v[104:107], v41 offset:16640\n"
"v_cvt_pk_bf16_f32 v164, v68, v69\nv_cvt_pk_bf16_f32 v165, v70, v71\nv_mfma_f32_16x16x32_bf16 v[88:91], v[108:111], a[132:135], v[88:91]\nv_cvt_pk_bf16_f32 v166, v76, v77\n"
"v_cvt_pk_bf16_f32 v167, v78, v79\nv_mfma_f32_16x16x32_bf16 v[92:95], v[108:111], a[164:167], v[92:95]\nds_read_b128 v[108:111], v45 offset:16640\ns_lshl_b32 s54, s42, 11\n"
"s_add_u32 s54, s54, 0\ns_waitcnt lgkmcnt(6)\nv_mfma_f32_16x16x32_bf16 v[80:83], v[112:115], a[136:139], v[80:83]\nbuffer_store_dwordx4 v[160:163], v13, s[16:19], s54 offen sc1\n"
"v_mfma_f32_16x16x32_bf16 v[84:87], v[112:115], a[168:171], v[84:87]\nds_read_b128 v[112:115], v42 offset:16640\ns_add_u32 s54, s54, 262144\nv_mfma_f32_16x16x32_bf16 v[88:91], v[116:119], a[136:139], v[88:91]\n"
"buffer_store_dwordx4 v[164:167], v13, s[16:19], s54 offen sc1\nv_mfma_f32_16x16x32_bf16 v[92:95], v[116:119], a[168:171], v[92:95]\nds_read_b128 v[116:119], v46 offset:16640\ns_add_u32 m0, s32, 20480\n"
"s_waitcnt lgkmcnt(6)\nv_mfma_f32_16x16x32_bf16 v[80:83], v[120:123], a[140:143], v[80:83]\ns_mov_b32 s53, 640\nv_mfma_f32_16x16x32_bf16 v[84:87], v[120:123], a[172:175], v[84:87]\n"
"ds_read_b128 v[120:123], v43 offset:16640\nbuffer_load_dwordx4 v10, s[12:15], s53 offen lds\nv_mfma_f32_16x16x32_bf16 v[88:91], v[124:127], a[140:143], v[88:91]\ns_add_u32 m0, s32, 24576\n"
"v_mfma_f32_16x16x32_bf16 v[92:95], v[124:127], a[172:175], v[92:95]\nds_read_b128 v[124:127], v47 offset:16640\ns_mov_b32 s53, 768\ns_waitcnt vmcnt(10)\n"
"s_barrier\ns_waitcnt lgkmcnt(6)\nv_mfma_f32_16x16x32_bf16 v[80:83], v[96:99], a[144:147], v[80:83]\nbuffer_load_dwordx4 v10, s[12:15], s53 offen lds\n"
"v_mfma_f32_16x16x32_bf16 v[84:87], v[96:99], a[176:179], v[84:87]\nds_read_b128 v[96:99], v40 offset:32768\nv_mfma_f32_16x16x32_bf16 v[88:91], v[100:103], a[144:147], v[88:91]\nv_mfma_f32_16x16x32_bf16 v[92:95], v[100:103], a[176:179], v[92:95]\n"
"ds_read_b128 v[100:103], v44 offset:32768\ns_waitcnt lgkmcnt(6)\nv_mfma_f32_16x16x32_bf16 v[80:83], v[104:107], a[148:151], v[80:83]\nv_mfma_f32_16x16x32_bf16 v[84:87], v[104:107], a[180:183], v[84:87]\n"
"ds_read_b128 v[104:107], v41 offset:32768\nv_mfma_f32_16x16x32_bf16 v[88:91], v[108:111], a[148:151], v[88:91]\nv_mfma_f32_16x16x32_bf16 v[92:95], v[108:111], a[180:183], v[92:95]\nds_read_b128 v[108:111], v45 offset:32768\n"
"s_waitcnt lgkmcnt(6)\nv_mfma_f32_16x16x32_bf16 v[80:83], v[112:115], a[152:155], v[80:83]\nv_mfma_f32_16x16x32_bf16 v[84:87], v[112:115], a[184:187], v[84:87]\nds_read_b128 v[112:115], v42 offset:32768\n"
"v_mfma_f32_16x16x32_bf16 v[88:91], v[116:119], a[152:155], v[88:91]\nv_mfma_f32_16x16x32_bf16 v[92:95], v[116:119], a[184:187], v[92:95]\nds_read_b128 v[116:119], v46 offset:32768\ns_waitcnt lgkmcnt(6)\n"
"v_mfma_f32_16x16x32_bf16 v[80:83], v[120:123], a[156:159], v[80:83]\nv_mfma_f32_16x16x32_bf16 v[84:87], v[120:123], a[188:191], v[84:87]\nds_read_b128 v[120:123], v43 offset:32768\nv_mfma_f32_16x16x32_bf16 v[88:91], v[124:127], a[156:159], v[88:91]\n"
"v_mfma_f32_16x16x32_bf16 v[92:95], v[124:127], a[188:191], v[92:95]\nds_read_b128 v[124:127], v47 offset:32768\ns_cmp_lt_u32 s34, 2\ns_cbranch_scc1 fg1_main\n"
"s_nop 7\nv_cvt_pk_bf16_f32 v168, v80, v81\nv_cvt_pk_bf16_f32 v169, v82, v83\nv_cvt_pk_bf16_f32 v170, v88, v89\n"
"v_cvt_pk_bf16_f32 v171, v90, v91\nv_cvt_pk_bf16_f32 v172, v84, v85\nv_cvt_pk_bf16_f32 v173, v86, v87\nv_cvt_pk_bf16_f32 v174, v92, v93\n"
"v_cvt_pk_bf16_f32 v175, v94, v95\ns_lshl_b32 s54, s42, 11\ns_add_u32 s54, s54, 1024\nbuffer_store_dwordx4 v[168:171], v13, s[16:19], s54 offen sc1\n"
"s_add_u32 s54, s54, 262144\nbuffer_store_dwordx4 v[172:175], v13, s[16:19], s54 offen sc1\nbuffer_load_dword v246, v21, s[24:27], 0 offen sc1\nbuffer_load_dwordx2 v[244:245], v20, s[24:27], 0 offen sc1\n"
"buffer_load_dwordx2 v[248:249], v15, s[24:27], 0 offen sc1\nv_mov_b32 v36, s28\ns_waitcnt vmcnt(3) lgkmcnt(0)\ns_barrier\n"
"buffer_store_dword v36, v19, s[24:27], 0 offen sc1\ns_waitcnt vmcnt(1)\ns_cmp_eq_u32 s34, 2\ns_cbranch_scc1 fg1_end\n"
"fg1_pp:\nv_cmp_ne_u32 vcc, s28, v246\ns_cbranch_vccz fg1_ppok\ns_add_u32 s55, s55, 1\n"
"s_cmp_gt_u32 s55, 262144\ns_cbranch_scc0 fg1_pp2\ns_endpgm\nfg1_pp2:\n"
"s_sleep 1\nbuffer_load_dword v246, v21, s[24:27], 0 offen sc1\ns_waitcnt vmcnt(0)\ns_branch fg1_pp\n"
"fg1_ppok:\ns_and_b32 s54, s31, 0x1800\ns_lshl_b32 s54, s54, 1\ns_cmp_ge_u32 s54, 8192\n"
"s_cbranch_scc1 fg1_k0\ns_add_u32 s53, s54, s36\nbuffer_load_dwordx4 a[0:3], v14, s[16:19], s53 offen sc1\nbuffer_load_dwordx4 a[4:7], v14, s[16:19], s53 offen offset:1024 sc1\n"
"s_add_u32 s53, s53, 14336\nbuffer_load_dwordx4 a[56:59], v14, s[16:19], s53 offen sc1\nbuffer_load_dwordx4 a[60:63], v14, s[16:19], s53 offen offset:1024 sc1\ns_add_u32 s53, s53, 14336\n"
"fg1_k0:\ns_add_u32 s54, s54, 2048\ns_cmp_eq_u32 s54, 14336\ns_cselect_b32 s54, 0, s54\n"
"s_cmp_ge_u32 s54, 8192\ns_cbranch_scc1 fg1_k1\ns_add_u32 s53, s54, s36\nbuffer_load_dwordx4 a[8:11], v14, s[16:19], s53 offen sc1\n"
"buffer_load_dwordx4 a[12:15], v14, s[16:19], s53 offen offset:1024 sc1\ns_add_u32 s53, s53, 14336\nbuffer_load_dwordx4 a[64:67], v14, s[16:19], s53 offen sc1\nbuffer_load_dwordx4 a[68:71], v14, s[16:19], s53 offen offset:1024 sc1\n"
"s_add_u32 s53, s53, 14336\nfg1_k1:\ns_add_u32 s54, s54, 2048\ns_cmp_eq_u32 s54, 14336\n"
"s_cselect_b32 s54, 0, s54\ns_cmp_ge_u32 s54, 8192\ns_cbranch_scc1 fg1_k2\ns_add_u32 s53, s54, s36\n"
"buffer_load_dwordx4 a[16:19], v14, s[16:19], s53 offen sc1\nbuffer_load_dwordx4 a[20:23], v14, s[16:19], s53 offen offset:1024 sc1\ns_add_u32 s53, s53, 14336\nbuffer_load_dwordx4 a[72:75], v14, s[16:19], s53 offen sc1\n"
"buffer_load_dwordx4 a[76:79], v14, s[16:19], s53 offen offset:1024 sc1\ns_add_u32 s53, s53, 14336\nfg1_k2:\ns_add_u32 s54, s54, 2048\n"
"s_cmp_eq_u32 s54, 14336\ns_cselect_b32 s54, 0, s54\ns_cmp_ge_u32 s54, 8192\ns_cbranch_scc1 fg1_k3\n"
"s_add_u32 s53, s54, s36\nbuffer_load_dwordx4 a[24:27], v14, s[16:19], s53 offen sc1\nbuffer_load_dwordx4 a[28:31], v14, s[16:19], s53 offen offset:1024 sc1\ns_add_u32 s53, s53, 14336\n"
"buffer_load_dwordx4 a[80:83], v14, s[16:19], s53 offen sc1\nbuffer_load_dwordx4 a[84:87], v14, s[16:19], s53 offen offset:1024 sc1\ns_add_u32 s53, s53, 14336\nfg1_k3:\n"
"s_add_u32 s54, s54, 2048\ns_cmp_eq_u32 s54, 14336\ns_cselect_b32 s54, 0, s54\ns_cmp_ge_u32 s54, 8192\n"
"s_cbranch_scc1 fg1_k4\ns_add_u32 s53, s54, s36\nbuffer_load_dwordx4 a[32:35], v14, s[16:19], s53 offen sc1\nbuffer_load_dwordx4 a[36:39], v14, s[16:19], s53 offen offset:1024 sc1\n"
"s_add_u32 s53, s53, 14336\nbuffer_load_dwordx4 a[88:91], v14, s[16:19], s53 offen sc1\nbuffer_load_dwordx4 a[92:95], v14, s[16:19], s53 offen offset:1024 sc1\ns_add_u32 s53, s53, 14336\n"
"fg1_k4:\ns_add_u32 s54, s54, 2048\ns_cmp_eq_u32 s54, 14336\ns_cselect_b32 s54, 0, s54\n"
"s_cmp_ge_u32 s54, 8192\ns_cbranch_scc1 fg1_k5\ns_add_u32 s53, s54, s36\nbuffer_load_dwordx4 a[40:43], v14, s[16:19], s53 offen sc1\n"
"buffer_load_dwordx4 a[44:47], v14, s[16:19], s53 offen offset:1024 sc1\ns_add_u32 s53, s53, 14336\nbuffer_load_dwordx4 a[96:99], v14, s[16:19], s53 offen sc1\nbuffer_load_dwordx4 a[100:103], v14, s[16:19], s53 offen offset:1024 sc1\n"
"s_add_u32 s53, s53, 14336\nfg1_k5:\ns_add_u32 s54, s54, 2048\ns_cmp_eq_u32 s54, 14336\n"
"s_cselect_b32 s54, 0, s54\ns_cmp_ge_u32 s54, 8192\ns_cbranch_scc1 fg1_k6\ns_add_u32 s53, s54, s36\n"
"buffer_load_dwordx4 a[48:51], v14, s[16:19], s53 offen sc1\nbuffer_load_dwordx4 a[52:55], v14, s[16:19], s53 offen offset:1024 sc1\ns_add_u32 s53, s53, 14336\nbuffer_load_dwordx4 a[104:107], v14, s[16:19], s53 offen sc1\n"
"buffer_load_dwordx4 a[108:111], v14, s[16:19], s53 offen offset:1024 sc1\ns_add_u32 s53, s53, 14336\nfg1_k6:\ns_add_u32 s54, s54, 2048\n"
"s_cmp_eq_u32 s54, 14336\ns_cselect_b32 s54, 0, s54\ns_branch fg1_end\nfg1_main:\n"
"s_waitcnt lgkmcnt(6)\nv_mfma_f32_16x16x32_bf16 v[64:67], v[96:99], a[128:131], 0\ns_add_u32 m0, s32, 32768\nv_mfma_f32_16x16x32_bf16 v[68:71], v[96:99], a[160:163], 0\n"
"ds_read_b128 v[96:99], v40 offset:33024\ns_mov_b32 s53, 28672\nv_mfma_f32_16x16x32_bf16 v[72:75], v[100:103], a[128:131], 0\nbuffer_load_dwordx4 v10, s[12:15], s53 offen lds\n"
"v_mfma_f32_16x16x32_bf16 v[76:79], v[100:103], a[160:163], 0\nds_read_b128 v[100:103], v44 offset:33024\nv_cvt_pk_bf16_f32 v168, v80, v81\nv_cvt_pk_bf16_f32 v169, v82, v83\n"
"s_waitcnt lgkmcnt(6)\nv_mfma_f32_16x16x32_bf16 v[64:67], v[104:107], a[132:135], v[64:67]\nv_cvt_pk_bf16_f32 v170, v88, v89\nv_cvt_pk_bf16_f32 v171, v90, v91\n"
"v_mfma_f32_16x16x32_bf16 v[68:71], v[104:107], a[164:167], v[68:71]\nds_read_b128 v[104:107], v41 offset:33024\nv_cvt_pk_bf16_f32 v172, v84, v85\nv_cvt_pk_bf16_f32 v173, v86, v87\n"
"v_mfma_f32_16x16x32_bf16 v[72:75], v[108:111], a[132:135], v[72:75]\nv_cvt_pk_bf16_f32 v174, v92, v93\nv_cvt_pk_bf16_f32 v175, v94, v95\nv_mfma_f32_16x16x32_bf16 v[76:79], v[108:111], a[164:167], v[76:79]\n"
"ds_read_b128 v[108:111], v45 offset:33024\ns_lshl_b32 s54, s42, 11\ns_add_u32 s54, s54, 1024\ns_waitcnt lgkmcnt(6)\n"
"v_mfma_f32_16x16x32_bf16 v[64:67], v[112:115], a[136:139], v[64:67]\nbuffer_store_dwordx4 v[168:171], v13, s[16:19], s54 offen sc1\nv_mfma_f32_16x16x32_bf16 v[68:71], v[112:115], a[168:171], v[68:71]\nds_read_b128 v[112:115], v42 offset:33024\n"
"s_add_u32 s54, s54, 262144\nv_mfma_f32_16x16x32_bf16 v[72:75], v[116:119], a[136:139], v[72:75]\nbuffer_store_dwordx4 v[172:175], v13, s[16:19], s54 offen sc1\nv_mfma_f32_16x16x32_bf16 v[76:79], v[116:119], a[168:171], v[76:79]\n"
"ds_read_b128 v[116:119], v46 offset:33024\ns_add_u32 m0, s32, 36864\ns_waitcnt lgkmcnt(6)\nv_mfma_f32_16x16x32_bf16 v[64:67], v[120:123], a[140:143], v[64:67]\n"
"s_mov_b32 s53, 28800\nv_mfma_f32_16x16x32_bf16 v[68:71], v[120:123], a[172:175], v[68:71]\nds_read_b128 v[120:123], v43 offset:33024\nbuffer_load_dwordx4 v10, s[12:15], s53 offen lds\n"
"v_mfma_f32_16x16x32_bf16 v[72:75], v[124:127], a[140:143], v[72:75]\ns_add_u32 m0, s32, 40960\nv_mfma_f32_16x16x32_bf16 v[76:79], v[124:127], a[172:175], v[76:79]\nds_read_b128 v[124:127], v47 offset:33024\n"
"s_mov_b32 s53, 28928\ns_waitcnt lgkmcnt(6)\nv_mfma_f32_16x16x32_bf16 v[64:67], v[96:99], a[144:147], v[64:67]\nbuffer_load_dwordx4 v10, s[12:15], s53 offen lds\n"
"v_mfma_f32_16x16x32_bf16 v[68:71], v[96:99], a[176:179], v[68:71]\nds_read_b128 v[96:99], v40 offset:49152\ns_add_u32 m0, s32, 45056\nv_mfma_f32_16x16x32_bf16 v[72:75], v[100:103], a[144:147], v[72:75]\n"
"s_mov_b32 s53, 29056\nv_mfma_f32_16x16x32_bf16 v[76:79], v[100:103], a[176:179], v[76:79]\nds_read_b128 v[100:103], v44 offset:49152\nbuffer_load_dwordx4 v10, s[12:15], s53 offen lds\n"
"s_waitcnt lgkmcnt(6)\nv_mfma_f32_16x16x32_bf16 v[64:67], v[104:107], a[148:151], v[64:67]\nv_mfma_f32_16x16x32_bf16 v[68:71], v[104:107], a[180:183], v[68:71]\nds_read_b128 v[104:107], v41 offset:49152\n"
"v_mfma_f32_16x16x32_bf16 v[72:75], v[108:111], a[148:151], v[72:75]\nv_mfma_f32_16x16x32_bf16 v[76:79], v[108:111], a[180:183], v[76:79]\nds_read_b128 v[108:111], v45 offset:49152\ns_waitcnt lgkmcnt(6)\n"
"v_mfma_f32_16x16x32_bf16 v[64:67], v[112:115], a[152:155], v[64:67]\nv_mfma_f32_16x16x32_bf16 v[68:71], v[112:115], a[184:187], v[68:71]\nds_read_b128 v[112:115], v42 offset:49152\nv_mfma_f32_16x16x32_bf16 v[72:75], v[116:119], a[152:155], v[72:75]\n"
"v_mfma_f32_16x16x32_bf16 v[76:79], v[116:119], a[184:187], v[76:79]\nds_read_b128 v[116:119], v46 offset:49152\ns_waitcnt lgkmcnt(6)\nv_mfma_f32_16x16x32_bf16 v[64:67], v[120:123], a[156:159], v[64:67]\n"
"v_mfma_f32_16x16x32_bf16 v[68:71], v[120:123], a[188:191], v[68:71]\nds_read_b128 v[120:123], v43 offset:49152\nv_mfma_f32_16x16x32_bf16 v[72:75], v[124:127], a[156:159], v[72:75]\nv_mfma_f32_16x16x32_bf16 v[76:79], v[124:127], a[188:191], v[76:79]\n"
"ds_read_b128 v[124:127], v47 offset:49152\ns_waitcnt lgkmcnt(6)\nv_mfma_f32_16x16x32_bf16 v[80:83], v[96:99], a[128:131], 0\ns_add_u32 m0, s32, 49152\n"
"v_mfma_f32_16x16x32_bf16 v[84:87], v[96:99], a[160:163], 0\nds_read_b128 v[96:99], v40 offset:49408\ns_mov_b32 s53, 29184\nv_mfma_f32_16x16x32_bf16 v[88:91], v[100:103], a[128:131], 0\n"
"buffer_load_dwordx4 v10, s[12:15], s53 offen lds\nv_mfma_f32_16x16x32_bf16 v[92:95], v[100:103], a[160:163], 0\nds_read_b128 v[100:103], v44 offset:49408\nv_cvt_pk_bf16_f32 v176, v64, v65\n"
"v_cvt_pk_bf16_f32 v177, v66, v67\ns_waitcnt lgkmcnt(6)\nv_mfma_f32_16x16x32_bf16 v[80:83], v[104:107], a[132:135], v[80:83]\nv_cvt_pk_bf16_f32 v178, v72, v73\n"
"v_cvt_pk_bf16_f32 v179, v74, v75\nv_mfma_f32_16x16x32_bf16 v[84:87], v[104:107], a[164:167], v[84:87]\nds_read_b128 v[104:107], v41 offset:49408\nv_cvt_pk_bf16_f32 v180, v68, v69\n"
"v_cvt_pk_bf16_f32 v181, v70, v71\nv_mfma_f32_16x16x32_bf16 v[88:91], v[108:111], a[132:135], v[88:91]\nv_cvt_pk_bf16_f32 v182, v76, v77\nv_cvt_pk_bf16_f32 v183, v78, v79\n"
"v_mfma_f32_16x16x32_bf16 v[92:95], v[108:111], a[164:167], v[92:95]\nds_read_b128 v[108:111], v45 offset:49408\ns_lshl_b32 s54, s43, 11\ns_add_u32 s54, s54, 0\n"
"s_waitcnt lgkmcnt(6)\nv_mfma_f32_16x16x32_bf16 v[80:83], v[112:115], a[136:139], v[80:83]\nbuffer_store_dwordx4 v[176:179], v13, s[16:19], s54 offen sc1\nv_mfma_f32_16x16x32_bf16 v[84:87], v[112:115], a[168:171], v[84:87]\n"
"ds_read_b128 v[112:115], v42 offset:49408\ns_add_u32 s54, s54, 262144\nv_mfma_f32_16x16x32_bf16 v[88:91], v[116:119], a[136:139], v[88:91]\nbuffer_store_dwordx4 v[180:183], v13, s[16:19], s54 offen sc1\n"
"v_mfma_f32_16x16x32_bf16 v[92:95], v[116:119], a[168:171], v[92:95]\nds_read_b128 v[116:119], v46 offset:49408\ns_add_u32 m0, s32, 53248\ns_waitcnt lgkmcnt(6)\n"
"v_mfma_f32_16x16x32_bf16 v[80:83], v[120:123], a[140:143], v[80:83]\ns_mov_b32 s53, 29312\nv_mfma_f32_16x16x32_bf16 v[84:87], v[120:123], a[172:175], v[84:87]\nds_read_b128 v[120:123], v43 offset:49408\n"
"buffer_load_dwordx4 v10, s[12:15], s53 offen lds\nv_mfma_f32_16x16x32_bf16 v[88:91], v[124:127], a[140:143], v[88:91]\ns_add_u32 m0, s32, 57344\nv_mfma_f32_16x16x32_bf16 v[92:95], v[124:127], a[172:175], v[92:95]\n"
"ds_read_b128 v[124:127], v47 offset:49408\ns_mov_b32 s53, 29440\ns_waitcnt lgkmcnt(6)\nv_mfma_f32_16x16x32_bf16 v[80:83], v[96:99], a[144:147], v[80:83]\n"
"buffer_load_dwordx4 v10, s[12:15], s53 offen lds\nv_mfma_f32_16x16x32_bf16 v[84:87], v[96:99], a[176:179], v[84:87]\nbuffer_load_dword v37, v21, s[24:27], 0 offen sc1\nv_mfma_f32_16x16x32_bf16 v[88:91], v[100:103], a[144:147], v[88:91]\n"
"v_mfma_f32_16x16x32_bf16 v[92:95], v[100:103], a[176:179], v[92:95]\ns_waitcnt lgkmcnt(4)\nv_mfma_f32_16x16x32_bf16 v[80:83], v[104:107], a[148:151], v[80:83]\nv_mfma_f32_16x16x32_bf16 v[84:87], v[104:107], a[180:183], v[84:87]\n"
"v_mfma_f32_16x16x32_bf16 v[88:91], v[108:111], a[148:151], v[88:91]\nv_mfma_f32_16x16x32_bf16 v[92:95], v[108:111], a[180:183], v[92:95]\ns_waitcnt lgkmcnt(2)\nv_mfma_f32_16x16x32_bf16 v[80:83], v[112:115], a[152:155], v[80:83]\n"
"v_mfma_f32_16x16x32_bf16 v[84:87], v[112:115], a[184:187], v[84:87]\nv_mfma_f32_16x16x32_bf16 v[88:91], v[116:119], a[152:155], v[88:91]\nv_mfma_f32_16x16x32_bf16 v[92:95], v[116:119], a[184:187], v[92:95]\ns_waitcnt lgkmcnt(0)\n"
"v_mfma_f32_16x16x32_bf16 v[80:83], v[120:123], a[156:159], v[80:83]\nv_mfma_f32_16x16x32_bf16 v[84:87], v[120:123], a[188:191], v[84:87]\nv_mfma_f32_16x16x32_bf16 v[88:91], v[124:127], a[156:159], v[88:91]\nv_mfma_f32_16x16x32_bf16 v[92:95], v[124:127], a[188:191], v[92:95]\n"
"s_nop 7\nv_cvt_pk_bf16_f32 v184, v80, v81\nv_cvt_pk_bf16_f32 v185, v82, v83\nv_cvt_pk_bf16_f32 v186, v88, v89\n"
"v_cvt_pk_bf16_f32 v187, v90, v91\nv_cvt_pk_bf16_f32 v188, v84, v85\nv_cvt_pk_bf16_f32 v189, v86, v87\nv_cvt_pk_bf16_f32 v190, v92, v93\n"
"v_cvt_pk_bf16_f32 v191, v94, v95\ns_lshl_b32 s54, s43, 11\ns_add_u32 s54, s54, 1024\nbuffer_store_dwordx4 v[184:187], v13, s[16:19], s54 offen sc1\n"
"s_add_u32 s54, s54, 262144\nbuffer_store_dwordx4 v[188:191], v13, s[16:19], s54 offen sc1\ns_waitcnt vmcnt(2)\nv_mov_b32 v36, s28\n"
"v_cmp_ne_u32 vcc, s28, v37\ns_cbranch_vccz fg1_e1\ns_waitcnt vmcnt(0)\ns_barrier\n"
"buffer_store_dword v36, v19, s[24:27], 0 offen sc1\nfg1_po:\nv_cmp_ne_u32 vcc, s28, v37\ns_cbranch_vccz fg1_pook\n"
"s_add_u32 s55, s55, 1\ns_cmp_gt_u32 s55, 262144\ns_cbranch_scc0 fg1_po2\ns_endpgm\n"
"fg1_po2:\ns_sleep 1\nbuffer_load_dword v37, v21, s[24:27], 0 offen sc1\ns_waitcnt vmcnt(0)\n"
"s_branch fg1_po\nfg1_pook:\ns_mov_b32 s53, 8192\nbuffer_load_dwordx4 v[192:195], v13, s[16:19], s53 offen sc1\n"
"s_mov_b32 s53, 270336\nbuffer_load_dwordx4 v[196:199], v13, s[16:19], s53 offen sc1\ns_mov_b32 s53, 9216\nbuffer_load_dwordx4 v[200:203], v13, s[16:19], s53 offen sc1\n"
"s_mov_b32 s53, 271360\nbuffer_load_dwordx4 v[204:207], v13, s[16:19], s53 offen sc1\ns_mov_b32 s53, 10240\nbuffer_load_dwordx4 v[208:211], v13, s[16:19], s53 offen sc1\n"
"s_mov_b32 s53, 272384\nbuffer_load_dwordx4 v[212:215], v13, s[16:19], s53 offen sc1\ns_mov_b32 s53, 11264\nbuffer_load_dwordx4 v[216:219], v13, s[16:19], s53 offen sc1\n"
"s_mov_b32 s53, 273408\nbuffer_load_dwordx4 v[220:223], v13, s[16:19], s53 offen sc1\ns_mov_b32 s53, 12288\nbuffer_load_dwordx4 v[224:227], v13, s[16:19], s53 offen sc1\n"
"s_mov_b32 s53, 274432\nbuffer_load_dwordx4 v[228:231], v13, s[16:19], s53 offen sc1\ns_mov_b32 s53, 13312\nbuffer_load_dwordx4 v[232:235], v13, s[16:19], s53 offen sc1\n"
"s_mov_b32 s53, 275456\nbuffer_load_dwordx4 v[236:239], v13, s[16:19], s53 offen sc1\nbuffer_store_dword v36, v19, s[24:27], 0 offen sc1\ns_branch fg1_e2\n"
"fg1_e1:\ns_mov_b32 s53, 8192\nbuffer_load_dwordx4 v[192:195], v13, s[16:19], s53 offen sc1\ns_mov_b32 s53, 270336\n"
"buffer_load_dwordx4 v[196:199], v13, s[16:19], s53 offen sc1\ns_mov_b32 s53, 9216\nbuffer_load_dwordx4 v[200:203], v13, s[16:19], s53 offen sc1\ns_mov_b32 s53, 271360\n"
"buffer_load_dwordx4 v[204:207], v13, s[16:19], s53 offen sc1\ns_mov_b32 s53, 10240\nbuffer_load_dwordx4 v[208:211], v13, s[16:19], s53 offen sc1\ns_mov_b32 s53, 272384\n"
"buffer_load_dwordx4 v[212:215], v13, s[16:19], s53 offen sc1\ns_mov_b32 s53, 11264\nbuffer_load_dwordx4 v[216:219], v13, s[16:19], s53 offen sc1\ns_mov_b32 s53, 273408\n"
"buffer_load_dwordx4 v[220:223], v13, s[16:19], s53 offen sc1\ns_mov_b32 s53, 12288\nbuffer_load_dwordx4 v[224:227], v13, s[16:19], s53 offen sc1\ns_mov_b32 s53, 274432\n"
"buffer_load_dwordx4 v[228:231], v13, s[16:19], s53 offen sc1\ns_mov_b32 s53, 13312\nbuffer_load_dwordx4 v[232:235], v13, s[16:19], s53 offen sc1\ns_mov_b32 s53, 275456\n"
"buffer_load_dwordx4 v[236:239], v13, s[16:19], s53 offen sc1\ns_waitcnt vmcnt(12)\ns_barrier\nbuffer_store_dword v36, v19, s[24:27], 0 offen sc1\n"
"fg1_e2:\ns_add_u32 m0, s32, 65536\ns_mov_b32 s53, 57344\nbuffer_load_dwordx4 v10, s[12:15], s53 offen lds\n"
"s_add_u32 m0, s32, 69632\ns_mov_b32 s53, 57472\nbuffer_load_dwordx4 v10, s[12:15], s53 offen lds\ns_add_u32 m0, s32, 73728\n"
"s_mov_b32 s53, 57600\nbuffer_load_dwordx4 v10, s[12:15], s53 offen lds\ns_add_u32 m0, s32, 77824\ns_mov_b32 s53, 57728\n"
"buffer_load_dwordx4 v10, s[12:15], s53 offen lds\ns_add_u32 m0, s32, 81920\ns_mov_b32 s53, 57856\nbuffer_load_dwordx4 v10, s[12:15], s53 offen lds\n"
"s_add_u32 m0, s32, 86016\ns_mov_b32 s53, 57984\nbuffer_load_dwordx4 v10, s[12:15], s53 offen lds\ns_add_u32 m0, s32, 90112\n"
"s_mov_b32 s53, 58112\nbuffer_load_dwordx4 v10, s[12:15], s53 offen lds\ns_sub_u32 s48, s28, 1\ns_mov_b32 s49, 86016\n"
"s_mov_b32 s50, 0\ns_mov_b32 s51, 0\ns_add_u32 s52, s32, 98304\ns_add_u32 s47, s32, 131072\n"
"ds_read_b128 v[40:43], v32 offset:0\nds_read_b128 v[44:47], v33 offset:0\nds_read_b128 v[48:51], v34 offset:0\nds_read_b128 v[52:55], v35 offset:0\n"
"s_barrier\ns_waitcnt lgkmcnt(3)\nv_mfma_f32_16x16x32_bf16 v[64:67], v[40:43], v[128:131], 0\nv_mov_b32 v36, s48\n"
"s_add_u32 s48, s48, 1\ns_nop 0\nbuffer_store_dword v36, v19, s[24:27], s33 offen sc1\nv_mfma_f32_16x16x32_bf16 v[68:71], v[40:43], v[132:135], 0\n"
"ds_read_b128 v[40:43], v32 offset:4096\ns_waitcnt lgkmcnt(3)\nv_mfma_f32_16x16x32_bf16 v[72:75], v[44:47], v[128:131], 0\nv_mfma_f32_16x16x32_bf16 v[76:79], v[44:47], v[132:135], 0\n"
"ds_read_b128 v[44:47], v33 offset:4096\ns_waitcnt lgkmcnt(3)\nv_mfma_f32_16x16x32_bf16 v[64:67], v[48:51], v[136:139], v[64:67]\ns_add_u32 m0, s52, 0\n"
"v_mfma_f32_16x16x32_bf16 v[68:71], v[48:51], v[140:143], v[68:71]\nds_read_b128 v[48:51], v34 offset:4096\ns_add_u32 s53, s49, 0\ns_waitcnt lgkmcnt(3)\n"
"v_mfma_f32_16x16x32_bf16 v[72:75], v[52:55], v[136:139], v[72:75]\nbuffer_load_dwordx4 v10, s[12:15], s53 offen lds\nv_mfma_f32_16x16x32_bf16 v[76:79], v[52:55], v[140:143], v[76:79]\nds_read_b128 v[52:55], v35 offset:4096\n"
"s_add_u32 m0, s52, 4096\ns_waitcnt lgkmcnt(3)\nv_mfma_f32_16x16x32_bf16 v[64:67], v[40:43], v[144:147], v[64:67]\ns_add_u32 s53, s49, 128\n"
"v_mfma_f32_16x16x32_bf16 v[68:71], v[40:43], v[148:151], v[68:71]\nds_read_b128 v[40:43], v32 offset:8192\nbuffer_load_dwordx4 v10, s[12:15], s53 offen lds\ns_waitcnt lgkmcnt(3)\n"
"v_mfma_f32_16x16x32_bf16 v[72:75], v[44:47], v[144:147], v[72:75]\ns_add_u32 m0, s52, 8192\nv_mfma_f32_16x16x32_bf16 v[76:79], v[44:47], v[148:151], v[76:79]\nds_read_b128 v[44:47], v33 offset:8192\n"
"s_add_u32 s53, s49, 256\ns_waitcnt lgkmcnt(3)\nv_mfma_f32_16x16x32_bf16 v[64:67], v[48:51], v[152:155], v[64:67]\nbuffer_load_dwordx4 v10, s[12:15], s53 offen lds\n"
"v_mfma_f32_16x16x32_bf16 v[68:71], v[48:51], v[156:159], v[68:71]\nds_read_b128 v[48:51], v34 offset:8192\ns_add_u32 m0, s52, 12288\ns_waitcnt lgkmcnt(3)\n"
"v_mfma_f32_16x16x32_bf16 v[72:75], v[52:55], v[152:155], v[72:75]\ns_add_u32 s53, s49, 384\nv_mfma_f32_16x16x32_bf16 v[76:79], v[52:55], v[156:159], v[76:79]\nds_read_b128 v[52:55], v35 offset:8192\n"
"buffer_load_dwordx4 v10, s[12:15], s53 offen lds\ns_waitcnt lgkmcnt(3)\nv_mfma_f32_16x16x32_bf16 v[64:67], v[40:43], v[160:163], v[64:67]\ns_add_u32 m0, s52, 16384\n"
"v_mfma_f32_16x16x32_bf16 v[68:71], v[40:43], v[164:167], v[68:71]\nds_read_b128 v[40:43], v32 offset:12288\ns_add_u32 s53, s49, 512\ns_waitcnt lgkmcnt(3)\n"
"v_mfma_f32_16x16x32_bf16 v[72:75], v[44:47], v[160:163], v[72:75]\nbuffer_load_dwordx4 v10, s[12:15], s53 offen lds\nv_mfma_f32_16x16x32_bf16 v[76:79], v[44:47], v[164:167], v[76:79]\nds_read_b128 v[44:47], v33 offset:12288\n"
"s_add_u32 m0, s52, 20480\ns_waitcnt lgkmcnt(3)\nv_mfma_f32_16x16x32_bf16 v[64:67], v[48:51], v[168:171], v[64:67]\ns_add_u32 s53, s49, 640\n"
"v_mfma_f32_16x16x32_bf16 v[68:71], v[48:51], v[172:175], v[68:71]\nds_read_b128 v[48:51], v34 offset:12288\nbuffer_load_dwordx4 v10, s[12:15], s53 offen lds\ns_waitcnt lgkmcnt(3)\n"
"v_mfma_f32_16x16x32_bf16 v[72:75], v[52:55], v[168:171], v[72:75]\ns_add_u32 m0, s52, 24576\nv_mfma_f32_16x16x32_bf16 v[76:79], v[52:55], v[172:175], v[76:79]\nds_read_b128 v[52:55], v35 offset:12288\n"
"s_add_u32 s53, s49, 768\ns_waitcnt lgkmcnt(3)\nv_mfma_f32_16x16x32_bf16 v[64:67], v[40:43], v[176:179], v[64:67]\nbuffer_load_dwordx4 v10, s[12:15], s53 offen lds\n"
"v_mfma_f32_16x16x32_bf16 v[68:71], v[40:43], v[180:183], v[68:71]\nds_read_b128 v[40:43], v32 offset:16384\ns_waitcnt lgkmcnt(3)\nv_mfma_f32_16x16x32_bf16 v[72:75], v[44:47], v[176:179], v[72:75]\n"
"v_mfma_f32_16x16x32_bf16 v[76:79], v[44:47], v[180:183], v[76:79]\nds_read_b128 v[44:47], v33 offset:16384\ns_waitcnt lgkmcnt(3)\nv_mfma_f32_16x16x32_bf16 v[64:67], v[48:51], v[184:187], v[64:67]\n"
"v_mfma_f32_16x16x32_bf16 v[68:71], v[48:51], v[188:191], v[68:71]\nds_read_b128 v[48:51], v34 offset:16384\ns_waitcnt lgkmcnt(3)\nv_mfma_f32_16x16x32_bf16 v[72:75], v[52:55], v[184:187], v[72:75]\n"
"v_mfma_f32_16x16x32_bf16 v[76:79], v[52:55], v[188:191], v[76:79]\nds_read_b128 v[52:55], v35 offset:16384\ns_waitcnt vmcnt(16)\ns_waitcnt lgkmcnt(3)\n"
"v_mfma_f32_16x16x32_bf16 v[64:67], v[40:43], v[192:195], v[64:67]\nv_mfma_f32_16x16x32_bf16 v[68:71], v[40:43], v[196:199], v[68:71]\nds_read_b128 v[40:43], v32 offset:20480\ns_waitcnt lgkmcnt(3)\n"
"v_mfma_f32_16x16x32_bf16 v[72:75], v[44:47], v[192:195], v[72:75]\nv_mfma_f32_16x16x32_bf16 v[76:79], v[44:47], v[196:199], v[76:79]\nds_read_b128 v[44:47], v33 offset:20480\ns_waitcnt lgkmcnt(3)\n"
"v_mfma_f32_16x16x32_bf16 v[64:67], v[48:51], v[200:203], v[64:67]\nv_mfma_f32_16x16x32_bf16 v[68:71], v[48:51], v[204:207], v[68:71]\nds_read_b128 v[48:51], v34 offset:20480\ns_waitcnt lgkmcnt(3)\n"
"v_mfma_f32_16x16x32_bf16 v[72:75], v[52:55], v[200:203], v[72:75]\nv_mfma_f32_16x16x32_bf16 v[76:79], v[52:55], v[204:207], v[76:79]\nds_read_b128 v[52:55], v35 offset:20480\ns_waitcnt lgkmcnt(3)\n"
"v_mfma_f32_16x16x32_bf16 v[64:67], v[40:43], v[208:211], v[64:67]\nv_mfma_f32_16x16x32_bf16 v[68:71], v[40:43], v[212:215], v[68:71]\nds_read_b128 v[40:43], v32 offset:24576\ns_waitcnt lgkmcnt(3)\n"
"v_mfma_f32_16x16x32_bf16 v[72:75], v[44:47], v[208:211], v[72:75]\nv_mfma_f32_16x16x32_bf16 v[76:79], v[44:47], v[212:215], v[76:79]\nds_read_b128 v[44:47], v33 offset:24576\ns_waitcnt lgkmcnt(3)\n"
"v_mfma_f32_16x16x32_bf16 v[64:67], v[48:51], v[216:219], v[64:67]\ns_add_u32 s54, s51, 32768\ns_cmp_eq_u32 s54, 163840\nv_mfma_f32_16x16x32_bf16 v[68:71], v[48:51], v[220:223], v[68:71]\n"
"s_cselect_b32 s54, 0, s54\ns_sub_u32 s54, s54, s51\nds_read_b128 v[48:51], v34 offset:24576\ns_waitcnt lgkmcnt(3)\n"
"v_mfma_f32_16x16x32_bf16 v[72:75], v[52:55], v[216:219], v[72:75]\nv_mfma_f32_16x16x32_bf16 v[76:79], v[52:55], v[220:223], v[76:79]\nds_read_b128 v[52:55], v35 offset:24576\ns_waitcnt lgkmcnt(3)\n"
"v_mfma_f32_16x16x32_bf16 v[64:67], v[40:43], v[224:227], v[64:67]\nv_add_u32 v32, s54, v32\nv_mfma_f32_16x16x32_bf16 v[68:71], v[40:43], v[228:231], v[68:71]\nds_read_b128 v[40:43], v32 offset:0\n"
"s_waitcnt lgkmcnt(3)\nv_mfma_f32_16x16x32_bf16 v[72:75], v[44:47], v[224:227], v[72:75]\nv_add_u32 v33, s54, v33\nv_mfma_f32_16x16x32_bf16 v[76:79], v[44:47], v[228:231], v[76:79]\n"
"ds_read_b128 v[44:47], v33 offset:0\ns_waitcnt lgkmcnt(3)\nv_mfma_f32_16x16x32_bf16 v[64:67], v[48:51], v[232:235], v[64:67]\nv_add_u32 v34, s54, v34\n"
"v_mfma_f32_16x16x32_bf16 v[68:71], v[48:51], v[236:239], v[68:71]\nds_read_b128 v[48:51], v34 offset:0\ns_waitcnt lgkmcnt(3)\nv_mfma_f32_16x16x32_bf16 v[72:75], v[52:55], v[232:235], v[72:75]\n"
"v_add_u32 v35, s54, v35\nv_mfma_f32_16x16x32_bf16 v[76:79], v[52:55], v[236:239], v[76:79]\nds_read_b128 v[52:55], v35 offset:0\ns_mov_b32 s52, s47\n"
"s_add_u32 s47, s51, s32\ns_add_u32 s51, s51, s54\ns_add_u32 s49, s49, 28672\ns_waitcnt vmcnt(8)\n"
"s_barrier\ns_waitcnt lgkmcnt(3)\nv_mfma_f32_16x16x32_bf16 v[96:99], v[40:43], v[128:131], 0\nv_mov_b32 v36, s48\n"
"s_add_u32 s48, s48, 1\ns_nop 0\nbuffer_store_dword v36, v19, s[24:27], s33 offen sc1\nv_mfma_f32_16x16x32_bf16 v[100:103], v[40:43], v[132:135], 0\n"
"ds_read_b128 v[40:43], v32 offset:4096\ns_waitcnt lgkmcnt(3)\nv_mfma_f32_16x16x32_bf16 v[104:107], v[44:47], v[128:131], 0\nv_mfma_f32_16x16x32_bf16 v[108:111], v[44:47], v[132:135], 0\n"
"ds_read_b128 v[44:47], v33 offset:4096\nv_cvt_pk_bf16_f32 v56, v64, v65\ns_waitcnt lgkmcnt(3)\nv_mfma_f32_16x16x32_bf16 v[96:99], v[48:51], v[136:139], v[96:99]\n"
"v_cvt_pk_bf16_f32 v57, v66, v67\nv_mfma_f32_16x16x32_bf16 v[100:103], v[48:51], v[140:143], v[100:103]\nds_read_b128 v[48:51], v34 offset:4096\nv_cvt_pk_bf16_f32 v58, v72, v73\n"
"s_waitcnt lgkmcnt(3)\nv_mfma_f32_16x16x32_bf16 v[104:107], v[52:55], v[136:139], v[104:107]\nv_cvt_pk_bf16_f32 v59, v74, v75\nv_mfma_f32_16x16x32_bf16 v[108:111], v[52:55], v[140:143], v[108:111]\n"
"ds_read_b128 v[52:55], v35 offset:4096\ns_waitcnt lgkmcnt(3)\nv_mfma_f32_16x16x32_bf16 v[96:99], v[40:43], v[144:147], v[96:99]\nv_mfma_f32_16x16x32_bf16 v[100:103], v[40:43], v[148:151], v[100:103]\n"
"ds_read_b128 v[40:43], v32 offset:8192\nbuffer_store_dwordx4 v[56:59], v15, s[20:23], s50 offen sc1\ns_waitcnt lgkmcnt(3)\nv_mfma_f32_16x16x32_bf16 v[104:107], v[44:47], v[144:147], v[104:107]\n"
"v_cvt_pk_bf16_f32 v60, v68, v69\nv_mfma_f32_16x16x32_bf16 v[108:111], v[44:47], v[148:151], v[108:111]\nds_read_b128 v[44:47], v33 offset:8192\nv_cvt_pk_bf16_f32 v61, v70, v71\n"
"s_waitcnt lgkmcnt(3)\nv_mfma_f32_16x16x32_bf16 v[96:99], v[48:51], v[152:155], v[96:99]\nv_cvt_pk_bf16_f32 v62, v76, v77\nv_mfma_f32_16x16x32_bf16 v[100:103], v[48:51], v[156:159], v[100:103]\n"
"ds_read_b128 v[48:51], v34 offset:8192\nv_cvt_pk_bf16_f32 v63, v78, v79\ns_waitcnt lgkmcnt(3)\nv_mfma_f32_16x16x32_bf16 v[104:107], v[52:55], v[152:155], v[104:107]\n"
"v_mfma_f32_16x16x32_bf16 v[108:111], v[52:55], v[156:159], v[108:111]\nds_read_b128 v[52:55], v35 offset:8192\ns_waitcnt lgkmcnt(3)\nv_mfma_f32_16x16x32_bf16 v[96:99], v[40:43], v[160:163], v[96:99]\n"
"buffer_store_dwordx4 v[60:63], v16, s[20:23], s50 offen sc1\nv_mfma_f32_16x16x32_bf16 v[100:103], v[40:43], v[164:167], v[100:103]\nds_read_b128 v[40:43], v32 offset:12288\ns_add_u32 s50, s50, s36\n"
"s_waitcnt lgkmcnt(3)\nv_mfma_f32_16x16x32_bf16 v[104:107], v[44:47], v[160:163], v[104:107]\ns_add_u32 m0, s52, 0\nv_mfma_f32_16x16x32_bf16 v[108:111], v[44:47], v[164:167], v[108:111]\n"
"ds_read_b128 v[44:47], v33 offset:12288\ns_add_u32 s53, s49, 0\ns_waitcnt lgkmcnt(3)\nv_mfma_f32_16x16x32_bf16 v[96:99], v[48:51], v[168:171], v[96:99]\n"
"buffer_load_dwordx4 v10, s[12:15], s53 offen lds\nv_mfma_f32_16x16x32_bf16 v[100:103], v[48:51], v[172:175], v[100:103]\nds_read_b128 v[48:51], v34 offset:12288\ns_add_u32 m0, s52, 4096\n"
"s_waitcnt lgkmcnt(3)\nv_mfma_f32_16x16x32_bf16 v[104:107], v[52:55], v[168:171], v[104:107]\ns_add_u32 s53, s49, 128\nv_mfma_f32_16x16x32_bf16 v[108:111], v[52:55], v[172:175], v[108:111]\n"
"ds_read_b128 v[52:55], v35 offset:12288\nbuffer_load_dwordx4 v10, s[12:15], s53 offen lds\ns_waitcnt lgkmcnt(3)\nv_mfma_f32_16x16x32_bf16 v[96:99], v[40:43], v[176:179], v[96:99]\n"
"s_add_u32 m0, s52, 8192\nv_mfma_f32_16x16x32_bf16 v[100:103], v[40:43], v[180:183], v[100:103]\nds_read_b128 v[40:43], v32 offset:16384\ns_add_u32 s53, s49, 256\n"
"s_waitcnt lgkmcnt(3)\nv_mfma_f32_16x16x32_bf16 v[104:107], v[44:47], v[176:179], v[104:107]\nbuffer_load_dwordx4 v10, s[12:15], s53 offen lds\nv_mfma_f32_16x16x32_bf16 v[108:111], v[44:47], v[180:183], v[108:111]\n"
"ds_read_b128 v[44:47], v33 offset:16384\ns_add_u32 m0, s52, 12288\ns_waitcnt lgkmcnt(3)\nv_mfma_f32_16x16x32_bf16 v[96:99], v[48:51], v[184:187], v[96:99]\n"
"s_add_u32 s53, s49, 384\nv_mfma_f32_16x16x32_bf16 v[100:103], v[48:51], v[188:191], v[100:103]\nds_read_b128 v[48:51], v34 offset:16384\nbuffer_load_dwordx4 v10, s[12:15], s53 offen lds\n"
"s_waitcnt lgkmcnt(3)\nv_mfma_f32_16x16x32_bf16 v[104:107], v[52:55], v[184:187], v[104:107]\ns_add_u32 m0, s52, 16384\nv_mfma_f32_16x16x32_bf16 v[108:111], v[52:55], v[188:191], v[108:111]\n"
"ds_read_b128 v[52:55], v35 offset:16384\ns_add_u32 s53, s49, 512\ns_waitcnt lgkmcnt(3)\nv_mfma_f32_16x16x32_bf16 v[96:99], v[40:43], v[192:195], v[96:99]\n"
"buffer_load_dwordx4 v10, s[12:15], s53 offen lds\nv_mfma_f32_16x16x32_bf16 v[100:103], v[40:43], v[196:199], v[100:103]\nds_read_b128 v[40:43], v32 offset:20480\ns_add_u32 m0, s52, 20480\n"
"s_waitcnt lgkmcnt(3)\nv_mfma_f32_16x16x32_bf16 v[104:107], v[44:47], v[192:195], v[104:107]\ns_add_u32 s53, s49, 640\nv_mfma_f32_16x16x32_bf16 v[108:111], v[44:47], v[196:199], v[108:111]\n"
"ds_read_b128 v[44:47], v33 offset:20480\nbuffer_load_dwordx4 v10, s[12:15], s53 offen lds\ns_waitcnt lgkmcnt(3)\nv_mfma_f32_16x16x32_bf16 v[96:99], v[48:51], v[200:203], v[96:99]\n"
"s_add_u32 m0, s52, 24576\nv_mfma_f32_16x16x32_bf16 v[100:103], v[48:51], v[204:207], v[100:103]\nds_read_b128 v[48:51], v34 offset:20480\ns_add_u32 s53, s49, 768\n"
"s_waitcnt lgkmcnt(3)\nv_mfma_f32_16x16x32_bf16 v[104:107], v[52:55], v[200:203], v[104:107]\nbuffer_load_dwordx4 v10, s[12:15], s53 offen lds\nv_mfma_f32_16x16x32_bf16 v[108:111], v[52:55], v[204:207], v[108:111]\n"
"ds_read_b128 v[52:55], v35 offset:20480\ns_waitcnt lgkmcnt(3)\nv_mfma_f32_16x16x32_bf16 v[96:99], v[40:43], v[208:211], v[96:99]\nv_mfma_f32_16x16x32_bf16 v[100:103], v[40:43], v[212:215], v[100:103]\n"
"ds_read_b128 v[40:43], v32 offset:24576\ns_waitcnt lgkmcnt(3)\nv_mfma_f32_16x16x32_bf16 v[104:107], v[44:47], v[208:211], v[104:107]\nv_mfma_f32_16x16x32_bf16 v[108:111], v[44:47], v[212:215], v[108:111]\n"
"ds_read_b128 v[44:47], v33 offset:24576\ns_waitcnt lgkmcnt(3)\nv_mfma_f32_16x16x32_bf16 v[96:99], v[48:51], v[216:219], v[96:99]\ns_add_u32 s54, s51, 32768\n"
"s_cmp_eq_u32 s54, 163840\nv_mfma_f32_16x16x32_bf16 v[100:103], v[48:51], v[220:223], v[100:103]\ns_cselect_b32 s54, 0, s54\ns_sub_u32 s54, s54, s51\n"
"ds_read_b128 v[48:51], v34 offset:24576\ns_waitcnt lgkmcnt(3)\nv_mfma_f32_16x16x32_bf16 v[104:107], v[52:55], v[216:219], v[104:107]\nv_mfma_f32_16x16x32_bf16 v[108:111], v[52:55], v[220:223], v[108:111]\n"
"ds_read_b128 v[52:55], v35 offset:24576\ns_waitcnt lgkmcnt(3)\nv_mfma_f32_16x16x32_bf16 v[96:99], v[40:43], v[224:227], v[96:99]\nv_add_u32 v32, s54, v32\n"
"v_mfma_f32_16x16x32_bf16 v[100:103], v[40:43], v[228:231], v[100:103]\nds_read_b128 v[40:43], v32 offset:0\ns_waitcnt lgkmcnt(3)\nv_mfma_f32_16x16x32_bf16 v[104:107], v[44:47], v[224:227], v[104:107]\n"
"v_add_u32 v33, s54, v33\nv_mfma_f32_16x16x32_bf16 v[108:111], v[44:47], v[228:231], v[108:111]\nds_read_b128 v[44:47], v33 offset:0\ns_waitcnt lgkmcnt(3)\n"
"v_mfma_f32_16x16x32_bf16 v[96:99], v[48:51], v[232:235], v[96:99]\nv_add_u32 v34, s54, v34\nv_mfma_f32_16x16x32_bf16 v[100:103], v[48:51], v[236:239], v[100:103]\nds_read_b128 v[48:51], v34 offset:0\n"
"s_waitcnt lgkmcnt(3)\nv_mfma_f32_16x16x32_bf16 v[104:107], v[52:55], v[232:235], v[104:107]\nv_add_u32 v35, s54, v35\nv_mfma_f32_16x16x32_bf16 v[108:111], v[52:55], v[236:239], v[108:111]\n"
"ds_read_b128 v[52:55], v35 offset:0\ns_mov_b32 s52, s47\ns_add_u32 s47, s51, s32\ns_add_u32 s51, s51, s54\n"
"s_add_u32 s49, s49, 28672\ns_waitcnt vmcnt(7)\ns_barrier\ns_waitcnt lgkmcnt(3)\n"
"v_mfma_f32_16x16x32_bf16 v[64:67], v[40:43], v[128:131], 0\nv_mov_b32 v36, s48\ns_add_u32 s48, s48, 1\ns_nop 0\n"
"buffer_store_dword v36, v19, s[24:27], s33 offen sc1\nbuffer_load_dword v37, v20, s[24:27], 0 offen sc1\nv_mfma_f32_16x16x32_bf16 v[68:71], v[40:43], v[132:135], 0\nds_read_b128 v[40:43], v32 offset:4096\n"
"s_waitcnt lgkmcnt(3)\nv_mfma_f32_16x16x32_bf16 v[72:75], v[44:47], v[128:131], 0\nv_mfma_f32_16x16x32_bf16 v[76:79], v[44:47], v[132:135], 0\nds_read_b128 v[44:47], v33 offset:4096\n"
"v_cvt_pk_bf16_f32 v56, v96, v97\ns_waitcnt lgkmcnt(3)\nv_mfma_f32_16x16x32_bf16 v[64:67], v[48:51], v[136:139], v[64:67]\nv_cvt_pk_bf16_f32 v57, v98, v99\n"
"v_mfma_f32_16x16x32_bf16 v[68:71], v[48:51], v[140:143], v[68:71]\nds_read_b128 v[48:51], v34 offset:4096\nv_cvt_pk_bf16_f32 v58, v104, v105\ns_waitcnt lgkmcnt(3)\n"
"v_mfma_f32_16x16x32_bf16 v[72:75], v[52:55], v[136:139], v[72:75]\nv_cvt_pk_bf16_f32 v59, v106, v107\nv_mfma_f32_16x16x32_bf16 v[76:79], v[52:55], v[140:143], v[76:79]\nds_read_b128 v[52:55], v35 offset:4096\n"
"s_waitcnt lgkmcnt(3)\nv_mfma_f32_16x16x32_bf16 v[64:67], v[40:43], v[144:147], v[64:67]\nv_mfma_f32_16x16x32_bf16 v[68:71], v[40:43], v[148:151], v[68:71]\nds_read_b128 v[40:43], v32 offset:8192\n"
"buffer_store_dwordx4 v[56:59], v15, s[20:23], s50 offen sc1\ns_waitcnt lgkmcnt(3)\nv_mfma_f32_16x16x32_bf16 v[72:75], v[44:47], v[144:147], v[72:75]\nv_cvt_pk_bf16_f32 v60, v100, v101\n"
"v_mfma_f32_16x16x32_bf16 v[76:79], v[44:47], v[148:151], v[76:79]\nds_read_b128 v[44:47], v33 offset:8192\nv_cvt_pk_bf16_f32 v61, v102, v103\ns_waitcnt lgkmcnt(3)\n"
"v_mfma_f32_16x16x32_bf16 v[64:67], v[48:51], v[152:155], v[64:67]\nv_cvt_pk_bf16_f32 v62, v108, v109\nv_mfma_f32_16x16x32_bf16 v[68:71], v[48:51], v[156:159], v[68:71]\nds_read_b128 v[48:51], v34 offset:8192\n"
"v_cvt_pk_bf16_f32 v63, v110, v111\ns_waitcnt lgkmcnt(3)\nv_mfma_f32_16x16x32_bf16 v[72:75], v[52:55], v[152:155], v[72:75]\nv_mfma_f32_16x16x32_bf16 v[76:79], v[52:55], v[156:159], v[76:79]\n"
"ds_read_b128 v[52:55], v35 offset:8192\ns_waitcnt lgkmcnt(3)\nv_mfma_f32_16x16x32_bf16 v[64:67], v[40:43], v[160:163], v[64:67]\nbuffer_store_dwordx4 v[60:63], v16, s[20:23], s50 offen sc1\n"
"v_mfma_f32_16x16x32_bf16 v[68:71], v[40:43], v[164:167], v[68:71]\nds_read_b128 v[40:43], v32 offset:12288\ns_add_u32 s50, s50, s36\ns_waitcnt lgkmcnt(3)\n"
"v_mfma_f32_16x16x32_bf16 v[72:75], v[44:47], v[160:163], v[72:75]\ns_add_u32 m0, s52, 0\nv_mfma_f32_16x16x32_bf16 v[76:79], v[44:47], v[164:167], v[76:79]\nds_read_b128 v[44:47], v33 offset:12288\n"
"s_add_u32 s53, s49, 0\ns_waitcnt lgkmcnt(3)\nv_mfma_f32_16x16x32_bf16 v[64:67], v[48:51], v[168:171], v[64:67]\nbuffer_load_dwordx4 v10, s[12:15], s53 offen lds\n"
"v_mfma_f32_16x16x32_bf16 v[68:71], v[48:51], v[172:175], v[68:71]\nds_read_b128 v[48:51], v34 offset:12288\ns_add_u32 m0, s52, 4096\ns_waitcnt lgkmcnt(3)\n"
"v_mfma_f32_16x16x32_bf16 v[72:75], v[52:55], v[168:171], v[72:75]\ns_add_u32 s53, s49, 128\nv_mfma_f32_16x16x32_bf16 v[76:79], v[52:55], v[172:175], v[76:79]\nds_read_b128 v[52:55], v35 offset:12288\n"
"buffer_load_dwordx4 v10, s[12:15], s53 offen lds\ns_waitcnt lgkmcnt(3)\nv_mfma_f32_16x16x32_bf16 v[64:67], v[40:43], v[176:179], v[64:67]\ns_add_u32 m0, s52, 8192\n"
"v_mfma_f32_16x16x32_bf16 v[68:71], v[40:43], v[180:183], v[68:71]\nds_read_b128 v[40:43], v32 offset:16384\ns_add_u32 s53, s49, 256\ns_waitcnt lgkmcnt(3)\n"
"v_mfma_f32_16x16x32_bf16 v[72:75], v[44:47], v[176:179], v[72:75]\nbuffer_load_dwordx4 v10, s[12:15], s53 offen lds\nv_mfma_f32_16x16x32_bf16 v[76:79], v[44:47], v[180:183], v[76:79]\nds_read_b128 v[44:47], v33 offset:16384\n"
"s_add_u32 m0, s52, 12288\ns_waitcnt lgkmcnt(3)\nv_mfma_f32_16x16x32_bf16 v[64:67], v[48:51], v[184:187], v[64:67]\ns_add_u32 s53, s49, 384\n"
"v_mfma_f32_16x16x32_bf16 v[68:71], v[48:51], v[188:191], v[68:71]\nds_read_b128 v[48:51], v34 offset:16384\nbuffer_load_dwordx4 v10, s[12:15], s53 offen lds\ns_waitcnt lgkmcnt(3)\n"
"v_mfma_f32_16x16x32_bf16 v[72:75], v[52:55], v[184:187], v[72:75]\ns_add_u32 m0, s52, 16384\nv_mfma_f32_16x16x32_bf16 v[76:79], v[52:55], v[188:191], v[76:79]\nds_read_b128 v[52:55], v35 offset:16384\n"
"s_add_u32 s53, s49, 512\ns_waitcnt lgkmcnt(3)\nv_mfma_f32_16x16x32_bf16 v[64:67], v[40:43], v[192:195], v[64:67]\nbuffer_load_dwordx4 v10, s[12:15], s53 offen lds\n"
"v_mfma_f32_16x16x32_bf16 v[68:71], v[40:43], v[196:199], v[68:71]\nds_read_b128 v[40:43], v32 offset:20480\ns_add_u32 m0, s52, 20480\ns_waitcnt lgkmcnt(3)\n"
"v_mfma_f32_16x16x32_bf16 v[72:75], v[44:47], v[192:195], v[72:75]\ns_add_u32 s53, s49, 640\nv_mfma_f32_16x16x32_bf16 v[76:79], v[44:47], v[196:199], v[76:79]\nds_read_b128 v[44:47], v33 offset:20480\n"
"buffer_load_dwordx4 v10, s[12:15], s53 offen lds\ns_waitcnt lgkmcnt(3)\nv_mfma_f32_16x16x32_bf16 v[64:67], v[48:51], v[200:203], v[64:67]\ns_add_u32 m0, s52, 24576\n"
"v_mfma_f32_16x16x32_bf16 v[68:71], v[48:51], v[204:207], v[68:71]\nds_read_b128 v[48:51], v34 offset:20480\ns_add_u32 s53, s49, 768\ns_waitcnt lgkmcnt(3)\n"
"v_mfma_f32_16x16x32_bf16 v[72:75], v[52:55], v[200:203], v[72:75]\nbuffer_load_dwordx4 v10, s[12:15], s53 offen lds\nv_mfma_f32_16x16x32_bf16 v[76:79], v[52:55], v[204:207], v[76:79]\nds_read_b128 v[52:55], v35 offset:20480\n"
"s_waitcnt lgkmcnt(3)\nv_mfma_f32_16x16x32_bf16 v[64:67], v[40:43], v[208:211], v[64:67]\nv_mfma_f32_16x16x32_bf16 v[68:71], v[40:43], v[212:215], v[68:71]\nds_read_b128 v[40:43], v32 offset:24576\n"
"s_waitcnt lgkmcnt(3)\nv_mfma_f32_16x16x32_bf16 v[72:75], v[44:47], v[208:211], v[72:75]\nv_mfma_f32_16x16x32_bf16 v[76:79], v[44:47], v[212:215], v[76:79]\nds_read_b128 v[44:47], v33 offset:24576\n"
"s_waitcnt lgkmcnt(3)\nv_mfma_f32_16x16x32_bf16 v[64:67], v[48:51], v[216:219], v[64:67]\ns_add_u32 s54, s51, 32768\ns_cmp_eq_u32 s54, 163840\n"
"v_mfma_f32_16x16x32_bf16 v[68:71], v[48:51], v[220:223], v[68:71]\ns_cselect_b32 s54, 0, s54\ns_sub_u32 s54, s54, s51\nds_read_b128 v[48:51], v34 offset:24576\n"
"s_waitcnt lgkmcnt(3)\nv_mfma_f32_16x16x32_bf16 v[72:75], v[52:55], v[216:219], v[72:75]\nv_mfma_f32_16x16x32_bf16 v[76:79], v[52:55], v[220:223], v[76:79]\nds_read_b128 v[52:55], v35 offset:24576\n"
"s_waitcnt lgkmcnt(3)\nv_mfma_f32_16x16x32_bf16 v[64:67], v[40:43], v[224:227], v[64:67]\nv_add_u32 v32, s54, v32\nv_mfma_f32_16x16x32_bf16 v[68:71], v[40:43], v[228:231], v[68:71]\n"
"ds_read_b128 v[40:43], v32 offset:0\ns_waitcnt lgkmcnt(3)\nv_mfma_f32_16x16x32_bf16 v[72:75], v[44:47], v[224:227], v[72:75]\nv_add_u32 v33, s54, v33\n"
"v_mfma_f32_16x16x32_bf16 v[76:79], v[44:47], v[228:231], v[76:79]\nds_read_b128 v[44:47], v33 offset:0\ns_waitcnt lgkmcnt(3)\nv_mfma_f32_16x16x32_bf16 v[64:67], v[48:51], v[232:235], v[64:67]\n"
"v_add_u32 v34, s54, v34\nv_mfma_f32_16x16x32_bf16 v[68:71], v[48:51], v[236:239], v[68:71]\nds_read_b128 v[48:51], v34 offset:0\ns_waitcnt lgkmcnt(3)\n"
"v_mfma_f32_16x16x32_bf16 v[72:75], v[52:55], v[232:235], v[72:75]\nv_add_u32 v35, s54, v35\nv_mfma_f32_16x16x32_bf16 v[76:79], v[52:55], v[236:239], v[76:79]\nds_read_b128 v[52:55], v35 offset:0\n"
"s_mov_b32 s52, s47\ns_add_u32 s47, s51, s32\ns_add_u32 s51, s51, s54\ns_add_u32 s49, s49, 28672\n"
"s_waitcnt vmcnt(7)\nfg1_pf:\nv_cmp_ne_u32 vcc, s28, v37\ns_cbranch_vccz fg1_pfok\n"
"s_add_u32 s55, s55, 1\ns_cmp_gt_u32 s55, 262144\ns_cbranch_scc0 fg1_pf2\ns_endpgm\n"
"fg1_pf2:\ns_sleep 1\nbuffer_load_dword v37, v20, s[24:27], 0 offen sc1\ns_waitcnt vmcnt(0)\n"
"s_branch fg1_pf\nfg1_pfok:\ns_barrier\ns_waitcnt lgkmcnt(3)\n"
"v_mfma_f32_16x16x32_bf16 v[96:99], v[40:43], v[128:131], 0\nv_mov_b32 v36, s48\ns_add_u32 s48, s48, 1\ns_nop 0\n"
"buffer_store_dword v36, v19, s[24:27], s33 offen sc1\nv_mfma_f32_16x16x32_bf16 v[100:103], v[40:43], v[132:135], 0\nds_read_b128 v[40:43], v32 offset:4096\ns_waitcnt lgkmcnt(3)\n"
"v_mfma_f32_16x16x32_bf16 v[104:107], v[44:47], v[128:131], 0\nv_mfma_f32_16x16x32_bf16 v[108:111], v[44:47], v[132:135], 0\nds_read_b128 v[44:47], v33 offset:4096\nv_cvt_pk_bf16_f32 v56, v64, v65\n"
"s_waitcnt lgkmcnt(3)\nv_mfma_f32_16x16x32_bf16 v[96:99], v[48:51], v[136:139], v[96:99]\nv_cvt_pk_bf16_f32 v57, v66, v67\nv_mfma_f32_16x16x32_bf16 v[100:103], v[48:51], v[140:143], v[100:103]\n"
"ds_read_b128 v[48:51], v34 offset:4096\nv_cvt_pk_bf16_f32 v58, v72, v73\ns_waitcnt lgkmcnt(3)\nv_mfma_f32_16x16x32_bf16 v[104:107], v[52:55], v[136:139], v[104:107]\n"
"v_cvt_pk_bf16_f32 v59, v74, v75\nv_mfma_f32_16x16x32_bf16 v[108:111], v[52:55], v[140:143], v[108:111]\nds_read_b128 v[52:55], v35 offset:4096\ns_waitcnt lgkmcnt(3)\n"
"v_mfma_f32_16x16x32_bf16 v[96:99], v[40:43], v[144:147], v[96:99]\nv_mfma_f32_16x16x32_bf16 v[100:103], v[40:43], v[148:151], v[100:103]\nds_read_b128 v[40:43], v32 offset:8192\nbuffer_store_dwordx4 v[56:59], v15, s[20:23], s50 offen sc1\n"
"s_waitcnt lgkmcnt(3)\nv_mfma_f32_16x16x32_bf16 v[104:107], v[44:47], v[144:147], v[104:107]\nv_cvt_pk_bf16_f32 v60, v68, v69\nv_mfma_f32_16x16x32_bf16 v[108:111], v[44:47], v[148:151], v[108:111]\n"
"ds_read_b128 v[44:47], v33 offset:8192\nv_cvt_pk_bf16_f32 v61, v70, v71\ns_waitcnt lgkmcnt(3)\nv_mfma_f32_16x16x32_bf16 v[96:99], v[48:51], v[152:155], v[96:99]\n"
"v_cvt_pk_bf16_f32 v62, v76, v77\nv_mfma_f32_16x16x32_bf16 v[100:103], v[48:51], v[156:159], v[100:103]\nds_read_b128 v[48:51], v34 offset:8192\nv_cvt_pk_bf16_f32 v63, v78, v79\n"
"s_waitcnt lgkmcnt(3)\nv_mfma_f32_16x16x32_bf16 v[104:107], v[52:55], v[152:155], v[104:107]\nv_mfma_f32_16x16x32_bf16 v[108:111], v[52:55], v[156:159], v[108:111]\nds_read_b128 v[52:55], v35 offset:8192\n"
"s_waitcnt lgkmcnt(3)\nv_mfma_f32_16x16x32_bf16 v[96:99], v[40:43], v[160:163], v[96:99]\nbuffer_store_dwordx4 v[60:63], v16, s[20:23], s50 offen sc1\nv_mfma_f32_16x16x32_bf16 v[100:103], v[40:43], v[164:167], v[100:103]\n"
"ds_read_b128 v[40:43], v32 offset:12288\ns_add_u32 s50, s50, s36\ns_waitcnt lgkmcnt(3)\nv_mfma_f32_16x16x32_bf16 v[104:107], v[44:47], v[160:163], v[104:107]\n"
"s_add_u32 m0, s52, 0\nv_mfma_f32_16x16x32_bf16 v[108:111], v[44:47], v[164:167], v[108:111]\nds_read_b128 v[44:47], v33 offset:12288\ns_add_u32 s53, s49, 0\n"
"s_waitcnt lgkmcnt(3)\nv_mfma_f32_16x16x32_bf16 v[96:99], v[48:51], v[168:171], v[96:99]\nbuffer_load_dwordx4 v10, s[12:15], s53 offen lds\nv_mfma_f32_16x16x32_bf16 v[100:103], v[48:51], v[172:175], v[100:103]\n"
"ds_read_b128 v[48:51], v34 offset:12288\ns_add_u32 m0, s52, 4096\ns_waitcnt lgkmcnt(3)\nv_mfma_f32_16x16x32_bf16 v[104:107], v[52:55], v[168:171], v[104:107]\n"
"s_add_u32 s53, s49, 128\nv_mfma_f32_16x16x32_bf16 v[108:111], v[52:55], v[172:175], v[108:111]\nds_read_b128 v[52:55], v35 offset:12288\nbuffer_load_dwordx4 v10, s[12:15], s53 offen lds\n"
"s_waitcnt lgkmcnt(3)\nv_mfma_f32_16x16x32_bf16 v[96:99], v[40:43], v[176:179], v[96:99]\ns_add_u32 m0, s52, 8192\nv_mfma_f32_16x16x32_bf16 v[100:103], v[40:43], v[180:183], v[100:103]\n"
"ds_read_b128 v[40:43], v32 offset:16384\ns_add_u32 s53, s49, 256\ns_waitcnt lgkmcnt(3)\nv_mfma_f32_16x16x32_bf16 v[104:107], v[44:47], v[176:179], v[104:107]\n"
"buffer_load_dwordx4 v10, s[12:15], s53 offen lds\nv_mfma_f32_16x16x32_bf16 v[108:111], v[44:47], v[180:183], v[108:111]\nds_read_b128 v[44:47], v33 offset:16384\ns_add_u32 m0, s52, 12288\n"
"s_waitcnt lgkmcnt(3)\nv_mfma_f32_16x16x32_bf16 v[96:99], v[48:51], v[184:187], v[96:99]\ns_add_u32 s53, s49, 384\nv_mfma_f32_16x16x32_bf16 v[100:103], v[48:51], v[188:191], v[100:103]\n"
"ds_read_b128 v[48:51], v34 offset:16384\nbuffer_load_dwordx4 v10, s[12:15], s53 offen lds\ns_waitcnt lgkmcnt(3)\nv_mfma_f32_16x16x32_bf16 v[104:107], v[52:55], v[184:187], v[104:107]\n"
"s_add_u32 m0, s52, 16384\nv_mfma_f32_16x16x32_bf16 v[108:111], v[52:55], v[188:191], v[108:111]\nds_read_b128 v[52:55], v35 offset:16384\ns_add_u32 s53, s49, 512\n"
"s_waitcnt lgkmcnt(3)\nv_mfma_f32_16x16x32_bf16 v[96:99], v[40:43], v[192:195], v[96:99]\nbuffer_load_dwordx4 v10, s[12:15], s53 offen lds\nv_mfma_f32_16x16x32_bf16 v[100:103], v[40:43], v[196:199], v[100:103]\n"
"ds_read_b128 v[40:43], v32 offset:20480\ns_add_u32 m0, s52, 20480\ns_waitcnt lgkmcnt(3)\nv_mfma_f32_16x16x32_bf16 v[104:107], v[44:47], v[192:195], v[104:107]\n"
"s_add_u32 s53, s49, 640\nv_mfma_f32_16x16x32_bf16 v[108:111], v[44:47], v[196:199], v[108:111]\nds_read_b128 v[44:47], v33 offset:20480\nbuffer_load_dwordx4 v10, s[12:15], s53 offen lds\n"
"s_waitcnt lgkmcnt(3)\nv_mfma_f32_16x16x32_bf16 v[96:99], v[48:51], v[200:203], v[96:99]\ns_add_u32 m0, s52, 24576\nv_mfma_f32_16x16x32_bf16 v[100:103], v[48:51], v[204:207], v[100:103]\n"
"ds_read_b128 v[48:51], v34 offset:20480\ns_add_u32 s53, s49, 768\ns_waitcnt lgkmcnt(3)\nv_mfma_f32_16x16x32_bf16 v[104:107], v[52:55], v[200:203], v[104:107]\n"
"buffer_load_dwordx4 v10, s[12:15], s53 offen lds\nv_mfma_f32_16x16x32_bf16 v[108:111], v[52:55], v[204:207], v[108:111]\nds_read_b128 v[52:55], v35 offset:20480\ns_mov_b32 s53, 0\n"
"buffer_load_dwordx4 a[0:3], v14, s[16:19], s53 offen sc1\ns_waitcnt lgkmcnt(3)\nv_mfma_f32_16x16x32_bf16 v[96:99], v[40:43], v[208:211], v[96:99]\ns_mov_b32 s53, 262144\n"
"buffer_load_dwordx4 a[4:7], v14, s[16:19], s53 offen sc1\nv_mfma_f32_16x16x32_bf16 v[100:103], v[40:43], v[212:215], v[100:103]\nds_read_b128 v[40:43], v32 offset:24576\ns_mov_b32 s53, 1024\n"
"buffer_load_dwordx4 a[8:11], v14, s[16:19], s53 offen sc1\ns_waitcnt lgkmcnt(3)\nv_mfma_f32_16x16x32_bf16 v[104:107], v[44:47], v[208:211], v[104:107]\ns_mov_b32 s53, 263168\n"
"buffer_load_dwordx4 a[12:15], v14, s[16:19], s53 offen sc1\nv_mfma_f32_16x16x32_bf16 v[108:111], v[44:47], v[212:215], v[108:111]\nds_read_b128 v[44:47], v33 offset:24576\ns_waitcnt lgkmcnt(3)\n"
"v_mfma_f32_16x16x32_bf16 v[96:99], v[48:51], v[216:219], v[96:99]\ns_add_u32 s54, s51, 32768\ns_cmp_eq_u32 s54, 163840\nv_mfma_f32_16x16x32_bf16 v[100:103], v[48:51], v[220:223], v[100:103]\n"
"s_cselect_b32 s54, 0, s54\ns_sub_u32 s54, s54, s51\nds_read_b128 v[48:51], v34 offset:24576\ns_waitcnt lgkmcnt(3)\n"
"v_mfma_f32_16x16x32_bf16 v[104:107], v[52:55], v[216:219], v[104:107]\nv_mfma_f32_16x16x32_bf16 v[108:111], v[52:55], v[220:223], v[108:111]\nds_read_b128 v[52:55], v35 offset:24576\ns_waitcnt lgkmcnt(3)\n"
"v_mfma_f32_16x16x32_bf16 v[96:99], v[40:43], v[224:227], v[96:99]\nv_add_u32 v32, s54, v32\nv_mfma_f32_16x16x32_bf16 v[100:103], v[40:43], v[228:231], v[100:103]\nds_read_b128 v[40:43], v32 offset:0\n"
"s_waitcnt lgkmcnt(3)\nv_mfma_f32_16x16x32_bf16 v[104:107], v[44:47], v[224:227], v[104:107]\nv_add_u32 v33, s54, v33\nv_mfma_f32_16x16x32_bf16 v[108:111], v[44:47], v[228:231], v[108:111]\n"
"ds_read_b128 v[44:47], v33 offset:0\ns_waitcnt lgkmcnt(3)\nv_mfma_f32_16x16x32_bf16 v[96:99], v[48:51], v[232:235], v[96:99]\nv_add_u32 v34, s54, v34\n"
"v_mfma_f32_16x16x32_bf16 v[100:103], v[48:51], v[236:239], v[100:103]\nds_read_b128 v[48:51], v34 offset:0\ns_waitcnt lgkmcnt(3)\nv_mfma_f32_16x16x32_bf16 v[104:107], v[52:55], v[232:235], v[104:107]\n"
"v_add_u32 v35, s54, v35\nv_mfma_f32_16x16x32_bf16 v[108:111], v[52:55], v[236:239], v[108:111]\nds_read_b128 v[52:55], v35 offset:0\ns_mov_b32 s52, s47\n"
"s_add_u32 s47, s51, s32\ns_add_u32 s51, s51, s54\ns_add_u32 s49, s49, 28672\ns_waitcnt vmcnt(11)\n"
"s_barrier\ns_waitcnt lgkmcnt(3)\nv_mfma_f32_16x16x32_bf16 v[64:67], v[40:43], v[128:131], 0\nv_mov_b32 v36, s48\n"
"s_add_u32 s48, s48, 1\ns_nop 0\nbuffer_store_dword v36, v19, s[24:27], s33 offen sc1\nv_mfma_f32_16x16x32_bf16 v[68:71], v[40:43], v[132:135], 0\n"
"ds_read_b128 v[40:43], v32 offset:4096\ns_waitcnt lgkmcnt(3)\nv_mfma_f32_16x16x32_bf16 v[72:75], v[44:47], v[128:131], 0\nv_mfma_f32_16x16x32_bf16 v[76:79], v[44:47], v[132:135], 0\n"
"ds_read_b128 v[44:47], v33 offset:4096\nv_cvt_pk_bf16_f32 v56, v96, v97\ns_waitcnt lgkmcnt(3)\nv_mfma_f32_16x16x32_bf16 v[64:67], v[48:51], v[136:139], v[64:67]\n"
"v_cvt_pk_bf16_f32 v57, v98, v99\nv_mfma_f32_16x16x32_bf16 v[68:71], v[48:51], v[140:143], v[68:71]\nds_read_b128 v[48:51], v34 offset:4096\nv_cvt_pk_bf16_f32 v58, v104, v105\n"
"s_waitcnt lgkmcnt(3)\nv_mfma_f32_16x16x32_bf16 v[72:75], v[52:55], v[136:139], v[72:75]\nv_cvt_pk_bf16_f32 v59, v106, v107\nv_mfma_f32_16x16x32_bf16 v[76:79], v[52:55], v[140:143], v[76:79]\n"
"ds_read_b128 v[52:55], v35 offset:4096\ns_waitcnt lgkmcnt(3)\nv_mfma_f32_16x16x32_bf16 v[64:67], v[40:43], v[144:147], v[64:67]\nv_mfma_f32_16x16x32_bf16 v[68:71], v[40:43], v[148:151], v[68:71]\n"
"ds_read_b128 v[40:43], v32 offset:8192\nbuffer_store_dwordx4 v[56:59], v15, s[20:23], s50 offen sc1\ns_waitcnt lgkmcnt(3)\nv_mfma_f32_16x16x32_bf16 v[72:75], v[44:47], v[144:147], v[72:75]\n"
"v_cvt_pk_bf16_f32 v60, v100, v101\nv_mfma_f32_16x16x32_bf16 v[76:79], v[44:47], v[148:151], v[76:79]\nds_read_b128 v[44:47], v33 offset:8192\nv_cvt_pk_bf16_f32 v61, v102, v103\n"
"s_waitcnt lgkmcnt(3)\nv_mfma_f32_16x16x32_bf16 v[64:67], v[48:51], v[152:155], v[64:67]\nv_cvt_pk_bf16_f32 v62, v108, v109\nv_mfma_f32_16x16x32_bf16 v[68:71], v[48:51], v[156:159], v[68:71]\n"
"ds_read_b128 v[48:51], v34 offset:8192\nv_cvt_pk_bf16_f32 v63, v110, v111\ns_waitcnt lgkmcnt(3)\nv_mfma_f32_16x16x32_bf16 v[72:75], v[52:55], v[152:155], v[72:75]\n"
"v_mfma_f32_16x16x32_bf16 v[76:79], v[52:55], v[156:159], v[76:79]\nds_read_b128 v[52:55], v35 offset:8192\ns_waitcnt lgkmcnt(3)\nv_mfma_f32_16x16x32_bf16 v[64:67], v[40:43], v[160:163], v[64:67]\n"
"buffer_store_dwordx4 v[60:63], v16, s[20:23], s50 offen sc1\nv_mfma_f32_16x16x32_bf16 v[68:71], v[40:43], v[164:167], v[68:71]\nds_read_b128 v[40:43], v32 offset:12288\ns_add_u32 s50, s50, s36\n"
"s_waitcnt lgkmcnt(3)\nv_mfma_f32_16x16x32_bf16 v[72:75], v[44:47], v[160:163], v[72:75]\ns_add_u32 m0, s52, 0\nv_mfma_f32_16x16x32_bf16 v[76:79], v[44:47], v[164:167], v[76:79]\n"
"ds_read_b128 v[44:47], v33 offset:12288\ns_add_u32 s53, s49, 0\ns_waitcnt lgkmcnt(3)\nv_mfma_f32_16x16x32_bf16 v[64:67], v[48:51], v[168:171], v[64:67]\n"
"buffer_load_dwordx4 v10, s[12:15], s53 offen lds\nv_mfma_f32_16x16x32_bf16 v[68:71], v[48:51], v[172:175], v[68:71]\nds_read_b128 v[48:51], v34 offset:12288\ns_add_u32 m0, s52, 4096\n"
"s_waitcnt lgkmcnt(3)\nv_mfma_f32_16x16x32_bf16 v[72:75], v[52:55], v[168:171], v[72:75]\ns_add_u32 s53, s49, 128\nv_mfma_f32_16x16x32_bf16 v[76:79], v[52:55], v[172:175], v[76:79]\n"
"ds_read_b128 v[52:55], v35 offset:12288\nbuffer_load_dwordx4 v10, s[12:15], s53 offen lds\ns_waitcnt lgkmcnt(3)\nv_mfma_f32_16x16x32_bf16 v[64:67], v[40:43], v[176:179], v[64:67]\n"
"s_add_u32 m0, s52, 8192\nv_mfma_f32_16x16x32_bf16 v[68:71], v[40:43], v[180:183], v[68:71]\nds_read_b128 v[40:43], v32 offset:16384\ns_add_u32 s53, s49, 256\n"
"s_waitcnt lgkmcnt(3)\nv_mfma_f32_16x16x32_bf16 v[72:75], v[44:47], v[176:179], v[72:75]\nbuffer_load_dwordx4 v10, s[12:15], s53 offen lds\nv_mfma_f32_16x16x32_bf16 v[76:79], v[44:47], v[180:183], v[76:79]\n"
"ds_read_b128 v[44:47], v33 offset:16384\ns_add_u32 m0, s52, 12288\ns_waitcnt lgkmcnt(3)\nv_mfma_f32_16x16x32_bf16 v[64:67], v[48:51], v[184:187], v[64:67]\n"
"s_add_u32 s53, s49, 384\nv_mfma_f32_16x16x32_bf16 v[68:71], v[48:51], v[188:191], v[68:71]\nds_read_b128 v[48:51], v34 offset:16384\nbuffer_load_dwordx4 v10, s[12:15], s53 offen lds\n"
"s_waitcnt lgkmcnt(3)\nv_mfma_f32_16x16x32_bf16 v[72:75], v[52:55], v[184:187], v[72:75]\ns_add_u32 m0, s52, 16384\nv_mfma_f32_16x16x32_bf16 v[76:79], v[52:55], v[188:191], v[76:79]\n"
"ds_read_b128 v[52:55], v35 offset:16384\ns_add_u32 s53, s49, 512\ns_waitcnt lgkmcnt(3)\nv_mfma_f32_16x16x32_bf16 v[64:67], v[40:43], v[192:195], v[64:67]\n"
"buffer_load_dwordx4 v10, s[12:15], s53 offen lds\nv_mfma_f32_16x16x32_bf16 v[68:71], v[40:43], v[196:199], v[68:71]\nds_read_b128 v[40:43], v32 offset:20480\ns_add_u32 m0, s52, 20480\n"
"s_waitcnt lgkmcnt(3)\nv_mfma_f32_16x16x32_bf16 v[72:75], v[44:47], v[192:195], v[72:75]\ns_add_u32 s53, s49, 640\nv_mfma_f32_16x16x32_bf16 v[76:79], v[44:47], v[196:199], v[76:79]\n"
"ds_read_b128 v[44:47], v33 offset:20480\nbuffer_load_dwordx4 v10, s[12:15], s53 offen lds\ns_waitcnt lgkmcnt(3)\nv_mfma_f32_16x16x32_bf16 v[64:67], v[48:51], v[200:203], v[64:67]\n"
"s_add_u32 m0, s52, 24576\nv_mfma_f32_16x16x32_bf16 v[68:71], v[48:51], v[204:207], v[68:71]\nds_read_b128 v[48:51], v34 offset:20480\ns_add_u32 s53, s49, 768\n"
"s_waitcnt lgkmcnt(3)\nv_mfma_f32_16x16x32_bf16 v[72:75], v[52:55], v[200:203], v[72:75]\nbuffer_load_dwordx4 v10, s[12:15], s53 offen lds\nv_mfma_f32_16x16x32_bf16 v[76:79], v[52:55], v[204:207], v[76:79]\n"
"ds_read_b128 v[52:55], v35 offset:20480\ns_mov_b32 s53, 2048\nbuffer_load_dwordx4 a[16:19], v14, s[16:19], s53 offen sc1\ns_waitcnt lgkmcnt(3)\n"
"v_mfma_f32_16x16x32_bf16 v[64:67], v[40:43], v[208:211], v[64:67]\ns_mov_b32 s53, 264192\nbuffer_load_dwordx4 a[20:23], v14, s[16:19], s53 offen sc1\nv_mfma_f32_16x16x32_bf16 v[68:71], v[40:43], v[212:215], v[68:71]\n"
"ds_read_b128 v[40:43], v32 offset:24576\ns_mov_b32 s53, 3072\nbuffer_load_dwordx4 a[24:27], v14, s[16:19], s53 offen sc1\ns_waitcnt lgkmcnt(3)\n"
"v_mfma_f32_16x16x32_bf16 v[72:75], v[44:47], v[208:211], v[72:75]\ns_mov_b32 s53, 265216\nbuffer_load_dwordx4 a[28:31], v14, s[16:19], s53 offen sc1\nv_mfma_f32_16x16x32_bf16 v[76:79], v[44:47], v[212:215], v[76:79]\n"
"ds_read_b128 v[44:47], v33 offset:24576\ns_waitcnt lgkmcnt(3)\nv_mfma_f32_16x16x32_bf16 v[64:67], v[48:51], v[216:219], v[64:67]\ns_add_u32 s54, s51, 32768\n"
"s_cmp_eq_u32 s54, 163840\nv_mfma_f32_16x16x32_bf16 v[68:71], v[48:51], v[220:223], v[68:71]\ns_cselect_b32 s54, 0, s54\ns_sub_u32 s54, s54, s51\n"
"ds_read_b128 v[48:51], v34 offset:24576\ns_waitcnt lgkmcnt(3)\nv_mfma_f32_16x16x32_bf16 v[72:75], v[52:55], v[216:219], v[72:75]\nv_mfma_f32_16x16x32_bf16 v[76:79], v[52:55], v[220:223], v[76:79]\n"
"ds_read_b128 v[52:55], v35 offset:24576\ns_waitcnt lgkmcnt(3)\nv_mfma_f32_16x16x32_bf16 v[64:67], v[40:43], v[224:227], v[64:67]\nv_add_u32 v32, s54, v32\n"
"v_mfma_f32_16x16x32_bf16 v[68:71], v[40:43], v[228:231], v[68:71]\nds_read_b128 v[40:43], v32 offset:0\ns_waitcnt lgkmcnt(3)\nv_mfma_f32_16x16x32_bf16 v[72:75], v[44:47], v[224:227], v[72:75]\n"
"v_add_u32 v33, s54, v33\nv_mfma_f32_16x16x32_bf16 v[76:79], v[44:47], v[228:231], v[76:79]\nds_read_b128 v[44:47], v33 offset:0\ns_waitcnt lgkmcnt(3)\n"
"v_mfma_f32_16x16x32_bf16 v[64:67], v[48:51], v[232:235], v[64:67]\nv_add_u32 v34, s54, v34\nv_mfma_f32_16x16x32_bf16 v[68:71], v[48:51], v[236:239], v[68:71]\nds_read_b128 v[48:51], v34 offset:0\n"
"s_waitcnt lgkmcnt(3)\nv_mfma_f32_16x16x32_bf16 v[72:75], v[52:55], v[232:235], v[72:75]\nv_add_u32 v35, s54, v35\nv_mfma_f32_16x16x32_bf16 v[76:79], v[52:55], v[236:239], v[76:79]\n"
"ds_read_b128 v[52:55], v35 offset:0\ns_mov_b32 s52, s47\ns_add_u32 s47, s51, s32\ns_add_u32 s51, s51, s54\n"
"s_add_u32 s49, s49, 28672\ns_waitcnt vmcnt(11)\ns_barrier\ns_waitcnt lgkmcnt(3)\n"
"v_mfma_f32_16x16x32_bf16 v[96:99], v[40:43], v[128:131], 0\nv_mov_b32 v36, s48\ns_add_u32 s48, s48, 1\ns_nop 0\n"
"buffer_store_dword v36, v19, s[24:27], s33 offen sc1\nv_mfma_f32_16x16x32_bf16 v[100:103], v[40:43], v[132:135], 0\nds_read_b128 v[40:43], v32 offset:4096\ns_waitcnt lgkmcnt(3)\n"
"v_mfma_f32_16x16x32_bf16 v[104:107], v[44:47], v[128:131], 0\nv_mfma_f32_16x16x32_bf16 v[108:111], v[44:47], v[132:135], 0\nds_read_b128 v[44:47], v33 offset:4096\nv_cvt_pk_bf16_f32 v56, v64, v65\n"
"s_waitcnt lgkmcnt(3)\nv_mfma_f32_16x16x32_bf16 v[96:99], v[48:51], v[136:139], v[96:99]\nv_cvt_pk_bf16_f32 v57, v66, v67\nv_mfma_f32_16x16x32_bf16 v[100:103], v[48:51], v[140:143], v[100:103]\n"
"ds_read_b128 v[48:51], v34 offset:4096\nv_cvt_pk_bf16_f32 v58, v72, v73\ns_waitcnt lgkmcnt(3)\nv_mfma_f32_16x16x32_bf16 v[104:107], v[52:55], v[136:139], v[104:107]\n"
"v_cvt_pk_bf16_f32 v59, v74, v75\nv_mfma_f32_16x16x32_bf16 v[108:111], v[52:55], v[140:143], v[108:111]\nds_read_b128 v[52:55], v35 offset:4096\ns_waitcnt lgkmcnt(3)\n"
"v_mfma_f32_16x16x32_bf16 v[96:99], v[40:43], v[144:147], v[96:99]\nv_mfma_f32_16x16x32_bf16 v[100:103], v[40:43], v[148:151], v[100:103]\nds_read_b128 v[40:43], v32 offset:8192\nbuffer_store_dwordx4 v[56:59], v15, s[20:23], s50 offen sc1\n"
"s_waitcnt lgkmcnt(3)\nv_mfma_f32_16x16x32_bf16 v[104:107], v[44:47], v[144:147], v[104:107]\nv_cvt_pk_bf16_f32 v60, v68, v69\nv_mfma_f32_16x16x32_bf16 v[108:111], v[44:47], v[148:151], v[108:111]\n"
"ds_read_b128 v[44:47], v33 offset:8192\nv_cvt_pk_bf16_f32 v61, v70, v71\ns_waitcnt lgkmcnt(3)\nv_mfma_f32_16x16x32_bf16 v[96:99], v[48:51], v[152:155], v[96:99]\n"
"v_cvt_pk_bf16_f32 v62, v76, v77\nv_mfma_f32_16x16x32_bf16 v[100:103], v[48:51], v[156:159], v[100:103]\nds_read_b128 v[48:51], v34 offset:8192\nv_cvt_pk_bf16_f32 v63, v78, v79\n"
"s_waitcnt lgkmcnt(3)\nv_mfma_f32_16x16x32_bf16 v[104:107], v[52:55], v[152:155], v[104:107]\nv_mfma_f32_16x16x32_bf16 v[108:111], v[52:55], v[156:159], v[108:111]\nds_read_b128 v[52:55], v35 offset:8192\n"
"s_waitcnt lgkmcnt(3)\nv_mfma_f32_16x16x32_bf16 v[96:99], v[40:43], v[160:163], v[96:99]\nbuffer_store_dwordx4 v[60:63], v16, s[20:23], s50 offen sc1\nv_mfma_f32_16x16x32_bf16 v[100:103], v[40:43], v[164:167], v[100:103]\n"
"ds_read_b128 v[40:43], v32 offset:12288\ns_add_u32 s50, s50, s36\ns_waitcnt lgkmcnt(3)\nv_mfma_f32_16x16x32_bf16 v[104:107], v[44:47], v[160:163], v[104:107]\n"
"s_add_u32 m0, s52, 0\nv_mfma_f32_16x16x32_bf16 v[108:111], v[44:47], v[164:167], v[108:111]\nds_read_b128 v[44:47], v33 offset:12288\ns_add_u32 s53, s49, 0\n"
"s_waitcnt lgkmcnt(3)\nv_mfma_f32_16x16x32_bf16 v[96:99], v[48:51], v[168:171], v[96:99]\nbuffer_load_dwordx4 v10, s[12:15], s53 offen lds\nv_mfma_f32_16x16x32_bf16 v[100:103], v[48:51], v[172:175], v[100:103]\n"
"ds_read_b128 v[48:51], v34 offset:12288\ns_add_u32 m0, s52, 4096\ns_waitcnt lgkmcnt(3)\nv_mfma_f32_16x16x32_bf16 v[104:107], v[52:55], v[168:171], v[104:107]\n"
"s_add_u32 s53, s49, 128\nv_mfma_f32_16x16x32_bf16 v[108:111], v[52:55], v[172:175], v[108:111]\nds_read_b128 v[52:55], v35 offset:12288\nbuffer_load_dwordx4 v10, s[12:15], s53 offen lds\n"
"s_waitcnt lgkmcnt(3)\nv_mfma_f32_16x16x32_bf16 v[96:99], v[40:43], v[176:179], v[96:99]\ns_add_u32 m0, s52, 8192\nv_mfma_f32_16x16x32_bf16 v[100:103], v[40:43], v[180:183], v[100:103]\n"
"ds_read_b128 v[40:43], v32 offset:16384\ns_add_u32 s53, s49, 256\ns_waitcnt lgkmcnt(3)\nv_mfma_f32_16x16x32_bf16 v[104:107], v[44:47], v[176:179], v[104:107]\n"
"buffer_load_dwordx4 v10, s[12:15], s53 offen lds\nv_mfma_f32_16x16x32_bf16 v[108:111], v[44:47], v[180:183], v[108:111]\nds_read_b128 v[44:47], v33 offset:16384\ns_add_u32 m0, s52, 12288\n"
"s_waitcnt lgkmcnt(3)\nv_mfma_f32_16x16x32_bf16 v[96:99], v[48:51], v[184:187], v[96:99]\ns_add_u32 s53, s49, 384\nv_mfma_f32_16x16x32_bf16 v[100:103], v[48:51], v[188:191], v[100:103]\n"
"ds_read_b128 v[48:51], v34 offset:16384\nbuffer_load_dwordx4 v10, s[12:15], s53 offen lds\ns_waitcnt lgkmcnt(3)\nv_mfma_f32_16x16x32_bf16 v[104:107], v[52:55], v[184:187], v[104:107]\n"
"s_add_u32 m0, s52, 16384\nv_mfma_f32_16x16x32_bf16 v[108:111], v[52:55], v[188:191], v[108:111]\nds_read_b128 v[52:55], v35 offset:16384\ns_add_u32 s53, s49, 512\n"
"s_waitcnt lgkmcnt(3)\nv_mfma_f32_16x16x32_bf16 v[96:99], v[40:43], v[192:195], v[96:99]\nbuffer_load_dwordx4 v10, s[12:15], s53 offen lds\nv_mfma_f32_16x16x32_bf16 v[100:103], v[40:43], v[196:199], v[100:103]\n"
"ds_read_b128 v[40:43], v32 offset:20480\ns_add_u32 m0, s52, 20480\ns_waitcnt lgkmcnt(3)\nv_mfma_f32_16x16x32_bf16 v[104:107], v[44:47], v[192:195], v[104:107]\n"
"s_add_u32 s53, s49, 640\nv_mfma_f32_16x16x32_bf16 v[108:111], v[44:47], v[196:199], v[108:111]\nds_read_b128 v[44:47], v33 offset:20480\nbuffer_load_dwordx4 v10, s[12:15], s53 offen lds\n"
"s_waitcnt lgkmcnt(3)\nv_mfma_f32_16x16x32_bf16 v[96:99], v[48:51], v[200:203], v[96:99]\ns_add_u32 m0, s52, 24576\nv_mfma_f32_16x16x32_bf16 v[100:103], v[48:51], v[204:207], v[100:103]\n"
"ds_read_b128 v[48:51], v34 offset:20480\ns_add_u32 s53, s49, 768\ns_waitcnt lgkmcnt(3)\nv_mfma_f32_16x16x32_bf16 v[104:107], v[52:55], v[200:203], v[104:107]\n"
"buffer_load_dwordx4 v10, s[12:15], s53 offen lds\nv_mfma_f32_16x16x32_bf16 v[108:111], v[52:55], v[204:207], v[108:111]\nds_read_b128 v[52:55], v35 offset:20480\ns_mov_b32 s53, 4096\n"
"buffer_load_dwordx4 a[32:35], v14, s[16:19], s53 offen sc1\ns_waitcnt lgkmcnt(3)\nv_mfma_f32_16x16x32_bf16 v[96:99], v[40:43], v[208:211], v[96:99]\ns_mov_b32 s53, 266240\n"
"buffer_load_dwordx4 a[36:39], v14, s[16:19], s53 offen sc1\nv_mfma_f32_16x16x32_bf16 v[100:103], v[40:43], v[212:215], v[100:103]\nds_read_b128 v[40:43], v32 offset:24576\ns_mov_b32 s53, 5120\n"
"buffer_load_dwordx4 a[40:43], v14, s[16:19], s53 offen sc1\ns_waitcnt lgkmcnt(3)\nv_mfma_f32_16x16x32_bf16 v[104:107], v[44:47], v[208:211], v[104:107]\ns_mov_b32 s53, 267264\n"
"buffer_load_dwordx4 a[44:47], v14, s[16:19], s53 offen sc1\nv_mfma_f32_16x16x32_bf16 v[108:111], v[44:47], v[212:215], v[108:111]\nds_read_b128 v[44:47], v33 offset:24576\ns_waitcnt lgkmcnt(3)\n"
"v_mfma_f32_16x16x32_bf16 v[96:99], v[48:51], v[216:219], v[96:99]\ns_add_u32 s54, s51, 32768\ns_cmp_eq_u32 s54, 163840\nv_mfma_f32_16x16x32_bf16 v[100:103], v[48:51], v[220:223], v[100:103]\n"
"s_cselect_b32 s54, 0, s54\ns_sub_u32 s54, s54, s51\nds_read_b128 v[48:51], v34 offset:24576\ns_waitcnt lgkmcnt(3)\n"
"v_mfma_f32_16x16x32_bf16 v[104:107], v[52:55], v[216:219], v[104:107]\nv_mfma_f32_16x16x32_bf16 v[108:111], v[52:55], v[220:223], v[108:111]\nds_read_b128 v[52:55], v35 offset:24576\ns_waitcnt lgkmcnt(3)\n"
"v_mfma_f32_16x16x32_bf16 v[96:99], v[40:43], v[224:227], v[96:99]\nv_add_u32 v32, s54, v32\nv_mfma_f32_16x16x32_bf16 v[100:103], v[40:43], v[228:231], v[100:103]\nds_read_b128 v[40:43], v32 offset:0\n"
"s_waitcnt lgkmcnt(3)\nv_mfma_f32_16x16x32_bf16 v[104:107], v[44:47], v[224:227], v[104:107]\nv_add_u32 v33, s54, v33\nv_mfma_f32_16x16x32_bf16 v[108:111], v[44:47], v[228:231], v[108:111]\n"
"ds_read_b128 v[44:47], v33 offset:0\ns_waitcnt lgkmcnt(3)\nv_mfma_f32_16x16x32_bf16 v[96:99], v[48:51], v[232:235], v[96:99]\nv_add_u32 v34, s54, v34\n"
"v_mfma_f32_16x16x32_bf16 v[100:103], v[48:51], v[236:239], v[100:103]\nds_read_b128 v[48:51], v34 offset:0\ns_waitcnt lgkmcnt(3)\nv_mfma_f32_16x16x32_bf16 v[104:107], v[52:55], v[232:235], v[104:107]\n"
"v_add_u32 v35, s54, v35\nv_mfma_f32_16x16x32_bf16 v[108:111], v[52:55], v[236:239], v[108:111]\nds_read_b128 v[52:55], v35 offset:0\ns_mov_b32 s52, s47\n"
"s_add_u32 s47, s51, s32\ns_add_u32 s51, s51, s54\ns_add_u32 s49, s49, 28672\ns_waitcnt vmcnt(11)\n"
"s_barrier\ns_waitcnt lgkmcnt(3)\nv_mfma_f32_16x16x32_bf16 v[64:67], v[40:43], v[128:131], 0\nv_mov_b32 v36, s48\n"
"s_add_u32 s48, s48, 1\ns_nop 0\nbuffer_store_dword v36, v19, s[24:27], s33 offen sc1\nv_mfma_f32_16x16x32_bf16 v[68:71], v[40:43], v[132:135], 0\n"
"ds_read_b128 v[40:43], v32 offset:4096\ns_waitcnt lgkmcnt(3)\nv_mfma_f32_16x16x32_bf16 v[72:75], v[44:47], v[128:131], 0\nv_mfma_f32_16x16x32_bf16 v[76:79], v[44:47], v[132:135], 0\n"
"ds_read_b128 v[44:47], v33 offset:4096\nv_cvt_pk_bf16_f32 v56, v96, v97\ns_waitcnt lgkmcnt(3)\nv_mfma_f32_16x16x32_bf16 v[64:67], v[48:51], v[136:139], v[64:67]\n"
"v_cvt_pk_bf16_f32 v57, v98, v99\nv_mfma_f32_16x16x32_bf16 v[68:71], v[48:51], v[140:143], v[68:71]\nds_read_b128 v[48:51], v34 offset:4096\nv_cvt_pk_bf16_f32 v58, v104, v105\n"
"s_waitcnt lgkmcnt(3)\nv_mfma_f32_16x16x32_bf16 v[72:75], v[52:55], v[136:139], v[72:75]\nv_cvt_pk_bf16_f32 v59, v106, v107\nv_mfma_f32_16x16x32_bf16 v[76:79], v[52:55], v[140:143], v[76:79]\n"
"ds_read_b128 v[52:55], v35 offset:4096\ns_waitcnt lgkmcnt(3)\nv_mfma_f32_16x16x32_bf16 v[64:67], v[40:43], v[144:147], v[64:67]\nv_mfma_f32_16x16x32_bf16 v[68:71], v[40:43], v[148:151], v[68:71]\n"
"ds_read_b128 v[40:43], v32 offset:8192\nbuffer_store_dwordx4 v[56:59], v15, s[20:23], s50 offen sc1\ns_waitcnt lgkmcnt(3)\nv_mfma_f32_16x16x32_bf16 v[72:75], v[44:47], v[144:147], v[72:75]\n"
"v_cvt_pk_bf16_f32 v60, v100, v101\nv_mfma_f32_16x16x32_bf16 v[76:79], v[44:47], v[148:151], v[76:79]\nds_read_b128 v[44:47], v33 offset:8192\nv_cvt_pk_bf16_f32 v61, v102, v103\n"
"s_waitcnt lgkmcnt(3)\nv_mfma_f32_16x16x32_bf16 v[64:67], v[48:51], v[152:155], v[64:67]\nv_cvt_pk_bf16_f32 v62, v108, v109\nv_mfma_f32_16x16x32_bf16 v[68:71], v[48:51], v[156:159], v[68:71]\n"
"ds_read_b128 v[48:51], v34 offset:8192\nv_cvt_pk_bf16_f32 v63, v110, v111\ns_waitcnt lgkmcnt(3)\nv_mfma_f32_16x16x32_bf16 v[72:75], v[52:55], v[152:155], v[72:75]\n"
"v_mfma_f32_16x16x32_bf16 v[76:79], v[52:55], v[156:159], v[76:79]\nds_read_b128 v[52:55], v35 offset:8192\ns_waitcnt lgkmcnt(3)\nv_mfma_f32_16x16x32_bf16 v[64:67], v[40:43], v[160:163], v[64:67]\n"
"buffer_store_dwordx4 v[60:63], v16, s[20:23], s50 offen sc1\nv_mfma_f32_16x16x32_bf16 v[68:71], v[40:43], v[164:167], v[68:71]\nds_read_b128 v[40:43], v32 offset:12288\ns_add_u32 s50, s50, s36\n"
"s_waitcnt lgkmcnt(3)\nv_mfma_f32_16x16x32_bf16 v[72:75], v[44:47], v[160:163], v[72:75]\ns_add_u32 m0, s52, 0\nv_mfma_f32_16x16x32_bf16 v[76:79], v[44:47], v[164:167], v[76:79]\n"
"ds_read_b128 v[44:47], v33 offset:12288\ns_add_u32 s53, s49, 0\ns_waitcnt lgkmcnt(3)\nv_mfma_f32_16x16x32_bf16 v[64:67], v[48:51], v[168:171], v[64:67]\n"
"buffer_load_dwordx4 v10, s[12:15], s53 offen lds\nv_mfma_f32_16x16x32_bf16 v[68:71], v[48:51], v[172:175], v[68:71]\nds_read_b128 v[48:51], v34 offset:12288\ns_add_u32 m0, s52, 4096\n"
"s_waitcnt lgkmcnt(3)\nv_mfma_f32_16x16x32_bf16 v[72:75], v[52:55], v[168:171], v[72:75]\ns_add_u32 s53, s49, 128\nv_mfma_f32_16x16x32_bf16 v[76:79], v[52:55], v[172:175], v[76:79]\n"
"ds_read_b128 v[52:55], v35 offset:12288\nbuffer_load_dwordx4 v10, s[12:15], s53 offen lds\ns_waitcnt lgkmcnt(3)\nv_mfma_f32_16x16x32_bf16 v[64:67], v[40:43], v[176:179], v[64:67]\n"
"s_add_u32 m0, s52, 8192\nv_mfma_f32_16x16x32_bf16 v[68:71], v[40:43], v[180:183], v[68:71]\nds_read_b128 v[40:43], v32 offset:16384\ns_add_u32 s53, s49, 256\n"
"s_waitcnt lgkmcnt(3)\nv_mfma_f32_16x16x32_bf16 v[72:75], v[44:47], v[176:179], v[72:75]\nbuffer_load_dwordx4 v10, s[12:15], s53 offen lds\nv_mfma_f32_16x16x32_bf16 v[76:79], v[44:47], v[180:183], v[76:79]\n"
"ds_read_b128 v[44:47], v33 offset:16384\ns_add_u32 m0, s52, 12288\ns_waitcnt lgkmcnt(3)\nv_mfma_f32_16x16x32_bf16 v[64:67], v[48:51], v[184:187], v[64:67]\n"
"s_add_u32 s53, s49, 384\nv_mfma_f32_16x16x32_bf16 v[68:71], v[48:51], v[188:191], v[68:71]\nds_read_b128 v[48:51], v34 offset:16384\nbuffer_load_dwordx4 v10, s[12:15], s53 offen lds\n"
"s_waitcnt lgkmcnt(3)\nv_mfma_f32_16x16x32_bf16 v[72:75], v[52:55], v[184:187], v[72:75]\ns_add_u32 m0, s52, 16384\nv_mfma_f32_16x16x32_bf16 v[76:79], v[52:55], v[188:191], v[76:79]\n"
"ds_read_b128 v[52:55], v35 offset:16384\ns_add_u32 s53, s49, 512\ns_waitcnt lgkmcnt(3)\nv_mfma_f32_16x16x32_bf16 v[64:67], v[40:43], v[192:195], v[64:67]\n"
"buffer_load_dwordx4 v10, s[12:15], s53 offen lds\nv_mfma_f32_16x16x32_bf16 v[68:71], v[40:43], v[196:199], v[68:71]\nds_read_b128 v[40:43], v32 offset:20480\ns_add_u32 m0, s52, 20480\n"
"s_waitcnt lgkmcnt(3)\nv_mfma_f32_16x16x32_bf16 v[72:75], v[44:47], v[192:195], v[72:75]\ns_add_u32 s53, s49, 640\nv_mfma_f32_16x16x32_bf16 v[76:79], v[44:47], v[196:199], v[76:79]\n"
"ds_read_b128 v[44:47], v33 offset:20480\nbuffer_load_dwordx4 v10, s[12:15], s53 offen lds\ns_waitcnt lgkmcnt(3)\nv_mfma_f32_16x16x32_bf16 v[64:67], v[48:51], v[200:203], v[64:67]\n"
"s_add_u32 m0, s52, 24576\nv_mfma_f32_16x16x32_bf16 v[68:71], v[48:51], v[204:207], v[68:71]\nds_read_b128 v[48:51], v34 offset:20480\ns_add_u32 s53, s49, 768\n"
"s_waitcnt lgkmcnt(3)\nv_mfma_f32_16x16x32_bf16 v[72:75], v[52:55], v[200:203], v[72:75]\nbuffer_load_dwordx4 v10, s[12:15], s53 offen lds\nv_mfma_f32_16x16x32_bf16 v[76:79], v[52:55], v[204:207], v[76:79]\n"
"ds_read_b128 v[52:55], v35 offset:20480\ns_mov_b32 s53, 6144\nbuffer_load_dwordx4 a[48:51], v14, s[16:19], s53 offen sc1\ns_waitcnt lgkmcnt(3)\n"
"v_mfma_f32_16x16x32_bf16 v[64:67], v[40:43], v[208:211], v[64:67]\ns_mov_b32 s53, 268288\nbuffer_load_dwordx4 a[52:55], v14, s[16:19], s53 offen sc1\nv_mfma_f32_16x16x32_bf16 v[68:71], v[40:43], v[212:215], v[68:71]\n"
"ds_read_b128 v[40:43], v32 offset:24576\ns_mov_b32 s53, 7168\nbuffer_load_dwordx4 a[56:59], v14, s[16:19], s53 offen sc1\ns_waitcnt lgkmcnt(3)\n"
"v_mfma_f32_16x16x32_bf16 v[72:75], v[44:47], v[208:211], v[72:75]\ns_mov_b32 s53, 269312\nbuffer_load_dwordx4 a[60:63], v14, s[16:19], s53 offen sc1\nv_mfma_f32_16x16x32_bf16 v[76:79], v[44:47], v[212:215], v[76:79]\n"
"ds_read_b128 v[44:47], v33 offset:24576\ns_waitcnt lgkmcnt(3)\nv_mfma_f32_16x16x32_bf16 v[64:67], v[48:51], v[216:219], v[64:67]\ns_add_u32 s54, s51, 32768\n"
"s_cmp_eq_u32 s54, 163840\nv_mfma_f32_16x16x32_bf16 v[68:71], v[48:51], v[220:223], v[68:71]\ns_cselect_b32 s54, 0, s54\ns_sub_u32 s54, s54, s51\n"
"ds_read_b128 v[48:51], v34 offset:24576\ns_waitcnt lgkmcnt(3)\nv_mfma_f32_16x16x32_bf16 v[72:75], v[52:55], v[216:219], v[72:75]\nv_mfma_f32_16x16x32_bf16 v[76:79], v[52:55], v[220:223], v[76:79]\n"
"ds_read_b128 v[52:55], v35 offset:24576\ns_waitcnt lgkmcnt(3)\nv_mfma_f32_16x16x32_bf16 v[64:67], v[40:43], v[224:227], v[64:67]\nv_add_u32 v32, s54, v32\n"
"v_mfma_f32_16x16x32_bf16 v[68:71], v[40:43], v[228:231], v[68:71]\nds_read_b128 v[40:43], v32 offset:0\ns_waitcnt lgkmcnt(3)\nv_mfma_f32_16x16x32_bf16 v[72:75], v[44:47], v[224:227], v[72:75]\n"
"v_add_u32 v33, s54, v33\nv_mfma_f32_16x16x32_bf16 v[76:79], v[44:47], v[228:231], v[76:79]\nds_read_b128 v[44:47], v33 offset:0\ns_waitcnt lgkmcnt(3)\n"
"v_mfma_f32_16x16x32_bf16 v[64:67], v[48:51], v[232:235], v[64:67]\nv_add_u32 v34, s54, v34\nv_mfma_f32_16x16x32_bf16 v[68:71], v[48:51], v[236:239], v[68:71]\nds_read_b128 v[48:51], v34 offset:0\n"
"s_waitcnt lgkmcnt(3)\nv_mfma_f32_16x16x32_bf16 v[72:75], v[52:55], v[232:235], v[72:75]\nv_add_u32 v35, s54, v35\nv_mfma_f32_16x16x32_bf16 v[76:79], v[52:55], v[236:239], v[76:79]\n"
"ds_read_b128 v[52:55], v35 offset:0\ns_mov_b32 s52, s47\ns_add_u32 s47, s51, s32\ns_add_u32 s51, s51, s54\n"
"s_add_u32 s49, s49, 28672\ns_waitcnt vmcnt(11)\ns_barrier\ns_waitcnt lgkmcnt(3)\n"
"v_mfma_f32_16x16x32_bf16 v[96:99], v[40:43], v[128:131], 0\nv_mov_b32 v36, s48\ns_add_u32 s48, s48, 1\ns_nop 0\n"
"buffer_store_dword v36, v19, s[24:27], s33 offen sc1\nv_mfma_f32_16x16x32_bf16 v[100:103], v[40:43], v[132:135], 0\nds_read_b128 v[40:43], v32 offset:4096\ns_waitcnt lgkmcnt(3)\n"
"v_mfma_f32_16x16x32_bf16 v[104:107], v[44:47], v[128:131], 0\nv_mfma_f32_16x16x32_bf16 v[108:111], v[44:47], v[132:135], 0\nds_read_b128 v[44:47], v33 offset:4096\nv_cvt_pk_bf16_f32 v56, v64, v65\n"
"s_waitcnt lgkmcnt(3)\nv_mfma_f32_16x16x32_bf16 v[96:99], v[48:51], v[136:139], v[96:99]\nv_cvt_pk_bf16_f32 v57, v66, v67\nv_mfma_f32_16x16x32_bf16 v[100:103], v[48:51], v[140:143], v[100:103]\n"
"ds_read_b128 v[48:51], v34 offset:4096\nv_cvt_pk_bf16_f32 v58, v72, v73\ns_waitcnt lgkmcnt(3)\nv_mfma_f32_16x16x32_bf16 v[104:107], v[52:55], v[136:139], v[104:107]\n"
"v_cvt_pk_bf16_f32 v59, v74, v75\nv_mfma_f32_16x16x32_bf16 v[108:111], v[52:55], v[140:143], v[108:111]\nds_read_b128 v[52:55], v35 offset:4096\ns_waitcnt lgkmcnt(3)\n"
"v_mfma_f32_16x16x32_bf16 v[96:99], v[40:43], v[144:147], v[96:99]\nv_mfma_f32_16x16x32_bf16 v[100:103], v[40:43], v[148:151], v[100:103]\nds_read_b128 v[40:43], v32 offset:8192\nbuffer_store_dwordx4 v[56:59], v15, s[20:23], s50 offen sc1\n"
"s_waitcnt lgkmcnt(3)\nv_mfma_f32_16x16x32_bf16 v[104:107], v[44:47], v[144:147], v[104:107]\nv_cvt_pk_bf16_f32 v60, v68, v69\nv_mfma_f32_16x16x32_bf16 v[108:111], v[44:47], v[148:151], v[108:111]\n"
"ds_read_b128 v[44:47], v33 offset:8192\nv_cvt_pk_bf16_f32 v61, v70, v71\ns_waitcnt lgkmcnt(3)\nv_mfma_f32_16x16x32_bf16 v[96:99], v[48:51], v[152:155], v[96:99]\n"
"v_cvt_pk_bf16_f32 v62, v76, v77\nv_mfma_f32_16x16x32_bf16 v[100:103], v[48:51], v[156:159], v[100:103]\nds_read_b128 v[48:51], v34 offset:8192\nv_cvt_pk_bf16_f32 v63, v78, v79\n"
"s_waitcnt lgkmcnt(3)\nv_mfma_f32_16x16x32_bf16 v[104:107], v[52:55], v[152:155], v[104:107]\nv_mfma_f32_16x16x32_bf16 v[108:111], v[52:55], v[156:159], v[108:111]\nds_read_b128 v[52:55], v35 offset:8192\n"
"s_waitcnt lgkmcnt(3)\nv_mfma_f32_16x16x32_bf16 v[96:99], v[40:43], v[160:163], v[96:99]\nbuffer_store_dwordx4 v[60:63], v16, s[20:23], s50 offen sc1\nv_mfma_f32_16x16x32_bf16 v[100:103], v[40:43], v[164:167], v[100:103]\n"
"ds_read_b128 v[40:43], v32 offset:12288\ns_add_u32 s50, s50, s36\ns_waitcnt lgkmcnt(3)\nv_mfma_f32_16x16x32_bf16 v[104:107], v[44:47], v[160:163], v[104:107]\n"
"s_add_u32 m0, s52, 0\nv_mfma_f32_16x16x32_bf16 v[108:111], v[44:47], v[164:167], v[108:111]\nds_read_b128 v[44:47], v33 offset:12288\ns_add_u32 s53, s49, 0\n"
"s_waitcnt lgkmcnt(3)\nv_mfma_f32_16x16x32_bf16 v[96:99], v[48:51], v[168:171], v[96:99]\nbuffer_load_dwordx4 v10, s[12:15], s53 offen lds\nv_mfma_f32_16x16x32_bf16 v[100:103], v[48:51], v[172:175], v[100:103]\n"
"ds_read_b128 v[48:51], v34 offset:12288\ns_add_u32 m0, s52, 4096\ns_waitcnt lgkmcnt(3)\nv_mfma_f32_16x16x32_bf16 v[104:107], v[52:55], v[168:171], v[104:107]\n"
"s_add_u32 s53, s49, 128\nv_mfma_f32_16x16x32_bf16 v[108:111], v[52:55], v[172:175], v[108:111]\nds_read_b128 v[52:55], v35 offset:12288\nbuffer_load_dwordx4 v10, s[12:15], s53 offen lds\n"
"s_waitcnt lgkmcnt(3)\nv_mfma_f32_16x16x32_bf16 v[96:99], v[40:43], v[176:179], v[96:99]\ns_add_u32 m0, s52, 8192\nv_mfma_f32_16x16x32_bf16 v[100:103], v[40:43], v[180:183], v[100:103]\n"
"ds_read_b128 v[40:43], v32 offset:16384\ns_add_u32 s53, s49, 256\ns_waitcnt lgkmcnt(3)\nv_mfma_f32_16x16x32_bf16 v[104:107], v[44:47], v[176:179], v[104:107]\n"
"buffer_load_dwordx4 v10, s[12:15], s53 offen lds\nv_mfma_f32_16x16x32_bf16 v[108:111], v[44:47], v[180:183], v[108:111]\nds_read_b128 v[44:47], v33 offset:16384\ns_add_u32 m0, s52, 12288\n"
"s_waitcnt lgkmcnt(3)\nv_mfma_f32_16x16x32_bf16 v[96:99], v[48:51], v[184:187], v[96:99]\ns_add_u32 s53, s49, 384\nv_mfma_f32_16x16x32_bf16 v[100:103], v[48:51], v[188:191], v[100:103]\n"
"ds_read_b128 v[48:51], v34 offset:16384\nbuffer_load_dwordx4 v10, s[12:15], s53 offen lds\ns_waitcnt lgkmcnt(3)\nv_mfma_f32_16x16x32_bf16 v[104:107], v[52:55], v[184:187], v[104:107]\n"
"s_add_u32 m0, s52, 16384\nv_mfma_f32_16x16x32_bf16 v[108:111], v[52:55], v[188:191], v[108:111]\nds_read_b128 v[52:55], v35 offset:16384\ns_add_u32 s53, s49, 512\n"
"s_waitcnt lgkmcnt(3)\nv_mfma_f32_16x16x32_bf16 v[96:99], v[40:43], v[192:195], v[96:99]\nbuffer_load_dwordx4 v10, s[12:15], s53 offen lds\nv_mfma_f32_16x16x32_bf16 v[100:103], v[40:43], v[196:199], v[100:103]\n"
"ds_read_b128 v[40:43], v32 offset:20480\ns_add_u32 m0, s52, 20480\ns_waitcnt lgkmcnt(3)\nv_mfma_f32_16x16x32_bf16 v[104:107], v[44:47], v[192:195], v[104:107]\n"
"s_add_u32 s53, s49, 640\nv_mfma_f32_16x16x32_bf16 v[108:111], v[44:47], v[196:199], v[108:111]\nds_read_b128 v[44:47], v33 offset:20480\nbuffer_load_dwordx4 v10, s[12:15], s53 offen lds\n"
"s_waitcnt lgkmcnt(3)\nv_mfma_f32_16x16x32_bf16 v[96:99], v[48:51], v[200:203], v[96:99]\ns_add_u32 m0, s52, 24576\nv_mfma_f32_16x16x32_bf16 v[100:103], v[48:51], v[204:207], v[100:103]\n"
"ds_read_b128 v[48:51], v34 offset:20480\ns_add_u32 s53, s49, 768\ns_waitcnt lgkmcnt(3)\nv_mfma_f32_16x16x32_bf16 v[104:107], v[52:55], v[200:203], v[104:107]\n"
"buffer_load_dwordx4 v10, s[12:15], s53 offen lds\nv_mfma_f32_16x16x32_bf16 v[108:111], v[52:55], v[204:207], v[108:111]\nds_read_b128 v[52:55], v35 offset:20480\ns_mov_b32 s53, 8192\n"
"buffer_load_dwordx4 a[64:67], v14, s[16:19], s53 offen sc1\ns_waitcnt lgkmcnt(3)\nv_mfma_f32_16x16x32_bf16 v[96:99], v[40:43], v[208:211], v[96:99]\ns_mov_b32 s53, 270336\n"
"buffer_load_dwordx4 a[68:71], v14, s[16:19], s53 offen sc1\nv_mfma_f32_16x16x32_bf16 v[100:103], v[40:43], v[212:215], v[100:103]\nds_read_b128 v[40:43], v32 offset:24576\ns_mov_b32 s53, 9216\n"
"buffer_load_dwordx4 a[72:75], v14, s[16:19], s53 offen sc1\ns_waitcnt lgkmcnt(3)\nv_mfma_f32_16x16x32_bf16 v[104:107], v[44:47], v[208:211], v[104:107]\ns_mov_b32 s53, 271360\n"
"buffer_load_dwordx4 a[76:79], v14, s[16:19], s53 offen sc1\nv_mfma_f32_16x16x32_bf16 v[108:111], v[44:47], v[212:215], v[108:111]\nds_read_b128 v[44:47], v33 offset:24576\ns_waitcnt lgkmcnt(3)\n"
"v_mfma_f32_16x16x32_bf16 v[96:99], v[48:51], v[216:219], v[96:99]\ns_add_u32 s54, s51, 32768\ns_cmp_eq_u32 s54, 163840\nv_mfma_f32_16x16x32_bf16 v[100:103], v[48:51], v[220:223], v[100:103]\n"
"s_cselect_b32 s54, 0, s54\ns_sub_u32 s54, s54, s51\nds_read_b128 v[48:51], v34 offset:24576\ns_waitcnt lgkmcnt(3)\n"
"v_mfma_f32_16x16x32_bf16 v[104:107], v[52:55], v[216:219], v[104:107]\nv_mfma_f32_16x16x32_bf16 v[108:111], v[52:55], v[220:223], v[108:111]\nds_read_b128 v[52:55], v35 offset:24576\ns_waitcnt lgkmcnt(3)\n"
"v_mfma_f32_16x16x32_bf16 v[96:99], v[40:43], v[224:227], v[96:99]\nv_add_u32 v32, s54, v32\nv_mfma_f32_16x16x32_bf16 v[100:103], v[40:43], v[228:231], v[100:103]\nds_read_b128 v[40:43], v32 offset:0\n"
"s_waitcnt lgkmcnt(3)\nv_mfma_f32_16x16x32_bf16 v[104:107], v[44:47], v[224:227], v[104:107]\nv_add_u32 v33, s54, v33\nv_mfma_f32_16x16x32_bf16 v[108:111], v[44:47], v[228:231], v[108:111]\n"
"ds_read_b128 v[44:47], v33 offset:0\ns_waitcnt lgkmcnt(3)\nv_mfma_f32_16x16x32_bf16 v[96:99], v[48:51], v[232:235], v[96:99]\nv_add_u32 v34, s54, v34\n"
"v_mfma_f32_16x16x32_bf16 v[100:103], v[48:51], v[236:239], v[100:103]\nds_read_b128 v[48:51], v34 offset:0\ns_waitcnt lgkmcnt(3)\nv_mfma_f32_16x16x32_bf16 v[104:107], v[52:55], v[232:235], v[104:107]\n"
"v_add_u32 v35, s54, v35\nv_mfma_f32_16x16x32_bf16 v[108:111], v[52:55], v[236:239], v[108:111]\nds_read_b128 v[52:55], v35 offset:0\ns_mov_b32 s52, s47\n"
"s_add_u32 s47, s51, s32\ns_add_u32 s51, s51, s54\ns_add_u32 s49, s49, 28672\ns_waitcnt vmcnt(11)\n"
"s_barrier\ns_waitcnt lgkmcnt(3)\nv_mfma_f32_16x16x32_bf16 v[64:67], v[40:43], v[128:131], 0\nv_mov_b32 v36, s48\n"
"s_add_u32 s48, s48, 1\ns_nop 0\nbuffer_store_dword v36, v19, s[24:27], s33 offen sc1\nv_mfma_f32_16x16x32_bf16 v[68:71], v[40:43], v[132:135], 0\n"
"ds_read_b128 v[40:43], v32 offset:4096\ns_waitcnt lgkmcnt(3)\nv_mfma_f32_16x16x32_bf16 v[72:75], v[44:47], v[128:131], 0\nv_mfma_f32_16x16x32_bf16 v[76:79], v[44:47], v[132:135], 0\n"
"ds_read_b128 v[44:47], v33 offset:4096\nv_cvt_pk_bf16_f32 v56, v96, v97\ns_waitcnt lgkmcnt(3)\nv_mfma_f32_16x16x32_bf16 v[64:67], v[48:51], v[136:139], v[64:67]\n"
"v_cvt_pk_bf16_f32 v57, v98, v99\nv_mfma_f32_16x16x32_bf16 v[68:71], v[48:51], v[140:143], v[68:71]\nds_read_b128 v[48:51], v34 offset:4096\nv_cvt_pk_bf16_f32 v58, v104, v105\n"
"s_waitcnt lgkmcnt(3)\nv_mfma_f32_16x16x32_bf16 v[72:75], v[52:55], v[136:139], v[72:75]\nv_cvt_pk_bf16_f32 v59, v106, v107\nv_mfma_f32_16x16x32_bf16 v[76:79], v[52:55], v[140:143], v[76:79]\n"
"ds_read_b128 v[52:55], v35 offset:4096\ns_waitcnt lgkmcnt(3)\nv_mfma_f32_16x16x32_bf16 v[64:67], v[40:43], v[144:147], v[64:67]\nv_mfma_f32_16x16x32_bf16 v[68:71], v[40:43], v[148:151], v[68:71]\n"
"ds_read_b128 v[40:43], v32 offset:8192\nbuffer_store_dwordx4 v[56:59], v15, s[20:23], s50 offen sc1\ns_waitcnt lgkmcnt(3)\nv_mfma_f32_16x16x32_bf16 v[72:75], v[44:47], v[144:147], v[72:75]\n"
"v_cvt_pk_bf16_f32 v60, v100, v101\nv_mfma_f32_16x16x32_bf16 v[76:79], v[44:47], v[148:151], v[76:79]\nds_read_b128 v[44:47], v33 offset:8192\nv_cvt_pk_bf16_f32 v61, v102, v103\n"
"s_waitcnt lgkmcnt(3)\nv_mfma_f32_16x16x32_bf16 v[64:67], v[48:51], v[152:155], v[64:67]\nv_cvt_pk_bf16_f32 v62, v108, v109\nv_mfma_f32_16x16x32_bf16 v[68:71], v[48:51], v[156:159], v[68:71]\n"
"ds_read_b128 v[48:51], v34 offset:8192\nv_cvt_pk_bf16_f32 v63, v110, v111\ns_waitcnt lgkmcnt(3)\nv_mfma_f32_16x16x32_bf16 v[72:75], v[52:55], v[152:155], v[72:75]\n"
"v_mfma_f32_16x16x32_bf16 v[76:79], v[52:55], v[156:159], v[76:79]\nds_read_b128 v[52:55], v35 offset:8192\ns_waitcnt lgkmcnt(3)\nv_mfma_f32_16x16x32_bf16 v[64:67], v[40:43], v[160:163], v[64:67]\n"
"buffer_store_dwordx4 v[60:63], v16, s[20:23], s50 offen sc1\nv_mfma_f32_16x16x32_bf16 v[68:71], v[40:43], v[164:167], v[68:71]\nds_read_b128 v[40:43], v32 offset:12288\ns_add_u32 s50, s50, s36\n"
"s_waitcnt lgkmcnt(3)\nv_mfma_f32_16x16x32_bf16 v[72:75], v[44:47], v[160:163], v[72:75]\ns_add_u32 m0, s52, 0\nv_mfma_f32_16x16x32_bf16 v[76:79], v[44:47], v[164:167], v[76:79]\n"
"ds_read_b128 v[44:47], v33 offset:12288\ns_add_u32 s53, s49, 0\ns_waitcnt lgkmcnt(3)\nv_mfma_f32_16x16x32_bf16 v[64:67], v[48:51], v[168:171], v[64:67]\n"
"buffer_load_dwordx4 v10, s[12:15], s53 offen lds\nv_mfma_f32_16x16x32_bf16 v[68:71], v[48:51], v[172:175], v[68:71]\nds_read_b128 v[48:51], v34 offset:12288\ns_add_u32 m0, s52, 4096\n"
"s_waitcnt lgkmcnt(3)\nv_mfma_f32_16x16x32_bf16 v[72:75], v[52:55], v[168:171], v[72:75]\ns_add_u32 s53, s49, 128\nv_mfma_f32_16x16x32_bf16 v[76:79], v[52:55], v[172:175], v[76:79]\n"
"ds_read_b128 v[52:55], v35 offset:12288\nbuffer_load_dwordx4 v10, s[12:15], s53 offen lds\ns_waitcnt lgkmcnt(3)\nv_mfma_f32_16x16x32_bf16 v[64:67], v[40:43], v[176:179], v[64:67]\n"
"s_add_u32 m0, s52, 8192\nv_mfma_f32_16x16x32_bf16 v[68:71], v[40:43], v[180:183], v[68:71]\nds_read_b128 v[40:43], v32 offset:16384\ns_add_u32 s53, s49, 256\n"
"s_waitcnt lgkmcnt(3)\nv_mfma_f32_16x16x32_bf16 v[72:75], v[44:47], v[176:179], v[72:75]\nbuffer_load_dwordx4 v10, s[12:15], s53 offen lds\nv_mfma_f32_16x16x32_bf16 v[76:79], v[44:47], v[180:183], v[76:79]\n"
"ds_read_b128 v[44:47], v33 offset:16384\ns_add_u32 m0, s52, 12288\ns_waitcnt lgkmcnt(3)\nv_mfma_f32_16x16x32_bf16 v[64:67], v[48:51], v[184:187], v[64:67]\n"
"s_add_u32 s53, s49, 384\nv_mfma_f32_16x16x32_bf16 v[68:71], v[48:51], v[188:191], v[68:71]\nds_read_b128 v[48:51], v34 offset:16384\nbuffer_load_dwordx4 v10, s[12:15], s53 offen lds\n"
"s_waitcnt lgkmcnt(3)\nv_mfma_f32_16x16x32_bf16 v[72:75], v[52:55], v[184:187], v[72:75]\ns_add_u32 m0, s52, 16384\nv_mfma_f32_16x16x32_bf16 v[76:79], v[52:55], v[188:191], v[76:79]\n"
"ds_read_b128 v[52:55], v35 offset:16384\ns_add_u32 s53, s49, 512\ns_waitcnt lgkmcnt(3)\nv_mfma_f32_16x16x32_bf16 v[64:67], v[40:43], v[192:195], v[64:67]\n"
"buffer_load_dwordx4 v10, s[12:15], s53 offen lds\nv_mfma_f32_16x16x32_bf16 v[68:71], v[40:43], v[196:199], v[68:71]\nds_read_b128 v[40:43], v32 offset:20480\ns_add_u32 m0, s52, 20480\n"
"s_waitcnt lgkmcnt(3)\nv_mfma_f32_16x16x32_bf16 v[72:75], v[44:47], v[192:195], v[72:75]\ns_add_u32 s53, s49, 640\nv_mfma_f32_16x16x32_bf16 v[76:79], v[44:47], v[196:199], v[76:79]\n"
"ds_read_b128 v[44:47], v33 offset:20480\nbuffer_load_dwordx4 v10, s[12:15], s53 offen lds\ns_waitcnt lgkmcnt(3)\nv_mfma_f32_16x16x32_bf16 v[64:67], v[48:51], v[200:203], v[64:67]\n"
"s_add_u32 m0, s52, 24576\nv_mfma_f32_16x16x32_bf16 v[68:71], v[48:51], v[204:207], v[68:71]\nds_read_b128 v[48:51], v34 offset:20480\ns_add_u32 s53, s49, 768\n"
"s_waitcnt lgkmcnt(3)\nv_mfma_f32_16x16x32_bf16 v[72:75], v[52:55], v[200:203], v[72:75]\nbuffer_load_dwordx4 v10, s[12:15], s53 offen lds\nv_mfma_f32_16x16x32_bf16 v[76:79], v[52:55], v[204:207], v[76:79]\n"
"ds_read_b128 v[52:55], v35 offset:20480\ns_mov_b32 s53, 10240\nbuffer_load_dwordx4 a[80:83], v14, s[16:19], s53 offen sc1\ns_waitcnt lgkmcnt(3)\n"
"v_mfma_f32_16x16x32_bf16 v[64:67], v[40:43], v[208:211], v[64:67]\ns_mov_b32 s53, 272384\nbuffer_load_dwordx4 a[84:87], v14, s[16:19], s53 offen sc1\nv_mfma_f32_16x16x32_bf16 v[68:71], v[40:43], v[212:215], v[68:71]\n"
"ds_read_b128 v[40:43], v32 offset:24576\ns_mov_b32 s53, 11264\nbuffer_load_dwordx4 a[88:91], v14, s[16:19], s53 offen sc1\ns_waitcnt lgkmcnt(3)\n"
"v_mfma_f32_16x16x32_bf16 v[72:75], v[44:47], v[208:211], v[72:75]\ns_mov_b32 s53, 273408\nbuffer_load_dwordx4 a[92:95], v14, s[16:19], s53 offen sc1\nv_mfma_f32_16x16x32_bf16 v[76:79], v[44:47], v[212:215], v[76:79]\n"
"ds_read_b128 v[44:47], v33 offset:24576\ns_waitcnt lgkmcnt(3)\nv_mfma_f32_16x16x32_bf16 v[64:67], v[48:51], v[216:219], v[64:67]\ns_add_u32 s54, s51, 32768\n"
"s_cmp_eq_u32 s54, 163840\nv_mfma_f32_16x16x32_bf16 v[68:71], v[48:51], v[220:223], v[68:71]\ns_cselect_b32 s54, 0, s54\ns_sub_u32 s54, s54, s51\n"
"ds_read_b128 v[48:51], v34 offset:24576\ns_waitcnt lgkmcnt(3)\nv_mfma_f32_16x16x32_bf16 v[72:75], v[52:55], v[216:219], v[72:75]\nv_mfma_f32_16x16x32_bf16 v[76:79], v[52:55], v[220:223], v[76:79]\n"
"ds_read_b128 v[52:55], v35 offset:24576\ns_waitcnt lgkmcnt(3)\nv_mfma_f32_16x16x32_bf16 v[64:67], v[40:43], v[224:227], v[64:67]\nv_add_u32 v32, s54, v32\n"
"v_mfma_f32_16x16x32_bf16 v[68:71], v[40:43], v[228:231], v[68:71]\nds_read_b128 v[40:43], v32 offset:0\ns_waitcnt lgkmcnt(3)\nv_mfma_f32_16x16x32_bf16 v[72:75], v[44:47], v[224:227], v[72:75]\n"
"v_add_u32 v33, s54, v33\nv_mfma_f32_16x16x32_bf16 v[76:79], v[44:47], v[228:231], v[76:79]\nds_read_b128 v[44:47], v33 offset:0\ns_waitcnt lgkmcnt(3)\n"
"v_mfma_f32_16x16x32_bf16 v[64:67], v[48:51], v[232:235], v[64:67]\nv_add_u32 v34, s54, v34\nv_mfma_f32_16x16x32_bf16 v[68:71], v[48:51], v[236:239], v[68:71]\nds_read_b128 v[48:51], v34 offset:0\n"
"s_waitcnt lgkmcnt(3)\nv_mfma_f32_16x16x32_bf16 v[72:75], v[52:55], v[232:235], v[72:75]\nv_add_u32 v35, s54, v35\nv_mfma_f32_16x16x32_bf16 v[76:79], v[52:55], v[236:239], v[76:79]\n"
"ds_read_b128 v[52:55], v35 offset:0\ns_mov_b32 s52, s47\ns_add_u32 s47, s51, s32\ns_add_u32 s51, s51, s54\n"
"s_add_u32 s49, s49, 28672\ns_waitcnt vmcnt(11)\ns_barrier\ns_waitcnt lgkmcnt(3)\n"
"v_mfma_f32_16x16x32_bf16 v[96:99], v[40:43], v[128:131], 0\nv_mov_b32 v36, s48\ns_add_u32 s48, s48, 1\ns_nop 0\n"
"buffer_store_dword v36, v19, s[24:27], s33 offen sc1\nv_mfma_f32_16x16x32_bf16 v[100:103], v[40:43], v[132:135], 0\nds_read_b128 v[40:43], v32 offset:4096\ns_waitcnt lgkmcnt(3)\n"
"v_mfma_f32_16x16x32_bf16 v[104:107], v[44:47], v[128:131], 0\nv_mfma_f32_16x16x32_bf16 v[108:111], v[44:47], v[132:135], 0\nds_read_b128 v[44:47], v33 offset:4096\nv_cvt_pk_bf16_f32 v56, v64, v65\n"
"s_waitcnt lgkmcnt(3)\nv_mfma_f32_16x16x32_bf16 v[96:99], v[48:51], v[136:139], v[96:99]\nv_cvt_pk_bf16_f32 v57, v66, v67\nv_mfma_f32_16x16x32_bf16 v[100:103], v[48:51], v[140:143], v[100:103]\n"
"ds_read_b128 v[48:51], v34 offset:4096\nv_cvt_pk_bf16_f32 v58, v72, v73\ns_waitcnt lgkmcnt(3)\nv_mfma_f32_16x16x32_bf16 v[104:107], v[52:55], v[136:139], v[104:107]\n"
"v_cvt_pk_bf16_f32 v59, v74, v75\nv_mfma_f32_16x16x32_bf16 v[108:111], v[52:55], v[140:143], v[108:111]\nds_read_b128 v[52:55], v35 offset:4096\ns_waitcnt lgkmcnt(3)\n"
"v_mfma_f32_16x16x32_bf16 v[96:99], v[40:43], v[144:147], v[96:99]\nv_mfma_f32_16x16x32_bf16 v[100:103], v[40:43], v[148:151], v[100:103]\nds_read_b128 v[40:43], v32 offset:8192\nbuffer_store_dwordx4 v[56:59], v15, s[20:23], s50 offen sc1\n"
"s_waitcnt lgkmcnt(3)\nv_mfma_f32_16x16x32_bf16 v[104:107], v[44:47], v[144:147], v[104:107]\nv_cvt_pk_bf16_f32 v60, v68, v69\nv_mfma_f32_16x16x32_bf16 v[108:111], v[44:47], v[148:151], v[108:111]\n"
"ds_read_b128 v[44:47], v33 offset:8192\nv_cvt_pk_bf16_f32 v61, v70, v71\ns_waitcnt lgkmcnt(3)\nv_mfma_f32_16x16x32_bf16 v[96:99], v[48:51], v[152:155], v[96:99]\n"
"v_cvt_pk_bf16_f32 v62, v76, v77\nv_mfma_f32_16x16x32_bf16 v[100:103], v[48:51], v[156:159], v[100:103]\nds_read_b128 v[48:51], v34 offset:8192\nv_cvt_pk_bf16_f32 v63, v78, v79\n"
"s_waitcnt lgkmcnt(3)\nv_mfma_f32_16x16x32_bf16 v[104:107], v[52:55], v[152:155], v[104:107]\nv_mfma_f32_16x16x32_bf16 v[108:111], v[52:55], v[156:159], v[108:111]\nds_read_b128 v[52:55], v35 offset:8192\n"
"s_waitcnt lgkmcnt(3)\nv_mfma_f32_16x16x32_bf16 v[96:99], v[40:43], v[160:163], v[96:99]\nbuffer_store_dwordx4 v[60:63], v16, s[20:23], s50 offen sc1\nv_mfma_f32_16x16x32_bf16 v[100:103], v[40:43], v[164:167], v[100:103]\n"
"ds_read_b128 v[40:43], v32 offset:12288\ns_add_u32 s50, s50, s36\ns_waitcnt lgkmcnt(3)\nv_mfma_f32_16x16x32_bf16 v[104:107], v[44:47], v[160:163], v[104:107]\n"
"s_add_u32 m0, s52, 0\nv_mfma_f32_16x16x32_bf16 v[108:111], v[44:47], v[164:167], v[108:111]\nds_read_b128 v[44:47], v33 offset:12288\ns_add_u32 s53, s49, 0\n"
"s_waitcnt lgkmcnt(3)\nv_mfma_f32_16x16x32_bf16 v[96:99], v[48:51], v[168:171], v[96:99]\nbuffer_load_dwordx4 v10, s[12:15], s53 offen lds\nv_mfma_f32_16x16x32_bf16 v[100:103], v[48:51], v[172:175], v[100:103]\n"
"ds_read_b128 v[48:51], v34 offset:12288\ns_add_u32 m0, s52, 4096\ns_waitcnt lgkmcnt(3)\nv_mfma_f32_16x16x32_bf16 v[104:107], v[52:55], v[168:171], v[104:107]\n"
"s_add_u32 s53, s49, 128\nv_mfma_f32_16x16x32_bf16 v[108:111], v[52:55], v[172:175], v[108:111]\nds_read_b128 v[52:55], v35 offset:12288\nbuffer_load_dwordx4 v10, s[12:15], s53 offen lds\n"
"s_waitcnt lgkmcnt(3)\nv_mfma_f32_16x16x32_bf16 v[96:99], v[40:43], v[176:179], v[96:99]\ns_add_u32 m0, s52, 8192\nv_mfma_f32_16x16x32_bf16 v[100:103], v[40:43], v[180:183], v[100:103]\n"
"ds_read_b128 v[40:43], v32 offset:16384\ns_add_u32 s53, s49, 256\ns_waitcnt lgkmcnt(3)\nv_mfma_f32_16x16x32_bf16 v[104:107], v[44:47], v[176:179], v[104:107]\n"
"buffer_load_dwordx4 v10, s[12:15], s53 offen lds\nv_mfma_f32_16x16x32_bf16 v[108:111], v[44:47], v[180:183], v[108:111]\nds_read_b128 v[44:47], v33 offset:16384\ns_add_u32 m0, s52, 12288\n"
"s_waitcnt lgkmcnt(3)\nv_mfma_f32_16x16x32_bf16 v[96:99], v[48:51], v[184:187], v[96:99]\ns_add_u32 s53, s49, 384\nv_mfma_f32_16x16x32_bf16 v[100:103], v[48:51], v[188:191], v[100:103]\n"
"ds_read_b128 v[48:51], v34 offset:16384\nbuffer_load_dwordx4 v10, s[12:15], s53 offen lds\ns_waitcnt lgkmcnt(3)\nv_mfma_f32_16x16x32_bf16 v[104:107], v[52:55], v[184:187], v[104:107]\n"
"s_add_u32 m0, s52, 16384\nv_mfma_f32_16x16x32_bf16 v[108:111], v[52:55], v[188:191], v[108:111]\nds_read_b128 v[52:55], v35 offset:16384\ns_add_u32 s53, s49, 512\n"
"s_waitcnt lgkmcnt(3)\nv_mfma_f32_16x16x32_bf16 v[96:99], v[40:43], v[192:195], v[96:99]\nbuffer_load_dwordx4 v10, s[12:15], s53 offen lds\nv_mfma_f32_16x16x32_bf16 v[100:103], v[40:43], v[196:199], v[100:103]\n"
"ds_read_b128 v[40:43], v32 offset:20480\ns_add_u32 m0, s52, 20480\ns_waitcnt lgkmcnt(3)\nv_mfma_f32_16x16x32_bf16 v[104:107], v[44:47], v[192:195], v[104:107]\n"
"s_add_u32 s53, s49, 640\nv_mfma_f32_16x16x32_bf16 v[108:111], v[44:47], v[196:199], v[108:111]\nds_read_b128 v[44:47], v33 offset:20480\nbuffer_load_dwordx4 v10, s[12:15], s53 offen lds\n"
"s_waitcnt lgkmcnt(3)\nv_mfma_f32_16x16x32_bf16 v[96:99], v[48:51], v[200:203], v[96:99]\ns_add_u32 m0, s52, 24576\nv_mfma_f32_16x16x32_bf16 v[100:103], v[48:51], v[204:207], v[100:103]\n"
"ds_read_b128 v[48:51], v34 offset:20480\ns_add_u32 s53, s49, 768\ns_waitcnt lgkmcnt(3)\nv_mfma_f32_16x16x32_bf16 v[104:107], v[52:55], v[200:203], v[104:107]\n"
"buffer_load_dwordx4 v10, s[12:15], s53 offen lds\nv_mfma_f32_16x16x32_bf16 v[108:111], v[52:55], v[204:207], v[108:111]\nds_read_b128 v[52:55], v35 offset:20480\ns_mov_b32 s53, 12288\n"
"buffer_load_dwordx4 a[96:99], v14, s[16:19], s53 offen sc1\ns_waitcnt lgkmcnt(3)\nv_mfma_f32_16x16x32_bf16 v[96:99], v[40:43], v[208:211], v[96:99]\ns_mov_b32 s53, 274432\n"
"buffer_load_dwordx4 a[100:103], v14, s[16:19], s53 offen sc1\nv_mfma_f32_16x16x32_bf16 v[100:103], v[40:43], v[212:215], v[100:103]\nds_read_b128 v[40:43], v32 offset:24576\ns_mov_b32 s53, 13312\n"
"buffer_load_dwordx4 a[104:107], v14, s[16:19], s53 offen sc1\ns_waitcnt lgkmcnt(3)\nv_mfma_f32_16x16x32_bf16 v[104:107], v[44:47], v[208:211], v[104:107]\ns_mov_b32 s53, 275456\n"
"buffer_load_dwordx4 a[108:111], v14, s[16:19], s53 offen sc1\nv_mfma_f32_16x16x32_bf16 v[108:111], v[44:47], v[212:215], v[108:111]\nds_read_b128 v[44:47], v33 offset:24576\ns_waitcnt lgkmcnt(3)\n"
"v_mfma_f32_16x16x32_bf16 v[96:99], v[48:51], v[216:219], v[96:99]\ns_add_u32 s54, s51, 32768\ns_cmp_eq_u32 s54, 163840\nv_mfma_f32_16x16x32_bf16 v[100:103], v[48:51], v[220:223], v[100:103]\n"
"s_cselect_b32 s54, 0, s54\ns_sub_u32 s54, s54, s51\nds_read_b128 v[48:51], v34 offset:24576\ns_waitcnt lgkmcnt(3)\n"
"v_mfma_f32_16x16x32_bf16 v[104:107], v[52:55], v[216:219], v[104:107]\nv_mfma_f32_16x16x32_bf16 v[108:111], v[52:55], v[220:223], v[108:111]\nds_read_b128 v[52:55], v35 offset:24576\ns_waitcnt lgkmcnt(3)\n"
"v_mfma_f32_16x16x32_bf16 v[96:99], v[40:43], v[224:227], v[96:99]\nv_add_u32 v32, s54, v32\nv_mfma_f32_16x16x32_bf16 v[100:103], v[40:43], v[228:231], v[100:103]\nds_read_b128 v[40:43], v32 offset:0\n"
"s_waitcnt lgkmcnt(3)\nv_mfma_f32_16x16x32_bf16 v[104:107], v[44:47], v[224:227], v[104:107]\nv_add_u32 v33, s54, v33\nv_mfma_f32_16x16x32_bf16 v[108:111], v[44:47], v[228:231], v[108:111]\n"
"ds_read_b128 v[44:47], v33 offset:0\ns_waitcnt lgkmcnt(3)\nv_mfma_f32_16x16x32_bf16 v[96:99], v[48:51], v[232:235], v[96:99]\nv_add_u32 v34, s54, v34\n"
"v_mfma_f32_16x16x32_bf16 v[100:103], v[48:51], v[236:239], v[100:103]\nds_read_b128 v[48:51], v34 offset:0\ns_waitcnt lgkmcnt(3)\nv_mfma_f32_16x16x32_bf16 v[104:107], v[52:55], v[232:235], v[104:107]\n"
"v_add_u32 v35, s54, v35\nv_mfma_f32_16x16x32_bf16 v[108:111], v[52:55], v[236:239], v[108:111]\nds_read_b128 v[52:55], v35 offset:0\ns_mov_b32 s52, s47\n"
"s_add_u32 s47, s51, s32\ns_add_u32 s51, s51, s54\ns_add_u32 s49, s49, 28672\ns_cmp_lg_u32 s34, 0\n"
"s_cbranch_scc1 fg1_stay\ns_nop 7\nv_cvt_pk_bf16_f32 v56, v96, v97\nv_cvt_pk_bf16_f32 v57, v98, v99\n"
"v_cvt_pk_bf16_f32 v58, v104, v105\nv_cvt_pk_bf16_f32 v59, v106, v107\ns_nop 0\ns_nop 0\n"
"buffer_store_dwordx4 v[56:59], v15, s[20:23], s50 offen sc1\nv_cvt_pk_bf16_f32 v60, v100, v101\nv_cvt_pk_bf16_f32 v61, v102, v103\nv_cvt_pk_bf16_f32 v62, v108, v109\n"
"v_cvt_pk_bf16_f32 v63, v110, v111\ns_nop 0\ns_nop 0\nbuffer_store_dwordx4 v[60:63], v16, s[20:23], s50 offen sc1\n"
"s_add_u32 s48, s28, 64\nv_mov_b32 v36, s48\ns_waitcnt vmcnt(0) lgkmcnt(0)\ns_barrier\n"
"buffer_store_dword v36, v19, s[24:27], s33 offen sc1\ns_branch fg1_end\nfg1_stay:\ns_waitcnt vmcnt(0)\n"
"s_barrier\ns_waitcnt lgkmcnt(3)\nv_mfma_f32_16x16x32_bf16 v[64:67], v[40:43], v[128:131], 0\nv_mov_b32 v36, s48\n"
"s_add_u32 s48, s48, 1\ns_nop 0\nbuffer_store_dword v36, v19, s[24:27], s33 offen sc1\nv_mfma_f32_16x16x32_bf16 v[68:71], v[40:43], v[132:135], 0\n"
"v_mfma_f32_16x16x32_bf16 v[72:75], v[40:43], a[0:3], 0\nv_mfma_f32_16x16x32_bf16 v[76:79], v[40:43], a[4:7], 0\nds_read_b128 v[40:43], v32 offset:4096\nv_cvt_pk_bf16_f32 v56, v96, v97\n"
"s_waitcnt lgkmcnt(3)\nv_mfma_f32_16x16x32_bf16 v[80:83], v[44:47], v[128:131], 0\nv_cvt_pk_bf16_f32 v57, v98, v99\nv_mfma_f32_16x16x32_bf16 v[84:87], v[44:47], v[132:135], 0\n"
"v_cvt_pk_bf16_f32 v58, v104, v105\nv_mfma_f32_16x16x32_bf16 v[88:91], v[44:47], a[0:3], 0\nv_cvt_pk_bf16_f32 v59, v106, v107\nv_mfma_f32_16x16x32_bf16 v[92:95], v[44:47], a[4:7], 0\n"
"ds_read_b128 v[44:47], v33 offset:4096\ns_waitcnt lgkmcnt(3)\nv_mfma_f32_16x16x32_bf16 v[64:67], v[48:51], v[136:139], v[64:67]\nv_mfma_f32_16x16x32_bf16 v[68:71], v[48:51], v[140:143], v[68:71]\n"
"buffer_store_dwordx4 v[56:59], v15, s[20:23], s50 offen sc1\nv_mfma_f32_16x16x32_bf16 v[72:75], v[48:51], a[8:11], v[72:75]\nv_cvt_pk_bf16_f32 v60, v100, v101\nv_mfma_f32_16x16x32_bf16 v[76:79], v[48:51], a[12:15], v[76:79]\n"
"ds_read_b128 v[48:51], v34 offset:4096\nv_cvt_pk_bf16_f32 v61, v102, v103\ns_waitcnt lgkmcnt(3)\nv_mfma_f32_16x16x32_bf16 v[80:83], v[52:55], v[136:139], v[80:83]\n"
"v_cvt_pk_bf16_f32 v62, v108, v109\nv_mfma_f32_16x16x32_bf16 v[84:87], v[52:55], v[140:143], v[84:87]\nv_cvt_pk_bf16_f32 v63, v110, v111\nv_mfma_f32_16x16x32_bf16 v[88:91], v[52:55], a[8:11], v[88:91]\n"
"v_mfma_f32_16x16x32_bf16 v[92:95], v[52:55], a[12:15], v[92:95]\nds_read_b128 v[52:55], v35 offset:4096\ns_waitcnt lgkmcnt(3)\nv_mfma_f32_16x16x32_bf16 v[64:67], v[40:43], v[144:147], v[64:67]\n"
"buffer_store_dwordx4 v[60:63], v16, s[20:23], s50 offen sc1\nv_mfma_f32_16x16x32_bf16 v[68:71], v[40:43], v[148:151], v[68:71]\ns_add_u32 s50, s50, s36\nv_mfma_f32_16x16x32_bf16 v[72:75], v[40:43], a[16:19], v[72:75]\n"
"s_add_u32 m0, s52, 0\nv_mfma_f32_16x16x32_bf16 v[76:79], v[40:43], a[20:23], v[76:79]\nds_read_b128 v[40:43], v32 offset:8192\ns_add_u32 s53, s49, 0\n"
"s_waitcnt lgkmcnt(3)\nv_mfma_f32_16x16x32_bf16 v[80:83], v[44:47], v[144:147], v[80:83]\nbuffer_load_dwordx4 v10, s[12:15], s53 offen lds\nv_mfma_f32_16x16x32_bf16 v[84:87], v[44:47], v[148:151], v[84:87]\n"
"v_mfma_f32_16x16x32_bf16 v[88:91], v[44:47], a[16:19], v[88:91]\nv_mfma_f32_16x16x32_bf16 v[92:95], v[44:47], a[20:23], v[92:95]\nds_read_b128 v[44:47], v33 offset:8192\ns_waitcnt lgkmcnt(3)\n"
"v_mfma_f32_16x16x32_bf16 v[64:67], v[48:51], v[152:155], v[64:67]\nv_mfma_f32_16x16x32_bf16 v[68:71], v[48:51], v[156:159], v[68:71]\nv_mfma_f32_16x16x32_bf16 v[72:75], v[48:51], a[24:27], v[72:75]\nv_mfma_f32_16x16x32_bf16 v[76:79], v[48:51], a[28:31], v[76:79]\n"
"ds_read_b128 v[48:51], v34 offset:8192\ns_waitcnt lgkmcnt(3)\nv_mfma_f32_16x16x32_bf16 v[80:83], v[52:55], v[152:155], v[80:83]\nv_mfma_f32_16x16x32_bf16 v[84:87], v[52:55], v[156:159], v[84:87]\n"
"s_add_u32 m0, s52, 4096\nv_mfma_f32_16x16x32_bf16 v[88:91], v[52:55], a[24:27], v[88:91]\ns_add_u32 s53, s49, 128\nv_mfma_f32_16x16x32_bf16 v[92:95], v[52:55], a[28:31], v[92:95]\n"
"ds_read_b128 v[52:55], v35 offset:8192\nbuffer_load_dwordx4 v10, s[12:15], s53 offen lds\ns_waitcnt lgkmcnt(3)\nv_mfma_f32_16x16x32_bf16 v[64:67], v[40:43], v[160:163], v[64:67]\n"
"v_mfma_f32_16x16x32_bf16 v[68:71], v[40:43], v[164:167], v[68:71]\nv_mfma_f32_16x16x32_bf16 v[72:75], v[40:43], a[32:35], v[72:75]\nv_mfma_f32_16x16x32_bf16 v[76:79], v[40:43], a[36:39], v[76:79]\nds_read_b128 v[40:43], v32 offset:12288\n"
"s_waitcnt lgkmcnt(3)\nv_mfma_f32_16x16x32_bf16 v[80:83], v[44:47], v[160:163], v[80:83]\nv_mfma_f32_16x16x32_bf16 v[84:87], v[44:47], v[164:167], v[84:87]\nv_mfma_f32_16x16x32_bf16 v[88:91], v[44:47], a[32:35], v[88:91]\n"
"v_mfma_f32_16x16x32_bf16 v[92:95], v[44:47], a[36:39], v[92:95]\nds_read_b128 v[44:47], v33 offset:12288\ns_waitcnt lgkmcnt(3)\nv_mfma_f32_16x16x32_bf16 v[64:67], v[48:51], v[168:171], v[64:67]\n"
"s_add_u32 m0, s52, 8192\nv_mfma_f32_16x16x32_bf16 v[68:71], v[48:51], v[172:175], v[68:71]\ns_add_u32 s53, s49, 256\nv_mfma_f32_16x16x32_bf16 v[72:75], v[48:51], a[40:43], v[72:75]\n"
"buffer_load_dwordx4 v10, s[12:15], s53 offen lds\nv_mfma_f32_16x16x32_bf16 v[76:79], v[48:51], a[44:47], v[76:79]\nds_read_b128 v[48:51], v34 offset:12288\ns_waitcnt lgkmcnt(3)\n"
"v_mfma_f32_16x16x32_bf16 v[80:83], v[52:55], v[168:171], v[80:83]\nv_mfma_f32_16x16x32_bf16 v[84:87], v[52:55], v[172:175], v[84:87]\nv_mfma_f32_16x16x32_bf16 v[88:91], v[52:55], a[40:43], v[88:91]\nv_mfma_f32_16x16x32_bf16 v[92:95], v[52:55], a[44:47], v[92:95]\n"
"ds_read_b128 v[52:55], v35 offset:12288\ns_waitcnt lgkmcnt(3)\nv_mfma_f32_16x16x32_bf16 v[64:67], v[40:43], v[176:179], v[64:67]\nv_mfma_f32_16x16x32_bf16 v[68:71], v[40:43], v[180:183], v[68:71]\n"
"v_mfma_f32_16x16x32_bf16 v[72:75], v[40:43], a[48:51], v[72:75]\nv_mfma_f32_16x16x32_bf16 v[76:79], v[40:43], a[52:55], v[76:79]\nds_read_b128 v[40:43], v32 offset:16384\ns_add_u32 m0, s52, 12288\n"
"s_waitcnt lgkmcnt(3)\nv_mfma_f32_16x16x32_bf16 v[80:83], v[44:47], v[176:179], v[80:83]\ns_add_u32 s53, s49, 384\nv_mfma_f32_16x16x32_bf16 v[84:87], v[44:47], v[180:183], v[84:87]\n"
"buffer_load_dwordx4 v10, s[12:15], s53 offen lds\nv_mfma_f32_16x16x32_bf16 v[88:91], v[44:47], a[48:51], v[88:91]\nv_mfma_f32_16x16x32_bf16 v[92:95], v[44:47], a[52:55], v[92:95]\nds_read_b128 v[44:47], v33 offset:16384\n"
"s_waitcnt lgkmcnt(3)\nv_mfma_f32_16x16x32_bf16 v[64:67], v[48:51], v[184:187], v[64:67]\nv_mfma_f32_16x16x32_bf16 v[68:71], v[48:51], v[188:191], v[68:71]\nv_mfma_f32_16x16x32_bf16 v[72:75], v[48:51], a[56:59], v[72:75]\n"
"v_mfma_f32_16x16x32_bf16 v[76:79], v[48:51], a[60:63], v[76:79]\nds_read_b128 v[48:51], v34 offset:16384\ns_waitcnt lgkmcnt(3)\nv_mfma_f32_16x16x32_bf16 v[80:83], v[52:55], v[184:187], v[80:83]\n"
"v_mfma_f32_16x16x32_bf16 v[84:87], v[52:55], v[188:191], v[84:87]\nv_mfma_f32_16x16x32_bf16 v[88:91], v[52:55], a[56:59], v[88:91]\ns_add_u32 m0, s52, 16384\nv_mfma_f32_16x16x32_bf16 v[92:95], v[52:55], a[60:63], v[92:95]\n"
"ds_read_b128 v[52:55], v35 offset:16384\ns_add_u32 s53, s49, 512\ns_waitcnt lgkmcnt(3)\nv_mfma_f32_16x16x32_bf16 v[64:67], v[40:43], v[192:195], v[64:67]\n"
"buffer_load_dwordx4 v10, s[12:15], s53 offen lds\nv_mfma_f32_16x16x32_bf16 v[68:71], v[40:43], v[196:199], v[68:71]\nv_mfma_f32_16x16x32_bf16 v[72:75], v[40:43], a[64:67], v[72:75]\nv_mfma_f32_16x16x32_bf16 v[76:79], v[40:43], a[68:71], v[76:79]\n"
"ds_read_b128 v[40:43], v32 offset:20480\ns_waitcnt lgkmcnt(3)\nv_mfma_f32_16x16x32_bf16 v[80:83], v[44:47], v[192:195], v[80:83]\nv_mfma_f32_16x16x32_bf16 v[84:87], v[44:47], v[196:199], v[84:87]\n"
"v_mfma_f32_16x16x32_bf16 v[88:91], v[44:47], a[64:67], v[88:91]\nv_mfma_f32_16x16x32_bf16 v[92:95], v[44:47], a[68:71], v[92:95]\nds_read_b128 v[44:47], v33 offset:20480\ns_waitcnt lgkmcnt(3)\n"
"v_mfma_f32_16x16x32_bf16 v[64:67], v[48:51], v[200:203], v[64:67]\nv_mfma_f32_16x16x32_bf16 v[68:71], v[48:51], v[204:207], v[68:71]\ns_add_u32 m0, s52, 20480\nv_mfma_f32_16x16x32_bf16 v[72:75], v[48:51], a[72:75], v[72:75]\n"
"s_add_u32 s53, s49, 640\nv_mfma_f32_16x16x32_bf16 v[76:79], v[48:51], a[76:79], v[76:79]\nds_read_b128 v[48:51], v34 offset:20480\nbuffer_load_dwordx4 v10, s[12:15], s53 offen lds\n"
"s_waitcnt lgkmcnt(3)\nv_mfma_f32_16x16x32_bf16 v[80:83], v[52:55], v[200:203], v[80:83]\nv_mfma_f32_16x16x32_bf16 v[84:87], v[52:55], v[204:207], v[84:87]\nv_mfma_f32_16x16x32_bf16 v[88:91], v[52:55], a[72:75], v[88:91]\n"
"v_mfma_f32_16x16x32_bf16 v[92:95], v[52:55], a[76:79], v[92:95]\nds_read_b128 v[52:55], v35 offset:20480\ns_waitcnt lgkmcnt(3)\nv_mfma_f32_16x16x32_bf16 v[64:67], v[40:43], v[208:211], v[64:67]\n"
"v_mfma_f32_16x16x32_bf16 v[68:71], v[40:43], v[212:215], v[68:71]\nv_mfma_f32_16x16x32_bf16 v[72:75], v[40:43], a[80:83], v[72:75]\nv_mfma_f32_16x16x32_bf16 v[76:79], v[40:43], a[84:87], v[76:79]\nds_read_b128 v[40:43], v32 offset:24576\n"
"s_waitcnt lgkmcnt(3)\nv_mfma_f32_16x16x32_bf16 v[80:83], v[44:47], v[208:211], v[80:83]\ns_add_u32 m0, s52, 24576\nv_mfma_f32_16x16x32_bf16 v[84:87], v[44:47], v[212:215], v[84:87]\n"
"s_add_u32 s53, s49, 768\nv_mfma_f32_16x16x32_bf16 v[88:91], v[44:47], a[80:83], v[88:91]\nbuffer_load_dwordx4 v10, s[12:15], s53 offen lds\nv_mfma_f32_16x16x32_bf16 v[92:95], v[44:47], a[84:87], v[92:95]\n"
"ds_read_b128 v[44:47], v33 offset:24576\ns_waitcnt lgkmcnt(3)\nv_mfma_f32_16x16x32_bf16 v[64:67], v[48:51], v[216:219], v[64:67]\ns_add_u32 s54, s51, 32768\n"
"s_cmp_eq_u32 s54, 163840\nv_mfma_f32_16x16x32_bf16 v[68:71], v[48:51], v[220:223], v[68:71]\nv_mfma_f32_16x16x32_bf16 v[72:75], v[48:51], a[88:91], v[72:75]\nv_mfma_f32_16x16x32_bf16 v[76:79], v[48:51], a[92:95], v[76:79]\n"
"s_cselect_b32 s54, 0, s54\ns_sub_u32 s54, s54, s51\nds_read_b128 v[48:51], v34 offset:24576\ns_waitcnt lgkmcnt(3)\n"
"v_mfma_f32_16x16x32_bf16 v[80:83], v[52:55], v[216:219], v[80:83]\nv_mfma_f32_16x16x32_bf16 v[84:87], v[52:55], v[220:223], v[84:87]\nv_mfma_f32_16x16x32_bf16 v[88:91], v[52:55], a[88:91], v[88:91]\nv_mfma_f32_16x16x32_bf16 v[92:95], v[52:55], a[92:95], v[92:95]\n"
"ds_read_b128 v[52:55], v35 offset:24576\ns_waitcnt lgkmcnt(3)\nv_mfma_f32_16x16x32_bf16 v[64:67], v[40:43], v[224:227], v[64:67]\nv_add_u32 v32, s54, v32\n"
"v_mfma_f32_16x16x32_bf16 v[68:71], v[40:43], v[228:231], v[68:71]\nv_mfma_f32_16x16x32_bf16 v[72:75], v[40:43], a[96:99], v[72:75]\nv_mfma_f32_16x16x32_bf16 v[76:79], v[40:43], a[100:103], v[76:79]\nds_read_b128 v[40:43], v32 offset:0\n"
"s_waitcnt lgkmcnt(3)\nv_mfma_f32_16x16x32_bf16 v[80:83], v[44:47], v[224:227], v[80:83]\nv_add_u32 v33, s54, v33\nv_mfma_f32_16x16x32_bf16 v[84:87], v[44:47], v[228:231], v[84:87]\n"
"v_mfma_f32_16x16x32_bf16 v[88:91], v[44:47], a[96:99], v[88:91]\nv_mfma_f32_16x16x32_bf16 v[92:95], v[44:47], a[100:103], v[92:95]\nds_read_b128 v[44:47], v33 offset:0\ns_waitcnt lgkmcnt(3)\n"
"v_mfma_f32_16x16x32_bf16 v[64:67], v[48:51], v[232:235], v[64:67]\nv_add_u32 v34, s54, v34\nv_mfma_f32_16x16x32_bf16 v[68:71], v[48:51], v[236:239], v[68:71]\nv_mfma_f32_16x16x32_bf16 v[72:75], v[48:51], a[104:107], v[72:75]\n"
"v_mfma_f32_16x16x32_bf16 v[76:79], v[48:51], a[108:111], v[76:79]\nds_read_b128 v[48:51], v34 offset:0\ns_waitcnt lgkmcnt(3)\nv_mfma_f32_16x16x32_bf16 v[80:83], v[52:55], v[232:235], v[80:83]\n"
"v_add_u32 v35, s54, v35\nv_mfma_f32_16x16x32_bf16 v[84:87], v[52:55], v[236:239], v[84:87]\nv_mfma_f32_16x16x32_bf16 v[88:91], v[52:55], a[104:107], v[88:91]\nv_mfma_f32_16x16x32_bf16 v[92:95], v[52:55], a[108:111], v[92:95]\n"
"ds_read_b128 v[52:55], v35 offset:0\ns_mov_b32 s52, s47\ns_add_u32 s47, s51, s32\ns_add_u32 s51, s51, s54\n"
"s_add_u32 s49, s49, 28672\ns_waitcnt vmcnt(7)\ns_barrier\ns_waitcnt lgkmcnt(3)\n"
"v_mfma_f32_16x16x32_bf16 v[96:99], v[40:43], v[128:131], 0\nv_mov_b32 v36, s48\ns_add_u32 s48, s48, 1\ns_nop 0\n"
"buffer_store_dword v36, v19, s[24:27], s33 offen sc1\nv_mfma_f32_16x16x32_bf16 v[100:103], v[40:43], v[132:135], 0\nv_mfma_f32_16x16x32_bf16 v[104:107], v[40:43], a[0:3], 0\nv_mfma_f32_16x16x32_bf16 v[108:111], v[40:43], a[4:7], 0\n"
"ds_read_b128 v[40:43], v32 offset:4096\nv_cvt_pk_bf16_f32 v56, v64, v65\ns_waitcnt lgkmcnt(3)\nv_mfma_f32_16x16x32_bf16 v[112:115], v[44:47], v[128:131], 0\n"
"v_cvt_pk_bf16_f32 v57, v66, v67\nv_mfma_f32_16x16x32_bf16 v[116:119], v[44:47], v[132:135], 0\nv_cvt_pk_bf16_f32 v58, v80, v81\nv_mfma_f32_16x16x32_bf16 v[120:123], v[44:47], a[0:3], 0\n"
"v_cvt_pk_bf16_f32 v59, v82, v83\nv_mfma_f32_16x16x32_bf16 v[124:127], v[44:47], a[4:7], 0\nds_read_b128 v[44:47], v33 offset:4096\ns_waitcnt lgkmcnt(3)\n"
"v_mfma_f32_16x16x32_bf16 v[96:99], v[48:51], v[136:139], v[96:99]\nv_mfma_f32_16x16x32_bf16 v[100:103], v[48:51], v[140:143], v[100:103]\nbuffer_store_dwordx4 v[56:59], v15, s[20:23], s50 offen sc1\nv_mfma_f32_16x16x32_bf16 v[104:107], v[48:51], a[8:11], v[104:107]\n"
"v_cvt_pk_bf16_f32 v60, v68, v69\nv_mfma_f32_16x16x32_bf16 v[108:111], v[48:51], a[12:15], v[108:111]\nds_read_b128 v[48:51], v34 offset:4096\nv_cvt_pk_bf16_f32 v61, v70, v71\n"
"s_waitcnt lgkmcnt(3)\nv_mfma_f32_16x16x32_bf16 v[112:115], v[52:55], v[136:139], v[112:115]\nv_cvt_pk_bf16_f32 v62, v84, v85\nv_mfma_f32_16x16x32_bf16 v[116:119], v[52:55], v[140:143], v[116:119]\n"
"v_cvt_pk_bf16_f32 v63, v86, v87\nv_mfma_f32_16x16x32_bf16 v[120:123], v[52:55], a[8:11], v[120:123]\nv_mfma_f32_16x16x32_bf16 v[124:127], v[52:55], a[12:15], v[124:127]\nds_read_b128 v[52:55], v35 offset:4096\n"
"s_waitcnt lgkmcnt(3)\nv_mfma_f32_16x16x32_bf16 v[96:99], v[40:43], v[144:147], v[96:99]\nbuffer_store_dwordx4 v[60:63], v16, s[20:23], s50 offen sc1\nv_mfma_f32_16x16x32_bf16 v[100:103], v[40:43], v[148:151], v[100:103]\n"
"v_cvt_pk_bf16_f32 v56, v72, v73\nv_mfma_f32_16x16x32_bf16 v[104:107], v[40:43], a[16:19], v[104:107]\nv_cvt_pk_bf16_f32 v57, v74, v75\nv_mfma_f32_16x16x32_bf16 v[108:111], v[40:43], a[20:23], v[108:111]\n"
"ds_read_b128 v[40:43], v32 offset:8192\nv_cvt_pk_bf16_f32 v58, v88, v89\ns_waitcnt lgkmcnt(3)\nv_mfma_f32_16x16x32_bf16 v[112:115], v[44:47], v[144:147], v[112:115]\n"
"v_cvt_pk_bf16_f32 v59, v90, v91\nv_mfma_f32_16x16x32_bf16 v[116:119], v[44:47], v[148:151], v[116:119]\nv_mfma_f32_16x16x32_bf16 v[120:123], v[44:47], a[16:19], v[120:123]\nv_mfma_f32_16x16x32_bf16 v[124:127], v[44:47], a[20:23], v[124:127]\n"
"ds_read_b128 v[44:47], v33 offset:8192\nbuffer_store_dwordx4 v[56:59], v17, s[20:23], s50 offen sc1\ns_waitcnt lgkmcnt(3)\nv_mfma_f32_16x16x32_bf16 v[96:99], v[48:51], v[152:155], v[96:99]\n"
"v_cvt_pk_bf16_f32 v60, v76, v77\nv_mfma_f32_16x16x32_bf16 v[100:103], v[48:51], v[156:159], v[100:103]\nv_cvt_pk_bf16_f32 v61, v78, v79\nv_mfma_f32_16x16x32_bf16 v[104:107], v[48:51], a[24:27], v[104:107]\n"
"v_cvt_pk_bf16_f32 v62, v92, v93\nv_mfma_f32_16x16x32_bf16 v[108:111], v[48:51], a[28:31], v[108:111]\nds_read_b128 v[48:51], v34 offset:8192\nv_cvt_pk_bf16_f32 v63, v94, v95\n"
"s_waitcnt lgkmcnt(3)\nv_mfma_f32_16x16x32_bf16 v[112:115], v[52:55], v[152:155], v[112:115]\nv_mfma_f32_16x16x32_bf16 v[116:119], v[52:55], v[156:159], v[116:119]\nv_mfma_f32_16x16x32_bf16 v[120:123], v[52:55], a[24:27], v[120:123]\n"
"buffer_store_dwordx4 v[60:63], v18, s[20:23], s50 offen sc1\nv_mfma_f32_16x16x32_bf16 v[124:127], v[52:55], a[28:31], v[124:127]\nds_read_b128 v[52:55], v35 offset:8192\ns_add_u32 s50, s50, s36\n"
"s_waitcnt lgkmcnt(3)\nv_mfma_f32_16x16x32_bf16 v[96:99], v[40:43], v[160:163], v[96:99]\ns_add_u32 m0, s52, 0\nv_mfma_f32_16x16x32_bf16 v[100:103], v[40:43], v[164:167], v[100:103]\n"
"s_add_u32 s53, s49, 0\nv_mfma_f32_16x16x32_bf16 v[104:107], v[40:43], a[32:35], v[104:107]\nbuffer_load_dwordx4 v10, s[12:15], s53 offen lds\nv_mfma_f32_16x16x32_bf16 v[108:111], v[40:43], a[36:39], v[108:111]\n"
"ds_read_b128 v[40:43], v32 offset:12288\ns_waitcnt lgkmcnt(3)\nv_mfma_f32_16x16x32_bf16 v[112:115], v[44:47], v[160:163], v[112:115]\nv_mfma_f32_16x16x32_bf16 v[116:119], v[44:47], v[164:167], v[116:119]\n"
"v_mfma_f32_16x16x32_bf16 v[120:123], v[44:47], a[32:35], v[120:123]\nv_mfma_f32_16x16x32_bf16 v[124:127], v[44:47], a[36:39], v[124:127]\nds_read_b128 v[44:47], v33 offset:12288\ns_waitcnt lgkmcnt(3)\n"
"v_mfma_f32_16x16x32_bf16 v[96:99], v[48:51], v[168:171], v[96:99]\nv_mfma_f32_16x16x32_bf16 v[100:103], v[48:51], v[172:175], v[100:103]\nv_mfma_f32_16x16x32_bf16 v[104:107], v[48:51], a[40:43], v[104:107]\nv_mfma_f32_16x16x32_bf16 v[108:111], v[48:51], a[44:47], v[108:111]\n"
"ds_read_b128 v[48:51], v34 offset:12288\ns_add_u32 m0, s52, 4096\ns_waitcnt lgkmcnt(3)\nv_mfma_f32_16x16x32_bf16 v[112:115], v[52:55], v[168:171], v[112:115]\n"
"s_add_u32 s53, s49, 128\nv_mfma_f32_16x16x32_bf16 v[116:119], v[52:55], v[172:175], v[116:119]\nbuffer_load_dwordx4 v10, s[12:15], s53 offen lds\nv_mfma_f32_16x16x32_bf16 v[120:123], v[52:55], a[40:43], v[120:123]\n"
"v_mfma_f32_16x16x32_bf16 v[124:127], v[52:55], a[44:47], v[124:127]\nds_read_b128 v[52:55], v35 offset:12288\ns_waitcnt lgkmcnt(3)\nv_mfma_f32_16x16x32_bf16 v[96:99], v[40:43], v[176:179], v[96:99]\n"
"v_mfma_f32_16x16x32_bf16 v[100:103], v[40:43], v[180:183], v[100:103]\nv_mfma_f32_16x16x32_bf16 v[104:107], v[40:43], a[48:51], v[104:107]\nv_mfma_f32_16x16x32_bf16 v[108:111], v[40:43], a[52:55], v[108:111]\nds_read_b128 v[40:43], v32 offset:16384\n"
"s_waitcnt lgkmcnt(3)\nv_mfma_f32_16x16x32_bf16 v[112:115], v[44:47], v[176:179], v[112:115]\nv_mfma_f32_16x16x32_bf16 v[116:119], v[44:47], v[180:183], v[116:119]\nv_mfma_f32_16x16x32_bf16 v[120:123], v[44:47], a[48:51], v[120:123]\n"
"s_add_u32 m0, s52, 8192\nv_mfma_f32_16x16x32_bf16 v[124:127], v[44:47], a[52:55], v[124:127]\nds_read_b128 v[44:47], v33 offset:16384\ns_add_u32 s53, s49, 256\n"
"s_waitcnt lgkmcnt(3)\nv_mfma_f32_16x16x32_bf16 v[96:99], v[48:51], v[184:187], v[96:99]\nbuffer_load_dwordx4 v10, s[12:15], s53 offen lds\nv_mfma_f32_16x16x32_bf16 v[100:103], v[48:51], v[188:191], v[100:103]\n"
"v_mfma_f32_16x16x32_bf16 v[104:107], v[48:51], a[56:59], v[104:107]\nv_mfma_f32_16x16x32_bf16 v[108:111], v[48:51], a[60:63], v[108:111]\nds_read_b128 v[48:51], v34 offset:16384\ns_waitcnt lgkmcnt(3)\n"
"v_mfma_f32_16x16x32_bf16 v[112:115], v[52:55], v[184:187], v[112:115]\nv_mfma_f32_16x16x32_bf16 v[116:119], v[52:55], v[188:191], v[116:119]\nv_mfma_f32_16x16x32_bf16 v[120:123], v[52:55], a[56:59], v[120:123]\nv_mfma_f32_16x16x32_bf16 v[124:127], v[52:55], a[60:63], v[124:127]\n"
"ds_read_b128 v[52:55], v35 offset:16384\ns_waitcnt lgkmcnt(3)\nv_mfma_f32_16x16x32_bf16 v[96:99], v[40:43], v[192:195], v[96:99]\nv_mfma_f32_16x16x32_bf16 v[100:103], v[40:43], v[196:199], v[100:103]\n"
"s_add_u32 m0, s52, 12288\nv_mfma_f32_16x16x32_bf16 v[104:107], v[40:43], a[64:67], v[104:107]\ns_add_u32 s53, s49, 384\nv_mfma_f32_16x16x32_bf16 v[108:111], v[40:43], a[68:71], v[108:111]\n"
"ds_read_b128 v[40:43], v32 offset:20480\nbuffer_load_dwordx4 v10, s[12:15], s53 offen lds\ns_waitcnt lgkmcnt(3)\nv_mfma_f32_16x16x32_bf16 v[112:115], v[44:47], v[192:195], v[112:115]\n"
"v_mfma_f32_16x16x32_bf16 v[116:119], v[44:47], v[196:199], v[116:119]\nv_mfma_f32_16x16x32_bf16 v[120:123], v[44:47], a[64:67], v[120:123]\nv_mfma_f32_16x16x32_bf16 v[124:127], v[44:47], a[68:71], v[124:127]\nds_read_b128 v[44:47], v33 offset:20480\n"
"s_waitcnt lgkmcnt(3)\nv_mfma_f32_16x16x32_bf16 v[96:99], v[48:51], v[200:203], v[96:99]\nv_mfma_f32_16x16x32_bf16 v[100:103], v[48:51], v[204:207], v[100:103]\nv_mfma_f32_16x16x32_bf16 v[104:107], v[48:51], a[72:75], v[104:107]\n"
"v_mfma_f32_16x16x32_bf16 v[108:111], v[48:51], a[76:79], v[108:111]\nds_read_b128 v[48:51], v34 offset:20480\ns_waitcnt lgkmcnt(3)\nv_mfma_f32_16x16x32_bf16 v[112:115], v[52:55], v[200:203], v[112:115]\n"
"s_add_u32 m0, s52, 16384\nv_mfma_f32_16x16x32_bf16 v[116:119], v[52:55], v[204:207], v[116:119]\ns_add_u32 s53, s49, 512\nv_mfma_f32_16x16x32_bf16 v[120:123], v[52:55], a[72:75], v[120:123]\n"
"buffer_load_dwordx4 v10, s[12:15], s53 offen lds\nv_mfma_f32_16x16x32_bf16 v[124:127], v[52:55], a[76:79], v[124:127]\nds_read_b128 v[52:55], v35 offset:20480\ns_waitcnt lgkmcnt(3)\n"
"v_mfma_f32_16x16x32_bf16 v[96:99], v[40:43], v[208:211], v[96:99]\nv_mfma_f32_16x16x32_bf16 v[100:103], v[40:43], v[212:215], v[100:103]\nv_mfma_f32_16x16x32_bf16 v[104:107], v[40:43], a[80:83], v[104:107]\nv_mfma_f32_16x16x32_bf16 v[108:111], v[40:43], a[84:87], v[108:111]\n"
"ds_read_b128 v[40:43], v32 offset:24576\ns_waitcnt lgkmcnt(3)\nv_mfma_f32_16x16x32_bf16 v[112:115], v[44:47], v[208:211], v[112:115]\nv_mfma_f32_16x16x32_bf16 v[116:119], v[44:47], v[212:215], v[116:119]\n"
"v_mfma_f32_16x16x32_bf16 v[120:123], v[44:47], a[80:83], v[120:123]\nv_mfma_f32_16x16x32_bf16 v[124:127], v[44:47], a[84:87], v[124:127]\nds_read_b128 v[44:47], v33 offset:24576\ns_add_u32 m0, s52, 20480\n"
"s_waitcnt lgkmcnt(3)\nv_mfma_f32_16x16x32_bf16 v[96:99], v[48:51], v[216:219], v[96:99]\ns_add_u32 s54, s51, 32768\ns_cmp_eq_u32 s54, 163840\n"
"v_mfma_f32_16x16x32_bf16 v[100:103], v[48:51], v[220:223], v[100:103]\nv_mfma_f32_16x16x32_bf16 v[104:107], v[48:51], a[88:91], v[104:107]\nv_mfma_f32_16x16x32_bf16 v[108:111], v[48:51], a[92:95], v[108:111]\ns_cselect_b32 s54, 0, s54\n"
"s_sub_u32 s54, s54, s51\nds_read_b128 v[48:51], v34 offset:24576\ns_add_u32 s53, s49, 640\ns_waitcnt lgkmcnt(3)\n"
"v_mfma_f32_16x16x32_bf16 v[112:115], v[52:55], v[216:219], v[112:115]\nbuffer_load_dwordx4 v10, s[12:15], s53 offen lds\nv_mfma_f32_16x16x32_bf16 v[116:119], v[52:55], v[220:223], v[116:119]\nv_mfma_f32_16x16x32_bf16 v[120:123], v[52:55], a[88:91], v[120:123]\n"
"v_mfma_f32_16x16x32_bf16 v[124:127], v[52:55], a[92:95], v[124:127]\nds_read_b128 v[52:55], v35 offset:24576\ns_waitcnt lgkmcnt(3)\nv_mfma_f32_16x16x32_bf16 v[96:99], v[40:43], v[224:227], v[96:99]\n"
"v_add_u32 v32, s54, v32\nv_mfma_f32_16x16x32_bf16 v[100:103], v[40:43], v[228:231], v[100:103]\nv_mfma_f32_16x16x32_bf16 v[104:107], v[40:43], a[96:99], v[104:107]\nv_mfma_f32_16x16x32_bf16 v[108:111], v[40:43], a[100:103], v[108:111]\n"
"ds_read_b128 v[40:43], v32 offset:0\ns_waitcnt lgkmcnt(3)\nv_mfma_f32_16x16x32_bf16 v[112:115], v[44:47], v[224:227], v[112:115]\nv_add_u32 v33, s54, v33\n"
"v_mfma_f32_16x16x32_bf16 v[116:119], v[44:47], v[228:231], v[116:119]\ns_add_u32 m0, s52, 24576\nv_mfma_f32_16x16x32_bf16 v[120:123], v[44:47], a[96:99], v[120:123]\ns_add_u32 s53, s49, 768\n"
"v_mfma_f32_16x16x32_bf16 v[124:127], v[44:47], a[100:103], v[124:127]\nds_read_b128 v[44:47], v33 offset:0\nbuffer_load_dwordx4 v10, s[12:15], s53 offen lds\ns_waitcnt lgkmcnt(3)\n"
"v_mfma_f32_16x16x32_bf16 v[96:99], v[48:51], v[232:235], v[96:99]\nv_add_u32 v34, s54, v34\nv_mfma_f32_16x16x32_bf16 v[100:103], v[48:51], v[236:239], v[100:103]\nv_mfma_f32_16x16x32_bf16 v[104:107], v[48:51], a[104:107], v[104:107]\n"
"v_mfma_f32_16x16x32_bf16 v[108:111], v[48:51], a[108:111], v[108:111]\nds_read_b128 v[48:51], v34 offset:0\ns_waitcnt lgkmcnt(3)\nv_mfma_f32_16x16x32_bf16 v[112:115], v[52:55], v[232:235], v[112:115]\n"
"v_add_u32 v35, s54, v35\nv_mfma_f32_16x16x32_bf16 v[116:119], v[52:55], v[236:239], v[116:119]\nv_mfma_f32_16x16x32_bf16 v[120:123], v[52:55], a[104:107], v[120:123]\nv_mfma_f32_16x16x32_bf16 v[124:127], v[52:55], a[108:111], v[124:127]\n"
"ds_read_b128 v[52:55], v35 offset:0\ns_mov_b32 s52, s47\ns_add_u32 s47, s51, s32\ns_add_u32 s51, s51, s54\n"
"s_add_u32 s49, s49, 28672\n.p2align 6\nfg1_loop:\ns_waitcnt vmcnt(7)\n"
"s_barrier\ns_waitcnt lgkmcnt(3)\nv_mfma_f32_16x16x32_bf16 v[64:67], v[40:43], v[128:131], 0\nv_mov_b32 v36, s48\n"
"s_add_u32 s48, s48, 1\ns_nop 0\nbuffer_store_dword v36, v19, s[24:27], s33 offen sc1\nv_mfma_f32_16x16x32_bf16 v[68:71], v[40:43], v[132:135], 0\n"
"v_mfma_f32_16x16x32_bf16 v[72:75], v[40:43], a[0:3], 0\nv_mfma_f32_16x16x32_bf16 v[76:79], v[40:43], a[4:7], 0\nds_read_b128 v[40:43], v32 offset:4096\nv_cvt_pk_bf16_f32 v56, v96, v97\n"
"s_waitcnt lgkmcnt(3)\nv_mfma_f32_16x16x32_bf16 v[80:83], v[44:47], v[128:131], 0\nv_cvt_pk_bf16_f32 v57, v98, v99\nv_mfma_f32_16x16x32_bf16 v[84:87], v[44:47], v[132:135], 0\n"
"v_cvt_pk_bf16_f32 v58, v112, v113\nv_mfma_f32_16x16x32_bf16 v[88:91], v[44:47], a[0:3], 0\nv_cvt_pk_bf16_f32 v59, v114, v115\nv_mfma_f32_16x16x32_bf16 v[92:95], v[44:47], a[4:7], 0\n"
"ds_read_b128 v[44:47], v33 offset:4096\ns_waitcnt lgkmcnt(3)\nv_mfma_f32_16x16x32_bf16 v[64:67], v[48:51], v[136:139], v[64:67]\nv_mfma_f32_16x16x32_bf16 v[68:71], v[48:51], v[140:143], v[68:71]\n"
"buffer_store_dwordx4 v[56:59], v15, s[20:23], s50 offen sc1\nv_mfma_f32_16x16x32_bf16 v[72:75], v[48:51], a[8:11], v[72:75]\nv_cvt_pk_bf16_f32 v60, v100, v101\nv_mfma_f32_16x16x32_bf16 v[76:79], v[48:51], a[12:15], v[76:79]\n"
"ds_read_b128 v[48:51], v34 offset:4096\nv_cvt_pk_bf16_f32 v61, v102, v103\ns_waitcnt lgkmcnt(3)\nv_mfma_f32_16x16x32_bf16 v[80:83], v[52:55], v[136:139], v[80:83]\n"
"v_cvt_pk_bf16_f32 v62, v116, v117\nv_mfma_f32_16x16x32_bf16 v[84:87], v[52:55], v[140:143], v[84:87]\nv_cvt_pk_bf16_f32 v63, v118, v119\nv_mfma_f32_16x16x32_bf16 v[88:91], v[52:55], a[8:11], v[88:91]\n"
"v_mfma_f32_16x16x32_bf16 v[92:95], v[52:55], a[12:15], v[92:95]\nds_read_b128 v[52:55], v35 offset:4096\ns_waitcnt lgkmcnt(3)\nv_mfma_f32_16x16x32_bf16 v[64:67], v[40:43], v[144:147], v[64:67]\n"
"buffer_store_dwordx4 v[60:63], v16, s[20:23], s50 offen sc1\nv_mfma_f32_16x16x32_bf16 v[68:71], v[40:43], v[148:151], v[68:71]\nv_cvt_pk_bf16_f32 v56, v104, v105\nv_mfma_f32_16x16x32_bf16 v[72:75], v[40:43], a[16:19], v[72:75]\n"
"v_cvt_pk_bf16_f32 v57, v106, v107\nv_mfma_f32_16x16x32_bf16 v[76:79], v[40:43], a[20:23], v[76:79]\nds_read_b128 v[40:43], v32 offset:8192\nv_cvt_pk_bf16_f32 v58, v120, v121\n"
"s_waitcnt lgkmcnt(3)\nv_mfma_f32_16x16x32_bf16 v[80:83], v[44:47], v[144:147], v[80:83]\nv_cvt_pk_bf16_f32 v59, v122, v123\nv_mfma_f32_16x16x32_bf16 v[84:87], v[44:47], v[148:151], v[84:87]\n"
"v_mfma_f32_16x16x32_bf16 v[88:91], v[44:47], a[16:19], v[88:91]\nv_mfma_f32_16x16x32_bf16 v[92:95], v[44:47], a[20:23], v[92:95]\nds_read_b128 v[44:47], v33 offset:8192\nbuffer_store_dwordx4 v[56:59], v17, s[20:23], s50 offen sc1\n"
"s_waitcnt lgkmcnt(3)\nv_mfma_f32_16x16x32_bf16 v[64:67], v[48:51], v[152:155], v[64:67]\nv_cvt_pk_bf16_f32 v60, v108, v109\nv_mfma_f32_16x16x32_bf16 v[68:71], v[48:51], v[156:159], v[68:71]\n"
"v_cvt_pk_bf16_f32 v61, v110, v111\nv_mfma_f32_16x16x32_bf16 v[72:75], v[48:51], a[24:27], v[72:75]\nv_cvt_pk_bf16_f32 v62, v124, v125\nv_mfma_f32_16x16x32_bf16 v[76:79], v[48:51], a[28:31], v[76:79]\n"
"ds_read_b128 v[48:51], v34 offset:8192\nv_cvt_pk_bf16_f32 v63, v126, v127\ns_waitcnt lgkmcnt(3)\nv_mfma_f32_16x16x32_bf16 v[80:83], v[52:55], v[152:155], v[80:83]\n"
"v_mfma_f32_16x16x32_bf16 v[84:87], v[52:55], v[156:159], v[84:87]\nv_mfma_f32_16x16x32_bf16 v[88:91], v[52:55], a[24:27], v[88:91]\nbuffer_store_dwordx4 v[60:63], v18, s[20:23], s50 offen sc1\nv_mfma_f32_16x16x32_bf16 v[92:95], v[52:55], a[28:31], v[92:95]\n"
"ds_read_b128 v[52:55], v35 offset:8192\ns_add_u32 s50, s50, s36\ns_waitcnt lgkmcnt(3)\nv_mfma_f32_16x16x32_bf16 v[64:67], v[40:43], v[160:163], v[64:67]\n"
"s_add_u32 m0, s52, 0\nv_mfma_f32_16x16x32_bf16 v[68:71], v[40:43], v[164:167], v[68:71]\ns_add_u32 s53, s49, 0\nv_mfma_f32_16x16x32_bf16 v[72:75], v[40:43], a[32:35], v[72:75]\n"
"buffer_load_dwordx4 v10, s[12:15], s53 offen lds\nv_mfma_f32_16x16x32_bf16 v[76:79], v[40:43], a[36:39], v[76:79]\nds_read_b128 v[40:43], v32 offset:12288\ns_waitcnt lgkmcnt(3)\n"
"v_mfma_f32_16x16x32_bf16 v[80:83], v[44:47], v[160:163], v[80:83]\nv_mfma_f32_16x16x32_bf16 v[84:87], v[44:47], v[164:167], v[84:87]\nv_mfma_f32_16x16x32_bf16 v[88:91], v[44:47], a[32:35], v[88:91]\nv_mfma_f32_16x16x32_bf16 v[92:95], v[44:47], a[36:39], v[92:95]\n"
"ds_read_b128 v[44:47], v33 offset:12288\ns_waitcnt lgkmcnt(3)\nv_mfma_f32_16x16x32_bf16 v[64:67], v[48:51], v[168:171], v[64:67]\nv_mfma_f32_16x16x32_bf16 v[68:71], v[48:51], v[172:175], v[68:71]\n"
"v_mfma_f32_16x16x32_bf16 v[72:75], v[48:51], a[40:43], v[72:75]\nv_mfma_f32_16x16x32_bf16 v[76:79], v[48:51], a[44:47], v[76:79]\nds_read_b128 v[48:51], v34 offset:12288\ns_add_u32 m0, s52, 4096\n"
"s_waitcnt lgkmcnt(3)\nv_mfma_f32_16x16x32_bf16 v[80:83], v[52:55], v[168:171], v[80:83]\ns_add_u32 s53, s49, 128\nv_mfma_f32_16x16x32_bf16 v[84:87], v[52:55], v[172:175], v[84:87]\n"
"buffer_load_dwordx4 v10, s[12:15], s53 offen lds\nv_mfma_f32_16x16x32_bf16 v[88:91], v[52:55], a[40:43], v[88:91]\nv_mfma_f32_16x16x32_bf16 v[92:95], v[52:55], a[44:47], v[92:95]\nds_read_b128 v[52:55], v35 offset:12288\n"
"s_waitcnt lgkmcnt(3)\nv_mfma_f32_16x16x32_bf16 v[64:67], v[40:43], v[176:179], v[64:67]\nv_mfma_f32_16x16x32_bf16 v[68:71], v[40:43], v[180:183], v[68:71]\nv_mfma_f32_16x16x32_bf16 v[72:75], v[40:43], a[48:51], v[72:75]\n"
"v_mfma_f32_16x16x32_bf16 v[76:79], v[40:43], a[52:55], v[76:79]\nds_read_b128 v[40:43], v32 offset:16384\ns_waitcnt lgkmcnt(3)\nv_mfma_f32_16x16x32_bf16 v[80:83], v[44:47], v[176:179], v[80:83]\n"
"v_mfma_f32_16x16x32_bf16 v[84:87], v[44:47], v[180:183], v[84:87]\nv_mfma_f32_16x16x32_bf16 v[88:91], v[44:47], a[48:51], v[88:91]\ns_add_u32 m0, s52, 8192\nv_mfma_f32_16x16x32_bf16 v[92:95], v[44:47], a[52:55], v[92:95]\n"
"ds_read_b128 v[44:47], v33 offset:16384\ns_add_u32 s53, s49, 256\ns_waitcnt lgkmcnt(3)\nv_mfma_f32_16x16x32_bf16 v[64:67], v[48:51], v[184:187], v[64:67]\n"
"buffer_load_dwordx4 v10, s[12:15], s53 offen lds\nv_mfma_f32_16x16x32_bf16 v[68:71], v[48:51], v[188:191], v[68:71]\nv_mfma_f32_16x16x32_bf16 v[72:75], v[48:51], a[56:59], v[72:75]\nv_mfma_f32_16x16x32_bf16 v[76:79], v[48:51], a[60:63], v[76:79]\n"
"ds_read_b128 v[48:51], v34 offset:16384\ns_waitcnt lgkmcnt(3)\nv_mfma_f32_16x16x32_bf16 v[80:83], v[52:55], v[184:187], v[80:83]\nv_mfma_f32_16x16x32_bf16 v[84:87], v[52:55], v[188:191], v[84:87]\n"
"v_mfma_f32_16x16x32_bf16 v[88:91], v[52:55], a[56:59], v[88:91]\nv_mfma_f32_16x16x32_bf16 v[92:95], v[52:55], a[60:63], v[92:95]\nds_read_b128 v[52:55], v35 offset:16384\ns_waitcnt lgkmcnt(3)\n"
"v_mfma_f32_16x16x32_bf16 v[64:67], v[40:43], v[192:195], v[64:67]\nv_mfma_f32_16x16x32_bf16 v[68:71], v[40:43], v[196:199], v[68:71]\ns_add_u32 m0, s52, 12288\nv_mfma_f32_16x16x32_bf16 v[72:75], v[40:43], a[64:67], v[72:75]\n"
"s_add_u32 s53, s49, 384\nv_mfma_f32_16x16x32_bf16 v[76:79], v[40:43], a[68:71], v[76:79]\nds_read_b128 v[40:43], v32 offset:20480\nbuffer_load_dwordx4 v10, s[12:15], s53 offen lds\n"
"s_waitcnt lgkmcnt(3)\nv_mfma_f32_16x16x32_bf16 v[80:83], v[44:47], v[192:195], v[80:83]\nv_mfma_f32_16x16x32_bf16 v[84:87], v[44:47], v[196:199], v[84:87]\nv_mfma_f32_16x16x32_bf16 v[88:91], v[44:47], a[64:67], v[88:91]\n"
"v_mfma_f32_16x16x32_bf16 v[92:95], v[44:47], a[68:71], v[92:95]\nds_read_b128 v[44:47], v33 offset:20480\ns_waitcnt lgkmcnt(3)\nv_mfma_f32_16x16x32_bf16 v[64:67], v[48:51], v[200:203], v[64:67]\n"
"v_mfma_f32_16x16x32_bf16 v[68:71], v[48:51], v[204:207], v[68:71]\nv_mfma_f32_16x16x32_bf16 v[72:75], v[48:51], a[72:75], v[72:75]\nv_mfma_f32_16x16x32_bf16 v[76:79], v[48:51], a[76:79], v[76:79]\nds_read_b128 v[48:51], v34 offset:20480\n"
"s_waitcnt lgkmcnt(3)\nv_mfma_f32_16x16x32_bf16 v[80:83], v[52:55], v[200:203], v[80:83]\ns_add_u32 m0, s52, 16384\nv_mfma_f32_16x16x32_bf16 v[84:87], v[52:55], v[204:207], v[84:87]\n"
"s_add_u32 s53, s49, 512\nv_mfma_f32_16x16x32_bf16 v[88:91], v[52:55], a[72:75], v[88:91]\nbuffer_load_dwordx4 v10, s[12:15], s53 offen lds\nv_mfma_f32_16x16x32_bf16 v[92:95], v[52:55], a[76:79], v[92:95]\n"
"ds_read_b128 v[52:55], v35 offset:20480\ns_waitcnt lgkmcnt(3)\nv_mfma_f32_16x16x32_bf16 v[64:67], v[40:43], v[208:211], v[64:67]\nv_mfma_f32_16x16x32_bf16 v[68:71], v[40:43], v[212:215], v[68:71]\n"
"v_mfma_f32_16x16x32_bf16 v[72:75], v[40:43], a[80:83], v[72:75]\nv_mfma_f32_16x16x32_bf16 v[76:79], v[40:43], a[84:87], v[76:79]\nds_read_b128 v[40:43], v32 offset:24576\ns_waitcnt lgkmcnt(3)\n"
"v_mfma_f32_16x16x32_bf16 v[80:83], v[44:47], v[208:211], v[80:83]\nv_mfma_f32_16x16x32_bf16 v[84:87], v[44:47], v[212:215], v[84:87]\nv_mfma_f32_16x16x32_bf16 v[88:91], v[44:47], a[80:83], v[88:91]\nv_mfma_f32_16x16x32_bf16 v[92:95], v[44:47], a[84:87], v[92:95]\n"
"ds_read_b128 v[44:47], v33 offset:24576\ns_add_u32 m0, s52, 20480\ns_waitcnt lgkmcnt(3)\nv_mfma_f32_16x16x32_bf16 v[64:67], v[48:51], v[216:219], v[64:67]\n"
"s_add_u32 s54, s51, 32768\ns_cmp_eq_u32 s54, 163840\nv_mfma_f32_16x16x32_bf16 v[68:71], v[48:51], v[220:223], v[68:71]\nv_mfma_f32_16x16x32_bf16 v[72:75], v[48:51], a[88:91], v[72:75]\n"
"v_mfma_f32_16x16x32_bf16 v[76:79], v[48:51], a[92:95], v[76:79]\ns_cselect_b32 s54, 0, s54\ns_sub_u32 s54, s54, s51\nds_read_b128 v[48:51], v34 offset:24576\n"
"s_add_u32 s53, s49, 640\ns_waitcnt lgkmcnt(3)\nv_mfma_f32_16x16x32_bf16 v[80:83], v[52:55], v[216:219], v[80:83]\nbuffer_load_dwordx4 v10, s[12:15], s53 offen lds\n"
"v_mfma_f32_16x16x32_bf16 v[84:87], v[52:55], v[220:223], v[84:87]\nv_mfma_f32_16x16x32_bf16 v[88:91], v[52:55], a[88:91], v[88:91]\nv_mfma_f32_16x16x32_bf16 v[92:95], v[52:55], a[92:95], v[92:95]\nds_read_b128 v[52:55], v35 offset:24576\n"
"s_waitcnt lgkmcnt(3)\nv_mfma_f32_16x16x32_bf16 v[64:67], v[40:43], v[224:227], v[64:67]\nv_add_u32 v32, s54, v32\nv_mfma_f32_16x16x32_bf16 v[68:71], v[40:43], v[228:231], v[68:71]\n"
"v_mfma_f32_16x16x32_bf16 v[72:75], v[40:43], a[96:99], v[72:75]\nv_mfma_f32_16x16x32_bf16 v[76:79], v[40:43], a[100:103], v[76:79]\nds_read_b128 v[40:43], v32 offset:0\ns_waitcnt lgkmcnt(3)\n"
"v_mfma_f32_16x16x32_bf16 v[80:83], v[44:47], v[224:227], v[80:83]\nv_add_u32 v33, s54, v33\nv_mfma_f32_16x16x32_bf16 v[84:87], v[44:47], v[228:231], v[84:87]\ns_add_u32 m0, s52, 24576\n"
"v_mfma_f32_16x16x32_bf16 v[88:91], v[44:47], a[96:99], v[88:91]\ns_add_u32 s53, s49, 768\nv_mfma_f32_16x16x32_bf16 v[92:95], v[44:47], a[100:103], v[92:95]\nds_read_b128 v[44:47], v33 offset:0\n"
"buffer_load_dwordx4 v10, s[12:15], s53 offen lds\ns_waitcnt lgkmcnt(3)\nv_mfma_f32_16x16x32_bf16 v[64:67], v[48:51], v[232:235], v[64:67]\nv_add_u32 v34, s54, v34\n"
"v_mfma_f32_16x16x32_bf16 v[68:71], v[48:51], v[236:239], v[68:71]\nv_mfma_f32_16x16x32_bf16 v[72:75], v[48:51], a[104:107], v[72:75]\nv_mfma_f32_16x16x32_bf16 v[76:79], v[48:51], a[108:111], v[76:79]\nds_read_b128 v[48:51], v34 offset:0\n"
"s_waitcnt lgkmcnt(3)\nv_mfma_f32_16x16x32_bf16 v[80:83], v[52:55], v[232:235], v[80:83]\nv_add_u32 v35, s54, v35\nv_mfma_f32_16x16x32_bf16 v[84:87], v[52:55], v[236:239], v[84:87]\n"
"v_mfma_f32_16x16x32_bf16 v[88:91], v[52:55], a[104:107], v[88:91]\nv_mfma_f32_16x16x32_bf16 v[92:95], v[52:55], a[108:111], v[92:95]\nds_read_b128 v[52:55], v35 offset:0\ns_mov_b32 s52, s47\n"
"s_add_u32 s47, s51, s32\ns_add_u32 s51, s51, s54\ns_add_u32 s49, s49, 28672\ns_waitcnt vmcnt(7)\n"
"s_barrier\ns_waitcnt lgkmcnt(3)\nv_mfma_f32_16x16x32_bf16 v[96:99], v[40:43], v[128:131], 0\nv_mov_b32 v36, s48\n"
"s_add_u32 s48, s48, 1\ns_nop 0\nbuffer_store_dword v36, v19, s[24:27], s33 offen sc1\nv_mfma_f32_16x16x32_bf16 v[100:103], v[40:43], v[132:135], 0\n"
"v_mfma_f32_16x16x32_bf16 v[104:107], v[40:43], a[0:3], 0\nv_mfma_f32_16x16x32_bf16 v[108:111], v[40:43], a[4:7], 0\nds_read_b128 v[40:43], v32 offset:4096\nv_cvt_pk_bf16_f32 v56, v64, v65\n"
"s_waitcnt lgkmcnt(3)\nv_mfma_f32_16x16x32_bf16 v[112:115], v[44:47], v[128:131], 0\nv_cvt_pk_bf16_f32 v57, v66, v67\nv_mfma_f32_16x16x32_bf16 v[116:119], v[44:47], v[132:135], 0\n"
"v_cvt_pk_bf16_f32 v58, v80, v81\nv_mfma_f32_16x16x32_bf16 v[120:123], v[44:47], a[0:3], 0\nv_cvt_pk_bf16_f32 v59, v82, v83\nv_mfma_f32_16x16x32_bf16 v[124:127], v[44:47], a[4:7], 0\n"
"ds_read_b128 v[44:47], v33 offset:4096\ns_waitcnt lgkmcnt(3)\nv_mfma_f32_16x16x32_bf16 v[96:99], v[48:51], v[136:139], v[96:99]\nv_mfma_f32_16x16x32_bf16 v[100:103], v[48:51], v[140:143], v[100:103]\n"
"buffer_store_dwordx4 v[56:59], v15, s[20:23], s50 offen sc1\nv_mfma_f32_16x16x32_bf16 v[104:107], v[48:51], a[8:11], v[104:107]\nv_cvt_pk_bf16_f32 v60, v68, v69\nv_mfma_f32_16x16x32_bf16 v[108:111], v[48:51], a[12:15], v[108:111]\n"
"ds_read_b128 v[48:51], v34 offset:4096\nv_cvt_pk_bf16_f32 v61, v70, v71\ns_waitcnt lgkmcnt(3)\nv_mfma_f32_16x16x32_bf16 v[112:115], v[52:55], v[136:139], v[112:115]\n"
"v_cvt_pk_bf16_f32 v62, v84, v85\nv_mfma_f32_16x16x32_bf16 v[116:119], v[52:55], v[140:143], v[116:119]\nv_cvt_pk_bf16_f32 v63, v86, v87\nv_mfma_f32_16x16x32_bf16 v[120:123], v[52:55], a[8:11], v[120:123]\n"
"v_mfma_f32_16x16x32_bf16 v[124:127], v[52:55], a[12:15], v[124:127]\nds_read_b128 v[52:55], v35 offset:4096\ns_waitcnt lgkmcnt(3)\nv_mfma_f32_16x16x32_bf16 v[96:99], v[40:43], v[144:147], v[96:99]\n"
"buffer_store_dwordx4 v[60:63], v16, s[20:23], s50 offen sc1\nv_mfma_f32_16x16x32_bf16 v[100:103], v[40:43], v[148:151], v[100:103]\nv_cvt_pk_bf16_f32 v56, v72, v73\nv_mfma_f32_16x16x32_bf16 v[104:107], v[40:43], a[16:19], v[104:107]\n"
"v_cvt_pk_bf16_f32 v57, v74, v75\nv_mfma_f32_16x16x32_bf16 v[108:111], v[40:43], a[20:23], v[108:111]\nds_read_b128 v[40:43], v32 offset:8192\nv_cvt_pk_bf16_f32 v58, v88, v89\n"
"s_waitcnt lgkmcnt(3)\nv_mfma_f32_16x16x32_bf16 v[112:115], v[44:47], v[144:147], v[112:115]\nv_cvt_pk_bf16_f32 v59, v90, v91\nv_mfma_f32_16x16x32_bf16 v[116:119], v[44:47], v[148:151], v[116:119]\n"
"v_mfma_f32_16x16x32_bf16 v[120:123], v[44:47], a[16:19], v[120:123]\nv_mfma_f32_16x16x32_bf16 v[124:127], v[44:47], a[20:23], v[124:127]\nds_read_b128 v[44:47], v33 offset:8192\nbuffer_store_dwordx4 v[56:59], v17, s[20:23], s50 offen sc1\n"
"s_waitcnt lgkmcnt(3)\nv_mfma_f32_16x16x32_bf16 v[96:99], v[48:51], v[152:155], v[96:99]\nv_cvt_pk_bf16_f32 v60, v76, v77\nv_mfma_f32_16x16x32_bf16 v[100:103], v[48:51], v[156:159], v[100:103]\n"
"v_cvt_pk_bf16_f32 v61, v78, v79\nv_mfma_f32_16x16x32_bf16 v[104:107], v[48:51], a[24:27], v[104:107]\nv_cvt_pk_bf16_f32 v62, v92, v93\nv_mfma_f32_16x16x32_bf16 v[108:111], v[48:51], a[28:31], v[108:111]\n"
"ds_read_b128 v[48:51], v34 offset:8192\nv_cvt_pk_bf16_f32 v63, v94, v95\ns_waitcnt lgkmcnt(3)\nv_mfma_f32_16x16x32_bf16 v[112:115], v[52:55], v[152:155], v[112:115]\n"
"v_mfma_f32_16x16x32_bf16 v[116:119], v[52:55], v[156:159], v[116:119]\nv_mfma_f32_16x16x32_bf16 v[120:123], v[52:55], a[24:27], v[120:123]\nbuffer_store_dwordx4 v[60:63], v18, s[20:23], s50 offen sc1\nv_mfma_f32_16x16x32_bf16 v[124:127], v[52:55], a[28:31], v[124:127]\n"
"ds_read_b128 v[52:55], v35 offset:8192\ns_add_u32 s50, s50, s36\ns_waitcnt lgkmcnt(3)\nv_mfma_f32_16x16x32_bf16 v[96:99], v[40:43], v[160:163], v[96:99]\n"
"s_add_u32 m0, s52, 0\nv_mfma_f32_16x16x32_bf16 v[100:103], v[40:43], v[164:167], v[100:103]\ns_add_u32 s53, s49, 0\nv_mfma_f32_16x16x32_bf16 v[104:107], v[40:43], a[32:35], v[104:107]\n"
"buffer_load_dwordx4 v10, s[12:15], s53 offen lds\nv_mfma_f32_16x16x32_bf16 v[108:111], v[40:43], a[36:39], v[108:111]\nds_read_b128 v[40:43], v32 offset:12288\ns_waitcnt lgkmcnt(3)\n"
"v_mfma_f32_16x16x32_bf16 v[112:115], v[44:47], v[160:163], v[112:115]\nv_mfma_f32_16x16x32_bf16 v[116:119], v[44:47], v[164:167], v[116:119]\nv_mfma_f32_16x16x32_bf16 v[120:123], v[44:47], a[32:35], v[120:123]\nv_mfma_f32_16x16x32_bf16 v[124:127], v[44:47], a[36:39], v[124:127]\n"
"ds_read_b128 v[44:47], v33 offset:12288\ns_waitcnt lgkmcnt(3)\nv_mfma_f32_16x16x32_bf16 v[96:99], v[48:51], v[168:171], v[96:99]\nv_mfma_f32_16x16x32_bf16 v[100:103], v[48:51], v[172:175], v[100:103]\n"
"v_mfma_f32_16x16x32_bf16 v[104:107], v[48:51], a[40:43], v[104:107]\nv_mfma_f32_16x16x32_bf16 v[108:111], v[48:51], a[44:47], v[108:111]\nds_read_b128 v[48:51], v34 offset:12288\ns_add_u32 m0, s52, 4096\n"
"s_waitcnt lgkmcnt(3)\nv_mfma_f32_16x16x32_bf16 v[112:115], v[52:55], v[168:171], v[112:115]\ns_add_u32 s53, s49, 128\nv_mfma_f32_16x16x32_bf16 v[116:119], v[52:55], v[172:175], v[116:119]\n"
"buffer_load_dwordx4 v10, s[12:15], s53 offen lds\nv_mfma_f32_16x16x32_bf16 v[120:123], v[52:55], a[40:43], v[120:123]\nv_mfma_f32_16x16x32_bf16 v[124:127], v[52:55], a[44:47], v[124:127]\nds_read_b128 v[52:55], v35 offset:12288\n"
"s_waitcnt lgkmcnt(3)\nv_mfma_f32_16x16x32_bf16 v[96:99], v[40:43], v[176:179], v[96:99]\nv_mfma_f32_16x16x32_bf16 v[100:103], v[40:43], v[180:183], v[100:103]\nv_mfma_f32_16x16x32_bf16 v[104:107], v[40:43], a[48:51], v[104:107]\n"
"v_mfma_f32_16x16x32_bf16 v[108:111], v[40:43], a[52:55], v[108:111]\nds_read_b128 v[40:43], v32 offset:16384\ns_waitcnt lgkmcnt(3)\nv_mfma_f32_16x16x32_bf16 v[112:115], v[44:47], v[176:179], v[112:115]\n"
"v_mfma_f32_16x16x32_bf16 v[116:119], v[44:47], v[180:183], v[116:119]\nv_mfma_f32_16x16x32_bf16 v[120:123], v[44:47], a[48:51], v[120:123]\ns_add_u32 m0, s52, 8192\nv_mfma_f32_16x16x32_bf16 v[124:127], v[44:47], a[52:55], v[124:127]\n"
"ds_read_b128 v[44:47], v33 offset:16384\ns_add_u32 s53, s49, 256\ns_waitcnt lgkmcnt(3)\nv_mfma_f32_16x16x32_bf16 v[96:99], v[48:51], v[184:187], v[96:99]\n"
"buffer_load_dwordx4 v10, s[12:15], s53 offen lds\nv_mfma_f32_16x16x32_bf16 v[100:103], v[48:51], v[188:191], v[100:103]\nv_mfma_f32_16x16x32_bf16 v[104:107], v[48:51], a[56:59], v[104:107]\nv_mfma_f32_16x16x32_bf16 v[108:111], v[48:51], a[60:63], v[108:111]\n"
"ds_read_b128 v[48:51], v34 offset:16384\ns_waitcnt lgkmcnt(3)\nv_mfma_f32_16x16x32_bf16 v[112:115], v[52:55], v[184:187], v[112:115]\nv_mfma_f32_16x16x32_bf16 v[116:119], v[52:55], v[188:191], v[116:119]\n"
"v_mfma_f32_16x16x32_bf16 v[120:123], v[52:55], a[56:59], v[120:123]\nv_mfma_f32_16x16x32_bf16 v[124:127], v[52:55], a[60:63], v[124:127]\nds_read_b128 v[52:55], v35 offset:16384\ns_waitcnt lgkmcnt(3)\n"
"v_mfma_f32_16x16x32_bf16 v[96:99], v[40:43], v[192:195], v[96:99]\nv_mfma_f32_16x16x32_bf16 v[100:103], v[40:43], v[196:199], v[100:103]\ns_add_u32 m0, s52, 12288\nv_mfma_f32_16x16x32_bf16 v[104:107], v[40:43], a[64:67], v[104:107]\n"
"s_add_u32 s53, s49, 384\nv_mfma_f32_16x16x32_bf16 v[108:111], v[40:43], a[68:71], v[108:111]\nds_read_b128 v[40:43], v32 offset:20480\nbuffer_load_dwordx4 v10, s[12:15], s53 offen lds\n"
"s_waitcnt lgkmcnt(3)\nv_mfma_f32_16x16x32_bf16 v[112:115], v[44:47], v[192:195], v[112:115]\nv_mfma_f32_16x16x32_bf16 v[116:119], v[44:47], v[196:199], v[116:119]\nv_mfma_f32_16x16x32_bf16 v[120:123], v[44:47], a[64:67], v[120:123]\n"
"v_mfma_f32_16x16x32_bf16 v[124:127], v[44:47], a[68:71], v[124:127]\nds_read_b128 v[44:47], v33 offset:20480\ns_waitcnt lgkmcnt(3)\nv_mfma_f32_16x16x32_bf16 v[96:99], v[48:51], v[200:203], v[96:99]\n"
"v_mfma_f32_16x16x32_bf16 v[100:103], v[48:51], v[204:207], v[100:103]\nv_mfma_f32_16x16x32_bf16 v[104:107], v[48:51], a[72:75], v[104:107]\nv_mfma_f32_16x16x32_bf16 v[108:111], v[48:51], a[76:79], v[108:111]\nds_read_b128 v[48:51], v34 offset:20480\n"
"s_waitcnt lgkmcnt(3)\nv_mfma_f32_16x16x32_bf16 v[112:115], v[52:55], v[200:203], v[112:115]\ns_add_u32 m0, s52, 16384\nv_mfma_f32_16x16x32_bf16 v[116:119], v[52:55], v[204:207], v[116:119]\n"
"s_add_u32 s53, s49, 512\nv_mfma_f32_16x16x32_bf16 v[120:123], v[52:55], a[72:75], v[120:123]\nbuffer_load_dwordx4 v10, s[12:15], s53 offen lds\nv_mfma_f32_16x16x32_bf16 v[124:127], v[52:55], a[76:79], v[124:127]\n"
"ds_read_b128 v[52:55], v35 offset:20480\ns_waitcnt lgkmcnt(3)\nv_mfma_f32_16x16x32_bf16 v[96:99], v[40:43], v[208:211], v[96:99]\nv_mfma_f32_16x16x32_bf16 v[100:103], v[40:43], v[212:215], v[100:103]\n"
"v_mfma_f32_16x16x32_bf16 v[104:107], v[40:43], a[80:83], v[104:107]\nv_mfma_f32_16x16x32_bf16 v[108:111], v[40:43], a[84:87], v[108:111]\nds_read_b128 v[40:43], v32 offset:24576\ns_waitcnt lgkmcnt(3)\n"
"v_mfma_f32_16x16x32_bf16 v[112:115], v[44:47], v[208:211], v[112:115]\nv_mfma_f32_16x16x32_bf16 v[116:119], v[44:47], v[212:215], v[116:119]\nv_mfma_f32_16x16x32_bf16 v[120:123], v[44:47], a[80:83], v[120:123]\nv_mfma_f32_16x16x32_bf16 v[124:127], v[44:47], a[84:87], v[124:127]\n"
"ds_read_b128 v[44:47], v33 offset:24576\ns_add_u32 m0, s52, 20480\ns_waitcnt lgkmcnt(3)\nv_mfma_f32_16x16x32_bf16 v[96:99], v[48:51], v[216:219], v[96:99]\n"
"s_add_u32 s54, s51, 32768\ns_cmp_eq_u32 s54, 163840\nv_mfma_f32_16x16x32_bf16 v[100:103], v[48:51], v[220:223], v[100:103]\nv_mfma_f32_16x16x32_bf16 v[104:107], v[48:51], a[88:91], v[104:107]\n"
"v_mfma_f32_16x16x32_bf16 v[108:111], v[48:51], a[92:95], v[108:111]\ns_cselect_b32 s54, 0, s54\ns_sub_u32 s54, s54, s51\nds_read_b128 v[48:51], v34 offset:24576\n"
"s_add_u32 s53, s49, 640\ns_waitcnt lgkmcnt(3)\nv_mfma_f32_16x16x32_bf16 v[112:115], v[52:55], v[216:219], v[112:115]\nbuffer_load_dwordx4 v10, s[12:15], s53 offen lds\n"
"v_mfma_f32_16x16x32_bf16 v[116:119], v[52:55], v[220:223], v[116:119]\nv_mfma_f32_16x16x32_bf16 v[120:123], v[52:55], a[88:91], v[120:123]\nv_mfma_f32_16x16x32_bf16 v[124:127], v[52:55], a[92:95], v[124:127]\nds_read_b128 v[52:55], v35 offset:24576\n"
"s_waitcnt lgkmcnt(3)\nv_mfma_f32_16x16x32_bf16 v[96:99], v[40:43], v[224:227], v[96:99]\nv_add_u32 v32, s54, v32\nv_mfma_f32_16x16x32_bf16 v[100:103], v[40:43], v[228:231], v[100:103]\n"
"v_mfma_f32_16x16x32_bf16 v[104:107], v[40:43], a[96:99], v[104:107]\nv_mfma_f32_16x16x32_bf16 v[108:111], v[40:43], a[100:103], v[108:111]\nds_read_b128 v[40:43], v32 offset:0\ns_waitcnt lgkmcnt(3)\n"
"v_mfma_f32_16x16x32_bf16 v[112:115], v[44:47], v[224:227], v[112:115]\nv_add_u32 v33, s54, v33\nv_mfma_f32_16x16x32_bf16 v[116:119], v[44:47], v[228:231], v[116:119]\ns_add_u32 m0, s52, 24576\n"
"v_mfma_f32_16x16x32_bf16 v[120:123], v[44:47], a[96:99], v[120:123]\ns_add_u32 s53, s49, 768\nv_mfma_f32_16x16x32_bf16 v[124:127], v[44:47], a[100:103], v[124:127]\nds_read_b128 v[44:47], v33 offset:0\n"
"buffer_load_dwordx4 v10, s[12:15], s53 offen lds\ns_waitcnt lgkmcnt(3)\nv_mfma_f32_16x16x32_bf16 v[96:99], v[48:51], v[232:235], v[96:99]\nv_add_u32 v34, s54, v34\n"
"v_mfma_f32_16x16x32_bf16 v[100:103], v[48:51], v[236:239], v[100:103]\nv_mfma_f32_16x16x32_bf16 v[104:107], v[48:51], a[104:107], v[104:107]\nv_mfma_f32_16x16x32_bf16 v[108:111], v[48:51], a[108:111], v[108:111]\nds_read_b128 v[48:51], v34 offset:0\n"
"s_waitcnt lgkmcnt(3)\nv_mfma_f32_16x16x32_bf16 v[112:115], v[52:55], v[232:235], v[112:115]\nv_add_u32 v35, s54, v35\nv_mfma_f32_16x16x32_bf16 v[116:119], v[52:55], v[236:239], v[116:119]\n"
"v_mfma_f32_16x16x32_bf16 v[120:123], v[52:55], a[104:107], v[120:123]\nv_mfma_f32_16x16x32_bf16 v[124:127], v[52:55], a[108:111], v[124:127]\nds_read_b128 v[52:55], v35 offset:0\ns_mov_b32 s52, s47\n"
"s_add_u32 s47, s51, s32\ns_add_u32 s51, s51, s54\ns_add_u32 s49, s49, 28672\ns_cmp_lt_u32 s49, 1921024\n"
"s_cbranch_scc1 fg1_loop\ns_nop 7\nv_cvt_pk_bf16_f32 v56, v96, v97\nv_cvt_pk_bf16_f32 v57, v98, v99\n"
"v_cvt_pk_bf16_f32 v58, v112, v113\nv_cvt_pk_bf16_f32 v59, v114, v115\ns_nop 0\ns_nop 0\n"
"buffer_store_dwordx4 v[56:59], v15, s[20:23], s50 offen sc1\nv_cvt_pk_bf16_f32 v60, v100, v101\nv_cvt_pk_bf16_f32 v61, v102, v103\nv_cvt_pk_bf16_f32 v62, v116, v117\n"
"v_cvt_pk_bf16_f32 v63, v118, v119\ns_nop 0\ns_nop 0\nbuffer_store_dwordx4 v[60:63], v16, s[20:23], s50 offen sc1\n"
"v_cvt_pk_bf16_f32 v56, v104, v105\nv_cvt_pk_bf16_f32 v57, v106, v107\nv_cvt_pk_bf16_f32 v58, v120, v121\nv_cvt_pk_bf16_f32 v59, v122, v123\n"
"s_nop 0\ns_nop 0\nbuffer_store_dwordx4 v[56:59], v17, s[20:23], s50 offen sc1\nv_cvt_pk_bf16_f32 v60, v108, v109\n"
"v_cvt_pk_bf16_f32 v61, v110, v111\nv_cvt_pk_bf16_f32 v62, v124, v125\nv_cvt_pk_bf16_f32 v63, v126, v127\ns_nop 0\n"
"s_nop 0\nbuffer_store_dwordx4 v[60:63], v18, s[20:23], s50 offen sc1\ns_add_u32 s48, s28, 64\nv_mov_b32 v36, s48\n"
"s_waitcnt vmcnt(0) lgkmcnt(0)\ns_barrier\nbuffer_store_dword v36, v19, s[24:27], s33 offen sc1\nfg1_end:\n"
    : "+{v[244:251]}"(w) : "{s[8:23]}"(s0), "{s[24:39]}"(s1), "{v[8:23]}"(v0) : "v24", "v25", "v26", "v27", "v28", "v29", "v30", "v31", "v32", "v33", "v34", "v35", "v36", "v37", "v38", "v39", "v40", "v41", "v42", "v43", "v44", "v45", "v46", "v47", "v48", "v49", "v50", "v51", "v52", "v53", "v54", "v55", "v56", "v57", "v58", "v59", "v60", "v61", "v62", "v63", "v64", "v65", "v66", "v67", "v68", "v69", "v70", "v71", "v72", "v73", "v74", "v75", "v76", "v77", "v78", "v79", "v80", "v81", "v82", "v83", "v84", "v85", "v86", "v87", "v88", "v89", "v90", "v91", "v92", "v93", "v94", "v95", "v96", "v97", "v98", "v99", "v100", "v101", "v102", "v103", "v104", "v105", "v106", "v107", "v108", "v109", "v110", "v111", "v112", "v113", "v114", "v115", "v116", "v117", "v118", "v119", "v120", "v121", "v122", "v123", "v124", "v125", "v126", "v127", "v128", "v129", "v130", "v131", "v132", "v133", "v134", "v135", "v136", "v137", "v138", "v139", "v140", "v141", "v142", "v143", "v144", "v145", "v146", "v147", "v148", "v149", "v150", "v151", "v152", "v153", "v154", "v155", "v156", "v157", "v158", "v159", "v160", "v161", "v162", "v163", "v164", "v165", "v166", "v167", "v168", "v169", "v170", "v171", "v172", "v173", "v174", "v175", "v176", "v177", "v178", "v179", "v180", "v181", "v182", "v183", "v184", "v185", "v186", "v187", "v188", "v189", "v190", "v191", "v192", "v193", "v194", "v195", "v196", "v197", "v198", "v199", "v200", "v201", "v202", "v203", "v204", "v205", "v206", "v207", "v208", "v209", "v210", "v211", "v212", "v213", "v214", "v215", "v216", "v217", "v218", "v219", "v220", "v221", "v222", "v223", "v224", "v225", "v226", "v227", "v228", "v229", "v230", "v231", "v232", "v233", "v234", "v235", "v236", "v237", "v238", "v239", "v240", "v241", "v242", "v243", "a0", "a1", "a2", "a3", "a4", "a5", "a6", "a7", "a8", "a9", "a10", "a11", "a12", "a13", "a14", "a15", "a16", "a17", "a18", "a19", "a20", "a21", "a22", "a23", "a24", "a25", "a26", "a27", "a28", "a29", "a30", "a31", "a32", "a33", "a34", "a35", "a36", "a37", "a38", "a39", "a40", "a41", "a42", "a43", "a44", "a45", "a46", "a47", "a48", "a49", "a50", "a51", "a52", "a53", "a54", "a55", "a56", "a57", "a58", "a59", "a60", "a61", "a62", "a63", "a64", "a65", "a66", "a67", "a68", "a69", "a70", "a71", "a72", "a73", "a74", "a75", "a76", "a77", "a78", "a79", "a80", "a81", "a82", "a83", "a84", "a85", "a86", "a87", "a88", "a89", "a90", "a91", "a92", "a93", "a94", "a95", "a96", "a97", "a98", "a99", "a100", "a101", "a102", "a103", "a104", "a105", "a106", "a107", "a108", "a109", "a110", "a111", "a112", "a113", "a114", "a115", "a116", "a117", "a118", "a119", "a120", "a121", "a122", "a123", "a124", "a125", "a126", "a127", "a128", "a129", "a130", "a131", "a132", "a133", "a134", "a135", "a136", "a137", "a138", "a139", "a140", "a141", "a142", "a143", "a144", "a145", "a146", "a147", "a148", "a149", "a150", "a151", "a152", "a153", "a154", "a155", "a156", "a157", "a158", "a159", "a160", "a161", "a162", "a163", "a164", "a165", "a166", "a167", "a168", "a169", "a170", "a171", "a172", "a173", "a174", "a175", "a176", "a177", "a178", "a179", "a180", "a181", "a182", "a183", "a184", "a185", "a186", "a187", "a188", "a189", "a190", "a191", "a192", "a193", "a194", "a195", "a196", "a197", "a198", "a199", "a200", "a201", "a202", "a203", "a204", "a205", "a206", "a207", "a208", "a209", "a210", "a211", "a212", "a213", "a214", "a215", "a216", "a217", "a218", "a219", "a220", "a221", "a222", "a223", "s40", "s41", "s42", "s43", "s44", "s45", "s46", "s47", "s48", "s49", "s50", "s51", "s52", "s53", "s54", "s55", "s56", "s57", "s58", "s59", "s60", "s61", "vcc", "scc", "memory");
    return o;
}
struct OCtx { u4 r; unsigned sg, ssx, sr, su, sum, nt, hx, p0, p1, p2, p3, p4; unsigned vg, vsx, vu, vum, vx, vt, vpx, vpm, vpr, vo, p5, p6, p7, p8, p9, pa; };
DI void o_asm(const OCtx& c) {
    const u16v s0 = {RFL(c.r[0]), RFL(c.r[1]), RFL(c.r[2]), RFL(c.r[3]), RFL(c.sg), RFL(c.ssx), RFL(c.sr), RFL(c.su), RFL(c.sum), RFL(c.nt), RFL(c.hx), RFL(c.p0), RFL(c.p1), RFL(c.p2), RFL(c.p3), RFL(c.p4)}, v0 = {c.vg, c.vsx, c.vu, c.vum, c.vx, c.vt, c.vpx, c.vpm, c.vpr, c.vo, c.p5, c.p6, c.p7, c.p8, c.p9, c.pa};
    asm volatile(
"s_nop 4\ns_xor_b32 s72, s18, 0\ns_mul_i32 s72, s72, s14\ns_add_u32 s72, s72, s12\n"
"buffer_load_dwordx4 a[0:3], v8, s[8:11], s72 offen offset:0 sc1\nbuffer_load_dwordx4 a[4:7], v8, s[8:11], s72 offen offset:512 sc1\nbuffer_load_dwordx4 a[8:11], v8, s[8:11], s72 offen offset:1024 sc1\nbuffer_load_dwordx4 a[12:15], v8, s[8:11], s72 offen offset:1536 sc1\n"
"buffer_load_dwordx4 a[16:19], v8, s[8:11], s72 offen offset:2048 sc1\nbuffer_load_dwordx4 a[20:23], v8, s[8:11], s72 offen offset:2560 sc1\nbuffer_load_dwordx4 a[24:27], v8, s[8:11], s72 offen offset:3072 sc1\nbuffer_load_dwordx4 a[28:31], v8, s[8:11], s72 offen offset:3584 sc1\n"
"s_add_u32 s72, s72, 4096\nbuffer_load_dwordx4 a[32:35], v8, s[8:11], s72 offen offset:0 sc1\nbuffer_load_dwordx4 a[36:39], v8, s[8:11], s72 offen offset:512 sc1\nbuffer_load_dwordx4 a[40:43], v8, s[8:11], s72 offen offset:1024 sc1\n"
"buffer_load_dwordx4 a[44:47], v8, s[8:11], s72 offen offset:1536 sc1\nbuffer_load_dwordx4 a[48:51], v8, s[8:11], s72 offen offset:2048 sc1\nbuffer_load_dwordx4 a[52:55], v8, s[8:11], s72 offen offset:2560 sc1\nbuffer_load_dwordx4 a[56:59], v8, s[8:11], s72 offen offset:3072 sc1\n"
"buffer_load_dwordx4 a[60:63], v8, s[8:11], s72 offen offset:3584 sc1\ns_add_u32 s72, s72, 4096\nbuffer_load_dwordx4 a[64:67], v8, s[8:11], s72 offen offset:0 sc1\nbuffer_load_dwordx4 a[68:71], v8, s[8:11], s72 offen offset:512 sc1\n"
"buffer_load_dwordx4 a[72:75], v8, s[8:11], s72 offen offset:1024 sc1\nbuffer_load_dwordx4 a[76:79], v8, s[8:11], s72 offen offset:1536 sc1\nbuffer_load_dwordx4 a[80:83], v8, s[8:11], s72 offen offset:2048 sc1\nbuffer_load_dwordx4 a[84:87], v8, s[8:11], s72 offen offset:2560 sc1\n"
"buffer_load_dwordx4 a[88:91], v8, s[8:11], s72 offen offset:3072 sc1\nbuffer_load_dwordx4 a[92:95], v8, s[8:11], s72 offen offset:3584 sc1\ns_add_u32 s72, s72, 4096\nbuffer_load_dwordx4 a[96:99], v8, s[8:11], s72 offen offset:0 sc1\n"
"buffer_load_dwordx4 a[100:103], v8, s[8:11], s72 offen offset:512 sc1\nbuffer_load_dwordx4 a[104:107], v8, s[8:11], s72 offen offset:1024 sc1\nbuffer_load_dwordx4 a[108:111], v8, s[8:11], s72 offen offset:1536 sc1\ns_xor_b32 s73, s18, 0\n"
"s_lshl_b32 s73, s73, 2\ns_mul_i32 s73, s73, s19\ns_add_u32 s73, s73, s13\nbuffer_load_dwordx4 v[64:67], v9, s[8:11], s73 offen sc1\n"
"s_add_u32 s73, s73, s19\nbuffer_load_dwordx4 v[68:71], v9, s[8:11], s73 offen sc1\ns_add_u32 s73, s73, s19\nbuffer_load_dwordx4 v[72:75], v9, s[8:11], s73 offen sc1\n"
"s_add_u32 s73, s73, s19\nbuffer_load_dwordx4 v[76:79], v9, s[8:11], s73 offen sc1\ns_xor_b32 s72, s18, 1\ns_mul_i32 s72, s72, s14\n"
"s_add_u32 s72, s72, s12\nbuffer_load_dwordx4 a[112:115], v8, s[8:11], s72 offen offset:0 sc1\nbuffer_load_dwordx4 a[116:119], v8, s[8:11], s72 offen offset:512 sc1\nbuffer_load_dwordx4 a[120:123], v8, s[8:11], s72 offen offset:1024 sc1\n"
"buffer_load_dwordx4 a[124:127], v8, s[8:11], s72 offen offset:1536 sc1\nbuffer_load_dwordx4 a[128:131], v8, s[8:11], s72 offen offset:2048 sc1\nbuffer_load_dwordx4 a[132:135], v8, s[8:11], s72 offen offset:2560 sc1\nbuffer_load_dwordx4 a[136:139], v8, s[8:11], s72 offen offset:3072 sc1\n"
"buffer_load_dwordx4 a[140:143], v8, s[8:11], s72 offen offset:3584 sc1\ns_add_u32 s72, s72, 4096\nbuffer_load_dwordx4 a[144:147], v8, s[8:11], s72 offen offset:0 sc1\nbuffer_load_dwordx4 a[148:151], v8, s[8:11], s72 offen offset:512 sc1\n"
"buffer_load_dwordx4 a[152:155], v8, s[8:11], s72 offen offset:1024 sc1\nbuffer_load_dwordx4 a[156:159], v8, s[8:11], s72 offen offset:1536 sc1\nbuffer_load_dwordx4 a[160:163], v8, s[8:11], s72 offen offset:2048 sc1\nbuffer_load_dwordx4 a[164:167], v8, s[8:11], s72 offen offset:2560 sc1\n"
"buffer_load_dwordx4 a[168:171], v8, s[8:11], s72 offen offset:3072 sc1\nbuffer_load_dwordx4 a[172:175], v8, s[8:11], s72 offen offset:3584 sc1\ns_add_u32 s72, s72, 4096\nbuffer_load_dwordx4 a[176:179], v8, s[8:11], s72 offen offset:0 sc1\n"
"buffer_load_dwordx4 a[180:183], v8, s[8:11], s72 offen offset:512 sc1\nbuffer_load_dwordx4 a[184:187], v8, s[8:11], s72 offen offset:1024 sc1\nbuffer_load_dwordx4 a[188:191], v8, s[8:11], s72 offen offset:1536 sc1\nbuffer_load_dwordx4 a[192:195], v8, s[8:11], s72 offen offset:2048 sc1\n"
"buffer_load_dwordx4 a[196:199], v8, s[8:11], s72 offen offset:2560 sc1\nbuffer_load_dwordx4 a[200:203], v8, s[8:11], s72 offen offset:3072 sc1\nbuffer_load_dwordx4 a[204:207], v8, s[8:11], s72 offen offset:3584 sc1\ns_add_u32 s72, s72, 4096\n"
"buffer_load_dwordx4 a[208:211], v8, s[8:11], s72 offen offset:0 sc1\nbuffer_load_dwordx4 a[212:215], v8, s[8:11], s72 offen offset:512 sc1\nbuffer_load_dwordx4 a[216:219], v8, s[8:11], s72 offen offset:1024 sc1\nbuffer_load_dwordx4 a[220:223], v8, s[8:11], s72 offen offset:1536 sc1\n"
"s_add_u32 s76, s15, 0\nv_xor_b32 v24, 0, v12\ns_add_u32 s77, s15, 4096\nv_xor_b32 v25, 32, v12\n"
"s_add_u32 s78, s15, 8192\nv_xor_b32 v26, 64, v12\ns_add_u32 s79, s15, 12288\nv_xor_b32 v27, 96, v12\n"
"s_mov_b32 s80, s16\nv_cmp_gt_i32 s[40:41], 0, v13\nv_cmp_gt_i32 s[42:43], 1, v13\nv_cmp_gt_i32 s[44:45], 2, v13\n"
"v_cmp_gt_i32 s[46:47], 3, v13\nv_cmp_gt_i32 s[48:49], 4, v13\nv_cmp_gt_i32 s[50:51], 5, v13\nv_cmp_gt_i32 s[52:53], 6, v13\n"
"v_cmp_gt_i32 s[54:55], 7, v13\nv_cmp_gt_i32 s[56:57], 16, v13\nv_cmp_gt_i32 s[58:59], 17, v13\nv_cmp_gt_i32 s[60:61], 18, v13\n"
"v_cmp_gt_i32 s[62:63], 19, v13\nv_cmp_gt_i32 s[64:65], 20, v13\nv_cmp_gt_i32 s[66:67], 21, v13\nv_cmp_gt_i32 s[68:69], 22, v13\n"
"v_cmp_gt_i32 s[70:71], 23, v13\ns_barrier\nds_read_b128 v[128:131], v24 offset:0\nds_read_b128 v[132:135], v25 offset:0\n"
"ds_read_b128 v[136:139], v26 offset:0\nds_read_b128 v[140:143], v27 offset:0\nds_read_b128 v[144:147], v24 offset:4096\nds_read_b128 v[148:151], v25 offset:4096\n"
"ds_read_b128 v[152:155], v26 offset:4096\nds_read_b128 v[156:159], v27 offset:4096\nds_read_b128 v[160:163], v24 offset:8192\nds_read_b128 v[164:167], v25 offset:8192\n"
"ds_read_b128 v[168:171], v26 offset:8192\nds_read_b128 v[172:175], v27 offset:8192\ns_waitcnt lgkmcnt(0)\nds_read_b128 v[176:179], v24 offset:12288\n"
"ds_read_b128 v[180:183], v25 offset:12288\nds_read_b128 v[184:187], v26 offset:12288\nds_read_b128 v[188:191], v27 offset:12288\nds_read_b128 v[192:195], v24 offset:16384\n"
"ds_read_b128 v[196:199], v25 offset:16384\nds_read_b128 v[200:203], v26 offset:16384\nds_read_b128 v[204:207], v27 offset:16384\nds_read_b128 v[208:211], v24 offset:20480\n"
"ds_read_b128 v[212:215], v25 offset:20480\nds_read_b128 v[216:219], v26 offset:20480\nds_read_b128 v[220:223], v27 offset:20480\ns_waitcnt lgkmcnt(0)\n"
"ds_read_b128 v[224:227], v24 offset:24576\nds_read_b128 v[228:231], v25 offset:24576\nds_read_b128 v[232:235], v26 offset:24576\nds_read_b128 v[236:239], v27 offset:24576\n"
"v_mov_b32 v31, 0xff800000\ns_waitcnt lgkmcnt(0)\ns_waitcnt vmcnt(59)\nv_mfma_f32_32x32x16_bf16 v[40:55], a[0:3], v[128:131], 0\n"
"s_xor_b32 s73, s18, 1\ns_lshl_b32 s73, s73, 2\ns_mul_i32 s73, s73, s19\ns_add_u32 s73, s73, s13\n"
"buffer_load_dwordx4 v[80:83], v9, s[8:11], s73 offen sc1\ns_add_u32 s73, s73, s19\nbuffer_load_dwordx4 v[84:87], v9, s[8:11], s73 offen sc1\ns_add_u32 s73, s73, s19\n"
"buffer_load_dwordx4 v[88:91], v9, s[8:11], s73 offen sc1\ns_add_u32 s73, s73, s19\nbuffer_load_dwordx4 v[92:95], v9, s[8:11], s73 offen sc1\ns_xor_b32 s72, s18, 2\n"
"s_mul_i32 s72, s72, s14\ns_add_u32 s72, s72, s12\nbuffer_load_dwordx4 a[0:3], v8, s[8:11], s72 offen offset:0 sc1\ns_waitcnt vmcnt(63)\n"
"v_mfma_f32_32x32x16_bf16 v[40:55], a[4:7], v[132:135], v[40:55]\nbuffer_load_dwordx4 a[4:7], v8, s[8:11], s72 offen offset:512 sc1\ns_waitcnt vmcnt(63)\nv_mfma_f32_32x32x16_bf16 v[40:55], a[8:11], v[136:139], v[40:55]\n"
"buffer_load_dwordx4 a[8:11], v8, s[8:11], s72 offen offset:1024 sc1\ns_waitcnt vmcnt(63)\nv_mfma_f32_32x32x16_bf16 v[40:55], a[12:15], v[140:143], v[40:55]\nbuffer_load_dwordx4 a[12:15], v8, s[8:11], s72 offen offset:1536 sc1\n"
"s_waitcnt vmcnt(63)\nv_mfma_f32_32x32x16_bf16 v[40:55], a[16:19], v[144:147], v[40:55]\nbuffer_load_dwordx4 a[16:19], v8, s[8:11], s72 offen offset:2048 sc1\ns_waitcnt vmcnt(63)\n"
"v_mfma_f32_32x32x16_bf16 v[40:55], a[20:23], v[148:151], v[40:55]\nbuffer_load_dwordx4 a[20:23], v8, s[8:11], s72 offen offset:2560 sc1\ns_waitcnt vmcnt(63)\nv_mfma_f32_32x32x16_bf16 v[40:55], a[24:27], v[152:155], v[40:55]\n"
"buffer_load_dwordx4 a[24:27], v8, s[8:11], s72 offen offset:3072 sc1\ns_waitcnt vmcnt(63)\nv_mfma_f32_32x32x16_bf16 v[40:55], a[28:31], v[156:159], v[40:55]\nbuffer_load_dwordx4 a[28:31], v8, s[8:11], s72 offen offset:3584 sc1\n"
"s_waitcnt vmcnt(63)\nv_mfma_f32_32x32x16_bf16 v[40:55], a[32:35], v[160:163], v[40:55]\ns_add_u32 s72, s72, 4096\nbuffer_load_dwordx4 a[32:35], v8, s[8:11], s72 offen offset:0 sc1\n"
"s_waitcnt vmcnt(63)\nv_mfma_f32_32x32x16_bf16 v[40:55], a[36:39], v[164:167], v[40:55]\nbuffer_load_dwordx4 a[36:39], v8, s[8:11], s72 offen offset:512 sc1\ns_waitcnt vmcnt(63)\n"
"v_mfma_f32_32x32x16_bf16 v[40:55], a[40:43], v[168:171], v[40:55]\nbuffer_load_dwordx4 a[40:43], v8, s[8:11], s72 offen offset:1024 sc1\ns_waitcnt vmcnt(63)\nv_mfma_f32_32x32x16_bf16 v[40:55], a[44:47], v[172:175], v[40:55]\n"
"buffer_load_dwordx4 a[44:47], v8, s[8:11], s72 offen offset:1536 sc1\ns_waitcnt vmcnt(63)\nv_mfma_f32_32x32x16_bf16 v[40:55], a[48:51], v[176:179], v[40:55]\nbuffer_load_dwordx4 a[48:51], v8, s[8:11], s72 offen offset:2048 sc1\n"
"s_waitcnt vmcnt(63)\nv_mfma_f32_32x32x16_bf16 v[40:55], a[52:55], v[180:183], v[40:55]\nbuffer_load_dwordx4 a[52:55], v8, s[8:11], s72 offen offset:2560 sc1\ns_waitcnt vmcnt(63)\n"
"v_mfma_f32_32x32x16_bf16 v[40:55], a[56:59], v[184:187], v[40:55]\nbuffer_load_dwordx4 a[56:59], v8, s[8:11], s72 offen offset:3072 sc1\ns_waitcnt vmcnt(63)\nv_mfma_f32_32x32x16_bf16 v[40:55], a[60:63], v[188:191], v[40:55]\n"
"buffer_load_dwordx4 a[60:63], v8, s[8:11], s72 offen offset:3584 sc1\ns_waitcnt vmcnt(63)\nv_mfma_f32_32x32x16_bf16 v[40:55], a[64:67], v[192:195], v[40:55]\ns_add_u32 s72, s72, 4096\n"
"buffer_load_dwordx4 a[64:67], v8, s[8:11], s72 offen offset:0 sc1\ns_waitcnt vmcnt(63)\nv_mfma_f32_32x32x16_bf16 v[40:55], a[68:71], v[196:199], v[40:55]\nbuffer_load_dwordx4 a[68:71], v8, s[8:11], s72 offen offset:512 sc1\n"
"s_waitcnt vmcnt(63)\nv_mfma_f32_32x32x16_bf16 v[40:55], a[72:75], v[200:203], v[40:55]\nbuffer_load_dwordx4 a[72:75], v8, s[8:11], s72 offen offset:1024 sc1\ns_waitcnt vmcnt(63)\n"
"v_mfma_f32_32x32x16_bf16 v[40:55], a[76:79], v[204:207], v[40:55]\nbuffer_load_dwordx4 a[76:79], v8, s[8:11], s72 offen offset:1536 sc1\ns_waitcnt vmcnt(63)\nv_mfma_f32_32x32x16_bf16 v[40:55], a[80:83], v[208:211], v[40:55]\n"
"buffer_load_dwordx4 a[80:83], v8, s[8:11], s72 offen offset:2048 sc1\ns_waitcnt vmcnt(63)\nv_mfma_f32_32x32x16_bf16 v[40:55], a[84:87], v[212:215], v[40:55]\nbuffer_load_dwordx4 a[84:87], v8, s[8:11], s72 offen offset:2560 sc1\n"
"s_waitcnt vmcnt(63)\nv_mfma_f32_32x32x16_bf16 v[40:55], a[88:91], v[216:219], v[40:55]\nbuffer_load_dwordx4 a[88:91], v8, s[8:11], s72 offen offset:3072 sc1\ns_waitcnt vmcnt(63)\n"
"v_mfma_f32_32x32x16_bf16 v[40:55], a[92:95], v[220:223], v[40:55]\nbuffer_load_dwordx4 a[92:95], v8, s[8:11], s72 offen offset:3584 sc1\ns_waitcnt vmcnt(63)\nv_mfma_f32_32x32x16_bf16 v[40:55], a[96:99], v[224:227], v[40:55]\n"
"s_add_u32 s72, s72, 4096\nbuffer_load_dwordx4 a[96:99], v8, s[8:11], s72 offen offset:0 sc1\ns_waitcnt vmcnt(63)\nv_mfma_f32_32x32x16_bf16 v[40:55], a[100:103], v[228:231], v[40:55]\n"
"buffer_load_dwordx4 a[100:103], v8, s[8:11], s72 offen offset:512 sc1\ns_waitcnt vmcnt(63)\nv_mfma_f32_32x32x16_bf16 v[40:55], a[104:107], v[232:235], v[40:55]\nbuffer_load_dwordx4 a[104:107], v8, s[8:11], s72 offen offset:1024 sc1\n"
"s_waitcnt vmcnt(63)\nv_mfma_f32_32x32x16_bf16 v[40:55], a[108:111], v[236:239], v[40:55]\nbuffer_load_dwordx4 a[108:111], v8, s[8:11], s72 offen offset:1536 sc1\ns_waitcnt vmcnt(60)\n"
"v_add_f32 v64, v64, v68\nv_add_f32 v72, v72, v76\nv_add_f32 v65, v65, v69\nv_add_f32 v73, v73, v77\n"
"v_add_f32 v66, v66, v70\nv_add_f32 v74, v74, v78\nv_add_f32 v67, v67, v71\nv_add_f32 v75, v75, v79\n"
"v_add_f32 v64, v64, v72\nv_add_f32 v65, v65, v73\nv_add_f32 v66, v66, v74\nv_add_f32 v67, v67, v75\n"
"v_max3_f32 v28, v64, v65, v66\nv_max_f32 v28, v28, v67\nv_cndmask_b32 v40, v40, v31, s[40:41]\nv_cndmask_b32 v41, v41, v31, s[42:43]\n"
"v_cndmask_b32 v42, v42, v31, s[44:45]\nv_cndmask_b32 v43, v43, v31, s[46:47]\nv_cndmask_b32 v44, v44, v31, s[48:49]\nv_cndmask_b32 v45, v45, v31, s[50:51]\n"
"v_cndmask_b32 v46, v46, v31, s[52:53]\nv_cndmask_b32 v47, v47, v31, s[54:55]\nv_cndmask_b32 v48, v48, v31, s[56:57]\nv_cndmask_b32 v49, v49, v31, s[58:59]\n"
"v_cndmask_b32 v50, v50, v31, s[60:61]\nv_cndmask_b32 v51, v51, v31, s[62:63]\nv_cndmask_b32 v52, v52, v31, s[64:65]\nv_cndmask_b32 v53, v53, v31, s[66:67]\n"
"v_cndmask_b32 v54, v54, v31, s[68:69]\nv_cndmask_b32 v55, v55, v31, s[70:71]\nv_max3_f32 v28, v28, v40, v41\nv_max3_f32 v28, v28, v42, v43\n"
"v_max3_f32 v28, v28, v44, v45\nv_max3_f32 v28, v28, v46, v47\nv_max3_f32 v28, v28, v48, v49\nv_max3_f32 v28, v28, v50, v51\n"
"v_max3_f32 v28, v28, v52, v53\nv_max3_f32 v28, v28, v54, v55\nv_mov_b32 v30, v28\ns_nop 1\n"
"v_permlane32_swap_b32 v28, v30\nv_max_f32 v28, v28, v30\nv_sub_f32 v40, v40, v28\nv_sub_f32 v41, v41, v28\n"
"v_sub_f32 v42, v42, v28\nv_sub_f32 v43, v43, v28\nv_sub_f32 v44, v44, v28\nv_sub_f32 v45, v45, v28\n"
"v_sub_f32 v46, v46, v28\nv_sub_f32 v47, v47, v28\nv_sub_f32 v48, v48, v28\nv_sub_f32 v49, v49, v28\n"
"v_sub_f32 v50, v50, v28\nv_sub_f32 v51, v51, v28\nv_sub_f32 v52, v52, v28\nv_sub_f32 v53, v53, v28\n"
"v_sub_f32 v54, v54, v28\nv_sub_f32 v55, v55, v28\nv_sub_f32 v64, v64, v28\nv_sub_f32 v65, v65, v28\n"
"v_sub_f32 v66, v66, v28\nv_sub_f32 v67, v67, v28\nv_exp_f32 v40, v40\nv_exp_f32 v41, v41\n"
"v_exp_f32 v42, v42\nv_exp_f32 v43, v43\nv_exp_f32 v44, v44\nv_exp_f32 v45, v45\n"
"v_exp_f32 v46, v46\nv_exp_f32 v47, v47\nv_exp_f32 v48, v48\nv_exp_f32 v49, v49\n"
"v_exp_f32 v50, v50\nv_exp_f32 v51, v51\nv_exp_f32 v52, v52\nv_exp_f32 v53, v53\n"
"v_exp_f32 v54, v54\nv_exp_f32 v55, v55\nv_exp_f32 v64, v64\nv_exp_f32 v65, v65\n"
"v_exp_f32 v66, v66\nv_exp_f32 v67, v67\nv_add_f32 v29, v40, v41\nv_add_f32 v30, v42, v43\n"
"v_add_f32 v29, v29, v44\nv_add_f32 v30, v30, v45\nv_add_f32 v29, v29, v46\nv_add_f32 v30, v30, v47\n"
"v_add_f32 v29, v29, v48\nv_add_f32 v30, v30, v49\nv_add_f32 v29, v29, v50\nv_add_f32 v30, v30, v51\n"
"v_add_f32 v29, v29, v52\nv_add_f32 v30, v30, v53\nv_add_f32 v29, v29, v54\nv_add_f32 v30, v30, v55\n"
"v_add_f32 v29, v29, v64\nv_add_f32 v30, v30, v65\nv_add_f32 v29, v29, v66\nv_add_f32 v30, v30, v67\n"
"v_add_f32 v29, v29, v30\nv_mov_b32 v30, v29\ns_nop 1\nv_permlane32_swap_b32 v29, v30\n"
"v_add_f32 v29, v29, v30\nv_rcp_f32 v29, v29\ns_nop 0\nv_mul_f32 v40, v40, v29\n"
"v_mul_f32 v41, v41, v29\nv_mul_f32 v42, v42, v29\nv_mul_f32 v43, v43, v29\nv_mul_f32 v44, v44, v29\n"
"v_mul_f32 v45, v45, v29\nv_mul_f32 v46, v46, v29\nv_mul_f32 v47, v47, v29\nv_mul_f32 v48, v48, v29\n"
"v_mul_f32 v49, v49, v29\nv_mul_f32 v50, v50, v29\nv_mul_f32 v51, v51, v29\nv_mul_f32 v52, v52, v29\n"
"v_mul_f32 v53, v53, v29\nv_mul_f32 v54, v54, v29\nv_mul_f32 v55, v55, v29\nv_mul_f32 v64, v64, v29\n"
"v_mul_f32 v65, v65, v29\nv_mul_f32 v66, v66, v29\nv_mul_f32 v67, v67, v29\nv_cvt_pk_bf16_f32 v56, v40, v41\n"
"v_cvt_pk_bf16_f32 v57, v42, v43\nv_cvt_pk_bf16_f32 v58, v44, v45\nv_cvt_pk_bf16_f32 v59, v46, v47\nv_cvt_pk_bf16_f32 v60, v48, v49\n"
"v_cvt_pk_bf16_f32 v61, v50, v51\nv_cvt_pk_bf16_f32 v62, v52, v53\nv_cvt_pk_bf16_f32 v63, v54, v55\nv_cvt_pk_bf16_f32 v32, v64, v65\n"
"v_cvt_pk_bf16_f32 v36, v66, v67\nv_xor_b32 v30, 0, v14\nds_write_b128 v30, v[56:59]\nds_write_b128 v30, v[60:63] offset:1024\n"
"s_waitcnt vmcnt(59)\nv_mfma_f32_32x32x16_bf16 v[40:55], a[112:115], v[128:131], 0\ns_xor_b32 s73, s18, 2\ns_lshl_b32 s73, s73, 2\n"
"s_mul_i32 s73, s73, s19\ns_add_u32 s73, s73, s13\nbuffer_load_dwordx4 v[96:99], v9, s[8:11], s73 offen sc1\ns_add_u32 s73, s73, s19\n"
"buffer_load_dwordx4 v[100:103], v9, s[8:11], s73 offen sc1\ns_add_u32 s73, s73, s19\nbuffer_load_dwordx4 v[104:107], v9, s[8:11], s73 offen sc1\ns_add_u32 s73, s73, s19\n"
"buffer_load_dwordx4 v[108:111], v9, s[8:11], s73 offen sc1\ns_xor_b32 s72, s18, 3\ns_mul_i32 s72, s72, s14\ns_add_u32 s72, s72, s12\n"
"buffer_load_dwordx4 a[112:115], v8, s[8:11], s72 offen offset:0 sc1\ns_waitcnt vmcnt(63)\nv_mfma_f32_32x32x16_bf16 v[40:55], a[116:119], v[132:135], v[40:55]\nbuffer_load_dwordx4 a[116:119], v8, s[8:11], s72 offen offset:512 sc1\n"
"s_waitcnt vmcnt(63)\nv_mfma_f32_32x32x16_bf16 v[40:55], a[120:123], v[136:139], v[40:55]\nbuffer_load_dwordx4 a[120:123], v8, s[8:11], s72 offen offset:1024 sc1\ns_waitcnt vmcnt(63)\n"
"v_mfma_f32_32x32x16_bf16 v[40:55], a[124:127], v[140:143], v[40:55]\nbuffer_load_dwordx4 a[124:127], v8, s[8:11], s72 offen offset:1536 sc1\ns_waitcnt vmcnt(63)\nv_mfma_f32_32x32x16_bf16 v[40:55], a[128:131], v[144:147], v[40:55]\n"
"buffer_load_dwordx4 a[128:131], v8, s[8:11], s72 offen offset:2048 sc1\ns_waitcnt vmcnt(63)\nv_mfma_f32_32x32x16_bf16 v[40:55], a[132:135], v[148:151], v[40:55]\nbuffer_load_dwordx4 a[132:135], v8, s[8:11], s72 offen offset:2560 sc1\n"
"s_waitcnt vmcnt(63)\nv_mfma_f32_32x32x16_bf16 v[40:55], a[136:139], v[152:155], v[40:55]\nbuffer_load_dwordx4 a[136:139], v8, s[8:11], s72 offen offset:3072 sc1\ns_waitcnt vmcnt(63)\n"
"v_mfma_f32_32x32x16_bf16 v[40:55], a[140:143], v[156:159], v[40:55]\nbuffer_load_dwordx4 a[140:143], v8, s[8:11], s72 offen offset:3584 sc1\ns_waitcnt vmcnt(63)\nv_mfma_f32_32x32x16_bf16 v[40:55], a[144:147], v[160:163], v[40:55]\n"
"s_add_u32 s72, s72, 4096\nbuffer_load_dwordx4 a[144:147], v8, s[8:11], s72 offen offset:0 sc1\ns_waitcnt vmcnt(63)\nv_mfma_f32_32x32x16_bf16 v[40:55], a[148:151], v[164:167], v[40:55]\n"
"buffer_load_dwordx4 a[148:151], v8, s[8:11], s72 offen offset:512 sc1\ns_waitcnt vmcnt(63)\nv_mfma_f32_32x32x16_bf16 v[40:55], a[152:155], v[168:171], v[40:55]\nbuffer_load_dwordx4 a[152:155], v8, s[8:11], s72 offen offset:1024 sc1\n"
"s_waitcnt vmcnt(63)\nv_mfma_f32_32x32x16_bf16 v[40:55], a[156:159], v[172:175], v[40:55]\nbuffer_load_dwordx4 a[156:159], v8, s[8:11], s72 offen offset:1536 sc1\ns_waitcnt vmcnt(63)\n"
"v_mfma_f32_32x32x16_bf16 v[40:55], a[160:163], v[176:179], v[40:55]\nbuffer_load_dwordx4 a[160:163], v8, s[8:11], s72 offen offset:2048 sc1\ns_waitcnt vmcnt(63)\nv_mfma_f32_32x32x16_bf16 v[40:55], a[164:167], v[180:183], v[40:55]\n"
"buffer_load_dwordx4 a[164:167], v8, s[8:11], s72 offen offset:2560 sc1\ns_waitcnt vmcnt(63)\nv_mfma_f32_32x32x16_bf16 v[40:55], a[168:171], v[184:187], v[40:55]\nbuffer_load_dwordx4 a[168:171], v8, s[8:11], s72 offen offset:3072 sc1\n"
"s_waitcnt vmcnt(63)\nv_mfma_f32_32x32x16_bf16 v[40:55], a[172:175], v[188:191], v[40:55]\nbuffer_load_dwordx4 a[172:175], v8, s[8:11], s72 offen offset:3584 sc1\ns_waitcnt vmcnt(63)\n"
"v_mfma_f32_32x32x16_bf16 v[40:55], a[176:179], v[192:195], v[40:55]\ns_add_u32 s72, s72, 4096\nbuffer_load_dwordx4 a[176:179], v8, s[8:11], s72 offen offset:0 sc1\ns_waitcnt vmcnt(63)\n"
"v_mfma_f32_32x32x16_bf16 v[40:55], a[180:183], v[196:199], v[40:55]\nbuffer_load_dwordx4 a[180:183], v8, s[8:11], s72 offen offset:512 sc1\ns_waitcnt vmcnt(63)\nv_mfma_f32_32x32x16_bf16 v[40:55], a[184:187], v[200:203], v[40:55]\n"
"buffer_load_dwordx4 a[184:187], v8, s[8:11], s72 offen offset:1024 sc1\ns_waitcnt vmcnt(63)\nv_mfma_f32_32x32x16_bf16 v[40:55], a[188:191], v[204:207], v[40:55]\nbuffer_load_dwordx4 a[188:191], v8, s[8:11], s72 offen offset:1536 sc1\n"
"s_waitcnt vmcnt(63)\nv_mfma_f32_32x32x16_bf16 v[40:55], a[192:195], v[208:211], v[40:55]\nbuffer_load_dwordx4 a[192:195], v8, s[8:11], s72 offen offset:2048 sc1\ns_waitcnt vmcnt(63)\n"
"v_mfma_f32_32x32x16_bf16 v[40:55], a[196:199], v[212:215], v[40:55]\nbuffer_load_dwordx4 a[196:199], v8, s[8:11], s72 offen offset:2560 sc1\ns_waitcnt vmcnt(63)\nv_mfma_f32_32x32x16_bf16 v[40:55], a[200:203], v[216:219], v[40:55]\n"
"buffer_load_dwordx4 a[200:203], v8, s[8:11], s72 offen offset:3072 sc1\ns_waitcnt vmcnt(63)\nv_mfma_f32_32x32x16_bf16 v[40:55], a[204:207], v[220:223], v[40:55]\nbuffer_load_dwordx4 a[204:207], v8, s[8:11], s72 offen offset:3584 sc1\n"
"s_waitcnt vmcnt(63)\nv_mfma_f32_32x32x16_bf16 v[40:55], a[208:211], v[224:227], v[40:55]\ns_add_u32 s72, s72, 4096\nbuffer_load_dwordx4 a[208:211], v8, s[8:11], s72 offen offset:0 sc1\n"
"s_waitcnt vmcnt(63)\nv_mfma_f32_32x32x16_bf16 v[40:55], a[212:215], v[228:231], v[40:55]\nbuffer_load_dwordx4 a[212:215], v8, s[8:11], s72 offen offset:512 sc1\ns_waitcnt vmcnt(63)\n"
"v_mfma_f32_32x32x16_bf16 v[40:55], a[216:219], v[232:235], v[40:55]\nbuffer_load_dwordx4 a[216:219], v8, s[8:11], s72 offen offset:1024 sc1\ns_waitcnt vmcnt(63)\nv_mfma_f32_32x32x16_bf16 v[40:55], a[220:223], v[236:239], v[40:55]\n"
"buffer_load_dwordx4 a[220:223], v8, s[8:11], s72 offen offset:1536 sc1\ns_waitcnt vmcnt(60)\nv_add_f32 v80, v80, v84\nv_add_f32 v88, v88, v92\n"
"v_add_f32 v81, v81, v85\nv_add_f32 v89, v89, v93\nv_add_f32 v82, v82, v86\nv_add_f32 v90, v90, v94\n"
"v_add_f32 v83, v83, v87\nv_add_f32 v91, v91, v95\nv_add_f32 v80, v80, v88\nv_add_f32 v81, v81, v89\n"
"v_add_f32 v82, v82, v90\nv_add_f32 v83, v83, v91\nv_max3_f32 v28, v80, v81, v82\nv_max_f32 v28, v28, v83\n"
"v_cndmask_b32 v40, v40, v31, s[40:41]\nv_cndmask_b32 v41, v41, v31, s[42:43]\nv_cndmask_b32 v42, v42, v31, s[44:45]\nv_cndmask_b32 v43, v43, v31, s[46:47]\n"
"v_cndmask_b32 v44, v44, v31, s[48:49]\nv_cndmask_b32 v45, v45, v31, s[50:51]\nv_cndmask_b32 v46, v46, v31, s[52:53]\nv_cndmask_b32 v47, v47, v31, s[54:55]\n"
"v_cndmask_b32 v48, v48, v31, s[56:57]\nv_cndmask_b32 v49, v49, v31, s[58:59]\nv_cndmask_b32 v50, v50, v31, s[60:61]\nv_cndmask_b32 v51, v51, v31, s[62:63]\n"
"v_cndmask_b32 v52, v52, v31, s[64:65]\nv_cndmask_b32 v53, v53, v31, s[66:67]\nv_cndmask_b32 v54, v54, v31, s[68:69]\nv_cndmask_b32 v55, v55, v31, s[70:71]\n"
"v_max3_f32 v28, v28, v40, v41\nv_max3_f32 v28, v28, v42, v43\nv_max3_f32 v28, v28, v44, v45\nv_max3_f32 v28, v28, v46, v47\n"
"v_max3_f32 v28, v28, v48, v49\nv_max3_f32 v28, v28, v50, v51\nv_max3_f32 v28, v28, v52, v53\nv_max3_f32 v28, v28, v54, v55\n"
"v_mov_b32 v30, v28\ns_nop 1\nv_permlane32_swap_b32 v28, v30\nv_max_f32 v28, v28, v30\n"
"v_sub_f32 v40, v40, v28\nv_sub_f32 v41, v41, v28\nv_sub_f32 v42, v42, v28\nv_sub_f32 v43, v43, v28\n"
"v_sub_f32 v44, v44, v28\nv_sub_f32 v45, v45, v28\nv_sub_f32 v46, v46, v28\nv_sub_f32 v47, v47, v28\n"
"v_sub_f32 v48, v48, v28\nv_sub_f32 v49, v49, v28\nv_sub_f32 v50, v50, v28\nv_sub_f32 v51, v51, v28\n"
"v_sub_f32 v52, v52, v28\nv_sub_f32 v53, v53, v28\nv_sub_f32 v54, v54, v28\nv_sub_f32 v55, v55, v28\n"
"v_sub_f32 v80, v80, v28\nv_sub_f32 v81, v81, v28\nv_sub_f32 v82, v82, v28\nv_sub_f32 v83, v83, v28\n"
"v_exp_f32 v40, v40\nv_exp_f32 v41, v41\nv_exp_f32 v42, v42\nv_exp_f32 v43, v43\n"
"v_exp_f32 v44, v44\nv_exp_f32 v45, v45\nv_exp_f32 v46, v46\nv_exp_f32 v47, v47\n"
"v_exp_f32 v48, v48\nv_exp_f32 v49, v49\nv_exp_f32 v50, v50\nv_exp_f32 v51, v51\n"
"v_exp_f32 v52, v52\nv_exp_f32 v53, v53\nv_exp_f32 v54, v54\nv_exp_f32 v55, v55\n"
"v_exp_f32 v80, v80\nv_exp_f32 v81, v81\nv_exp_f32 v82, v82\nv_exp_f32 v83, v83\n"
"v_add_f32 v29, v40, v41\nv_add_f32 v30, v42, v43\nv_add_f32 v29, v29, v44\nv_add_f32 v30, v30, v45\n"
"v_add_f32 v29, v29, v46\nv_add_f32 v30, v30, v47\nv_add_f32 v29, v29, v48\nv_add_f32 v30, v30, v49\n"
"v_add_f32 v29, v29, v50\nv_add_f32 v30, v30, v51\nv_add_f32 v29, v29, v52\nv_add_f32 v30, v30, v53\n"
"v_add_f32 v29, v29, v54\nv_add_f32 v30, v30, v55\nv_add_f32 v29, v29, v80\nv_add_f32 v30, v30, v81\n"
"v_add_f32 v29, v29, v82\nv_add_f32 v30, v30, v83\nv_add_f32 v29, v29, v30\nv_mov_b32 v30, v29\n"
"s_nop 1\nv_permlane32_swap_b32 v29, v30\nv_add_f32 v29, v29, v30\nv_rcp_f32 v29, v29\n"
"s_nop 0\nv_mul_f32 v40, v40, v29\nv_mul_f32 v41, v41, v29\nv_mul_f32 v42, v42, v29\n"
"v_mul_f32 v43, v43, v29\nv_mul_f32 v44, v44, v29\nv_mul_f32 v45, v45, v29\nv_mul_f32 v46, v46, v29\n"
"v_mul_f32 v47, v47, v29\nv_mul_f32 v48, v48, v29\nv_mul_f32 v49, v49, v29\nv_mul_f32 v50, v50, v29\n"
"v_mul_f32 v51, v51, v29\nv_mul_f32 v52, v52, v29\nv_mul_f32 v53, v53, v29\nv_mul_f32 v54, v54, v29\n"
"v_mul_f32 v55, v55, v29\nv_mul_f32 v80, v80, v29\nv_mul_f32 v81, v81, v29\nv_mul_f32 v82, v82, v29\n"
"v_mul_f32 v83, v83, v29\nv_cvt_pk_bf16_f32 v56, v40, v41\nv_cvt_pk_bf16_f32 v57, v42, v43\nv_cvt_pk_bf16_f32 v58, v44, v45\n"
"v_cvt_pk_bf16_f32 v59, v46, v47\nv_cvt_pk_bf16_f32 v60, v48, v49\nv_cvt_pk_bf16_f32 v61, v50, v51\nv_cvt_pk_bf16_f32 v62, v52, v53\n"
"v_cvt_pk_bf16_f32 v63, v54, v55\nv_cvt_pk_bf16_f32 v33, v80, v81\nv_cvt_pk_bf16_f32 v37, v82, v83\nv_xor_b32 v30, 2048, v14\n"
"ds_write_b128 v30, v[56:59]\nds_write_b128 v30, v[60:63] offset:1024\ns_waitcnt vmcnt(59)\nv_mfma_f32_32x32x16_bf16 v[40:55], a[0:3], v[128:131], 0\n"
"s_xor_b32 s73, s18, 3\ns_lshl_b32 s73, s73, 2\ns_mul_i32 s73, s73, s19\ns_add_u32 s73, s73, s13\n"
"buffer_load_dwordx4 v[112:115], v9, s[8:11], s73 offen sc1\ns_add_u32 s73, s73, s19\nbuffer_load_dwordx4 v[116:119], v9, s[8:11], s73 offen sc1\ns_add_u32 s73, s73, s19\n"
"buffer_load_dwordx4 v[120:123], v9, s[8:11], s73 offen sc1\ns_add_u32 s73, s73, s19\nbuffer_load_dwordx4 v[124:127], v9, s[8:11], s73 offen sc1\nbuffer_load_dwordx4 a[0:3], v10, s[8:11], s76 offen offset:0 sc1\n"
"s_waitcnt vmcnt(63)\nv_mfma_f32_32x32x16_bf16 v[40:55], a[4:7], v[132:135], v[40:55]\nbuffer_load_dwordx4 a[4:7], v10, s[8:11], s76 offen offset:512 sc1\ns_waitcnt vmcnt(63)\n"
"v_mfma_f32_32x32x16_bf16 v[40:55], a[8:11], v[136:139], v[40:55]\nbuffer_load_dwordx4 a[8:11], v10, s[8:11], s76 offen offset:1024 sc1\ns_waitcnt vmcnt(63)\nv_mfma_f32_32x32x16_bf16 v[40:55], a[12:15], v[140:143], v[40:55]\n"
"buffer_load_dwordx4 a[12:15], v10, s[8:11], s76 offen offset:1536 sc1\ns_waitcnt vmcnt(63)\nv_mfma_f32_32x32x16_bf16 v[40:55], a[16:19], v[144:147], v[40:55]\nbuffer_load_dwordx4 a[16:19], v10, s[8:11], s76 offen offset:2048 sc1\n"
"s_waitcnt vmcnt(63)\nv_mfma_f32_32x32x16_bf16 v[40:55], a[20:23], v[148:151], v[40:55]\nbuffer_load_dwordx4 a[20:23], v10, s[8:11], s76 offen offset:2560 sc1\ns_waitcnt vmcnt(63)\n"
"v_mfma_f32_32x32x16_bf16 v[40:55], a[24:27], v[152:155], v[40:55]\nbuffer_load_dwordx4 a[24:27], v10, s[8:11], s76 offen offset:3072 sc1\ns_waitcnt vmcnt(63)\nv_mfma_f32_32x32x16_bf16 v[40:55], a[28:31], v[156:159], v[40:55]\n"
"buffer_load_dwordx4 a[28:31], v10, s[8:11], s76 offen offset:3584 sc1\ns_waitcnt vmcnt(63)\nv_mfma_f32_32x32x16_bf16 v[40:55], a[32:35], v[160:163], v[40:55]\nbuffer_load_dwordx4 a[32:35], v10, s[8:11], s77 offen offset:0 sc1\n"
"s_waitcnt vmcnt(63)\nv_mfma_f32_32x32x16_bf16 v[40:55], a[36:39], v[164:167], v[40:55]\nbuffer_load_dwordx4 a[36:39], v10, s[8:11], s77 offen offset:512 sc1\ns_waitcnt vmcnt(63)\n"
"v_mfma_f32_32x32x16_bf16 v[40:55], a[40:43], v[168:171], v[40:55]\nbuffer_load_dwordx4 a[40:43], v10, s[8:11], s77 offen offset:1024 sc1\ns_waitcnt vmcnt(63)\nv_mfma_f32_32x32x16_bf16 v[40:55], a[44:47], v[172:175], v[40:55]\n"
"buffer_load_dwordx4 a[44:47], v10, s[8:11], s77 offen offset:1536 sc1\ns_waitcnt vmcnt(63)\nv_mfma_f32_32x32x16_bf16 v[40:55], a[48:51], v[176:179], v[40:55]\nbuffer_load_dwordx4 a[48:51], v10, s[8:11], s77 offen offset:2048 sc1\n"
"s_waitcnt vmcnt(63)\nv_mfma_f32_32x32x16_bf16 v[40:55], a[52:55], v[180:183], v[40:55]\nbuffer_load_dwordx4 a[52:55], v10, s[8:11], s77 offen offset:2560 sc1\ns_waitcnt vmcnt(63)\n"
"v_mfma_f32_32x32x16_bf16 v[40:55], a[56:59], v[184:187], v[40:55]\nbuffer_load_dwordx4 a[56:59], v10, s[8:11], s77 offen offset:3072 sc1\ns_waitcnt vmcnt(63)\nv_mfma_f32_32x32x16_bf16 v[40:55], a[60:63], v[188:191], v[40:55]\n"
"buffer_load_dwordx4 a[60:63], v10, s[8:11], s77 offen offset:3584 sc1\ns_waitcnt vmcnt(63)\nv_mfma_f32_32x32x16_bf16 v[40:55], a[64:67], v[192:195], v[40:55]\nbuffer_load_dwordx4 a[64:67], v10, s[8:11], s78 offen offset:0 sc1\n"
"s_waitcnt vmcnt(63)\nv_mfma_f32_32x32x16_bf16 v[40:55], a[68:71], v[196:199], v[40:55]\nbuffer_load_dwordx4 a[68:71], v10, s[8:11], s78 offen offset:512 sc1\ns_waitcnt vmcnt(63)\n"
"v_mfma_f32_32x32x16_bf16 v[40:55], a[72:75], v[200:203], v[40:55]\nbuffer_load_dwordx4 a[72:75], v10, s[8:11], s78 offen offset:1024 sc1\ns_waitcnt vmcnt(63)\nv_mfma_f32_32x32x16_bf16 v[40:55], a[76:79], v[204:207], v[40:55]\n"
"buffer_load_dwordx4 a[76:79], v10, s[8:11], s78 offen offset:1536 sc1\ns_waitcnt vmcnt(63)\nv_mfma_f32_32x32x16_bf16 v[40:55], a[80:83], v[208:211], v[40:55]\nbuffer_load_dwordx4 a[80:83], v10, s[8:11], s78 offen offset:2048 sc1\n"
"s_waitcnt vmcnt(63)\nv_mfma_f32_32x32x16_bf16 v[40:55], a[84:87], v[212:215], v[40:55]\nbuffer_load_dwordx4 a[84:87], v10, s[8:11], s78 offen offset:2560 sc1\ns_waitcnt vmcnt(63)\n"
"v_mfma_f32_32x32x16_bf16 v[40:55], a[88:91], v[216:219], v[40:55]\nbuffer_load_dwordx4 a[88:91], v10, s[8:11], s78 offen offset:3072 sc1\ns_waitcnt vmcnt(63)\nv_mfma_f32_32x32x16_bf16 v[40:55], a[92:95], v[220:223], v[40:55]\n"
"buffer_load_dwordx4 a[92:95], v10, s[8:11], s78 offen offset:3584 sc1\ns_waitcnt vmcnt(63)\nv_mfma_f32_32x32x16_bf16 v[40:55], a[96:99], v[224:227], v[40:55]\nbuffer_load_dwordx4 a[96:99], v10, s[8:11], s79 offen offset:0 sc1\n"
"s_waitcnt vmcnt(63)\nv_mfma_f32_32x32x16_bf16 v[40:55], a[100:103], v[228:231], v[40:55]\nbuffer_load_dwordx4 a[100:103], v10, s[8:11], s79 offen offset:512 sc1\ns_waitcnt vmcnt(63)\n"
"v_mfma_f32_32x32x16_bf16 v[40:55], a[104:107], v[232:235], v[40:55]\nbuffer_load_dwordx4 a[104:107], v10, s[8:11], s79 offen offset:1024 sc1\ns_waitcnt vmcnt(63)\nv_mfma_f32_32x32x16_bf16 v[40:55], a[108:111], v[236:239], v[40:55]\n"
"buffer_load_dwordx4 a[108:111], v10, s[8:11], s79 offen offset:1536 sc1\ns_waitcnt vmcnt(60)\nv_add_f32 v96, v96, v100\nv_add_f32 v104, v104, v108\n"
"v_add_f32 v97, v97, v101\nv_add_f32 v105, v105, v109\nv_add_f32 v98, v98, v102\nv_add_f32 v106, v106, v110\n"
"v_add_f32 v99, v99, v103\nv_add_f32 v107, v107, v111\nv_add_f32 v96, v96, v104\nv_add_f32 v97, v97, v105\n"
"v_add_f32 v98, v98, v106\nv_add_f32 v99, v99, v107\nv_max3_f32 v28, v96, v97, v98\nv_max_f32 v28, v28, v99\n"
"v_cndmask_b32 v40, v40, v31, s[40:41]\nv_cndmask_b32 v41, v41, v31, s[42:43]\nv_cndmask_b32 v42, v42, v31, s[44:45]\nv_cndmask_b32 v43, v43, v31, s[46:47]\n"
"v_cndmask_b32 v44, v44, v31, s[48:49]\nv_cndmask_b32 v45, v45, v31, s[50:51]\nv_cndmask_b32 v46, v46, v31, s[52:53]\nv_cndmask_b32 v47, v47, v31, s[54:55]\n"
"v_cndmask_b32 v48, v48, v31, s[56:57]\nv_cndmask_b32 v49, v49, v31, s[58:59]\nv_cndmask_b32 v50, v50, v31, s[60:61]\nv_cndmask_b32 v51, v51, v31, s[62:63]\n"
"v_cndmask_b32 v52, v52, v31, s[64:65]\nv_cndmask_b32 v53, v53, v31, s[66:67]\nv_cndmask_b32 v54, v54, v31, s[68:69]\nv_cndmask_b32 v55, v55, v31, s[70:71]\n"
"v_max3_f32 v28, v28, v40, v41\nv_max3_f32 v28, v28, v42, v43\nv_max3_f32 v28, v28, v44, v45\nv_max3_f32 v28, v28, v46, v47\n"
"v_max3_f32 v28, v28, v48, v49\nv_max3_f32 v28, v28, v50, v51\nv_max3_f32 v28, v28, v52, v53\nv_max3_f32 v28, v28, v54, v55\n"
"v_mov_b32 v30, v28\ns_nop 1\nv_permlane32_swap_b32 v28, v30\nv_max_f32 v28, v28, v30\n"
"v_sub_f32 v40, v40, v28\nv_sub_f32 v41, v41, v28\nv_sub_f32 v42, v42, v28\nv_sub_f32 v43, v43, v28\n"
"v_sub_f32 v44, v44, v28\nv_sub_f32 v45, v45, v28\nv_sub_f32 v46, v46, v28\nv_sub_f32 v47, v47, v28\n"
"v_sub_f32 v48, v48, v28\nv_sub_f32 v49, v49, v28\nv_sub_f32 v50, v50, v28\nv_sub_f32 v51, v51, v28\n"
"v_sub_f32 v52, v52, v28\nv_sub_f32 v53, v53, v28\nv_sub_f32 v54, v54, v28\nv_sub_f32 v55, v55, v28\n"
"v_sub_f32 v96, v96, v28\nv_sub_f32 v97, v97, v28\nv_sub_f32 v98, v98, v28\nv_sub_f32 v99, v99, v28\n"
"v_exp_f32 v40, v40\nv_exp_f32 v41, v41\nv_exp_f32 v42, v42\nv_exp_f32 v43, v43\n"
"v_exp_f32 v44, v44\nv_exp_f32 v45, v45\nv_exp_f32 v46, v46\nv_exp_f32 v47, v47\n"
"v_exp_f32 v48, v48\nv_exp_f32 v49, v49\nv_exp_f32 v50, v50\nv_exp_f32 v51, v51\n"
"v_exp_f32 v52, v52\nv_exp_f32 v53, v53\nv_exp_f32 v54, v54\nv_exp_f32 v55, v55\n"
"v_exp_f32 v96, v96\nv_exp_f32 v97, v97\nv_exp_f32 v98, v98\nv_exp_f32 v99, v99\n"
"v_add_f32 v29, v40, v41\nv_add_f32 v30, v42, v43\nv_add_f32 v29, v29, v44\nv_add_f32 v30, v30, v45\n"
"v_add_f32 v29, v29, v46\nv_add_f32 v30, v30, v47\nv_add_f32 v29, v29, v48\nv_add_f32 v30, v30, v49\n"
"v_add_f32 v29, v29, v50\nv_add_f32 v30, v30, v51\nv_add_f32 v29, v29, v52\nv_add_f32 v30, v30, v53\n"
"v_add_f32 v29, v29, v54\nv_add_f32 v30, v30, v55\nv_add_f32 v29, v29, v96\nv_add_f32 v30, v30, v97\n"
"v_add_f32 v29, v29, v98\nv_add_f32 v30, v30, v99\nv_add_f32 v29, v29, v30\nv_mov_b32 v30, v29\n"
"s_nop 1\nv_permlane32_swap_b32 v29, v30\nv_add_f32 v29, v29, v30\nv_rcp_f32 v29, v29\n"
"s_nop 0\nv_mul_f32 v40, v40, v29\nv_mul_f32 v41, v41, v29\nv_mul_f32 v42, v42, v29\n"
"v_mul_f32 v43, v43, v29\nv_mul_f32 v44, v44, v29\nv_mul_f32 v45, v45, v29\nv_mul_f32 v46, v46, v29\n"
"v_mul_f32 v47, v47, v29\nv_mul_f32 v48, v48, v29\nv_mul_f32 v49, v49, v29\nv_mul_f32 v50, v50, v29\n"
"v_mul_f32 v51, v51, v29\nv_mul_f32 v52, v52, v29\nv_mul_f32 v53, v53, v29\nv_mul_f32 v54, v54, v29\n"
"v_mul_f32 v55, v55, v29\nv_mul_f32 v96, v96, v29\nv_mul_f32 v97, v97, v29\nv_mul_f32 v98, v98, v29\n"
"v_mul_f32 v99, v99, v29\nv_cvt_pk_bf16_f32 v56, v40, v41\nv_cvt_pk_bf16_f32 v57, v42, v43\nv_cvt_pk_bf16_f32 v58, v44, v45\n"
"v_cvt_pk_bf16_f32 v59, v46, v47\nv_cvt_pk_bf16_f32 v60, v48, v49\nv_cvt_pk_bf16_f32 v61, v50, v51\nv_cvt_pk_bf16_f32 v62, v52, v53\n"
"v_cvt_pk_bf16_f32 v63, v54, v55\nv_cvt_pk_bf16_f32 v34, v96, v97\nv_cvt_pk_bf16_f32 v38, v98, v99\nv_xor_b32 v30, 4096, v14\n"
"ds_write_b128 v30, v[56:59]\nds_write_b128 v30, v[60:63] offset:1024\ns_waitcnt vmcnt(59)\nv_mfma_f32_32x32x16_bf16 v[40:55], a[112:115], v[128:131], 0\n"
"buffer_load_dwordx4 a[112:115], v10, s[8:11], s79 offen offset:2048 sc1\ns_waitcnt vmcnt(59)\nv_mfma_f32_32x32x16_bf16 v[40:55], a[116:119], v[132:135], v[40:55]\nbuffer_load_dwordx4 a[116:119], v10, s[8:11], s79 offen offset:2560 sc1\n"
"s_waitcnt vmcnt(59)\nv_mfma_f32_32x32x16_bf16 v[40:55], a[120:123], v[136:139], v[40:55]\nbuffer_load_dwordx4 a[120:123], v10, s[8:11], s79 offen offset:3072 sc1\ns_waitcnt vmcnt(59)\n"
"v_mfma_f32_32x32x16_bf16 v[40:55], a[124:127], v[140:143], v[40:55]\nbuffer_load_dwordx4 a[124:127], v10, s[8:11], s79 offen offset:3584 sc1\ns_waitcnt vmcnt(59)\nv_mfma_f32_32x32x16_bf16 v[40:55], a[128:131], v[144:147], v[40:55]\n"
"buffer_load_dwordx4 a[128:131], v11, s[8:11], s80 offen offset:0 sc1\ns_waitcnt vmcnt(59)\nv_mfma_f32_32x32x16_bf16 v[40:55], a[132:135], v[148:151], v[40:55]\nbuffer_load_dwordx4 a[132:135], v11, s[8:11], s80 offen offset:256 sc1\n"
"s_waitcnt vmcnt(59)\nv_mfma_f32_32x32x16_bf16 v[40:55], a[136:139], v[152:155], v[40:55]\nbuffer_load_dwordx4 a[136:139], v11, s[8:11], s80 offen offset:1024 sc1\ns_waitcnt vmcnt(59)\n"
"v_mfma_f32_32x32x16_bf16 v[40:55], a[140:143], v[156:159], v[40:55]\nbuffer_load_dwordx4 a[140:143], v11, s[8:11], s80 offen offset:1280 sc1\ns_waitcnt vmcnt(59)\nv_mfma_f32_32x32x16_bf16 v[40:55], a[144:147], v[160:163], v[40:55]\n"
"buffer_load_dwordx4 a[144:147], v11, s[8:11], s80 offen offset:2048 sc1\ns_waitcnt vmcnt(59)\nv_mfma_f32_32x32x16_bf16 v[40:55], a[148:151], v[164:167], v[40:55]\nbuffer_load_dwordx4 a[148:151], v11, s[8:11], s80 offen offset:2304 sc1\n"
"s_waitcnt vmcnt(59)\nv_mfma_f32_32x32x16_bf16 v[40:55], a[152:155], v[168:171], v[40:55]\nbuffer_load_dwordx4 a[152:155], v11, s[8:11], s80 offen offset:3072 sc1\ns_waitcnt vmcnt(59)\n"
"v_mfma_f32_32x32x16_bf16 v[40:55], a[156:159], v[172:175], v[40:55]\nbuffer_load_dwordx4 a[156:159], v11, s[8:11], s80 offen offset:3328 sc1\ns_waitcnt vmcnt(59)\nv_mfma_f32_32x32x16_bf16 v[40:55], a[160:163], v[176:179], v[40:55]\n"
"s_waitcnt vmcnt(58)\nv_mfma_f32_32x32x16_bf16 v[40:55], a[164:167], v[180:183], v[40:55]\ns_waitcnt vmcnt(57)\nv_mfma_f32_32x32x16_bf16 v[40:55], a[168:171], v[184:187], v[40:55]\n"
"s_waitcnt vmcnt(56)\nv_mfma_f32_32x32x16_bf16 v[40:55], a[172:175], v[188:191], v[40:55]\ns_waitcnt vmcnt(55)\nv_mfma_f32_32x32x16_bf16 v[40:55], a[176:179], v[192:195], v[40:55]\n"
"s_waitcnt vmcnt(54)\nv_mfma_f32_32x32x16_bf16 v[40:55], a[180:183], v[196:199], v[40:55]\ns_waitcnt vmcnt(53)\nv_mfma_f32_32x32x16_bf16 v[40:55], a[184:187], v[200:203], v[40:55]\n"
"s_waitcnt vmcnt(52)\nv_mfma_f32_32x32x16_bf16 v[40:55], a[188:191], v[204:207], v[40:55]\ns_waitcnt vmcnt(51)\nv_mfma_f32_32x32x16_bf16 v[40:55], a[192:195], v[208:211], v[40:55]\n"
"s_waitcnt vmcnt(50)\nv_mfma_f32_32x32x16_bf16 v[40:55], a[196:199], v[212:215], v[40:55]\ns_waitcnt vmcnt(49)\nv_mfma_f32_32x32x16_bf16 v[40:55], a[200:203], v[216:219], v[40:55]\n"
"s_waitcnt vmcnt(48)\nv_mfma_f32_32x32x16_bf16 v[40:55], a[204:207], v[220:223], v[40:55]\ns_waitcnt vmcnt(47)\nv_mfma_f32_32x32x16_bf16 v[40:55], a[208:211], v[224:227], v[40:55]\n"
"s_waitcnt vmcnt(46)\nv_mfma_f32_32x32x16_bf16 v[40:55], a[212:215], v[228:231], v[40:55]\ns_waitcnt vmcnt(45)\nv_mfma_f32_32x32x16_bf16 v[40:55], a[216:219], v[232:235], v[40:55]\n"
"s_waitcnt vmcnt(44)\nv_mfma_f32_32x32x16_bf16 v[40:55], a[220:223], v[236:239], v[40:55]\ns_waitcnt vmcnt(40)\nv_add_f32 v112, v112, v116\n"
"v_add_f32 v120, v120, v124\nv_add_f32 v113, v113, v117\nv_add_f32 v121, v121, v125\nv_add_f32 v114, v114, v118\n"
"v_add_f32 v122, v122, v126\nv_add_f32 v115, v115, v119\nv_add_f32 v123, v123, v127\nv_add_f32 v112, v112, v120\n"
"v_add_f32 v113, v113, v121\nv_add_f32 v114, v114, v122\nv_add_f32 v115, v115, v123\nv_max3_f32 v28, v112, v113, v114\n"
"v_max_f32 v28, v28, v115\nv_cndmask_b32 v40, v40, v31, s[40:41]\nv_cndmask_b32 v41, v41, v31, s[42:43]\nv_cndmask_b32 v42, v42, v31, s[44:45]\n"
"v_cndmask_b32 v43, v43, v31, s[46:47]\nv_cndmask_b32 v44, v44, v31, s[48:49]\nv_cndmask_b32 v45, v45, v31, s[50:51]\nv_cndmask_b32 v46, v46, v31, s[52:53]\n"
"v_cndmask_b32 v47, v47, v31, s[54:55]\nv_cndmask_b32 v48, v48, v31, s[56:57]\nv_cndmask_b32 v49, v49, v31, s[58:59]\nv_cndmask_b32 v50, v50, v31, s[60:61]\n"
"v_cndmask_b32 v51, v51, v31, s[62:63]\nv_cndmask_b32 v52, v52, v31, s[64:65]\nv_cndmask_b32 v53, v53, v31, s[66:67]\nv_cndmask_b32 v54, v54, v31, s[68:69]\n"
"v_cndmask_b32 v55, v55, v31, s[70:71]\nv_max3_f32 v28, v28, v40, v41\nv_max3_f32 v28, v28, v42, v43\nv_max3_f32 v28, v28, v44, v45\n"
"v_max3_f32 v28, v28, v46, v47\nv_max3_f32 v28, v28, v48, v49\nv_max3_f32 v28, v28, v50, v51\nv_max3_f32 v28, v28, v52, v53\n"
"v_max3_f32 v28, v28, v54, v55\nv_mov_b32 v30, v28\ns_nop 1\nv_permlane32_swap_b32 v28, v30\n"
"v_max_f32 v28, v28, v30\nv_sub_f32 v40, v40, v28\nv_sub_f32 v41, v41, v28\nv_sub_f32 v42, v42, v28\n"
"v_sub_f32 v43, v43, v28\nv_sub_f32 v44, v44, v28\nv_sub_f32 v45, v45, v28\nv_sub_f32 v46, v46, v28\n"
"v_sub_f32 v47, v47, v28\nv_sub_f32 v48, v48, v28\nv_sub_f32 v49, v49, v28\nv_sub_f32 v50, v50, v28\n"
"v_sub_f32 v51, v51, v28\nv_sub_f32 v52, v52, v28\nv_sub_f32 v53, v53, v28\nv_sub_f32 v54, v54, v28\n"
"v_sub_f32 v55, v55, v28\nv_sub_f32 v112, v112, v28\nv_sub_f32 v113, v113, v28\nv_sub_f32 v114, v114, v28\n"
"v_sub_f32 v115, v115, v28\nv_exp_f32 v40, v40\nv_exp_f32 v41, v41\nv_exp_f32 v42, v42\n"
"v_exp_f32 v43, v43\nv_exp_f32 v44, v44\nv_exp_f32 v45, v45\nv_exp_f32 v46, v46\n"
"v_exp_f32 v47, v47\nv_exp_f32 v48, v48\nv_exp_f32 v49, v49\nv_exp_f32 v50, v50\n"
"v_exp_f32 v51, v51\nv_exp_f32 v52, v52\nv_exp_f32 v53, v53\nv_exp_f32 v54, v54\n"
"v_exp_f32 v55, v55\nv_exp_f32 v112, v112\nv_exp_f32 v113, v113\nv_exp_f32 v114, v114\n"
"v_exp_f32 v115, v115\nv_add_f32 v29, v40, v41\nv_add_f32 v30, v42, v43\nv_add_f32 v29, v29, v44\n"
"v_add_f32 v30, v30, v45\nv_add_f32 v29, v29, v46\nv_add_f32 v30, v30, v47\nv_add_f32 v29, v29, v48\n"
"v_add_f32 v30, v30, v49\nv_add_f32 v29, v29, v50\nv_add_f32 v30, v30, v51\nv_add_f32 v29, v29, v52\n"
"v_add_f32 v30, v30, v53\nv_add_f32 v29, v29, v54\nv_add_f32 v30, v30, v55\nv_add_f32 v29, v29, v112\n"
"v_add_f32 v30, v30, v113\nv_add_f32 v29, v29, v114\nv_add_f32 v30, v30, v115\nv_add_f32 v29, v29, v30\n"
"v_mov_b32 v30, v29\ns_nop 1\nv_permlane32_swap_b32 v29, v30\nv_add_f32 v29, v29, v30\n"
"v_rcp_f32 v29, v29\ns_nop 0\nv_mul_f32 v40, v40, v29\nv_mul_f32 v41, v41, v29\n"
"v_mul_f32 v42, v42, v29\nv_mul_f32 v43, v43, v29\nv_mul_f32 v44, v44, v29\nv_mul_f32 v45, v45, v29\n"
"v_mul_f32 v46, v46, v29\nv_mul_f32 v47, v47, v29\nv_mul_f32 v48, v48, v29\nv_mul_f32 v49, v49, v29\n"
"v_mul_f32 v50, v50, v29\nv_mul_f32 v51, v51, v29\nv_mul_f32 v52, v52, v29\nv_mul_f32 v53, v53, v29\n"
"v_mul_f32 v54, v54, v29\nv_mul_f32 v55, v55, v29\nv_mul_f32 v112, v112, v29\nv_mul_f32 v113, v113, v29\n"
"v_mul_f32 v114, v114, v29\nv_mul_f32 v115, v115, v29\nv_cvt_pk_bf16_f32 v56, v40, v41\nv_cvt_pk_bf16_f32 v57, v42, v43\n"
"v_cvt_pk_bf16_f32 v58, v44, v45\nv_cvt_pk_bf16_f32 v59, v46, v47\nv_cvt_pk_bf16_f32 v60, v48, v49\nv_cvt_pk_bf16_f32 v61, v50, v51\n"
"v_cvt_pk_bf16_f32 v62, v52, v53\nv_cvt_pk_bf16_f32 v63, v54, v55\nv_cvt_pk_bf16_f32 v35, v112, v113\nv_cvt_pk_bf16_f32 v39, v114, v115\n"
"v_xor_b32 v30, 6144, v14\nds_write_b128 v30, v[56:59]\nds_write_b128 v30, v[60:63] offset:1024\nds_write_b128 v15, v[32:35]\n"
"ds_write_b128 v15, v[36:39] offset:1024\ns_waitcnt lgkmcnt(0)\ns_barrier\nds_read_b128 v[96:99], v16 offset:0\n"
"ds_read_b128 v[100:103], v16 offset:1024\nds_read_b128 v[104:107], v16 offset:2048\nds_read_b128 v[108:111], v16 offset:3072\nds_read_b128 v[112:115], v16 offset:4096\n"
"ds_read_b128 v[116:119], v16 offset:5120\nds_read_b128 v[120:123], v16 offset:6144\nds_read_b128 v[124:127], v16 offset:7168\nds_read_b128 v[128:131], v16 offset:8192\n"
"ds_read_b128 v[132:135], v16 offset:9216\nds_read_b128 v[136:139], v16 offset:10240\nds_read_b128 v[140:143], v16 offset:11264\ns_waitcnt lgkmcnt(0)\n"
"ds_read_b128 v[144:147], v16 offset:12288\nds_read_b128 v[148:151], v16 offset:13312\nds_read_b128 v[152:155], v16 offset:14336\nds_read_b128 v[156:159], v16 offset:15360\n"
"ds_read_b128 v[160:163], v16 offset:16384\nds_read_b128 v[164:167], v16 offset:17408\nds_read_b128 v[168:171], v16 offset:18432\nds_read_b128 v[172:175], v16 offset:19456\n"
"ds_read_b128 v[176:179], v16 offset:20480\nds_read_b128 v[180:183], v16 offset:21504\nds_read_b128 v[184:187], v16 offset:22528\nds_read_b128 v[188:191], v16 offset:23552\n"
"s_waitcnt lgkmcnt(0)\nds_read_b128 v[192:195], v16 offset:24576\nds_read_b128 v[196:199], v16 offset:25600\nds_read_b128 v[200:203], v16 offset:26624\n"
"ds_read_b128 v[204:207], v16 offset:27648\nds_read_b128 v[208:211], v16 offset:28672\nds_read_b128 v[212:215], v16 offset:29696\nds_read_b128 v[216:219], v16 offset:30720\n"
"ds_read_b128 v[220:223], v16 offset:31744\nds_read_b128 v[224:227], v16 offset:32768\nds_read_b128 v[228:231], v16 offset:33792\nds_read_b128 v[232:235], v16 offset:34816\n"
"ds_read_b128 v[236:239], v16 offset:35840\ns_waitcnt lgkmcnt(0)\nds_read_b128 v[240:243], v16 offset:36864\nds_read_b128 v[244:247], v16 offset:37888\n"
"ds_read_b128 v[248:251], v16 offset:38912\nds_read_b128 v[252:255], v16 offset:39936\nv_mov_b32 v24, v17\ns_sub_u32 s72, s17, 1\n"
"s_waitcnt lgkmcnt(0)\nfo_loop:\ns_add_u32 s76, s76, s20\ns_add_u32 s77, s77, s20\n"
"s_add_u32 s78, s78, s20\ns_add_u32 s79, s79, s20\ns_add_u32 s80, s80, s21\ns_waitcnt vmcnt(39)\n"
"v_mfma_f32_32x32x16_bf16 v[40:55], a[0:3], v[96:99], 0\nbuffer_load_dwordx4 a[0:3], v10, s[8:11], s76 offen offset:0 sc1\ns_waitcnt vmcnt(39)\nv_mfma_f32_32x32x16_bf16 v[40:55], a[4:7], v[100:103], v[40:55]\n"
"buffer_load_dwordx4 a[4:7], v10, s[8:11], s76 offen offset:512 sc1\ns_waitcnt vmcnt(39)\nv_mfma_f32_32x32x16_bf16 v[40:55], a[8:11], v[104:107], v[40:55]\nbuffer_load_dwordx4 a[8:11], v10, s[8:11], s76 offen offset:1024 sc1\n"
"s_waitcnt vmcnt(39)\nv_mfma_f32_32x32x16_bf16 v[40:55], a[12:15], v[108:111], v[40:55]\nbuffer_load_dwordx4 a[12:15], v10, s[8:11], s76 offen offset:1536 sc1\ns_waitcnt vmcnt(39)\n"
"v_mfma_f32_32x32x16_bf16 v[40:55], a[16:19], v[112:115], v[40:55]\nbuffer_load_dwordx4 a[16:19], v10, s[8:11], s76 offen offset:2048 sc1\ns_waitcnt vmcnt(39)\nv_mfma_f32_32x32x16_bf16 v[40:55], a[20:23], v[116:119], v[40:55]\n"
"buffer_load_dwordx4 a[20:23], v10, s[8:11], s76 offen offset:2560 sc1\ns_waitcnt vmcnt(39)\nv_mfma_f32_32x32x16_bf16 v[40:55], a[24:27], v[120:123], v[40:55]\nbuffer_load_dwordx4 a[24:27], v10, s[8:11], s76 offen offset:3072 sc1\n"
"s_waitcnt vmcnt(39)\nv_mfma_f32_32x32x16_bf16 v[40:55], a[28:31], v[124:127], v[40:55]\nbuffer_load_dwordx4 a[28:31], v10, s[8:11], s76 offen offset:3584 sc1\ns_waitcnt vmcnt(39)\n"
"v_mfma_f32_32x32x16_bf16 v[40:55], a[32:35], v[128:131], v[40:55]\nbuffer_load_dwordx4 a[32:35], v10, s[8:11], s77 offen offset:0 sc1\ns_waitcnt vmcnt(39)\nv_mfma_f32_32x32x16_bf16 v[40:55], a[36:39], v[132:135], v[40:55]\n"
"buffer_load_dwordx4 a[36:39], v10, s[8:11], s77 offen offset:512 sc1\ns_waitcnt vmcnt(39)\nv_mfma_f32_32x32x16_bf16 v[40:55], a[40:43], v[136:139], v[40:55]\nbuffer_load_dwordx4 a[40:43], v10, s[8:11], s77 offen offset:1024 sc1\n"
"s_waitcnt vmcnt(39)\nv_mfma_f32_32x32x16_bf16 v[40:55], a[44:47], v[140:143], v[40:55]\nbuffer_load_dwordx4 a[44:47], v10, s[8:11], s77 offen offset:1536 sc1\ns_waitcnt vmcnt(39)\n"
"v_mfma_f32_32x32x16_bf16 v[40:55], a[48:51], v[144:147], v[40:55]\nbuffer_load_dwordx4 a[48:51], v10, s[8:11], s77 offen offset:2048 sc1\ns_waitcnt vmcnt(39)\nv_mfma_f32_32x32x16_bf16 v[40:55], a[52:55], v[148:151], v[40:55]\n"
"buffer_load_dwordx4 a[52:55], v10, s[8:11], s77 offen offset:2560 sc1\ns_waitcnt vmcnt(39)\nv_mfma_f32_32x32x16_bf16 v[40:55], a[56:59], v[152:155], v[40:55]\nbuffer_load_dwordx4 a[56:59], v10, s[8:11], s77 offen offset:3072 sc1\n"
"s_waitcnt vmcnt(39)\nv_mfma_f32_32x32x16_bf16 v[40:55], a[60:63], v[156:159], v[40:55]\nbuffer_load_dwordx4 a[60:63], v10, s[8:11], s77 offen offset:3584 sc1\ns_waitcnt vmcnt(39)\n"
"v_mfma_f32_32x32x16_bf16 v[40:55], a[64:67], v[160:163], v[40:55]\nbuffer_load_dwordx4 a[64:67], v10, s[8:11], s78 offen offset:0 sc1\ns_waitcnt vmcnt(39)\nv_mfma_f32_32x32x16_bf16 v[40:55], a[68:71], v[164:167], v[40:55]\n"
"buffer_load_dwordx4 a[68:71], v10, s[8:11], s78 offen offset:512 sc1\ns_waitcnt vmcnt(39)\nv_mfma_f32_32x32x16_bf16 v[40:55], a[72:75], v[168:171], v[40:55]\nbuffer_load_dwordx4 a[72:75], v10, s[8:11], s78 offen offset:1024 sc1\n"
"s_waitcnt vmcnt(39)\nv_mfma_f32_32x32x16_bf16 v[40:55], a[76:79], v[172:175], v[40:55]\nbuffer_load_dwordx4 a[76:79], v10, s[8:11], s78 offen offset:1536 sc1\ns_waitcnt vmcnt(39)\n"
"v_mfma_f32_32x32x16_bf16 v[40:55], a[80:83], v[176:179], v[40:55]\nbuffer_load_dwordx4 a[80:83], v10, s[8:11], s78 offen offset:2048 sc1\ns_waitcnt vmcnt(39)\nv_mfma_f32_32x32x16_bf16 v[40:55], a[84:87], v[180:183], v[40:55]\n"
"buffer_load_dwordx4 a[84:87], v10, s[8:11], s78 offen offset:2560 sc1\ns_waitcnt vmcnt(39)\nv_mfma_f32_32x32x16_bf16 v[40:55], a[88:91], v[184:187], v[40:55]\nbuffer_load_dwordx4 a[88:91], v10, s[8:11], s78 offen offset:3072 sc1\n"
"s_waitcnt vmcnt(39)\nv_mfma_f32_32x32x16_bf16 v[40:55], a[92:95], v[188:191], v[40:55]\nbuffer_load_dwordx4 a[92:95], v10, s[8:11], s78 offen offset:3584 sc1\ns_waitcnt vmcnt(39)\n"
"v_mfma_f32_32x32x16_bf16 v[40:55], a[96:99], v[192:195], v[40:55]\nbuffer_load_dwordx4 a[96:99], v10, s[8:11], s79 offen offset:0 sc1\ns_waitcnt vmcnt(39)\nv_mfma_f32_32x32x16_bf16 v[40:55], a[100:103], v[196:199], v[40:55]\n"
"buffer_load_dwordx4 a[100:103], v10, s[8:11], s79 offen offset:512 sc1\ns_waitcnt vmcnt(39)\nv_mfma_f32_32x32x16_bf16 v[40:55], a[104:107], v[200:203], v[40:55]\nbuffer_load_dwordx4 a[104:107], v10, s[8:11], s79 offen offset:1024 sc1\n"
"s_waitcnt vmcnt(39)\nv_mfma_f32_32x32x16_bf16 v[40:55], a[108:111], v[204:207], v[40:55]\nbuffer_load_dwordx4 a[108:111], v10, s[8:11], s79 offen offset:1536 sc1\ns_waitcnt vmcnt(39)\n"
"v_mfma_f32_32x32x16_bf16 v[40:55], a[112:115], v[208:211], v[40:55]\nbuffer_load_dwordx4 a[112:115], v10, s[8:11], s79 offen offset:2048 sc1\ns_waitcnt vmcnt(39)\nv_mfma_f32_32x32x16_bf16 v[40:55], a[116:119], v[212:215], v[40:55]\n"
"buffer_load_dwordx4 a[116:119], v10, s[8:11], s79 offen offset:2560 sc1\ns_waitcnt vmcnt(39)\nv_mfma_f32_32x32x16_bf16 v[40:55], a[120:123], v[216:219], v[40:55]\nbuffer_load_dwordx4 a[120:123], v10, s[8:11], s79 offen offset:3072 sc1\n"
"s_waitcnt vmcnt(39)\nv_mfma_f32_32x32x16_bf16 v[40:55], a[124:127], v[220:223], v[40:55]\nbuffer_load_dwordx4 a[124:127], v10, s[8:11], s79 offen offset:3584 sc1\ns_waitcnt vmcnt(39)\n"
"v_mfma_f32_32x32x16_bf16 v[40:55], a[128:131], v[224:227], v[40:55]\nbuffer_load_dwordx4 a[128:131], v11, s[8:11], s80 offen offset:0 sc1\ns_waitcnt vmcnt(39)\nv_mfma_f32_32x32x16_bf16 v[40:55], a[132:135], v[228:231], v[40:55]\n"
"buffer_load_dwordx4 a[132:135], v11, s[8:11], s80 offen offset:256 sc1\ns_waitcnt vmcnt(39)\nv_mfma_f32_32x32x16_bf16 v[40:55], a[136:139], v[232:235], v[40:55]\nbuffer_load_dwordx4 a[136:139], v11, s[8:11], s80 offen offset:1024 sc1\n"
"s_waitcnt vmcnt(39)\nv_mfma_f32_32x32x16_bf16 v[40:55], a[140:143], v[236:239], v[40:55]\nbuffer_load_dwordx4 a[140:143], v11, s[8:11], s80 offen offset:1280 sc1\ns_waitcnt vmcnt(39)\n"
"v_mfma_f32_32x32x16_bf16 v[40:55], a[144:147], v[240:243], v[40:55]\nbuffer_load_dwordx4 a[144:147], v11, s[8:11], s80 offen offset:2048 sc1\ns_waitcnt vmcnt(39)\nv_mfma_f32_32x32x16_bf16 v[40:55], a[148:151], v[244:247], v[40:55]\n"
"buffer_load_dwordx4 a[148:151], v11, s[8:11], s80 offen offset:2304 sc1\ns_waitcnt vmcnt(39)\nv_mfma_f32_32x32x16_bf16 v[40:55], a[152:155], v[248:251], v[40:55]\nbuffer_load_dwordx4 a[152:155], v11, s[8:11], s80 offen offset:3072 sc1\n"
"s_waitcnt vmcnt(39)\nv_mfma_f32_32x32x16_bf16 v[40:55], a[156:159], v[252:255], v[40:55]\nbuffer_load_dwordx4 a[156:159], v11, s[8:11], s80 offen offset:3328 sc1\ns_nop 11\n"
"v_cvt_pk_bf16_f32 v56, v40, v41\nv_cvt_pk_bf16_f32 v57, v42, v43\nv_cvt_pk_bf16_f32 v58, v44, v45\nv_cvt_pk_bf16_f32 v59, v46, v47\n"
"v_cvt_pk_bf16_f32 v60, v48, v49\nv_cvt_pk_bf16_f32 v61, v50, v51\nv_cvt_pk_bf16_f32 v62, v52, v53\nv_cvt_pk_bf16_f32 v63, v54, v55\n"
"ds_write_b128 v24, v[56:59]\nds_write_b128 v24, v[60:63] offset:32\nv_add_u32 v24, 256, v24\ns_sub_u32 s72, s72, 1\n"
"s_cmp_lg_u32 s72, 0\ns_cbranch_scc1 fo_loop\ns_waitcnt vmcnt(39)\nv_mfma_f32_32x32x16_bf16 v[40:55], a[0:3], v[96:99], 0\n"
"s_waitcnt vmcnt(38)\nv_mfma_f32_32x32x16_bf16 v[40:55], a[4:7], v[100:103], v[40:55]\ns_waitcnt vmcnt(37)\nv_mfma_f32_32x32x16_bf16 v[40:55], a[8:11], v[104:107], v[40:55]\n"
"s_waitcnt vmcnt(36)\nv_mfma_f32_32x32x16_bf16 v[40:55], a[12:15], v[108:111], v[40:55]\ns_waitcnt vmcnt(35)\nv_mfma_f32_32x32x16_bf16 v[40:55], a[16:19], v[112:115], v[40:55]\n"
"s_waitcnt vmcnt(34)\nv_mfma_f32_32x32x16_bf16 v[40:55], a[20:23], v[116:119], v[40:55]\ns_waitcnt vmcnt(33)\nv_mfma_f32_32x32x16_bf16 v[40:55], a[24:27], v[120:123], v[40:55]\n"
"s_waitcnt vmcnt(32)\nv_mfma_f32_32x32x16_bf16 v[40:55], a[28:31], v[124:127], v[40:55]\ns_waitcnt vmcnt(31)\nv_mfma_f32_32x32x16_bf16 v[40:55], a[32:35], v[128:131], v[40:55]\n"
"s_waitcnt vmcnt(30)\nv_mfma_f32_32x32x16_bf16 v[40:55], a[36:39], v[132:135], v[40:55]\ns_waitcnt vmcnt(29)\nv_mfma_f32_32x32x16_bf16 v[40:55], a[40:43], v[136:139], v[40:55]\n"
"s_waitcnt vmcnt(28)\nv_mfma_f32_32x32x16_bf16 v[40:55], a[44:47], v[140:143], v[40:55]\ns_waitcnt vmcnt(27)\nv_mfma_f32_32x32x16_bf16 v[40:55], a[48:51], v[144:147], v[40:55]\n"
"s_waitcnt vmcnt(26)\nv_mfma_f32_32x32x16_bf16 v[40:55], a[52:55], v[148:151], v[40:55]\ns_waitcnt vmcnt(25)\nv_mfma_f32_32x32x16_bf16 v[40:55], a[56:59], v[152:155], v[40:55]\n"
"s_waitcnt vmcnt(24)\nv_mfma_f32_32x32x16_bf16 v[40:55], a[60:63], v[156:159], v[40:55]\ns_waitcnt vmcnt(23)\nv_mfma_f32_32x32x16_bf16 v[40:55], a[64:67], v[160:163], v[40:55]\n"
"s_waitcnt vmcnt(22)\nv_mfma_f32_32x32x16_bf16 v[40:55], a[68:71], v[164:167], v[40:55]\ns_waitcnt vmcnt(21)\nv_mfma_f32_32x32x16_bf16 v[40:55], a[72:75], v[168:171], v[40:55]\n"
"s_waitcnt vmcnt(20)\nv_mfma_f32_32x32x16_bf16 v[40:55], a[76:79], v[172:175], v[40:55]\ns_waitcnt vmcnt(19)\nv_mfma_f32_32x32x16_bf16 v[40:55], a[80:83], v[176:179], v[40:55]\n"
"s_waitcnt vmcnt(18)\nv_mfma_f32_32x32x16_bf16 v[40:55], a[84:87], v[180:183], v[40:55]\ns_waitcnt vmcnt(17)\nv_mfma_f32_32x32x16_bf16 v[40:55], a[88:91], v[184:187], v[40:55]\n"
"s_waitcnt vmcnt(16)\nv_mfma_f32_32x32x16_bf16 v[40:55], a[92:95], v[188:191], v[40:55]\ns_waitcnt vmcnt(15)\nv_mfma_f32_32x32x16_bf16 v[40:55], a[96:99], v[192:195], v[40:55]\n"
"s_waitcnt vmcnt(14)\nv_mfma_f32_32x32x16_bf16 v[40:55], a[100:103], v[196:199], v[40:55]\ns_waitcnt vmcnt(13)\nv_mfma_f32_32x32x16_bf16 v[40:55], a[104:107], v[200:203], v[40:55]\n"
"s_waitcnt vmcnt(12)\nv_mfma_f32_32x32x16_bf16 v[40:55], a[108:111], v[204:207], v[40:55]\ns_waitcnt vmcnt(11)\nv_mfma_f32_32x32x16_bf16 v[40:55], a[112:115], v[208:211], v[40:55]\n"
"s_waitcnt vmcnt(10)\nv_mfma_f32_32x32x16_bf16 v[40:55], a[116:119], v[212:215], v[40:55]\ns_waitcnt vmcnt(9)\nv_mfma_f32_32x32x16_bf16 v[40:55], a[120:123], v[216:219], v[40:55]\n"
"s_waitcnt vmcnt(8)\nv_mfma_f32_32x32x16_bf16 v[40:55], a[124:127], v[220:223], v[40:55]\ns_waitcnt vmcnt(7)\nv_mfma_f32_32x32x16_bf16 v[40:55], a[128:131], v[224:227], v[40:55]\n"
"s_waitcnt vmcnt(6)\nv_mfma_f32_32x32x16_bf16 v[40:55], a[132:135], v[228:231], v[40:55]\ns_waitcnt vmcnt(5)\nv_mfma_f32_32x32x16_bf16 v[40:55], a[136:139], v[232:235], v[40:55]\n"
"s_waitcnt vmcnt(4)\nv_mfma_f32_32x32x16_bf16 v[40:55], a[140:143], v[236:239], v[40:55]\ns_waitcnt vmcnt(3)\nv_mfma_f32_32x32x16_bf16 v[40:55], a[144:147], v[240:243], v[40:55]\n"
"s_waitcnt vmcnt(2)\nv_mfma_f32_32x32x16_bf16 v[40:55], a[148:151], v[244:247], v[40:55]\ns_waitcnt vmcnt(1)\nv_mfma_f32_32x32x16_bf16 v[40:55], a[152:155], v[248:251], v[40:55]\n"
"s_waitcnt vmcnt(0)\nv_mfma_f32_32x32x16_bf16 v[40:55], a[156:159], v[252:255], v[40:55]\ns_nop 11\nv_cvt_pk_bf16_f32 v56, v40, v41\n"
"v_cvt_pk_bf16_f32 v57, v42, v43\nv_cvt_pk_bf16_f32 v58, v44, v45\nv_cvt_pk_bf16_f32 v59, v46, v47\nv_cvt_pk_bf16_f32 v60, v48, v49\n"
"v_cvt_pk_bf16_f32 v61, v50, v51\nv_cvt_pk_bf16_f32 v62, v52, v53\nv_cvt_pk_bf16_f32 v63, v54, v55\nds_write_b128 v24, v[56:59]\n"
"ds_write_b128 v24, v[60:63] offset:32\nv_add_u32 v24, 256, v24\n"
    :: "{s[8:23]}"(s0), "{v[8:23]}"(v0) : "v24", "v25", "v26", "v27", "v28", "v29", "v30", "v31", "v32", "v33", "v34", "v35", "v36", "v37", "v38", "v39", "v40", "v41", "v42", "v43", "v44", "v45", "v46", "v47", "v48", "v49", "v50", "v51", "v52", "v53", "v54", "v55", "v56", "v57", "v58", "v59", "v60", "v61", "v62", "v63", "v64", "v65", "v66", "v67", "v68", "v69", "v70", "v71", "v72", "v73", "v74", "v75", "v76", "v77", "v78", "v79", "v80", "v81", "v82", "v83", "v84", "v85", "v86", "v87", "v88", "v89", "v90", "v91", "v92", "v93", "v94", "v95", "v96", "v97", "v98", "v99", "v100", "v101", "v102", "v103", "v104", "v105", "v106", "v107", "v108", "v109", "v110", "v111", "v112", "v113", "v114", "v115", "v116", "v117", "v118", "v119", "v120", "v121", "v122", "v123", "v124", "v125", "v126", "v127", "v128", "v129", "v130", "v131", "v132", "v133", "v134", "v135", "v136", "v137", "v138", "v139", "v140", "v141", "v142", "v143", "v144", "v145", "v146", "v147", "v148", "v149", "v150", "v151", "v152", "v153", "v154", "v155", "v156", "v157", "v158", "v159", "v160", "v161", "v162", "v163", "v164", "v165", "v166", "v167", "v168", "v169", "v170", "v171", "v172", "v173", "v174", "v175", "v176", "v177", "v178", "v179", "v180", "v181", "v182", "v183", "v184", "v185", "v186", "v187", "v188", "v189", "v190", "v191", "v192", "v193", "v194", "v195", "v196", "v197", "v198", "v199", "v200", "v201", "v202", "v203", "v204", "v205", "v206", "v207", "v208", "v209", "v210", "v211", "v212", "v213", "v214", "v215", "v216", "v217", "v218", "v219", "v220", "v221", "v222", "v223", "v224", "v225", "v226", "v227", "v228", "v229", "v230", "v231", "v232", "v233", "v234", "v235", "v236", "v237", "v238", "v239", "v240", "v241", "v242", "v243", "v244", "v245", "v246", "v247", "v248", "v249", "v250", "v251", "v252", "v253", "v254", "v255", "a0", "a1", "a2", "a3", "a4", "a5", "a6", "a7", "a8", "a9", "a10", "a11", "a12", "a13", "a14", "a15", "a16", "a17", "a18", "a19", "a20", "a21", "a22", "a23", "a24", "a25", "a26", "a27", "a28", "a29", "a30", "a31", "a32", "a33", "a34", "a35", "a36", "a37", "a38", "a39", "a40", "a41", "a42", "a43", "a44", "a45", "a46", "a47", "a48", "a49", "a50", "a51", "a52", "a53", "a54", "a55", "a56", "a57", "a58", "a59", "a60", "a61", "a62", "a63", "a64", "a65", "a66", "a67", "a68", "a69", "a70", "a71", "a72", "a73", "a74", "a75", "a76", "a77", "a78", "a79", "a80", "a81", "a82", "a83", "a84", "a85", "a86", "a87", "a88", "a89", "a90", "a91", "a92", "a93", "a94", "a95", "a96", "a97", "a98", "a99", "a100", "a101", "a102", "a103", "a104", "a105", "a106", "a107", "a108", "a109", "a110", "a111", "a112", "a113", "a114", "a115", "a116", "a117", "a118", "a119", "a120", "a121", "a122", "a123", "a124", "a125", "a126", "a127", "a128", "a129", "a130", "a131", "a132", "a133", "a134", "a135", "a136", "a137", "a138", "a139", "a140", "a141", "a142", "a143", "a144", "a145", "a146", "a147", "a148", "a149", "a150", "a151", "a152", "a153", "a154", "a155", "a156", "a157", "a158", "a159", "a160", "a161", "a162", "a163", "a164", "a165", "a166", "a167", "a168", "a169", "a170", "a171", "a172", "a173", "a174", "a175", "a176", "a177", "a178", "a179", "a180", "a181", "a182", "a183", "a184", "a185", "a186", "a187", "a188", "a189", "a190", "a191", "a192", "a193", "a194", "a195", "a196", "a197", "a198", "a199", "a200", "a201", "a202", "a203", "a204", "a205", "a206", "a207", "a208", "a209", "a210", "a211", "a212", "a213", "a214", "a215", "a216", "a217", "a218", "a219", "a220", "a221", "a222", "a223", "s40", "s41", "s42", "s43", "s44", "s45", "s46", "s47", "s48", "s49", "s50", "s51", "s52", "s53", "s54", "s55", "s56", "s57", "s58", "s59", "s60", "s61", "s62", "s63", "s64", "s65", "s66", "s67", "s68", "s69", "s70", "s71", "s72", "s73", "s74", "s75", "s76", "s77", "s78", "s79", "s80", "s81", "vcc", "scc", "memory");
}
DI u4 pack8(const f16v& x, int s, float sc) { return (u4){pk2(x[8 * s] * sc, x[8 * s + 1] * sc), pk2(x[8 * s + 2] * sc, x[8 * s + 3] * sc), pk2(x[8 * s + 4] * sc, x[8 * s + 5] * sc), pk2(x[8 * s + 6] * sc, x[8 * s + 7] * sc)}; }
DI u4 pack8(const f16v& x, int s) { return (u4){pk2(x[8 * s], x[8 * s + 1]), pk2(x[8 * s + 2], x[8 * s + 3]), pk2(x[8 * s + 4], x[8 * s + 5]), pk2(x[8 * s + 6], x[8 * s + 7])}; }
DI void dma_pair(u4 srd, int v0, int v1, int soff, unsigned m) {
    asm volatile("s_mov_b32 m0, %4\n\ts_nop 0\n\tbuffer_load_dwordx4 %0, %2, %3 offen lds\n\tbuffer_load_dwordx4 %1, %2, %3 offen offset:1024 lds" :: "v"(v0), "v"(v1), "s"(srd), "s"(soff), "s"(m) : "memory");
}
DI void dma_one(u4 srd, int v0, int soff, unsigned m) {
    asm volatile("s_mov_b32 m0, %3\n\ts_nop 0\n\tbuffer_load_dwordx4 %0, %1, %2 offen lds" :: "v"(v0), "s"(srd), "s"(soff), "s"(m) : "memory");
}
DI void mV_va(f16v& c, u4 x, u4 y) { asm volatile("v_mfma_f32_32x32x16_bf16 %0, %1, %2, %0" : "+v"(c) : "v"(x), "a"(y)); }
DI void mV_va0(f16v& c, u4 x, u4 y) { asm volatile("v_mfma_f32_32x32x16_bf16 %0, %1, %2, 0" : "=&v"(c) : "v"(x), "a"(y)); }
DI void mV_av(f16v& c, u4 x, u4 y) { asm volatile("v_mfma_f32_32x32x16_bf16 %0, %1, %2, %0" : "+v"(c) : "a"(x), "v"(y)); }
DI void mV_av0(f16v& c, u4 x, u4 y) { asm volatile("v_mfma_f32_32x32x16_bf16 %0, %1, %2, 0" : "=&v"(c) : "a"(x), "v"(y)); }
DI void mV_vv(f16v& c, u4 x, u4 y) { asm volatile("v_mfma_f32_32x32x16_bf16 %0, %1, %2, %0" : "+v"(c) : "v"(x), "v"(y)); }
DI void mV_vv0(f16v& c, u4 x, u4 y) { asm volatile("v_mfma_f32_32x32x16_bf16 %0, %1, %2, 0" : "=&v"(c) : "v"(x), "v"(y)); }
template <int N, int PD, class RD, class MM, class EX> DI void pipe(RD rd, MM mm, EX ex) {
    u4 f[PD];
#pragma unroll
    for (int j = 0; j < PD; ++j) f[j] = rd(j);
    SB();
#pragma unroll
    for (int j = 0; j < N; ++j) { mm(j, f[j % PD]); if (j + PD < N) f[j % PD] = rd(j + PD); ex(j); SB(); }
}
DI int swzr(int row) { return ((row >> 2) & 1) | ((row & 3) << 1) | (row & 8); }
DI int swap23(int x) { return (x & ~12) | ((x & 4) << 1) | ((x & 8) >> 1); }
#define SLICE 32768
#define MFW32(x, y) asm volatile("s_nop 11" : "+v"(x), "+v"(y))
struct SideCtx {
    char LAS* sm; unsigned smb; int lane, wave;
    int vr0, vr1, vc0, vc1;
};
DI SideCtx make_ctx(int wave) {
    SideCtx c; c.sm = (char LAS*)smem_; c.smb = (unsigned)(size_t)c.sm; c.lane = fresh_lane(); c.wave = wave;
    asm volatile("" : "+s"(c.wave), "+s"(c.smb));
    const int l = c.lane;
    { int row = 4 * c.wave + (l >> 5); c.vr0 = row * 8192 + (((l & 31) ^ swzr(row)) * 16); row += 2; c.vr1 = row * 8192 + (((l & 31) ^ swzr(row)) * 16) - 1024; }
    { int row = l >> 3; c.vc0 = row * 896 + (((l & 7) ^ (((row >> 1) & 1) << 2)) * 16); c.vc1 = c.vc0 + 8 * 896 - 1024; }
    return c;
}
DI void dma_rows_q(const SideCtx& c, u4 srd, int h, int r0, int slot, int g) { dma_pair(srd, c.vr0, c.vr1, (r0 + 16 * g) * 8192 + h * 512, c.smb + slot * SLICE + (8 * g + 2 * c.wave) * 1024); }
DI void dma_cols_q(const SideCtx& c, u4 srd, int h, int c0, int slot, int g) { const int k = 8 * c.wave + 2 * g; dma_pair(srd, c.vc0, c.vc1, (h * 256 + 8 * k) * 896 + c0 * 2, c.smb + slot * SLICE + k * 1024); }
DI void dma_rows(const SideCtx& c, u4 srd, int h, int r0, int slot) {
#pragma unroll
    for (int g = 0; g < 4; ++g) dma_rows_q(c, srd, h, r0, slot, g);
}
DI void dma_cols(const SideCtx& c, u4 srd, int h, int c0, int slot) {
#pragma unroll
    for (int g = 0; g < 4; ++g) dma_cols_q(c, srd, h, c0, slot, g);
}
DI void set_flag(const SideCtx& c, u4 rhb, int off, unsigned tag) {
    waitvm<0>(); bar();
    if (c.wave == 0) sst32(tag, rhb, c.lane == 0 ? off : OOB, 0);
}

DI void slp(int n) {
#pragma nounroll
    for (int i = 0; i < n; ++i) asm volatile("s_sleep 1");
}
DI int xsrc(int wave, int lane, int k) { const int n = 64 * (7 * (wave & 1) + k) + lane, row = (n * 1171) >> 16, pos = n - 56 * row; return (row + 16 * (wave >> 1)) * 896 + ((pos ^ ((row >> 1) & 7)) * 16); }
DI int xrd(int row, int hi, int k) { return row * 896 + (((2 * k + hi) ^ ((row >> 1) & 7)) * 16); }
#define WSL(u) ((u) + 1)

DI int xswz(int type, int row) { return type ? ((row >> 1) & 1) | (((row >> 3) & 3) << 1) : (row >> 1) & 7; }
DI int xsrc8(int type, int wave, int lane) { const int row = 8 * wave + (lane >> 3); return row * 896 + (((lane & 7) ^ xswz(type, row)) * 16); }
#define C_AT -1
#define C_PF 3
#define W_PF 2
#define X_PF 4
DI void dma_line(u4 rw1, const SideCtx& c, int half, int h, int sub) {
    const int n = (sub * 4 + c.wave) * 48 + c.lane;
    asm volatile("s_mov_b32 m0, %3\n\ts_nop 0\n\tbuffer_load_dword %0, %1, %2 offen lds" :: "v"(c.lane < 48 ? (n >> 2) * 8192 + (n & 3) * 128 : OOB), "s"(rw1), "s"((256 * half + 64) * 8192 + h * 512), "s"(c.smb + 28672 + 256) : "memory");
}
DI const char* cold_line(int half, int type, int h, int sub, int wave, int l) {
    CArgs& a = get_args();
    const int wid = (((half * 2 + type) * 16 + h) * 4 + sub) * 4 + wave;
    if (!(C_PF & 2) && l >= 14) l = 13;
    return l < 14 ? (const char*)a.X + (wid * 14 + l) * 128 : l < 28 ? (const char*)a.Au + (wid * 14 + l - 14) * 128
         : (const char*)((wid & 2) ? ((wid & 1) ? a.ACi : a.ACr) : ((wid & 1) ? a.Aim : a.AlogA)) + ((wid >> 2) * 7 + min(l - 28, 6)) * 128;
}
template <int PART>
DI void gemm_first(int wave, int half, int type, int h, int sub) {
    CArgs& a = get_args();
    const SideCtx c = make_ctx(wave);
    h = __builtin_amdgcn_readfirstlane(h); sub = __builtin_amdgcn_readfirstlane(sub); half = __builtin_amdgcn_readfirstlane(half); asm volatile("" : "+s"(h), "+s"(sub), "+s"(half));
    if (PART == 0) {
        const u4 rw2 = mksrd(type ? a.Wo : a.Wq, 448 * 8192);
        const int c1 = sub == 3 ? 384 : sub * 128 + 64;
        if (type) { dma_cols(c, rw2, h, sub * 128, WSL(2)); dma_cols(c, rw2, h, c1, WSL(3)); } else { dma_rows(c, rw2, h, sub * 128, WSL(2)); dma_rows(c, rw2, h, c1, WSL(3)); }
    } else {
        const u4 rw1 = mksrd(type ? a.Wv : a.Wk, 448 * 8192);
        if (W_PF == 1) dma_line(rw1, c, half, h, sub);
        dma_rows(c, rw1, h, 256 * half, WSL(0));
        if (W_PF == 2) dma_line(rw1, c, half, h, sub);
        const int n = (((type * 4 + (h & 3)) * 4 + sub) * 4 + c.wave) * 8 + c.lane;
        asm volatile("s_mov_b32 m0, %2\n\ts_nop 0\n\tbuffer_load_dword %0, %1, 0 offen lds" :: "v"(c.lane < 8 ? n * 128 : OOB), "s"(mksrd(a.X, half ? 28672 : X_PF * 28672)), "s"(c.smb + 28672 + 256) : "memory");
        if (C_AT < 0) asm volatile("global_load_lds_dword %0, off" :: "v"(cold_line(half, type, h, sub, c.wave, c.lane)) : "memory");
    }
}
template <int TYPE>
DI u8v gemm_role(unsigned tag, int vb, int wave, int half, int h, int sub) {
    CArgs& a = get_args();
    const SideCtx c = make_ctx(wave);
    const int lane = c.lane, g = lane >> 4, n = lane & 15;
    h = __builtin_amdgcn_readfirstlane(h); sub = __builtin_amdgcn_readfirstlane(sub); half = __builtin_amdgcn_readfirstlane(half); asm volatile("" : "+s"(h), "+s"(sub), "+s"(half));
    const int B = 4 * sub + c.wave, prow = 8 * (n >> 2) + (n & 3);
    GCtx x;
    x.rw1 = mksrd(TYPE ? a.Wv : a.Wk, 448 * 8192); x.rx = mksrd(a.X, half ? 0 : 2048 * 896); x.rhb = mksrd(a.HB, HB_SZ);
    x.rexp = TYPE ? mksrd(a.UM, 28 * MH) : mksrd(a.SXM, 64 * SH); x.rout = mksrd(TYPE ? a.UF : a.GF, GSZ);
    x.tag = tag; x.rot = 4 * half; x.sw1 = h * 512; x.mw = c.smb + c.wave * 2048; x.mx = c.smb + c.wave * 1024;
    x.sfg = O_FG - O_FW + (h * 2 + TYPE - TYPE * 16 - h) * 256; x.stay = half ? 2 : sub < 2; x.csc = __float_as_uint(CSCALE); x.ostr = TYPE ? 16384 : 28672;
    x.p1 = (unsigned)(u64)a.Au; x.p2 = (unsigned)((u64)a.Au >> 32); x.p5 = min(((TYPE * 16 + h) * 16 + B) * 3584, 2048 * 896 - 4096) + lane * 16; x.kb = 65536 + c.wave * 16384;
    x.vr0 = c.vr0; x.vr1 = c.vr1; x.vx = xsrc8(TYPE, c.wave, lane);
    x.wa = c.smb + SLICE + prow * 512 + ((g ^ swzr(prow)) * 16);
    { const int row = TYPE ? prow : n; x.xa = c.smb + row * 128 + ((g ^ xswz(TYPE, row)) * 16); }
    auto wof = [&](int blk) { return (TYPE ? 2 * blk * MH + NC * 4096 + h * 14336 : (h * 4 + (blk >> 2)) * SH + NC * 1024 + (blk & 3) * 28672) + lane * 16; };
    auto oof = [&](int blk, int i) { return blk > 13 ? OOB : (TYPE ? (2 * blk + i) * UH + h * 1024 : h * GH + i * 14336 + blk * 1024) + lane * 16; };
    x.vexp = B > 13 ? OOB : wof(B); x.vimp = B > 5 ? OOB : wof(B + 8);
    x.vk = c.smb + (3 + (c.wave >> 1)) * SLICE + (8 * g + (n >> 2)) * 128 + (((4 * (c.wave & 1) + ((n & 3) >> 1)) ^ ((n >> 3) << 2)) * 16) + 8 * (n & 1);
    x.vo0 = oof(B, 0); x.vo1 = oof(B, 1); x.vo2 = oof(B + 8, 0); x.vo3 = oof(B + 8, 1);
    x.vfl = (c.wave == 0 && lane == 0) ? O_FW + half * 8192 + ((TYPE * 16 + h) * 4 + sub) * 64 : OOB;
    x.vpf = O_FW + (lane & 1) * 8192 + ((TYPE * 16 + h) * 4 + (sub | 2)) * 64; x.vpo = O_FW + 8192 + ((TYPE * 16 + h) * 4 + sub) * 64;
    if (half) {
        const int id = vb >> 3;
        x.vpo = need_off(vb & 3, id, c.wave, lane); x.vpf = O_PAR + 4; x.vo0 = O_PAR + 512 + min(lane, 35) * 32;
        if (TYPE && id >= 20 && id < 27) { x.stay = 3; x.vimp = lane * 16; x.ostr = ((vb & 3) * 7 + id - 20) * MH + NC * 4096 + 4 * c.wave * 14336; }
    }
    PSTAMP(1);
    u8v w = {};
    { const u64 p = (u64)cold_line(half, TYPE, h, sub, c.wave, lane); w[0] = (unsigned)p; w[1] = (unsigned)(p >> 32); }
    const unsigned tw = TYPE ? gemm_asm1(x, w) : gemm_asm0(x, w);
    return w;
}

#define O_LAG 2
#define O_LAGN 24
DI void o_task(unsigned tag, int wave, int s) {
    {
        CArgs& a = get_args();
        const SideCtx c = make_ctx(wave);
        const int lane = c.lane, hi = lane >> 5, l31 = lane & 31, w = c.wave, pc = swap23(l31);
        s = __builtin_amdgcn_readfirstlane(s); asm volatile("" : "+s"(s));
        const u4 rhb = mksrd(a.HB, HB_SZ), rx = mksrd(a.X, 2048 * 896);
        bar();
        const int vx = xsrc8(0, w, lane);
#pragma unroll
        for (int k = 0; k < 7; ++k) dma_one(rx, vx, s * 28672 + k * 128, c.smb + k * 4096 + w * 1024);
        wait_flags(rhb, lane < 16 ? O_FH + (16 * w + lane) * 128 : OOB, tag + (s < O_LAGN ? s + O_LAG - (O_LAG - 2) * s / O_LAGN : s + 2));
        OCtx x;
        x.r = mksrd(a.HB, 0x7000000);
        x.sg = (unsigned)((char*)a.GF - a.HB) + s * 28672; x.ssx = (unsigned)((char*)a.SXM - a.HB) + s * 1024; x.hx = 4 * w;
        x.su = (unsigned)((char*)a.UF - a.HB) + s * 16384 + w * 2 * UH; x.sum = (unsigned)((char*)a.UM - a.HB) + s * 4096 + w * 2 * MH; x.nt = w < 2 ? 4 : 3;
        x.sr = GH; x.p0 = SH; x.p1 = 8 * UH; x.p2 = 8 * MH; x.p3 = x.p4 = x.p5 = x.p6 = x.p7 = x.p8 = x.p9 = x.pa = 0;
        x.vg = (pc >> 4) * 14336 + hi * 256 + (pc & 15) * 16;
        x.vsx = (l31 >> 4) * 512 + hi * 256 + (l31 & 15) * 16;
        x.vu = (pc >> 4) * UH + hi * 256 + (pc & 15) * 16; x.vum = (pc >> 4) * MH + hi * 512 + (pc & 15) * 16;
        x.vx = c.smb + l31 * 128 + ((hi ^ xswz(0, l31)) * 16); x.vt = l31 - 8 * hi;
        x.vpx = c.smb + 32768 + x.hx * 2048 + lane * 16; x.vpm = c.smb + 65536 + w * 2048 + lane * 16; x.vpr = c.smb + 32768 + lane * 16;
        x.vo = c.smb + 73728 + l31 * 912 + w * 64 + hi * 16;
        o_asm(x);
    }
    bar();
    const int tid = wave * 64 + fresh_lane();
    const u4 ro = mksrd(get_args().out, 2048 * 1792);
#pragma unroll
    for (int k = 0; k < 7; ++k) sst128(*(const u4 LAS*)(smem_ + 73728 + (tid >> 3) * 912 + (tid & 7) * 16 + k * 128), ro, (tid >> 3) * 1792 + (tid & 7) * 16, s * 32 * 1792 + 896 + k * 128);
}

DI void e_task(unsigned tag, int wave, int s, int hg) {
    CArgs& a = get_args();
    const SideCtx c = make_ctx(wave);
    const int lane = c.lane, hi = lane >> 5, l31 = lane & 31;
    s = __builtin_amdgcn_readfirstlane(s); hg = __builtin_amdgcn_readfirstlane(hg); asm volatile("" : "+s"(s), "+s"(hg));
    const int h = 4 * hg + c.wave;
    const u4 rhb = mksrd(a.HB, HB_SZ), rg = mksrd(a.GF, GSZ), rx = mksrd(a.X, 2048 * 896);
    bar();
#pragma unroll
    for (int k = 0; k < 7; ++k) dma_one(rx, xsrc(c.wave, lane, k), s * 28672, c.smb + (7 * c.wave + k) * 1024);
    wait_flags(rhb, lane < 4 ? O_FG + (h * 8 + lane) * 64 : OOB, tag + s + 1);
    u4 gf[28];
    const int key = swap23(l31), o_g = (key >> 4) * 14336 + hi * 256 + (key & 15) * 16;
#pragma unroll
    for (int k = 0; k < 28; ++k) asm volatile("buffer_load_dwordx4 %0, %1, %2, %3 offen sc1" : "=a"(gf[k]) : "v"(o_g), "s"(rg), "s"(h * GH + s * 28672 + k * 512) : "memory");
    bar();
    f16v sx;
    pipe<28, 4>([&](int k) { return *(const u4 LAS*)(c.sm + xrd(l31, hi, k & 3) + (k >> 2) * 128); },
                [&](int k, u4 f) { if (k == 0) WAITV(21, "+a"(gf[0]), "+a"(gf[1]), "+a"(gf[2]), "+a"(gf[3]), "+a"(gf[4]), "+a"(gf[5]), "+a"(gf[6]));
                                   if (k == 7) WAITV(14, "+a"(gf[7]), "+a"(gf[8]), "+a"(gf[9]), "+a"(gf[10]), "+a"(gf[11]), "+a"(gf[12]), "+a"(gf[13]));
                                   if (k == 14) WAITV(7, "+a"(gf[14]), "+a"(gf[15]), "+a"(gf[16]), "+a"(gf[17]), "+a"(gf[18]), "+a"(gf[19]), "+a"(gf[20]));
                                   if (k == 21) WAITV(0, "+a"(gf[21]), "+a"(gf[22]), "+a"(gf[23]), "+a"(gf[24]), "+a"(gf[25]), "+a"(gf[26]), "+a"(gf[27]));
                                   if (k == 0) mV_av0(sx, gf[k], f); else mV_av(sx, gf[k], f); },
                [&](int) {});
    asm volatile("s_nop 11" : "+v"(sx));
    float m = -3.0e38f;
#pragma unroll
    for (int j = 0; j < 16; ++j) { if (16 * (j >> 3) + 8 * hi + (j & 7) > l31) sx[j] = -INFINITY; m = fmaxf(m, sx[j]); }
    m = x32max(m);
    float z = 0.f;
#pragma unroll
    for (int j = 0; j < 16; ++j) { sx[j] = __builtin_amdgcn_exp2f(sx[j] - m); z += sx[j]; }
    z = x32add(z);
    const u4 rex = mksrd(a.EX, 64 * 16 * 2048), rmz = mksrd(a.MZX, 64 * 16 * 256);
    sst128(pack8(sx, 0), rex, lane * 16, (s * 16 + h) * 2048); sst128(pack8(sx, 1), rex, lane * 16, (s * 16 + h) * 2048 + 1024);
    sst64((u2){__float_as_uint(m), __float_as_uint(z)}, rmz, lane < 32 ? lane * 8 : OOB, (s * 16 + h) * 256);
    set_flag(c, rhb, O_FE + (s * 4 + hg) * 64, tag);
}

#define NA 4
#define A_SLP 0
#define A_STG 0
#define U_W 0
#define A_LATE 1
#define X_JIT 4
#define P_XSLP 24
#define P_USLP 4
#define XT(k)
DI void x_task(unsigned tag, int wave, int s, int cb, int skip) {
    CArgs& a = get_args();
    const SideCtx c = make_ctx(wave);
    const int lane = c.lane, hi = lane >> 5, l31 = lane & 31, w = c.wave;
    s = __builtin_amdgcn_readfirstlane(s); cb = __builtin_amdgcn_readfirstlane(cb); asm volatile("" : "+s"(s), "+s"(cb));
    const u4 rhb = mksrd(a.HB, HB_SZ);
    const u4 ru = mksrd(a.UF, GSZ), rex = mksrd(a.EX, 64 * 16 * 2048), rmz = mksrd(a.MZX, 64 * 16 * 256);
    const u4 rum = mksrd(a.UM, 28 * MH), rsx = mksrd(a.SXM, 64 * SH);
    if (X_JIT) wait_flags(rhb, lane == 0 ? O_FH + (cb * 4 + w) * 128 : OOB, tag + s - X_JIT);
    wait_flags(rhb, lane < 8 ? O_FG + (((4 * w + (lane & 3)) * 2 + 1) * 4 + ((cb >> 1) & (lane < 4 ? 3 : 1))) * 64 : lane == 8 ? O_FE + (s * 4 + w) * 64 : OOB, lane < 8 ? tag + s + 1 : tag);
    unsigned xt_ = 0;
    u4 uf[4][2][2], ex[4][2]; u2 mz[4];
    const int o_u = (l31 >> 4) * UH + hi * 256 + (l31 & 15) * 16;
#pragma unroll
    for (int k = 0; k < 4; ++k) {
        const int h = 4 * w + k, sh = s * 16 + h;
#pragma unroll
        for (int sg = 0; sg < 2; ++sg) {
            ex[k][sg] = sld128(rex, lane * 16, sh * 2048 + sg * 1024);
#pragma unroll
            for (int nt = 0; nt < 2; ++nt) uf[k][sg][nt] = sld128(ru, o_u, (4 * cb + 2 * nt) * UH + sh * 1024 + sg * 512);
        }
        mz[k] = sld64(rmz, l31 * 8, sh * 256);
        asm volatile("s_sleep %0" :: "i"(P_XSLP));
    }
    waitvm<0>();
    f16v rr[4][2];
#pragma unroll
    for (int k = 0; k < 4; ++k) {
        asm volatile("" : "+v"(mz[k]), "+v"(ex[k][0]), "+v"(ex[k][1]), "+v"(uf[k][0][0]), "+v"(uf[k][0][1]), "+v"(uf[k][1][0]), "+v"(uf[k][1][1]));
        mV_vv0(rr[k][0], uf[k][0][0], ex[k][0]); mV_vv0(rr[k][1], uf[k][0][1], ex[k][0]);
        mV_vv(rr[k][0], uf[k][1][0], ex[k][1]); mV_vv(rr[k][1], uf[k][1][1], ex[k][1]);
        MFW32(rr[k][0], rr[k][1]);
    }
    bar();
    wait_flags<1>(rhb, s < 61 ? (lane < 16 ? O_FH + (16 * w + lane) * 128 : OOB) : lane ? OOB : s < 63 ? O_FH + (cb * 4 + w + 32) * 128 : O_FP + ((cb * 4 + w) & 15) * 64, tag + (s < 61 ? s + 2 : s));
    XT(0);
    u4 um[2][2]; u2 pm[4]; f16v a0, a1;
    const int o_sx = (l31 >> 4) == skip ? OOB : (l31 >> 4) * 512 + hi * 256 + (l31 & 15) * 16, o_um = (l31 >> 4) * MH + hi * 512 + (l31 & 15) * 16;
#pragma nounroll
    for (unsigned spin = 0;; ++spin) {
        u4 sp[4][4];
        asm volatile("s_nop 4");
#pragma unroll
        for (int k = 0; k < 16; ++k) sp[k >> 2][k & 3] = ld128(rsx, o_sx, ((4 * w + (k >> 2)) * 4 + (k & 3)) * SH + s * 1024);
#pragma unroll
        for (int k = 0; k < 4; ++k) um[k >> 1][k & 1] = ld128(rum, o_um, (4 * cb + 2 * (k & 1)) * MH + s * 4096 + w * 1024 + (k >> 1) * 256);
        XT(2);
        unsigned chk = 0;
#pragma unroll
        for (int k = 0; k < 4; ++k) {
            if (k == 0) WAITV(16, "+v"(sp[0][0]), "+v"(sp[0][1]), "+v"(sp[0][2]), "+v"(sp[0][3]));
            if (k == 1) WAITV(12, "+v"(sp[1][0]), "+v"(sp[1][1]), "+v"(sp[1][2]), "+v"(sp[1][3]));
            if (k == 2) WAITV(8, "+v"(sp[2][0]), "+v"(sp[2][1]), "+v"(sp[2][2]), "+v"(sp[2][3]));
            if (k == 3) WAITV(4, "+v"(sp[3][0]), "+v"(sp[3][1]), "+v"(sp[3][2]), "+v"(sp[3][3]));
#pragma unroll
            for (int p = 0; p < 4; ++p) chk = max(max(chk, max(sp[k][p][0], sp[k][p][1])), max(sp[k][p][2], sp[k][p][3]));
            const f4 sc = (__builtin_bit_cast(f4, sp[k][0]) + __builtin_bit_cast(f4, sp[k][1])) + (__builtin_bit_cast(f4, sp[k][2]) + __builtin_bit_cast(f4, sp[k][3]));
            const float mx = __uint_as_float(mz[k][0]);
            float m = fmaxf(x32max(fmaxf(fmaxf(sc[0], sc[1]), fmaxf(sc[2], sc[3]))), mx);
            float e[4];
#pragma unroll
            for (int j = 0; j < 4; ++j) e[j] = __builtin_amdgcn_exp2f(sc[j] - m);
            const float cx = __builtin_amdgcn_exp2f(mx - m);
            const float z = x32add((e[0] + e[1]) + (e[2] + e[3])) + cx * __uint_as_float(mz[k][1]);
            const float inv = __builtin_amdgcn_rcpf(z);
            const float scx = cx * inv;
            pm[k] = (u2){pk2(e[0] * inv, e[1] * inv), pk2(e[2] * inv, e[3] * inv)};
            if (k == 0) { a0 = rr[0][0] * scx; a1 = rr[0][1] * scx; } else { a0 += rr[k][0] * scx; a1 += rr[k][1] * scx; }
            asm volatile("" : "+v"(pm[k]), "+v"(a0), "+v"(a1), "+v"(chk));
        }
        XT(3);
        WAITV(0, "+v"(um[0][0]), "+v"(um[0][1]), "+v"(um[1][0]), "+v"(um[1][1]));
#pragma unroll
        for (int p = 0; p < 4; ++p) chk = max(max(chk, max(um[p >> 1][p & 1][0], um[p >> 1][p & 1][1])), max(um[p >> 1][p & 1][2], um[p >> 1][p & 1][3]));
        if (__all(chk != FF)) break;
        if (spin > SPIN_MAX / 16) __builtin_amdgcn_endpgm();
        XT(1);
    }
    XT(4);
    {
        u4 p0 = {pm[0][0], pm[1][0], pm[2][0], pm[3][0]}, p1 = {pm[0][1], pm[1][1], pm[2][1], pm[3][1]};
        asm volatile("s_nop 1\n\tv_mfma_f32_32x32x16_bf16 %0, %2, %6, %0\n\tv_mfma_f32_32x32x16_bf16 %1, %3, %6, %1\n\tv_mfma_f32_32x32x16_bf16 %0, %4, %7, %0\n\tv_mfma_f32_32x32x16_bf16 %1, %5, %7, %1\n\ts_nop 11"
                     : "+v"(a0), "+v"(a1) : "v"(um[0][0]), "v"(um[0][1]), "v"(um[1][0]), "v"(um[1][1]), "v"(p0), "v"(p1));
    }
    XT(5);
#pragma unroll
    for (int g = 0; g < 4; ++g) {
        *(f4 LAS*)(c.sm + w * 8704 + l31 * 272 + (8 * g + 4 * hi) * 4) = (f4){a0[4 * g], a0[4 * g + 1], a0[4 * g + 2], a0[4 * g + 3]};
        *(f4 LAS*)(c.sm + w * 8704 + l31 * 272 + 128 + (8 * g + 4 * hi) * 4) = (f4){a1[4 * g], a1[4 * g + 1], a1[4 * g + 2], a1[4 * g + 3]};
    }
    bar();
    {
        const int t = w * 8 + (lane >> 3), c8 = lane & 7;
        f4 s0 = {0.f, 0.f, 0.f, 0.f}, s1 = s0;
#pragma unroll
        for (int p = 0; p < 4; ++p) { s0 += *(const f4 LAS*)(c.sm + p * 8704 + t * 272 + c8 * 32); s1 += *(const f4 LAS*)(c.sm + p * 8704 + t * 272 + c8 * 32 + 16); }
        sst128((u4){pk2(s0[0], s0[1]), pk2(s0[2], s0[3]), pk2(s1[0], s1[1]), pk2(s1[2], s1[3])}, mksrd(a.out, 2048 * 1792), (t >> 4) == skip ? OOB : t * 1792 + c8 * 16, s * 32 * 1792 + 896 + cb * 128);
        XT(7);
    }
}

#define PA_UT 0
#define PA_TU 32896
#define PA_KH (PA_TU + 33280)
#define PA_SC (PA_KH + 7680)
#define PA_KT (PA_SC + 128)
#define PA_RED (PA_KT + 24576)
typedef __attribute__((ext_vector_type(2))) _Float16 h2;
typedef __attribute__((ext_vector_type(8))) _Float16 h8;
DI unsigned pkh(float a, float b) { f2 v = {a, b}; return __builtin_bit_cast(unsigned, __builtin_convertvector(v, h2)); }
DI void parta_task(unsigned tag, int wave, int task) {
    CArgs& a = get_args();
    const SideCtx c = make_ctx(wave);
    char LAS* sm = c.sm;
    const int lane = c.lane, w = c.wave, tid = w * 64 + lane, hi = lane >> 5, l31 = lane & 31, l15 = lane & 15, kk = lane >> 4;
    task = __builtin_amdgcn_readfirstlane(task); asm volatile("" : "+s"(task));
    const int d0 = task * 8;
    const u4 rpa = mksrd(a.PA, 56 * 32768);
    if (task >= NA) slp(((task * 23) % 52) * A_STG);
    bar();
#pragma unroll
    for (int g = 0; g < 2; ++g) {
        const int d = d0 + 4 * g + w;
        const float dt = bfr(expf(bf2f(a.Alogdt[d])));
        const u2 x0 = *(const u2*)(a.AlogA + d * 256 + lane * 4), x1 = *(const u2*)(a.Aim + d * 256 + lane * 4), x2 = *(const u2*)(a.ACr + d * 256 + lane * 4), x3 = *(const u2*)(a.ACi + d * 256 + lane * 4);
        f4 z[4]; unsigned mx = 0;
#pragma unroll
        for (int i = 0; i < 4; ++i) {
            auto el = [&](u2 x) { return bf2f((u16)(x[i >> 1] >> (16 * (i & 1)))); };
            z[i] = (f4){-bfr(expf(el(x0))) * dt * LOG2E, el(x1) * dt * 0.15915494309189535f, 2.f * el(x2) * dt, 2.f * el(x3) * dt};
            mx = max(mx, max(__float_as_uint(z[i][2]) & 0x7f800000u, __float_as_uint(z[i][3]) & 0x7f800000u));
        }
#pragma unroll
        for (int o = 1; o < 64; o <<= 1) mx = max(mx, (unsigned)__shfl_xor((int)mx, o));
        mx = min(max(mx, 0x00800000u), 0x7e800000u);
        *(unsigned LAS*)(sm + PA_SC + (4 * g + w) * 4) = mx;
        const float inv = __uint_as_float(0x7f000000u - mx);
#pragma unroll
        for (int i = 0; i < 4; ++i) { z[i][2] *= inv; z[i][3] *= inv; *(f4 LAS*)(sm + PA_RED + w * 9216 + g * 4096 + (i * 64 + lane) * 16) = z[i]; }
    }
    const u4 dsk4 = *(const u4*)(a.ADsk + d0);
    if (tid < 8) *(u4 LAS*)(sm + PA_UT + tid * 4112 + 4096) = (u4){0u, 0u, 0u, 0u};
    if (tid < 155) *(u4 LAS*)(sm + PA_KH + tid * 16) = (u4){0u, 0u, 0u, 0u};
    float qpos[4];
    qpos[0] = (float)l15;
    qpos[1] = l15 < 8 ? (float)(256 + 2 * l15) : (float)(512 + ((l15 - 8) >> 2) * 256 + (l15 & 3) * 4);
    qpos[2] = l15 < 8 ? (float)(1024 + (l15 >> 1) * 256 + (l15 & 1) * 8) : 2048.f;
    qpos[3] = (float)(16 * l15);
    auto kgen = [&](int g) __attribute__((always_inline)) {
        const char LAS* pt = sm + PA_RED + w * 9216 + g * 4096 + kk * 16;
        f4 acc[3] = {{0.f, 0.f, 0.f, 0.f}, {0.f, 0.f, 0.f, 0.f}, {0.f, 0.f, 0.f, 0.f}};
#pragma nounroll
        for (int st = 0; st < 16; ++st) {
            u4 op[4];
#pragma unroll
            for (int i = 0; i < 4; ++i) {
                const f4 z = *(const f4 LAS*)(pt + st * 256 + i * 64);
#pragma unroll
                for (int t = 0; t < 4; ++t) {
                    const float ex = __builtin_amdgcn_exp2f(z[0] * qpos[t]), rv = __builtin_amdgcn_fractf(z[1] * qpos[t]);
                    const float er = ex * __builtin_amdgcn_cosf(rv), ei = ex * __builtin_amdgcn_sinf(rv);
                    op[t][i] = t < 3 ? pkh(er, ei) : pkh(z[2] * er - z[3] * ei, -(z[2] * ei + z[3] * er));
                }
            }
#pragma unroll
            for (int t = 0; t < 3; ++t) acc[t] = __builtin_amdgcn_mfma_f32_16x16x32_f16(__builtin_bit_cast(h8, op[3]), __builtin_bit_cast(h8, op[t]), acc[t], 0, 0, 0);
        }
#pragma unroll
        for (int t = 0; t < 3; ++t) *(f4 LAS*)(sm + PA_KT + (4 * g + w) * 3072 + ((t * 16 + l15) * 16 + 4 * kk) * 4) = acc[t];
    };
    if (U_W) {
        const u4 ru = mksrd(a.Au, 2048 * 896);
#pragma unroll
        for (int i = 0; i < 8; ++i) { dma_one(ru, tid * 16, task * 32768 + i * 4096, c.smb + PA_TU + w * 1024); asm volatile("s_sleep 4"); }
    }
    kgen(0);
    if (A_LATE && task < NA) kgen(1);
    {
        const u4 ru = mksrd(a.Au, 2048 * 896);
#pragma unroll
        for (int i = 0; i < 8; ++i) { dma_one(ru, tid * 896, i * 256 * 896 + d0 * 2, c.smb + PA_TU + i * 4096 + w * 1024); asm volatile("s_sleep %0" :: "i"(P_USLP)); }
    }
    if (!(A_LATE && task < NA)) kgen(1);
    waitvm<0>(); bar();
    u4 uv[8];
#pragma unroll
    for (int i = 0; i < 8; ++i) uv[i] = *(const u4 LAS*)(sm + PA_TU + tid * 128 + i * 16);
#pragma unroll
    for (int ch = 0; ch < 8; ++ch) {
        u4 o;
#pragma unroll
        for (int j = 0; j < 4; ++j) o[j] = __builtin_amdgcn_perm(uv[6 - 2 * j][ch >> 1], uv[7 - 2 * j][ch >> 1], (ch & 1) ? 0x07060302u : 0x05040100u);
        *(u4 LAS*)(sm + PA_UT + ch * 4112 + (255 - tid) * 16) = o;
    }
    {
        typedef __attribute__((ext_vector_type(2))) unsigned short us2;
        u4 mm;
#pragma unroll
        for (int j = 0; j < 4; ++j) {
            us2 m = {0, 0};
#pragma unroll
            for (int i = 0; i < 8; ++i) m = __builtin_elementwise_max(m, __builtin_bit_cast(us2, uv[i][j] & 0x7f807f80u));
#pragma unroll
            for (int o = 1; o < 64; o <<= 1) m = __builtin_elementwise_max(m, __builtin_bit_cast(us2, __shfl_xor(__builtin_bit_cast(int, m), o)));
            mm[j] = __builtin_bit_cast(unsigned, m);
        }
        *(u4 LAS*)(sm + PA_SC + 64 + w * 16) = mm;
    }
    int toff[8];
#pragma unroll
    for (int j = 0; j < 8; ++j) {
        const int p = (int)bfr((float)(tid * 8 + j));
        int col, ar = (p & 255) >> 4;
        if (p < 256) col = p & 15; else if (p < 512) col = 16 + ((p & 15) >> 1); else if (p < 1024) col = 24 + ((p - 512) >> 8) * 4 + ((p & 15) >> 2);
        else if (p < 2048) col = 32 + ((p - 1024) >> 8) * 2 + ((p & 15) >> 3); else col = 40;
        toff[j] = (col * 16 + ar) * 4;
    }
    bar();
    if (tid < 32) *(u4 LAS*)(sm + PA_TU + ((tid >> 2) * 260 + 256 + (tid & 3)) * 16) = (u4){0u, 0u, 0u, 0u};
    {
#pragma nounroll
        for (int ch = 0; ch < 8; ++ch) {
            if (task < NA) slp(ch ? A_SLP : (task % 10) * (A_SLP + 70) / 10);
            const u16 LAS* mp = (const u16 LAS*)(sm + PA_SC + 64) + ch;
            const unsigned ue = min(max((unsigned)max(max(mp[0], mp[8]), max(mp[16], mp[24])) << 16, 0x07800000u), 0x7e800000u) - 0x07000000u;
            {
                const u4 x0 = *(const u4 LAS*)(sm + PA_UT + ch * 4112 + tid * 16), x1 = *(const u4 LAS*)(sm + PA_UT + ch * 4112 + tid * 16 + 16);
                float kv[8];
#pragma unroll
                for (int j = 0; j < 8; ++j) kv[j] = *(const float LAS*)(sm + PA_KT + ch * 3072 + toff[j]);
                const float us = __uint_as_float(0x7f000000u - ue);
                unsigned r[8];
#pragma unroll
                for (int j = 0; j < 8; ++j) { const unsigned x = j < 4 ? x0[j & 3] : x1[j & 3]; r[j] = pkh(__uint_as_float(x << 16) * us, __uint_as_float(x & 0xffff0000u) * us); }
#pragma unroll
                for (int cp = 0; cp < 8; ++cp) {
                    u4 o;
#pragma unroll
                    for (int j = 0; j < 4; ++j) o[j] = (cp & 1) ? __builtin_amdgcn_alignbit(r[(cp >> 1) + j + 1], r[(cp >> 1) + j], 16) : r[(cp >> 1) + j];
                    *(u4 LAS*)(sm + PA_TU + (cp * 260 + tid) * 16) = o;
                }
                *(u4 LAS*)(sm + PA_KH + (31 + (tid >> 2)) * 80 + (tid & 3) * 16) = (u4){pkh(kv[0], kv[1]), pkh(kv[2], kv[3]), pkh(kv[4], kv[5]), pkh(kv[6], kv[7])};
            }
            bar();
            {
                f16v y0, y1;
                const int m00 = 2047 - l31 + 8 * hi, cp = m00 & 7;
                const char LAS* ab = sm + PA_TU + (cp * 260 + (m00 >> 3)) * 16 - 63 * 64;
                const char LAS* bb = sm + PA_KH + l31 * 80 + hi * 16;
                const char LAS* a1w = ab - w * 64; const char LAS* b1w = bb - w * 80;
                const char LAS* a2w = ab + w * 64; const char LAS* b2w = bb + w * 80;
                u4 fr[4][3];
                auto rdg = [&](int g) __attribute__((always_inline)) {
                    const int i = (g & 15) >> 1, ks = g & 1, sl = g & 3;
                    if (g < 16) { fr[sl][0] = *(const u4 LAS*)(b1w + (31 - 4 * i) * 80 + ks * 32); fr[sl][1] = *(const u4 LAS*)(a1w + (63 - 4 * i) * 64 + ks * 32);
                                  fr[sl][2] = *(const u4 LAS*)(a1w + (31 - 4 * i) * 64 + ks * 32); }
                    else { fr[sl][0] = *(const u4 LAS*)(b2w + (32 + 4 * i) * 80 + ks * 32); fr[sl][2] = *(const u4 LAS*)(a2w + (32 + 4 * i) * 64 + ks * 32); }
                };
                rdg(0); rdg(1); rdg(2); SB();
#pragma unroll
                for (int g = 0; g < 32; ++g) {
                    const int sl = g & 3;
                    if (g + 3 < 32) rdg(g + 3);
                    SB();
                    if (g == 0) asm volatile("v_mfma_f32_32x32x16_f16 %0, %2, %4, 0\n\tv_mfma_f32_32x32x16_f16 %1, %3, %4, 0" : "=&v"(y0), "=&v"(y1) : "v"(fr[0][1]), "v"(fr[0][2]), "v"(fr[0][0]));
                    else { if (g < 16) asm volatile("v_mfma_f32_32x32x16_f16 %0, %1, %2, %0" : "+v"(y0) : "v"(fr[sl][1]), "v"(fr[sl][0]));
                           asm volatile("v_mfma_f32_32x32x16_f16 %0, %1, %2, %0" : "+v"(y1) : "v"(fr[sl][2]), "v"(fr[sl][0])); }
                    SB();
                }
                MFW32(y0, y1);
#pragma unroll
                for (int g = 0; g < 4; ++g) {
                    *(f4 LAS*)(sm + PA_RED + w * 9216 + l31 * 144 + (8 * g + 4 * hi) * 4) = (f4){y0[4 * g], y0[4 * g + 1], y0[4 * g + 2], y0[4 * g + 3]};
                    *(f4 LAS*)(sm + PA_RED + w * 9216 + (32 + l31) * 144 + (8 * g + 4 * hi) * 4) = (f4){y1[4 * g], y1[4 * g + 1], y1[4 * g + 2], y1[4 * g + 3]};
                }
            }
            bar();
            {
                f4 s0 = {0.f, 0.f, 0.f, 0.f}, s1 = s0;
#pragma unroll
                for (int p = 0; p < 4; ++p) { const char LAS* q_ = sm + PA_RED + p * 9216 + (tid >> 2) * 144 + (tid & 3) * 32; s0 += *(const f4 LAS*)q_; s1 += *(const f4 LAS*)(q_ + 16); }
                const u4 uu = *(const u4 LAS*)(sm + PA_UT + ch * 4112 + (255 - tid) * 16);
                const unsigned dw = (ch & 4) ? ((ch & 2) ? dsk4[3] : dsk4[2]) : ((ch & 2) ? dsk4[1] : dsk4[0]);
                const float dsk = __uint_as_float((ch & 1) ? dw & 0xffff0000u : dw << 16);
                const float ks_ = *(const float LAS*)(sm + PA_SC + ch * 4), us_ = __uint_as_float(ue);
                float yv[8];
#pragma unroll
                for (int j = 0; j < 8; ++j) yv[j] = bfr((j < 4 ? s0[j & 3] : s1[j & 3]) * us_ * ks_) + bfr(__uint_as_float((j & 1) ? uu[3 - (j >> 1)] << 16 : uu[3 - (j >> 1)] & 0xffff0000u) * dsk);
                sst128((u4){pk2(yv[0], yv[1]), pk2(yv[2], yv[3]), pk2(yv[4], yv[5]), pk2(yv[6], yv[7])}, rpa, tid * 16, (d0 + ch) * 4096);
            }
        }
    }
    set_flag(c, mksrd(a.HB, HB_SZ), O_FA + task * 64, tag);
}
#define P_ASLP 8
DI void pa_out(unsigned tag, int wave, int t) {
    CArgs& a = get_args();
    const SideCtx c = make_ctx(wave);
    const int lane = c.lane, tid = c.wave * 64 + lane;
    t = __builtin_amdgcn_readfirstlane(t); asm volatile("" : "+s"(t));
    const u4 rpa = mksrd(a.PA, 56 * 32768), ro = mksrd(a.out, 2048 * 1792);
    wait_flags(mksrd(a.HB, HB_SZ), lane < 8 ? O_FA + ((t & ~7) + lane) * 64 : OOB, tag);
    u4 v[8];
#pragma unroll
    for (int i = 0; i < 8; ++i) v[i] = sld128(rpa, (tid >> 5) * 4096 + (tid & 31) * 16, ((t >> 3) * 64 + i * 8) * 4096 + (t & 7) * 512);
    WAITV(0, "+v"(v[0]), "+v"(v[1]), "+v"(v[2]), "+v"(v[3]), "+v"(v[4]), "+v"(v[5]), "+v"(v[6]), "+v"(v[7]));
    bar();
#pragma unroll
    for (int i = 0; i < 8; ++i) *(u4 LAS*)(c.sm + (i * 8 + (tid >> 5)) * 528 + (tid & 31) * 16) = v[i];
    bar();
#pragma unroll
    for (int k = 0; k < 8; ++k) {
        const char LAS* p = c.sm + (tid & 7) * 8 * 528 + ((tid >> 3) + 32 * k) * 2;
        u4 o;
#pragma unroll
        for (int j = 0; j < 4; ++j) o[j] = (unsigned)*(const u16 LAS*)(p + (2 * j) * 528) | ((unsigned)*(const u16 LAS*)(p + (2 * j + 1) * 528) << 16);
        sst128(o, ro, (tid >> 3) * 1792 + (tid & 7) * 16, ((t & 7) * 256 + 32 * k) * 1792 + (t >> 3) * 128);
        asm volatile("s_sleep %0" :: "i"(P_ASLP));
    }
}
#define P_A 196
#define P_B 252
#define P_R 92
#define P_EPS 64
#define P_M 0ull

#define P_OWN 1
#define P_S1 1
#define P_S3 5
#define P_STG 8
#define P_XOR 4
#define L_SCR 28672
#define V_M P_M
#define V_A P_A
#define V_B P_B

DI void chain_role(unsigned tag, int vb, int wave, u8v pre) {
    CArgs& a = get_args();
    const int nx = vb & 3, id = vb >> 3, W = wave;
    const int lane = fresh_lane(), q = lane >> 4, n = lane & 15;
    const u4 r = mksrd(a.HB, HB_SZ);
    if (W == 0 && lane == 0) *(u4 LAS*)(smem_ + 24576) = (u4){0u, 0u, 0u, 0u};
    const unsigned want = tag + (id >= 16 && id < 20);
    u2 v = {pre[0], pre[1]}, w = {pre[4], pre[5]}; unsigned f = pre[2], sp = 0;
#pragma nounroll
    while (!__all((v[0] ^ v[1]) == tag && f - want <= 72u && (w[0] ^ w[1]) == tag)) {
        if (++sp > SPIN_MAX) __builtin_amdgcn_endpgm();
        asm volatile("s_sleep 2" ::: "memory");
        v = sld64(r, O_PAR + 4, 0); f = sld32(r, need_off(nx, id, W, lane), 0); w = sld64(r, O_PAR + 512 + min(lane, 35) * 32, 0); WAITV(0, "+v"(v), "+v"(f), "+v"(w));
    }
    unsigned win = w[0]; const unsigned t0 = __builtin_amdgcn_readfirstlane(v[0]);
    asm volatile("s_nop 1\n\tv_min_u32_dpp %0, %0, %0 row_ror:8 row_mask:0xf bank_mask:0xf\n\ts_nop 1\n\tv_min_u32_dpp %0, %0, %0 row_ror:4 row_mask:0xf bank_mask:0xf\n\ts_nop 1\n\t"
                 "v_min_u32_dpp %0, %0, %0 row_ror:2 row_mask:0xf bank_mask:0xf\n\ts_nop 1\n\tv_min_u32_dpp %0, %0, %0 row_ror:1 row_mask:0xf bank_mask:0xf\n\ts_nop 1" : "+v"(win));
    win = min(min((unsigned)__builtin_amdgcn_readlane(win, 0), (unsigned)__builtin_amdgcn_readlane(win, 16)), (unsigned)__builtin_amdgcn_readlane(win, 32)) & 3u;
    const int wA = win * 8192, wB = wA ^ 16384, wF = 524288 + wA;
    auto copy_off = [&](int g) { return (2 + (g >> 1)) * 32768 + ((g & 1) ? wA : wB); };
    if (id == 27) {
        unsigned m[7];
#pragma unroll
        for (int k = 0; k < 7; ++k) m[k] = san(*(const unsigned*)(a.mem0 + (lane >> 3) * 448 + (4 * k + W) * 16 + 4 * (lane & 3) + 2 * ((lane >> 2) & 1)));
        bar();
#pragma unroll
        for (int k = 0; k < 7; ++k) sst32(m[k], r, copy_off(nx) + lane * 4, 3 * 131072 + (4 * k + W) * 256);
        return;
    }
    Ctx c;
    c.r = r; c.rts = r; c.t0 = t0; c.per = V_A + V_B; c.R = P_R; c.tag = tag; c.lv0 = (unsigned)(V_M); c.lv1 = (unsigned)((V_M) >> 32); c.des = P_EPS; c.role = (id < 16 ? 0 : id < 20 ? 4 : 8) + W; c.tf0 = 0;
    c.idx = (lane & 1) ? 0xEEEE : 0x4444; c.vl16 = lane * 16;
    c.vg = c.vg3 = c.vtl = c.vtr = c.vsel = c.vraw = c.vfo = c.ve = c.vs = c.vrw = c.vrr = c.vr1 = c.vr2 = c.vdbg = 0;
    c.vtb = lane < 28 ? O_ANN + lane * 128 : OOB;
    c.vts = 32768 + W * 4096 + (STAMPS ? lane * 4 + (lane < 16 ? 0 : 32768) : 0);
    if (id < 16) {
        const int hb = nx * 16 + id, h = hb >> 2, part = hb & 3;
        c.rw = mksrd(a.SXM, 64 * SH); c.sw = part * 7; c.vsel = h * 4 * SH + NC * 1024 + ((n >> 2) & 1) * 14336 + q * 256 + (4 * (n >> 3) + (n & 3)) * 16;
        c.rx = mksrd(a.SXM, 64 * SH); c.sx = hb * SH + W * 512;
        c.rg = mksrd(a.X, 2048 * 896); c.sg = 0;
        c.vm = copy_off(nx) + (W >> 1) * 4096 + lane * 16;
        c.vtl = q * 1088 + n * 8 + (W >> 1) * 512;
        const int r_ = (lane >> 2) & 3, c_ = lane & 3;
        const int rec = (h >> 1) * 1024 + part * 256 + (h & 1) * 128;
        const int raw = (h >> 2) * 32768 + wF + (h & 3) * 2048 + part * 512;
        c.vfl = lane == 0 ? O_FH + hb * 128 + W * 64 : OOB; c.vpub = OOB;
        if (W < 2) {
            c.vg = (16 * W + (n & 7)) * 896 + (112 * part + 32 * (q >> 1) + 16 * (n >> 3) + 8 * (q & 1)) * 2; c.vg3 = (q >= 2 && n >= 8) ? OOB : c.vg + 128;
            c.vtr = c_ * 1088 + (8 * q + r_) * 8;
            c.vpub = (q >> 1) * 256 + n * 16 + (q & 1) * 8;
        } else if (W == 2) {
            c.rg = mksrd(a.UM, 28 * MH); c.vg = (hb >> 1) * MH + 61 * 4096 + (hb & 1) * 6144 + lane * 16;
            c.vraw = lane < 16 ? (lane < 8 ? wA : wB) + rec + (lane & 7) * 16 : lane < 32 ? raw + (lane - 16) * 16 : id < 14 ? copy_off(nx) + id * 512 + (lane - 32) * 16 : OOB;
        } else {
            c.vg = copy_off(nx) + (7 * part + 2 * (q >> 1) + (n >> 3)) * 256 + (2 * (n & 7) + (q & 1)) * 16; c.vg3 = (q >= 2 && n >= 8) ? OOB : c.vg + 1024;
            c.vtr = c_ * 1088 + (16 * (q >> 1) + 4 * (r_ >> 1) + 2 * (q & 1) + (r_ & 1)) * 8;
            c.vpub = (n < 8 ? wA : wB) + rec + (n & 7) * 16 + q * 4;
            c.vraw = n < 8 ? raw + n * 32 + q * 8 : OOB;
        }
    } else if (id < 20) {
        const int h = nx * 4 + id - 16;
        c.rw = r; c.sw = 0; c.rx = r; c.sx = 0;
        c.rg = mksrd(a.GF, GSZ); c.sg = h * GH + W * 4096;
        c.vm = copy_off(nx) + W * 2048 + lane * 16;
        c.vtl = W * 1280 + 2 * q * 160 + n * 4;
        c.vtr = ((lane >> 2) & 7) * 160 + (8 * W + 2 * (lane & 3)) * 4;
        const int rec = 32768 + (h >> 1) * 1024 + W * 256 + (h & 1) * 128;
        const int raw = (h >> 2) * 32768 + wF + (h & 3) * 2048 + W * 512 + 256;
        c.vpub = (lane < 32 ? wA : wB) + rec + (lane & 31) * 4;
        c.vraw = lane < 32 ? raw + lane * 8 : OOB;
        c.vsel = lane < 16 ? (lane < 8 ? wA : wB) + rec + (lane & 7) * 16 : lane < 32 ? raw + (lane - 16) * 16 : OOB;
        c.vfl = O_FG + (h * 8 + (lane & 7)) * 64; c.vfo = lane == 0 ? O_FP + h * 64 : OOB;
    } else {
        const int pid = nx * 7 + id - 20;
        c.rw = mksrd(a.UM, 28 * MH); c.sw = pid * MH + NC * 4096 + 4 * W * 14336;
        c.rg = mksrd(a.UF, GSZ); c.sg = pid * UH + W * 4096;
        c.rx = mksrd(a.UM, 28 * MH); c.sx = pid * MH + W * 1024;
        c.tf0 = V_A;
        c.vm = copy_off(nx) + W * 2048 + lane * 16;
        c.vpub = copy_off(W) + pid * 256 + lane * 4;
        c.vs = ((nx & 1) ? wA : wB) + W * 2048 + lane * 16; c.ve = c.vs + 32768;
        auto qb = [](int x) { return x * 256 + (x & 1) * 32 + (x >> 1) * 128; };
        c.vrw = 8192 + W * 1280 + qb(q) + n * 8;
        c.vrr = 8192 + qb(lane & 3) + ((lane >> 3) + 8 * ((lane >> 2) & 1)) * 8;
        c.vr1 = W * 32768 + wF + (n >> 3) * 2048 + (n & 7) * 32 + q * 8;
        c.vr2 = W * 32768 + wF + (n >> 3) * 2048 + q * 512 + 256 + (n & 7) * 32;
        c.vg = O_FG + (((4 * W + (lane & 3)) * 2 + 1) * 4 + (pid >> 3)) * 64;
        c.vfl = OOB;
        { const int o = P_OWN ? (W - nx - 1) & 3 : W; c.vtl = 8 + 16 * (o == 3 ? P_S3 : o * P_S1); }
        c.vtr = need_off(nx, id, W, lane) + 8192;
        c.vg3 = W < 3 ? c.vm + 1024 : c.vm - 6144;
        c.vsel = (pid == 0 && W == 0 && lane == 0) ? O_EPOCH : OOB;
        c.vfo = (lane == 0 && W == 0) ? O_ANN + pid * 128 : OOB;
    }
    PSTAMP(5);
    asm volatile("s_waitcnt lgkmcnt(0)" ::: "memory");
    chain_asm(c);
}

__global__ __launch_bounds__(256, 1) void fused_kernel(Args a_) {
    CArgs& a = get_args();
    const int wave = __builtin_amdgcn_readfirstlane(threadIdx.x >> 6);
#define PT(k)
    const u4 rhb = mksrd(a.HB, HB_SZ);
    unsigned tag;
    asm volatile("s_load_dword %0, %1, %2" : "=s"(tag) : "s"(a.HB), "s"(O_EPOCH) : "memory");
    const int xcc = (__builtin_amdgcn_s_getreg((31 << 11) | 20) & 7) ^ P_XOR;
    int vb = xcc + (blockIdx.x & ~7);
    const bool prober = (vb & 4) == 0 && (vb >= 216 || (vb >> 5) == 4) && wave == 3;
    const int po = O_PROBE + (vb & 3) * 32768 + ((vb >> 3) & 15) * 128;
    if (prober) { const int l = fresh_lane(); sst32(0u, rhb, l < 4 ? l * 8192 : OOB, po); }
    if (wave) {
        u64 pc; unsigned len;
        asm volatile("s_getpc_b64 %0\nfk_code_lo:\n\ts_max_i32 %1, fk_code_hi-fk_code_lo, 0" : "=s"(pc), "=s"(len) :: "scc");
        const u4 rc = mksrd((const void*)pc, len & ~15u);
        const int vo = (((blockIdx.x >> 3) * 3 + wave - 1) * 2) * 1024 + (threadIdx.x & 63) * 16;
#pragma unroll
        for (int i = 0; i < 2; ++i) dma_one(rc, vo + i * 1024, 0, (unsigned)(size_t)(char LAS*)smem_ + L_SCR + wave * 1024);
    }
    PT(4);
    unsigned best = 0xfffu;
#pragma nounroll
    for (int pass = 0;; ++pass) {
        gemm_first<0>(wave, (vb & 4) == 0, vb >> 7, 4 * (vb & 3) + ((vb >> 5) & 3), (vb >> 3) & 3);
        unsigned old = 0;
        PT(0);
        if (pass == 0) {
            asm volatile("s_waitcnt lgkmcnt(0)" : "+s"(tag) :: "memory");
            tag += 0x9E3779B4u; old = tag;
            if (wave == 0) asm volatile("s_nop 4\n\tbuffer_atomic_swap %0, %1, %2, %3 offen sc0 sc1" : "+v"(old) : "v"(threadIdx.x == 0 ? O_CNT : OOB), "s"(rhb), "s"((xcc * 32 + (int)(blockIdx.x >> 3)) * 128) : "memory");
        }
        if (pass == 0 && prober) {
            waitvm<18>();
#pragma nounroll
            for (int k = 0; k < 4; ++k) {
                u64 c0, c1; unsigned d;
                asm volatile("s_memtime %0\n\ts_waitcnt lgkmcnt(0)\n\ts_load_dword %2, %3, %4 glc\n\ts_waitcnt lgkmcnt(0)\n\ts_memtime %1\n\ts_waitcnt lgkmcnt(0)" : "=&s"(c0), "=&s"(c1), "=&s"(d) : "s"(a.HB), "s"(po + ((k + (vb >> 3)) & 3) * 8192) : "memory");
                best = min(best, ((unsigned)(c1 - c0) & ~3u) + (((k + (vb >> 3)) & 3) ^ (~vb & 1) * 2));
            }
        }
        PT(1);
        gemm_first<1>(wave, (vb & 4) == 0, vb >> 7, 4 * (vb & 3) + ((vb >> 5) & 3), (vb >> 3) & 3);
        PT(2);
        if (C_AT < 0) WAITV(11, "+v"(old)); else WAITV(10, "+v"(old));
        PT(3);
        if (pass) { bar(); break; }
        if (threadIdx.x == 0) {
            unsigned slot = (unsigned)xcc * 32u + (blockIdx.x >> 3);
            if (old == tag)
                for (int k = 0; k < 256; ++k) { slot = (slot + 1u) & 255u; if (__hip_atomic_exchange((unsigned*)(a.HB + O_CNT) + slot * 32u, tag, __ATOMIC_RELAXED, __HIP_MEMORY_SCOPE_AGENT) != tag) break; }
            *(unsigned LAS*)(smem_ + L_SCR) = (slot >> 5) + 8u * (slot & 31u);
        }
        bar();
        const int got = __builtin_amdgcn_readfirstlane(*(const unsigned LAS*)(smem_ + L_SCR));
        if (got == vb) break;
        best = 0xfffu;
        vb = got; waitvm<0>(); bar();
    }
    vb = __builtin_amdgcn_readfirstlane(vb); tag = __builtin_amdgcn_readfirstlane(tag);
    const int res = vb & 7, idx = vb >> 3;
    if (res < 4) {
        const int lane = fresh_lane(), nb = res * 32 + idx;
        if (vb == 249 && wave == 2) { const unsigned t0 = rt_now(); sst128((u4){0u, t0, t0 ^ tag, tag}, rhb, lane == 0 ? O_PAR : OOB, 0); }
        if ((idx >= 27 || (idx >> 2) == 4) && wave == 3) sst64((u2){best, best ^ tag}, rhb, lane == 0 ? O_PAR + 512 + ((idx >= 27 ? idx - 23 : idx - 16) * 4 + res) * 32 : OOB, 0);
        sst128((u4){FF, FF, FF, FF}, rhb, lane * 16, nb * 8192 + wave * 2048); sst128((u4){FF, FF, FF, FF}, rhb, lane * 16, nb * 8192 + wave * 2048 + 1024);
        if (nb == 0) sst128((u4){0u, 0u, 0u, 0u}, rhb, lane * 16, O_ANN + wave * 1024);
    }
    const u8v pre = (idx >> 4) ? gemm_role<1>(tag, vb, wave, res < 4, 4 * (res & 3) + ((idx >> 2) & 3), idx & 3) : gemm_role<0>(tag, vb, wave, res < 4, 4 * (res & 3) + ((idx >> 2) & 3), idx & 3);
    PSTAMP(3);
    if (res < 4 && idx < 28) chain_role(tag, vb, wave, pre);
    constexpr int NO = 8 + NA;
    if (res < 4 && idx < 32 - NA / 4) return;
    const int f = res < 4 ? -1 : ((idx & 2) ? 0 : 64) + (idx >> 2) * 8 + (idx & 1) * 4 + (res - 4);
    if (f < 56 - NA) { parta_task(tag, wave, f < 0 ? res * (NA / 4) + 31 - idx : NA + (f & 3) * (14 - NA / 4) + (f >> 2)); PSTAMP(5); }
    if (f < 0) return;
    int t1 = 0;
#pragma nounroll
    for (int it = 0; it < 6; ++it) {
        int t = 0;
        if (t1) t = t1;
        else if (f >= 64) t = it ? 0 : f < 108 ? 0x3000 + 20 + f - 64 : f < 115 ? 0x3000 + 84 + f - 108 : 0;
        else if (f >= 56 - NA) { const int i = f - (56 - NA) + NO * it; t = i < 36 ? 0x1000 + i : i < 36 + NO ? 0x4000 + i - 36 : 0; }
        else if (f < 16) t = it == 0 ? 0x1000 + 36 + f : it == 1 && f < 8 ? 0x4000 + 48 + f : 0;
        else { const int i = f - 16 + (40 - NA) * it; t = i < 48 - NO ? 0x4000 + NO + i : i < 96 - NO ? 0x2000 + i - (48 - NO) : i < 116 - NO ? 0x3000 + i - (96 - NO) : 0; if (it == 0) slp((f - 16) * P_STG); }
        t1 = t >= 0x3000 && t < 0x3000 + 20 ? t + 64 : 0;
        const int k = t >> 12, j = t & 4095;
        if (k == 1) o_task(tag, wave, j); else if (k == 2) e_task(tag, wave, 52 + (j >> 2), j & 3); else if (k == 3) x_task(tag, wave, min(52 + j / 7, 63), j % 7, j < 77 ? 2 : j < 84); else if (k == 4) pa_out(tag, wave, j);
    }
    PSTAMP(6);
}
__device__ __attribute__((used, noinline)) void fk_tail() { asm volatile("fk_code_hi:"); }

extern "C" void kernel_launch(void* const* d_in, const int* in_sizes, int n_in, void* d_out, int out_size, void* d_ws, size_t ws_size, hipStream_t stream) {
    Args a;
    a.Au = (const u16*)d_in[0]; a.AlogA = (const u16*)d_in[1]; a.Aim = (const u16*)d_in[2]; a.ACr = (const u16*)d_in[3]; a.ACi = (const u16*)d_in[4];
    a.Alogdt = (const u16*)d_in[5]; a.ADsk = (const u16*)d_in[6]; a.X = (const u16*)d_in[7]; a.mem0 = (const u16*)d_in[8];
    a.Wq = (const u16*)d_in[9]; a.Wk = (const u16*)d_in[10]; a.Wv = (const u16*)d_in[11]; a.Wo = (const u16*)d_in[12];
    a.out = (u16*)d_out;
    char* base = (char*)(((size_t)d_ws + (2u << 20) - 1) & ~(size_t)((2u << 20) - 1));
    a.HB = base;
    size_t off = HB_SZ;
    auto alloc = [&](size_t n) { char* p = base + off; off += (n + 4095) & ~(size_t)4095; return p; };
    a.TS = (unsigned*)alloc(8u << 20);
    a.UM = (u16*)alloc(28 * MH); a.SXM = (float*)alloc(64 * SH); a.GF = (u16*)alloc(GSZ); a.UF = (u16*)alloc(GSZ); a.EX = (u16*)alloc(2u << 20); a.PA = (u16*)alloc(2u << 20); a.MZX = (float*)alloc(64 * 16 * 256);
    (void)hipFuncSetAttribute((const void*)fused_kernel, hipFuncAttributeMaxDynamicSharedMemorySize, 160 * 1024);
    fused_kernel<<<256, 256, 160 * 1024, stream>>>(a);
}
```
